```python
import torch

def _softmax(x, axis=-1):
    return torch.softmax(x, dim=axis)

def _gelu(x):
    return torch.nn.functional.gelu(x, approximate='tanh')

def _silu(x):
    return torch.nn.functional.silu(x)

def _sigmoid(x):
    return torch.sigmoid(x)

def _layernorm(x, axis=-1, eps=1e-05):
    m = x.mean(dim=axis, keepdim=True)
    v = x.var(dim=axis, keepdim=True, unbiased=False)
    return (x - m) / torch.sqrt(v + eps)

def _scatter_add(tgt, idx, src, axis=0):
    return tgt.clone().index_add_(axis, idx, src)

def _segment_sum(src, seg, n):
    out = torch.zeros((n,) + src.shape[1:], dtype=src.dtype)
    return out.index_add_(0, seg, src)

def _pad(x, pw):
    flat = tuple((v for pair in reversed(pw) for v in pair))
    return torch.nn.functional.pad(x, flat)
kernel_name = 'adaLN_cond__spectral_gfnet__mixer_token_mlp__prompt'

def setup_inputs():

    _w = lambda a: (lambda t: t.to(torch.bfloat16) if t.is_floating_point() else t)(torch.as_tensor(a))

    def _A_setup():
        B, S, D = (1, 2048, 576)
        Df = D // 2 + 1
        return {'x': _w((lambda *s: torch.randn(*s, dtype=torch.bfloat16) * 0.05)(B, S, D)), 'w_r': _w((lambda *s: torch.randn(*s, dtype=torch.bfloat16) * 1.6)(S, Df) * 0.02), 'w_i': _w((lambda *s: torch.randn(*s, dtype=torch.bfloat16) * 1.6)(S, Df) * 0.02)}
    _A_in = _A_setup()

    def _B_setup():
        B, S, D, F = (1, 2048, 576, 4608)
        return {'x': _w((lambda *s: torch.randn(*s, dtype=torch.bfloat16) * 0.05)(B, S, D)), 'w1': _w((lambda *s: torch.randn(*s, dtype=torch.bfloat16) * 0.8)(S, F) * S ** (-0.5)), 'b1': _w((lambda *s: torch.randn(*s, dtype=torch.bfloat16) * 12.8)(F) * 0.02), 'w2': _w((lambda *s: torch.randn(*s, dtype=torch.bfloat16) * 0.2)(F, S) * F ** (-0.5)), 'b2': _w((lambda *s: torch.randn(*s, dtype=torch.bfloat16) * 1.6)(S) * 0.02)}
    _B_in = _B_setup()
    return {'A_x': _A_in['x'], 'A_w_r': _A_in['w_r'], 'A_w_i': _A_in['w_i'], 'B_x': _B_in['x'], 'B_w1': _B_in['w1'], 'B_b1': _B_in['b1'], 'B_w2': _B_in['w2'], 'B_b2': _B_in['b2'], 'c': _w((lambda *s: torch.randn(*s, dtype=torch.bfloat16) * 0.1)(1, 576) * 8.0), 'w_mod': _w((lambda *s: torch.randn(*s, dtype=torch.bfloat16) * 0.1)(576, 6 * 576) * 576 ** (-0.5) * 8.0)}

def reference(A_x, A_w_r, A_w_i, B_x, B_w1, B_b1, B_w2, B_b2, c, w_mod):

    def _A_ref(x, w_r, w_i):
        B, S, D = x.shape
        X = (lambda x, axes=(-2, -1): torch.fft.rfft2(x.float(), dim=axes))(x, axes=(-2, -1))
        W = w_r + 1j * w_i
        X = X * W
        y = (lambda x, s, axes=(-2, -1): torch.fft.irfft2(x, s=s, dim=axes).to(torch.bfloat16))(X, (S, D), axes=(-2, -1))
        return y

    def _B_ref(x, w1, b1, w2, b2):
        B, S, D = x.shape
        h = _layernorm(x, axis=-1)
        u = torch.einsum('bsd,sf->bdf', h, w1) + b1
        u = _gelu(u)
        y = torch.einsum('bdf,fs->bds', u, w2) + b2
        return (lambda x, *d: x.permute(*d))(y, 0, 2, 1)
    x = A_x
    mod = (_silu(c) @ w_mod).reshape(c.shape[0], 6, 1, -1)
    sa, ca, ga, sb, cb, gb = (mod[:, 0].clone(), mod[:, 1].clone(), mod[:, 2].clone(), mod[:, 3].clone(), mod[:, 4].clone(), mod[:, 5].clone())
    h = _layernorm(x, axis=-1) * (1 + ca) + sa
    x = x + ga * _A_ref(h, A_w_r, A_w_i)
    h = _layernorm(x, axis=-1) * (1 + cb) + sb
    x = x + gb * _B_ref(h, B_w1, B_b1, B_w2, B_b2)
    return x
```

```cpp
#include <hip/hip_runtime.h>
typedef unsigned short u16;
static __device__ const unsigned __attribute__((aligned(128))) TAB[5632] = {
13312,13312,13312,13312,13312,0x2e1f3364,0x31a831a8,0x33642e1f,13312,0x31a831a8,0x34000000,0x31a8b1a8,13312,0x33642e1f,0x31a8b1a8,0xae1fb364,
13312,0x34000000,46080,0xb4000000,13312,0x3364ae1f,0xb1a8b1a8,0xae1f3364,13312,0x31a8b1a8,0xb4000000,0x31a831a8,13312,0x2e1fb364,0xb1a831a8,0x3364ae1f,
13312,46080,13312,46080,13312,0xae1fb364,0x31a831a8,0xb364ae1f,13312,0xb1a8b1a8,0x34000000,0xb1a831a8,13312,0xb364ae1f,0x31a8b1a8,0x2e1f3364,
13312,0xb4000000,46080,0x34000000,13312,0xb3642e1f,0xb1a8b1a8,0x2e1fb364,13312,0xb1a831a8,0xb4000000,0xb1a8b1a8,13312,0xae1f3364,0xb1a831a8,0xb3642e1f,
13312,13312,13312,13312,0x34000000,0x3364ae1f,0x31a8b1a8,0x2e1fb364,46080,0xb1a8b1a8,0xb4000000,0xb1a831a8,0xb4000000,0xae1f3364,0x31a831a8,0x3364ae1f,
13312,0x34000000,46080,0xb4000000,0x34000000,0xae1fb364,0xb1a831a8,0x33642e1f,46080,0xb1a831a8,0x34000000,0xb1a8b1a8,0xb4000000,0x33642e1f,0xb1a8b1a8,0x2e1f3364,
13312,46080,13312,46080,0x34000000,0xb3642e1f,0x31a8b1a8,0xae1f3364,46080,0x31a831a8,0xb4000000,0x31a8b1a8,0xb4000000,0x2e1fb364,0x31a831a8,0xb3642e1f,
13312,0xb4000000,46080,0x34000000,0x34000000,0x2e1f3364,0xb1a831a8,0xb364ae1f,46080,0x31a8b1a8,0x34000000,0x31a831a8,0xb4000000,0xb364ae1f,0xb1a8b1a8,0xae1fb364,
13312,13312,13312,13312,46080,0xae1fb364,0xb1a8b1a8,0xb364ae1f,13312,0x31a831a8,0x34000000,0x31a8b1a8,46080,0xb364ae1f,0xb1a831a8,0x2e1f3364,
13312,0x34000000,46080,0xb4000000,46080,0xb3642e1f,0x31a831a8,0x2e1fb364,13312,0x31a8b1a8,0xb4000000,0x31a831a8,46080,0xae1f3364,0x31a8b1a8,0xb3642e1f,
13312,46080,13312,46080,46080,0x2e1f3364,0xb1a8b1a8,0x33642e1f,13312,0xb1a8b1a8,0x34000000,0xb1a831a8,46080,0x33642e1f,0xb1a831a8,0xae1fb364,
13312,0xb4000000,46080,0x34000000,46080,0x3364ae1f,0x31a831a8,0xae1f3364,13312,0xb1a831a8,0xb4000000,0xb1a8b1a8,46080,0x2e1fb364,0x31a8b1a8,0x3364ae1f,
13312,13312,13312,13312,0xb4000000,0xb3642e1f,0xb1a831a8,0xae1f3364,46080,0xb1a8b1a8,0xb4000000,0xb1a831a8,0x34000000,0x2e1fb364,0xb1a8b1a8,0xb3642e1f,
13312,0x34000000,46080,0xb4000000,0xb4000000,0x2e1f3364,0x31a8b1a8,0xb364ae1f,46080,0xb1a831a8,0x34000000,0xb1a8b1a8,0x34000000,0xb364ae1f,0x31a831a8,0xae1fb364,
13312,46080,13312,46080,0xb4000000,0x3364ae1f,0xb1a831a8,0x2e1fb364,46080,0x31a831a8,0xb4000000,0x31a8b1a8,0x34000000,0xae1f3364,0xb1a8b1a8,0x3364ae1f,
13312,0xb4000000,46080,0x34000000,0xb4000000,0xae1fb364,0x31a8b1a8,0x33642e1f,46080,0x31a8b1a8,0x34000000,0x31a831a8,0x34000000,0x33642e1f,0x31a831a8,0x2e1f3364,
0x34000000,0x34000000,0x34000000,0x34000000,0x34000000,0x3364ae1f,0x31a8b1a8,0x2e1fb364,0x34000000,0x31a8b1a8,46080,0xb1a8b1a8,0x34000000,0x2e1fb364,0xb1a8b1a8,0xb3642e1f,
0x34000000,46080,0xb4000000,13312,0x34000000,0xae1fb364,0xb1a831a8,0x33642e1f,0x34000000,0xb1a8b1a8,13312,0x31a8b1a8,0x34000000,0xb364ae1f,0x31a831a8,0xae1fb364,
0x34000000,0xb4000000,0x34000000,0xb4000000,0x34000000,0xb3642e1f,0x31a8b1a8,0xae1f3364,0x34000000,0xb1a831a8,46080,0x31a831a8,0x34000000,0xae1f3364,0xb1a8b1a8,0x3364ae1f,
0x34000000,13312,0xb4000000,46080,0x34000000,0x2e1f3364,0xb1a831a8,0xb364ae1f,0x34000000,0x31a831a8,13312,0xb1a831a8,0x34000000,0x33642e1f,0x31a831a8,0x2e1f3364,
0x34000000,0x34000000,0x34000000,0x34000000,46080,0xae1fb364,0xb1a8b1a8,0xb364ae1f,0xb4000000,0xb1a831a8,13312,0x31a831a8,13312,0x33642e1f,0x31a8b1a8,0xae1fb364,
0x34000000,46080,0xb4000000,13312,46080,0xb3642e1f,0x31a831a8,0x2e1fb364,0xb4000000,0x31a831a8,46080,0xb1a831a8,13312,0x2e1fb364,0xb1a831a8,0x3364ae1f,
0x34000000,0xb4000000,0x34000000,0xb4000000,46080,0x2e1f3364,0xb1a8b1a8,0x33642e1f,0xb4000000,0x31a8b1a8,13312,0xb1a8b1a8,13312,0xb364ae1f,0x31a8b1a8,0x2e1f3364,
0x34000000,13312,0xb4000000,46080,46080,0x3364ae1f,0x31a831a8,0xae1f3364,0xb4000000,0xb1a8b1a8,46080,0x31a8b1a8,13312,0xae1f3364,0xb1a831a8,0xb3642e1f,
0x34000000,0x34000000,0x34000000,0x34000000,0xb4000000,0xb3642e1f,0xb1a831a8,0xae1f3364,0x34000000,0x31a8b1a8,46080,0xb1a8b1a8,0xb4000000,0xae1f3364,0x31a831a8,0x3364ae1f,
0x34000000,46080,0xb4000000,13312,0xb4000000,0x2e1f3364,0x31a8b1a8,0xb364ae1f,0x34000000,0xb1a8b1a8,13312,0x31a8b1a8,0xb4000000,0x33642e1f,0xb1a8b1a8,0x2e1f3364,
0x34000000,0xb4000000,0x34000000,0xb4000000,0xb4000000,0x3364ae1f,0xb1a831a8,0x2e1fb364,0x34000000,0xb1a831a8,46080,0x31a831a8,0xb4000000,0x2e1fb364,0x31a831a8,0xb3642e1f,
0x34000000,13312,0xb4000000,46080,0xb4000000,0xae1fb364,0x31a8b1a8,0x33642e1f,0x34000000,0x31a831a8,13312,0xb1a831a8,0xb4000000,0xb364ae1f,0xb1a8b1a8,0xae1fb364,
0x34000000,0x34000000,0x34000000,0x34000000,13312,0x2e1f3364,0x31a831a8,0x33642e1f,0xb4000000,0xb1a831a8,13312,0x31a831a8,46080,0xb364ae1f,0xb1a831a8,0x2e1f3364,
0x34000000,46080,0xb4000000,13312,13312,0x3364ae1f,0xb1a8b1a8,0xae1f3364,0xb4000000,0x31a831a8,46080,0xb1a831a8,46080,0xae1f3364,0x31a8b1a8,0xb3642e1f,
0x34000000,0xb4000000,0x34000000,0xb4000000,13312,0xae1fb364,0x31a831a8,0xb364ae1f,0xb4000000,0x31a8b1a8,13312,0xb1a8b1a8,46080,0x33642e1f,0xb1a831a8,0xae1fb364,
0x34000000,13312,0xb4000000,46080,13312,0xb3642e1f,0xb1a8b1a8,0x2e1fb364,0xb4000000,0xb1a8b1a8,46080,0x31a8b1a8,46080,0x2e1fb364,0x31a8b1a8,0x3364ae1f,
13312,13312,13312,0,13312,0x31243221,0x33e1298f,0,13312,0x33e1298f,0x2d79b384,0,0,0,0,0,
13312,0x32eeb000,0xb2eeb000,0,13312,0x2d79b384,0xb1243221,0,13312,0xad79b384,0x31243221,0,0,0,0,0,
13312,0xb2eeb000,0x32eeb000,0,13312,0xb3e1298f,0xad79b384,0,13312,0xb1243221,0xb3e1298f,0,0,0,0,0,
0,0,0,0,0,0,0,0,0,0,0,0,0,0,0,0,
13312,13312,13312,0,0x32eeb000,0x2d79b384,0xad79b384,0,0xb2eeb000,0xb1243221,0x31243221,0,0,0,0,0,
13312,0x32eeb000,0xb2eeb000,0,0x32eeb000,0xb3e1298f,0x33e1298f,0,0xb2eeb000,0x33e1298f,0xb3e1298f,0,0,0,0,0,
13312,0xb2eeb000,0x32eeb000,0,0x32eeb000,0x31243221,0xb1243221,0,0xb2eeb000,0xad79b384,0x2d79b384,0,0,0,0,0,
0,0,0,0,0,0,0,0,0,0,0,0,0,0,0,0,
13312,13312,13312,0,0xb2eeb000,0xb3e1298f,0xb1243221,0,0x32eeb000,0xad79b384,0xb3e1298f,0,0,0,0,0,
13312,0x32eeb000,0xb2eeb000,0,0xb2eeb000,0x31243221,0xad79b384,0,0x32eeb000,0xb1243221,0x2d79b384,0,0,0,0,0,
13312,0xb2eeb000,0x32eeb000,0,0xb2eeb000,0x2d79b384,0x33e1298f,0,0x32eeb000,0x33e1298f,0x31243221,0,0,0,0,0,
0,0,0,0,0,0,0,0,0,0,0,0,0,0,0,0,
0,0,0,0,0,0,0,0,0,0,0,0,0,0,0,0,
0,0,0,0,0,0,0,0,0,0,0,0,0,0,0,0,
0,0,0,0,0,0,0,0,0,0,0,0,0,0,0,0,
0,0,0,0,0,0,0,0,0,0,0,0,0,0,0,0,
0x34000000,0x34000000,0x34000000,0,0x34000000,0x3221b124,0x298fb3e1,0,0x34000000,0x298fb3e1,0xb384ad79,0,0,0,0,0,
0x34000000,0xb000b2ee,0xb00032ee,0,0x34000000,0xb384ad79,0x32213124,0,0x34000000,0xb3842d79,0x3221b124,0,0,0,0,0,
0x34000000,0xb00032ee,0xb000b2ee,0,0x34000000,0x298f33e1,0xb3842d79,0,0x34000000,0x32213124,0x298f33e1,0,0,0,0,0,
0,0,0,0,0,0,0,0,0,0,0,0,0,0,0,0,
0x34000000,0x34000000,0x34000000,0,0xb000b2ee,0xb384ad79,0xb3842d79,0,0xb00032ee,0x32213124,0x3221b124,0,0,0,0,0,
0x34000000,0xb000b2ee,0xb00032ee,0,0xb000b2ee,0x298f33e1,0x298fb3e1,0,0xb00032ee,0x298fb3e1,0x298f33e1,0,0,0,0,0,
0x34000000,0xb00032ee,0xb000b2ee,0,0xb000b2ee,0x3221b124,0x32213124,0,0xb00032ee,0xb3842d79,0xb384ad79,0,0,0,0,0,
0,0,0,0,0,0,0,0,0,0,0,0,0,0,0,0,
0x34000000,0x34000000,0x34000000,0,0xb00032ee,0x298f33e1,0x32213124,0,0xb000b2ee,0xb3842d79,0x298f33e1,0,0,0,0,0,
0x34000000,0xb000b2ee,0xb00032ee,0,0xb00032ee,0x3221b124,0xb3842d79,0,0xb000b2ee,0x32213124,0xb384ad79,0,0,0,0,0,
0x34000000,0xb00032ee,0xb000b2ee,0,0xb00032ee,0xb384ad79,0x298fb3e1,0,0xb000b2ee,0x298fb3e1,0x3221b124,0,0,0,0,0,
0,0,0,0,0,0,0,0,0,0,0,0,0,0,0,0,
0,0,0,0,0,0,0,0,0,0,0,0,0,0,0,0,
0,0,0,0,0,0,0,0,0,0,0,0,0,0,0,0,
0,0,0,0,0,0,0,0,0,0,0,0,0,0,0,0,
0,0,0,0,0,0,0,0,0,0,0,0,0,0,0,0,
13312,0x2a3e33d9,0x2e1f3364,0x307232a7,13312,0x307232a7,0x33642e1f,0x33d9aa3e,13312,0x32a73072,0x3364ae1f,0x2a3eb3d9,13312,0x33d92a3e,0x2e1fb364,0xb2a7b072,
13312,0x33d9aa3e,0xae1fb364,0xb2a73072,13312,0x32a7b072,0xb364ae1f,0x2a3e33d9,13312,0x3072b2a7,0xb3642e1f,0x33d92a3e,13312,0x2a3eb3d9,0xae1f3364,0x3072b2a7,
13312,0xaa3eb3d9,0x2e1f3364,0xb072b2a7,13312,0xb072b2a7,0x33642e1f,0xb3d92a3e,13312,0xb2a7b072,0x3364ae1f,0xaa3e33d9,13312,0xb3d9aa3e,0x2e1fb364,0x32a73072,
13312,0xb3d92a3e,0xae1fb364,0x32a7b072,13312,0xb2a73072,0xb364ae1f,0xaa3eb3d9,13312,0xb07232a7,0xb3642e1f,0xb3d9aa3e,13312,0xaa3e33d9,0xae1f3364,0xb07232a7,
0x31a831a8,0x32a73072,0x33642e1f,0x33d92a3e,0x31a8b1a8,0x2a3eb3d9,0xae1fb364,0xb2a7b072,0xb1a8b1a8,0xb3d92a3e,0xae1f3364,0x307232a7,0xb1a831a8,0x307232a7,0x3364ae1f,0xaa3eb3d9,
0x31a831a8,0x3072b2a7,0xb364ae1f,0xaa3e33d9,0x31a8b1a8,0xb3d9aa3e,0x2e1f3364,0x3072b2a7,0xb1a8b1a8,0x2a3e33d9,0x2e1fb364,0xb2a73072,0xb1a831a8,0x32a7b072,0xb3642e1f,0x33d9aa3e,
0x31a831a8,0xb2a7b072,0x33642e1f,0xb3d9aa3e,0x31a8b1a8,0xaa3e33d9,0xae1fb364,0x32a73072,0xb1a8b1a8,0x33d9aa3e,0xae1f3364,0xb072b2a7,0xb1a831a8,0xb072b2a7,0x3364ae1f,0x2a3e33d9,
0x31a831a8,0xb07232a7,0xb364ae1f,0x2a3eb3d9,0x31a8b1a8,0x33d92a3e,0x2e1f3364,0xb07232a7,0xb1a8b1a8,0xaa3eb3d9,0x2e1fb364,0x32a7b072,0xb1a831a8,0xb2a73072,0xb3642e1f,0xb3d92a3e,
0x34000000,0x33d9aa3e,0x3364ae1f,0x32a7b072,0xb4000000,0xb2a73072,0xae1f3364,0x2a3e33d9,0x34000000,0x3072b2a7,0xae1fb364,0xb3d9aa3e,0xb4000000,0xaa3e33d9,0x33642e1f,0x3072b2a7,
0x34000000,0xaa3eb3d9,0xb3642e1f,0x307232a7,0xb4000000,0x307232a7,0x2e1fb364,0xb3d92a3e,0x34000000,0xb2a7b072,0x2e1f3364,0x2a3eb3d9,0xb4000000,0x33d92a3e,0xb364ae1f,0x32a73072,
0x34000000,0xb3d92a3e,0x3364ae1f,0xb2a73072,0xb4000000,0x32a7b072,0xae1f3364,0xaa3eb3d9,0x34000000,0xb07232a7,0xae1fb364,0x33d92a3e,0xb4000000,0x2a3eb3d9,0x33642e1f,0xb07232a7,
0x34000000,0x2a3e33d9,0xb3642e1f,0xb072b2a7,0xb4000000,0xb072b2a7,0x2e1fb364,0x33d9aa3e,0x34000000,0x32a73072,0x2e1f3364,0xaa3e33d9,0xb4000000,0xb3d9aa3e,0xb364ae1f,0xb2a7b072,
0x31a8b1a8,0x3072b2a7,0x2e1fb364,0x2a3eb3d9,0x31a831a8,0x33d92a3e,0x3364ae1f,0x3072b2a7,0xb1a831a8,0x2a3e33d9,0x33642e1f,0x32a7b072,0xb1a8b1a8,0xb2a73072,0x2e1f3364,0x33d9aa3e,
0x31a8b1a8,0xb2a7b072,0xae1f3364,0x33d92a3e,0x31a831a8,0x2a3eb3d9,0xb3642e1f,0x32a73072,0xb1a831a8,0x33d9aa3e,0xb364ae1f,0x307232a7,0xb1a8b1a8,0x307232a7,0xae1fb364,0x2a3e33d9,
0x31a8b1a8,0xb07232a7,0x2e1fb364,0xaa3e33d9,0x31a831a8,0xb3d9aa3e,0x3364ae1f,0xb07232a7,0xb1a831a8,0xaa3eb3d9,0x33642e1f,0xb2a73072,0xb1a8b1a8,0x32a7b072,0x2e1f3364,0xb3d92a3e,
0x31a8b1a8,0x32a73072,0xae1f3364,0xb3d9aa3e,0x31a831a8,0xaa3e33d9,0xb3642e1f,0xb2a7b072,0xb1a831a8,0xb3d92a3e,0xb364ae1f,0xb072b2a7,0xb1a8b1a8,0xb072b2a7,0xae1fb364,0xaa3eb3d9,
0x34000000,0x33d9aa3e,0x3364ae1f,0x32a7b072,0x34000000,0x32a7b072,0x2e1fb364,0xaa3eb3d9,0x34000000,0x3072b2a7,0xae1fb364,0xb3d9aa3e,0x34000000,0x2a3eb3d9,0xb364ae1f,0xb07232a7,
0x34000000,0xaa3eb3d9,0xb3642e1f,0x307232a7,0x34000000,0xb072b2a7,0xae1f3364,0x33d9aa3e,0x34000000,0xb2a7b072,0x2e1f3364,0x2a3eb3d9,0x34000000,0xb3d9aa3e,0x33642e1f,0xb2a7b072,
0x34000000,0xb3d92a3e,0x3364ae1f,0xb2a73072,0x34000000,0xb2a73072,0x2e1fb364,0x2a3e33d9,0x34000000,0xb07232a7,0xae1fb364,0x33d92a3e,0x34000000,0xaa3e33d9,0xb364ae1f,0x3072b2a7,
0x34000000,0x2a3e33d9,0xb3642e1f,0xb072b2a7,0x34000000,0x307232a7,0xae1f3364,0xb3d92a3e,0x34000000,0x32a73072,0x2e1f3364,0xaa3e33d9,0x34000000,0x33d92a3e,0x33642e1f,0x32a73072,
0x31a8b1a8,0x3072b2a7,0x2e1fb364,0x2a3eb3d9,0xb1a8b1a8,0xb3d9aa3e,0xb3642e1f,0xb07232a7,0xb1a831a8,0x2a3e33d9,0x33642e1f,0x32a7b072,0x31a831a8,0x32a7b072,0xae1fb364,0xb3d92a3e,
0x31a8b1a8,0xb2a7b072,0xae1f3364,0x33d92a3e,0xb1a8b1a8,0xaa3e33d9,0x3364ae1f,0xb2a7b072,0xb1a831a8,0x33d9aa3e,0xb364ae1f,0x307232a7,0x31a831a8,0xb072b2a7,0x2e1f3364,0xaa3eb3d9,
0x31a8b1a8,0xb07232a7,0x2e1fb364,0xaa3e33d9,0xb1a8b1a8,0x33d92a3e,0xb3642e1f,0x3072b2a7,0xb1a831a8,0xaa3eb3d9,0x33642e1f,0xb2a73072,0x31a831a8,0xb2a73072,0xae1fb364,0x33d9aa3e,
0x31a8b1a8,0x32a73072,0xae1f3364,0xb3d9aa3e,0xb1a8b1a8,0x2a3eb3d9,0x3364ae1f,0x32a73072,0xb1a831a8,0xb3d92a3e,0xb364ae1f,0xb072b2a7,0x31a831a8,0x307232a7,0x2e1f3364,0x2a3e33d9,
46080,0xaa3eb3d9,0xae1fb364,0xb072b2a7,13312,0x307232a7,0x33642e1f,0x33d9aa3e,46080,0xb2a7b072,0xb3642e1f,0xaa3e33d9,13312,0x33d92a3e,0x2e1fb364,0xb2a7b072,
46080,0xb3d92a3e,0x2e1f3364,0x32a7b072,13312,0x32a7b072,0xb364ae1f,0x2a3e33d9,46080,0xb07232a7,0x3364ae1f,0xb3d9aa3e,13312,0x2a3eb3d9,0xae1f3364,0x3072b2a7,
46080,0x2a3e33d9,0xae1fb364,0x307232a7,13312,0xb072b2a7,0x33642e1f,0xb3d92a3e,46080,0x32a73072,0xb3642e1f,0x2a3eb3d9,13312,0xb3d9aa3e,0x2e1fb364,0x32a73072,
46080,0x33d9aa3e,0x2e1f3364,0xb2a73072,13312,0xb2a73072,0xb364ae1f,0xaa3eb3d9,46080,0x3072b2a7,0x3364ae1f,0x33d92a3e,13312,0xaa3e33d9,0xae1f3364,0xb07232a7,
0xb1a8b1a8,0xb2a7b072,0xb364ae1f,0xb3d9aa3e,0x31a8b1a8,0x2a3eb3d9,0xae1fb364,0xb2a7b072,0x31a831a8,0x33d9aa3e,0x2e1fb364,0xb072b2a7,0xb1a831a8,0x307232a7,0x3364ae1f,0xaa3eb3d9,
0xb1a8b1a8,0xb07232a7,0x33642e1f,0x2a3eb3d9,0x31a8b1a8,0xb3d9aa3e,0x2e1f3364,0x3072b2a7,0x31a831a8,0xaa3eb3d9,0xae1f3364,0x32a7b072,0xb1a831a8,0x32a7b072,0xb3642e1f,0x33d9aa3e,
0xb1a8b1a8,0x32a73072,0xb364ae1f,0x33d92a3e,0x31a8b1a8,0xaa3e33d9,0xae1fb364,0x32a73072,0x31a831a8,0xb3d92a3e,0x2e1fb364,0x307232a7,0xb1a831a8,0xb072b2a7,0x3364ae1f,0x2a3e33d9,
0xb1a8b1a8,0x3072b2a7,0x33642e1f,0xaa3e33d9,0x31a8b1a8,0x33d92a3e,0x2e1f3364,0xb07232a7,0x31a831a8,0x2a3e33d9,0xae1f3364,0xb2a73072,0xb1a831a8,0xb2a73072,0xb3642e1f,0xb3d92a3e,
13312,13312,13312,13312,0x2a3e33d9,0x307232a7,0x32a73072,0x33d92a3e,0x2e1f3364,0x33642e1f,0x3364ae1f,0x2e1fb364,0x307232a7,0x33d9aa3e,0x2a3eb3d9,0xb2a7b072,
0x31a831a8,0x31a8b1a8,0xb1a8b1a8,0xb1a831a8,0x32a73072,0x2a3eb3d9,0xb3d92a3e,0x307232a7,0x33642e1f,0xae1fb364,0xae1f3364,0x3364ae1f,0x33d92a3e,0xb2a7b072,0x307232a7,0xaa3eb3d9,
0x34000000,0xb4000000,0x34000000,0xb4000000,0x33d9aa3e,0xb2a73072,0x3072b2a7,0xaa3e33d9,0x3364ae1f,0xae1f3364,0xae1fb364,0x33642e1f,0x32a7b072,0x2a3e33d9,0xb3d9aa3e,0x3072b2a7,
0x31a8b1a8,0x31a831a8,0xb1a831a8,0xb1a8b1a8,0x3072b2a7,0x33d92a3e,0x2a3e33d9,0xb2a73072,0x2e1fb364,0x3364ae1f,0x33642e1f,0x2e1f3364,0x2a3eb3d9,0x3072b2a7,0x32a7b072,0x33d9aa3e,
13312,13312,13312,13312,0x33d9aa3e,0x32a7b072,0x3072b2a7,0x2a3eb3d9,0xae1fb364,0xb364ae1f,0xb3642e1f,0xae1f3364,0xb2a73072,0x2a3e33d9,0x33d92a3e,0x3072b2a7,
0x31a831a8,0x31a8b1a8,0xb1a8b1a8,0xb1a831a8,0x3072b2a7,0xb3d9aa3e,0x2a3e33d9,0x32a7b072,0xb364ae1f,0x2e1f3364,0x2e1fb364,0xb3642e1f,0xaa3e33d9,0x3072b2a7,0xb2a73072,0x33d9aa3e,
0x34000000,0xb4000000,0x34000000,0xb4000000,0xaa3eb3d9,0x307232a7,0xb2a7b072,0x33d92a3e,0xb3642e1f,0x2e1fb364,0x2e1f3364,0xb364ae1f,0x307232a7,0xb3d92a3e,0x2a3eb3d9,0x32a73072,
0x31a8b1a8,0x31a831a8,0xb1a831a8,0xb1a8b1a8,0xb2a7b072,0x2a3eb3d9,0x33d9aa3e,0x307232a7,0xae1f3364,0xb3642e1f,0xb364ae1f,0xae1fb364,0x33d92a3e,0x32a73072,0x307232a7,0x2a3e33d9,
13312,13312,13312,13312,0xaa3eb3d9,0xb072b2a7,0xb2a7b072,0xb3d9aa3e,0x2e1f3364,0x33642e1f,0x3364ae1f,0x2e1fb364,0xb072b2a7,0xb3d92a3e,0xaa3e33d9,0x32a73072,
0x31a831a8,0x31a8b1a8,0xb1a8b1a8,0xb1a831a8,0xb2a7b072,0xaa3e33d9,0x33d9aa3e,0xb072b2a7,0x33642e1f,0xae1fb364,0xae1f3364,0x3364ae1f,0xb3d9aa3e,0x32a73072,0xb072b2a7,0x2a3e33d9,
0x34000000,0xb4000000,0x34000000,0xb4000000,0xb3d92a3e,0x32a7b072,0xb07232a7,0x2a3eb3d9,0x3364ae1f,0xae1f3364,0xae1fb364,0x33642e1f,0xb2a73072,0xaa3eb3d9,0x33d92a3e,0xb07232a7,
0x31a8b1a8,0x31a831a8,0xb1a831a8,0xb1a8b1a8,0xb07232a7,0xb3d9aa3e,0xaa3eb3d9,0x32a7b072,0x2e1fb364,0x3364ae1f,0x33642e1f,0x2e1f3364,0xaa3e33d9,0xb07232a7,0xb2a73072,0xb3d92a3e,
13312,13312,13312,13312,0xb3d92a3e,0xb2a73072,0xb07232a7,0xaa3e33d9,0xae1fb364,0xb364ae1f,0xb3642e1f,0xae1f3364,0x32a7b072,0xaa3eb3d9,0xb3d9aa3e,0xb07232a7,
0x31a831a8,0x31a8b1a8,0xb1a8b1a8,0xb1a831a8,0xb07232a7,0x33d92a3e,0xaa3eb3d9,0xb2a73072,0xb364ae1f,0x2e1f3364,0x2e1fb364,0xb3642e1f,0x2a3eb3d9,0xb07232a7,0x32a7b072,0xb3d92a3e,
0x34000000,0xb4000000,0x34000000,0xb4000000,0x2a3e33d9,0xb072b2a7,0x32a73072,0xb3d9aa3e,0xb3642e1f,0x2e1fb364,0x2e1f3364,0xb364ae1f,0xb072b2a7,0x33d9aa3e,0xaa3e33d9,0xb2a7b072,
0x31a8b1a8,0x31a831a8,0xb1a831a8,0xb1a8b1a8,0x32a73072,0xaa3e33d9,0xb3d92a3e,0xb072b2a7,0xae1f3364,0xb3642e1f,0xb364ae1f,0xae1fb364,0xb3d9aa3e,0xb2a7b072,0xb072b2a7,0xaa3eb3d9,
0x34000000,0x34000000,0x34000000,0x34000000,0x33d9aa3e,0x32a7b072,0x3072b2a7,0x2a3eb3d9,0x3364ae1f,0x2e1fb364,0xae1fb364,0xb364ae1f,0x32a7b072,0xaa3eb3d9,0xb3d9aa3e,0xb07232a7,
0x31a8b1a8,0xb1a8b1a8,0xb1a831a8,0x31a831a8,0x3072b2a7,0xb3d9aa3e,0x2a3e33d9,0x32a7b072,0x2e1fb364,0xb3642e1f,0x33642e1f,0xae1fb364,0x2a3eb3d9,0xb07232a7,0x32a7b072,0xb3d92a3e,
46080,13312,46080,13312,0xaa3eb3d9,0x307232a7,0xb2a7b072,0x33d92a3e,0xae1fb364,0x33642e1f,0xb3642e1f,0x2e1fb364,0xb072b2a7,0x33d9aa3e,0xaa3e33d9,0xb2a7b072,
0xb1a8b1a8,0x31a8b1a8,0x31a831a8,0xb1a831a8,0xb2a7b072,0x2a3eb3d9,0x33d9aa3e,0x307232a7,0xb364ae1f,0xae1fb364,0x2e1fb364,0x3364ae1f,0xb3d9aa3e,0xb2a7b072,0xb072b2a7,0xaa3eb3d9,
0x34000000,0x34000000,0x34000000,0x34000000,0xaa3eb3d9,0xb072b2a7,0xb2a7b072,0xb3d9aa3e,0xb3642e1f,0xae1f3364,0x2e1f3364,0x33642e1f,0x307232a7,0x33d9aa3e,0x2a3eb3d9,0xb2a7b072,
0x31a8b1a8,0xb1a8b1a8,0xb1a831a8,0x31a831a8,0xb2a7b072,0xaa3e33d9,0x33d9aa3e,0xb072b2a7,0xae1f3364,0x3364ae1f,0xb364ae1f,0x2e1f3364,0x33d92a3e,0xb2a7b072,0x307232a7,0xaa3eb3d9,
46080,13312,46080,13312,0xb3d92a3e,0x32a7b072,0xb07232a7,0x2a3eb3d9,0x2e1f3364,0xb364ae1f,0x3364ae1f,0xae1f3364,0x32a7b072,0x2a3e33d9,0xb3d9aa3e,0x3072b2a7,
0xb1a8b1a8,0x31a8b1a8,0x31a831a8,0xb1a831a8,0xb07232a7,0xb3d9aa3e,0xaa3eb3d9,0x32a7b072,0x33642e1f,0x2e1f3364,0xae1f3364,0xb3642e1f,0x2a3eb3d9,0x3072b2a7,0x32a7b072,0x33d9aa3e,
0x34000000,0x34000000,0x34000000,0x34000000,0xb3d92a3e,0xb2a73072,0xb07232a7,0xaa3e33d9,0x3364ae1f,0x2e1fb364,0xae1fb364,0xb364ae1f,0xb2a73072,0x2a3e33d9,0x33d92a3e,0x3072b2a7,
0x31a8b1a8,0xb1a8b1a8,0xb1a831a8,0x31a831a8,0xb07232a7,0x33d92a3e,0xaa3eb3d9,0xb2a73072,0x2e1fb364,0xb3642e1f,0x33642e1f,0xae1fb364,0xaa3e33d9,0x3072b2a7,0xb2a73072,0x33d9aa3e,
46080,13312,46080,13312,0x2a3e33d9,0xb072b2a7,0x32a73072,0xb3d9aa3e,0xae1fb364,0x33642e1f,0xb3642e1f,0x2e1fb364,0x307232a7,0xb3d92a3e,0x2a3eb3d9,0x32a73072,
0xb1a8b1a8,0x31a8b1a8,0x31a831a8,0xb1a831a8,0x32a73072,0xaa3e33d9,0xb3d92a3e,0xb072b2a7,0xb364ae1f,0xae1fb364,0x2e1fb364,0x3364ae1f,0x33d92a3e,0x32a73072,0x307232a7,0x2a3e33d9,
0x34000000,0x34000000,0x34000000,0x34000000,0x2a3e33d9,0x307232a7,0x32a73072,0x33d92a3e,0xb3642e1f,0xae1f3364,0x2e1f3364,0x33642e1f,0xb072b2a7,0xb3d92a3e,0xaa3e33d9,0x32a73072,
0x31a8b1a8,0xb1a8b1a8,0xb1a831a8,0x31a831a8,0x32a73072,0x2a3eb3d9,0xb3d92a3e,0x307232a7,0xae1f3364,0x3364ae1f,0xb364ae1f,0x2e1f3364,0xb3d9aa3e,0x32a73072,0xb072b2a7,0x2a3e33d9,
46080,13312,46080,13312,0x33d9aa3e,0xb2a73072,0x3072b2a7,0xaa3e33d9,0x2e1f3364,0xb364ae1f,0x3364ae1f,0xae1f3364,0xb2a73072,0xaa3eb3d9,0x33d92a3e,0xb07232a7,
0xb1a8b1a8,0x31a8b1a8,0x31a831a8,0xb1a831a8,0x3072b2a7,0x33d92a3e,0x2a3e33d9,0xb2a73072,0x33642e1f,0x2e1f3364,0xae1f3364,0xb3642e1f,0xaa3e33d9,0xb07232a7,0xb2a73072,0xb3d92a3e,
0,304,648,952,576,880,1224,1528,1152,1456,1800,2104,1728,2032,104,408,
1728,2032,104,408,32,336,680,984,608,912,1256,1560,1184,1488,1832,2136,
1184,1488,1832,2136,1760,2064,352,1648,64,1360,72,376,64,1360,72,376,
64,1360,72,376,64,1360,72,376,64,1360,72,376,64,1360,72,376,
1296,1600,1944,2248,1872,2176,248,552,176,480,824,1128,752,1056,1400,1704,
752,1056,1400,1704,1328,1632,1976,2280,1904,2208,928,2224,640,1936,216,520,
640,1936,216,520,144,448,792,1096,720,1024,1368,1672,720,1024,1368,1672,
720,1024,1368,1672,720,1024,1368,1672,720,1024,1368,1672,720,1024,1368,1672,
320,624,968,1272,896,1200,1544,1848,1472,1776,2120,496,2048,208,1504,88,
2048,208,1504,88,1216,16,360,664,288,592,936,1240,864,1168,1512,1816,
864,1168,1512,1816,1440,1744,2088,120,2016,48,392,696,2016,48,392,696,
2016,48,392,696,2016,48,392,696,2016,48,392,696,2016,48,392,696,
1616,1920,2264,1072,2192,784,2080,232,1792,160,504,808,432,736,1080,1384,
432,736,1080,1384,1008,1312,1656,1960,1584,1888,2232,264,2160,192,536,840,
2160,192,536,840,464,768,1112,1416,1040,1344,1688,1992,1040,1344,1688,1992,
1040,1344,1688,1992,1040,1344,1688,1992,1040,1344,1688,1992,1040,1344,1688,1992,
152,456,800,1104,728,1032,1376,1680,1304,1608,1952,2256,1880,2184,256,560,
1880,2184,256,560,184,488,832,1136,760,1064,1408,1712,1336,1640,1984,2288,
1336,1640,1984,2288,1912,2216,1000,2296,712,2008,224,528,712,2008,224,528,
712,2008,224,528,712,2008,224,528,712,2008,224,528,712,2008,224,528,
1448,1752,2096,128,2024,56,400,704,328,632,976,1280,904,1208,1552,1856,
904,1208,1552,1856,1480,1784,2128,568,2056,280,1576,96,1288,24,368,672,
1288,24,368,672,296,600,944,1248,872,1176,1520,1824,872,1176,1520,1824,
872,1176,1520,1824,872,1176,1520,1824,872,1176,1520,1824,872,1176,1520,1824,
472,776,1120,1424,1048,1352,1696,2000,1624,1928,2272,1144,2200,856,2152,240,
2200,856,2152,240,1864,168,512,816,440,744,1088,1392,1016,1320,1664,1968,
1016,1320,1664,1968,1592,1896,2240,272,2168,200,544,848,2168,200,544,848,
2168,200,544,848,2168,200,544,848,2168,200,544,848,2168,200,544,848,
1768,2072,424,1720,136,1432,80,384,8,312,656,960,584,888,1232,1536,
584,888,1232,1536,1160,1464,1808,2112,1736,2040,112,416,40,344,688,992,
40,344,688,992,616,920,1264,1568,1192,1496,1840,2144,1192,1496,1840,2144,
1192,1496,1840,2144,1192,1496,1840,2144,1192,1496,1840,2144,1192,1496,1840,2144,
9920,10176,9792,0,12232,12488,12104,16,10818,10562,10946,32,13130,12874,13258,48,
9794,10050,10242,64,12106,12362,12554,80,10944,10688,10496,96,13256,13000,12808,112,
10244,9924,10116,128,12556,12236,12428,144,10494,10814,10622,160,12806,13126,12934,176,
10118,9798,9990,192,12430,12110,12302,208,10620,10940,10748,224,12932,13252,13060,240,
9992,10248,9864,4608,12304,12560,12176,4624,10746,10490,10874,4640,13058,12802,13186,4656,
9866,10122,10314,4672,12178,12434,12626,4688,10872,10616,10424,4704,13184,12928,12736,4720,
10316,9996,10188,4736,12628,12308,12500,4752,10422,10742,10550,4768,12734,13054,12862,4784,
10190,9870,10062,4800,12502,12182,12374,4816,10548,10868,10676,4832,12860,13180,12988,4848,
10064,10320,9936,1048576,12376,12632,12248,496,10674,10418,10802,480,12986,12730,13114,464,
9938,10194,9810,448,12250,12506,12122,432,10800,10544,10928,416,13112,12856,13240,400,
9812,10068,10260,384,12124,12380,12572,368,10926,10670,10478,352,13238,12982,12790,336,
10262,9942,10134,320,12574,12254,12446,304,10476,10796,10604,288,12788,13108,12916,272,
10136,9816,10008,1048576,12448,12128,12320,5104,10602,10922,10730,5088,12914,13234,13042,5072,
10010,10266,9882,5056,12322,12578,12194,5040,10728,10472,10856,5024,13040,12784,13168,5008,
9884,10140,10332,4992,12196,12452,12644,4976,10854,10598,10406,4960,13166,12910,12718,4944,
10334,10014,10206,4928,12646,12326,12518,4912,10404,10724,10532,4896,12716,13036,12844,4880,
10208,9888,10080,256,12520,12200,12392,1048576,10530,10850,10658,1048576,12842,13162,12970,1048576,
10082,10338,10368,1048576,12394,12650,12680,1048576,10656,10400,10370,1048576,12968,12712,12682,1048576,
9956,10212,10368,1048576,12268,12524,12680,1048576,10782,10526,10370,1048576,13094,12838,12682,1048576,
9830,10086,10368,1048576,12142,12398,12680,1048576,10908,10652,10370,1048576,13220,12964,12682,1048576,
10280,9960,10368,4864,12592,12272,12680,1048576,10458,10778,10370,1048576,12770,13090,12682,1048576,
10154,9834,10368,1048576,12466,12146,12680,1048576,10584,10904,10370,1048576,12896,13216,12682,1048576,
10028,10284,10368,1048576,12340,12596,12680,1048576,10710,10454,10370,1048576,13022,12766,12682,1048576,
9902,10158,10368,1048576,12214,12470,12680,1048576,10836,10580,10370,1048576,13148,12892,12682,1048576,
10352,10032,10368,1048576,12664,12344,12680,1048576,10386,10706,10370,1048576,12698,13018,12682,1048576,
10226,9906,10368,1048576,12538,12218,12680,1048576,10512,10832,10370,1048576,12824,13144,12682,1048576,
10100,10356,10368,1048576,12412,12668,12680,1048576,10638,10382,10370,1048576,12950,12694,12682,1048576,
9974,10230,10368,1048576,12286,12542,12680,1048576,10764,10508,10370,1048576,13076,12820,12682,1048576,
9848,10104,10368,1048576,12160,12416,12680,1048576,10890,10634,10370,1048576,13202,12946,12682,1048576,
10298,9978,10368,1048576,12610,12290,12680,1048576,10440,10760,10370,1048576,12752,13072,12682,1048576,
10172,9852,10368,1048576,12484,12164,12680,1048576,10566,10886,10370,1048576,12878,13198,12682,1048576,
10046,10302,10368,1048576,12358,12614,12680,1048576,10692,10436,10370,1048576,13004,12748,12682,1048576,
10048,10304,10368,1048576,12360,12616,12680,1048576,10690,10434,10370,1048576,13002,12746,12682,1048576,
9922,10178,10368,1048576,12234,12490,12680,1048576,10816,10560,10370,1048576,13128,12872,12682,1048576,
9796,10052,10368,1048576,12108,12364,12680,1048576,10942,10686,10370,1048576,13254,12998,12682,1048576,
10246,9926,10368,1048576,12558,12238,12680,1048576,10492,10812,10370,1048576,12804,13124,12682,1048576,
10120,9800,10368,1048576,12432,12112,12680,1048576,10618,10938,10370,1048576,12930,13250,12682,1048576,
9994,10250,10368,1048576,12306,12562,12680,1048576,10744,10488,10370,1048576,13056,12800,12682,1048576,
9868,10124,10368,1048576,12180,12436,12680,1048576,10870,10614,10370,1048576,13182,12926,12682,1048576,
10318,9998,10368,1048576,12630,12310,12680,1048576,10420,10740,10370,1048576,12732,13052,12682,1048576,
10192,9872,10368,1048576,12504,12184,12680,1048576,10546,10866,10370,1048576,12858,13178,12682,1048576,
10066,10322,10368,1048576,12378,12634,12680,1048576,10672,10416,10370,1048576,12984,12728,12682,1048576,
9940,10196,10368,1048576,12252,12508,12680,1048576,10798,10542,10370,1048576,13110,12854,12682,1048576,
9814,10070,10368,1048576,12126,12382,12680,1048576,10924,10668,10370,1048576,13236,12980,12682,1048576,
10264,9944,10368,1048576,12576,12256,12680,1048576,10474,10794,10370,1048576,12786,13106,12682,1048576,
10138,9818,10368,1048576,12450,12130,12680,1048576,10600,10920,10370,1048576,12912,13232,12682,1048576,
10012,10268,10368,1048576,12324,12580,12680,1048576,10726,10470,10370,1048576,13038,12782,12682,1048576,
9886,10142,10368,1048576,12198,12454,12680,1048576,10852,10596,10370,1048576,13164,12908,12682,1048576,
10336,10016,10368,1048576,12648,12328,12680,1048576,10402,10722,10370,1048576,12714,13034,12682,1048576,
10210,9890,10368,1048576,12522,12202,12680,1048576,10528,10848,10370,1048576,12840,13160,12682,1048576,
10084,10340,10368,1048576,12396,12652,12680,1048576,10654,10398,10370,1048576,12966,12710,12682,1048576,
9958,10214,10368,1048576,12270,12526,12680,1048576,10780,10524,10370,1048576,13092,12836,12682,1048576,
9832,10088,10368,1048576,12144,12400,12680,1048576,10906,10650,10370,1048576,13218,12962,12682,1048576,
10282,9962,10368,1048576,12594,12274,12680,1048576,10456,10776,10370,1048576,12768,13088,12682,1048576,
10156,9836,10368,1048576,12468,12148,12680,1048576,10582,10902,10370,1048576,12894,13214,12682,1048576,
10030,10286,10368,1048576,12342,12598,12680,1048576,10708,10452,10370,1048576,13020,12764,12682,1048576,
9904,10160,10368,1048576,12216,12472,12680,1048576,10834,10578,10370,1048576,13146,12890,12682,1048576,
10354,10034,10368,1048576,12666,12346,12680,1048576,10384,10704,10370,1048576,12696,13016,12682,1048576,
10228,9908,10368,1048576,12540,12220,12680,1048576,10510,10830,10370,1048576,12822,13142,12682,1048576,
10102,10358,10368,1048576,12414,12670,12680,1048576,10636,10380,10370,1048576,12948,12692,12682,1048576,
9976,10232,10368,1048576,12288,12544,12680,1048576,10762,10506,10370,1048576,13074,12818,12682,1048576,
9850,10106,10368,1048576,12162,12418,12680,1048576,10888,10632,10370,1048576,13200,12944,12682,1048576,
10300,9980,10368,1048576,12612,12292,12680,1048576,10438,10758,10370,1048576,12750,13070,12682,1048576,
10174,9854,10368,1048576,12486,12166,12680,1048576,10564,10884,10370,1048576,12876,13196,12682,1048576,
0,512,1536,0,16,528,1552,16,32,544,1568,32,48,560,1584,48,
64,576,1600,64,80,592,1616,80,96,608,1632,96,112,624,1648,112,
128,640,1664,128,144,656,1680,144,160,672,1696,160,176,688,1712,176,
192,704,1728,192,208,720,1744,208,224,736,1760,224,240,752,1776,240,
1536,5120,6144,4608,1552,5136,6160,4624,1568,5152,6176,4640,1584,5168,6192,4656,
1600,5184,6208,4672,1616,5200,6224,4688,1632,5216,6240,4704,1648,5232,6256,4720,
1664,5248,6272,4736,1680,5264,6288,4752,1696,5280,6304,4768,1712,5296,6320,4784,
1728,5312,6336,4800,1744,5328,6352,4816,1760,5344,6368,4832,1776,5360,6384,4848,
3072,4096,3072,0,3088,4592,3568,496,3104,4576,3552,480,3120,4560,3536,464,
3136,4544,3520,448,3152,4528,3504,432,3168,4512,3488,416,3184,4496,3472,400,
3200,4480,3456,384,3216,4464,3440,368,3232,4448,3424,352,3248,4432,3408,336,
3264,4416,3392,320,3280,4400,3376,304,3296,4384,3360,288,3312,4368,3344,272,
3072,8704,7680,4608,3088,9200,8176,5104,3104,9184,8160,5088,3120,9168,8144,5072,
3136,9152,8128,5056,3152,9136,8112,5040,3168,9120,8096,5024,3184,9104,8080,5008,
3200,9088,8064,4992,3216,9072,8048,4976,3232,9056,8032,4960,3248,9040,8016,4944,
3264,9024,8000,4928,3280,9008,7984,4912,3296,8992,7968,4896,3312,8976,7952,4880,
8,768,1792,256,24,784,1808,272,40,800,1824,288,56,816,1840,304,
72,832,1856,320,88,848,1872,336,104,864,1888,352,120,880,1904,368,
136,896,1920,384,152,912,1936,400,168,928,1952,416,184,944,1968,432,
200,960,1984,448,216,976,2000,464,232,992,2016,480,248,1008,2032,496,
1544,5376,6400,4864,1560,5392,6416,4880,1576,5408,6432,4896,1592,5424,6448,4912,
1608,5440,6464,4928,1624,5456,6480,4944,1640,5472,6496,4960,1656,5488,6512,4976,
1672,5504,6528,4992,1688,5520,6544,5008,1704,5536,6560,5024,1720,5552,6576,5040,
1736,5568,6592,5056,1752,5584,6608,5072,1768,5600,6624,5088,1784,5616,6640,5104,
3080,4352,3328,256,3096,4336,3312,240,3112,4320,3296,224,3128,4304,3280,208,
3144,4288,3264,192,3160,4272,3248,176,3176,4256,3232,160,3192,4240,3216,144,
3208,4224,3200,128,3224,4208,3184,112,3240,4192,3168,96,3256,4176,3152,80,
3272,4160,3136,64,3288,4144,3120,48,3304,4128,3104,32,3320,4112,3088,16,
3080,8960,7936,4864,3096,8944,7920,4848,3112,8928,7904,4832,3128,8912,7888,4816,
3144,8896,7872,4800,3160,8880,7856,4784,3176,8864,7840,4768,3192,8848,7824,4752,
3208,8832,7808,4736,3224,8816,7792,4720,3240,8800,7776,4704,3256,8784,7760,4688,
3272,8768,7744,4672,3288,8752,7728,4656,3304,8736,7712,4640,3320,8720,7696,4624,
4608,1024,2048,0,4624,1040,2064,16,4640,1056,2080,32,4656,1072,2096,48,
4672,1088,2112,64,4688,1104,2128,80,4704,1120,2144,96,4720,1136,2160,112,
4736,1152,2176,128,4752,1168,2192,144,4768,1184,2208,160,4784,1200,2224,176,
4800,1216,2240,192,4816,1232,2256,208,4832,1248,2272,224,4848,1264,2288,240,
6144,5632,6656,4608,6160,5648,6672,4624,6176,5664,6688,4640,6192,5680,6704,4656,
6208,5696,6720,4672,6224,5712,6736,4688,6240,5728,6752,4704,6256,5744,6768,4720,
6272,5760,6784,4736,6288,5776,6800,4752,6304,5792,6816,4768,6320,5808,6832,4784,
6336,5824,6848,4800,6352,5840,6864,4816,6368,5856,6880,4832,6384,5872,6896,4848,
7680,3584,2560,0,7696,4080,3056,496,7712,4064,3040,480,7728,4048,3024,464,
7744,4032,3008,448,7760,4016,2992,432,7776,4000,2976,416,7792,3984,2960,400,
7808,3968,2944,384,7824,3952,2928,368,7840,3936,2912,352,7856,3920,2896,336,
7872,3904,2880,320,7888,3888,2864,304,7904,3872,2848,288,7920,3856,2832,272,
7680,8192,7168,4608,7696,8688,7664,5104,7712,8672,7648,5088,7728,8656,7632,5072,
7744,8640,7616,5056,7760,8624,7600,5040,7776,8608,7584,5024,7792,8592,7568,5008,
7808,8576,7552,4992,7824,8560,7536,4976,7840,8544,7520,4960,7856,8528,7504,4944,
7872,8512,7488,4928,7888,8496,7472,4912,7904,8480,7456,4896,7920,8464,7440,4880,
4616,1280,2304,256,4632,1296,2320,272,4648,1312,2336,288,4664,1328,2352,304,
4680,1344,2368,320,4696,1360,2384,336,4712,1376,2400,352,4728,1392,2416,368,
4744,1408,2432,384,4760,1424,2448,400,4776,1440,2464,416,4792,1456,2480,432,
4808,1472,2496,448,4824,1488,2512,464,4840,1504,2528,480,4856,1520,2544,496,
6152,5888,6912,4864,6168,5904,6928,4880,6184,5920,6944,4896,6200,5936,6960,4912,
6216,5952,6976,4928,6232,5968,6992,4944,6248,5984,7008,4960,6264,6000,7024,4976,
6280,6016,7040,4992,6296,6032,7056,5008,6312,6048,7072,5024,6328,6064,7088,5040,
6344,6080,7104,5056,6360,6096,7120,5072,6376,6112,7136,5088,6392,6128,7152,5104,
7688,3840,2816,256,7704,3824,2800,240,7720,3808,2784,224,7736,3792,2768,208,
7752,3776,2752,192,7768,3760,2736,176,7784,3744,2720,160,7800,3728,2704,144,
7816,3712,2688,128,7832,3696,2672,112,7848,3680,2656,96,7864,3664,2640,80,
7880,3648,2624,64,7896,3632,2608,48,7912,3616,2592,32,7928,3600,2576,16,
7688,8448,7424,4864,7704,8432,7408,4848,7720,8416,7392,4832,7736,8400,7376,4816,
7752,8384,7360,4800,7768,8368,7344,4784,7784,8352,7328,4768,7800,8336,7312,4752,
7816,8320,7296,4736,7832,8304,7280,4720,7848,8288,7264,4704,7864,8272,7248,4688,
7880,8256,7232,4672,7896,8240,7216,4656,7912,8224,7200,4640,7928,8208,7184,4624,
4,68,1760,0,12,140,1768,16,20,212,1776,32,28,284,1784,48,
76,356,1832,64,84,428,1840,80,92,500,1848,96,100,572,1856,112,
148,644,1904,128,156,716,1912,144,164,788,1920,160,172,860,1928,176,
220,932,1976,192,228,1004,1984,208,236,1076,1992,224,244,1148,2000,240,
292,1220,2048,1536,300,1292,2056,1552,308,1364,2064,1568,316,1436,2072,1584,
364,1508,2120,1600,372,1580,2128,1616,380,1652,2136,1632,388,1724,2144,1648,
436,1796,2192,1664,444,1868,2200,1680,452,1940,2208,1696,460,2012,2216,1712,
508,2084,2264,1728,516,2156,2272,1744,524,2228,2280,1760,532,2300,2288,1776,
580,0,64,3072,588,8,136,3088,596,16,208,3104,604,24,280,3120,
652,72,352,3136,660,80,424,3152,668,88,496,3168,676,96,568,3184,
724,144,640,3200,732,152,712,3216,740,160,784,3232,748,168,856,3248,
796,216,928,3264,804,224,1000,3280,812,232,1072,3296,820,240,1144,3312,
868,288,1216,1048576,876,296,1288,1048576,884,304,1360,1048576,892,312,1432,1048576,
940,360,1504,1048576,948,368,1576,1048576,956,376,1648,1048576,964,384,1720,1048576,
1012,432,1792,1048576,1020,440,1864,1048576,1028,448,1936,1048576,1036,456,2008,1048576,
1084,504,2080,1048576,1092,512,2152,1048576,1100,520,2224,1048576,1108,528,2296,1048576,
1156,576,1760,8,1164,584,1768,24,1172,592,1776,40,1180,600,1784,56,
1228,648,1832,72,1236,656,1840,88,1244,664,1848,104,1252,672,1856,120,
1300,720,1904,136,1308,728,1912,152,1316,736,1920,168,1324,744,1928,184,
1372,792,1976,200,1380,800,1984,216,1388,808,1992,232,1396,816,2000,248,
1444,864,2048,1544,1452,872,2056,1560,1460,880,2064,1576,1468,888,2072,1592,
1516,936,2120,1608,1524,944,2128,1624,1532,952,2136,1640,1540,960,2144,1656,
1588,1008,2192,1672,1596,1016,2200,1688,1604,1024,2208,1704,1612,1032,2216,1720,
1660,1080,2264,1736,1668,1088,2272,1752,1676,1096,2280,1768,1684,1104,2288,1784,
1732,1152,64,3080,1740,1160,136,3096,1748,1168,208,3112,1756,1176,280,3128,
1804,1224,352,3144,1812,1232,424,3160,1820,1240,496,3176,1828,1248,568,3192,
1876,1296,640,3208,1884,1304,712,3224,1892,1312,784,3240,1900,1320,856,3256,
1948,1368,928,3272,1956,1376,1000,3288,1964,1384,1072,3304,1972,1392,1144,3320,
2020,1440,1216,1048576,2028,1448,1288,1048576,2036,1456,1360,1048576,2044,1464,1432,1048576,
2092,1512,1504,1048576,2100,1520,1576,1048576,2108,1528,1648,1048576,2116,1536,1720,1048576,
2164,1584,1792,1048576,2172,1592,1864,1048576,2180,1600,1936,1048576,2188,1608,2008,1048576,
2236,1656,2080,1048576,2244,1664,2152,1048576,2252,1672,2224,1048576,2260,1680,2296,1048576,
36,1728,1760,4608,44,1736,1768,4624,52,1744,1776,4640,60,1752,1784,4656,
108,1800,1832,4672,116,1808,1840,4688,124,1816,1848,4704,132,1824,1856,4720,
180,1872,1904,4736,188,1880,1912,4752,196,1888,1920,4768,204,1896,1928,4784,
252,1944,1976,4800,260,1952,1984,4816,268,1960,1992,4832,276,1968,2000,4848,
324,2016,2048,6144,332,2024,2056,6160,340,2032,2064,6176,348,2040,2072,6192,
396,2088,2120,6208,404,2096,2128,6224,412,2104,2136,6240,420,2112,2144,6256,
468,2160,2192,6272,476,2168,2200,6288,484,2176,2208,6304,492,2184,2216,6320,
540,2232,2264,6336,548,2240,2272,6352,556,2248,2280,6368,564,2256,2288,6384,
612,32,64,7680,620,40,136,7696,628,48,208,7712,636,56,280,7728,
684,104,352,7744,692,112,424,7760,700,120,496,7776,708,128,568,7792,
756,176,640,7808,764,184,712,7824,772,192,784,7840,780,200,856,7856,
828,248,928,7872,836,256,1000,7888,844,264,1072,7904,852,272,1144,7920,
900,320,1216,1048576,908,328,1288,1048576,916,336,1360,1048576,924,344,1432,1048576,
972,392,1504,1048576,980,400,1576,1048576,988,408,1648,1048576,996,416,1720,1048576,
1044,464,1792,1048576,1052,472,1864,1048576,1060,480,1936,1048576,1068,488,2008,1048576,
1116,536,2080,1048576,1124,544,2152,1048576,1132,552,2224,1048576,1140,560,2296,1048576,
1188,608,1760,4616,1196,616,1768,4632,1204,624,1776,4648,1212,632,1784,4664,
1260,680,1832,4680,1268,688,1840,4696,1276,696,1848,4712,1284,704,1856,4728,
1332,752,1904,4744,1340,760,1912,4760,1348,768,1920,4776,1356,776,1928,4792,
1404,824,1976,4808,1412,832,1984,4824,1420,840,1992,4840,1428,848,2000,4856,
1476,896,2048,6152,1484,904,2056,6168,1492,912,2064,6184,1500,920,2072,6200,
1548,968,2120,6216,1556,976,2128,6232,1564,984,2136,6248,1572,992,2144,6264,
1620,1040,2192,6280,1628,1048,2200,6296,1636,1056,2208,6312,1644,1064,2216,6328,
1692,1112,2264,6344,1700,1120,2272,6360,1708,1128,2280,6376,1716,1136,2288,6392,
1764,1184,64,7688,1772,1192,136,7704,1780,1200,208,7720,1788,1208,280,7736,
1836,1256,352,7752,1844,1264,424,7768,1852,1272,496,7784,1860,1280,568,7800,
1908,1328,640,7816,1916,1336,712,7832,1924,1344,784,7848,1932,1352,856,7864,
1980,1400,928,7880,1988,1408,1000,7896,1996,1416,1072,7912,2004,1424,1144,7928,
2052,1472,1216,1048576,2060,1480,1288,1048576,2068,1488,1360,1048576,2076,1496,1432,1048576,
2124,1544,1504,1048576,2132,1552,1576,1048576,2140,1560,1648,1048576,2148,1568,1720,1048576,
2196,1616,1792,1048576,2204,1624,1864,1048576,2212,1632,1936,1048576,2220,1640,2008,1048576,
2268,1688,2080,1048576,2276,1696,2152,1048576,2284,1704,2224,1048576,2292,1712,2296,1048576,
};
#define ASM_ALL \
"s_load_dwordx2 s[12:13],s[0:1],0\ns_load_dwordx4 s[16:19],s[0:1],8\ns_load_dwordx2 s[36:37],s[0:1],48\ns_load_dwordx2 s[8:9],s[0:1],24\n" \
"s_load_dwordx2 s[20:21],s[0:1],32\ns_load_dwordx2 s[24:25],s[0:1],40\ns_getpc_b64 s[96:97]\n.Lc0_%=:\n" \
"v_and_b32 v1,0xe0,v0\nv_lshlrev_b32 v2,7,v0\ns_lshr_b32 s33,s2,3\ns_mov_b32 s98,.Lend_%=-.Lc0_%=\n" \
"s_mov_b32 s99,0x20000\ns_sub_u32 s95,s33,9\nv_lshl_or_b32 v1,v1,24,v2\ns_lshl_b32 s95,s95,12\n" \
"s_mov_b32 m0,162176\nv_readfirstlane_b32 s3,v0\nbuffer_load_dword v1,s[96:99],s95 offen lds\ns_and_b32 s32,s2,7\n" \
"s_mov_b32 s15,0x20000\ns_mov_b32 s14,2359296\ns_lshr_b32 s3,s3,6\ns_lshl_b32 s34,s32,5\n" \
"v_and_b32 v13,31,v0\nv_bfe_u32 v12,v0,5,1\ns_or_b32 s34,s34,s33\nv_mul_u32_u24 v12,1179648,v12\n" \
"s_lshl_b32 s100,s3,8\nv_lshlrev_b32 v2,4,v0\ns_add_u32 s100,s100,s34\nv_lshl_add_u32 v15,v13,4,v12\n" \
"s_mul_i32 s89,s100,1152\nv_lshl_add_u32 v13,v13,2,v12\nv_mov_b32 v12,v15\nv_and_b32 v1,0x3f0,v2\n" \
"s_mov_b32 s38,134217728\ns_mov_b32 s39,0x20000\ns_cmp_gt_u32 s2,71\ns_cbranch_scc1 .Lnp_%=\n" \
"s_and_b32 s95,s32,3\ns_lshr_b32 s100,s32,2\ns_mul_i32 s95,s95,9\ns_mul_i32 s101,s100,576\n" \
"s_add_u32 s95,s95,s33\ns_mul_i32 s100,s100,1990656\ns_lshl_b32 s95,s95,6\ns_mov_b32 s22,576\n" \
"s_mov_b32 s23,0x20000\ns_mov_b32 s26,1990656\ns_mov_b32 s27,0x20000\ns_waitcnt lgkmcnt(0)\n" \
"s_add_u32 s20,s20,s101\ns_addc_u32 s21,s21,0\ns_add_u32 s24,s24,s100\ns_addc_u32 s25,s25,0\n" \
"buffer_load_dwordx2 v[126:127],off,s[36:39],0 sc1\nv_lshrrev_b32 v120,2,v0\nv_and_b32 v121,3,v0\nv_mul_u32_u24 v122,6912,v120\n" \
"v_lshlrev_b32 v123,3,v120\nv_lshl_add_u32 v121,v121,4,s95\nbuffer_load_dwordx2 v[24:25],v123,s[20:23],0 offen\nv_lshlrev_b32 v120,1,v120\n" \
"v_lshl_add_u32 v123,v122,2,v121\nbuffer_load_ushort v28,v120,s[20:23],0 offen offset:512\nv_add_u32 v121,v121,v122\nbuffer_load_dwordx4 v[80:83],v123,s[24:27],0 offen\n" \
"s_mov_b32 s95,6912\nbuffer_load_dwordx4 v[84:87],v123,s[24:27],s95 offen\ns_mov_b32 s95,13824\nbuffer_load_dwordx4 v[88:91],v123,s[24:27],s95 offen\n" \
"s_mov_b32 s95,20736\nbuffer_load_dwordx4 v[92:95],v123,s[24:27],s95 offen\ns_mov_b32 s95,1769472\nbuffer_load_dwordx4 v[96:99],v121,s[24:27],s95 offen\n" \
"s_branch .Lnq_%=\n.Lnp_%=:\ns_waitcnt lgkmcnt(0)\nbuffer_load_dwordx2 v[126:127],off,s[36:39],0 sc1\n" \
".Lnq_%=:\ns_memrealtime s[30:31]\nbuffer_load_dwordx4 v[224:227],v12,s[12:15],s89 offen\nbuffer_load_dwordx4 v[228:231],v12,s[12:15],s89 offen offset:512\n" \
"buffer_load_dword v232,v13,s[12:15],s89 offen offset:1024\ns_cmp_lt_u32 s3,2\ns_cselect_b64 s[16:17],s[16:17],s[18:19]\ns_mov_b32 s18,1183744\n" \
"s_mov_b32 s19,0x20000\ns_mul_i32 s100,s34,4624\nv_and_b32 v16,0x7f0,v2\ns_lshr_b32 s95,s3,1\n" \
"s_mul_i32 s95,s95,3168\ns_lshl_b32 s88,s3,10\ns_add_u32 s95,s95,s88\ns_add_u32 m0,s95,9792\n" \
"v_add_u32 v17,s100,v16\nbuffer_load_dwordx4 v16,s[16:19],s100 offen lds\nbuffer_load_dwordx4 v16,s[16:19],s100 offen offset:2048 lds\nv_add_u32 v17,4096,v17\n" \
"v_subrev_u32 v18,1183744,v17\ns_add_u32 m0,s95,13888\nv_min_u32 v17,v17,v18\ns_bitcmp1_b32 s3,0\n" \
"s_cselect_b64 exec,63,-1\nbuffer_load_dwordx4 v17,s[16:19],0 offen lds\ns_mov_b64 exec,-1\ns_mov_b32 s6,22528\n" \
"s_mov_b32 s7,0x20000\ns_mov_b32 s95,10240\nbuffer_load_dwordx4 v[246:249],v2,s[4:7],s95 offen\ns_cmp_gt_u32 s2,110\n" \
"s_cbranch_scc1 .LC_%=\ns_cmp_gt_u32 s2,71\ns_cbranch_scc1 .LN_%=\n.LP_%=:\n" \
"v_bfe_u32 v112,v0,3,2\nv_bfe_u32 v113,v0,5,1\nv_lshl_add_u32 v112,v112,8,v113\ns_lshl_b32 s48,s33,2\n" \
"v_lshrrev_b32 v113,3,v0\ns_add_u32 s48,s48,s3\nv_and_b32 v113,6,v113\nv_and_b32 v114,7,v0\n" \
"s_mul_i32 s48,s48,18432\ns_mul_i32 s49,s32,1152\nv_mul_u32_u24 v112,9216,v112\nv_xor_b32 v113,v113,v114\n" \
"s_add_u32 s48,s48,s49\nv_lshl_add_u32 v4,v113,4,v112\ns_mov_b32 s10,18874368\ns_mov_b32 s11,0x20000\n" \
"s_add_u32 s48,s48,1024\ns_add_u32 m0,s88,20224\ns_add_u32 s50,s48,9437184\nbuffer_load_dwordx4 v4,s[8:11],s48 offen lds\n" \
"s_add_u32 m0,s88,24320\nv_add_u32 v5,17408,v4\nbuffer_load_dwordx4 v4,s[8:11],s50 offen lds\nv_add_u32 v6,34816,v4\n" \
"v_add_u32 v7,52224,v4\nv_lshlrev_b32 v158,3,v0\nv_add_u32 v158,161152,v158\ns_mul_i32 s63,s32,9\n" \
"s_add_u32 s63,s63,s33\nv_bfrev_b32 v159,1\nv_cmp_gt_u32 s[64:65],16,v0\ns_lshl_b32 s86,s63,8\n" \
"s_add_u32 s63,s86,8192\nv_cndmask_b32 v159,v159,v2,s[64:65]\ns_mov_b32 s51,4096\ns_add_u32 m0,s88,28416\n" \
"s_waitcnt vmcnt(15)\nv_lshlrev_b32 v128,16,v24\nv_mul_f32 v129,0xbfb8aa3b,v128\nv_exp_f32 v129,v129\n" \
"v_and_b32 v130,0xffff0000,v24\nv_add_f32 v129,1.0,v129\nv_mul_f32 v131,0xbfb8aa3b,v130\nv_lshlrev_b32 v132,16,v25\n" \
"v_rcp_f32 v129,v129\nv_exp_f32 v131,v131\nv_mul_f32 v133,0xbfb8aa3b,v132\nv_exp_f32 v133,v133\n" \
"v_mul_f32 v128,v128,v129\nv_add_f32 v131,1.0,v131\nv_and_b32 v134,0xffff0000,v25\nv_cvt_pk_bf16_f32 v128,v128,v128\n" \
"v_rcp_f32 v131,v131\nv_add_f32 v133,1.0,v133\nv_mul_f32 v135,0xbfb8aa3b,v134\nv_lshlrev_b32 v128,16,v128\n" \
"s_waitcnt vmcnt(14)\nv_lshlrev_b32 v136,16,v28\nv_rcp_f32 v133,v133\nv_exp_f32 v135,v135\n" \
"v_mul_f32 v137,0xbfb8aa3b,v136\nv_mul_f32 v130,v130,v131\nv_exp_f32 v137,v137\nv_cvt_pk_bf16_f32 v130,v130,v130\n" \
"v_mul_f32 v132,v132,v133\nv_add_f32 v135,1.0,v135\nv_lshlrev_b32 v130,16,v130\nv_cvt_pk_bf16_f32 v132,v132,v132\n" \
"v_rcp_f32 v135,v135\nv_add_f32 v137,1.0,v137\nv_lshlrev_b32 v132,16,v132\nv_rcp_f32 v137,v137\n" \
"v_mul_f32 v134,v134,v135\nv_cvt_pk_bf16_f32 v134,v134,v134\nv_mul_f32 v136,v136,v137\nv_lshlrev_b32 v134,16,v134\n" \
"v_cvt_pk_bf16_f32 v136,v136,v136\nv_lshlrev_b32 v136,16,v136\ns_mov_b32 s52,8192\ns_mov_b32 s53,14336\n" \
"s_mul_i32 s57,s3,30720\ns_add_u32 s57,s57,38272\ns_and_b32 s60,s33,7\ns_mul_i32 s92,s3,589824\n" \
"s_add_u32 s92,s92,s49\ns_lshl_b32 s62,s60,7\ns_add_u32 s92,s92,s62\ns_load_dwordx4 s[44:47],s[0:1],72\n" \
"v_mov_b32 v16,-1\nv_mov_b32 v20,0\nv_mov_b32 v17,-1\nv_mov_b32 v21,0\n" \
"v_mov_b32 v18,-1\nv_mov_b32 v22,0\nv_mov_b32 v19,-1\nv_mov_b32 v23,0\n" \
"s_mul_i32 s54,s34,9344\ns_add_u32 s35,s54,524288\ns_add_u32 s55,s35,4096\nv_and_b32 v112,15,v0\n" \
"v_bfe_u32 v113,v0,4,2\nv_mul_u32_u24 v113,589824,v113\nv_lshl_add_u32 v113,v112,4,v113\ns_lshl_b32 s56,s34,8\n" \
"s_add_u32 s56,s56,10289152\ns_cmp_eq_u32 s3,2\ns_cselect_b32 s56,s56,2147483648\nv_add_u32 v113,s56,v113\n" \
"s_add_u32 s58,s35,8192\nv_add_u32 v114,s58,v2\nv_cmp_gt_u32 vcc,68,v0\ns_lshr_b32 s59,s33,3\n" \
"s_lshl_b32 s59,s59,3\nv_cndmask_b32 v113,v113,v114,vcc\ns_add_u32 s59,s59,s32\ns_lshl_b32 s59,s59,3\n" \
"s_add_u32 s59,s59,s60\ns_lshl_b32 s59,s59,7\ns_add_u32 s87,s59,262144\nv_lshrrev_b32 v115,3,v0\n" \
"v_and_b32 v116,0x70,v2\nv_lshl_add_u32 v115,v115,16,v116\nv_add_u32 v115,s87,v115\ns_lshl_b32 s61,s34,4\n" \
"s_add_u32 s61,s61,65536\nv_mov_b32 v116,s61\nv_cmp_gt_u32 vcc,24,v0\nv_and_b32 v118,15,v0\n" \
"v_bfe_u32 v119,v0,4,2\nv_cndmask_b32 v115,v116,v115,vcc\nv_bfrev_b32 v116,1\nv_cmp_gt_u32 vcc,25,v0\n" \
"v_lshlrev_b32 v3,4,v119\nv_lshl_add_u32 v3,v118,6,v3\nv_cndmask_b32 v115,v116,v115,vcc\nv_lshrrev_b32 v15,1,v1\n" \
"v_add_u32 v15,s57,v15\nv_and_b32 v120,0x3c0,v1\nv_bfe_u32 v121,v0,3,2\nv_and_b32 v122,3,v0\n" \
"v_lshlrev_b32 v120,1,v120\nv_lshl_add_u32 v120,v121,5,v120\nv_lshl_add_u32 v120,v122,3,v120\nv_xor_b32 v8,0,v120\n" \
"v_add_u32 v8,s57,v8\nv_xor_b32 v9,32,v120\nv_add_u32 v9,s57,v9\nv_xor_b32 v10,64,v120\n" \
"v_add_u32 v10,s57,v10\nv_xor_b32 v11,96,v120\nv_add_u32 v11,s57,v11\nv_cvt_f32_u32 v120,s34\n" \
"v_lshrrev_b32 v48,1,v118\nv_cvt_f32_u32 v48,v48\nv_and_b32 v49,1,v119\nv_cvt_f32_u32 v49,v49\n" \
"v_mul_f32 v48,0xbe000000,v48\nv_mul_f32 v50,0x3b800000,v120\nv_fma_f32 v49,v49,2.0,v50\nv_mul_f32 v49,v48,v49\n" \
"v_lshrrev_b32 v50,1,v119\nv_and_b32 v51,1,v0\nv_cmp_eq_u32 vcc,v50,v51\nv_mov_b32 v50,0x3e800000\n" \
"v_lshlrev_b32 v51,31,v0\nv_cndmask_b32 v52,0,v50,vcc\nv_xor_b32 v54,v51,v52\nv_cos_f32 v56,v49\n" \
"v_sin_f32 v57,v49\nv_fmamk_f32 v63,v48,0x40800000,v49\nv_pk_mul_f32 v[58:59],v[56:57],v[52:53] op_sel_hi:[1,0]\nv_pk_mul_f32 v[60:61],v[56:57],v[54:55] op_sel_hi:[1,0]\n" \
"v_cvt_pk_f16_f32 v40,v58,-v61\nv_cvt_pk_f16_f32 v44,v59,v60\nv_cos_f32 v62,v63\nv_sin_f32 v63,v63\n" \
"v_fmamk_f32 v57,v48,0x3f800000,v49\nv_pk_mul_f32 v[64:65],v[62:63],v[52:53] op_sel_hi:[1,0]\nv_pk_mul_f32 v[66:67],v[62:63],v[54:55] op_sel_hi:[1,0]\nv_cvt_pk_f16_f32 v41,v64,-v67\n" \
"v_cvt_pk_f16_f32 v45,v65,v66\nv_cos_f32 v56,v57\nv_sin_f32 v57,v57\nv_fmamk_f32 v63,v48,0x40a00000,v49\n" \
"v_pk_mul_f32 v[58:59],v[56:57],v[52:53] op_sel_hi:[1,0]\nv_pk_mul_f32 v[60:61],v[56:57],v[54:55] op_sel_hi:[1,0]\nv_cvt_pk_f16_f32 v42,v58,-v61\nv_cvt_pk_f16_f32 v46,v59,v60\n" \
"v_cos_f32 v62,v63\nv_sin_f32 v63,v63\nv_bfe_u32 v100,v0,1,2\nv_pk_mul_f32 v[64:65],v[62:63],v[52:53] op_sel_hi:[1,0]\n" \
"v_pk_mul_f32 v[66:67],v[62:63],v[54:55] op_sel_hi:[1,0]\nv_cvt_pk_f16_f32 v43,v64,-v67\nv_cvt_pk_f16_f32 v47,v65,v66\nv_and_b32 v101,1,v0\n" \
"v_lshl_add_u32 v100,v101,2,v100\nv_cvt_f32_u32 v100,v100\nv_mul_f32 v101,0x3a000000,v120\nv_fmamk_f32 v100,v100,0x3e000000,v101\n" \
"v_cvt_f32_u32 v101,v119\nv_add_f32 v101,v101,v101\nv_mul_f32 v101,v101,v100\nv_cmp_gt_u32 vcc,8,v118\n" \
"v_cos_f32 v106,v101\nv_sin_f32 v107,v101\nv_cndmask_b32 v102,0,0.5,vcc\nv_sub_f32_e64 v104,0.5,v102\n" \
"v_pk_mul_f32 v[108:109],v[106:107],v[102:103] op_sel_hi:[1,0]\nv_pk_mul_f32 v[110:111],v[106:107],v[104:105] op_sel_hi:[1,0]\nv_cvt_pk_f16_f32 v216,v108,-v109\nv_cvt_pk_f16_f32 v220,v109,v108\n" \
"v_cvt_pk_f16_f32 v217,v110,-v111\nv_cvt_pk_f16_f32 v221,-v111,-v110\nv_fmamk_f32 v107,v100,0x3f800000,v101\nv_cos_f32 v106,v107\n" \
"v_sin_f32 v107,v107\ns_nop 0\nv_pk_mul_f32 v[108:109],v[106:107],v[102:103] op_sel_hi:[1,0]\nv_pk_mul_f32 v[110:111],v[106:107],v[104:105] op_sel_hi:[1,0]\n" \
"v_cvt_pk_f16_f32 v218,v108,-v109\nv_cvt_pk_f16_f32 v222,v109,v108\nv_cvt_pk_f16_f32 v219,v110,-v111\nv_cvt_pk_f16_f32 v223,-v111,-v110\n" \
"v_readfirstlane_b32 s28,v126\nv_readfirstlane_b32 s29,v127\ns_add_u32 s28,s28,1\ns_addc_u32 s29,s29,0\n" \
"v_mov_b32 v168,s28\nv_mov_b32 v169,s29\ns_waitcnt vmcnt(13)\nv_and_b32 v149,0xffff0000,v80\n" \
"v_and_b32 v151,0xffff0000,v81\nv_lshlrev_b32 v148,16,v80\nv_lshlrev_b32 v150,16,v81\nv_pk_mul_f32 v[140:141],v[148:149],v[128:129] op_sel_hi:[1,0]\n" \
"v_pk_mul_f32 v[142:143],v[150:151],v[128:129] op_sel_hi:[1,0]\nv_and_b32 v149,0xffff0000,v82\nv_and_b32 v151,0xffff0000,v83\nv_lshlrev_b32 v148,16,v82\n" \
"v_lshlrev_b32 v150,16,v83\nv_pk_mul_f32 v[144:145],v[148:149],v[128:129] op_sel_hi:[1,0]\nv_pk_mul_f32 v[146:147],v[150:151],v[128:129] op_sel_hi:[1,0]\ns_waitcnt vmcnt(12)\n" \
"v_and_b32 v153,0xffff0000,v84\nv_and_b32 v155,0xffff0000,v85\nv_lshlrev_b32 v152,16,v84\nv_lshlrev_b32 v154,16,v85\n" \
"v_pk_fma_f32 v[140:141],v[152:153],v[130:131],v[140:141] op_sel_hi:[1,0,1]\nv_pk_fma_f32 v[142:143],v[154:155],v[130:131],v[142:143] op_sel_hi:[1,0,1]\ns_waitcnt vmcnt(11)\nv_and_b32 v149,0xffff0000,v88\n" \
"v_and_b32 v151,0xffff0000,v89\nv_lshlrev_b32 v148,16,v88\nv_lshlrev_b32 v150,16,v89\nv_and_b32 v153,0xffff0000,v86\n" \
"v_and_b32 v155,0xffff0000,v87\nv_pk_fma_f32 v[140:141],v[148:149],v[132:133],v[140:141] op_sel_hi:[1,0,1]\nv_pk_fma_f32 v[142:143],v[150:151],v[132:133],v[142:143] op_sel_hi:[1,0,1]\nv_lshlrev_b32 v152,16,v86\n" \
"v_lshlrev_b32 v154,16,v87\nv_pk_fma_f32 v[144:145],v[152:153],v[130:131],v[144:145] op_sel_hi:[1,0,1]\nv_pk_fma_f32 v[146:147],v[154:155],v[130:131],v[146:147] op_sel_hi:[1,0,1]\nv_and_b32 v149,0xffff0000,v90\n" \
"v_and_b32 v151,0xffff0000,v91\nv_lshlrev_b32 v148,16,v90\nv_lshlrev_b32 v150,16,v91\nv_pk_fma_f32 v[144:145],v[148:149],v[132:133],v[144:145] op_sel_hi:[1,0,1]\n" \
"v_pk_fma_f32 v[146:147],v[150:151],v[132:133],v[146:147] op_sel_hi:[1,0,1]\ns_waitcnt vmcnt(10)\nv_and_b32 v153,0xffff0000,v92\nv_and_b32 v155,0xffff0000,v93\n" \
"v_lshlrev_b32 v152,16,v92\nv_lshlrev_b32 v154,16,v93\nv_pk_fma_f32 v[140:141],v[152:153],v[134:135],v[140:141] op_sel_hi:[1,0,1]\nv_pk_fma_f32 v[142:143],v[154:155],v[134:135],v[142:143] op_sel_hi:[1,0,1]\n" \
"s_waitcnt vmcnt(9)\nv_and_b32 v149,0xffff0000,v96\nv_and_b32 v151,0xffff0000,v97\nv_lshlrev_b32 v148,16,v96\n" \
"v_lshlrev_b32 v150,16,v97\nv_and_b32 v153,0xffff0000,v94\nv_and_b32 v155,0xffff0000,v95\nv_pk_fma_f32 v[140:141],v[148:149],v[136:137],v[140:141] op_sel_hi:[1,0,1]\n" \
"v_pk_fma_f32 v[142:143],v[150:151],v[136:137],v[142:143] op_sel_hi:[1,0,1]\nv_lshlrev_b32 v152,16,v94\nv_lshlrev_b32 v154,16,v95\nv_pk_fma_f32 v[144:145],v[152:153],v[134:135],v[144:145] op_sel_hi:[1,0,1]\n" \
"v_pk_fma_f32 v[146:147],v[154:155],v[134:135],v[146:147] op_sel_hi:[1,0,1]\nv_and_b32 v149,0xffff0000,v98\nv_and_b32 v151,0xffff0000,v99\nv_lshlrev_b32 v148,16,v98\n" \
"v_lshlrev_b32 v150,16,v99\nv_pk_fma_f32 v[144:145],v[148:149],v[136:137],v[144:145] op_sel_hi:[1,0,1]\nv_pk_fma_f32 v[146:147],v[150:151],v[136:137],v[146:147] op_sel_hi:[1,0,1]\nv_and_b32 v148,3,v0\n" \
"v_permlane32_swap_b32 v140,v144\nv_permlane32_swap_b32 v141,v145\nv_permlane32_swap_b32 v142,v146\nv_permlane32_swap_b32 v143,v147\n" \
"v_pk_add_f32 v[140:141],v[140:141],v[144:145]\nv_pk_add_f32 v[142:143],v[142:143],v[146:147]\nv_lshrrev_b32 v149,4,v0\nv_and_b32 v150,12,v149\n" \
"v_permlane16_swap_b32 v140,v142\nv_permlane16_swap_b32 v141,v143\nv_lshlrev_b32 v148,5,v148\nv_pk_add_f32 v[140:141],v[140:141],v[142:143]\n" \
"v_mad_u32_u24 v148,v150,24,v148\nv_lshl_add_u32 v148,v149,3,v148\nv_add_f32_dpp v140,v140,v140 row_ror:8 row_mask:0xf bank_mask:0xf bound_ctrl:1\nv_add_f32_dpp v141,v141,v141 row_ror:8 row_mask:0xf bank_mask:0xf bound_ctrl:1\n" \
"v_add_u32 v148,161152,v148\nv_add_f32_dpp v140,v140,v140 row_ror:4 row_mask:0xf bank_mask:0xf bound_ctrl:1\nv_add_f32_dpp v141,v141,v141 row_ror:4 row_mask:0xf bank_mask:0xf bound_ctrl:1\nds_write_b64 v148,v[140:141]\n" \
"s_waitcnt lgkmcnt(0)\ns_barrier\nds_read_b64 v[160:161],v158\nds_read_b64 v[162:163],v158 offset:128\n" \
"ds_read_b64 v[164:165],v158 offset:256\nds_read_b64 v[166:167],v158 offset:384\ns_waitcnt lgkmcnt(2)\nv_pk_add_f32 v[160:161],v[160:161],v[162:163]\n" \
"s_waitcnt lgkmcnt(0)\nv_pk_add_f32 v[164:165],v[164:165],v[166:167]\nv_pk_add_f32 v[170:171],v[160:161],v[164:165]\nbuffer_store_dwordx4 v[168:171],v159,s[36:39],s63 offen sc1\n" \
"buffer_load_dwordx4 v2,s[4:7],0 offen lds\ns_add_u32 m0,s88,32512\ns_nop 0\nbuffer_load_dwordx4 v2,s[4:7],s51 offen lds\n" \
"buffer_load_dwordx4 v[234:237],v1,s[4:7],s52 offen\nbuffer_load_dwordx4 v[238:241],v1,s[4:7],s52 offen offset:1024\nbuffer_load_dwordx4 v[242:245],v2,s[4:7],s53 offen\ns_mov_b32 s52,18432\n" \
"buffer_load_dwordx4 v[252:255],v2,s[4:7],s52 offen\nbuffer_store_dwordx4 v[16:19],v2,s[36:39],s35 offen sc1\nbuffer_store_dwordx4 v[16:19],v2,s[36:39],s55 offen sc1\nbuffer_store_dwordx4 v[16:19],v113,s[36:39],0 offen sc1\n" \
"buffer_store_dwordx4 v[20:23],v115,s[36:39],0 offen sc1\nv_lshrrev_b32 v77,5,v0\nv_bfe_u32 v113,v0,1,5\nv_and_b32 v115,1,v0\n" \
"s_lshl_b32 s63,s3,1\nv_mul_u32_u24 v77,1160,v77\nv_and_b32 v76,31,v0\nv_and_b32 v114,6,v113\n" \
"v_add_u32 v115,s63,v115\nv_mad_u32_u24 v78,v76,36,v77\nv_xor_b32 v114,v114,v115\nv_lshlrev_b32 v113,7,v113\n" \
"v_lshl_add_u32 v113,v114,4,v113\ns_add_u32 s64,s54,2916352\ns_add_u32 s65,s64,4096\nv_bfrev_b32 v114,1\n" \
"v_cmp_gt_u32 vcc,72,v0\ns_lshl_b32 s67,s32,2\ns_add_u32 s66,s64,8192\nv_cndmask_b32 v114,v114,v2,vcc\n" \
"s_add_u32 s67,s67,s3\ns_lshl_b32 s67,s67,5\ns_add_u32 s67,s67,s33\ns_lshl_b32 s67,s67,11\n" \
"s_add_u32 s67,s67,5308416\ns_lshl2_add_u32 s69,s3,161664\ns_mul_i32 s72,s3,2320\nv_mov_b32 v251,s69\n" \
"v_cmp_gt_u32 vcc,32,v0\ns_lshl_b32 s7,s32,11\ns_mov_b32 s93,128\nv_cndmask_b32 v74,2.0,0,vcc\n" \
"s_mov_b32 s73,8192\ns_mov_b32 s74,12288\ns_mov_b32 s75,17408\ns_mov_b32 s76,21504\n" \
"s_mov_b32 s77,16384\ns_mov_b32 s78,25600\ns_add_u32 s7,s7,40960\ns_mov_b32 s81,0x3d800000\n" \
"s_mov_b32 s42,18874368\ns_mov_b32 s43,0x20000\ns_waitcnt vmcnt(7)\nv_lshrrev_b32 v96,1,v234\n" \
"v_lshrrev_b32 v97,1,v235\nv_lshrrev_b32 v98,1,v236\nv_lshrrev_b32 v99,1,v237\nv_add_u32 v96,s72,v96\n" \
"s_waitcnt vmcnt(6)\nv_lshrrev_b32 v100,1,v238\nv_lshrrev_b32 v101,1,v239\nv_lshrrev_b32 v102,1,v240\n" \
"v_lshrrev_b32 v103,1,v241\nv_add_u32 v97,s72,v97\nv_add_u32 v98,s72,v98\nv_add_u32 v99,s72,v99\n" \
"v_add_u32 v100,s72,v100\nv_add_u32 v101,s72,v101\nv_add_u32 v102,s72,v102\nv_add_u32 v103,s72,v103\n" \
"v_lshlrev_b32 v48,16,v224\nv_and_b32 v49,0xffff0000,v224\nv_readlane_b32 s70,v48,0\nv_readlane_b32 s71,v48,32\n" \
"v_and_b32 v51,0xffff0000,v225\nv_mov_b32 v72,s70\nv_mov_b32 v73,s71\nv_lshlrev_b32 v50,16,v225\n" \
"v_and_b32 v53,0xffff0000,v226\nv_permlane32_swap_b32 v72,v73\nv_pk_add_f32 v[48:49],v[48:49],v[72:73] op_sel_hi:[1,0] neg_lo:[0,1] neg_hi:[0,1]\nv_pk_add_f32 v[50:51],v[50:51],v[72:73] op_sel_hi:[1,0] neg_lo:[0,1] neg_hi:[0,1]\n" \
"v_pk_mul_f32 v[68:69],v[48:49],v[48:49]\nv_lshlrev_b32_e64 v52,16,v226\nv_and_b32 v55,0xffff0000,v227\nv_pk_add_f32 v[52:53],v[52:53],v[72:73] op_sel_hi:[1,0] neg_lo:[0,1] neg_hi:[0,1]\n" \
"v_pk_add_f32 v[66:67],v[48:49],v[50:51]\nv_pk_fma_f32 v[68:69],v[50:51],v[50:51],v[68:69]\nv_lshlrev_b32_e64 v54,16,v227\nv_and_b32 v57,0xffff0000,v228\n" \
"v_pk_add_f32 v[54:55],v[54:55],v[72:73] op_sel_hi:[1,0] neg_lo:[0,1] neg_hi:[0,1]\nv_pk_add_f32 v[66:67],v[66:67],v[52:53]\nv_pk_fma_f32 v[68:69],v[52:53],v[52:53],v[68:69]\nv_lshlrev_b32_e64 v56,16,v228\n" \
"v_and_b32 v59,0xffff0000,v229\nv_pk_add_f32 v[56:57],v[56:57],v[72:73] op_sel_hi:[1,0] neg_lo:[0,1] neg_hi:[0,1]\nv_pk_add_f32 v[66:67],v[66:67],v[54:55]\nv_pk_fma_f32 v[68:69],v[54:55],v[54:55],v[68:69]\n" \
"v_lshlrev_b32_e64 v58,16,v229\nv_and_b32 v61,0xffff0000,v230\nv_pk_add_f32 v[58:59],v[58:59],v[72:73] op_sel_hi:[1,0] neg_lo:[0,1] neg_hi:[0,1]\nv_pk_add_f32 v[66:67],v[66:67],v[56:57]\n" \
"v_pk_fma_f32 v[68:69],v[56:57],v[56:57],v[68:69]\nv_lshlrev_b32_e64 v60,16,v230\nv_and_b32 v63,0xffff0000,v231\nv_pk_add_f32 v[60:61],v[60:61],v[72:73] op_sel_hi:[1,0] neg_lo:[0,1] neg_hi:[0,1]\n" \
"v_pk_add_f32 v[66:67],v[66:67],v[58:59]\nv_pk_fma_f32 v[68:69],v[58:59],v[58:59],v[68:69]\nv_lshlrev_b32_e64 v62,16,v231\nv_and_b32 v65,0xffff0000,v232\n" \
"v_pk_add_f32 v[62:63],v[62:63],v[72:73] op_sel_hi:[1,0] neg_lo:[0,1] neg_hi:[0,1]\nv_pk_add_f32 v[66:67],v[66:67],v[60:61]\nv_pk_fma_f32 v[68:69],v[60:61],v[60:61],v[68:69]\nv_lshlrev_b32_e64 v64,16,v232\n" \
"v_pk_add_f32 v[64:65],v[64:65],v[72:73] op_sel_hi:[1,0] neg_lo:[0,1] neg_hi:[0,1]\nv_pk_add_f32 v[66:67],v[66:67],v[62:63]\nv_pk_fma_f32 v[68:69],v[62:63],v[62:63],v[68:69]\nv_pk_add_f32 v[66:67],v[66:67],v[64:65]\n" \
"v_pk_fma_f32 v[68:69],v[64:65],v[64:65],v[68:69]\nv_add_f32 v66,v66,v67\nv_add_f32 v68,v68,v69\ns_mov_b64 s[40:41],s[44:45]\n" \
"s_mov_b64 s[44:45],s[46:47]\nv_permlane16_swap_b32 v66,v68\nv_add_f32 v66,v66,v68\ns_mov_b32 s46,2359296\n" \
"s_mov_b32 s47,0x20000\nv_add_f32_dpp v66,v66,v66 quad_perm:[1,0,3,2] row_mask:0xf bank_mask:0xf bound_ctrl:1\ns_nop 1\nv_add_f32_dpp v66,v66,v66 quad_perm:[2,3,0,1] row_mask:0xf bank_mask:0xf bound_ctrl:1\n" \
"s_nop 1\nv_add_f32_dpp v66,v66,v66 row_half_mirror row_mask:0xf bank_mask:0xf bound_ctrl:1\ns_nop 1\nv_add_f32_dpp v66,v66,v66 row_mirror row_mask:0xf bank_mask:0xf bound_ctrl:1\n" \
"v_mul_f32 v70,0x3ae38e39,v66\nv_mul_f32 v68,0x3ae38e39,v66\ns_nop 1\nv_permlane16_swap_b32 v70,v68\n" \
"v_fma_f32 v68,-v70,v70,v68\nv_max_f32 v68,0,v68\nv_add_f32 v68,0x3727c5ac,v68\nv_rsq_f32 v66,v68\n" \
"v_add_f32 v250,v70,v72\nv_mul_f32 v67,-v70,v66\nv_pk_fma_f32 v[48:49],v[48:49],v[66:67],v[66:67] op_sel:[0,0,1] op_sel_hi:[1,0,1]\nv_pk_fma_f32 v[50:51],v[50:51],v[66:67],v[66:67] op_sel:[0,0,1] op_sel_hi:[1,0,1]\n" \
"v_cvt_pk_f16_f32 v80,v48,v49\nv_cvt_pk_f16_f32 v81,v50,v51\nv_pk_fma_f32 v[52:53],v[52:53],v[66:67],v[66:67] op_sel:[0,0,1] op_sel_hi:[1,0,1]\nv_pk_fma_f32 v[54:55],v[54:55],v[66:67],v[66:67] op_sel:[0,0,1] op_sel_hi:[1,0,1]\n" \
"ds_write2_b32 v78,v80,v81 offset0:0 offset1:1\nv_cvt_pk_f16_f32 v82,v52,v53\nv_cvt_pk_f16_f32 v83,v54,v55\nv_pk_fma_f32 v[56:57],v[56:57],v[66:67],v[66:67] op_sel:[0,0,1] op_sel_hi:[1,0,1]\n" \
"v_pk_fma_f32 v[58:59],v[58:59],v[66:67],v[66:67] op_sel:[0,0,1] op_sel_hi:[1,0,1]\nds_write2_b32 v78,v82,v83 offset0:2 offset1:3\nv_cvt_pk_f16_f32 v84,v56,v57\nv_cvt_pk_f16_f32 v85,v58,v59\n" \
"v_pk_fma_f32 v[60:61],v[60:61],v[66:67],v[66:67] op_sel:[0,0,1] op_sel_hi:[1,0,1]\nv_pk_fma_f32 v[62:63],v[62:63],v[66:67],v[66:67] op_sel:[0,0,1] op_sel_hi:[1,0,1]\nds_write2_b32 v78,v84,v85 offset0:4 offset1:5\nv_cvt_pk_f16_f32 v86,v60,v61\n" \
"v_cvt_pk_f16_f32 v87,v62,v63\nv_pk_fma_f32 v[64:65],v[64:65],v[66:67],v[66:67] op_sel:[0,0,1] op_sel_hi:[1,0,1]\nds_write2_b32 v78,v86,v87 offset0:6 offset1:7\nv_cvt_pk_f16_f32 v88,v64,v65\n" \
"ds_write_b32 v78,v88 offset:32\ns_barrier\nds_read_u16 v16,v246\nds_read_u16 v17,v246 offset:5216\n" \
"ds_read_u16 v18,v246 offset:578\nds_read_u16 v19,v246 offset:5794\nds_read_u16 v20,v246 offset:1156\nds_read_u16 v21,v246 offset:6372\n" \
"ds_read_u16 v22,v246 offset:1734\nds_read_u16 v23,v246 offset:6950\nds_read_u16 v24,v247\ns_waitcnt lgkmcnt(7)\n" \
"v_lshl_or_b32 v16,v17,16,v16\nds_read_u16 v25,v247 offset:5216\nds_read_u16 v26,v247 offset:578\ns_waitcnt lgkmcnt(7)\n" \
"v_lshl_or_b32 v17,v19,16,v18\nv_pk_maximum3_f16 v124,v16,v17,v16\nds_read_u16 v27,v247 offset:5794\nv_pk_minimum3_f16 v125,v16,v17,v16\n" \
"ds_read_u16 v28,v247 offset:1156\ns_waitcnt lgkmcnt(7)\nv_lshl_or_b32 v18,v21,16,v20\nds_read_u16 v29,v247 offset:6372\n" \
"ds_read_u16 v30,v247 offset:1734\ns_waitcnt lgkmcnt(7)\nv_lshl_or_b32 v19,v23,16,v22\nbuffer_store_dwordx4 v[16:19],v2,s[36:39],s64 offen sc1\n" \
"ds_read_u16 v31,v247 offset:6950\nds_read_u16 v104,v248\nv_pk_maximum3_f16 v124,v18,v19,v124\nv_pk_minimum3_f16 v125,v18,v19,v125\n" \
"s_waitcnt lgkmcnt(7)\nv_lshl_or_b32 v24,v25,16,v24\nds_read_u16 v105,v248 offset:5216\nds_read_u16 v106,v248 offset:578\n" \
"s_waitcnt lgkmcnt(7)\nv_lshl_or_b32 v25,v27,16,v26\nv_pk_maximum3_f16 v124,v24,v25,v124\nds_read_u16 v107,v248 offset:5794\n" \
"v_pk_minimum3_f16 v125,v24,v25,v125\nds_read_u16 v108,v248 offset:1156\ns_waitcnt lgkmcnt(7)\nv_lshl_or_b32 v26,v29,16,v28\n" \
"ds_read_u16 v109,v248 offset:6372\nds_read_u16 v110,v248 offset:1734\ns_waitcnt lgkmcnt(7)\nv_lshl_or_b32 v27,v31,16,v30\n" \
"buffer_store_dwordx4 v[24:27],v2,s[36:39],s65 offen sc1\nds_read_u16 v111,v248 offset:6950\nds_read_b128 v[116:119],v113 offset:20224\nv_pk_maximum3_f16 v124,v26,v27,v124\n" \
"v_pk_minimum3_f16 v125,v26,v27,v125\nds_read_b128 v[120:123],v113 offset:24320\ns_waitcnt lgkmcnt(8)\nv_lshl_or_b32 v104,v105,16,v104\n" \
"ds_read_b32 v16,v96\nds_read_b32 v24,v96 offset:1160\nds_read_b32 v17,v97\nds_read_b32 v25,v97 offset:1160\n" \
"s_waitcnt lgkmcnt(10)\nv_lshl_or_b32 v105,v107,16,v106\nds_read_b32 v18,v98\nv_pk_maximum3_f16 v124,v104,v105,v124\n" \
"ds_read_b32 v26,v98 offset:1160\nv_pk_minimum3_f16 v125,v104,v105,v125\nds_read_b32 v19,v99\nds_read_b32 v27,v99 offset:1160\n" \
"ds_read_b32 v20,v100\ns_waitcnt lgkmcnt(13)\nv_lshl_or_b32 v106,v109,16,v108\nds_read_b32 v28,v100 offset:1160\n" \
"ds_read_b32 v21,v101\nds_read_b32 v29,v101 offset:1160\ns_waitcnt lgkmcnt(14)\nv_lshl_or_b32 v107,v111,16,v110\n" \
"buffer_store_dwordx4 v[104:107],v114,s[36:39],s66 offen sc1\ns_waitcnt lgkmcnt(13)\nbuffer_store_dwordx4 v[116:119],v1,s[36:39],s67 offen sc1\ns_waitcnt lgkmcnt(12)\n" \
"buffer_store_dwordx4 v[120:123],v1,s[36:39],s67 offen offset:1024 sc1\nds_read_b32 v22,v102\nds_read_b32 v30,v102 offset:1160\nds_read_b32 v23,v103\n" \
"ds_read_b32 v31,v103 offset:1160\nv_pk_maximum3_f16 v124,v106,v107,v124\nv_and_b32 v124,0x7fff7fff,v124\nv_pk_minimum3_f16 v125,v106,v107,v125\n" \
"v_and_b32 v125,0x7fff7fff,v125\nv_pk_max_u16 v124,v124,v125\nv_lshlrev_b32 v125,16,v124\nv_max_u32 v124,v124,v125\n" \
"s_nop 1\nv_max_u32_dpp v124,v124,v124 quad_perm:[1,0,3,2] row_mask:0xf bank_mask:0xf bound_ctrl:1\ns_nop 1\nv_max_u32_dpp v124,v124,v124 quad_perm:[2,3,0,1] row_mask:0xf bank_mask:0xf bound_ctrl:1\n" \
"s_nop 1\nv_max_u32_dpp v124,v124,v124 row_half_mirror row_mask:0xf bank_mask:0xf bound_ctrl:1\ns_nop 1\nv_max_u32_dpp v124,v124,v124 row_mirror row_mask:0xf bank_mask:0xf bound_ctrl:1\n" \
"s_nop 0\nv_readlane_b32 s68,v124,0\nv_readlane_b32 s100,v124,16\nv_readlane_b32 s101,v124,32\n" \
"v_readlane_b32 s31,v124,48\ns_max_u32 s100,s100,s101\ns_max_u32 s68,s68,s31\ns_max_u32 s68,s68,s100\n" \
"v_mov_b32 v124,s68\nds_write_b32 v251,v124 offset:32\nv_mov_b32 v114,161664\nbuffer_load_dwordx4 v[48:51],v2,s[36:39],s73 offen sc1\n" \
"buffer_load_dwordx4 v[60:63],v2,s[36:39],s75 offen sc1\nbuffer_load_dwordx4 v[52:55],v2,s[36:39],s74 offen sc1\nbuffer_load_dwordx4 v[64:67],v2,s[36:39],s76 offen sc1\nbuffer_load_dwordx4 v[56:59],v1,s[36:39],s77 offen sc1\n" \
"buffer_load_dwordx4 v[68:71],v1,s[36:39],s78 offen sc1\ns_mov_b32 s79,0xffffff\ns_lshl3_add_u32 s80,s2,4294615040\nv_add_u32 v112,28416,v1\n" \
"s_waitcnt vmcnt(6) lgkmcnt(0)\ns_barrier\nds_read_b128 v[116:119],v114 offset:32\nds_read_b128 v[192:195],v112\n" \
"ds_read_b128 v[196:199],v112 offset:1024\nds_read_b128 v[200:203],v112 offset:2048\nds_read_b128 v[204:207],v112 offset:3072\nds_read_b128 v[208:211],v112 offset:4096\n" \
"ds_read_b128 v[212:215],v112 offset:5120\nds_read_b128 v[32:35],v112 offset:6144\nds_read_b128 v[36:39],v112 offset:7168\ns_waitcnt lgkmcnt(8)\n" \
"v_max3_u32 v116,v116,v117,v118\nv_max_u32 v116,v116,v119\nv_and_b32 v119,0xf8,v0\nv_cmp_ne_u32 vcc,192,v119\n" \
"v_lshlrev_b32 v116,1,v116\nv_mov_b32 v117,s29\nv_lshlrev_b32 v118,11,v0\nv_bfi_b32 v117,s79,v117,v116\n" \
"v_add_u32 v118,s80,v118\nv_bfrev_b32 v119,1\nv_cndmask_b32 v118,v118,v119,vcc\nv_mov_b32 v116,s28\n" \
"buffer_store_dwordx2 v[116:117],v118,s[36:39],0 offen sc1\ns_waitcnt vmcnt(6)\nv_cmp_ne_u64 vcc,s[28:29],v[48:49]\ns_waitcnt vmcnt(4)\n" \
"v_cmp_ne_u64 s[84:85],s[28:29],v[52:53]\ns_waitcnt vmcnt(2)\nv_cmp_ne_u64 s[82:83],s[28:29],v[56:57]\ns_or_b64 vcc,vcc,s[84:85]\n" \
"v_cmp_ne_u64 s[84:85],s[28:29],v[60:61]\ns_or_b64 vcc,vcc,s[82:83]\nv_cmp_ne_u64 s[82:83],s[28:29],v[64:65]\ns_or_b64 vcc,vcc,s[84:85]\n" \
"s_waitcnt vmcnt(1)\nv_cmp_ne_u64 s[84:85],s[28:29],v[68:69]\ns_or_b64 vcc,vcc,s[82:83]\ns_or_b64 vcc,vcc,s[84:85]\n" \
"s_cbranch_vccnz .LsmP_%=\n.LbmP_%=:\nv_pk_add_f32 v[50:51],v[50:51],v[62:63]\nv_pk_add_f32 v[54:55],v[54:55],v[66:67]\n" \
"v_pk_mul_f32 v[62:63],v[50:51],2.0 op_sel_hi:[1,0]\nv_pk_add_f32 v[58:59],v[58:59],v[70:71]\nv_max3_f32 v112,|v62|,|v63|,0\nv_pk_fma_f32 v[66:67],v[54:55],2.0,v[74:75] op_sel_hi:[1,0,0]\n" \
"v_max3_f32 v112,|v66|,|v67|,v112\nv_pk_fma_f32 v[70:71],v[58:59],2.0,2.0 op_sel_hi:[1,0,0]\nv_max3_f32 v112,|v70|,|v71|,v112\nv_cmp_lt_f32 s[82:83],s81,v112\n" \
"v_cmp_ngt_f32 vcc,0x41000000,v112\ns_cmp_lg_u64 s[82:83],0\ns_cselect_b32 s82,2,0\nv_cvt_pk_f16_f32 v62,v62,v63\n" \
"s_cmp_lg_u64 vcc,0\ns_addc_u32 s82,s82,0\nds_write_b32 v252,v62 offset:62848\nv_cvt_pk_f16_f32 v66,v66,v67\n" \
"ds_write_b32 v253,v66 offset:62848\nv_cvt_pk_f16_f32 v70,v70,v71\nv_mov_b32 v113,s82\nds_write_b32 v254,v70 offset:62848\n" \
"ds_write_b32 v251,v113\ns_waitcnt lgkmcnt(0)\ns_barrier\nds_read_b128 v[124:127],v114\n" \
"ds_read_b64 v[96:97],v234 offset:62848\nds_read_b64 v[98:99],v235 offset:62848\nds_read_b64 v[100:101],v236 offset:62848\nds_read_b64 v[102:103],v237 offset:62848\n" \
"ds_read_b64 v[104:105],v238 offset:62848\nds_read_b64 v[106:107],v239 offset:62848\nds_read_b64 v[108:109],v240 offset:62848\nds_read_b64 v[110:111],v241 offset:62848\n" \
"s_waitcnt lgkmcnt(8)\nv_or3_b32 v124,v124,v125,v126\nv_or_b32 v124,v124,v127\nv_cmp_ne_u32 vcc,2,v124\n" \
"s_nop 0\ns_cbranch_vccnz .LsxP_%=\n.LbxP_%=:\ns_waitcnt lgkmcnt(7)\n" \
"v_pk_fma_f16 v80,v16,v96,v97\ns_waitcnt lgkmcnt(6)\nv_pk_fma_f16 v81,v17,v98,v99\ns_waitcnt lgkmcnt(5)\n" \
"v_pk_fma_f16 v82,v18,v100,v101\ns_waitcnt lgkmcnt(4)\nv_pk_fma_f16 v83,v19,v102,v103\nv_pk_fma_f16 v88,v24,v96,v97\n" \
"s_waitcnt lgkmcnt(3)\nv_pk_fma_f16 v84,v20,v104,v105\ns_waitcnt lgkmcnt(2)\nv_pk_fma_f16 v85,v21,v106,v107\n" \
"v_pk_fma_f16 v89,v25,v98,v99\ns_waitcnt lgkmcnt(1)\nv_pk_fma_f16 v86,v22,v108,v109\nv_pk_fma_f16 v90,v26,v100,v101\n" \
"s_waitcnt lgkmcnt(0)\nv_pk_fma_f16 v87,v23,v110,v111\nv_pk_fma_f16 v91,v27,v102,v103\nv_pk_fma_f16 v92,v28,v104,v105\n" \
"v_pk_fma_f16 v93,v29,v106,v107\nv_pk_fma_f16 v94,v30,v108,v109\nv_bfe_u32 v233,v0,3,3\nv_mul_u32_u24 v233,8192,v233\n" \
"v_lshl_add_u32 v233,v1,3,v233\ns_lshl2_add_u32 s96,s33,s3\ns_add_u32 s96,s96,138\ns_and_b32 s97,s96,31\n" \
"s_lshl_b32 s97,s97,3\ns_cmp_lt_u32 s96,256\ns_cselect_b32 s96,s96,0x40000\ns_lshr_b32 s96,s96,5\n" \
"s_add_u32 s96,s96,s97\ns_lshl_b32 s96,s96,6\ns_add_u32 s96,s96,s32\ns_mul_i32 s96,s96,1152\n" \
"s_mov_b32 m0,162176\nv_pk_fma_f16 v95,v31,v110,v111\nbuffer_load_dword v233,s[8:11],s96 offen lds\nv_mfma_f32_16x16x32_f16 v[48:51],v[80:83],v[192:195],0\n" \
"s_add_u32 s73,s35,s88\ns_add_u32 s74,s73,4096\nv_mfma_f32_16x16x32_f16 v[52:55],v[80:83],v[196:199],0\nv_and_b32 v112,15,v0\n" \
"v_bfrev_b32 v113,1\nv_mfma_f32_16x16x32_f16 v[56:59],v[84:87],v[32:35],0\nv_cmp_eq_u32 vcc,0,v112\ns_add_u32 s75,s73,8192\n" \
"v_mfma_f32_16x16x32_f16 v[60:63],v[84:87],v[36:39],0\nv_cndmask_b32 v113,v113,v3,vcc\nv_cmp_gt_u32 vcc,64,v0\nv_mfma_f32_16x16x32_f16 v[64:67],v[88:91],v[192:195],0\n" \
"s_cmp_lt_u32 s3,2\ns_cselect_b32 s75,s75,2147483648\nv_mfma_f32_16x16x32_f16 v[68:71],v[88:91],v[196:199],0\nv_pk_add_f32 v[56:57],v[48:49],v[56:57] neg_lo:[0,1] neg_hi:[0,1]\n" \
"v_sub_f32 v58,v50,v58\nv_mfma_f32_16x16x32_f16 v[72:75],v[92:95],v[32:35],0\nv_pk_add_f32 v[60:61],v[52:53],v[60:61] neg_lo:[0,1] neg_hi:[0,1]\nv_sub_f32 v62,v54,v62\n" \
"v_mfma_f32_16x16x32_f16 v[76:79],v[92:95],v[36:39],0\nv_pk_fma_f32 v[48:49],v[48:49],2.0,v[56:57] op_sel_hi:[1,0,1] neg_lo:[0,0,1] neg_hi:[0,0,1]\nv_fma_f32 v50,v50,2.0,-v58\nv_pk_fma_f32 v[52:53],v[52:53],2.0,v[60:61] op_sel_hi:[1,0,1] neg_lo:[0,0,1] neg_hi:[0,0,1]\n" \
"v_fma_f32 v54,v54,2.0,-v62\nv_cvt_pk_f16_f32 v16,v48,v52\nv_pk_add_f32 v[72:73],v[64:65],v[72:73] neg_lo:[0,1] neg_hi:[0,1]\nv_sub_f32 v74,v66,v74\n" \
"v_pk_fma_f32 v[64:65],v[64:65],2.0,v[72:73] op_sel_hi:[1,0,1] neg_lo:[0,0,1] neg_hi:[0,0,1]\nv_pk_add_f32 v[76:77],v[68:69],v[76:77] neg_lo:[0,1] neg_hi:[0,1]\nv_sub_f32 v78,v70,v78\nv_fma_f32 v66,v66,2.0,-v74\n" \
"v_pk_fma_f32 v[68:69],v[68:69],2.0,v[76:77] op_sel_hi:[1,0,1] neg_lo:[0,0,1] neg_hi:[0,0,1]\nv_fma_f32 v70,v70,2.0,-v78\nv_cvt_pk_f16_f32 v17,v49,v53\nv_cvt_pk_f16_f32 v18,v50,v54\n" \
"v_cvt_pk_f16_f32 v24,v64,v68\nv_cvt_pk_f16_f32 v25,v65,v69\nv_cvt_pk_f16_f32 v26,v66,v70\nv_mfma_f32_16x16x32_f16 v[48:51],v[200:203],v[16:19],0\n" \
"v_cvt_pk_f16_f32 v20,v56,v60\nv_cvt_pk_f16_f32 v21,v57,v61\nv_mfma_f32_16x16x32_f16 v[52:55],v[204:207],v[16:19],0\nv_cvt_pk_f16_f32 v22,v58,v62\n" \
"v_cvt_pk_f16_f32 v28,v72,v76\nv_mfma_f32_16x16x32_f16 v[64:67],v[200:203],v[24:27],0\nv_cvt_pk_f16_f32 v29,v73,v77\nv_cvt_pk_f16_f32 v30,v74,v78\n" \
"v_mfma_f32_16x16x32_f16 v[68:71],v[204:207],v[24:27],0\nv_cndmask_b32_e64 v113,v113,v3,vcc\nv_mfma_f32_16x16x32_f16 v[56:59],v[200:203],v[20:23],0\nv_cvt_pk_f16_f32 v80,v48,v52\n" \
"v_cvt_pk_f16_f32 v82,v49,v53\nv_mfma_f32_16x16x32_f16 v[60:63],v[204:207],v[20:23],0\nv_cvt_pk_f16_f32 v84,v50,v54\nv_mfma_f32_16x16x32_f16 v[72:75],v[200:203],v[28:31],0\n" \
"s_nop 0\nv_cvt_pk_f16_f32 v81,v64,v68\nv_mfma_f32_16x16x32_f16 v[76:79],v[204:207],v[28:31],0\nds_write_b64 v255,v[80:81]\n" \
"v_cvt_pk_f16_f32 v83,v65,v69\nds_write_b64 v255,v[82:83] offset:512\nv_cvt_pk_f16_f32 v85,v66,v70\nds_write_b64 v255,v[84:85] offset:1024\n" \
"v_cvt_pk_f16_f32 v86,v56,v60\nv_cvt_pk_f16_f32 v88,v57,v61\nv_cvt_pk_f16_f32 v90,v58,v62\nv_cvt_pk_f16_f32 v87,v72,v76\n" \
"ds_write_b64 v255,v[86:87] offset:256\nv_cvt_pk_f16_f32 v89,v73,v77\nds_write_b64 v255,v[88:89] offset:768\nv_cvt_pk_f16_f32 v91,v74,v78\n" \
"ds_write_b64 v255,v[90:91] offset:1280\ns_waitcnt lgkmcnt(0)\ns_barrier\nds_read_b128 v[16:19],v243\n" \
"ds_read_b128 v[20:23],v244\nds_read_b128 v[24:27],v245\ns_waitcnt lgkmcnt(2)\nv_mfma_f32_16x16x32_f16 v[48:51],v[40:43],v[16:19],0\n" \
"v_mfma_f32_16x16x32_f16 v[52:55],v[44:47],v[16:19],0\ns_waitcnt lgkmcnt(1)\nv_mfma_f32_16x16x32_f16 v[56:59],v[40:43],v[20:23],0\nv_mfma_f32_16x16x32_f16 v[60:63],v[44:47],v[20:23],0\n" \
"s_waitcnt lgkmcnt(0)\nv_mfma_f32_16x16x32_f16 v[64:67],v[40:43],v[24:27],0\nv_mfma_f32_16x16x32_f16 v[68:71],v[44:47],v[24:27],0\ns_nop 1\n" \
"v_cvt_pk_f16_f32 v80,v48,v52\nv_and_b32 v80,-2,v80\nv_cvt_pk_f16_f32 v81,v49,v53\nv_cvt_pk_f16_f32 v82,v50,v54\n" \
"v_cvt_pk_f16_f32 v83,v51,v55\nbuffer_store_dwordx4 v[80:83],v3,s[36:39],s73 offen sc1\nv_cvt_pk_f16_f32 v84,v56,v60\nv_cvt_pk_f16_f32 v85,v57,v61\n" \
"v_cvt_pk_f16_f32 v86,v58,v62\nv_cvt_pk_f16_f32 v87,v59,v63\nv_and_b32_e64 v84,-2,v84\nbuffer_store_dwordx4 v[84:87],v3,s[36:39],s74 offen sc1\n" \
"v_cvt_pk_f16_f32 v88,v64,v68\nv_cvt_pk_f16_f32 v89,v65,v69\nv_cvt_pk_f16_f32 v90,v66,v70\nv_cvt_pk_f16_f32 v91,v67,v71\n" \
"v_and_b32 v88,-2,v88\nbuffer_store_dwordx4 v[88:91],v113,s[36:39],s75 offen sc1\ns_cmp_lt_u32 s3,2\ns_cselect_b32 s18,s75,s73\n" \
"s_mov_b32 s12,s88\ns_mov_b32 s13,s89\ns_mov_b32 s14,s87\ns_mov_b32 s16,s73\n" \
"s_mov_b32 s17,s74\ns_load_dwordx2 s[20:21],s[0:1],32\ns_load_dwordx2 s[24:25],s[0:1],40\ns_mov_b32 s22,1152\n" \
"s_mov_b32 s26,3981312\ns_lshr_b32 s95,s86,2\ns_add_u32 s95,s95,2304\ns_waitcnt lgkmcnt(0)\n" \
"v_lshrrev_b32 v120,2,v0\nv_and_b32 v121,3,v0\nv_mul_u32_u24 v122,6912,v120\nv_lshlrev_b32 v123,4,v120\n" \
"v_lshl_add_u32 v121,v121,4,s95\nbuffer_load_dwordx4 v[112:115],v123,s[20:23],0 offen\nv_lshlrev_b32 v120,1,v120\nv_lshl_add_u32 v123,v122,3,v121\n" \
"buffer_load_ushort v116,v120,s[20:23],0 offen offset:1024\nv_add_u32_e64 v121,v121,v122\nbuffer_load_dwordx4 v[16:19],v123,s[24:27],0 offen\ns_mov_b32 s95,6912\n" \
"buffer_load_dwordx4 v[20:23],v123,s[24:27],s95 offen\ns_mov_b32 s95,13824\nbuffer_load_dwordx4 v[24:27],v123,s[24:27],s95 offen\ns_mov_b32 s95,20736\n" \
"buffer_load_dwordx4 v[28:31],v123,s[24:27],s95 offen\ns_mov_b32 s95,27648\nbuffer_load_dwordx4 v[32:35],v123,s[24:27],s95 offen\ns_mov_b32 s95,34560\n" \
"buffer_load_dwordx4 v[36:39],v123,s[24:27],s95 offen\ns_mov_b32 s95,41472\nbuffer_load_dwordx4 v[40:43],v123,s[24:27],s95 offen\ns_mov_b32 s95,48384\n" \
"buffer_load_dwordx4 v[44:47],v123,s[24:27],s95 offen\ns_mov_b32 s95,3538944\nbuffer_load_dwordx4 v[48:51],v121,s[24:27],s95 offen\ns_lshr_b32 s26,s32,1\n" \
"s_cmp_eq_u32 s26,2\ns_cselect_b32 s26,1.0,0\nv_cmp_gt_u32 vcc,8,v0\nv_mov_b32 v100,s28\n" \
"v_mov_b32 v101,s29\ns_waitcnt vmcnt(10)\nv_lshlrev_b32 v52,16,v112\nv_mul_f32 v53,0xbfb8aa3b,v52\n" \
"v_exp_f32 v53,v53\nv_and_b32 v54,0xffff0000,v112\nv_add_f32 v53,1.0,v53\nv_mul_f32 v55,0xbfb8aa3b,v54\n" \
"v_lshlrev_b32 v56,16,v113\nv_rcp_f32 v53,v53\nv_exp_f32 v55,v55\nv_mul_f32 v57,0xbfb8aa3b,v56\n" \
"v_exp_f32 v57,v57\nv_mul_f32 v52,v52,v53\nv_add_f32 v55,1.0,v55\nv_and_b32 v58,0xffff0000,v113\n" \
"v_cvt_pk_bf16_f32 v52,v52,v52\nv_rcp_f32 v55,v55\nv_add_f32 v57,1.0,v57\nv_mul_f32 v59,0xbfb8aa3b,v58\n" \
"v_lshlrev_b32 v52,16,v52\nv_lshlrev_b32 v60,16,v114\nv_rcp_f32 v57,v57\nv_exp_f32 v59,v59\n" \
"v_mul_f32 v61,0xbfb8aa3b,v60\nv_mul_f32 v54,v54,v55\nv_exp_f32 v61,v61\nv_cvt_pk_bf16_f32 v54,v54,v54\n" \
"v_mul_f32 v56,v56,v57\nv_add_f32 v59,1.0,v59\nv_and_b32 v62,0xffff0000,v114\nv_lshlrev_b32 v54,16,v54\n" \
"v_cvt_pk_bf16_f32 v56,v56,v56\nv_rcp_f32 v59,v59\nv_add_f32 v61,1.0,v61\nv_mul_f32 v63,0xbfb8aa3b,v62\n" \
"v_lshlrev_b32 v56,16,v56\nv_lshlrev_b32 v64,16,v115\nv_rcp_f32 v61,v61\nv_exp_f32 v63,v63\n" \
"v_mul_f32 v65,0xbfb8aa3b,v64\nv_mul_f32 v58,v58,v59\nv_exp_f32 v65,v65\nv_cvt_pk_bf16_f32 v58,v58,v58\n" \
"v_mul_f32 v60,v60,v61\nv_add_f32 v63,1.0,v63\nv_and_b32 v66,0xffff0000,v115\nv_lshlrev_b32 v58,16,v58\n" \
"v_cvt_pk_bf16_f32 v60,v60,v60\nv_rcp_f32 v63,v63\nv_add_f32 v65,1.0,v65\nv_mul_f32 v67,0xbfb8aa3b,v66\n" \
"v_lshlrev_b32 v60,16,v60\ns_waitcnt vmcnt(9)\nv_lshlrev_b32 v68,16,v116\nv_rcp_f32 v65,v65\n" \
"v_exp_f32 v67,v67\nv_mul_f32 v69,0xbfb8aa3b,v68\nv_mul_f32 v62,v62,v63\nv_exp_f32 v69,v69\n" \
"v_cvt_pk_bf16_f32 v62,v62,v62\nv_mul_f32 v64,v64,v65\nv_add_f32 v67,1.0,v67\nv_lshlrev_b32 v62,16,v62\n" \
"v_cvt_pk_bf16_f32 v64,v64,v64\nv_rcp_f32 v67,v67\nv_add_f32 v69,1.0,v69\nv_lshlrev_b32 v64,16,v64\n" \
"v_rcp_f32 v69,v69\nv_mul_f32 v66,v66,v67\nv_cvt_pk_bf16_f32 v66,v66,v66\nv_mul_f32 v68,v68,v69\n" \
"v_lshlrev_b32 v66,16,v66\nv_cvt_pk_bf16_f32 v68,v68,v68\nv_lshlrev_b32 v68,16,v68\ns_waitcnt vmcnt(8)\n" \
"v_and_b32 v81,0xffff0000,v16\nv_and_b32 v83,0xffff0000,v17\nv_lshlrev_b32 v80,16,v16\nv_lshlrev_b32_e64 v82,16,v17\n" \
"v_pk_mul_f32 v[70:71],v[80:81],v[52:53] op_sel_hi:[1,0]\nv_pk_mul_f32 v[72:73],v[82:83],v[52:53] op_sel_hi:[1,0]\nv_and_b32 v81,0xffff0000,v18\nv_and_b32 v83,0xffff0000,v19\n" \
"v_lshlrev_b32 v80,16,v18\nv_lshlrev_b32 v82,16,v19\nv_pk_mul_f32 v[74:75],v[80:81],v[52:53] op_sel_hi:[1,0]\nv_pk_mul_f32 v[76:77],v[82:83],v[52:53] op_sel_hi:[1,0]\n" \
"s_waitcnt vmcnt(7)\nv_and_b32 v85,0xffff0000,v20\nv_and_b32 v87,0xffff0000,v21\nv_lshlrev_b32 v84,16,v20\n" \
"v_lshlrev_b32 v86,16,v21\nv_pk_fma_f32 v[70:71],v[84:85],v[54:55],v[70:71] op_sel_hi:[1,0,1]\nv_pk_fma_f32 v[72:73],v[86:87],v[54:55],v[72:73] op_sel_hi:[1,0,1]\ns_waitcnt vmcnt(6)\n" \
"v_and_b32 v81,0xffff0000,v24\nv_and_b32 v83,0xffff0000,v25\nv_lshlrev_b32 v80,16,v24\nv_lshlrev_b32 v82,16,v25\n" \
"v_and_b32 v85,0xffff0000,v22\nv_and_b32 v87,0xffff0000,v23\nv_pk_fma_f32 v[70:71],v[80:81],v[56:57],v[70:71] op_sel_hi:[1,0,1]\nv_pk_fma_f32 v[72:73],v[82:83],v[56:57],v[72:73] op_sel_hi:[1,0,1]\n" \
"v_lshlrev_b32 v84,16,v22\nv_lshlrev_b32 v86,16,v23\nv_pk_fma_f32 v[74:75],v[84:85],v[54:55],v[74:75] op_sel_hi:[1,0,1]\nv_pk_fma_f32 v[76:77],v[86:87],v[54:55],v[76:77] op_sel_hi:[1,0,1]\n" \
"v_and_b32 v81,0xffff0000,v26\nv_and_b32 v83,0xffff0000,v27\nv_lshlrev_b32 v80,16,v26\nv_lshlrev_b32 v82,16,v27\n" \
"v_pk_fma_f32 v[74:75],v[80:81],v[56:57],v[74:75] op_sel_hi:[1,0,1]\nv_pk_fma_f32 v[76:77],v[82:83],v[56:57],v[76:77] op_sel_hi:[1,0,1]\ns_waitcnt vmcnt(5)\nv_and_b32 v85,0xffff0000,v28\n" \
"v_and_b32 v87,0xffff0000,v29\nv_lshlrev_b32 v84,16,v28\nv_lshlrev_b32 v86,16,v29\nv_pk_fma_f32 v[70:71],v[84:85],v[58:59],v[70:71] op_sel_hi:[1,0,1]\n" \
"v_pk_fma_f32 v[72:73],v[86:87],v[58:59],v[72:73] op_sel_hi:[1,0,1]\ns_waitcnt vmcnt(4)\nv_and_b32 v81,0xffff0000,v32\nv_and_b32 v83,0xffff0000,v33\n" \
"v_lshlrev_b32 v80,16,v32\nv_lshlrev_b32 v82,16,v33\nv_and_b32 v85,0xffff0000,v30\nv_and_b32 v87,0xffff0000,v31\n" \
"v_pk_fma_f32 v[70:71],v[80:81],v[60:61],v[70:71] op_sel_hi:[1,0,1]\nv_pk_fma_f32 v[72:73],v[82:83],v[60:61],v[72:73] op_sel_hi:[1,0,1]\nv_lshlrev_b32 v84,16,v30\nv_lshlrev_b32 v86,16,v31\n" \
"v_pk_fma_f32 v[74:75],v[84:85],v[58:59],v[74:75] op_sel_hi:[1,0,1]\nv_pk_fma_f32 v[76:77],v[86:87],v[58:59],v[76:77] op_sel_hi:[1,0,1]\nv_and_b32 v81,0xffff0000,v34\nv_and_b32 v83,0xffff0000,v35\n" \
"v_lshlrev_b32 v80,16,v34\nv_lshlrev_b32 v82,16,v35\nv_pk_fma_f32 v[74:75],v[80:81],v[60:61],v[74:75] op_sel_hi:[1,0,1]\nv_pk_fma_f32 v[76:77],v[82:83],v[60:61],v[76:77] op_sel_hi:[1,0,1]\n" \
"s_waitcnt vmcnt(3)\nv_and_b32 v85,0xffff0000,v36\nv_and_b32 v87,0xffff0000,v37\nv_lshlrev_b32 v84,16,v36\n" \
"v_lshlrev_b32 v86,16,v37\nv_pk_fma_f32 v[70:71],v[84:85],v[62:63],v[70:71] op_sel_hi:[1,0,1]\nv_pk_fma_f32 v[72:73],v[86:87],v[62:63],v[72:73] op_sel_hi:[1,0,1]\ns_waitcnt vmcnt(2)\n" \
"v_and_b32 v81,0xffff0000,v40\nv_and_b32 v83,0xffff0000,v41\nv_lshlrev_b32 v80,16,v40\nv_lshlrev_b32 v82,16,v41\n" \
"v_and_b32 v85,0xffff0000,v38\nv_and_b32 v87,0xffff0000,v39\nv_pk_fma_f32 v[70:71],v[80:81],v[64:65],v[70:71] op_sel_hi:[1,0,1]\nv_pk_fma_f32 v[72:73],v[82:83],v[64:65],v[72:73] op_sel_hi:[1,0,1]\n" \
"v_lshlrev_b32 v84,16,v38\nv_lshlrev_b32 v86,16,v39\nv_pk_fma_f32 v[74:75],v[84:85],v[62:63],v[74:75] op_sel_hi:[1,0,1]\nv_pk_fma_f32 v[76:77],v[86:87],v[62:63],v[76:77] op_sel_hi:[1,0,1]\n" \
"v_and_b32 v81,0xffff0000,v42\nv_and_b32 v83,0xffff0000,v43\nv_lshlrev_b32 v80,16,v42\nv_lshlrev_b32 v82,16,v43\n" \
"v_pk_fma_f32 v[74:75],v[80:81],v[64:65],v[74:75] op_sel_hi:[1,0,1]\nv_pk_fma_f32 v[76:77],v[82:83],v[64:65],v[76:77] op_sel_hi:[1,0,1]\ns_waitcnt vmcnt(1)\nv_and_b32 v85,0xffff0000,v44\n" \
"v_and_b32 v87,0xffff0000,v45\nv_lshlrev_b32 v84,16,v44\nv_lshlrev_b32 v86,16,v45\nv_pk_fma_f32 v[70:71],v[84:85],v[66:67],v[70:71] op_sel_hi:[1,0,1]\n" \
"v_pk_fma_f32 v[72:73],v[86:87],v[66:67],v[72:73] op_sel_hi:[1,0,1]\ns_waitcnt vmcnt(0)\nv_and_b32 v81,0xffff0000,v48\nv_and_b32 v83,0xffff0000,v49\n" \
"v_lshlrev_b32 v80,16,v48\nv_lshlrev_b32 v82,16,v49\nv_and_b32 v85,0xffff0000,v46\nv_and_b32 v87,0xffff0000,v47\n" \
"v_pk_fma_f32 v[70:71],v[80:81],v[68:69],v[70:71] op_sel_hi:[1,0,1]\nv_pk_fma_f32 v[72:73],v[82:83],v[68:69],v[72:73] op_sel_hi:[1,0,1]\nv_lshlrev_b32 v84,16,v46\nv_lshlrev_b32 v86,16,v47\n" \
"v_pk_fma_f32 v[74:75],v[84:85],v[66:67],v[74:75] op_sel_hi:[1,0,1]\nv_pk_fma_f32 v[76:77],v[86:87],v[66:67],v[76:77] op_sel_hi:[1,0,1]\nv_and_b32 v81,0xffff0000,v50\nv_and_b32 v83,0xffff0000,v51\n" \
"v_lshlrev_b32 v80,16,v50\nv_lshlrev_b32 v82,16,v51\nv_pk_fma_f32 v[74:75],v[80:81],v[68:69],v[74:75] op_sel_hi:[1,0,1]\nv_pk_fma_f32 v[76:77],v[82:83],v[68:69],v[76:77] op_sel_hi:[1,0,1]\n" \
"v_and_b32 v80,3,v0\nv_permlane32_swap_b32 v70,v74\nv_permlane32_swap_b32 v71,v75\nv_permlane32_swap_b32 v72,v76\n" \
"v_permlane32_swap_b32 v73,v77\nv_pk_add_f32 v[70:71],v[70:71],v[74:75]\nv_pk_add_f32 v[72:73],v[72:73],v[76:77]\nv_lshrrev_b32 v81,4,v0\n" \
"v_and_b32 v82,12,v81\nv_permlane16_swap_b32 v70,v72\nv_permlane16_swap_b32 v71,v73\nv_lshlrev_b32 v80,5,v80\n" \
"v_pk_add_f32 v[70:71],v[70:71],v[72:73]\nv_mad_u32_u24 v80,v82,24,v80\nv_lshl_add_u32 v80,v81,3,v80\nv_add_f32_dpp v70,v70,v70 row_ror:8 row_mask:0xf bank_mask:0xf bound_ctrl:1\n" \
"v_add_f32_dpp v71,v71,v71 row_ror:8 row_mask:0xf bank_mask:0xf bound_ctrl:1\nv_add_u32 v80,161152,v80\nv_add_f32_dpp v70,v70,v70 row_ror:4 row_mask:0xf bank_mask:0xf bound_ctrl:1\nv_add_f32_dpp v71,v71,v71 row_ror:4 row_mask:0xf bank_mask:0xf bound_ctrl:1\n" \
"ds_write_b64 v80,v[70:71]\nv_and_b32 v80,7,v0\nv_lshlrev_b32 v80,4,v80\nv_add_u32 v80,161152,v80\n" \
"v_bfrev_b32 v81,1\nv_cndmask_b32 v81,v81,v2,vcc\ns_waitcnt lgkmcnt(0)\ns_barrier\n" \
"ds_read_b128 v[84:87],v80\nds_read_b128 v[88:91],v80 offset:128\nds_read_b128 v[92:95],v80 offset:256\nds_read_b128 v[96:99],v80 offset:384\n" \
"s_waitcnt lgkmcnt(2)\nv_pk_add_f32 v[84:85],v[84:85],v[88:89]\nv_pk_add_f32 v[86:87],v[86:87],v[90:91]\ns_waitcnt lgkmcnt(0)\n" \
"v_pk_add_f32 v[92:93],v[92:93],v[96:97]\nv_pk_add_f32 v[94:95],v[94:95],v[98:99]\nv_pk_add_f32 v[84:85],v[84:85],v[92:93]\nv_pk_add_f32 v[86:87],v[86:87],v[94:95]\n" \
"v_cvt_pk_bf16_f32 v88,v84,v85\nv_cvt_pk_bf16_f32 v90,v86,v87\nv_and_b32 v85,0xffff0000,v88\nv_and_b32 v87,0xffff0000,v90\n" \
"v_lshlrev_b32 v84,16,v88\nv_lshlrev_b32 v86,16,v90\nv_add_f32 v84,s26,v84\nv_add_f32 v85,s26,v85\n" \
"v_add_f32 v86,s26,v86\nv_add_f32 v87,s26,v87\ns_lshr_b32 s26,s86,1\nv_cvt_pk_bf16_f32 v102,v84,v85\n" \
"v_cvt_pk_bf16_f32 v103,v86,v87\ns_add_u32 s26,s26,26624\nbuffer_store_dwordx4 v[100:103],v81,s[36:39],s26 offen sc1\ns_and_b32 s22,s33,7\n" \
"s_and_b32 s24,s32,1\ns_lshl3_add_u32 s22,s24,s22\ns_bfe_u32 s24,s33,0x10002\ns_lshl1_add_u32 s24,s32,s24\n" \
"s_mul_i32 s19,s24,18\ns_and_b32 s24,s33,3\ns_lshl2_add_u32 s23,s24,s19\ns_lshl_b32 s24,s3,5\n" \
"s_lshr_b32 s20,s33,3\ns_lshr_b32 s21,s32,1\ns_add_u32 s54,s14,s24\ns_lshl2_add_u32 s24,s20,s21\n" \
"s_add_u32 s23,s23,s3\ns_lshl2_add_u32 s24,s24,s3\ns_add_u32 s23,s23,1\ns_lshl_b32 s24,s24,9\n" \
"s_lshl_b32 s23,s23,9\nv_bfe_u32 v127,v0,3,3\nv_mul_u32_u24 v127,8192,v127\nv_lshl_add_u32 v127,v1,3,v127\n" \
"s_lshl2_add_u32 s95,s33,s3\ns_add_u32 s95,s95,190\ns_and_b32 s100,s95,31\ns_lshl_b32 s100,s100,3\n" \
"s_cmp_lt_u32 s95,256\ns_cselect_b32 s95,s95,0x40000\ns_lshr_b32 s95,s95,5\ns_add_u32 s95,s95,s100\n" \
"s_lshl_b32 s95,s95,6\ns_add_u32 s95,s95,s32\ns_mul_i32 s95,s95,1152\ns_mov_b32 m0,162176\n" \
"s_add_u32 s101,s92,0\nbuffer_load_dword v127,s[8:11],s95 offen lds\ns_add_u32 s24,s24,262144\ns_sub_u32 s59,s24,s23\n" \
"s_lshr_b32 s25,s3,1\ns_mul_i32 s24,s21,1152\ns_mul_i32 s25,s25,576\ns_add_u32 s24,s24,s25\n" \
"s_lshl4_add_u32 s24,s24,s22\ns_and_b32 s25,s3,1\ns_lshl_b32 s24,s24,8\ns_lshl_b32 s65,s25,17\n" \
"s_add_u32 s65,s65,s24\ns_lshl_b32 s25,s25,20\ns_add_u32 s24,s24,s25\ns_add_u32 s60,s24,655488\n" \
"s_add_u32 s61,s24,786432\ns_add_u32 s62,s24,786560\ns_add_u32 m0,s57,0\ns_add_u32 s31,s92,9437184\n" \
"buffer_load_dwordx4 v4,s[8:11],s101 offen lds\ns_add_u32 s63,s24,917504\ns_add_u32 s64,s24,917632\ns_mul_i32 s52,s20,4505600\n" \
"s_lshl2_add_u32 s24,s21,s3\ns_add_u32 s52,s52,12123648\ns_mul_i32 s24,s24,9216\ns_add_u32 s24,s24,s52\n" \
"s_add_u32 s68,s24,819712\ns_lshl_b32 s24,s22,14\nbuffer_load_dwordx4 v5,s[8:11],s101 offen offset:1024 lds\ns_add_u32 s24,s24,s52\n" \
"s_lshl2_add_u32 s25,s21,s3\ns_add_u32 s24,s24,967168\ns_lshl_b32 s25,s25,8\ns_add_u32 s79,s25,s24\n" \
"s_add_u32 s25,s21,4\ns_and_b32 s25,s25,3\ns_add_u32 s24,s24,s12\ns_lshl_b32 s25,s25,12\n" \
"buffer_load_dwordx4 v6,s[8:11],s101 offen offset:2048 lds\ns_add_u32 s25,s25,s24\ns_sub_u32 s74,s25,819200\ns_add_u32 s25,s21,3\n" \
"s_and_b32 s25,s25,3\ns_lshl_b32 s25,s25,12\ns_add_u32 s25,s25,s24\ns_sub_u32 s71,s25,2048000\n" \
"s_sub_u32 s76,s25,1638400\ns_add_u32 s25,s21,2\nbuffer_load_dwordx4 v7,s[8:11],s101 offen offset:3072 lds\ns_and_b32 s25,s25,3\n" \
"s_lshl_b32 s25,s25,12\ns_add_u32 s25,s25,s24\ns_sub_u32 s72,s25,1638400\ns_sub_u32 s77,s25,1638400\n" \
"s_add_u32 s25,s21,1\ns_and_b32 s25,s25,3\ns_lshl_b32 s25,s25,12\ns_add_u32 s25,s25,s24\n" \
"s_add_u32 m0,s57,4096\ns_add_u32 s96,s92,73728\nbuffer_load_dwordx4 v4,s[8:11],s31 offen lds\ns_sub_u32 s73,s25,1638400\n" \
"s_sub_u32 s78,s25,1228800\ns_lshl2_add_u32 s26,s33,s3\ns_and_b32 s24,s26,15\ns_and_b32 s25,s26,8\n" \
"s_lshl2_add_u32 s24,s24,192\ns_mul_i32 s25,s25,28\ns_add_u32 s25,s25,s24\ns_sub_u32 s25,s25,32\n" \
"buffer_load_dwordx4 v5,s[8:11],s31 offen offset:1024 lds\ns_bitcmp1_b32 s26,4\ns_cselect_b32 s24,s25,s24\ns_and_b32 s25,s26,31\n" \
"s_lshl2_add_u32 s25,s25,448\ns_bitcmp1_b32 s26,5\ns_cselect_b32 s24,s25,s24\ns_lshr_b32 s25,s26,6\n" \
"s_lshl1_add_u32 s25,s21,s25\ns_mul_i32 s25,s25,576\nbuffer_load_dwordx4 v6,s[8:11],s31 offen offset:2048 lds\ns_add_u32 s24,s24,s25\n" \
"s_and_b32 s25,s32,1\ns_lshl1_add_u32 s24,s24,s25\nv_and_b32 v112,3,v0\ns_lshl_b32 s24,s24,11\n" \
"s_and_b32 s25,s33,3\ns_mul_i32 s27,s3,4896\nv_and_b32 v113,12,v0\nv_lshlrev_b32 v112,2,v112\n" \
"buffer_load_dwordx4 v7,s[8:11],s31 offen offset:3072 lds\ns_add_u32 s24,s24,s25\ns_add_u32 s27,s27,9216\nv_max_u32 v112,v112,v113\n" \
"s_mul_i32 s48,s20,589824\ns_add_u32 s89,s24,1\nv_mov_b32 v114,1048576\nv_mov_b32 v113,s27\n" \
"v_cmp_gt_u32 vcc,12,v112\ns_add_u32 m0,s57,8192\ns_add_u32 s97,s92,9510912\nbuffer_load_dwordx4 v4,s[8:11],s96 offen lds\n" \
"s_lshl4_add_u32 s48,s12,s48\ns_lshl_b32 s86,s21,11\ns_lshl2_add_u32 s24,s2,s3\nv_and_b32 v63,0xffff0000,v224\n" \
"v_and_b32 v65,0xffff0000,v225\nv_and_b32 v67,0xffff0000,v226\nv_and_b32 v69,0xffff0000,v227\nv_and_b32 v71,0xffff0000,v228\n" \
"buffer_load_dwordx4 v5,s[8:11],s96 offen offset:1024 lds\nv_and_b32 v73,0xffff0000,v229\nv_and_b32 v75,0xffff0000,v230\nv_and_b32 v77,0xffff0000,v231\n" \
"v_and_b32 v79,0xffff0000,v232\nv_cndmask_b32 v112,v114,v113,vcc\nv_lshlrev_b32 v62,16,v224\nv_lshlrev_b32 v64,16,v225\n" \
"v_lshlrev_b32 v66,16,v226\nbuffer_load_dwordx4 v6,s[8:11],s96 offen offset:2048 lds\nv_lshlrev_b32 v68,16,v227\nv_lshlrev_b32 v70,16,v228\n" \
"v_lshlrev_b32 v72,16,v229\nv_lshlrev_b32 v74,16,v230\nv_lshlrev_b32 v76,16,v231\nv_lshlrev_b32 v78,16,v232\n" \
"s_add_u32 s48,s48,9764864\ns_add_u32 s65,s65,2097152\ns_lshl_b32 s67,s21,7\nbuffer_load_dwordx4 v7,s[8:11],s96 offen offset:3072 lds\n" \
"s_lshl_b32 s85,s21,17\ns_sub_u32 s86,s14,s86\ns_lshl_b32 s24,s24,8\nv_pk_add_f32 v[62:63],v[62:63],v[250:251] op_sel_hi:[1,0] neg_lo:[0,1] neg_hi:[0,1]\n" \
"v_pk_add_f32 v[64:65],v[64:65],v[250:251] op_sel_hi:[1,0] neg_lo:[0,1] neg_hi:[0,1]\nv_pk_add_f32 v[66:67],v[66:67],v[250:251] op_sel_hi:[1,0] neg_lo:[0,1] neg_hi:[0,1]\nv_pk_add_f32 v[68:69],v[68:69],v[250:251] op_sel_hi:[1,0] neg_lo:[0,1] neg_hi:[0,1]\nv_pk_add_f32 v[70:71],v[70:71],v[250:251] op_sel_hi:[1,0] neg_lo:[0,1] neg_hi:[0,1]\n" \
"s_add_u32 m0,s57,12288\ns_add_u32 s100,s92,9584640\nbuffer_load_dwordx4 v4,s[8:11],s97 offen lds\nv_pk_add_f32 v[72:73],v[72:73],v[250:251] op_sel_hi:[1,0] neg_lo:[0,1] neg_hi:[0,1]\n" \
"v_pk_add_f32 v[74:75],v[74:75],v[250:251] op_sel_hi:[1,0] neg_lo:[0,1] neg_hi:[0,1]\nv_pk_add_f32 v[76:77],v[76:77],v[250:251] op_sel_hi:[1,0] neg_lo:[0,1] neg_hi:[0,1]\nv_pk_add_f32 v[78:79],v[78:79],v[250:251] op_sel_hi:[1,0] neg_lo:[0,1] neg_hi:[0,1]\nv_mov_b64 v[16:17],v[192:193]\n" \
"v_mov_b64 v[18:19],v[194:195]\nv_mov_b64 v[20:21],v[196:197]\nv_mov_b64 v[22:23],v[198:199]\nbuffer_load_dwordx4 v5,s[8:11],s97 offen offset:1024 lds\n" \
"v_mov_b64 v[24:25],v[208:209]\nv_mov_b64 v[26:27],v[210:211]\nv_mov_b64 v[28:29],v[212:213]\nv_mov_b64 v[30:31],v[214:215]\n" \
"v_mov_b64 v[32:33],v[216:217]\nv_mov_b64 v[34:35],v[218:219]\nv_mov_b64 v[36:37],v[220:221]\nv_mov_b64 v[38:39],v[222:223]\n" \
"v_mov_b64 v[40:41],v[200:201]\nbuffer_load_dwordx4 v6,s[8:11],s97 offen offset:2048 lds\nv_mov_b64 v[42:43],v[202:203]\nv_mov_b64 v[44:45],v[204:205]\n" \
"v_mov_b64 v[46:47],v[206:207]\nv_mov_b64 v[56:57],v[242:243]\nv_mov_b64 v[58:59],v[244:245]\nv_mov_b32 v60,v249\n" \
"v_mov_b32 v61,v250\nv_add_u32 v48,v234,v112\nv_add_u32 v49,v235,v112\nbuffer_load_dwordx4 v7,s[8:11],s97 offen offset:3072 lds\n" \
"v_add_u32 v50,v236,v112\nv_add_u32 v51,v237,v112\nv_add_u32 v52,v238,v112\nv_add_u32 v53,v239,v112\n" \
"v_add_u32 v54,v240,v112\nv_add_u32 v55,v241,v112\ns_add_u32 s49,s48,4096\ns_add_u32 s50,s48,8192\n" \
"s_add_u32 s51,s48,12288\ns_add_u32 m0,s57,16384\ns_add_u32 s53,s52,3276800\ns_add_u32 s83,s52,2048000\n" \
"s_mov_b32 s55,0xbdd2d3e7\ns_mul_i32 s56,s3,9568\ns_lshl_b32 s58,s3,13\ns_add_u32 s75,s59,196608\n" \
"s_add_u32 s66,s65,128\ns_add_u32 s67,s67,163200\ns_add_u32 s69,s68,3072\ns_add_u32 s70,s68,6144\n" \
"s_add_u32 s80,s79,4096\ns_add_u32 s81,s79,8192\ns_add_u32 s82,s79,12288\ns_and_b32 s84,s33,1\n" \
"s_add_u32 s85,s85,65536\ns_add_u32 s87,s12,65536\ns_add_u32 s88,s74,819200\ns_add_u32 s86,s86,0x10000\n" \
"s_mov_b32 s90,7405568\ns_add_u32 s91,s24,69206016\ns_sub_u32 s98,s57,s12\ns_mov_b32 s99,s12\n" \
"s_add_u32 s95,s92,147456\nbuffer_load_dwordx4 v4,s[8:11],s95 offen lds\ns_add_u32 s101,s92,221184\nbuffer_load_dwordx4 v5,s[8:11],s95 offen offset:1024 lds\n" \
"buffer_load_dwordx4 v6,s[8:11],s95 offen offset:2048 lds\nbuffer_load_dwordx4 v7,s[8:11],s95 offen offset:3072 lds\ns_add_u32 m0,s57,20480\ns_nop 0\n" \
"buffer_load_dwordx4 v4,s[8:11],s100 offen lds\nbuffer_load_dwordx4 v5,s[8:11],s100 offen offset:1024 lds\nbuffer_load_dwordx4 v6,s[8:11],s100 offen offset:2048 lds\nbuffer_load_dwordx4 v7,s[8:11],s100 offen offset:3072 lds\n" \
"s_add_u32 m0,s57,24576\ns_nop 0\nbuffer_load_dwordx4 v4,s[8:11],s101 offen lds\nbuffer_load_dwordx4 v5,s[8:11],s101 offen offset:1024 lds\n" \
"buffer_load_dwordx4 v6,s[8:11],s101 offen offset:2048 lds\nbuffer_load_dwordx4 v7,s[8:11],s101 offen offset:3072 lds\nbuffer_load_dwordx4 v[104:107],v1,s[36:39],s7 offen sc1\nbuffer_load_dwordx4 v[108:111],v1,s[36:39],s7 offen offset:1024 sc1\n" \
"s_waitcnt vmcnt(1)\nv_xor_b32 v112,s29,v105\nv_xor_b32 v113,s29,v107\ns_waitcnt vmcnt(0)\n" \
"v_xor_b32 v114,s29,v109\nv_xor_b32 v115,s29,v111\nv_or3_b32 v112,v112,v113,v114\nv_xor_b32 v113,s28,v104\n" \
"v_xor_b32 v114,s28,v106\nv_or_b32 v112,v112,v115\nv_xor_b32 v115,s28,v108\nv_lshl_or_b32 v112,v112,8,v113\n" \
"v_xor_b32 v113,s28,v110\nv_or3_b32 v112,v112,v114,v115\nv_or_b32 v112,v112,v113\nv_cmp_ne_u32 vcc,0,v112\n" \
"s_nop 0\ns_cbranch_vccnz .LsrP_%=\n.LbrP_%=:\nv_min_u32 v120,2288,v2\n" \
"s_mov_b32 s5,26624\nv_max3_u32 v112,v105,v107,v109\nv_max_u32 v112,v112,v111\ns_mov_b32 s6,31232\n" \
"v_or_b32 v102,s2,v0\nv_max_u32_dpp v112,v112,v112 quad_perm:[1,0,3,2] row_mask:0xf bank_mask:0xf bound_ctrl:1\nv_bfrev_b32 v103,1\nv_cmp_eq_u32 vcc,0,v102\n" \
"v_max_u32_dpp v112,v112,v112 quad_perm:[2,3,0,1] row_mask:0xf bank_mask:0xf bound_ctrl:1\nv_mov_b32 v100,s28\nv_cndmask_b32 v102,v103,v102,vcc\nv_max_u32_dpp v112,v112,v112 row_half_mirror row_mask:0xf bank_mask:0xf bound_ctrl:1\n" \
"v_mov_b32 v101,s29\nbuffer_store_dwordx2 v[100:101],v102,s[36:39],0 offen sc1\nv_max_u32_dpp v112,v112,v112 row_mirror row_mask:0xf bank_mask:0xf bound_ctrl:1\nds_read_b64_tr_b16 a[0:1],v8\n" \
"v_readlane_b32 s94,v112,0\nv_readlane_b32 s31,v112,16\nv_readlane_b32 s96,v112,32\nv_readlane_b32 s97,v112,48\n" \
"s_max_u32 s31,s31,s96\ns_max_u32 s94,s94,s97\ns_max_u32 s94,s94,s31\ns_lshr_b32 s94,s94,24\n" \
"s_max_u32 s94,s94,64\ns_min_u32 s94,s94,250\nds_read_b64_tr_b16 a[4:5],v9\nds_read_b64_tr_b16 a[8:9],v10\n" \
"ds_read_b64_tr_b16 a[12:13],v11\nds_read_b64_tr_b16 a[16:17],v8 offset:2048\nds_read_b64_tr_b16 a[20:21],v9 offset:2048\nds_read_b64_tr_b16 a[24:25],v10 offset:2048\n" \
"ds_read_b64_tr_b16 a[28:29],v11 offset:2048\ns_add_u32 s95,s92,9658368\ns_add_u32 m0,s57,0\nds_read_b64_tr_b16 a[2:3],v8 offset:4096\n" \
"s_waitcnt lgkmcnt(1)\nbuffer_load_dwordx4 v4,s[8:11],s95 offen lds\nbuffer_load_dwordx4 v5,s[8:11],s95 offen offset:1024 lds\nbuffer_load_dwordx4 v6,s[8:11],s95 offen offset:2048 lds\n" \
"buffer_load_dwordx4 v7,s[8:11],s95 offen offset:3072 lds\nds_read_b64_tr_b16 a[6:7],v9 offset:4096\nds_read_b64_tr_b16 a[10:11],v10 offset:4096\nds_read_b64_tr_b16 a[14:15],v11 offset:4096\n" \
"ds_read_b64_tr_b16 a[18:19],v8 offset:6144\nds_read_b64_tr_b16 a[22:23],v9 offset:6144\nds_read_b64_tr_b16 a[26:27],v10 offset:6144\nds_read_b64_tr_b16 a[30:31],v11 offset:6144\n" \
"s_add_u32 s100,s92,294912\ns_add_u32 m0,s57,4096\nds_read_b64_tr_b16 a[32:33],v8 offset:8192\ns_waitcnt lgkmcnt(1)\n" \
"buffer_load_dwordx4 v4,s[8:11],s100 offen lds\nbuffer_load_dwordx4 v5,s[8:11],s100 offen offset:1024 lds\nbuffer_load_dwordx4 v6,s[8:11],s100 offen offset:2048 lds\nbuffer_load_dwordx4 v7,s[8:11],s100 offen offset:3072 lds\n" \
"ds_read_b64_tr_b16 a[36:37],v9 offset:8192\nds_read_b64_tr_b16 a[40:41],v10 offset:8192\nds_read_b64_tr_b16 a[44:45],v11 offset:8192\nds_read_b64_tr_b16 a[48:49],v8 offset:10240\n" \
"ds_read_b64_tr_b16 a[52:53],v9 offset:10240\nds_read_b64_tr_b16 a[56:57],v10 offset:10240\nds_read_b64_tr_b16 a[60:61],v11 offset:10240\ns_add_u32 s101,s92,9732096\n" \
"s_add_u32 m0,s57,8192\nds_read_b64_tr_b16 a[34:35],v8 offset:12288\ns_waitcnt lgkmcnt(1)\nbuffer_load_dwordx4 v4,s[8:11],s101 offen lds\n" \
"buffer_load_dwordx4 v5,s[8:11],s101 offen offset:1024 lds\nbuffer_load_dwordx4 v6,s[8:11],s101 offen offset:2048 lds\nbuffer_load_dwordx4 v7,s[8:11],s101 offen offset:3072 lds\nds_read_b64_tr_b16 a[38:39],v9 offset:12288\n" \
"ds_read_b64_tr_b16 a[42:43],v10 offset:12288\nds_read_b64_tr_b16 a[46:47],v11 offset:12288\nds_read_b64_tr_b16 a[50:51],v8 offset:14336\nds_read_b64_tr_b16 a[54:55],v9 offset:14336\n" \
"ds_read_b64_tr_b16 a[58:59],v10 offset:14336\nds_read_b64_tr_b16 a[62:63],v11 offset:14336\ns_add_u32 s31,s92,368640\ns_add_u32 m0,s57,12288\n" \
"ds_read_b64_tr_b16 a[64:65],v8 offset:16384\ns_waitcnt lgkmcnt(1)\nbuffer_load_dwordx4 v4,s[8:11],s31 offen lds\nbuffer_load_dwordx4 v5,s[8:11],s31 offen offset:1024 lds\n" \
"buffer_load_dwordx4 v6,s[8:11],s31 offen offset:2048 lds\nbuffer_load_dwordx4 v7,s[8:11],s31 offen offset:3072 lds\nbuffer_load_dwordx4 v[80:83],v120,s[36:39],s5 offen sc1\nbuffer_load_dwordx4 v[84:87],v120,s[36:39],s5 offen offset:2304 sc1\n" \
"buffer_load_dwordx4 v[88:91],v120,s[36:39],s6 offen sc1\nbuffer_load_dwordx4 v[92:95],v120,s[36:39],s6 offen offset:2304 sc1\nds_read_b64_tr_b16 a[68:69],v9 offset:16384\nds_read_b64_tr_b16 a[72:73],v10 offset:16384\n" \
"ds_read_b64_tr_b16 a[76:77],v11 offset:16384\nds_read_b64_tr_b16 a[80:81],v8 offset:18432\nds_read_b64_tr_b16 a[84:85],v9 offset:18432\nds_read_b64_tr_b16 a[88:89],v10 offset:18432\n" \
"ds_read_b64_tr_b16 a[92:93],v11 offset:18432\ns_add_u32 s96,s92,9805824\ns_add_u32 m0,s57,16384\nds_read_b64_tr_b16 a[66:67],v8 offset:20480\n" \
"s_waitcnt lgkmcnt(1)\nbuffer_load_dwordx4 v4,s[8:11],s96 offen lds\nbuffer_load_dwordx4 v5,s[8:11],s96 offen offset:1024 lds\nbuffer_load_dwordx4 v6,s[8:11],s96 offen offset:2048 lds\n" \
"buffer_load_dwordx4 v7,s[8:11],s96 offen offset:3072 lds\nds_read_b64_tr_b16 a[70:71],v9 offset:20480\nds_read_b64_tr_b16 a[74:75],v10 offset:20480\nds_read_b64_tr_b16 a[78:79],v11 offset:20480\n" \
"ds_read_b64_tr_b16 a[82:83],v8 offset:22528\nds_read_b64_tr_b16 a[86:87],v9 offset:22528\nds_read_b64_tr_b16 a[90:91],v10 offset:22528\nds_read_b64_tr_b16 a[94:95],v11 offset:22528\n" \
"s_add_u32 s97,s92,442368\ns_add_u32 m0,s57,20480\nds_read_b64_tr_b16 a[96:97],v8 offset:24576\ns_waitcnt lgkmcnt(1)\n" \
"buffer_load_dwordx4 v4,s[8:11],s97 offen lds\nbuffer_load_dwordx4 v5,s[8:11],s97 offen offset:1024 lds\nbuffer_load_dwordx4 v6,s[8:11],s97 offen offset:2048 lds\nbuffer_load_dwordx4 v7,s[8:11],s97 offen offset:3072 lds\n" \
"ds_read_b64_tr_b16 a[100:101],v9 offset:24576\nds_read_b64_tr_b16 a[104:105],v10 offset:24576\nds_read_b64_tr_b16 a[108:109],v11 offset:24576\nds_read_b64_tr_b16 a[112:113],v8 offset:26624\n" \
"ds_read_b64_tr_b16 a[116:117],v9 offset:26624\nds_read_b64_tr_b16 a[120:121],v10 offset:26624\nds_read_b64_tr_b16 a[124:125],v11 offset:26624\ns_add_u32 s95,s92,9879552\n" \
"s_add_u32 m0,s57,24576\ns_waitcnt vmcnt(24)\nds_read_b64_tr_b16 a[98:99],v8\ns_waitcnt lgkmcnt(1)\n" \
"buffer_load_dwordx4 v4,s[8:11],s95 offen lds\nbuffer_load_dwordx4 v5,s[8:11],s95 offen offset:1024 lds\nbuffer_load_dwordx4 v6,s[8:11],s95 offen offset:2048 lds\nbuffer_load_dwordx4 v7,s[8:11],s95 offen offset:3072 lds\n" \
"ds_read_b64_tr_b16 a[102:103],v9\nds_read_b64_tr_b16 a[106:107],v10\nds_read_b64_tr_b16 a[110:111],v11\nds_read_b64_tr_b16 a[114:115],v8 offset:2048\n" \
"ds_read_b64_tr_b16 a[118:119],v9 offset:2048\nds_read_b64_tr_b16 a[122:123],v10 offset:2048\nds_read_b64_tr_b16 a[126:127],v11 offset:2048\ns_add_u32 s100,s92,516096\n" \
"s_add_u32 m0,s57,0\ns_waitcnt vmcnt(24)\nds_read_b64_tr_b16 a[128:129],v8 offset:4096\ns_waitcnt lgkmcnt(1)\n" \
"buffer_load_dwordx4 v4,s[8:11],s100 offen lds\nbuffer_load_dwordx4 v5,s[8:11],s100 offen offset:1024 lds\nbuffer_load_dwordx4 v6,s[8:11],s100 offen offset:2048 lds\nbuffer_load_dwordx4 v7,s[8:11],s100 offen offset:3072 lds\n" \
"ds_read_b64_tr_b16 a[132:133],v9 offset:4096\nds_read_b64_tr_b16 a[136:137],v10 offset:4096\nds_read_b64_tr_b16 a[140:141],v11 offset:4096\nds_read_b64_tr_b16 a[144:145],v8 offset:6144\n" \
"ds_read_b64_tr_b16 a[148:149],v9 offset:6144\nds_read_b64_tr_b16 a[152:153],v10 offset:6144\nds_read_b64_tr_b16 a[156:157],v11 offset:6144\ns_add_u32 s101,s92,9953280\n" \
"s_add_u32 m0,s57,4096\ns_waitcnt vmcnt(24)\nds_read_b64_tr_b16 a[130:131],v8 offset:8192\ns_waitcnt lgkmcnt(1)\n" \
"buffer_load_dwordx4 v4,s[8:11],s101 offen lds\nbuffer_load_dwordx4 v5,s[8:11],s101 offen offset:1024 lds\nbuffer_load_dwordx4 v6,s[8:11],s101 offen offset:2048 lds\nbuffer_load_dwordx4 v7,s[8:11],s101 offen offset:3072 lds\n" \
"ds_read_b64_tr_b16 a[134:135],v9 offset:8192\nds_read_b64_tr_b16 a[138:139],v10 offset:8192\nds_read_b64_tr_b16 a[142:143],v11 offset:8192\nds_read_b64_tr_b16 a[146:147],v8 offset:10240\n" \
"ds_read_b64_tr_b16 a[150:151],v9 offset:10240\nds_read_b64_tr_b16 a[154:155],v10 offset:10240\nds_read_b64_tr_b16 a[158:159],v11 offset:10240\ns_bfe_u32 s26,s33,0x20001\n" \
"s_lshl2_add_u32 s15,s32,s26\ns_lshl2_add_u32 s15,s15,s3\ns_lshl_b32 s15,s15,14\ns_add_u32 s15,s15,5308416\n" \
"s_add_u32 s31,s15,0\ns_add_u32 m0,s57,8192\ns_waitcnt vmcnt(24)\nds_read_b64_tr_b16 a[160:161],v8 offset:12288\n" \
"s_waitcnt lgkmcnt(1)\nbuffer_load_dwordx4 v1,s[36:39],s31 offen sc1 lds\nbuffer_load_dwordx4 v1,s[36:39],s31 offen offset:1024 sc1 lds\nbuffer_load_dwordx4 v1,s[36:39],s31 offen offset:2048 sc1 lds\n" \
"buffer_load_dwordx4 v1,s[36:39],s31 offen offset:3072 sc1 lds\nds_read_b64_tr_b16 a[164:165],v9 offset:12288\nds_read_b64_tr_b16 a[168:169],v10 offset:12288\nds_read_b64_tr_b16 a[172:173],v11 offset:12288\n" \
"ds_read_b64_tr_b16 a[176:177],v8 offset:14336\nds_read_b64_tr_b16 a[180:181],v9 offset:14336\nds_read_b64_tr_b16 a[184:185],v10 offset:14336\nds_read_b64_tr_b16 a[188:189],v11 offset:14336\n" \
"s_add_u32 s96,s15,4096\ns_add_u32 m0,s57,12288\ns_waitcnt vmcnt(20)\nds_read_b64_tr_b16 a[162:163],v8 offset:16384\n" \
"s_waitcnt lgkmcnt(1)\nbuffer_load_dwordx4 v1,s[36:39],s96 offen sc1 lds\nbuffer_load_dwordx4 v1,s[36:39],s96 offen offset:1024 sc1 lds\nbuffer_load_dwordx4 v1,s[36:39],s96 offen offset:2048 sc1 lds\n" \
"buffer_load_dwordx4 v1,s[36:39],s96 offen offset:3072 sc1 lds\nds_read_b64_tr_b16 a[166:167],v9 offset:16384\nds_read_b64_tr_b16 a[170:171],v10 offset:16384\nds_read_b64_tr_b16 a[174:175],v11 offset:16384\n" \
"ds_read_b64_tr_b16 a[178:179],v8 offset:18432\nds_read_b64_tr_b16 a[182:183],v9 offset:18432\nds_read_b64_tr_b16 a[186:187],v10 offset:18432\nds_read_b64_tr_b16 a[190:191],v11 offset:18432\n" \
"s_add_u32 s97,s15,8192\ns_add_u32 m0,s57,16384\ns_waitcnt vmcnt(20)\nds_read_b64_tr_b16 a[192:193],v8 offset:20480\n" \
"s_waitcnt lgkmcnt(1)\nbuffer_load_dwordx4 v1,s[36:39],s97 offen sc1 lds\nbuffer_load_dwordx4 v1,s[36:39],s97 offen offset:1024 sc1 lds\nbuffer_load_dwordx4 v1,s[36:39],s97 offen offset:2048 sc1 lds\n" \
"buffer_load_dwordx4 v1,s[36:39],s97 offen offset:3072 sc1 lds\nds_read_b64_tr_b16 a[196:197],v9 offset:20480\nds_read_b64_tr_b16 a[200:201],v10 offset:20480\nds_read_b64_tr_b16 a[204:205],v11 offset:20480\n" \
"ds_read_b64_tr_b16 a[208:209],v8 offset:22528\nds_read_b64_tr_b16 a[212:213],v9 offset:22528\nds_read_b64_tr_b16 a[216:217],v10 offset:22528\nds_read_b64_tr_b16 a[220:221],v11 offset:22528\n" \
"s_add_u32 s95,s15,12288\ns_add_u32 m0,s57,20480\ns_waitcnt lgkmcnt(0)\nbuffer_load_dwordx4 v1,s[36:39],s95 offen sc1 lds\n" \
"buffer_load_dwordx4 v1,s[36:39],s95 offen offset:1024 sc1 lds\nbuffer_load_dwordx4 v1,s[36:39],s95 offen offset:2048 sc1 lds\nbuffer_load_dwordx4 v1,s[36:39],s95 offen offset:3072 sc1 lds\nv_cmp_ne_u64 vcc,s[28:29],v[80:81]\n" \
"v_cmp_ne_u64 s[24:25],s[28:29],v[84:85]\nv_cmp_ne_u64 s[26:27],s[28:29],v[88:89]\ns_or_b64 vcc,vcc,s[24:25]\nv_cmp_ne_u64 s[24:25],s[28:29],v[92:93]\n" \
"s_or_b64 vcc,vcc,s[26:27]\ns_or_b64 vcc,vcc,s[24:25]\ns_cbranch_vccnz .LsnP_%=\n.LbnP_%=:\n" \
"v_bfe_u32 v112,v0,1,5\nv_mul_u32_u24 v112,72,v112\nv_lshrrev_b32 v113,6,v0\nv_mul_u32_u24 v114,144,v0\n" \
"v_lshl_add_u32 v112,v113,5,v112\nv_subrev_u32 v114,18368,v114\nv_mov_b32 v115,1048576\nv_cmp_gt_u32 vcc,144,v0\n" \
"v_and_b32 v113,1,v0\nv_lshl_add_u32 v112,v113,4,v112\nv_cndmask_b32 v114,v115,v114,vcc\nv_and_b32 v97,0xffff0000,v82\n" \
"v_lshlrev_b32 v96,16,v82\ns_add_u32 s24,s93,s94\nv_cmp_gt_u32 vcc,128,v0\nv_add_u32 v113,8,v112\n" \
"v_mul_f32 v96,0x3ee38e39,v96\nv_mul_f32 v97,0x3ee38e39,v97\ns_sub_u32 s24,s24,253\nv_cndmask_b32 v112,v114,v112,vcc\n" \
"v_and_b32 v99,0xffff0000,v83\nv_ldexp_f32 v96,v96,s24\nv_ldexp_f32 v97,v97,s24\nv_lshlrev_b32_e64 v98,16,v83\n" \
"ds_write_b64 v112,v[96:97] offset:28800\nv_add_u32 v115,72,v114\nv_mul_f32 v98,0x3ee38e39,v98\nv_mul_f32 v99,0x3ee38e39,v99\n" \
"v_cndmask_b32_e64 v113,v115,v113,vcc\nv_ldexp_f32 v98,v98,s24\nv_ldexp_f32 v99,v99,s24\nds_write_b64 v113,v[98:99] offset:28800\n" \
"v_and_b32 v101,0xffff0000,v86\nv_lshlrev_b32 v100,16,v86\nds_write_b64 v112,v[100:101] offset:31104\nv_and_b32 v103,0xffff0000,v87\n" \
"v_lshlrev_b32 v102,16,v87\ns_lshr_b32 s25,s33,3\nds_write_b64 v113,v[102:103] offset:31104\ns_mul_i32 s25,s25,576\n" \
"v_and_b32 v105,0xffff0000,v90\nv_lshlrev_b32 v104,16,v90\ns_sub_u32 s25,163200,s25\nds_write_b64 v112,v[104:105] offset:33408\n" \
"v_add_u32 v114,s25,v2\nv_subrev_u32 v115,163200,v114\nv_and_b32 v107,0xffff0000,v91\nv_lshlrev_b32 v106,16,v91\n" \
"v_mov_b32 v116,1048576\nv_cmp_gt_u32 vcc,576,v115\nds_write_b64 v113,v[106:107] offset:33408\nv_and_b32 v109,0xffff0000,v94\n" \
"v_cndmask_b32 v114,v116,v114,vcc\nv_and_b32 v111,0xffff0000,v95\nv_lshlrev_b32 v108,16,v94\nv_lshlrev_b32 v110,16,v95\n" \
"ds_write_b128 v114,v[108:111]\nv_and_b32 v124,48,v0\nv_cmp_gt_u32 vcc,64,v0\nv_bfe_u32 v127,v0,5,1\n" \
"v_and_b32 v13,31,v0\nv_cndmask_b32_e64 v124,v124,v3,vcc\nv_mul_u32_u24 v127,2448,v127\nv_mul_u32_u24 v13,72,v13\n" \
"s_mul_i32 s27,s3,4896\nv_add3_u32 v13,v13,v127,s27\nv_mov_b32 v127,0\ns_waitcnt vmcnt(24)\n" \
"ds_read_b64_tr_b16 a[194:195],v8 offset:24576\nds_read_b64_tr_b16 a[198:199],v9 offset:24576\nds_read_b64_tr_b16 a[202:203],v10 offset:24576\nds_read_b64_tr_b16 a[206:207],v11 offset:24576\n" \
"ds_read_b64_tr_b16 a[210:211],v8 offset:26624\nds_read_b64_tr_b16 a[214:215],v9 offset:26624\nds_read_b64_tr_b16 a[218:219],v10 offset:26624\nds_read_b64_tr_b16 a[222:223],v11 offset:26624\n" \
"s_waitcnt vmcnt(20)\nds_read_b64_tr_b16 a[224:225],v8\nds_read_b64_tr_b16 a[228:229],v9\nds_read_b64_tr_b16 a[232:233],v10\n" \
"ds_read_b64_tr_b16 a[236:237],v11\nds_read_b64_tr_b16 a[240:241],v8 offset:2048\nds_read_b64_tr_b16 a[244:245],v9 offset:2048\ns_waitcnt lgkmcnt(14)\n" \
"s_barrier\nbuffer_load_dwordx4 v[112:115],v3,s[36:39],s16 offen sc1\nbuffer_load_dwordx4 v[116:119],v3,s[36:39],s17 offen sc1\nbuffer_load_dwordx4 v[120:123],v124,s[36:39],s18 offen sc1\n" \
"ds_read_b64_tr_b16 a[248:249],v10 offset:2048\nds_read_b64_tr_b16 a[252:253],v11 offset:2048\ns_waitcnt vmcnt(19)\nds_read_b64_tr_b16 a[226:227],v8 offset:4096\n" \
"ds_read_b64_tr_b16 a[230:231],v9 offset:4096\nds_read_b64_tr_b16 a[234:235],v10 offset:4096\nds_read_b64_tr_b16 a[238:239],v11 offset:4096\nds_read_b64_tr_b16 a[242:243],v8 offset:6144\n" \
"ds_read_b64_tr_b16 a[246:247],v9 offset:6144\nds_read_b64_tr_b16 a[250:251],v10 offset:6144\nds_read_b64_tr_b16 a[254:255],v11 offset:6144\ns_waitcnt vmcnt(15)\n" \
"ds_read_b64_tr_b16 v[192:193],v15 offset:8192\nds_read_b64_tr_b16 v[196:197],v15 offset:8704\nds_read_b64_tr_b16 v[194:195],v15 offset:9216\nds_read_b64_tr_b16 v[198:199],v15 offset:9728\n" \
"ds_read_b64_tr_b16 v[200:201],v15 offset:10240\nds_read_b64_tr_b16 v[204:205],v15 offset:10752\nds_read_b64_tr_b16 v[202:203],v15 offset:11264\nds_read_b64_tr_b16 v[206:207],v15 offset:11776\n" \
"s_waitcnt vmcnt(11)\nds_read_b64_tr_b16 v[208:209],v15 offset:12288\nds_read_b64_tr_b16 v[212:213],v15 offset:12800\nds_read_b64_tr_b16 v[210:211],v15 offset:13312\n" \
"ds_read_b64_tr_b16 v[214:215],v15 offset:13824\nds_read_b64_tr_b16 v[216:217],v15 offset:14336\nds_read_b64_tr_b16 v[220:221],v15 offset:14848\nds_read_b64_tr_b16 v[218:219],v15 offset:15360\n" \
"ds_read_b64_tr_b16 v[222:223],v15 offset:15872\ns_waitcnt vmcnt(7)\nds_read_b64_tr_b16 v[224:225],v15 offset:16384\nds_read_b64_tr_b16 v[228:229],v15 offset:16896\n" \
"ds_read_b64_tr_b16 v[226:227],v15 offset:17408\nds_read_b64_tr_b16 v[230:231],v15 offset:17920\nds_read_b64_tr_b16 v[232:233],v15 offset:18432\nds_read_b64_tr_b16 v[236:237],v15 offset:18944\n" \
"ds_read_b64_tr_b16 v[234:235],v15 offset:19456\nds_read_b64_tr_b16 v[238:239],v15 offset:19968\ns_waitcnt vmcnt(3)\nds_read_b64_tr_b16 v[240:241],v15 offset:20480\n" \
"ds_read_b64_tr_b16 v[244:245],v15 offset:20992\nds_read_b64_tr_b16 v[242:243],v15 offset:21504\nds_read_b64_tr_b16 v[246:247],v15 offset:22016\nds_read_b64_tr_b16 v[248:249],v15 offset:22528\n" \
"ds_read_b64_tr_b16 v[252:253],v15 offset:23040\nds_read_b64_tr_b16 v[250:251],v15 offset:23552\nds_read_b64_tr_b16 v[254:255],v15 offset:24064\ns_waitcnt vmcnt(1)\n" \
"v_and_b32 v125,v112,v116\ns_waitcnt vmcnt(0)\nv_bitop3_b32 v125,v125,v120,1 bitop3:0x80\nv_cmp_eq_u32 vcc,0,v125\n" \
"s_nop 0\ns_cbranch_vccnz .LsuP_%=\n.LbuP_%=:\nv_mfma_f32_16x16x32_f16 v[80:83],v[32:35],v[112:115],0\n" \
"v_and_b32 v3,31,v0\nv_mul_u32_u24 v3,72,v3\nv_mfma_f32_16x16x32_f16 v[84:87],v[36:39],v[112:115],0\nv_add_u32 v13,9216,v13\n" \
"v_mov_b32 v104,-1\nv_mfma_f32_16x16x32_f16 v[88:91],v[32:35],v[116:119],0\nv_mov_b32 v105,-1\nv_mov_b32 v106,-1\n" \
"v_mfma_f32_16x16x32_f16 v[92:95],v[36:39],v[116:119],0\nv_mov_b32 v107,-1\ns_add_u32 s27,s2,0\nv_mfma_f32_16x16x32_f16 v[96:99],v[32:35],v[120:123],0\n" \
"s_lshr_b32 s100,s27,4\ns_mul_i32 s101,s100,37\ns_lshr_b32 s101,s101,8\ns_lshl2_add_u32 s100,s101,s100\n" \
"s_mul_i32 s100,s100,409600\ns_and_b32 s101,s27,15\ns_lshl_b32 s101,s101,14\ns_add_u32 s100,s100,s101\n" \
"s_add_u32 s100,s100,13090816\ns_cmp_lt_u32 s27,158\ns_cselect_b32 s100,s100,2147483648\nbuffer_store_dwordx4 v[104:107],v2,s[36:39],s100 offen sc1\n" \
"v_mfma_f32_16x16x32_f16 v[100:103],v[36:39],v[120:123],0\nv_cvt_pk_f16_f32 v112,v84,v80\nv_cvt_pk_f16_f32 v113,v85,v81\nv_cvt_pk_f16_f32 v114,v86,v82\n" \
"v_cvt_pk_f16_f32 v115,v87,v83\nds_write_b128 v57,v[112:115]\nv_cvt_pk_f16_f32 v116,v92,v88\nv_cvt_pk_f16_f32 v117,v93,v89\n" \
"s_add_u32 s100,s100,4096\nbuffer_store_dwordx4 v[104:107],v2,s[36:39],s100 offen sc1\nv_cvt_pk_f16_f32 v118,v94,v90\nv_cvt_pk_f16_f32 v119,v95,v91\n" \
"ds_write_b128 v58,v[116:119]\nv_cvt_pk_f16_f32 v120,v100,v96\nv_cvt_pk_f16_f32 v121,v101,v97\nv_cvt_pk_f16_f32 v122,v102,v98\n" \
"v_cvt_pk_f16_f32 v123,v103,v99\nds_write_b128 v60,v[120:123]\ns_add_u32 s100,s100,4096\nbuffer_store_dwordx4 v[104:107],v2,s[36:39],s100 offen sc1\n" \
"v_add_u32 v60,31104,v3\nv_add_u32 v3,28800,v3\nds_read2_b64 v[32:35],v3 offset0:0 offset1:1\nds_read2_b64 v[36:39],v3 offset0:2 offset1:3\n" \
"s_add_u32 s100,s100,4096\ns_add_u32 s27,s2,111\ns_lshr_b32 s31,s27,4\ns_mul_i32 s96,s31,37\n" \
"s_lshr_b32 s96,s96,8\ns_lshl2_add_u32 s31,s96,s31\ns_mul_i32 s31,s31,409600\ns_and_b32 s96,s27,15\n" \
"s_lshl_b32 s96,s96,14\nbuffer_store_dwordx4 v[104:107],v2,s[36:39],s100 offen sc1\ns_add_u32 s31,s31,s96\ns_add_u32 s31,s31,13090816\n" \
"s_cmp_lt_u32 s27,158\ns_cselect_b32 s31,s31,2147483648\ns_waitcnt lgkmcnt(2)\ns_barrier\n" \
"ds_read_b64 v[112:113],v56\nds_read_b64 v[116:117],v56 offset:512\nds_read_b64 v[120:121],v56 offset:256\nds_read_b64 v[124:125],v56 offset:768\n" \
"ds_read_b64 v[114:115],v56 offset:1024\nds_read_b64 v[122:123],v56 offset:1280\nds_read_b64 v[56:57],v3 offset:64\nbuffer_store_dwordx4 v[104:107],v2,s[36:39],s31 offen sc1\n" \
"s_add_u32 s31,s31,4096\nv_bfe_u32 v110,v0,1,2\nv_bfe_u32 v111,v0,4,1\nv_xor_b32 v110,v110,v111\n" \
"v_and_b32 v111,1,v0\nv_lshl_add_u32 v110,v110,1,v111\nv_bfe_u32 v111,v0,3,3\nv_lshlrev_b32 v111,12,v111\n" \
"v_lshl_add_u32 v4,v110,4,v111\nv_xor_b32 v5,64,v4\nv_add_u32 v5,31744,v5\nv_add_u32 v6,63488,v4\n" \
"buffer_store_dwordx4 v[104:107],v2,s[36:39],s31 offen sc1\ns_waitcnt lgkmcnt(5)\nv_swap_b32 v113,v116\ns_add_u32 s31,s31,4096\n" \
"s_waitcnt lgkmcnt(3)\nv_swap_b32 v121,v124\nv_add_u32 v7,63488,v5\ns_waitcnt lgkmcnt(2)\n" \
"v_mfma_f32_16x16x32_f16 v[80:83],v[112:115],v[40:43],0\nv_mov_b32 v118,v115\ns_waitcnt lgkmcnt(1)\nv_mov_b32 v126,v123\n" \
"v_mfma_f32_16x16x32_f16 v[84:87],v[112:115],v[44:47],0\ns_lshr_b32 s26,s32,1\ns_mul_i32 s92,s26,1152\nv_mfma_f32_16x16x32_f16 v[88:91],v[120:123],v[40:43],0\n" \
"buffer_store_dwordx4 v[104:107],v2,s[36:39],s31 offen sc1\nv_mfma_f32_16x16x32_f16 v[92:95],v[120:123],v[44:47],0\ns_add_u32 s31,s31,4096\ns_lshr_b32 s26,s3,1\n" \
"v_mfma_f32_16x16x32_f16 v[96:99],v[116:119],v[40:43],0\ns_lshl_b32 s26,s26,6\ns_add_u32 s92,s92,s26\nv_mfma_f32_16x16x32_f16 v[100:103],v[116:119],v[44:47],0\n" \
"v_pk_add_f32 v[88:89],v[80:81],v[88:89] neg_lo:[0,1] neg_hi:[0,1]\nv_pk_add_f32 v[90:91],v[82:83],v[90:91] neg_lo:[0,1] neg_hi:[0,1]\nv_pk_add_f32 v[92:93],v[84:85],v[92:93] neg_lo:[0,1] neg_hi:[0,1]\nbuffer_store_dwordx4 v[104:107],v2,s[36:39],s31 offen sc1\n" \
"v_pk_add_f32 v[94:95],v[86:87],v[94:95] neg_lo:[0,1] neg_hi:[0,1]\nv_pk_fma_f32 v[80:81],v[80:81],2.0,v[88:89] op_sel_hi:[1,0,1] neg_lo:[0,0,1] neg_hi:[0,0,1]\nv_pk_fma_f32 v[82:83],v[82:83],2.0,v[90:91] op_sel_hi:[1,0,1] neg_lo:[0,0,1] neg_hi:[0,0,1]\nv_pk_fma_f32 v[84:85],v[84:85],2.0,v[92:93] op_sel_hi:[1,0,1] neg_lo:[0,0,1] neg_hi:[0,0,1]\n" \
"v_pk_fma_f32 v[86:87],v[86:87],2.0,v[94:95] op_sel_hi:[1,0,1] neg_lo:[0,0,1] neg_hi:[0,0,1]\nv_cvt_pk_f16_f32 v112,v80,v84\nv_cvt_pk_f16_f32 v113,v81,v85\nv_cvt_pk_f16_f32 v114,v82,v86\n" \
"v_cvt_pk_f16_f32 v115,v83,v87\nv_mfma_f32_16x16x32_f16 v[104:107],v[124:127],v[40:43],0\nv_cvt_pk_f16_f32 v116,v88,v92\ns_lshr_b32 s97,s33,3\n" \
"s_add_u32 s97,s97,0\ns_sub_u32 s95,s97,4\ns_min_u32 s97,s97,s95\ns_lshl_b32 s95,s97,12\n" \
"s_add_u32 s95,s95,s57\ns_add_u32 m0,s95,0\ns_and_b32 s95,s97,1\ns_lshr_b32 s97,s97,1\n" \
"s_lshl_b32 s97,s97,10\ns_add_u32 s97,s97,s95\ns_lshl_b32 s97,s97,7\ns_lshr_b32 s100,s33,3\n" \
"s_add_u32 s100,s100,1\ns_sub_u32 s101,s100,4\ns_min_u32 s100,s100,s101\ns_lshl_b32 s101,s100,12\n" \
"s_add_u32 s101,s101,s57\ns_lshr_b32 s31,s33,3\ns_add_u32 s31,s31,2\ns_sub_u32 s96,s31,4\n" \
"s_min_u32 s31,s31,s96\ns_lshl_b32 s96,s31,12\ns_add_u32 s96,s96,s57\nv_mfma_f32_16x16x32_f16 v[80:83],v[16:19],v[112:115],0\n" \
"v_cvt_pk_f16_f32 v117,v89,v93\nv_cvt_pk_f16_f32 v118,v90,v94\nv_mfma_f32_16x16x32_f16 v[84:87],v[20:23],v[112:115],0\nv_cvt_pk_f16_f32 v119,v91,v95\n" \
"v_pk_add_f32 v[104:105],v[96:97],v[104:105] neg_lo:[0,1] neg_hi:[0,1]\nv_mfma_f32_16x16x32_f16 v[108:111],v[124:127],v[44:47],0\nv_pk_add_f32 v[106:107],v[98:99],v[106:107] neg_lo:[0,1] neg_hi:[0,1]\nv_pk_fma_f32 v[96:97],v[96:97],2.0,v[104:105] op_sel_hi:[1,0,1] neg_lo:[0,0,1] neg_hi:[0,0,1]\n" \
"v_mfma_f32_16x16x32_f16 v[88:91],v[24:27],v[116:119],0\nv_pk_fma_f32 v[98:99],v[98:99],2.0,v[106:107] op_sel_hi:[1,0,1] neg_lo:[0,0,1] neg_hi:[0,0,1]\nds_read2_b64 v[40:43],v3 offset0:4 offset1:5\nv_mfma_f32_16x16x32_f16 v[92:95],v[28:31],v[116:119],0\n" \
"ds_write2_b32 v48,v84,v80 offset0:0 offset1:1\nds_write2_b32 v49,v85,v81 offset0:0 offset1:1\nds_write2_b32 v50,v86,v82 offset0:0 offset1:1\nv_pk_add_f32 v[108:109],v[100:101],v[108:109] neg_lo:[0,1] neg_hi:[0,1]\n" \
"v_pk_add_f32 v[110:111],v[102:103],v[110:111] neg_lo:[0,1] neg_hi:[0,1]\nv_pk_fma_f32 v[100:101],v[100:101],2.0,v[108:109] op_sel_hi:[1,0,1] neg_lo:[0,0,1] neg_hi:[0,0,1]\nv_pk_fma_f32 v[102:103],v[102:103],2.0,v[110:111] op_sel_hi:[1,0,1] neg_lo:[0,0,1] neg_hi:[0,0,1]\nds_write2_b32 v51,v87,v83 offset0:0 offset1:1\n" \
"v_cvt_pk_f16_f32 v120,v96,v100\nv_cvt_pk_f16_f32 v121,v97,v101\nv_cvt_pk_f16_f32 v122,v98,v102\nv_cvt_pk_f16_f32 v123,v99,v103\n" \
"ds_write2_b32 v52,v92,v88 offset0:0 offset1:1\nds_write2_b32 v53,v93,v89 offset0:0 offset1:1\nv_mfma_f32_16x16x32_f16 v[96:99],v[16:19],v[120:123],0\nds_write2_b32 v54,v94,v90 offset0:0 offset1:1\n" \
"v_mfma_f32_16x16x32_f16 v[100:103],v[20:23],v[120:123],0\nds_write2_b32 v55,v95,v91 offset0:0 offset1:1\nv_cvt_pk_f16_f32 v124,v104,v108\nv_cvt_pk_f16_f32 v125,v105,v109\n" \
"v_cvt_pk_f16_f32 v126,v106,v110\nv_cvt_pk_f16_f32 v127,v107,v111\nv_add_u32 v48,2448,v48\nv_add_u32 v49,2448,v49\n" \
"v_mfma_f32_16x16x32_f16 v[104:107],v[24:27],v[124:127],0\nds_write2_b32 v48,v100,v96 offset0:0 offset1:1\nv_mfma_f32_16x16x32_f16 v[108:111],v[28:31],v[124:127],0\nds_write2_b32 v49,v101,v97 offset0:0 offset1:1\n" \
"v_add_u32 v50,2448,v50\nds_write2_b32 v50,v102,v98 offset0:0 offset1:1\nv_add_u32 v51,2448,v51\nds_write2_b32 v51,v103,v99 offset0:0 offset1:1\n" \
"v_add_u32 v52,2448,v52\nv_add_u32 v53,2448,v53\nv_add_u32 v54,2448,v54\nds_write2_b32 v52,v108,v104 offset0:0 offset1:1\n" \
"ds_write2_b32 v53,v109,v105 offset0:0 offset1:1\nds_write2_b32 v54,v110,v106 offset0:0 offset1:1\nv_add_u32 v55,2448,v55\nds_write2_b32 v55,v111,v107 offset0:0 offset1:1\n" \
"ds_read2_b64 v[80:83],v13 offset0:0 offset1:1\nds_read2_b64 v[84:87],v13 offset0:2 offset1:3\ns_lshl_b32 s26,s3,8\ns_add_u32 s92,s92,s26\n" \
"s_lshl_b32 s92,s92,4\ns_and_b32 s26,s32,1\ns_lshl3_add_u32 s92,s26,s92\ns_and_b32 s26,s33,7\n" \
"s_add_u32 s92,s92,s26\nds_read2_b64 v[88:91],v13 offset0:4 offset1:5\ns_lshl_b32 s92,s92,8\ns_add_u32 s97,s97,s92\n" \
"buffer_load_dwordx4 v4,s[40:43],s97 offen lds\nds_read2_b64 v[44:47],v3 offset0:6 offset1:7\nv_and_b32 v48,31,v0\nds_read2_b64 v[92:95],v13 offset0:6 offset1:7\n" \
"v_mad_u32_u24 v49,v48,56,0\nv_sub_u32 v48,v13,v49\nv_and_b32 v49,31,v0\nv_mul_u32_u24 v49,68,v49\n" \
"v_sub_u32 v49,v13,v49\nbuffer_load_dwordx4 v5,s[40:43],s97 offen offset:1024 lds\nds_read_b64 v[96:97],v13 offset:64\ns_waitcnt lgkmcnt(5)\n" \
"v_pk_fma_f32 v[62:63],v[80:81],v[32:33],v[62:63]\nv_pk_fma_f32 v[64:65],v[82:83],v[34:35],v[64:65]\nv_pk_mul_f32 v[102:103],v[62:63],v[62:63]\ns_waitcnt lgkmcnt(4)\n" \
"v_pk_fma_f32 v[66:67],v[84:85],v[36:37],v[66:67]\nv_pk_add_f32 v[100:101],v[62:63],v[64:65]\nv_pk_fma_f32 v[102:103],v[64:65],v[64:65],v[102:103]\nv_pk_fma_f32 v[68:69],v[86:87],v[38:39],v[68:69]\n" \
"buffer_load_dwordx4 v6,s[40:43],s97 offen offset:2048 lds\nv_pk_add_f32 v[100:101],v[100:101],v[66:67]\nv_pk_fma_f32 v[102:103],v[66:67],v[66:67],v[102:103]\nds_read2_b64 v[112:115],v60 offset0:0 offset1:1\n" \
"ds_read2_b64 v[116:119],v60 offset0:2 offset1:3\ns_waitcnt lgkmcnt(5)\nv_pk_fma_f32 v[70:71],v[88:89],v[40:41],v[70:71]\nv_pk_add_f32 v[100:101],v[100:101],v[68:69]\n" \
"ds_read2_b64 v[120:123],v60 offset0:4 offset1:5\nv_pk_fma_f32 v[102:103],v[68:69],v[68:69],v[102:103]\nbuffer_load_dwordx4 v7,s[40:43],s97 offen offset:3072 lds\nds_read2_b64 v[124:127],v60 offset0:6 offset1:7\n" \
"v_pk_fma_f32 v[72:73],v[90:91],v[42:43],v[72:73]\nv_pk_add_f32 v[100:101],v[100:101],v[70:71]\nds_read_b64 v[98:99],v60 offset:64\nv_add_u32 v60,2304,v60\n" \
"v_pk_fma_f32 v[102:103],v[70:71],v[70:71],v[102:103]\nds_read2_b64 v[16:19],v60 offset0:0 offset1:1\ns_waitcnt lgkmcnt(7)\nv_pk_fma_f32 v[74:75],v[92:93],v[44:45],v[74:75]\n" \
"s_add_u32 m0,s101,0\ns_and_b32 s101,s100,1\ns_lshr_b32 s100,s100,1\ns_lshl_b32 s100,s100,10\n" \
"s_add_u32 s100,s100,s101\ns_lshl_b32 s100,s100,7\ns_add_u32 s100,s100,s92\nbuffer_load_dwordx4 v4,s[40:43],s100 offen lds\n" \
"v_pk_add_f32 v[100:101],v[100:101],v[72:73]\nv_pk_fma_f32 v[102:103],v[72:73],v[72:73],v[102:103]\nv_pk_fma_f32 v[76:77],v[94:95],v[46:47],v[76:77]\nv_pk_add_f32 v[100:101],v[100:101],v[74:75]\n" \
"v_pk_fma_f32 v[102:103],v[74:75],v[74:75],v[102:103]\nds_read2_b64 v[20:23],v60 offset0:2 offset1:3\ns_waitcnt lgkmcnt(7)\nv_pk_fma_f32 v[78:79],v[96:97],v[56:57],v[78:79]\n" \
"v_pk_add_f32 v[100:101],v[100:101],v[76:77]\nbuffer_load_dwordx4 v5,s[40:43],s100 offen offset:1024 lds\nv_pk_fma_f32 v[102:103],v[76:77],v[76:77],v[102:103]\nv_pk_add_f32 v[100:101],v[100:101],v[78:79]\n" \
"v_pk_fma_f32 v[102:103],v[78:79],v[78:79],v[102:103]\nds_read2_b64 v[24:27],v60 offset0:4 offset1:5\nv_add_f32 v100,v100,v101\nv_add_f32 v102,v102,v103\n" \
"ds_read2_b64 v[28:31],v60 offset0:6 offset1:7\nds_read_b64 v[58:59],v60 offset:64\nbuffer_load_dwordx4 v6,s[40:43],s100 offen offset:2048 lds\nv_permlane16_swap_b32 v100,v102\n" \
"v_add_f32 v100,v100,v102\ns_lshr_b32 s97,s33,3\ns_add_u32 s97,s97,3\nv_add_f32_dpp v100,v100,v100 quad_perm:[1,0,3,2] row_mask:0xf bank_mask:0xf bound_ctrl:1\n" \
"s_sub_u32 s95,s97,4\ns_min_u32 s97,s97,s95\nv_add_f32_dpp v100,v100,v100 quad_perm:[2,3,0,1] row_mask:0xf bank_mask:0xf bound_ctrl:1\nbuffer_load_dwordx4 v7,s[40:43],s100 offen offset:3072 lds\n" \
"s_add_u32 m0,s96,0\nv_add_f32_dpp v100,v100,v100 row_half_mirror row_mask:0xf bank_mask:0xf bound_ctrl:1\ns_and_b32 s96,s31,1\ns_lshr_b32 s31,s31,1\n" \
"v_add_f32_dpp v100,v100,v100 row_mirror row_mask:0xf bank_mask:0xf bound_ctrl:1\nv_mul_f32 v104,0x3ae38e39,v100\nv_mul_f32 v102,0x3ae38e39,v100\ns_lshl_b32 s31,s31,10\n" \
"s_add_u32 s31,s31,s96\ns_lshl_b32 s31,s31,7\ns_add_u32 s31,s31,s92\nbuffer_load_dwordx4 v4,s[40:43],s31 offen lds\n" \
"v_permlane16_swap_b32 v104,v102\nv_fma_f32 v102,-v104,v104,v102\nv_max_f32 v102,0,v102\nv_add_f32 v102,0x3727c5ac,v102\n" \
"v_rsq_f32 v100,v102\ns_lshl_b32 s95,s97,12\nbuffer_load_dwordx4 v5,s[40:43],s31 offen offset:1024 lds\nv_mul_f32 v101,-v104,v100\n" \
"v_pk_fma_f32 v[80:81],v[62:63],v[100:101],v[100:101] op_sel:[0,0,1] op_sel_hi:[1,0,1]\nv_pk_fma_f32 v[82:83],v[64:65],v[100:101],v[100:101] op_sel:[0,0,1] op_sel_hi:[1,0,1]\nv_pk_fma_f32 v[84:85],v[66:67],v[100:101],v[100:101] op_sel:[0,0,1] op_sel_hi:[1,0,1]\nv_pk_fma_f32 v[86:87],v[68:69],v[100:101],v[100:101] op_sel:[0,0,1] op_sel_hi:[1,0,1]\n" \
"v_pk_fma_f32 v[88:89],v[70:71],v[100:101],v[100:101] op_sel:[0,0,1] op_sel_hi:[1,0,1]\nv_pk_fma_f32 v[90:91],v[72:73],v[100:101],v[100:101] op_sel:[0,0,1] op_sel_hi:[1,0,1]\nv_pk_fma_f32 v[92:93],v[74:75],v[100:101],v[100:101] op_sel:[0,0,1] op_sel_hi:[1,0,1]\nbuffer_load_dwordx4 v6,s[40:43],s31 offen offset:2048 lds\n" \
"v_pk_fma_f32 v[94:95],v[76:77],v[100:101],v[100:101] op_sel:[0,0,1] op_sel_hi:[1,0,1]\nv_pk_fma_f32 v[96:97],v[78:79],v[100:101],v[100:101] op_sel:[0,0,1] op_sel_hi:[1,0,1]\ns_add_u32 s95,s95,s57\ns_waitcnt lgkmcnt(9)\n" \
"v_readlane_b32 s24,v112,0\nv_mov_b32 v60,v61\ns_mov_b32 s25,s24\nv_pk_add_f32 v[112:113],v[112:113],s[24:25] neg_lo:[0,1] neg_hi:[0,1]\n" \
"s_waitcnt lgkmcnt(4)\nv_pk_fma_f32 v[80:81],v[80:81],v[16:17],v[112:113]\nbuffer_load_dwordx4 v7,s[40:43],s31 offen offset:3072 lds\nv_pk_mul_f32 v[102:103],v[80:81],v[80:81]\n" \
"s_add_u32 m0,s95,0\ns_and_b32 s95,s97,1\ns_lshr_b32 s97,s97,1\ns_lshl_b32 s97,s97,10\n" \
"s_add_u32 s97,s97,s95\ns_lshl_b32 s97,s97,7\ns_add_u32 s97,s97,s92\nbuffer_load_dwordx4 v4,s[40:43],s97 offen lds\n" \
"v_pk_add_f32 v[114:115],v[114:115],s[24:25] neg_lo:[0,1] neg_hi:[0,1]\nv_pk_fma_f32 v[82:83],v[82:83],v[18:19],v[114:115]\nv_pk_add_f32 v[100:101],v[80:81],v[82:83]\nv_pk_fma_f32 v[102:103],v[82:83],v[82:83],v[102:103]\n" \
"v_pk_add_f32 v[116:117],v[116:117],s[24:25] neg_lo:[0,1] neg_hi:[0,1]\ns_waitcnt lgkmcnt(3)\nv_pk_fma_f32 v[84:85],v[84:85],v[20:21],v[116:117]\nv_pk_add_f32 v[100:101],v[100:101],v[84:85]\n" \
"buffer_load_dwordx4 v5,s[40:43],s97 offen offset:1024 lds\nv_pk_fma_f32 v[102:103],v[84:85],v[84:85],v[102:103]\nv_pk_add_f32 v[118:119],v[118:119],s[24:25] neg_lo:[0,1] neg_hi:[0,1]\nv_pk_fma_f32 v[86:87],v[86:87],v[22:23],v[118:119]\n" \
"v_pk_add_f32 v[100:101],v[100:101],v[86:87]\nv_pk_fma_f32 v[102:103],v[86:87],v[86:87],v[102:103]\nv_pk_add_f32 v[120:121],v[120:121],s[24:25] neg_lo:[0,1] neg_hi:[0,1]\ns_waitcnt lgkmcnt(2)\n" \
"v_pk_fma_f32 v[88:89],v[88:89],v[24:25],v[120:121]\nbuffer_load_dwordx4 v6,s[40:43],s97 offen offset:2048 lds\nv_pk_add_f32 v[100:101],v[100:101],v[88:89]\nv_pk_fma_f32 v[102:103],v[88:89],v[88:89],v[102:103]\n" \
"v_pk_add_f32 v[122:123],v[122:123],s[24:25] neg_lo:[0,1] neg_hi:[0,1]\nv_pk_fma_f32 v[90:91],v[90:91],v[26:27],v[122:123]\nv_pk_add_f32 v[100:101],v[100:101],v[90:91]\nv_pk_fma_f32 v[102:103],v[90:91],v[90:91],v[102:103]\n" \
"v_pk_add_f32 v[124:125],v[124:125],s[24:25] neg_lo:[0,1] neg_hi:[0,1]\nbuffer_load_dwordx4 v7,s[40:43],s97 offen offset:3072 lds\ns_waitcnt lgkmcnt(1)\nv_pk_fma_f32 v[92:93],v[92:93],v[28:29],v[124:125]\n" \
"v_pk_add_f32 v[100:101],v[100:101],v[92:93]\nv_pk_fma_f32 v[102:103],v[92:93],v[92:93],v[102:103]\nv_pk_add_f32 v[126:127],v[126:127],s[24:25] neg_lo:[0,1] neg_hi:[0,1]\nv_pk_fma_f32 v[94:95],v[94:95],v[30:31],v[126:127]\n" \
"v_pk_add_f32 v[100:101],v[100:101],v[94:95]\nv_pk_fma_f32 v[102:103],v[94:95],v[94:95],v[102:103]\nv_pk_add_f32 v[98:99],v[98:99],s[24:25] neg_lo:[0,1] neg_hi:[0,1]\ns_waitcnt lgkmcnt(0)\n" \
"v_pk_fma_f32 v[96:97],v[96:97],v[58:59],v[98:99]\nv_pk_add_f32 v[100:101],v[100:101],v[96:97]\nv_pk_fma_f32 v[102:103],v[96:97],v[96:97],v[102:103]\nv_add_f32 v100,v100,v101\n" \
"v_add_f32 v102,v102,v103\nv_pk_add_f32 v[62:63],v[62:63],v[60:61] op_sel_hi:[1,0]\nv_cvt_pk_bf16_f32 v32,v62,v63\nv_permlane16_swap_b32 v100,v102\n" \
"v_add_f32 v100,v100,v102\nv_pk_add_f32 v[64:65],v[64:65],v[60:61] op_sel_hi:[1,0]\nv_cvt_pk_bf16_f32 v33,v64,v65\nv_add_f32_dpp v100,v100,v100 quad_perm:[1,0,3,2] row_mask:0xf bank_mask:0xf bound_ctrl:1\n" \
"v_pk_add_f32 v[66:67],v[66:67],v[60:61] op_sel_hi:[1,0]\nv_cvt_pk_bf16_f32 v34,v66,v67\nv_add_f32_dpp v100,v100,v100 quad_perm:[2,3,0,1] row_mask:0xf bank_mask:0xf bound_ctrl:1\nv_pk_add_f32 v[68:69],v[68:69],v[60:61] op_sel_hi:[1,0]\n" \
"v_cvt_pk_bf16_f32 v35,v68,v69\nv_add_f32_dpp v100,v100,v100 row_half_mirror row_mask:0xf bank_mask:0xf bound_ctrl:1\nv_pk_add_f32 v[70:71],v[70:71],v[60:61] op_sel_hi:[1,0]\nv_cvt_pk_bf16_f32 v36,v70,v71\n" \
"v_add_f32_dpp v100,v100,v100 row_mirror row_mask:0xf bank_mask:0xf bound_ctrl:1\nv_mul_f32 v104,0x3ae38e39,v100\nv_mul_f32 v102,0x3ae38e39,v100\nv_pk_add_f32 v[72:73],v[72:73],v[60:61] op_sel_hi:[1,0]\n" \
"v_cvt_pk_bf16_f32 v37,v72,v73\nv_permlane16_swap_b32 v104,v102\nv_fma_f32 v102,-v104,v104,v102\nv_max_f32 v102,0,v102\n" \
"v_add_f32 v102,0x3727c5ac,v102\nv_rsq_f32_e64 v100,v102\nv_pk_add_f32 v[74:75],v[74:75],v[60:61] op_sel_hi:[1,0]\nv_mul_f32 v101,-v104,v100\n" \
"v_pk_fma_f32 v[80:81],v[80:81],v[100:101],v[100:101] op_sel:[0,0,1] op_sel_hi:[1,0,1]\nv_pk_fma_f32 v[82:83],v[82:83],v[100:101],v[100:101] op_sel:[0,0,1] op_sel_hi:[1,0,1]\nv_pk_fma_f32 v[84:85],v[84:85],v[100:101],v[100:101] op_sel:[0,0,1] op_sel_hi:[1,0,1]\nv_pk_fma_f32 v[86:87],v[86:87],v[100:101],v[100:101] op_sel:[0,0,1] op_sel_hi:[1,0,1]\n" \
"v_cvt_pk_bf16_f32 v16,v80,v81\nv_cvt_pk_bf16_f32 v17,v82,v83\nv_cvt_pk_bf16_f32 v18,v84,v85\nv_cvt_pk_bf16_f32 v19,v86,v87\n" \
"ds_write_b128 v48,v[16:19]\nv_pk_fma_f32 v[88:89],v[88:89],v[100:101],v[100:101] op_sel:[0,0,1] op_sel_hi:[1,0,1]\nv_pk_fma_f32 v[90:91],v[90:91],v[100:101],v[100:101] op_sel:[0,0,1] op_sel_hi:[1,0,1]\nv_pk_fma_f32 v[92:93],v[92:93],v[100:101],v[100:101] op_sel:[0,0,1] op_sel_hi:[1,0,1]\n" \
"v_pk_fma_f32 v[94:95],v[94:95],v[100:101],v[100:101] op_sel:[0,0,1] op_sel_hi:[1,0,1]\nv_cvt_pk_bf16_f32 v20,v88,v89\nv_cvt_pk_bf16_f32 v21,v90,v91\nv_cvt_pk_bf16_f32 v22,v92,v93\n" \
"v_cvt_pk_bf16_f32 v23,v94,v95\nds_write_b128 v48,v[20:23] offset:512\nv_pk_fma_f32 v[96:97],v[96:97],v[100:101],v[100:101] op_sel:[0,0,1] op_sel_hi:[1,0,1]\nv_cvt_pk_bf16_f32 v24,v96,v97\n" \
"ds_write_b32 v49,v24 offset:1024\nv_cvt_pk_bf16_f32 v38,v74,v75\nv_pk_add_f32 v[76:77],v[76:77],v[60:61] op_sel_hi:[1,0]\nv_cvt_pk_bf16_f32 v39,v76,v77\n" \
"v_pk_add_f32 v[78:79],v[78:79],v[60:61] op_sel_hi:[1,0]\nv_cvt_pk_bf16_f32 v40,v78,v79\nv_bfe_u32 v50,v0,2,2\nv_mul_u32_u24 v50,4896,v50\n" \
"v_bfe_u32 v51,v0,4,2\nv_lshl_add_u32 v51,v51,2,s3\nv_lshl_add_u32 v50,v51,5,v50\nv_and_b32 v52,3,v0\n" \
"v_lshl_add_u32 v50,v52,3,v50\nv_and_b32 v52,0xf0,v1\nv_lshl_add_u32 v51,v51,16,v52\nv_bfrev_b32 v52,1\n" \
"v_cmp_gt_u32 vcc,256,v1\nv_and_b32 v53,31,v0\nv_mul_u32_u24 v53,12,v53\nv_cndmask_b32 v52,v52,v51,vcc\n" \
"v_sub_u32 v53,v12,v53\ns_lshl_b32 s24,s34,8\ns_add_u32 s24,s24,9764864\ns_waitcnt lgkmcnt(0)\n" \
"s_barrier\nds_read_b64_tr_b16 v[112:113],v50 offset:9216\nds_read_b64_tr_b16 v[114:115],v50 offset:11664\nds_read_b64_tr_b16 v[116:117],v50 offset:9728\n" \
"ds_read_b64_tr_b16 v[118:119],v50 offset:12176\nds_read_b64_tr_b16 v[120:121],v50 offset:10240\nds_read_b64_tr_b16 v[122:123],v50 offset:12688\ns_add_u32 s25,s24,1048576\n" \
"s_add_u32 s26,s24,2097152\ns_add_u32 s13,s13,7405568\ns_load_dwordx2 s[4:5],s[0:1],56\ns_load_dwordx2 s[16:17],s[0:1],64\n" \
"s_waitcnt lgkmcnt(0)\nbuffer_store_dwordx4 v[112:115],v51,s[36:39],s24 offen sc1\nbuffer_store_dwordx4 v[116:119],v51,s[36:39],s25 offen sc1\nbuffer_store_dwordx4 v[120:123],v52,s[36:39],s26 offen sc1\n" \
"buffer_store_dwordx4 v[32:35],v12,s[36:39],s13 offen sc1\nbuffer_store_dwordx4 v[36:39],v12,s[36:39],s13 offen offset:512 sc1\nbuffer_store_dword v40,v53,s[36:39],s13 offen offset:1024 sc1\ns_lshr_b32 s100,s33,3\n" \
"s_lshl1_add_u32 s100,s100,0\ns_sub_u32 s101,s100,7\ns_min_u32 s100,s100,s101\ns_lshl_b32 s101,s100,12\n" \
"s_add_u32 s101,s101,s57\ns_add_u32 m0,s101,0\ns_and_b32 s101,s100,1\ns_lshr_b32 s100,s100,1\n" \
"s_lshl_b32 s100,s100,10\ns_add_u32 s100,s100,s101\ns_lshl_b32 s100,s100,7\ns_add_u32 s100,s100,s92\n" \
"s_add_u32 s100,s100,262144\ns_lshr_b32 s31,s33,3\ns_lshl1_add_u32 s31,s31,1\ns_sub_u32 s96,s31,7\n" \
"s_min_u32 s31,s31,s96\ns_lshl_b32 s96,s31,12\ns_add_u32 s96,s96,s57\ns_lshr_b32 s97,s33,3\n" \
"s_lshl1_add_u32 s97,s97,2\ns_sub_u32 s95,s97,7\ns_min_u32 s97,s97,s95\ns_lshl_b32 s95,s97,12\n" \
"s_add_u32 s95,s95,s57\nv_and_b32 v64,63,v0\ns_and_b32 s25,s33,7\nv_lshrrev_b32 v66,5,v64\n" \
"v_and_b32 v67,1,v0\ns_lshl2_add_u32 s25,s25,s3\ns_and_b32 s26,s33,6\ns_lshr_b32 s27,s3,1\n" \
"v_lshl_add_u32 v66,v66,1,v67\ns_mul_i32 s24,s32,1152\ns_lshl_b32 s25,s25,5\ns_add_u32 s26,s26,s27\n" \
"s_mov_b32 s6,9216\ns_mov_b32 s7,0x20000\nv_lshlrev_b32 v68,3,v66\ns_add_u32 s25,s25,s24\n" \
"v_bfe_u32 v72,v0,1,1\nv_lshrrev_b32 v74,2,v64\ns_lshl4_add_u32 s26,s26,s24\nv_and_b32 v73,12,v0\n" \
"v_lshrrev_b32 v71,4,v64\nv_and_b32 v69,7,v0\nbuffer_load_dwordx2 v[80:81],v68,s[4:7],s25 offen\nv_lshl_add_u32 v72,v72,2,s21\n" \
"v_and_b32 v74,12,v74\nv_add_u32 v71,v71,v73\ns_lshl_b32 s24,s3,4\nv_lshlrev_b32 v70,1,v69\n" \
"v_lshl_add_u32 v72,v72,4,v74\ns_mov_b32 s15,s19\nv_add_u32 v71,s24,v71\nbuffer_load_ushort v82,v70,s[4:7],s26 offen offset:1024\n" \
"s_mov_b32 s18,4096\ns_mov_b32 s19,0x20000\nv_lshlrev_b32 v74,1,v71\ns_lshl_b32 s24,s22,8\n" \
"buffer_load_ushort v83,v74,s[16:19],s24 offen\nv_add_u32 v72,s3,v72\nbuffer_load_ushort v84,v74,s[16:19],s24 offen offset:128\nv_lshlrev_b32_e64 v75,1,v72\n" \
"buffer_load_ushort v85,v75,s[16:19],s24 offen\nv_lshl_add_u32 v73,v67,1,v73\nv_mul_u32_u24 v91,1152,v72\nv_lshl_add_u32 v91,v73,1,v91\n" \
"v_lshlrev_b32_e64 v73,2,v73\nv_add_u32 v73,163712,v73\nds_read_b64 v[86:87],v73\ns_mul_i32 s24,s22,147456\n" \
"s_mul_i32 s25,s20,288\nv_bfe_u32 v58,v0,1,1\ns_add_u32 s24,s24,s25\nv_mul_u32_u24 v58,544,v58\n" \
"v_bfe_u32 v75,v0,2,1\ns_lshl_b32 s25,s21,6\nv_lshl_add_u32 v58,v75,8,v58\ns_add_u32 s25,s25,s24\n" \
"v_bfe_u32 v75,v0,3,2\ns_add_u32 s24,s24,256\nv_lshl_add_u32 v58,v75,6,v58\nv_add_u32 v91,s24,v91\n" \
"v_mul_u32_u24 v90,1152,v71\nv_and_b32 v74,3,v0\nv_lshl_add_u32 v58,v66,4,v58\ns_mul_i32 s24,s3,1056\n" \
"v_lshl_add_u32 v90,v74,3,v90\nv_add_u32 v58,s24,v58\nv_add_u32 v90,s25,v90\ns_and_b32 s24,s3,1\n" \
"v_mul_u32_u24 v60,288,v75\ns_lshl_b32 s24,s24,2\ns_lshr_b32 s25,s3,1\nv_lshrrev_b32 v75,5,v64\n" \
"s_add_u32 s24,s24,s25\nv_lshl_add_u32 v60,v75,6,v60\ns_lshl_b32 s24,s24,5\nv_lshl_add_u32 v60,v69,2,v60\n" \
"s_add_u32 s24,s24,4224\nv_add_u32 v60,s24,v60\ns_bfe_u32 s24,s33,0x10001\ns_add_u32 s24,s24,s15\n" \
"s_add_u32 s24,s24,17\ns_lshl2_add_u32 s24,s24,s3\ns_waitcnt lgkmcnt(0)\nv_lshrrev_b32 v86,16,v86\n" \
"s_lshl_b32 s24,s24,7\nv_mov_b32 v92,v4\nv_mov_b32 v93,v5\nv_add_u32 v74,s57,v1\n" \
"v_or_b32 v95,v86,v87\nv_lshl_add_u32 v62,v64,1,s24\nv_bfrev_b32 v76,1\nv_cmp_eq_u32 vcc,0,v64\n" \
"s_lshl2_add_u32 s24,s34,s3\ns_lshl2_add_u32 s24,s24,65536\nv_cndmask_b32 v76,v76,0,vcc\nv_mov_b32 v77,1\n" \
"s_barrier\nv_mov_b32 v57,0xc0135761\nv_add_u32 v59,0x80000,v1\nv_lshl_add_u32 v61,v64,3,s23\n" \
"v_mov_b32 v63,0\nv_bfe_u32 v100,v0,4,2\nv_bfe_u32 v101,v0,2,2\nv_lshl_add_u32 v100,v100,3,v101\n" \
"v_bfe_u32 v101,v0,3,2\nv_lshl_add_u32 v100,v100,2,v101\nv_and_b32 v101,3,v0\nv_lshl_add_u32 v100,v100,2,v101\n" \
"v_lshlrev_b32 v100,3,v100\nv_xor_b32 v8,0,v100\nv_add_u32 v8,s57,v8\nv_mov_b32 v88,v8\n" \
"v_xor_b32 v9,32,v100\nv_add_u32 v9,s57,v9\nv_xor_b32 v10,64,v100\nv_add_u32 v10,s57,v10\n" \
"v_xor_b32 v11,96,v100\nv_add_u32 v11,s57,v11\ns_waitcnt vmcnt(5)\nbuffer_store_dword v77,v76,s[36:39],s24 offen sc1\n" \
"ds_read_b64_tr_b16 v[128:129],v8\nds_read_b64_tr_b16 v[130:131],v8 offset:512\nds_read_b64_tr_b16 v[132:133],v9\nds_read_b64_tr_b16 v[134:135],v9 offset:512\n" \
"ds_read_b64_tr_b16 v[136:137],v10\nds_read_b64_tr_b16 v[138:139],v10 offset:512\nds_read_b64_tr_b16 v[140:141],v11\nds_read_b64_tr_b16 v[142:143],v11 offset:512\n" \
"ds_read_b64_tr_b16 v[144:145],v8 offset:4096\nds_read_b64_tr_b16 v[146:147],v8 offset:4608\nds_read_b64_tr_b16 v[148:149],v9 offset:4096\nds_read_b64_tr_b16 v[150:151],v9 offset:4608\n" \
"ds_read_b64_tr_b16 v[152:153],v10 offset:4096\nds_read_b64_tr_b16 v[154:155],v10 offset:4608\nds_read_b64_tr_b16 v[156:157],v11 offset:4096\nds_read_b64_tr_b16 v[158:159],v11 offset:4608\n" \
"ds_read_b64_tr_b16 v[160:161],v8 offset:8192\nds_read_b64_tr_b16 v[162:163],v8 offset:8704\ns_waitcnt vmcnt(5)\nv_and_b32 v53,0xffff0000,v80\n" \
"v_and_b32 v55,0xffff0000,v81\nv_lshlrev_b32 v52,16,v80\nv_lshlrev_b32 v54,16,v81\nds_read_b64_tr_b16 v[164:165],v9 offset:8192\n" \
"ds_read_b64_tr_b16 v[166:167],v9 offset:8704\nds_read_b64_tr_b16 v[168:169],v10 offset:8192\nds_read_b64_tr_b16 v[170:171],v10 offset:8704\nds_read_b64_tr_b16 v[172:173],v11 offset:8192\n" \
"s_waitcnt vmcnt(4)\nv_lshlrev_b32 v56,16,v82\nds_read_b64_tr_b16 v[174:175],v11 offset:8704\nds_read_b64_tr_b16 v[176:177],v8 offset:12288\n" \
"ds_read_b64_tr_b16 v[178:179],v8 offset:12800\nds_read_b64_tr_b16 v[180:181],v9 offset:12288\nds_read_b64_tr_b16 v[182:183],v9 offset:12800\ns_waitcnt vmcnt(2)\n" \
"v_lshl_or_b32 v89,v84,16,v83\nds_write_b128 v74,v[88:91] offset:28672\ns_waitcnt vmcnt(1)\nv_lshlrev_b32_e64 v94,16,v85\n" \
"ds_write_b128 v74,v[92:95] offset:29696\nds_read_b64_tr_b16 v[184:185],v10 offset:12288\nds_read_b64_tr_b16 v[186:187],v10 offset:12800\nds_read_b64_tr_b16 v[188:189],v11 offset:12288\n" \
"ds_read_b64_tr_b16 v[190:191],v11 offset:12800\ns_waitcnt lgkmcnt(0)\nbuffer_load_dwordx4 v4,s[40:43],s100 offen lds\nbuffer_load_dwordx4 v5,s[40:43],s100 offen offset:1024 lds\n" \
"buffer_load_dwordx4 v6,s[40:43],s100 offen offset:2048 lds\nbuffer_load_dwordx4 v7,s[40:43],s100 offen offset:3072 lds\ns_add_u32 m0,s96,0\ns_and_b32 s96,s31,1\n" \
"s_lshr_b32 s31,s31,1\ns_lshl_b32 s31,s31,10\ns_add_u32 s31,s31,s96\ns_lshl_b32 s31,s31,7\n" \
"s_add_u32 s31,s31,s92\ns_add_u32 s31,s31,262144\nbuffer_load_dwordx4 v4,s[40:43],s31 offen lds\nbuffer_load_dwordx4 v5,s[40:43],s31 offen offset:1024 lds\n" \
"buffer_load_dwordx4 v6,s[40:43],s31 offen offset:2048 lds\nbuffer_load_dwordx4 v7,s[40:43],s31 offen offset:3072 lds\ns_add_u32 m0,s95,0\ns_and_b32 s95,s97,1\n" \
"s_lshr_b32 s97,s97,1\ns_lshl_b32 s97,s97,10\ns_add_u32 s97,s97,s95\ns_lshl_b32 s97,s97,7\n" \
"s_add_u32 s97,s97,s92\ns_add_u32 s97,s97,262144\nbuffer_load_dwordx4 v4,s[40:43],s97 offen lds\nbuffer_load_dwordx4 v5,s[40:43],s97 offen offset:1024 lds\n" \
"buffer_load_dwordx4 v6,s[40:43],s97 offen offset:2048 lds\nbuffer_load_dwordx4 v7,s[40:43],s97 offen offset:3072 lds\ns_lshr_b32 s100,s33,3\ns_lshl1_add_u32 s100,s100,3\n" \
"s_sub_u32 s101,s100,7\ns_min_u32 s100,s100,s101\ns_lshl_b32 s101,s100,12\ns_add_u32 s101,s101,s57\n" \
"s_add_u32 m0,s101,0\ns_and_b32 s101,s100,1\ns_lshr_b32 s100,s100,1\ns_lshl_b32 s100,s100,10\n" \
"s_add_u32 s100,s100,s101\ns_lshl_b32 s100,s100,7\ns_add_u32 s100,s100,s92\ns_add_u32 s100,s100,262144\n" \
"buffer_load_dwordx4 v4,s[40:43],s100 offen lds\nbuffer_load_dwordx4 v5,s[40:43],s100 offen offset:1024 lds\nbuffer_load_dwordx4 v6,s[40:43],s100 offen offset:2048 lds\nbuffer_load_dwordx4 v7,s[40:43],s100 offen offset:3072 lds\n" \
"s_lshr_b32 s31,s33,3\ns_lshl1_add_u32 s31,s31,4\ns_sub_u32 s96,s31,7\ns_min_u32 s31,s31,s96\n" \
"s_lshl_b32 s96,s31,12\ns_add_u32 s96,s96,s57\ns_add_u32 m0,s96,0\ns_and_b32 s96,s31,1\n" \
"s_lshr_b32 s31,s31,1\ns_lshl_b32 s31,s31,10\ns_add_u32 s31,s31,s96\ns_lshl_b32 s31,s31,7\n" \
"s_add_u32 s31,s31,s92\ns_add_u32 s31,s31,262144\nbuffer_load_dwordx4 v4,s[40:43],s31 offen lds\nbuffer_load_dwordx4 v5,s[40:43],s31 offen offset:1024 lds\n" \
"buffer_load_dwordx4 v6,s[40:43],s31 offen offset:2048 lds\nbuffer_load_dwordx4 v7,s[40:43],s31 offen offset:3072 lds\ns_lshr_b32 s97,s33,3\ns_lshl1_add_u32 s97,s97,5\n" \
"s_sub_u32 s95,s97,7\ns_min_u32 s97,s97,s95\ns_lshl_b32 s95,s97,12\ns_add_u32 s95,s95,s57\n" \
"s_add_u32 m0,s95,0\ns_and_b32 s95,s97,1\ns_lshr_b32 s97,s97,1\ns_lshl_b32 s97,s97,10\n" \
"s_add_u32 s97,s97,s95\ns_lshl_b32 s97,s97,7\ns_add_u32 s97,s97,s92\ns_add_u32 s97,s97,262144\n" \
"buffer_load_dwordx4 v4,s[40:43],s97 offen lds\nbuffer_load_dwordx4 v5,s[40:43],s97 offen offset:1024 lds\nbuffer_load_dwordx4 v6,s[40:43],s97 offen offset:2048 lds\nbuffer_load_dwordx4 v7,s[40:43],s97 offen offset:3072 lds\n" \
"s_lshr_b32 s100,s33,3\ns_lshl1_add_u32 s100,s100,6\ns_sub_u32 s101,s100,7\ns_min_u32 s100,s100,s101\n" \
"s_lshl_b32 s101,s100,12\ns_add_u32 s101,s101,s57\ns_add_u32 m0,s101,0\ns_and_b32 s101,s100,1\n" \
"s_lshr_b32 s100,s100,1\ns_lshl_b32 s100,s100,10\ns_add_u32 s100,s100,s101\ns_lshl_b32 s100,s100,7\n" \
"s_add_u32 s100,s100,s92\ns_add_u32 s100,s100,262144\nbuffer_load_dwordx4 v4,s[40:43],s100 offen lds\nbuffer_load_dwordx4 v5,s[40:43],s100 offen offset:1024 lds\n" \
"buffer_load_dwordx4 v6,s[40:43],s100 offen offset:2048 lds\nbuffer_load_dwordx4 v7,s[40:43],s100 offen offset:3072 lds\ns_branch .Lgemm_%=\n.LN_%=:\n" \
"v_bfe_u32 v112,v0,3,2\nv_bfe_u32 v113,v0,5,1\nv_lshl_add_u32 v112,v112,8,v113\ns_lshl_b32 s48,s33,2\n" \
"v_lshrrev_b32 v113,3,v0\ns_add_u32 s48,s48,s3\nv_and_b32 v113,6,v113\nv_and_b32 v114,7,v0\n" \
"s_mul_i32 s48,s48,18432\ns_mul_i32 s49,s32,1152\nv_mul_u32_u24 v112,9216,v112\nv_xor_b32 v113,v113,v114\n" \
"s_add_u32 s48,s48,s49\nv_lshl_add_u32 v4,v113,4,v112\ns_mov_b32 s10,18874368\ns_mov_b32 s11,0x20000\n" \
"s_add_u32 s48,s48,1024\ns_add_u32 m0,s88,20224\ns_add_u32 s50,s48,9437184\nbuffer_load_dwordx4 v4,s[8:11],s48 offen lds\n" \
"s_add_u32 m0,s88,24320\ns_mov_b32 s51,4096\nbuffer_load_dwordx4 v4,s[8:11],s50 offen lds\ns_add_u32 m0,s88,28416\n" \
"s_mov_b32 s52,8192\nbuffer_load_dwordx4 v2,s[4:7],0 offen lds\ns_add_u32 m0,s88,32512\ns_mov_b32 s53,14336\n" \
"buffer_load_dwordx4 v2,s[4:7],s51 offen lds\nbuffer_load_dwordx4 v[234:237],v1,s[4:7],s52 offen\nbuffer_load_dwordx4 v[238:241],v1,s[4:7],s52 offen offset:1024\nbuffer_load_dwordx4 v[242:245],v2,s[4:7],s53 offen\n" \
"s_mov_b32 s52,18432\nbuffer_load_dwordx4 v[252:255],v2,s[4:7],s52 offen\ns_and_b32 s60,s33,7\ns_mul_i32 s92,s3,589824\n" \
"s_mul_i32 s57,s3,30720\ns_add_u32 s92,s92,s49\ns_lshl_b32 s62,s60,7\nv_add_u32 v5,17408,v4\n" \
"v_add_u32 v6,34816,v4\nv_add_u32 v7,52224,v4\ns_add_u32 s57,s57,38272\ns_add_u32 s92,s92,s62\n" \
"s_load_dwordx4 s[44:47],s[0:1],72\nv_and_b32 v120,0x3c0,v1\ns_lshr_b32 s59,s33,3\nv_bfe_u32 v121,v0,3,2\n" \
"v_lshlrev_b32 v120,1,v120\ns_lshl_b32 s56,s34,8\ns_lshl_b32 s59,s59,3\nv_bfe_u32 v113,v0,4,2\n" \
"v_lshl_add_u32 v120,v121,5,v120\nv_and_b32 v122,3,v0\ns_mul_i32 s54,s34,9344\ns_add_u32 s56,s56,10289152\n" \
"s_add_u32 s59,s59,s32\nv_mul_u32_u24 v113,589824,v113\nv_and_b32 v112,15,v0\nv_lshl_add_u32 v120,v122,3,v120\n" \
"s_add_u32 s35,s54,524288\ns_cmp_eq_u32 s3,2\ns_cselect_b32 s56,s56,2147483648\ns_lshl_b32 s59,s59,3\n" \
"v_lshl_add_u32 v113,v112,4,v113\ns_add_u32 s58,s35,8192\nv_bfe_u32 v119,v0,4,2\nv_bfe_u32 v233,v0,3,3\n" \
"v_mul_u32_u24 v233,8192,v233\nv_lshl_add_u32 v233,v1,3,v233\ns_lshl2_add_u32 s100,s33,s3\ns_add_u32 s100,s100,4294967260\n" \
"s_and_b32 s101,s100,31\ns_lshl_b32 s101,s101,3\ns_cmp_lt_u32 s100,256\ns_cselect_b32 s100,s100,0x40000\n" \
"s_lshr_b32 s100,s100,5\ns_add_u32 s100,s100,s101\ns_lshl_b32 s100,s100,6\ns_add_u32 s100,s100,s32\n" \
"s_mul_i32 s100,s100,1152\ns_mov_b32 m0,162176\nv_xor_b32 v8,0,v120\nv_xor_b32 v9,32,v120\n" \
"v_xor_b32 v10,64,v120\nv_xor_b32 v11,96,v120\ns_add_u32 s59,s59,s60\nv_and_b32 v118,15,v0\n" \
"v_and_b32 v116,0x70,v2\nv_add_u32 v113,s56,v113\nv_add_u32 v114,s58,v2\nv_cmp_gt_u32 vcc,68,v0\n" \
"v_lshrrev_b32 v115,3,v0\nv_cvt_f32_u32 v120,s34\ns_lshl_b32 s59,s59,7\ns_lshl_b32 s61,s34,4\n" \
"v_lshrrev_b32 v48,1,v118\nv_and_b32 v49,1,v119\nv_cndmask_b32_e64 v113,v113,v114,vcc\nv_lshl_add_u32 v115,v115,16,v116\n" \
"v_mul_f32 v50,0x3b800000,v120\ns_add_u32 s87,s59,262144\ns_add_u32 s61,s61,65536\nv_cvt_f32_u32 v48,v48\n" \
"v_cvt_f32_u32 v49,v49\nv_bfe_u32 v100,v0,1,2\nv_and_b32 v101,1,v0\nv_add_u32 v115,s87,v115\n" \
"v_mov_b32 v116,s61\nv_cmp_gt_u32 vcc,24,v0\nv_mul_f32 v48,0xbe000000,v48\nv_fma_f32 v49,v49,2.0,v50\n" \
"v_lshl_add_u32 v100,v101,2,v100\nv_cndmask_b32 v115,v116,v115,vcc\nv_mul_f32 v49,v48,v49\nv_mul_f32 v101,0x3a000000,v120\n" \
"v_cvt_f32_u32 v100,v100\nv_bfrev_b32 v116,1\nv_cmp_gt_u32 vcc,25,v0\nv_fmamk_f32 v63,v48,0x40800000,v49\n" \
"v_fmamk_f32 v100,v100,0x3e000000,v101\nv_cndmask_b32 v115,v116,v115,vcc\nv_mov_b32 v16,-1\nv_mov_b32 v17,-1\n" \
"v_mov_b32 v18,-1\nv_mov_b32 v19,-1\nv_lshrrev_b32 v50,1,v119\nv_and_b32 v51,1,v0\n" \
"v_cos_f32 v62,v63\nv_cvt_f32_u32 v101,v119\nbuffer_store_dwordx4 v[16:19],v2,s[36:39],s35 offen sc1\nv_cmp_eq_u32 vcc,v50,v51\n" \
"v_add_f32 v101,v101,v101\nv_mov_b32 v50,0x3e800000\nv_mul_f32 v101,v101,v100\nv_cndmask_b32 v52,0,v50,vcc\n" \
"v_cos_f32 v56,v49\nv_sin_f32 v57,v49\nv_sin_f32 v63,v63\nv_cos_f32 v106,v101\n" \
"v_sin_f32 v107,v101\nv_cmp_gt_u32 vcc,8,v118\ns_add_u32 s55,s35,4096\nv_lshlrev_b32 v51,31,v0\n" \
"v_cndmask_b32 v102,0,0.5,vcc\nbuffer_store_dwordx4 v[16:19],v2,s[36:39],s55 offen sc1\nv_xor_b32 v54,v51,v52\nv_sub_f32_e64 v104,0.5,v102\n" \
"v_pk_mul_f32 v[58:59],v[56:57],v[52:53] op_sel_hi:[1,0]\nv_pk_mul_f32 v[60:61],v[56:57],v[54:55] op_sel_hi:[1,0]\nv_pk_mul_f32 v[64:65],v[62:63],v[52:53] op_sel_hi:[1,0]\nv_pk_mul_f32 v[66:67],v[62:63],v[54:55] op_sel_hi:[1,0]\n" \
"v_pk_mul_f32 v[108:109],v[106:107],v[102:103] op_sel_hi:[1,0]\nv_pk_mul_f32 v[110:111],v[106:107],v[104:105] op_sel_hi:[1,0]\nv_fmamk_f32 v57,v48,0x3f800000,v49\nv_fmamk_f32 v63,v48,0x40a00000,v49\n" \
"v_fmamk_f32 v107,v100,0x3f800000,v101\nv_cos_f32 v56,v57\nv_cos_f32 v62,v63\nv_cos_f32 v106,v107\n" \
"buffer_store_dwordx4 v[16:19],v113,s[36:39],0 offen sc1\nv_sin_f32 v57,v57\nv_sin_f32 v63,v63\nv_sin_f32 v107,v107\n" \
"v_mov_b32 v20,0\nv_mov_b32 v21,0\nv_mov_b32 v22,0\nv_mov_b32 v23,0\n" \
"buffer_store_dwordx4 v[20:23],v115,s[36:39],0 offen sc1\nv_cvt_pk_f16_f32 v40,v58,-v61\nv_cvt_pk_f16_f32 v44,v59,v60\nv_cvt_pk_f16_f32 v41,v64,-v67\n" \
"v_cvt_pk_f16_f32 v45,v65,v66\nv_cvt_pk_f16_f32 v216,v108,-v109\nv_cvt_pk_f16_f32 v220,v109,v108\nv_cvt_pk_f16_f32 v217,v110,-v111\n" \
"v_cvt_pk_f16_f32 v221,-v111,-v110\nv_pk_mul_f32 v[58:59],v[56:57],v[52:53] op_sel_hi:[1,0]\nv_pk_mul_f32 v[60:61],v[56:57],v[54:55] op_sel_hi:[1,0]\nv_pk_mul_f32 v[64:65],v[62:63],v[52:53] op_sel_hi:[1,0]\n" \
"v_pk_mul_f32 v[66:67],v[62:63],v[54:55] op_sel_hi:[1,0]\nv_pk_mul_f32 v[108:109],v[106:107],v[102:103] op_sel_hi:[1,0]\nv_pk_mul_f32 v[110:111],v[106:107],v[104:105] op_sel_hi:[1,0]\nv_lshlrev_b32 v3,4,v119\n" \
"v_lshrrev_b32 v15,1,v1\nv_lshl_add_u32 v3,v118,6,v3\nbuffer_load_dword v233,s[8:11],s100 offen lds\nv_cvt_pk_f16_f32 v42,v58,-v61\n" \
"v_cvt_pk_f16_f32 v46,v59,v60\nv_cvt_pk_f16_f32 v43,v64,-v67\nv_cvt_pk_f16_f32 v47,v65,v66\nv_cvt_pk_f16_f32 v218,v108,-v109\n" \
"v_cvt_pk_f16_f32 v222,v109,v108\nv_cvt_pk_f16_f32 v219,v110,-v111\nv_cvt_pk_f16_f32 v223,-v111,-v110\nv_add_u32 v15,s57,v15\n" \
"v_add_u32 v8,s57,v8\nv_add_u32 v9,s57,v9\nv_add_u32 v10,s57,v10\nv_add_u32 v11,s57,v11\n" \
"v_lshrrev_b32 v77,5,v0\nv_bfe_u32 v113,v0,1,5\nv_and_b32 v115,1,v0\ns_lshl_b32 s63,s3,1\n" \
"v_mul_u32_u24 v77,1160,v77\nv_and_b32 v76,31,v0\nv_and_b32 v114,6,v113\nv_add_u32 v115,s63,v115\n" \
"v_xor_b32 v114,v114,v115\nv_lshlrev_b32 v113,7,v113\nv_lshl_add_u32 v113,v114,4,v113\ns_add_u32 s64,s54,2916352\n" \
"s_add_u32 s65,s64,4096\nv_bfrev_b32 v114,1\ns_lshl1_add_u32 s95,s33,s3\ns_add_u32 s95,s95,74\n" \
"s_cmp_lt_u32 s3,2\ns_cselect_b32 s95,s95,256\ns_and_b32 s100,s95,31\ns_lshl_b32 s100,s100,3\n" \
"s_cmp_lt_u32 s95,256\ns_cselect_b32 s95,s95,0x40000\ns_lshr_b32 s95,s95,5\ns_add_u32 s95,s95,s100\n" \
"s_lshl_b32 s95,s95,6\ns_add_u32 s95,s95,s32\ns_mul_i32 s95,s95,1152\ns_mov_b32 m0,162176\n" \
"s_lshl_b32 s101,s33,7\ns_sub_u32 s101,s101,1280\nv_add_u32 v79,s101,v0\nv_mul_u32_u24 v78,52429,v79\n" \
"v_lshrrev_b32 v78,18,v78\nv_lshlrev_b32 v79,7,v79\ns_mov_b32 s101,6272\nv_mad_u32_u24 v79,v78,s101,v79\n" \
"s_mov_b32 s26,3981312\ns_mov_b32 s27,0x20000\nv_mad_u32_u24 v78,v76,36,v77\nv_cmp_gt_u32 vcc,72,v0\n" \
"s_lshl_b32 s67,s32,2\ns_add_u32 s66,s64,8192\nv_cndmask_b32 v114,v114,v2,vcc\ns_add_u32 s67,s67,s3\n" \
"s_lshl_b32 s67,s67,5\ns_add_u32 s67,s67,s33\ns_lshl_b32 s67,s67,11\ns_add_u32 s67,s67,5308416\n" \
"s_lshl2_add_u32 s69,s3,161664\ns_waitcnt vmcnt(20)\nv_readfirstlane_b32 s28,v126\nv_readfirstlane_b32 s29,v127\n" \
"s_mul_i32 s72,s3,2320\nv_mov_b32 v251,s69\nv_cmp_gt_u32 vcc,32,v0\ns_add_u32 s28,s28,1\n" \
"s_addc_u32 s29,s29,0\ns_lshl_b32 s7,s32,11\nv_cndmask_b32 v74,2.0,0,vcc\ns_mov_b32 s93,128\n" \
"s_mov_b32 s73,8192\ns_mov_b32 s74,12288\ns_mov_b32 s75,17408\ns_mov_b32 s76,21504\n" \
"s_mov_b32 s77,16384\ns_mov_b32 s78,25600\ns_add_u32 s7,s7,40960\ns_mov_b32 s81,0x3d800000\n" \
"s_mov_b32 s42,18874368\ns_mov_b32 s43,0x20000\ns_waitcnt lgkmcnt(0)\ns_mov_b64 s[40:41],s[44:45]\n" \
"s_mov_b64 s[44:45],s[46:47]\ns_mov_b32 s46,2359296\ns_mov_b32 s47,0x20000\ns_waitcnt vmcnt(19)\n" \
"v_lshlrev_b32_e64 v48,16,v224\nv_and_b32 v49,0xffff0000,v224\nv_readlane_b32 s70,v48,0\nv_readlane_b32 s71,v48,32\n" \
"v_and_b32 v51,0xffff0000,v225\nv_mov_b32 v72,s70\nv_mov_b32 v73,s71\nv_lshlrev_b32 v50,16,v225\n" \
"v_and_b32 v53,0xffff0000,v226\nv_permlane32_swap_b32 v72,v73\nv_pk_add_f32 v[48:49],v[48:49],v[72:73] op_sel_hi:[1,0] neg_lo:[0,1] neg_hi:[0,1]\nv_pk_add_f32 v[50:51],v[50:51],v[72:73] op_sel_hi:[1,0] neg_lo:[0,1] neg_hi:[0,1]\n" \
"v_pk_mul_f32 v[68:69],v[48:49],v[48:49]\nv_lshlrev_b32_e64 v52,16,v226\nv_and_b32 v55,0xffff0000,v227\nv_pk_add_f32 v[52:53],v[52:53],v[72:73] op_sel_hi:[1,0] neg_lo:[0,1] neg_hi:[0,1]\n" \
"v_pk_add_f32 v[66:67],v[48:49],v[50:51]\nv_pk_fma_f32 v[68:69],v[50:51],v[50:51],v[68:69]\nv_lshlrev_b32 v54,16,v227\ns_waitcnt vmcnt(18)\n" \
"v_and_b32 v57,0xffff0000,v228\nv_pk_add_f32 v[54:55],v[54:55],v[72:73] op_sel_hi:[1,0] neg_lo:[0,1] neg_hi:[0,1]\nv_pk_add_f32 v[66:67],v[66:67],v[52:53]\nv_pk_fma_f32 v[68:69],v[52:53],v[52:53],v[68:69]\n" \
"v_lshlrev_b32_e64 v56,16,v228\nv_and_b32 v59,0xffff0000,v229\nv_pk_add_f32 v[56:57],v[56:57],v[72:73] op_sel_hi:[1,0] neg_lo:[0,1] neg_hi:[0,1]\nv_pk_add_f32 v[66:67],v[66:67],v[54:55]\n" \
"v_pk_fma_f32 v[68:69],v[54:55],v[54:55],v[68:69]\nv_lshlrev_b32_e64 v58,16,v229\nv_and_b32 v61,0xffff0000,v230\nv_pk_add_f32 v[58:59],v[58:59],v[72:73] op_sel_hi:[1,0] neg_lo:[0,1] neg_hi:[0,1]\n" \
"v_pk_add_f32 v[66:67],v[66:67],v[56:57]\nv_pk_fma_f32 v[68:69],v[56:57],v[56:57],v[68:69]\nv_lshlrev_b32_e64 v60,16,v230\nv_and_b32 v63,0xffff0000,v231\n" \
"v_pk_add_f32 v[60:61],v[60:61],v[72:73] op_sel_hi:[1,0] neg_lo:[0,1] neg_hi:[0,1]\nv_pk_add_f32 v[66:67],v[66:67],v[58:59]\nv_pk_fma_f32 v[68:69],v[58:59],v[58:59],v[68:69]\nv_lshlrev_b32 v62,16,v231\n" \
"s_waitcnt vmcnt(17)\nv_and_b32 v65,0xffff0000,v232\nv_pk_add_f32 v[62:63],v[62:63],v[72:73] op_sel_hi:[1,0] neg_lo:[0,1] neg_hi:[0,1]\nv_pk_add_f32 v[66:67],v[66:67],v[60:61]\n" \
"v_pk_fma_f32 v[68:69],v[60:61],v[60:61],v[68:69]\nv_lshlrev_b32_e64 v64,16,v232\nv_pk_add_f32 v[64:65],v[64:65],v[72:73] op_sel_hi:[1,0] neg_lo:[0,1] neg_hi:[0,1]\nv_pk_add_f32 v[66:67],v[66:67],v[62:63]\n" \
"v_pk_fma_f32 v[68:69],v[62:63],v[62:63],v[68:69]\nv_pk_add_f32 v[66:67],v[66:67],v[64:65]\nv_pk_fma_f32 v[68:69],v[64:65],v[64:65],v[68:69]\nv_add_f32 v66,v66,v67\n" \
"v_add_f32 v68,v68,v69\ns_nop 1\nv_permlane16_swap_b32 v66,v68\nv_add_f32 v66,v66,v68\n" \
"s_nop 1\nv_add_f32_dpp v66,v66,v66 quad_perm:[1,0,3,2] row_mask:0xf bank_mask:0xf bound_ctrl:1\ns_nop 1\nv_add_f32_dpp v66,v66,v66 quad_perm:[2,3,0,1] row_mask:0xf bank_mask:0xf bound_ctrl:1\n" \
"s_nop 1\nv_add_f32_dpp v66,v66,v66 row_half_mirror row_mask:0xf bank_mask:0xf bound_ctrl:1\ns_nop 1\nv_add_f32_dpp v66,v66,v66 row_mirror row_mask:0xf bank_mask:0xf bound_ctrl:1\n" \
"v_mul_f32 v70,0x3ae38e39,v66\nv_mul_f32 v68,0x3ae38e39,v66\ns_nop 1\nv_permlane16_swap_b32 v70,v68\n" \
"v_fma_f32 v68,-v70,v70,v68\nv_max_f32 v68,0,v68\nv_add_f32 v68,0x3727c5ac,v68\nv_rsq_f32 v66,v68\n" \
"v_add_f32 v250,v70,v72\nv_mul_f32 v67,-v70,v66\nv_pk_fma_f32 v[48:49],v[48:49],v[66:67],v[66:67] op_sel:[0,0,1] op_sel_hi:[1,0,1]\nv_pk_fma_f32 v[50:51],v[50:51],v[66:67],v[66:67] op_sel:[0,0,1] op_sel_hi:[1,0,1]\n" \
"v_cvt_pk_f16_f32 v80,v48,v49\nv_cvt_pk_f16_f32 v81,v50,v51\nv_pk_fma_f32 v[52:53],v[52:53],v[66:67],v[66:67] op_sel:[0,0,1] op_sel_hi:[1,0,1]\nv_pk_fma_f32 v[54:55],v[54:55],v[66:67],v[66:67] op_sel:[0,0,1] op_sel_hi:[1,0,1]\n" \
"ds_write2_b32 v78,v80,v81 offset0:0 offset1:1\nv_cvt_pk_f16_f32 v82,v52,v53\nv_cvt_pk_f16_f32 v83,v54,v55\nv_pk_fma_f32 v[56:57],v[56:57],v[66:67],v[66:67] op_sel:[0,0,1] op_sel_hi:[1,0,1]\n" \
"v_pk_fma_f32 v[58:59],v[58:59],v[66:67],v[66:67] op_sel:[0,0,1] op_sel_hi:[1,0,1]\nds_write2_b32 v78,v82,v83 offset0:2 offset1:3\nv_cvt_pk_f16_f32 v84,v56,v57\nv_cvt_pk_f16_f32 v85,v58,v59\n" \
"v_pk_fma_f32 v[60:61],v[60:61],v[66:67],v[66:67] op_sel:[0,0,1] op_sel_hi:[1,0,1]\nv_pk_fma_f32 v[62:63],v[62:63],v[66:67],v[66:67] op_sel:[0,0,1] op_sel_hi:[1,0,1]\nds_write2_b32 v78,v84,v85 offset0:4 offset1:5\nv_cvt_pk_f16_f32 v86,v60,v61\n" \
"v_cvt_pk_f16_f32 v87,v62,v63\nv_pk_fma_f32 v[64:65],v[64:65],v[66:67],v[66:67] op_sel:[0,0,1] op_sel_hi:[1,0,1]\nds_write2_b32 v78,v86,v87 offset0:6 offset1:7\nv_cvt_pk_f16_f32 v88,v64,v65\n" \
"ds_write_b32 v78,v88 offset:32\ns_waitcnt vmcnt(11)\ns_barrier\nds_read_u16 v16,v246\n" \
"ds_read_u16 v17,v246 offset:5216\nds_read_u16 v18,v246 offset:578\nds_read_u16 v19,v246 offset:5794\nds_read_u16 v20,v246 offset:1156\n" \
"ds_read_u16 v21,v246 offset:6372\nds_read_u16 v22,v246 offset:1734\nds_read_u16 v23,v246 offset:6950\ns_waitcnt vmcnt(8)\n" \
"v_lshrrev_b32 v96,1,v234\nds_read_u16 v24,v247\nv_lshrrev_b32 v97,1,v235\nv_lshrrev_b32 v98,1,v236\n" \
"s_waitcnt lgkmcnt(7)\nv_lshl_or_b32 v16,v17,16,v16\nv_lshrrev_b32 v99,1,v237\nds_read_u16 v25,v247 offset:5216\n" \
"s_waitcnt vmcnt(7)\nv_lshrrev_b32 v100,1,v238\nv_lshrrev_b32 v101,1,v239\nv_lshrrev_b32 v102,1,v240\n" \
"v_lshrrev_b32 v103,1,v241\nds_read_u16 v26,v247 offset:578\nv_add_u32 v96,s72,v96\nv_add_u32 v97,s72,v97\n" \
"s_waitcnt lgkmcnt(7)\nv_lshl_or_b32 v17,v19,16,v18\nv_add_u32 v98,s72,v98\nds_read_u16 v27,v247 offset:5794\n" \
"v_add_u32 v99,s72,v99\nv_add_u32 v100,s72,v100\nv_add_u32 v101,s72,v101\nv_add_u32 v102,s72,v102\n" \
"ds_read_u16 v28,v247 offset:1156\nv_add_u32 v103,s72,v103\nv_pk_maximum3_f16 v124,v16,v17,v16\ns_waitcnt lgkmcnt(7)\n" \
"v_lshl_or_b32 v18,v21,16,v20\nv_pk_minimum3_f16 v125,v16,v17,v16\nds_read_u16 v29,v247 offset:6372\nds_read_u16 v30,v247 offset:1734\n" \
"s_waitcnt lgkmcnt(7)\nv_lshl_or_b32 v19,v23,16,v22\nbuffer_store_dwordx4 v[16:19],v2,s[36:39],s64 offen sc1\nds_read_u16 v31,v247 offset:6950\n" \
"ds_read_u16 v104,v248\nv_pk_maximum3_f16 v124,v18,v19,v124\nv_pk_minimum3_f16 v125,v18,v19,v125\ns_waitcnt lgkmcnt(7)\n" \
"v_lshl_or_b32 v24,v25,16,v24\nds_read_u16 v105,v248 offset:5216\nds_read_u16 v106,v248 offset:578\ns_waitcnt lgkmcnt(7)\n" \
"v_lshl_or_b32 v25,v27,16,v26\nv_pk_maximum3_f16 v124,v24,v25,v124\nds_read_u16 v107,v248 offset:5794\nv_pk_minimum3_f16 v125,v24,v25,v125\n" \
"ds_read_u16 v108,v248 offset:1156\ns_waitcnt lgkmcnt(7)\nv_lshl_or_b32 v26,v29,16,v28\nds_read_u16 v109,v248 offset:6372\n" \
"ds_read_u16 v110,v248 offset:1734\ns_waitcnt lgkmcnt(7)\nv_lshl_or_b32 v27,v31,16,v30\nbuffer_store_dwordx4 v[24:27],v2,s[36:39],s65 offen sc1\n" \
"ds_read_u16 v111,v248 offset:6950\nds_read_b128 v[116:119],v113 offset:20224\nv_pk_maximum3_f16 v124,v26,v27,v124\nv_pk_minimum3_f16 v125,v26,v27,v125\n" \
"ds_read_b128 v[120:123],v113 offset:24320\ns_waitcnt lgkmcnt(8)\nv_lshl_or_b32 v104,v105,16,v104\nds_read_b32 v16,v96\n" \
"ds_read_b32 v24,v96 offset:1160\nds_read_b32 v17,v97\nds_read_b32 v25,v97 offset:1160\ns_waitcnt lgkmcnt(10)\n" \
"v_lshl_or_b32 v105,v107,16,v106\nds_read_b32 v18,v98\nv_pk_maximum3_f16 v124,v104,v105,v124\nds_read_b32 v26,v98 offset:1160\n" \
"v_pk_minimum3_f16 v125,v104,v105,v125\nds_read_b32 v19,v99\nds_read_b32 v27,v99 offset:1160\nds_read_b32 v20,v100\n" \
"s_waitcnt lgkmcnt(13)\nv_lshl_or_b32 v106,v109,16,v108\nds_read_b32 v28,v100 offset:1160\nds_read_b32 v21,v101\n" \
"ds_read_b32 v29,v101 offset:1160\ns_waitcnt lgkmcnt(14)\nv_lshl_or_b32 v107,v111,16,v110\nbuffer_store_dwordx4 v[104:107],v114,s[36:39],s66 offen sc1\n" \
"s_waitcnt lgkmcnt(13)\nbuffer_store_dwordx4 v[116:119],v1,s[36:39],s67 offen sc1\ns_waitcnt lgkmcnt(12)\nbuffer_store_dwordx4 v[120:123],v1,s[36:39],s67 offen offset:1024 sc1\n" \
"ds_read_b32 v22,v102\nds_read_b32 v30,v102 offset:1160\nds_read_b32 v23,v103\nds_read_b32 v31,v103 offset:1160\n" \
"v_pk_maximum3_f16 v124,v106,v107,v124\nbuffer_load_dword v233,s[8:11],s95 offen lds\nv_and_b32 v124,0x7fff7fff,v124\nv_pk_minimum3_f16 v125,v106,v107,v125\n" \
"v_and_b32 v125,0x7fff7fff,v125\nv_pk_max_u16 v124,v124,v125\nv_lshlrev_b32 v125,16,v124\nv_max_u32 v124,v124,v125\n" \
"s_mov_b32 m0,162176\ns_nop 0\nv_max_u32_dpp v124,v124,v124 quad_perm:[1,0,3,2] row_mask:0xf bank_mask:0xf bound_ctrl:1\ns_nop 1\n" \
"v_max_u32_dpp v124,v124,v124 quad_perm:[2,3,0,1] row_mask:0xf bank_mask:0xf bound_ctrl:1\ns_nop 1\nv_max_u32_dpp v124,v124,v124 row_half_mirror row_mask:0xf bank_mask:0xf bound_ctrl:1\ns_nop 1\n" \
"v_max_u32_dpp v124,v124,v124 row_mirror row_mask:0xf bank_mask:0xf bound_ctrl:1\ns_nop 0\nv_readlane_b32 s68,v124,0\nv_readlane_b32 s31,v124,16\n" \
"v_readlane_b32 s96,v124,32\nv_readlane_b32 s97,v124,48\ns_max_u32 s31,s31,s96\ns_max_u32 s68,s68,s97\n" \
"s_max_u32 s68,s68,s31\ns_mul_i32 s31,s32,576\ns_add_u32 s31,s31,2304\ns_andn2_b32 s31,s31,127\n" \
"s_cmp_gt_u32 s3,1\ns_cselect_b32 s31,s31,2147483648\nbuffer_load_dword v79,s[24:27],s31 offen lds\nv_mov_b32 v124,s68\n" \
"ds_write_b32 v251,v124 offset:32\nv_mov_b32 v114,161664\nbuffer_load_dwordx4 v[48:51],v2,s[36:39],s73 offen sc1\nbuffer_load_dwordx4 v[60:63],v2,s[36:39],s75 offen sc1\n" \
"buffer_load_dwordx4 v[52:55],v2,s[36:39],s74 offen sc1\nbuffer_load_dwordx4 v[64:67],v2,s[36:39],s76 offen sc1\nbuffer_load_dwordx4 v[56:59],v1,s[36:39],s77 offen sc1\nbuffer_load_dwordx4 v[68:71],v1,s[36:39],s78 offen sc1\n" \
"s_mov_b32 s79,0xffffff\ns_lshl3_add_u32 s80,s2,4294615040\nv_add_u32 v112,28416,v1\ns_waitcnt vmcnt(8) lgkmcnt(0)\n" \
"s_barrier\nds_read_b128 v[116:119],v114 offset:32\nds_read_b128 v[192:195],v112\nds_read_b128 v[196:199],v112 offset:1024\n" \
"ds_read_b128 v[200:203],v112 offset:2048\nds_read_b128 v[204:207],v112 offset:3072\nds_read_b128 v[208:211],v112 offset:4096\nds_read_b128 v[212:215],v112 offset:5120\n" \
"ds_read_b128 v[32:35],v112 offset:6144\nds_read_b128 v[36:39],v112 offset:7168\ns_waitcnt lgkmcnt(8)\nv_max3_u32 v116,v116,v117,v118\n" \
"v_max_u32 v116,v116,v119\nv_and_b32 v119,0xf8,v0\nv_cmp_ne_u32 vcc,192,v119\nv_lshlrev_b32 v116,1,v116\n" \
"v_mov_b32 v117,s29\nv_lshlrev_b32 v118,11,v0\nv_bfi_b32 v117,s79,v117,v116\nv_add_u32 v118,s80,v118\n" \
"v_bfrev_b32 v119,1\nv_cndmask_b32 v118,v118,v119,vcc\nv_mov_b32 v116,s28\nbuffer_store_dwordx2 v[116:117],v118,s[36:39],0 offen sc1\n" \
"s_waitcnt vmcnt(6)\nv_cmp_ne_u64 vcc,s[28:29],v[48:49]\ns_waitcnt vmcnt(4)\nv_cmp_ne_u64 s[84:85],s[28:29],v[52:53]\n" \
"s_waitcnt vmcnt(2)\nv_cmp_ne_u64 s[82:83],s[28:29],v[56:57]\ns_or_b64 vcc,vcc,s[84:85]\nv_cmp_ne_u64 s[84:85],s[28:29],v[60:61]\n" \
"s_or_b64 vcc,vcc,s[82:83]\nv_cmp_ne_u64 s[82:83],s[28:29],v[64:65]\ns_or_b64 vcc,vcc,s[84:85]\ns_waitcnt vmcnt(1)\n" \
"v_cmp_ne_u64 s[84:85],s[28:29],v[68:69]\ns_or_b64 vcc,vcc,s[82:83]\ns_or_b64 vcc,vcc,s[84:85]\ns_cbranch_vccnz .LsmN_%=\n" \
".LbmN_%=:\nv_pk_add_f32 v[50:51],v[50:51],v[62:63]\nv_pk_add_f32 v[54:55],v[54:55],v[66:67]\nv_pk_mul_f32 v[62:63],v[50:51],2.0 op_sel_hi:[1,0]\n" \
"v_pk_add_f32 v[58:59],v[58:59],v[70:71]\nv_max3_f32 v112,|v62|,|v63|,0\nv_pk_fma_f32 v[66:67],v[54:55],2.0,v[74:75] op_sel_hi:[1,0,0]\nv_max3_f32 v112,|v66|,|v67|,v112\n" \
"v_pk_fma_f32 v[70:71],v[58:59],2.0,2.0 op_sel_hi:[1,0,0]\nv_max3_f32 v112,|v70|,|v71|,v112\nv_cmp_lt_f32 s[82:83],s81,v112\nv_cmp_ngt_f32 vcc,0x41000000,v112\n" \
"s_cmp_lg_u64 s[82:83],0\ns_cselect_b32 s82,2,0\nv_cvt_pk_f16_f32 v62,v62,v63\ns_cmp_lg_u64 vcc,0\n" \
"s_addc_u32 s82,s82,0\nds_write_b32 v252,v62 offset:62848\nv_cvt_pk_f16_f32 v66,v66,v67\nds_write_b32 v253,v66 offset:62848\n" \
"v_cvt_pk_f16_f32 v70,v70,v71\nv_mov_b32 v113,s82\nds_write_b32 v254,v70 offset:62848\nds_write_b32 v251,v113\n" \
"s_waitcnt lgkmcnt(0)\ns_barrier\nds_read_b128 v[124:127],v114\nds_read_b64 v[96:97],v234 offset:62848\n" \
"ds_read_b64 v[98:99],v235 offset:62848\nds_read_b64 v[100:101],v236 offset:62848\nds_read_b64 v[102:103],v237 offset:62848\nds_read_b64 v[104:105],v238 offset:62848\n" \
"ds_read_b64 v[106:107],v239 offset:62848\nds_read_b64 v[108:109],v240 offset:62848\nds_read_b64 v[110:111],v241 offset:62848\ns_waitcnt lgkmcnt(8)\n" \
"v_or3_b32 v124,v124,v125,v126\nv_or_b32 v124,v124,v127\nv_cmp_ne_u32 vcc,2,v124\ns_nop 0\n" \
"s_cbranch_vccnz .LsxN_%=\n.LbxN_%=:\ns_waitcnt lgkmcnt(7)\nv_pk_fma_f16 v80,v16,v96,v97\n" \
"s_waitcnt lgkmcnt(6)\nv_pk_fma_f16 v81,v17,v98,v99\ns_waitcnt lgkmcnt(5)\nv_pk_fma_f16 v82,v18,v100,v101\n" \
"s_waitcnt lgkmcnt(4)\nv_pk_fma_f16 v83,v19,v102,v103\nv_pk_fma_f16 v88,v24,v96,v97\ns_waitcnt lgkmcnt(3)\n" \
"v_pk_fma_f16 v84,v20,v104,v105\ns_waitcnt lgkmcnt(2)\nv_pk_fma_f16 v85,v21,v106,v107\nv_pk_fma_f16 v89,v25,v98,v99\n" \
"s_waitcnt lgkmcnt(1)\nv_pk_fma_f16 v86,v22,v108,v109\nv_pk_fma_f16 v90,v26,v100,v101\ns_waitcnt lgkmcnt(0)\n" \
"v_pk_fma_f16 v87,v23,v110,v111\nv_pk_fma_f16 v91,v27,v102,v103\nv_pk_fma_f16 v92,v28,v104,v105\nv_pk_fma_f16 v93,v29,v106,v107\n" \
"v_pk_fma_f16 v94,v30,v108,v109\nv_bfe_u32 v233,v0,3,3\nv_mul_u32_u24 v233,8192,v233\nv_lshl_add_u32 v233,v1,3,v233\n" \
"s_lshl2_add_u32 s96,s33,s3\ns_add_u32 s96,s96,138\ns_cmp_lt_u32 s33,13\ns_cselect_b32 s96,s96,256\n" \
"s_and_b32 s97,s96,31\ns_lshl_b32 s97,s97,3\ns_cmp_lt_u32 s96,256\ns_cselect_b32 s96,s96,0x40000\n" \
"s_lshr_b32 s96,s96,5\ns_add_u32 s96,s96,s97\ns_lshl_b32 s96,s96,6\ns_add_u32 s96,s96,s32\n" \
"s_mul_i32 s96,s96,1152\ns_mov_b32 m0,162176\nv_pk_fma_f16 v95,v31,v110,v111\nbuffer_load_dword v233,s[8:11],s96 offen lds\n" \
"v_mfma_f32_16x16x32_f16 v[48:51],v[80:83],v[192:195],0\ns_add_u32 s73,s35,s88\ns_add_u32 s74,s73,4096\nv_mfma_f32_16x16x32_f16 v[52:55],v[80:83],v[196:199],0\n" \
"v_and_b32 v112,15,v0\nv_bfrev_b32 v113,1\nv_mfma_f32_16x16x32_f16 v[56:59],v[84:87],v[32:35],0\nv_cmp_eq_u32 vcc,0,v112\n" \
"s_add_u32 s75,s73,8192\nv_mfma_f32_16x16x32_f16 v[60:63],v[84:87],v[36:39],0\nv_cndmask_b32 v113,v113,v3,vcc\nv_cmp_gt_u32 vcc,64,v0\n" \
"v_mfma_f32_16x16x32_f16 v[64:67],v[88:91],v[192:195],0\ns_cmp_lt_u32 s3,2\ns_cselect_b32 s75,s75,2147483648\nv_mfma_f32_16x16x32_f16 v[68:71],v[88:91],v[196:199],0\n" \
"v_pk_add_f32 v[56:57],v[48:49],v[56:57] neg_lo:[0,1] neg_hi:[0,1]\nv_sub_f32 v58,v50,v58\nv_mfma_f32_16x16x32_f16 v[72:75],v[92:95],v[32:35],0\nv_pk_add_f32 v[60:61],v[52:53],v[60:61] neg_lo:[0,1] neg_hi:[0,1]\n" \
"v_sub_f32_e64 v62,v54,v62\nv_mfma_f32_16x16x32_f16 v[76:79],v[92:95],v[36:39],0\nv_pk_fma_f32 v[48:49],v[48:49],2.0,v[56:57] op_sel_hi:[1,0,1] neg_lo:[0,0,1] neg_hi:[0,0,1]\nv_fma_f32 v50,v50,2.0,-v58\n" \
"v_pk_fma_f32 v[52:53],v[52:53],2.0,v[60:61] op_sel_hi:[1,0,1] neg_lo:[0,0,1] neg_hi:[0,0,1]\nv_fma_f32 v54,v54,2.0,-v62\nv_cvt_pk_f16_f32 v16,v48,v52\nv_pk_add_f32 v[72:73],v[64:65],v[72:73] neg_lo:[0,1] neg_hi:[0,1]\n" \
"v_sub_f32 v74,v66,v74\nv_pk_fma_f32 v[64:65],v[64:65],2.0,v[72:73] op_sel_hi:[1,0,1] neg_lo:[0,0,1] neg_hi:[0,0,1]\nv_pk_add_f32 v[76:77],v[68:69],v[76:77] neg_lo:[0,1] neg_hi:[0,1]\nv_sub_f32 v78,v70,v78\n" \
"v_fma_f32 v66,v66,2.0,-v74\nv_pk_fma_f32 v[68:69],v[68:69],2.0,v[76:77] op_sel_hi:[1,0,1] neg_lo:[0,0,1] neg_hi:[0,0,1]\nv_fma_f32 v70,v70,2.0,-v78\nv_cvt_pk_f16_f32 v17,v49,v53\n" \
"v_cvt_pk_f16_f32 v18,v50,v54\nv_cvt_pk_f16_f32 v24,v64,v68\nv_cvt_pk_f16_f32 v25,v65,v69\nv_cvt_pk_f16_f32 v26,v66,v70\n" \
"v_mfma_f32_16x16x32_f16 v[48:51],v[200:203],v[16:19],0\nv_cvt_pk_f16_f32 v20,v56,v60\nv_cvt_pk_f16_f32 v21,v57,v61\nv_mfma_f32_16x16x32_f16 v[52:55],v[204:207],v[16:19],0\n" \
"v_cvt_pk_f16_f32 v22,v58,v62\nv_cvt_pk_f16_f32 v28,v72,v76\nv_mfma_f32_16x16x32_f16 v[64:67],v[200:203],v[24:27],0\nv_cvt_pk_f16_f32 v29,v73,v77\n" \
"v_cvt_pk_f16_f32 v30,v74,v78\nv_mfma_f32_16x16x32_f16 v[68:71],v[204:207],v[24:27],0\nv_cndmask_b32_e64 v113,v113,v3,vcc\nv_mfma_f32_16x16x32_f16 v[56:59],v[200:203],v[20:23],0\n" \
"v_cvt_pk_f16_f32 v80,v48,v52\nv_cvt_pk_f16_f32 v82,v49,v53\nv_mfma_f32_16x16x32_f16 v[60:63],v[204:207],v[20:23],0\nv_cvt_pk_f16_f32 v84,v50,v54\n" \
"v_mfma_f32_16x16x32_f16 v[72:75],v[200:203],v[28:31],0\ns_nop 0\nv_cvt_pk_f16_f32 v81,v64,v68\nv_mfma_f32_16x16x32_f16 v[76:79],v[204:207],v[28:31],0\n" \
"ds_write_b64 v255,v[80:81]\nv_cvt_pk_f16_f32 v83,v65,v69\nds_write_b64 v255,v[82:83] offset:512\nv_cvt_pk_f16_f32 v85,v66,v70\n" \
"ds_write_b64 v255,v[84:85] offset:1024\nv_cvt_pk_f16_f32 v86,v56,v60\nv_cvt_pk_f16_f32 v88,v57,v61\nv_cvt_pk_f16_f32 v90,v58,v62\n" \
"v_cvt_pk_f16_f32 v87,v72,v76\nds_write_b64 v255,v[86:87] offset:256\nv_cvt_pk_f16_f32 v89,v73,v77\nds_write_b64 v255,v[88:89] offset:768\n" \
"v_cvt_pk_f16_f32 v91,v74,v78\nds_write_b64 v255,v[90:91] offset:1280\ns_waitcnt lgkmcnt(0)\ns_barrier\n" \
"ds_read_b128 v[16:19],v243\nds_read_b128 v[20:23],v244\nds_read_b128 v[24:27],v245\ns_waitcnt lgkmcnt(2)\n" \
"v_mfma_f32_16x16x32_f16 v[48:51],v[40:43],v[16:19],0\nv_mfma_f32_16x16x32_f16 v[52:55],v[44:47],v[16:19],0\ns_waitcnt lgkmcnt(1)\nv_mfma_f32_16x16x32_f16 v[56:59],v[40:43],v[20:23],0\n" \
"v_mfma_f32_16x16x32_f16 v[60:63],v[44:47],v[20:23],0\ns_waitcnt lgkmcnt(0)\nv_mfma_f32_16x16x32_f16 v[64:67],v[40:43],v[24:27],0\nv_mfma_f32_16x16x32_f16 v[68:71],v[44:47],v[24:27],0\n" \
"s_nop 1\nv_cvt_pk_f16_f32 v80,v48,v52\nv_and_b32 v80,-2,v80\nv_cvt_pk_f16_f32 v81,v49,v53\n" \
"v_cvt_pk_f16_f32 v82,v50,v54\nv_cvt_pk_f16_f32 v83,v51,v55\nbuffer_store_dwordx4 v[80:83],v3,s[36:39],s73 offen sc1\nv_cvt_pk_f16_f32 v84,v56,v60\n" \
"v_cvt_pk_f16_f32 v85,v57,v61\nv_cvt_pk_f16_f32 v86,v58,v62\nv_cvt_pk_f16_f32 v87,v59,v63\nv_and_b32_e64 v84,-2,v84\n" \
"buffer_store_dwordx4 v[84:87],v3,s[36:39],s74 offen sc1\nv_cvt_pk_f16_f32 v88,v64,v68\nv_cvt_pk_f16_f32 v89,v65,v69\nv_cvt_pk_f16_f32 v90,v66,v70\n" \
"v_cvt_pk_f16_f32 v91,v67,v71\nv_and_b32 v88,-2,v88\nbuffer_store_dwordx4 v[88:91],v113,s[36:39],s75 offen sc1\ns_load_dwordx4 s[20:23],s[0:1],56\n" \
"s_mov_b32 s14,s87\ns_mov_b32 s12,s88\ns_mov_b32 s17,s74\ns_cmp_lt_u32 s3,2\n" \
"s_cselect_b32 s18,s75,s73\ns_mov_b32 s16,s73\ns_mov_b32 s26,4096\ns_mov_b32 s27,0x20000\n" \
"v_and_b32 v112,3,v0\nv_and_b32 v113,12,v0\nv_lshlrev_b32 v112,2,v112\ns_mov_b32 s13,s89\n" \
"v_max_u32 v112,v112,v113\nv_mov_b32 v114,1048576\nv_cmp_gt_u32 vcc,12,v112\nv_and_b32 v63,0xffff0000,v224\n" \
"v_and_b32 v65,0xffff0000,v225\ns_lshl2_add_u32 s95,s33,s3\ns_add_u32 s95,s95,190\ns_cmp_lt_u32 s33,13\n" \
"s_cselect_b32 s95,s95,256\ns_and_b32 s100,s95,31\ns_lshl_b32 s100,s100,3\ns_cmp_lt_u32 s95,256\n" \
"s_cselect_b32 s95,s95,0x40000\ns_lshr_b32 s95,s95,5\ns_add_u32 s95,s95,s100\ns_lshl_b32 s95,s95,6\n" \
"s_add_u32 s95,s95,s32\ns_mul_i32 s95,s95,1152\ns_lshl2_add_u32 s101,s33,s3\ns_add_u32 s101,s101,206\n" \
"s_cmp_lt_u32 s33,13\ns_cselect_b32 s101,s101,256\ns_add_u32 s96,s92,0\ns_add_u32 s97,s92,9437184\n" \
"s_add_u32 s100,s92,9510912\nv_and_b32 v67,0xffff0000,v226\nv_and_b32 v69,0xffff0000,v227\nv_and_b32 v71,0xffff0000,v228\n" \
"v_and_b32 v73,0xffff0000,v229\nv_and_b32 v75,0xffff0000,v230\nv_and_b32 v77,0xffff0000,v231\nv_and_b32 v79,0xffff0000,v232\n" \
"v_lshlrev_b32 v126,3,v1\nv_lshlrev_b32 v62,16,v224\nv_lshlrev_b32 v64,16,v225\nv_lshlrev_b32 v66,16,v226\n" \
"v_lshlrev_b32 v68,16,v227\nv_lshlrev_b32 v70,16,v228\nv_lshlrev_b32 v72,16,v229\nv_lshlrev_b32 v74,16,v230\n" \
"v_lshlrev_b32 v76,16,v231\nv_lshlrev_b32 v78,16,v232\nv_min_u32 v126,0x480,v126\nv_pk_add_f32 v[62:63],v[62:63],v[250:251] op_sel_hi:[1,0] neg_lo:[0,1] neg_hi:[0,1]\n" \
"v_pk_add_f32 v[64:65],v[64:65],v[250:251] op_sel_hi:[1,0] neg_lo:[0,1] neg_hi:[0,1]\nv_pk_add_f32 v[66:67],v[66:67],v[250:251] op_sel_hi:[1,0] neg_lo:[0,1] neg_hi:[0,1]\nv_pk_add_f32 v[68:69],v[68:69],v[250:251] op_sel_hi:[1,0] neg_lo:[0,1] neg_hi:[0,1]\nv_pk_add_f32 v[70:71],v[70:71],v[250:251] op_sel_hi:[1,0] neg_lo:[0,1] neg_hi:[0,1]\n" \
"v_pk_add_f32 v[72:73],v[72:73],v[250:251] op_sel_hi:[1,0] neg_lo:[0,1] neg_hi:[0,1]\nv_pk_add_f32 v[74:75],v[74:75],v[250:251] op_sel_hi:[1,0] neg_lo:[0,1] neg_hi:[0,1]\nv_pk_add_f32 v[76:77],v[76:77],v[250:251] op_sel_hi:[1,0] neg_lo:[0,1] neg_hi:[0,1]\nv_pk_add_f32 v[78:79],v[78:79],v[250:251] op_sel_hi:[1,0] neg_lo:[0,1] neg_hi:[0,1]\n" \
"s_mul_i32 s31,s32,1152\nv_lshlrev_b32 v127,3,v1\ns_waitcnt lgkmcnt(0)\ns_mov_b64 s[24:25],s[22:23]\n" \
"s_mov_b32 s22,9216\ns_mov_b32 s23,0x20000\nv_mov_b64 v[16:17],v[192:193]\nv_mov_b64 v[18:19],v[194:195]\n" \
"v_mov_b64 v[20:21],v[196:197]\nv_mov_b64 v[22:23],v[198:199]\nv_mov_b64 v[24:25],v[208:209]\nv_mov_b64 v[26:27],v[210:211]\n" \
"v_mov_b64 v[28:29],v[212:213]\nv_mov_b64 v[30:31],v[214:215]\nv_mov_b64 v[32:33],v[216:217]\nv_mov_b64 v[34:35],v[218:219]\n" \
"v_mov_b64 v[36:37],v[220:221]\nv_mov_b64 v[38:39],v[222:223]\nv_mov_b64 v[40:41],v[200:201]\nv_mov_b64 v[42:43],v[202:203]\n" \
"v_mov_b64 v[44:45],v[204:205]\nv_mov_b64 v[46:47],v[206:207]\nv_mov_b64 v[56:57],v[242:243]\nv_mov_b64 v[58:59],v[244:245]\n" \
"v_mov_b32 v60,v249\nv_mov_b32 v61,v250\ns_mov_b32 s55,0xbdd2d3e7\ns_mul_i32 s56,s3,9568\n" \
"s_lshl_b32 s58,s3,13\ns_and_b32 s84,s33,1\ns_add_u32 s87,s12,65536\ns_mov_b32 s90,7405568\n" \
"s_sub_u32 s98,s57,s12\ns_mov_b32 s99,s12\ns_mov_b32 m0,162176\ns_nop 0\n" \
"buffer_load_dword v126,s[20:23],s31 offen lds\ns_and_b32 s22,s33,7\ns_lshr_b32 s20,s33,3\ns_lshr_b32 s21,s32,1\n" \
"s_mul_i32 s52,s20,4505600\ns_add_u32 s52,s52,12123648\ns_mul_i32 s48,s20,589824\ns_lshl4_add_u32 s48,s12,s48\n" \
"s_lshl_b32 s86,s21,11\ns_add_u32 s48,s48,9764864\ns_lshl_b32 s67,s21,7\ns_lshl_b32 s85,s21,17\n" \
"s_sub_u32 s86,s14,s86\ns_add_u32 s49,s48,4096\ns_add_u32 s50,s48,8192\ns_add_u32 s51,s48,12288\n" \
"s_add_u32 s53,s52,3276800\ns_and_b32 s31,s101,31\ns_lshl_b32 s31,s31,3\ns_cmp_lt_u32 s101,256\n" \
"s_cselect_b32 s101,s101,0x40000\ns_lshr_b32 s101,s101,5\ns_add_u32 s101,s101,s31\ns_lshl_b32 s101,s101,6\n" \
"s_add_u32 s101,s101,s32\ns_mul_i32 s101,s101,1152\ns_add_u32 s31,s92,9584640\ns_add_u32 s83,s52,2048000\n" \
"s_add_u32 s67,s67,163200\ns_add_u32 s85,s85,65536\ns_add_u32 s86,s86,0x10000\ns_mov_b32 m0,162176\n" \
"s_nop 0\nbuffer_load_dword v127,s[24:27],0 offen lds\ns_and_b32 s24,s32,1\ns_lshl3_add_u32 s22,s24,s22\n" \
"s_bfe_u32 s24,s33,0x10002\ns_lshl1_add_u32 s24,s32,s24\ns_mul_i32 s19,s24,18\ns_and_b32 s24,s33,3\n" \
"s_lshl2_add_u32 s23,s24,s19\ns_lshl_b32 s24,s3,5\ns_add_u32 s54,s14,s24\ns_lshl2_add_u32 s24,s20,s21\n" \
"s_add_u32 s23,s23,s3\ns_lshl2_add_u32 s24,s24,s3\ns_add_u32 s23,s23,1\ns_lshl_b32 s24,s24,9\n" \
"s_lshl_b32 s23,s23,9\ns_add_u32 s24,s24,262144\nv_bfe_u32 v127,v0,3,3\nv_mul_u32_u24 v127,8192,v127\n" \
"v_lshl_add_u32 v127,v1,3,v127\ns_mov_b32 m0,162176\ns_sub_u32 s59,s24,s23\nbuffer_load_dword v127,s[8:11],s95 offen lds\n" \
"s_lshr_b32 s25,s3,1\ns_mul_i32 s24,s21,1152\ns_mul_i32 s25,s25,576\ns_add_u32 s24,s24,s25\n" \
"s_lshl4_add_u32 s24,s24,s22\ns_and_b32 s25,s3,1\ns_lshl_b32 s24,s24,8\ns_lshl_b32 s65,s25,17\n" \
"s_add_u32 s65,s65,s24\ns_lshl_b32 s25,s25,20\ns_add_u32 s24,s24,s25\ns_add_u32 s60,s24,655488\n" \
"s_add_u32 s61,s24,786432\ns_add_u32 s62,s24,786560\ns_add_u32 s63,s24,917504\ns_add_u32 s64,s24,917632\n" \
"s_mov_b32 m0,162176\ns_add_u32 s95,s92,73728\nbuffer_load_dword v127,s[8:11],s101 offen lds\ns_lshl2_add_u32 s24,s21,s3\n" \
"s_mul_i32 s24,s24,9216\ns_add_u32 s24,s24,s52\ns_add_u32 s68,s24,819712\ns_lshl_b32 s24,s22,14\n" \
"s_add_u32 s24,s24,s52\ns_lshl2_add_u32 s25,s21,s3\ns_add_u32 s24,s24,967168\ns_lshl_b32 s25,s25,8\n" \
"s_add_u32 s79,s25,s24\ns_add_u32 s25,s21,4\ns_and_b32 s25,s25,3\ns_add_u32 s24,s24,s12\n" \
"s_lshl_b32 s25,s25,12\ns_add_u32 s25,s25,s24\ns_sub_u32 s74,s25,819200\ns_add_u32 m0,s57,0\n" \
"s_add_u32 s101,s92,147456\nbuffer_load_dwordx4 v4,s[8:11],s96 offen lds\ns_add_u32 s25,s21,3\ns_and_b32 s25,s25,3\n" \
"s_lshl_b32 s25,s25,12\ns_add_u32 s25,s25,s24\ns_sub_u32 s71,s25,2048000\ns_sub_u32 s76,s25,1638400\n" \
"s_add_u32 s25,s21,2\ns_and_b32 s25,s25,3\ns_lshl_b32 s25,s25,12\nbuffer_load_dwordx4 v5,s[8:11],s96 offen offset:1024 lds\n" \
"s_add_u32 s25,s25,s24\ns_sub_u32 s72,s25,1638400\ns_sub_u32 s77,s25,1638400\ns_add_u32 s25,s21,1\n" \
"s_and_b32 s25,s25,3\ns_lshl_b32 s25,s25,12\ns_add_u32 s25,s25,s24\ns_sub_u32 s73,s25,1638400\n" \
"s_sub_u32 s78,s25,1228800\nbuffer_load_dwordx4 v6,s[8:11],s96 offen offset:2048 lds\ns_lshl2_add_u32 s26,s33,s3\ns_and_b32 s24,s26,15\n" \
"s_and_b32 s25,s26,8\ns_lshl2_add_u32 s24,s24,192\ns_mul_i32 s25,s25,28\ns_add_u32 s25,s25,s24\n" \
"s_sub_u32 s25,s25,32\ns_bitcmp1_b32 s26,4\ns_cselect_b32 s24,s25,s24\nbuffer_load_dwordx4 v7,s[8:11],s96 offen offset:3072 lds\n" \
"s_and_b32 s25,s26,31\ns_lshl2_add_u32 s25,s25,448\ns_bitcmp1_b32 s26,5\ns_cselect_b32 s24,s25,s24\n" \
"s_lshr_b32 s25,s26,6\ns_lshl1_add_u32 s25,s21,s25\ns_mul_i32 s25,s25,576\ns_add_u32 s24,s24,s25\n" \
"s_and_b32 s25,s32,1\ns_add_u32 m0,s57,4096\ns_add_u32 s96,s92,221184\nbuffer_load_dwordx4 v4,s[8:11],s97 offen lds\n" \
"s_lshl1_add_u32 s24,s24,s25\ns_lshl_b32 s24,s24,11\ns_and_b32 s25,s33,3\ns_mul_i32 s27,s3,4896\n" \
"s_add_u32 s24,s24,s25\ns_add_u32 s27,s27,9216\ns_add_u32 s89,s24,1\nv_mov_b32 v113,s27\n" \
"s_lshl2_add_u32 s24,s2,s3\nbuffer_load_dwordx4 v5,s[8:11],s97 offen offset:1024 lds\nv_cndmask_b32 v112,v114,v113,vcc\ns_add_u32 s65,s65,2097152\n" \
"s_lshl_b32 s24,s24,8\nv_add_u32 v48,v234,v112\nv_add_u32 v49,v235,v112\nv_add_u32 v50,v236,v112\n" \
"v_add_u32 v51,v237,v112\nv_add_u32 v52,v238,v112\nbuffer_load_dwordx4 v6,s[8:11],s97 offen offset:2048 lds\nv_add_u32 v53,v239,v112\n" \
"v_add_u32 v54,v240,v112\nv_add_u32 v55,v241,v112\ns_add_u32 s75,s59,196608\ns_add_u32 s66,s65,128\n" \
"s_add_u32 s69,s68,3072\ns_add_u32 s70,s68,6144\ns_add_u32 s80,s79,4096\ns_add_u32 s81,s79,8192\n" \
"buffer_load_dwordx4 v7,s[8:11],s97 offen offset:3072 lds\ns_add_u32 s82,s79,12288\ns_add_u32 s88,s74,819200\ns_add_u32 s91,s24,69206016\n" \
"s_add_u32 m0,s57,8192\ns_nop 0\nbuffer_load_dwordx4 v4,s[8:11],s95 offen lds\nbuffer_load_dwordx4 v5,s[8:11],s95 offen offset:1024 lds\n" \
"buffer_load_dwordx4 v6,s[8:11],s95 offen offset:2048 lds\nbuffer_load_dwordx4 v7,s[8:11],s95 offen offset:3072 lds\ns_add_u32 m0,s57,12288\ns_nop 0\n" \
"buffer_load_dwordx4 v4,s[8:11],s100 offen lds\nbuffer_load_dwordx4 v5,s[8:11],s100 offen offset:1024 lds\nbuffer_load_dwordx4 v6,s[8:11],s100 offen offset:2048 lds\nbuffer_load_dwordx4 v7,s[8:11],s100 offen offset:3072 lds\n" \
"s_add_u32 m0,s57,16384\ns_nop 0\nbuffer_load_dwordx4 v4,s[8:11],s101 offen lds\nbuffer_load_dwordx4 v5,s[8:11],s101 offen offset:1024 lds\n" \
"buffer_load_dwordx4 v6,s[8:11],s101 offen offset:2048 lds\nbuffer_load_dwordx4 v7,s[8:11],s101 offen offset:3072 lds\ns_add_u32 m0,s57,20480\ns_nop 0\n" \
"buffer_load_dwordx4 v4,s[8:11],s31 offen lds\nbuffer_load_dwordx4 v5,s[8:11],s31 offen offset:1024 lds\nbuffer_load_dwordx4 v6,s[8:11],s31 offen offset:2048 lds\nbuffer_load_dwordx4 v7,s[8:11],s31 offen offset:3072 lds\n" \
"s_add_u32 m0,s57,24576\ns_nop 0\nbuffer_load_dwordx4 v4,s[8:11],s96 offen lds\nbuffer_load_dwordx4 v5,s[8:11],s96 offen offset:1024 lds\n" \
"buffer_load_dwordx4 v6,s[8:11],s96 offen offset:2048 lds\nbuffer_load_dwordx4 v7,s[8:11],s96 offen offset:3072 lds\nbuffer_load_dwordx4 v[104:107],v1,s[36:39],s7 offen sc1\nbuffer_load_dwordx4 v[108:111],v1,s[36:39],s7 offen offset:1024 sc1\n" \
"s_waitcnt vmcnt(1)\nv_xor_b32 v112,s29,v105\nv_xor_b32 v113,s29,v107\ns_waitcnt vmcnt(0)\n" \
"v_xor_b32 v114,s29,v109\nv_xor_b32 v115,s29,v111\nv_or3_b32 v112,v112,v113,v114\nv_xor_b32 v113,s28,v104\n" \
"v_xor_b32 v114,s28,v106\nv_or_b32 v112,v112,v115\nv_xor_b32 v115,s28,v108\nv_lshl_or_b32 v112,v112,8,v113\n" \
"v_xor_b32 v113,s28,v110\nv_or3_b32 v112,v112,v114,v115\nv_or_b32 v112,v112,v113\nv_cmp_ne_u32 vcc,0,v112\n" \
"s_nop 0\ns_cbranch_vccnz .LsrN_%=\n.LbrN_%=:\nv_min_u32 v120,2288,v2\n" \
"s_mov_b32 s5,26624\nv_max3_u32 v112,v105,v107,v109\nv_max_u32_e64 v112,v112,v111\ns_mov_b32 s6,31232\n" \
"ds_read_b64_tr_b16 a[0:1],v8\nv_max_u32_dpp v112,v112,v112 quad_perm:[1,0,3,2] row_mask:0xf bank_mask:0xf bound_ctrl:1\nds_read_b64_tr_b16 a[4:5],v9\nds_read_b64_tr_b16 a[8:9],v10\n" \
"v_max_u32_dpp v112,v112,v112 quad_perm:[2,3,0,1] row_mask:0xf bank_mask:0xf bound_ctrl:1\nds_read_b64_tr_b16 a[12:13],v11\nds_read_b64_tr_b16 a[16:17],v8 offset:2048\nv_max_u32_dpp v112,v112,v112 row_half_mirror row_mask:0xf bank_mask:0xf bound_ctrl:1\n" \
"ds_read_b64_tr_b16 a[20:21],v9 offset:2048\nds_read_b64_tr_b16 a[24:25],v10 offset:2048\nv_max_u32_dpp v112,v112,v112 row_mirror row_mask:0xf bank_mask:0xf bound_ctrl:1\nds_read_b64_tr_b16 a[28:29],v11 offset:2048\n" \
"v_readlane_b32 s94,v112,0\nv_readlane_b32 s97,v112,16\nv_readlane_b32 s95,v112,32\nv_readlane_b32 s100,v112,48\n" \
"s_max_u32 s97,s97,s95\ns_max_u32 s94,s94,s100\ns_max_u32 s94,s94,s97\ns_lshr_b32 s94,s94,24\n" \
"s_max_u32 s94,s94,64\ns_min_u32 s94,s94,250\ns_add_u32 s101,s92,9658368\ns_add_u32 m0,s57,0\n" \
"ds_read_b64_tr_b16 a[2:3],v8 offset:4096\ns_waitcnt lgkmcnt(1)\nbuffer_load_dwordx4 v4,s[8:11],s101 offen lds\nbuffer_load_dwordx4 v5,s[8:11],s101 offen offset:1024 lds\n" \
"buffer_load_dwordx4 v6,s[8:11],s101 offen offset:2048 lds\nbuffer_load_dwordx4 v7,s[8:11],s101 offen offset:3072 lds\nds_read_b64_tr_b16 a[6:7],v9 offset:4096\nds_read_b64_tr_b16 a[10:11],v10 offset:4096\n" \
"ds_read_b64_tr_b16 a[14:15],v11 offset:4096\nds_read_b64_tr_b16 a[18:19],v8 offset:6144\nds_read_b64_tr_b16 a[22:23],v9 offset:6144\nds_read_b64_tr_b16 a[26:27],v10 offset:6144\n" \
"ds_read_b64_tr_b16 a[30:31],v11 offset:6144\ns_add_u32 s31,s92,294912\ns_add_u32 m0,s57,4096\nds_read_b64_tr_b16 a[32:33],v8 offset:8192\n" \
"s_waitcnt lgkmcnt(1)\nbuffer_load_dwordx4 v4,s[8:11],s31 offen lds\nbuffer_load_dwordx4 v5,s[8:11],s31 offen offset:1024 lds\nbuffer_load_dwordx4 v6,s[8:11],s31 offen offset:2048 lds\n" \
"buffer_load_dwordx4 v7,s[8:11],s31 offen offset:3072 lds\nds_read_b64_tr_b16 a[36:37],v9 offset:8192\nds_read_b64_tr_b16 a[40:41],v10 offset:8192\nds_read_b64_tr_b16 a[44:45],v11 offset:8192\n" \
"ds_read_b64_tr_b16 a[48:49],v8 offset:10240\nds_read_b64_tr_b16 a[52:53],v9 offset:10240\nds_read_b64_tr_b16 a[56:57],v10 offset:10240\nds_read_b64_tr_b16 a[60:61],v11 offset:10240\n" \
"s_add_u32 s96,s92,9732096\ns_add_u32 m0,s57,8192\nds_read_b64_tr_b16 a[34:35],v8 offset:12288\ns_waitcnt lgkmcnt(1)\n" \
"buffer_load_dwordx4 v4,s[8:11],s96 offen lds\nbuffer_load_dwordx4 v5,s[8:11],s96 offen offset:1024 lds\nbuffer_load_dwordx4 v6,s[8:11],s96 offen offset:2048 lds\nbuffer_load_dwordx4 v7,s[8:11],s96 offen offset:3072 lds\n" \
"ds_read_b64_tr_b16 a[38:39],v9 offset:12288\nds_read_b64_tr_b16 a[42:43],v10 offset:12288\nds_read_b64_tr_b16 a[46:47],v11 offset:12288\nds_read_b64_tr_b16 a[50:51],v8 offset:14336\n" \
"ds_read_b64_tr_b16 a[54:55],v9 offset:14336\nds_read_b64_tr_b16 a[58:59],v10 offset:14336\nds_read_b64_tr_b16 a[62:63],v11 offset:14336\ns_add_u32 s97,s92,368640\n" \
"s_add_u32 m0,s57,12288\nds_read_b64_tr_b16 a[64:65],v8 offset:16384\ns_waitcnt lgkmcnt(1)\nbuffer_load_dwordx4 v4,s[8:11],s97 offen lds\n" \
"buffer_load_dwordx4 v5,s[8:11],s97 offen offset:1024 lds\nbuffer_load_dwordx4 v6,s[8:11],s97 offen offset:2048 lds\nbuffer_load_dwordx4 v7,s[8:11],s97 offen offset:3072 lds\nbuffer_load_dwordx4 v[80:83],v120,s[36:39],s5 offen sc1\n" \
"buffer_load_dwordx4 v[84:87],v120,s[36:39],s5 offen offset:2304 sc1\nbuffer_load_dwordx4 v[88:91],v120,s[36:39],s6 offen sc1\nbuffer_load_dwordx4 v[92:95],v120,s[36:39],s6 offen offset:2304 sc1\nds_read_b64_tr_b16 a[68:69],v9 offset:16384\n" \
"ds_read_b64_tr_b16 a[72:73],v10 offset:16384\nds_read_b64_tr_b16 a[76:77],v11 offset:16384\nds_read_b64_tr_b16 a[80:81],v8 offset:18432\nds_read_b64_tr_b16 a[84:85],v9 offset:18432\n" \
"ds_read_b64_tr_b16 a[88:89],v10 offset:18432\nds_read_b64_tr_b16 a[92:93],v11 offset:18432\ns_add_u32 s95,s92,9805824\ns_add_u32 m0,s57,16384\n" \
"ds_read_b64_tr_b16 a[66:67],v8 offset:20480\ns_waitcnt lgkmcnt(1)\nbuffer_load_dwordx4 v4,s[8:11],s95 offen lds\nbuffer_load_dwordx4 v5,s[8:11],s95 offen offset:1024 lds\n" \
"buffer_load_dwordx4 v6,s[8:11],s95 offen offset:2048 lds\nbuffer_load_dwordx4 v7,s[8:11],s95 offen offset:3072 lds\nds_read_b64_tr_b16 a[70:71],v9 offset:20480\nds_read_b64_tr_b16 a[74:75],v10 offset:20480\n" \
"ds_read_b64_tr_b16 a[78:79],v11 offset:20480\nds_read_b64_tr_b16 a[82:83],v8 offset:22528\nds_read_b64_tr_b16 a[86:87],v9 offset:22528\nds_read_b64_tr_b16 a[90:91],v10 offset:22528\n" \
"ds_read_b64_tr_b16 a[94:95],v11 offset:22528\ns_add_u32 s100,s92,442368\ns_add_u32 m0,s57,20480\nds_read_b64_tr_b16 a[96:97],v8 offset:24576\n" \
"s_waitcnt lgkmcnt(1)\nbuffer_load_dwordx4 v4,s[8:11],s100 offen lds\nbuffer_load_dwordx4 v5,s[8:11],s100 offen offset:1024 lds\nbuffer_load_dwordx4 v6,s[8:11],s100 offen offset:2048 lds\n" \
"buffer_load_dwordx4 v7,s[8:11],s100 offen offset:3072 lds\nds_read_b64_tr_b16 a[100:101],v9 offset:24576\nds_read_b64_tr_b16 a[104:105],v10 offset:24576\nds_read_b64_tr_b16 a[108:109],v11 offset:24576\n" \
"ds_read_b64_tr_b16 a[112:113],v8 offset:26624\nds_read_b64_tr_b16 a[116:117],v9 offset:26624\nds_read_b64_tr_b16 a[120:121],v10 offset:26624\nds_read_b64_tr_b16 a[124:125],v11 offset:26624\n" \
"s_add_u32 s101,s92,9879552\ns_add_u32 m0,s57,24576\ns_waitcnt vmcnt(24)\nds_read_b64_tr_b16 a[98:99],v8\n" \
"s_waitcnt lgkmcnt(1)\nbuffer_load_dwordx4 v4,s[8:11],s101 offen lds\nbuffer_load_dwordx4 v5,s[8:11],s101 offen offset:1024 lds\nbuffer_load_dwordx4 v6,s[8:11],s101 offen offset:2048 lds\n" \
"buffer_load_dwordx4 v7,s[8:11],s101 offen offset:3072 lds\nds_read_b64_tr_b16 a[102:103],v9\nds_read_b64_tr_b16 a[106:107],v10\nds_read_b64_tr_b16 a[110:111],v11\n" \
"ds_read_b64_tr_b16 a[114:115],v8 offset:2048\nds_read_b64_tr_b16 a[118:119],v9 offset:2048\nds_read_b64_tr_b16 a[122:123],v10 offset:2048\nds_read_b64_tr_b16 a[126:127],v11 offset:2048\n" \
"s_add_u32 s31,s92,516096\ns_add_u32 m0,s57,0\ns_waitcnt vmcnt(24)\nds_read_b64_tr_b16 a[128:129],v8 offset:4096\n" \
"s_waitcnt lgkmcnt(1)\nbuffer_load_dwordx4 v4,s[8:11],s31 offen lds\nbuffer_load_dwordx4 v5,s[8:11],s31 offen offset:1024 lds\nbuffer_load_dwordx4 v6,s[8:11],s31 offen offset:2048 lds\n" \
"buffer_load_dwordx4 v7,s[8:11],s31 offen offset:3072 lds\nds_read_b64_tr_b16 a[132:133],v9 offset:4096\nds_read_b64_tr_b16 a[136:137],v10 offset:4096\nds_read_b64_tr_b16 a[140:141],v11 offset:4096\n" \
"ds_read_b64_tr_b16 a[144:145],v8 offset:6144\nds_read_b64_tr_b16 a[148:149],v9 offset:6144\nds_read_b64_tr_b16 a[152:153],v10 offset:6144\nds_read_b64_tr_b16 a[156:157],v11 offset:6144\n" \
"s_add_u32 s96,s92,9953280\ns_add_u32 m0,s57,4096\ns_waitcnt vmcnt(24)\nds_read_b64_tr_b16 a[130:131],v8 offset:8192\n" \
"s_waitcnt lgkmcnt(1)\nbuffer_load_dwordx4 v4,s[8:11],s96 offen lds\nbuffer_load_dwordx4 v5,s[8:11],s96 offen offset:1024 lds\nbuffer_load_dwordx4 v6,s[8:11],s96 offen offset:2048 lds\n" \
"buffer_load_dwordx4 v7,s[8:11],s96 offen offset:3072 lds\nds_read_b64_tr_b16 a[134:135],v9 offset:8192\nds_read_b64_tr_b16 a[138:139],v10 offset:8192\nds_read_b64_tr_b16 a[142:143],v11 offset:8192\n" \
"ds_read_b64_tr_b16 a[146:147],v8 offset:10240\nds_read_b64_tr_b16 a[150:151],v9 offset:10240\nds_read_b64_tr_b16 a[154:155],v10 offset:10240\nds_read_b64_tr_b16 a[158:159],v11 offset:10240\n" \
"s_bfe_u32 s26,s33,0x20001\ns_lshl2_add_u32 s15,s32,s26\ns_lshl2_add_u32 s15,s15,s3\ns_lshl_b32 s15,s15,14\n" \
"s_add_u32 s15,s15,5308416\ns_add_u32 s97,s15,0\ns_add_u32 m0,s57,8192\ns_waitcnt vmcnt(24)\n" \
"ds_read_b64_tr_b16 a[160:161],v8 offset:12288\ns_waitcnt lgkmcnt(1)\nbuffer_load_dwordx4 v1,s[36:39],s97 offen sc1 lds\nbuffer_load_dwordx4 v1,s[36:39],s97 offen offset:1024 sc1 lds\n" \
"buffer_load_dwordx4 v1,s[36:39],s97 offen offset:2048 sc1 lds\nbuffer_load_dwordx4 v1,s[36:39],s97 offen offset:3072 sc1 lds\nds_read_b64_tr_b16 a[164:165],v9 offset:12288\nds_read_b64_tr_b16 a[168:169],v10 offset:12288\n" \
"ds_read_b64_tr_b16 a[172:173],v11 offset:12288\nds_read_b64_tr_b16 a[176:177],v8 offset:14336\nds_read_b64_tr_b16 a[180:181],v9 offset:14336\nds_read_b64_tr_b16 a[184:185],v10 offset:14336\n" \
"ds_read_b64_tr_b16 a[188:189],v11 offset:14336\ns_add_u32 s95,s15,4096\ns_add_u32 m0,s57,12288\ns_waitcnt vmcnt(20)\n" \
"ds_read_b64_tr_b16 a[162:163],v8 offset:16384\ns_waitcnt lgkmcnt(1)\nbuffer_load_dwordx4 v1,s[36:39],s95 offen sc1 lds\nbuffer_load_dwordx4 v1,s[36:39],s95 offen offset:1024 sc1 lds\n" \
"buffer_load_dwordx4 v1,s[36:39],s95 offen offset:2048 sc1 lds\nbuffer_load_dwordx4 v1,s[36:39],s95 offen offset:3072 sc1 lds\nds_read_b64_tr_b16 a[166:167],v9 offset:16384\nds_read_b64_tr_b16 a[170:171],v10 offset:16384\n" \
"ds_read_b64_tr_b16 a[174:175],v11 offset:16384\nds_read_b64_tr_b16 a[178:179],v8 offset:18432\nds_read_b64_tr_b16 a[182:183],v9 offset:18432\nds_read_b64_tr_b16 a[186:187],v10 offset:18432\n" \
"ds_read_b64_tr_b16 a[190:191],v11 offset:18432\ns_add_u32 s100,s15,8192\ns_add_u32 m0,s57,16384\ns_waitcnt vmcnt(20)\n" \
"ds_read_b64_tr_b16 a[192:193],v8 offset:20480\ns_waitcnt lgkmcnt(1)\nbuffer_load_dwordx4 v1,s[36:39],s100 offen sc1 lds\nbuffer_load_dwordx4 v1,s[36:39],s100 offen offset:1024 sc1 lds\n" \
"buffer_load_dwordx4 v1,s[36:39],s100 offen offset:2048 sc1 lds\nbuffer_load_dwordx4 v1,s[36:39],s100 offen offset:3072 sc1 lds\nds_read_b64_tr_b16 a[196:197],v9 offset:20480\nds_read_b64_tr_b16 a[200:201],v10 offset:20480\n" \
"ds_read_b64_tr_b16 a[204:205],v11 offset:20480\nds_read_b64_tr_b16 a[208:209],v8 offset:22528\nds_read_b64_tr_b16 a[212:213],v9 offset:22528\nds_read_b64_tr_b16 a[216:217],v10 offset:22528\n" \
"ds_read_b64_tr_b16 a[220:221],v11 offset:22528\ns_add_u32 s101,s15,12288\ns_add_u32 m0,s57,20480\ns_waitcnt lgkmcnt(0)\n" \
"buffer_load_dwordx4 v1,s[36:39],s101 offen sc1 lds\nbuffer_load_dwordx4 v1,s[36:39],s101 offen offset:1024 sc1 lds\nbuffer_load_dwordx4 v1,s[36:39],s101 offen offset:2048 sc1 lds\nbuffer_load_dwordx4 v1,s[36:39],s101 offen offset:3072 sc1 lds\n" \
"v_cmp_ne_u64 vcc,s[28:29],v[80:81]\nv_cmp_ne_u64 s[24:25],s[28:29],v[84:85]\nv_cmp_ne_u64 s[26:27],s[28:29],v[88:89]\ns_or_b64 vcc,vcc,s[24:25]\n" \
"v_cmp_ne_u64 s[24:25],s[28:29],v[92:93]\ns_or_b64 vcc,vcc,s[26:27]\ns_or_b64 vcc,vcc,s[24:25]\ns_cbranch_vccnz .LsnN_%=\n" \
".LbnN_%=:\nv_bfe_u32 v112,v0,1,5\nv_mul_u32_u24 v112,72,v112\nv_lshrrev_b32 v113,6,v0\n" \
"v_mul_u32_u24 v114,144,v0\nv_lshl_add_u32 v112,v113,5,v112\nv_subrev_u32 v114,18368,v114\nv_mov_b32 v115,1048576\n" \
"v_cmp_gt_u32 vcc,144,v0\nv_and_b32 v113,1,v0\nv_lshl_add_u32 v112,v113,4,v112\nv_cndmask_b32 v114,v115,v114,vcc\n" \
"v_and_b32 v97,0xffff0000,v82\nv_lshlrev_b32 v96,16,v82\ns_add_u32 s24,s93,s94\nv_cmp_gt_u32 vcc,128,v0\n" \
"v_add_u32 v113,8,v112\nv_mul_f32 v96,0x3ee38e39,v96\nv_mul_f32 v97,0x3ee38e39,v97\ns_sub_u32 s24,s24,253\n" \
"v_cndmask_b32 v112,v114,v112,vcc\nv_and_b32 v99,0xffff0000,v83\nv_ldexp_f32 v96,v96,s24\nv_ldexp_f32 v97,v97,s24\n" \
"v_lshlrev_b32_e64 v98,16,v83\nds_write_b64 v112,v[96:97] offset:28800\nv_add_u32 v115,72,v114\nv_mul_f32 v98,0x3ee38e39,v98\n" \
"v_mul_f32 v99,0x3ee38e39,v99\nv_cndmask_b32_e64 v113,v115,v113,vcc\nv_ldexp_f32 v98,v98,s24\nv_ldexp_f32 v99,v99,s24\n" \
"ds_write_b64 v113,v[98:99] offset:28800\nv_and_b32 v101,0xffff0000,v86\nv_lshlrev_b32 v100,16,v86\nds_write_b64 v112,v[100:101] offset:31104\n" \
"v_and_b32 v103,0xffff0000,v87\nv_lshlrev_b32 v102,16,v87\ns_lshr_b32 s25,s33,3\nds_write_b64 v113,v[102:103] offset:31104\n" \
"s_mul_i32 s25,s25,576\nv_and_b32 v105,0xffff0000,v90\nv_lshlrev_b32 v104,16,v90\ns_sub_u32 s25,163200,s25\n" \
"ds_write_b64 v112,v[104:105] offset:33408\nv_add_u32 v114,s25,v2\nv_subrev_u32 v115,163200,v114\nv_and_b32 v107,0xffff0000,v91\n" \
"v_lshlrev_b32 v106,16,v91\nv_mov_b32 v116,1048576\nv_cmp_gt_u32 vcc,576,v115\nds_write_b64 v113,v[106:107] offset:33408\n" \
"v_and_b32 v109,0xffff0000,v94\nv_cndmask_b32 v114,v116,v114,vcc\nv_and_b32 v111,0xffff0000,v95\nv_lshlrev_b32 v108,16,v94\n" \
"v_lshlrev_b32 v110,16,v95\nds_write_b128 v114,v[108:111]\nv_and_b32 v124,48,v0\nv_cmp_gt_u32 vcc,64,v0\n" \
"v_bfe_u32 v127,v0,5,1\nv_and_b32 v13,31,v0\nv_cndmask_b32_e64 v124,v124,v3,vcc\nv_mul_u32_u24 v127,2448,v127\n" \
"v_mul_u32_u24 v13,72,v13\ns_mul_i32 s27,s3,4896\nv_add3_u32 v13,v13,v127,s27\nv_mov_b32 v127,0\n" \
"s_waitcnt vmcnt(24)\nds_read_b64_tr_b16 a[194:195],v8 offset:24576\nds_read_b64_tr_b16 a[198:199],v9 offset:24576\nds_read_b64_tr_b16 a[202:203],v10 offset:24576\n" \
"ds_read_b64_tr_b16 a[206:207],v11 offset:24576\nds_read_b64_tr_b16 a[210:211],v8 offset:26624\nds_read_b64_tr_b16 a[214:215],v9 offset:26624\nds_read_b64_tr_b16 a[218:219],v10 offset:26624\n" \
"ds_read_b64_tr_b16 a[222:223],v11 offset:26624\ns_waitcnt vmcnt(20)\nds_read_b64_tr_b16 a[224:225],v8\nds_read_b64_tr_b16 a[228:229],v9\n" \
"ds_read_b64_tr_b16 a[232:233],v10\nds_read_b64_tr_b16 a[236:237],v11\nds_read_b64_tr_b16 a[240:241],v8 offset:2048\nds_read_b64_tr_b16 a[244:245],v9 offset:2048\n" \
"s_waitcnt lgkmcnt(14)\ns_barrier\nbuffer_load_dwordx4 v[112:115],v3,s[36:39],s16 offen sc1\nbuffer_load_dwordx4 v[116:119],v3,s[36:39],s17 offen sc1\n" \
"buffer_load_dwordx4 v[120:123],v124,s[36:39],s18 offen sc1\nds_read_b64_tr_b16 a[248:249],v10 offset:2048\nds_read_b64_tr_b16 a[252:253],v11 offset:2048\ns_waitcnt vmcnt(19)\n" \
"ds_read_b64_tr_b16 a[226:227],v8 offset:4096\nds_read_b64_tr_b16 a[230:231],v9 offset:4096\nds_read_b64_tr_b16 a[234:235],v10 offset:4096\nds_read_b64_tr_b16 a[238:239],v11 offset:4096\n" \
"ds_read_b64_tr_b16 a[242:243],v8 offset:6144\nds_read_b64_tr_b16 a[246:247],v9 offset:6144\nds_read_b64_tr_b16 a[250:251],v10 offset:6144\nds_read_b64_tr_b16 a[254:255],v11 offset:6144\n" \
"s_waitcnt vmcnt(15)\nds_read_b64_tr_b16 v[192:193],v15 offset:8192\nds_read_b64_tr_b16 v[196:197],v15 offset:8704\nds_read_b64_tr_b16 v[194:195],v15 offset:9216\n" \
"ds_read_b64_tr_b16 v[198:199],v15 offset:9728\nds_read_b64_tr_b16 v[200:201],v15 offset:10240\nds_read_b64_tr_b16 v[204:205],v15 offset:10752\nds_read_b64_tr_b16 v[202:203],v15 offset:11264\n" \
"ds_read_b64_tr_b16 v[206:207],v15 offset:11776\ns_waitcnt vmcnt(11)\nds_read_b64_tr_b16 v[208:209],v15 offset:12288\nds_read_b64_tr_b16 v[212:213],v15 offset:12800\n" \
"ds_read_b64_tr_b16 v[210:211],v15 offset:13312\nds_read_b64_tr_b16 v[214:215],v15 offset:13824\nds_read_b64_tr_b16 v[216:217],v15 offset:14336\nds_read_b64_tr_b16 v[220:221],v15 offset:14848\n" \
"ds_read_b64_tr_b16 v[218:219],v15 offset:15360\nds_read_b64_tr_b16 v[222:223],v15 offset:15872\ns_waitcnt vmcnt(7)\nds_read_b64_tr_b16 v[224:225],v15 offset:16384\n" \
"ds_read_b64_tr_b16 v[228:229],v15 offset:16896\nds_read_b64_tr_b16 v[226:227],v15 offset:17408\nds_read_b64_tr_b16 v[230:231],v15 offset:17920\nds_read_b64_tr_b16 v[232:233],v15 offset:18432\n" \
"ds_read_b64_tr_b16 v[236:237],v15 offset:18944\nds_read_b64_tr_b16 v[234:235],v15 offset:19456\nds_read_b64_tr_b16 v[238:239],v15 offset:19968\ns_waitcnt vmcnt(3)\n" \
"ds_read_b64_tr_b16 v[240:241],v15 offset:20480\nds_read_b64_tr_b16 v[244:245],v15 offset:20992\nds_read_b64_tr_b16 v[242:243],v15 offset:21504\nds_read_b64_tr_b16 v[246:247],v15 offset:22016\n" \
"ds_read_b64_tr_b16 v[248:249],v15 offset:22528\nds_read_b64_tr_b16 v[252:253],v15 offset:23040\nds_read_b64_tr_b16 v[250:251],v15 offset:23552\nds_read_b64_tr_b16 v[254:255],v15 offset:24064\n" \
"s_waitcnt vmcnt(1)\nv_and_b32 v125,v112,v116\ns_waitcnt vmcnt(0)\nv_bitop3_b32 v125,v125,v120,1 bitop3:0x80\n" \
"v_cmp_eq_u32 vcc,0,v125\ns_nop 0\ns_cbranch_vccnz .LsuN_%=\n.LbuN_%=:\n" \
"v_mfma_f32_16x16x32_f16 v[80:83],v[32:35],v[112:115],0\nv_and_b32 v3,31,v0\nv_mul_u32_u24 v3,72,v3\nv_mfma_f32_16x16x32_f16 v[84:87],v[36:39],v[112:115],0\n" \
"v_add_u32 v13,9216,v13\nv_mov_b32 v104,-1\nv_mfma_f32_16x16x32_f16 v[88:91],v[32:35],v[116:119],0\nv_mov_b32 v105,-1\n" \
"v_mov_b32 v106,-1\nv_mfma_f32_16x16x32_f16 v[92:95],v[36:39],v[116:119],0\nv_mov_b32 v107,-1\ns_add_u32 s27,s2,0\n" \
"v_mfma_f32_16x16x32_f16 v[96:99],v[32:35],v[120:123],0\ns_lshr_b32 s31,s27,4\ns_mul_i32 s96,s31,37\ns_lshr_b32 s96,s96,8\n" \
"s_lshl2_add_u32 s31,s96,s31\ns_mul_i32 s31,s31,409600\ns_and_b32 s96,s27,15\ns_lshl_b32 s96,s96,14\n" \
"s_add_u32 s31,s31,s96\ns_add_u32 s31,s31,13090816\ns_cmp_lt_u32 s27,158\ns_cselect_b32 s31,s31,2147483648\n" \
"buffer_store_dwordx4 v[104:107],v2,s[36:39],s31 offen sc1\nv_mfma_f32_16x16x32_f16 v[100:103],v[36:39],v[120:123],0\nv_cvt_pk_f16_f32 v112,v84,v80\nv_cvt_pk_f16_f32 v113,v85,v81\n" \
"v_cvt_pk_f16_f32 v114,v86,v82\nv_cvt_pk_f16_f32 v115,v87,v83\nds_write_b128 v57,v[112:115]\nv_cvt_pk_f16_f32 v116,v92,v88\n" \
"v_cvt_pk_f16_f32 v117,v93,v89\ns_add_u32 s31,s31,4096\nbuffer_store_dwordx4 v[104:107],v2,s[36:39],s31 offen sc1\nv_cvt_pk_f16_f32 v118,v94,v90\n" \
"v_cvt_pk_f16_f32 v119,v95,v91\nds_write_b128 v58,v[116:119]\nv_cvt_pk_f16_f32 v120,v100,v96\nv_cvt_pk_f16_f32 v121,v101,v97\n" \
"v_cvt_pk_f16_f32 v122,v102,v98\nv_cvt_pk_f16_f32 v123,v103,v99\nds_write_b128 v60,v[120:123]\ns_add_u32 s31,s31,4096\n" \
"buffer_store_dwordx4 v[104:107],v2,s[36:39],s31 offen sc1\nv_add_u32 v60,31104,v3\nv_add_u32 v3,28800,v3\nds_read2_b64 v[32:35],v3 offset0:0 offset1:1\n" \
"ds_read2_b64 v[36:39],v3 offset0:2 offset1:3\ns_add_u32 s31,s31,4096\ns_add_u32 s27,s2,111\ns_lshr_b32 s97,s27,4\n" \
"s_mul_i32 s95,s97,37\ns_lshr_b32 s95,s95,8\ns_lshl2_add_u32 s97,s95,s97\ns_mul_i32 s97,s97,409600\n" \
"s_and_b32 s95,s27,15\ns_lshl_b32 s95,s95,14\nbuffer_store_dwordx4 v[104:107],v2,s[36:39],s31 offen sc1\ns_add_u32 s97,s97,s95\n" \
"s_add_u32 s97,s97,13090816\ns_cmp_lt_u32 s27,158\ns_cselect_b32 s97,s97,2147483648\ns_waitcnt lgkmcnt(2)\n" \
"s_barrier\nds_read_b64 v[112:113],v56\nds_read_b64 v[116:117],v56 offset:512\nds_read_b64 v[120:121],v56 offset:256\n" \
"ds_read_b64 v[124:125],v56 offset:768\nds_read_b64 v[114:115],v56 offset:1024\nds_read_b64 v[122:123],v56 offset:1280\nds_read_b64 v[56:57],v3 offset:64\n" \
"buffer_store_dwordx4 v[104:107],v2,s[36:39],s97 offen sc1\ns_add_u32 s97,s97,4096\nv_bfe_u32 v110,v0,1,2\nv_bfe_u32 v111,v0,4,1\n" \
"v_xor_b32 v110,v110,v111\nv_and_b32 v111,1,v0\nv_lshl_add_u32 v110,v110,1,v111\nv_bfe_u32 v111,v0,3,3\n" \
"v_lshlrev_b32 v111,12,v111\nv_lshl_add_u32 v4,v110,4,v111\nv_xor_b32 v5,64,v4\nv_add_u32 v5,31744,v5\n" \
"v_add_u32 v6,63488,v4\nbuffer_store_dwordx4 v[104:107],v2,s[36:39],s97 offen sc1\ns_waitcnt lgkmcnt(5)\nv_swap_b32 v113,v116\n" \
"s_add_u32 s97,s97,4096\ns_waitcnt lgkmcnt(3)\nv_swap_b32 v121,v124\nv_add_u32 v7,63488,v5\n" \
"s_waitcnt lgkmcnt(2)\nv_mfma_f32_16x16x32_f16 v[80:83],v[112:115],v[40:43],0\nv_mov_b32 v118,v115\ns_waitcnt lgkmcnt(1)\n" \
"v_mov_b32 v126,v123\nv_mfma_f32_16x16x32_f16 v[84:87],v[112:115],v[44:47],0\ns_lshr_b32 s26,s32,1\ns_mul_i32 s92,s26,1152\n" \
"v_mfma_f32_16x16x32_f16 v[88:91],v[120:123],v[40:43],0\nbuffer_store_dwordx4 v[104:107],v2,s[36:39],s97 offen sc1\nv_mfma_f32_16x16x32_f16 v[92:95],v[120:123],v[44:47],0\ns_add_u32 s97,s97,4096\n" \
"s_lshr_b32 s26,s3,1\nv_mfma_f32_16x16x32_f16 v[96:99],v[116:119],v[40:43],0\ns_lshl_b32 s26,s26,6\ns_add_u32 s92,s92,s26\n" \
"v_mfma_f32_16x16x32_f16 v[100:103],v[116:119],v[44:47],0\nv_pk_add_f32 v[88:89],v[80:81],v[88:89] neg_lo:[0,1] neg_hi:[0,1]\nv_pk_add_f32 v[90:91],v[82:83],v[90:91] neg_lo:[0,1] neg_hi:[0,1]\nv_pk_add_f32 v[92:93],v[84:85],v[92:93] neg_lo:[0,1] neg_hi:[0,1]\n" \
"buffer_store_dwordx4 v[104:107],v2,s[36:39],s97 offen sc1\nv_pk_add_f32 v[94:95],v[86:87],v[94:95] neg_lo:[0,1] neg_hi:[0,1]\nv_pk_fma_f32 v[80:81],v[80:81],2.0,v[88:89] op_sel_hi:[1,0,1] neg_lo:[0,0,1] neg_hi:[0,0,1]\nv_pk_fma_f32 v[82:83],v[82:83],2.0,v[90:91] op_sel_hi:[1,0,1] neg_lo:[0,0,1] neg_hi:[0,0,1]\n" \
"v_pk_fma_f32 v[84:85],v[84:85],2.0,v[92:93] op_sel_hi:[1,0,1] neg_lo:[0,0,1] neg_hi:[0,0,1]\nv_pk_fma_f32 v[86:87],v[86:87],2.0,v[94:95] op_sel_hi:[1,0,1] neg_lo:[0,0,1] neg_hi:[0,0,1]\nv_cvt_pk_f16_f32 v112,v80,v84\nv_cvt_pk_f16_f32 v113,v81,v85\n" \
"v_cvt_pk_f16_f32 v114,v82,v86\nv_cvt_pk_f16_f32 v115,v83,v87\nv_mfma_f32_16x16x32_f16 v[104:107],v[124:127],v[40:43],0\nv_cvt_pk_f16_f32 v116,v88,v92\n" \
"s_lshr_b32 s100,s33,3\ns_add_u32 s100,s100,0\ns_sub_u32 s101,s100,4\ns_min_u32 s100,s100,s101\n" \
"s_lshl_b32 s101,s100,12\ns_add_u32 s101,s101,s57\ns_add_u32 m0,s101,0\ns_and_b32 s101,s100,1\n" \
"s_lshr_b32 s100,s100,1\ns_lshl_b32 s100,s100,10\ns_add_u32 s100,s100,s101\ns_lshl_b32 s100,s100,7\n" \
"s_lshr_b32 s31,s33,3\ns_add_u32 s31,s31,1\ns_sub_u32 s96,s31,4\ns_min_u32 s31,s31,s96\n" \
"s_lshl_b32 s96,s31,12\ns_add_u32 s96,s96,s57\ns_lshr_b32 s97,s33,3\ns_add_u32 s97,s97,2\n" \
"s_sub_u32 s95,s97,4\ns_min_u32 s97,s97,s95\ns_lshl_b32 s95,s97,12\ns_add_u32 s95,s95,s57\n" \
"v_mfma_f32_16x16x32_f16 v[80:83],v[16:19],v[112:115],0\nv_cvt_pk_f16_f32 v117,v89,v93\nv_cvt_pk_f16_f32 v118,v90,v94\nv_mfma_f32_16x16x32_f16 v[84:87],v[20:23],v[112:115],0\n" \
"v_cvt_pk_f16_f32 v119,v91,v95\nv_pk_add_f32 v[104:105],v[96:97],v[104:105] neg_lo:[0,1] neg_hi:[0,1]\nv_mfma_f32_16x16x32_f16 v[108:111],v[124:127],v[44:47],0\nv_pk_add_f32 v[106:107],v[98:99],v[106:107] neg_lo:[0,1] neg_hi:[0,1]\n" \
"v_pk_fma_f32 v[96:97],v[96:97],2.0,v[104:105] op_sel_hi:[1,0,1] neg_lo:[0,0,1] neg_hi:[0,0,1]\nv_mfma_f32_16x16x32_f16 v[88:91],v[24:27],v[116:119],0\nv_pk_fma_f32 v[98:99],v[98:99],2.0,v[106:107] op_sel_hi:[1,0,1] neg_lo:[0,0,1] neg_hi:[0,0,1]\nds_read2_b64 v[40:43],v3 offset0:4 offset1:5\n" \
"v_mfma_f32_16x16x32_f16 v[92:95],v[28:31],v[116:119],0\nds_write2_b32 v48,v84,v80 offset0:0 offset1:1\nds_write2_b32 v49,v85,v81 offset0:0 offset1:1\nds_write2_b32 v50,v86,v82 offset0:0 offset1:1\n" \
"v_pk_add_f32 v[108:109],v[100:101],v[108:109] neg_lo:[0,1] neg_hi:[0,1]\nv_pk_add_f32 v[110:111],v[102:103],v[110:111] neg_lo:[0,1] neg_hi:[0,1]\nv_pk_fma_f32 v[100:101],v[100:101],2.0,v[108:109] op_sel_hi:[1,0,1] neg_lo:[0,0,1] neg_hi:[0,0,1]\nv_pk_fma_f32 v[102:103],v[102:103],2.0,v[110:111] op_sel_hi:[1,0,1] neg_lo:[0,0,1] neg_hi:[0,0,1]\n" \
"ds_write2_b32 v51,v87,v83 offset0:0 offset1:1\nv_cvt_pk_f16_f32 v120,v96,v100\nv_cvt_pk_f16_f32 v121,v97,v101\nv_cvt_pk_f16_f32 v122,v98,v102\n" \
"v_cvt_pk_f16_f32 v123,v99,v103\nds_write2_b32 v52,v92,v88 offset0:0 offset1:1\nds_write2_b32 v53,v93,v89 offset0:0 offset1:1\nv_mfma_f32_16x16x32_f16 v[96:99],v[16:19],v[120:123],0\n" \
"ds_write2_b32 v54,v94,v90 offset0:0 offset1:1\nv_mfma_f32_16x16x32_f16 v[100:103],v[20:23],v[120:123],0\nds_write2_b32 v55,v95,v91 offset0:0 offset1:1\nv_cvt_pk_f16_f32 v124,v104,v108\n" \
"v_cvt_pk_f16_f32 v125,v105,v109\nv_cvt_pk_f16_f32 v126,v106,v110\nv_cvt_pk_f16_f32 v127,v107,v111\nv_add_u32 v48,2448,v48\n" \
"v_add_u32 v49,2448,v49\nv_mfma_f32_16x16x32_f16 v[104:107],v[24:27],v[124:127],0\nds_write2_b32 v48,v100,v96 offset0:0 offset1:1\nv_mfma_f32_16x16x32_f16 v[108:111],v[28:31],v[124:127],0\n" \
"ds_write2_b32 v49,v101,v97 offset0:0 offset1:1\nv_add_u32 v50,2448,v50\nds_write2_b32 v50,v102,v98 offset0:0 offset1:1\nv_add_u32 v51,2448,v51\n" \
"ds_write2_b32 v51,v103,v99 offset0:0 offset1:1\nv_add_u32 v52,2448,v52\nv_add_u32 v53,2448,v53\nv_add_u32 v54,2448,v54\n" \
"ds_write2_b32 v52,v108,v104 offset0:0 offset1:1\nds_write2_b32 v53,v109,v105 offset0:0 offset1:1\nds_write2_b32 v54,v110,v106 offset0:0 offset1:1\nv_add_u32 v55,2448,v55\n" \
"ds_write2_b32 v55,v111,v107 offset0:0 offset1:1\nds_read2_b64 v[80:83],v13 offset0:0 offset1:1\nds_read2_b64 v[84:87],v13 offset0:2 offset1:3\ns_lshl_b32 s26,s3,8\n" \
"s_add_u32 s92,s92,s26\ns_lshl_b32 s92,s92,4\ns_and_b32 s26,s32,1\ns_lshl3_add_u32 s92,s26,s92\n" \
"s_and_b32 s26,s33,7\ns_add_u32 s92,s92,s26\nds_read2_b64 v[88:91],v13 offset0:4 offset1:5\ns_lshl_b32 s92,s92,8\n" \
"s_add_u32 s100,s100,s92\nbuffer_load_dwordx4 v4,s[40:43],s100 offen lds\nds_read2_b64 v[44:47],v3 offset0:6 offset1:7\nv_and_b32 v48,31,v0\n" \
"ds_read2_b64 v[92:95],v13 offset0:6 offset1:7\nv_mad_u32_u24 v49,v48,56,0\nv_sub_u32 v48,v13,v49\nv_and_b32 v49,31,v0\n" \
"v_mul_u32_u24 v49,68,v49\nv_sub_u32 v49,v13,v49\nbuffer_load_dwordx4 v5,s[40:43],s100 offen offset:1024 lds\nds_read_b64 v[96:97],v13 offset:64\n" \
"s_waitcnt lgkmcnt(5)\nv_pk_fma_f32 v[62:63],v[80:81],v[32:33],v[62:63]\nv_pk_fma_f32 v[64:65],v[82:83],v[34:35],v[64:65]\nv_pk_mul_f32 v[102:103],v[62:63],v[62:63]\n" \
"s_waitcnt lgkmcnt(4)\nv_pk_fma_f32 v[66:67],v[84:85],v[36:37],v[66:67]\nv_pk_add_f32 v[100:101],v[62:63],v[64:65]\nv_pk_fma_f32 v[102:103],v[64:65],v[64:65],v[102:103]\n" \
"v_pk_fma_f32 v[68:69],v[86:87],v[38:39],v[68:69]\nbuffer_load_dwordx4 v6,s[40:43],s100 offen offset:2048 lds\nv_pk_add_f32 v[100:101],v[100:101],v[66:67]\nv_pk_fma_f32 v[102:103],v[66:67],v[66:67],v[102:103]\n" \
"ds_read2_b64 v[112:115],v60 offset0:0 offset1:1\nds_read2_b64 v[116:119],v60 offset0:2 offset1:3\ns_waitcnt lgkmcnt(5)\nv_pk_fma_f32 v[70:71],v[88:89],v[40:41],v[70:71]\n" \
"v_pk_add_f32 v[100:101],v[100:101],v[68:69]\nds_read2_b64 v[120:123],v60 offset0:4 offset1:5\nv_pk_fma_f32 v[102:103],v[68:69],v[68:69],v[102:103]\nbuffer_load_dwordx4 v7,s[40:43],s100 offen offset:3072 lds\n" \
"ds_read2_b64 v[124:127],v60 offset0:6 offset1:7\nv_pk_fma_f32 v[72:73],v[90:91],v[42:43],v[72:73]\nv_pk_add_f32 v[100:101],v[100:101],v[70:71]\nds_read_b64 v[98:99],v60 offset:64\n" \
"v_add_u32 v60,2304,v60\nv_pk_fma_f32 v[102:103],v[70:71],v[70:71],v[102:103]\nds_read2_b64 v[16:19],v60 offset0:0 offset1:1\ns_waitcnt lgkmcnt(7)\n" \
"v_pk_fma_f32 v[74:75],v[92:93],v[44:45],v[74:75]\ns_add_u32 m0,s96,0\ns_and_b32 s96,s31,1\ns_lshr_b32 s31,s31,1\n" \
"s_lshl_b32 s31,s31,10\ns_add_u32 s31,s31,s96\ns_lshl_b32 s31,s31,7\ns_add_u32 s31,s31,s92\n" \
"buffer_load_dwordx4 v4,s[40:43],s31 offen lds\nv_pk_add_f32 v[100:101],v[100:101],v[72:73]\nv_pk_fma_f32 v[102:103],v[72:73],v[72:73],v[102:103]\nv_pk_fma_f32 v[76:77],v[94:95],v[46:47],v[76:77]\n" \
"v_pk_add_f32 v[100:101],v[100:101],v[74:75]\nv_pk_fma_f32 v[102:103],v[74:75],v[74:75],v[102:103]\nds_read2_b64 v[20:23],v60 offset0:2 offset1:3\ns_waitcnt lgkmcnt(7)\n" \
"v_pk_fma_f32 v[78:79],v[96:97],v[56:57],v[78:79]\nv_pk_add_f32 v[100:101],v[100:101],v[76:77]\nbuffer_load_dwordx4 v5,s[40:43],s31 offen offset:1024 lds\nv_pk_fma_f32 v[102:103],v[76:77],v[76:77],v[102:103]\n" \
"v_pk_add_f32 v[100:101],v[100:101],v[78:79]\nv_pk_fma_f32 v[102:103],v[78:79],v[78:79],v[102:103]\nds_read2_b64 v[24:27],v60 offset0:4 offset1:5\nv_add_f32 v100,v100,v101\n" \
"v_add_f32 v102,v102,v103\nds_read2_b64 v[28:31],v60 offset0:6 offset1:7\nds_read_b64 v[58:59],v60 offset:64\nbuffer_load_dwordx4 v6,s[40:43],s31 offen offset:2048 lds\n" \
"v_permlane16_swap_b32 v100,v102\nv_add_f32 v100,v100,v102\ns_lshr_b32 s100,s33,3\ns_add_u32 s100,s100,3\n" \
"v_add_f32_dpp v100,v100,v100 quad_perm:[1,0,3,2] row_mask:0xf bank_mask:0xf bound_ctrl:1\ns_sub_u32 s101,s100,4\ns_min_u32 s100,s100,s101\nv_add_f32_dpp v100,v100,v100 quad_perm:[2,3,0,1] row_mask:0xf bank_mask:0xf bound_ctrl:1\n" \
"buffer_load_dwordx4 v7,s[40:43],s31 offen offset:3072 lds\ns_add_u32 m0,s95,0\nv_add_f32_dpp v100,v100,v100 row_half_mirror row_mask:0xf bank_mask:0xf bound_ctrl:1\ns_and_b32 s95,s97,1\n" \
"s_lshr_b32 s97,s97,1\nv_add_f32_dpp v100,v100,v100 row_mirror row_mask:0xf bank_mask:0xf bound_ctrl:1\nv_mul_f32 v104,0x3ae38e39,v100\nv_mul_f32 v102,0x3ae38e39,v100\n" \
"s_lshl_b32 s97,s97,10\ns_add_u32 s97,s97,s95\ns_lshl_b32 s97,s97,7\ns_add_u32 s97,s97,s92\n" \
"buffer_load_dwordx4 v4,s[40:43],s97 offen lds\nv_permlane16_swap_b32 v104,v102\nv_fma_f32 v102,-v104,v104,v102\nv_max_f32 v102,0,v102\n" \
"v_add_f32 v102,0x3727c5ac,v102\nv_rsq_f32 v100,v102\ns_lshl_b32 s101,s100,12\nbuffer_load_dwordx4 v5,s[40:43],s97 offen offset:1024 lds\n" \
"v_mul_f32 v101,-v104,v100\nv_pk_fma_f32 v[80:81],v[62:63],v[100:101],v[100:101] op_sel:[0,0,1] op_sel_hi:[1,0,1]\nv_pk_fma_f32 v[82:83],v[64:65],v[100:101],v[100:101] op_sel:[0,0,1] op_sel_hi:[1,0,1]\nv_pk_fma_f32 v[84:85],v[66:67],v[100:101],v[100:101] op_sel:[0,0,1] op_sel_hi:[1,0,1]\n" \
"v_pk_fma_f32 v[86:87],v[68:69],v[100:101],v[100:101] op_sel:[0,0,1] op_sel_hi:[1,0,1]\nv_pk_fma_f32 v[88:89],v[70:71],v[100:101],v[100:101] op_sel:[0,0,1] op_sel_hi:[1,0,1]\nv_pk_fma_f32 v[90:91],v[72:73],v[100:101],v[100:101] op_sel:[0,0,1] op_sel_hi:[1,0,1]\nv_pk_fma_f32 v[92:93],v[74:75],v[100:101],v[100:101] op_sel:[0,0,1] op_sel_hi:[1,0,1]\n" \
"buffer_load_dwordx4 v6,s[40:43],s97 offen offset:2048 lds\nv_pk_fma_f32 v[94:95],v[76:77],v[100:101],v[100:101] op_sel:[0,0,1] op_sel_hi:[1,0,1]\nv_pk_fma_f32 v[96:97],v[78:79],v[100:101],v[100:101] op_sel:[0,0,1] op_sel_hi:[1,0,1]\ns_add_u32 s101,s101,s57\n" \
"s_waitcnt lgkmcnt(9)\nv_readlane_b32 s24,v112,0\nv_mov_b32 v60,v61\ns_mov_b32 s25,s24\n" \
"v_pk_add_f32 v[112:113],v[112:113],s[24:25] neg_lo:[0,1] neg_hi:[0,1]\ns_waitcnt lgkmcnt(4)\nv_pk_fma_f32 v[80:81],v[80:81],v[16:17],v[112:113]\nbuffer_load_dwordx4 v7,s[40:43],s97 offen offset:3072 lds\n" \
"v_pk_mul_f32 v[102:103],v[80:81],v[80:81]\ns_add_u32 m0,s101,0\ns_and_b32 s101,s100,1\ns_lshr_b32 s100,s100,1\n" \
"s_lshl_b32 s100,s100,10\ns_add_u32 s100,s100,s101\ns_lshl_b32 s100,s100,7\ns_add_u32 s100,s100,s92\n" \
"buffer_load_dwordx4 v4,s[40:43],s100 offen lds\nv_pk_add_f32 v[114:115],v[114:115],s[24:25] neg_lo:[0,1] neg_hi:[0,1]\nv_pk_fma_f32 v[82:83],v[82:83],v[18:19],v[114:115]\nv_pk_add_f32 v[100:101],v[80:81],v[82:83]\n" \
"v_pk_fma_f32 v[102:103],v[82:83],v[82:83],v[102:103]\nv_pk_add_f32 v[116:117],v[116:117],s[24:25] neg_lo:[0,1] neg_hi:[0,1]\ns_waitcnt lgkmcnt(3)\nv_pk_fma_f32 v[84:85],v[84:85],v[20:21],v[116:117]\n" \
"v_pk_add_f32 v[100:101],v[100:101],v[84:85]\nbuffer_load_dwordx4 v5,s[40:43],s100 offen offset:1024 lds\nv_pk_fma_f32 v[102:103],v[84:85],v[84:85],v[102:103]\nv_pk_add_f32 v[118:119],v[118:119],s[24:25] neg_lo:[0,1] neg_hi:[0,1]\n" \
"v_pk_fma_f32 v[86:87],v[86:87],v[22:23],v[118:119]\nv_pk_add_f32 v[100:101],v[100:101],v[86:87]\nv_pk_fma_f32 v[102:103],v[86:87],v[86:87],v[102:103]\nv_pk_add_f32 v[120:121],v[120:121],s[24:25] neg_lo:[0,1] neg_hi:[0,1]\n" \
"s_waitcnt lgkmcnt(2)\nv_pk_fma_f32 v[88:89],v[88:89],v[24:25],v[120:121]\nbuffer_load_dwordx4 v6,s[40:43],s100 offen offset:2048 lds\nv_pk_add_f32 v[100:101],v[100:101],v[88:89]\n" \
"v_pk_fma_f32 v[102:103],v[88:89],v[88:89],v[102:103]\nv_pk_add_f32 v[122:123],v[122:123],s[24:25] neg_lo:[0,1] neg_hi:[0,1]\nv_pk_fma_f32 v[90:91],v[90:91],v[26:27],v[122:123]\nv_pk_add_f32 v[100:101],v[100:101],v[90:91]\n" \
"v_pk_fma_f32 v[102:103],v[90:91],v[90:91],v[102:103]\nv_pk_add_f32 v[124:125],v[124:125],s[24:25] neg_lo:[0,1] neg_hi:[0,1]\nbuffer_load_dwordx4 v7,s[40:43],s100 offen offset:3072 lds\ns_waitcnt lgkmcnt(1)\n" \
"v_pk_fma_f32 v[92:93],v[92:93],v[28:29],v[124:125]\nv_pk_add_f32 v[100:101],v[100:101],v[92:93]\nv_pk_fma_f32 v[102:103],v[92:93],v[92:93],v[102:103]\nv_pk_add_f32 v[126:127],v[126:127],s[24:25] neg_lo:[0,1] neg_hi:[0,1]\n" \
"v_pk_fma_f32 v[94:95],v[94:95],v[30:31],v[126:127]\nv_pk_add_f32 v[100:101],v[100:101],v[94:95]\nv_pk_fma_f32 v[102:103],v[94:95],v[94:95],v[102:103]\nv_pk_add_f32 v[98:99],v[98:99],s[24:25] neg_lo:[0,1] neg_hi:[0,1]\n" \
"s_waitcnt lgkmcnt(0)\nv_pk_fma_f32 v[96:97],v[96:97],v[58:59],v[98:99]\nv_pk_add_f32 v[100:101],v[100:101],v[96:97]\nv_pk_fma_f32 v[102:103],v[96:97],v[96:97],v[102:103]\n" \
"v_add_f32 v100,v100,v101\nv_add_f32 v102,v102,v103\nv_pk_add_f32 v[62:63],v[62:63],v[60:61] op_sel_hi:[1,0]\nv_cvt_pk_bf16_f32 v32,v62,v63\n" \
"v_permlane16_swap_b32 v100,v102\nv_add_f32 v100,v100,v102\nv_pk_add_f32 v[64:65],v[64:65],v[60:61] op_sel_hi:[1,0]\nv_cvt_pk_bf16_f32 v33,v64,v65\n" \
"v_add_f32_dpp v100,v100,v100 quad_perm:[1,0,3,2] row_mask:0xf bank_mask:0xf bound_ctrl:1\nv_pk_add_f32 v[66:67],v[66:67],v[60:61] op_sel_hi:[1,0]\nv_cvt_pk_bf16_f32 v34,v66,v67\nv_add_f32_dpp v100,v100,v100 quad_perm:[2,3,0,1] row_mask:0xf bank_mask:0xf bound_ctrl:1\n" \
"v_pk_add_f32 v[68:69],v[68:69],v[60:61] op_sel_hi:[1,0]\nv_cvt_pk_bf16_f32 v35,v68,v69\nv_add_f32_dpp v100,v100,v100 row_half_mirror row_mask:0xf bank_mask:0xf bound_ctrl:1\nv_pk_add_f32 v[70:71],v[70:71],v[60:61] op_sel_hi:[1,0]\n" \
"v_cvt_pk_bf16_f32 v36,v70,v71\nv_add_f32_dpp v100,v100,v100 row_mirror row_mask:0xf bank_mask:0xf bound_ctrl:1\nv_mul_f32 v104,0x3ae38e39,v100\nv_mul_f32 v102,0x3ae38e39,v100\n" \
"v_pk_add_f32 v[72:73],v[72:73],v[60:61] op_sel_hi:[1,0]\nv_cvt_pk_bf16_f32 v37,v72,v73\nv_permlane16_swap_b32 v104,v102\nv_fma_f32 v102,-v104,v104,v102\n" \
"v_max_f32 v102,0,v102\nv_add_f32 v102,0x3727c5ac,v102\nv_rsq_f32_e64 v100,v102\nv_pk_add_f32 v[74:75],v[74:75],v[60:61] op_sel_hi:[1,0]\n" \
"v_mul_f32 v101,-v104,v100\nv_pk_fma_f32 v[80:81],v[80:81],v[100:101],v[100:101] op_sel:[0,0,1] op_sel_hi:[1,0,1]\nv_pk_fma_f32 v[82:83],v[82:83],v[100:101],v[100:101] op_sel:[0,0,1] op_sel_hi:[1,0,1]\nv_pk_fma_f32 v[84:85],v[84:85],v[100:101],v[100:101] op_sel:[0,0,1] op_sel_hi:[1,0,1]\n" \
"v_pk_fma_f32 v[86:87],v[86:87],v[100:101],v[100:101] op_sel:[0,0,1] op_sel_hi:[1,0,1]\nv_cvt_pk_bf16_f32 v16,v80,v81\nv_cvt_pk_bf16_f32 v17,v82,v83\nv_cvt_pk_bf16_f32 v18,v84,v85\n" \
"v_cvt_pk_bf16_f32 v19,v86,v87\nds_write_b128 v48,v[16:19]\nv_pk_fma_f32 v[88:89],v[88:89],v[100:101],v[100:101] op_sel:[0,0,1] op_sel_hi:[1,0,1]\nv_pk_fma_f32 v[90:91],v[90:91],v[100:101],v[100:101] op_sel:[0,0,1] op_sel_hi:[1,0,1]\n" \
"v_pk_fma_f32 v[92:93],v[92:93],v[100:101],v[100:101] op_sel:[0,0,1] op_sel_hi:[1,0,1]\nv_pk_fma_f32 v[94:95],v[94:95],v[100:101],v[100:101] op_sel:[0,0,1] op_sel_hi:[1,0,1]\nv_cvt_pk_bf16_f32 v20,v88,v89\nv_cvt_pk_bf16_f32 v21,v90,v91\n" \
"v_cvt_pk_bf16_f32 v22,v92,v93\nv_cvt_pk_bf16_f32 v23,v94,v95\nds_write_b128 v48,v[20:23] offset:512\nv_pk_fma_f32 v[96:97],v[96:97],v[100:101],v[100:101] op_sel:[0,0,1] op_sel_hi:[1,0,1]\n" \
"v_cvt_pk_bf16_f32 v24,v96,v97\nds_write_b32 v49,v24 offset:1024\nv_cvt_pk_bf16_f32 v38,v74,v75\nv_pk_add_f32 v[76:77],v[76:77],v[60:61] op_sel_hi:[1,0]\n" \
"v_cvt_pk_bf16_f32 v39,v76,v77\nv_pk_add_f32 v[78:79],v[78:79],v[60:61] op_sel_hi:[1,0]\nv_cvt_pk_bf16_f32 v40,v78,v79\nv_bfe_u32 v50,v0,2,2\n" \
"v_mul_u32_u24 v50,4896,v50\nv_bfe_u32 v51,v0,4,2\nv_lshl_add_u32 v51,v51,2,s3\nv_lshl_add_u32 v50,v51,5,v50\n" \
"v_and_b32 v52,3,v0\nv_lshl_add_u32 v50,v52,3,v50\nv_and_b32 v52,0xf0,v1\nv_lshl_add_u32 v51,v51,16,v52\n" \
"v_bfrev_b32 v52,1\nv_cmp_gt_u32 vcc,256,v1\nv_and_b32 v53,31,v0\nv_mul_u32_u24 v53,12,v53\n" \
"v_cndmask_b32 v52,v52,v51,vcc\nv_sub_u32 v53,v12,v53\ns_lshl_b32 s24,s34,8\ns_add_u32 s24,s24,9764864\n" \
"s_waitcnt lgkmcnt(0)\ns_barrier\nds_read_b64_tr_b16 v[112:113],v50 offset:9216\nds_read_b64_tr_b16 v[114:115],v50 offset:11664\n" \
"ds_read_b64_tr_b16 v[116:117],v50 offset:9728\nds_read_b64_tr_b16 v[118:119],v50 offset:12176\nds_read_b64_tr_b16 v[120:121],v50 offset:10240\nds_read_b64_tr_b16 v[122:123],v50 offset:12688\n" \
"s_add_u32 s25,s24,1048576\ns_add_u32 s26,s24,2097152\ns_add_u32 s13,s13,7405568\ns_load_dwordx2 s[4:5],s[0:1],56\n" \
"s_load_dwordx2 s[16:17],s[0:1],64\ns_waitcnt lgkmcnt(0)\nbuffer_store_dwordx4 v[112:115],v51,s[36:39],s24 offen sc1\nbuffer_store_dwordx4 v[116:119],v51,s[36:39],s25 offen sc1\n" \
"buffer_store_dwordx4 v[120:123],v52,s[36:39],s26 offen sc1\nbuffer_store_dwordx4 v[32:35],v12,s[36:39],s13 offen sc1\nbuffer_store_dwordx4 v[36:39],v12,s[36:39],s13 offen offset:512 sc1\nbuffer_store_dword v40,v53,s[36:39],s13 offen offset:1024 sc1\n" \
"s_lshr_b32 s31,s33,3\ns_lshl1_add_u32 s31,s31,0\ns_sub_u32 s96,s31,7\ns_min_u32 s31,s31,s96\n" \
"s_lshl_b32 s96,s31,12\ns_add_u32 s96,s96,s57\ns_add_u32 m0,s96,0\ns_and_b32 s96,s31,1\n" \
"s_lshr_b32 s31,s31,1\ns_lshl_b32 s31,s31,10\ns_add_u32 s31,s31,s96\ns_lshl_b32 s31,s31,7\n" \
"s_add_u32 s31,s31,s92\ns_add_u32 s31,s31,262144\ns_lshr_b32 s97,s33,3\ns_lshl1_add_u32 s97,s97,1\n" \
"s_sub_u32 s95,s97,7\ns_min_u32 s97,s97,s95\ns_lshl_b32 s95,s97,12\ns_add_u32 s95,s95,s57\n" \
"s_lshr_b32 s100,s33,3\ns_lshl1_add_u32 s100,s100,2\ns_sub_u32 s101,s100,7\ns_min_u32 s100,s100,s101\n" \
"s_lshl_b32 s101,s100,12\ns_add_u32 s101,s101,s57\nv_and_b32 v64,63,v0\ns_and_b32 s25,s33,7\n" \
"v_lshrrev_b32 v66,5,v64\nv_and_b32 v67,1,v0\ns_lshl2_add_u32 s25,s25,s3\ns_and_b32 s26,s33,6\n" \
"s_lshr_b32 s27,s3,1\nv_lshl_add_u32 v66,v66,1,v67\ns_mul_i32 s24,s32,1152\ns_lshl_b32 s25,s25,5\n" \
"s_add_u32 s26,s26,s27\ns_mov_b32 s6,9216\ns_mov_b32 s7,0x20000\nv_lshlrev_b32 v68,3,v66\n" \
"s_add_u32 s25,s25,s24\nv_bfe_u32 v72,v0,1,1\nv_lshrrev_b32 v74,2,v64\ns_lshl4_add_u32 s26,s26,s24\n" \
"v_and_b32 v73,12,v0\nv_lshrrev_b32 v71,4,v64\nv_and_b32 v69,7,v0\nbuffer_load_dwordx2 v[80:81],v68,s[4:7],s25 offen\n" \
"v_lshl_add_u32 v72,v72,2,s21\nv_and_b32 v74,12,v74\nv_add_u32 v71,v71,v73\ns_lshl_b32 s24,s3,4\n" \
"v_lshlrev_b32 v70,1,v69\nv_lshl_add_u32 v72,v72,4,v74\ns_mov_b32 s15,s19\nv_add_u32 v71,s24,v71\n" \
"buffer_load_ushort v82,v70,s[4:7],s26 offen offset:1024\ns_mov_b32 s18,4096\ns_mov_b32 s19,0x20000\nv_lshlrev_b32 v74,1,v71\n" \
"s_lshl_b32 s24,s22,8\nbuffer_load_ushort v83,v74,s[16:19],s24 offen\nv_add_u32 v72,s3,v72\nbuffer_load_ushort v84,v74,s[16:19],s24 offen offset:128\n" \
"v_lshlrev_b32_e64 v75,1,v72\nbuffer_load_ushort v85,v75,s[16:19],s24 offen\nv_lshl_add_u32 v73,v67,1,v73\nv_mul_u32_u24 v91,1152,v72\n" \
"v_lshl_add_u32 v91,v73,1,v91\nv_lshlrev_b32_e64 v73,2,v73\nv_add_u32 v73,163712,v73\nds_read_b64 v[86:87],v73\n" \
"s_mul_i32 s24,s22,147456\ns_mul_i32 s25,s20,288\nv_bfe_u32 v58,v0,1,1\ns_add_u32 s24,s24,s25\n" \
"v_mul_u32_u24 v58,544,v58\nv_bfe_u32 v75,v0,2,1\ns_lshl_b32 s25,s21,6\nv_lshl_add_u32 v58,v75,8,v58\n" \
"s_add_u32 s25,s25,s24\nv_bfe_u32 v75,v0,3,2\ns_add_u32 s24,s24,256\nv_lshl_add_u32 v58,v75,6,v58\n" \
"v_add_u32 v91,s24,v91\nv_mul_u32_u24 v90,1152,v71\nv_and_b32 v74,3,v0\nv_lshl_add_u32 v58,v66,4,v58\n" \
"s_mul_i32 s24,s3,1056\nv_lshl_add_u32 v90,v74,3,v90\nv_add_u32 v58,s24,v58\nv_add_u32 v90,s25,v90\n" \
"s_and_b32 s24,s3,1\nv_mul_u32_u24 v60,288,v75\ns_lshl_b32 s24,s24,2\ns_lshr_b32 s25,s3,1\n" \
"v_lshrrev_b32 v75,5,v64\ns_add_u32 s24,s24,s25\nv_lshl_add_u32 v60,v75,6,v60\ns_lshl_b32 s24,s24,5\n" \
"v_lshl_add_u32 v60,v69,2,v60\ns_add_u32 s24,s24,4224\nv_add_u32 v60,s24,v60\ns_bfe_u32 s24,s33,0x10001\n" \
"s_add_u32 s24,s24,s15\ns_add_u32 s24,s24,17\ns_lshl2_add_u32 s24,s24,s3\ns_waitcnt lgkmcnt(0)\n" \
"v_lshrrev_b32 v86,16,v86\ns_lshl_b32 s24,s24,7\nv_mov_b32 v92,v4\nv_mov_b32 v93,v5\n" \
"v_add_u32 v74,s57,v1\nv_or_b32 v95,v86,v87\nv_lshl_add_u32 v62,v64,1,s24\nv_bfrev_b32 v76,1\n" \
"v_cmp_eq_u32 vcc,0,v64\ns_lshl2_add_u32 s24,s34,s3\ns_lshl2_add_u32 s24,s24,65536\nv_cndmask_b32 v76,v76,0,vcc\n" \
"v_mov_b32 v77,1\ns_barrier\nv_mov_b32 v57,0xc0135761\nv_add_u32 v59,0x80000,v1\n" \
"v_lshl_add_u32 v61,v64,3,s23\nv_mov_b32 v63,0\nv_bfe_u32 v100,v0,4,2\nv_bfe_u32 v101,v0,2,2\n" \
"v_lshl_add_u32 v100,v100,3,v101\nv_bfe_u32 v101,v0,3,2\nv_lshl_add_u32 v100,v100,2,v101\nv_and_b32 v101,3,v0\n" \
"v_lshl_add_u32 v100,v100,2,v101\nv_lshlrev_b32 v100,3,v100\nv_xor_b32 v8,0,v100\nv_add_u32 v8,s57,v8\n" \
"v_mov_b32 v88,v8\nv_xor_b32 v9,32,v100\nv_add_u32 v9,s57,v9\nv_xor_b32 v10,64,v100\n" \
"v_add_u32 v10,s57,v10\nv_xor_b32 v11,96,v100\nv_add_u32 v11,s57,v11\ns_waitcnt vmcnt(5)\n" \
"buffer_store_dword v77,v76,s[36:39],s24 offen sc1\nds_read_b64_tr_b16 v[128:129],v8\nds_read_b64_tr_b16 v[130:131],v8 offset:512\nds_read_b64_tr_b16 v[132:133],v9\n" \
"ds_read_b64_tr_b16 v[134:135],v9 offset:512\nds_read_b64_tr_b16 v[136:137],v10\nds_read_b64_tr_b16 v[138:139],v10 offset:512\nds_read_b64_tr_b16 v[140:141],v11\n" \
"ds_read_b64_tr_b16 v[142:143],v11 offset:512\nds_read_b64_tr_b16 v[144:145],v8 offset:4096\nds_read_b64_tr_b16 v[146:147],v8 offset:4608\nds_read_b64_tr_b16 v[148:149],v9 offset:4096\n" \
"ds_read_b64_tr_b16 v[150:151],v9 offset:4608\nds_read_b64_tr_b16 v[152:153],v10 offset:4096\nds_read_b64_tr_b16 v[154:155],v10 offset:4608\nds_read_b64_tr_b16 v[156:157],v11 offset:4096\n" \
"ds_read_b64_tr_b16 v[158:159],v11 offset:4608\nds_read_b64_tr_b16 v[160:161],v8 offset:8192\nds_read_b64_tr_b16 v[162:163],v8 offset:8704\ns_waitcnt vmcnt(5)\n" \
"v_and_b32 v53,0xffff0000,v80\nv_and_b32 v55,0xffff0000,v81\nv_lshlrev_b32 v52,16,v80\nv_lshlrev_b32 v54,16,v81\n" \
"ds_read_b64_tr_b16 v[164:165],v9 offset:8192\nds_read_b64_tr_b16 v[166:167],v9 offset:8704\nds_read_b64_tr_b16 v[168:169],v10 offset:8192\nds_read_b64_tr_b16 v[170:171],v10 offset:8704\n" \
"ds_read_b64_tr_b16 v[172:173],v11 offset:8192\ns_waitcnt vmcnt(4)\nv_lshlrev_b32 v56,16,v82\nds_read_b64_tr_b16 v[174:175],v11 offset:8704\n" \
"ds_read_b64_tr_b16 v[176:177],v8 offset:12288\nds_read_b64_tr_b16 v[178:179],v8 offset:12800\nds_read_b64_tr_b16 v[180:181],v9 offset:12288\nds_read_b64_tr_b16 v[182:183],v9 offset:12800\n" \
"s_waitcnt vmcnt(2)\nv_lshl_or_b32 v89,v84,16,v83\nds_write_b128 v74,v[88:91] offset:28672\ns_waitcnt vmcnt(1)\n" \
"v_lshlrev_b32_e64 v94,16,v85\nds_write_b128 v74,v[92:95] offset:29696\nds_read_b64_tr_b16 v[184:185],v10 offset:12288\nds_read_b64_tr_b16 v[186:187],v10 offset:12800\n" \
"ds_read_b64_tr_b16 v[188:189],v11 offset:12288\nds_read_b64_tr_b16 v[190:191],v11 offset:12800\ns_waitcnt lgkmcnt(0)\nbuffer_load_dwordx4 v4,s[40:43],s31 offen lds\n" \
"buffer_load_dwordx4 v5,s[40:43],s31 offen offset:1024 lds\nbuffer_load_dwordx4 v6,s[40:43],s31 offen offset:2048 lds\nbuffer_load_dwordx4 v7,s[40:43],s31 offen offset:3072 lds\ns_add_u32 m0,s95,0\n" \
"s_and_b32 s95,s97,1\ns_lshr_b32 s97,s97,1\ns_lshl_b32 s97,s97,10\ns_add_u32 s97,s97,s95\n" \
"s_lshl_b32 s97,s97,7\ns_add_u32 s97,s97,s92\ns_add_u32 s97,s97,262144\nbuffer_load_dwordx4 v4,s[40:43],s97 offen lds\n" \
"buffer_load_dwordx4 v5,s[40:43],s97 offen offset:1024 lds\nbuffer_load_dwordx4 v6,s[40:43],s97 offen offset:2048 lds\nbuffer_load_dwordx4 v7,s[40:43],s97 offen offset:3072 lds\ns_add_u32 m0,s101,0\n" \
"s_and_b32 s101,s100,1\ns_lshr_b32 s100,s100,1\ns_lshl_b32 s100,s100,10\ns_add_u32 s100,s100,s101\n" \
"s_lshl_b32 s100,s100,7\ns_add_u32 s100,s100,s92\ns_add_u32 s100,s100,262144\nbuffer_load_dwordx4 v4,s[40:43],s100 offen lds\n" \
"buffer_load_dwordx4 v5,s[40:43],s100 offen offset:1024 lds\nbuffer_load_dwordx4 v6,s[40:43],s100 offen offset:2048 lds\nbuffer_load_dwordx4 v7,s[40:43],s100 offen offset:3072 lds\ns_lshr_b32 s31,s33,3\n" \
"s_lshl1_add_u32 s31,s31,3\ns_sub_u32 s96,s31,7\ns_min_u32 s31,s31,s96\ns_lshl_b32 s96,s31,12\n" \
"s_add_u32 s96,s96,s57\ns_add_u32 m0,s96,0\ns_and_b32 s96,s31,1\ns_lshr_b32 s31,s31,1\n" \
"s_lshl_b32 s31,s31,10\ns_add_u32 s31,s31,s96\ns_lshl_b32 s31,s31,7\ns_add_u32 s31,s31,s92\n" \
"s_add_u32 s31,s31,262144\nbuffer_load_dwordx4 v4,s[40:43],s31 offen lds\nbuffer_load_dwordx4 v5,s[40:43],s31 offen offset:1024 lds\nbuffer_load_dwordx4 v6,s[40:43],s31 offen offset:2048 lds\n" \
"buffer_load_dwordx4 v7,s[40:43],s31 offen offset:3072 lds\ns_lshr_b32 s97,s33,3\ns_lshl1_add_u32 s97,s97,4\ns_sub_u32 s95,s97,7\n" \
"s_min_u32 s97,s97,s95\ns_lshl_b32 s95,s97,12\ns_add_u32 s95,s95,s57\ns_add_u32 m0,s95,0\n" \
"s_and_b32 s95,s97,1\ns_lshr_b32 s97,s97,1\ns_lshl_b32 s97,s97,10\ns_add_u32 s97,s97,s95\n" \
"s_lshl_b32 s97,s97,7\ns_add_u32 s97,s97,s92\ns_add_u32 s97,s97,262144\nbuffer_load_dwordx4 v4,s[40:43],s97 offen lds\n" \
"buffer_load_dwordx4 v5,s[40:43],s97 offen offset:1024 lds\nbuffer_load_dwordx4 v6,s[40:43],s97 offen offset:2048 lds\nbuffer_load_dwordx4 v7,s[40:43],s97 offen offset:3072 lds\ns_lshr_b32 s100,s33,3\n" \
"s_lshl1_add_u32 s100,s100,5\ns_sub_u32 s101,s100,7\ns_min_u32 s100,s100,s101\ns_lshl_b32 s101,s100,12\n" \
"s_add_u32 s101,s101,s57\ns_add_u32 m0,s101,0\ns_and_b32 s101,s100,1\ns_lshr_b32 s100,s100,1\n" \
"s_lshl_b32 s100,s100,10\ns_add_u32 s100,s100,s101\ns_lshl_b32 s100,s100,7\ns_add_u32 s100,s100,s92\n" \
"s_add_u32 s100,s100,262144\nbuffer_load_dwordx4 v4,s[40:43],s100 offen lds\nbuffer_load_dwordx4 v5,s[40:43],s100 offen offset:1024 lds\nbuffer_load_dwordx4 v6,s[40:43],s100 offen offset:2048 lds\n" \
"buffer_load_dwordx4 v7,s[40:43],s100 offen offset:3072 lds\ns_lshr_b32 s31,s33,3\ns_lshl1_add_u32 s31,s31,6\ns_sub_u32 s96,s31,7\n" \
"s_min_u32 s31,s31,s96\ns_lshl_b32 s96,s31,12\ns_add_u32 s96,s96,s57\ns_add_u32 m0,s96,0\n" \
"s_and_b32 s96,s31,1\ns_lshr_b32 s31,s31,1\ns_lshl_b32 s31,s31,10\ns_add_u32 s31,s31,s96\n" \
"s_lshl_b32 s31,s31,7\ns_add_u32 s31,s31,s92\ns_add_u32 s31,s31,262144\nbuffer_load_dwordx4 v4,s[40:43],s31 offen lds\n" \
"buffer_load_dwordx4 v5,s[40:43],s31 offen offset:1024 lds\nbuffer_load_dwordx4 v6,s[40:43],s31 offen offset:2048 lds\nbuffer_load_dwordx4 v7,s[40:43],s31 offen offset:3072 lds\ns_branch .Lgemm_%=\n" \
".LC_%=:\nv_bfe_u32 v112,v0,3,2\nv_bfe_u32 v113,v0,5,1\nv_lshl_add_u32 v112,v112,8,v113\n" \
"s_lshl_b32 s48,s33,2\nv_lshrrev_b32 v113,3,v0\ns_add_u32 s48,s48,s3\nv_and_b32 v113,6,v113\n" \
"v_and_b32 v114,7,v0\ns_mul_i32 s48,s48,18432\ns_mul_i32 s49,s32,1152\nv_mul_u32_u24 v112,9216,v112\n" \
"v_xor_b32 v113,v113,v114\ns_add_u32 s48,s48,s49\nv_lshl_add_u32 v4,v113,4,v112\ns_mov_b32 s10,18874368\n" \
"s_mov_b32 s11,0x20000\ns_add_u32 s48,s48,1024\ns_add_u32 m0,s88,20224\ns_add_u32 s50,s48,9437184\n" \
"buffer_load_dwordx4 v4,s[8:11],s48 offen lds\ns_add_u32 m0,s88,24320\ns_mov_b32 s51,4096\nbuffer_load_dwordx4 v4,s[8:11],s50 offen lds\n" \
"s_add_u32 m0,s88,28416\ns_mov_b32 s52,8192\nbuffer_load_dwordx4 v2,s[4:7],0 offen lds\ns_add_u32 m0,s88,32512\n" \
"s_mov_b32 s53,14336\nbuffer_load_dwordx4 v2,s[4:7],s51 offen lds\nbuffer_load_dwordx4 v[234:237],v1,s[4:7],s52 offen\nbuffer_load_dwordx4 v[238:241],v1,s[4:7],s52 offen offset:1024\n" \
"buffer_load_dwordx4 v[242:245],v2,s[4:7],s53 offen\ns_mov_b32 s52,18432\nbuffer_load_dwordx4 v[252:255],v2,s[4:7],s52 offen\ns_and_b32 s60,s33,7\n" \
"s_mul_i32 s92,s3,589824\ns_mul_i32 s57,s3,30720\ns_add_u32 s92,s92,s49\ns_lshl_b32 s62,s60,7\n" \
"v_add_u32 v5,17408,v4\nv_add_u32 v6,34816,v4\nv_add_u32 v7,52224,v4\ns_add_u32 s57,s57,38272\n" \
"s_add_u32 s92,s92,s62\ns_load_dwordx4 s[44:47],s[0:1],72\nv_and_b32 v120,0x3c0,v1\ns_lshr_b32 s59,s33,3\n" \
"v_bfe_u32 v121,v0,3,2\nv_lshlrev_b32 v120,1,v120\ns_lshl_b32 s56,s34,8\nv_bfe_u32 v73,v0,4,2\n" \
"s_lshl_b32 s59,s59,3\nv_bfe_u32 v113,v0,4,2\nv_lshl_add_u32 v120,v121,5,v120\nv_and_b32 v122,3,v0\n" \
"s_mul_i32 s54,s34,9344\ns_add_u32 s56,s56,10289152\ns_add_u32 s59,s59,s32\nv_and_b32 v72,15,v0\n" \
"v_cvt_f32_u32 v73,v73\nv_cvt_f32_u32 v74,s3\nv_mul_u32_u24 v113,589824,v113\nv_and_b32 v112,15,v0\n" \
"v_lshl_add_u32 v120,v122,3,v120\ns_add_u32 s35,s54,524288\ns_cmp_eq_u32 s3,2\ns_cselect_b32 s56,s56,2147483648\n" \
"s_lshl_b32 s59,s59,3\nv_bfe_u32 v233,v0,3,3\nv_mul_u32_u24 v233,8192,v233\nv_lshl_add_u32 v233,v1,3,v233\n" \
"s_lshl2_add_u32 s100,s33,s3\ns_add_u32 s100,s100,4294967260\ns_and_b32 s101,s100,31\ns_lshl_b32 s101,s101,3\n" \
"s_cmp_lt_u32 s100,256\ns_cselect_b32 s100,s100,0x40000\ns_lshr_b32 s100,s100,5\ns_add_u32 s100,s100,s101\n" \
"s_lshl_b32 s100,s100,6\ns_add_u32 s100,s100,s32\ns_mul_i32 s100,s100,1152\ns_mov_b32 m0,162176\n" \
"v_cvt_f32_u32_e64 v72,v72\nv_fmamk_f32 v73,v73,0x41800000,v74\nv_lshl_add_u32 v113,v112,4,v113\ns_add_u32 s58,s35,8192\n" \
"v_bfe_u32 v119,v0,4,2\nv_xor_b32 v8,0,v120\nv_xor_b32 v9,32,v120\nv_xor_b32 v10,64,v120\n" \
"v_xor_b32 v11,96,v120\ns_add_u32 s59,s59,s60\nv_mul_f32 v72,0xbb800000,v72\nv_and_b32 v118,15,v0\n" \
"v_mul_f32 v73,4.0,v73\nv_and_b32 v116,0x70,v2\nv_add_u32 v113,s56,v113\nv_add_u32 v114,s58,v2\n" \
"v_cmp_gt_u32 vcc,68,v0\nv_lshrrev_b32 v115,3,v0\nv_cvt_f32_u32 v120,s34\ns_lshl_b32 s59,s59,7\n" \
"s_lshl_b32 s61,s34,4\nv_lshrrev_b32 v48,1,v118\nv_and_b32 v49,1,v119\nv_mul_f32 v73,v73,v72\n" \
"v_cndmask_b32 v113,v113,v114,vcc\nv_lshl_add_u32 v115,v115,16,v116\nv_mul_f32 v50,0x3b800000,v120\ns_add_u32 s87,s59,262144\n" \
"s_add_u32 s61,s61,65536\nv_cvt_f32_u32 v48,v48\nv_cvt_f32_u32 v49,v49\nv_fmamk_f32 v79,v72,0x41800000,v73\n" \
"v_bfe_u32 v100,v0,1,2\nv_and_b32 v101,1,v0\nv_add_u32 v115,s87,v115\nv_mov_b32 v116,s61\n" \
"v_cmp_gt_u32 vcc,24,v0\nv_mul_f32 v48,0xbe000000,v48\nv_fma_f32 v49,v49,2.0,v50\nv_cos_f32 v78,v79\n" \
"v_lshl_add_u32 v100,v101,2,v100\nv_cndmask_b32 v115,v116,v115,vcc\nv_mul_f32 v49,v48,v49\nv_fmamk_f32 v81,v72,0x42000000,v73\n" \
"v_fmamk_f32 v83,v72,0x42400000,v73\nv_mul_f32 v101,0x3a000000,v120\nv_cvt_f32_u32 v100,v100\nv_bfrev_b32 v116,1\n" \
"v_cmp_gt_u32 vcc,25,v0\nv_fmamk_f32 v63,v48,0x40800000,v49\nv_cos_f32 v80,v81\nv_cos_f32 v82,v83\n" \
"v_fmamk_f32 v100,v100,0x3e000000,v101\nv_cndmask_b32 v115,v116,v115,vcc\nv_mov_b32 v16,-1\nv_mov_b32 v17,-1\n" \
"v_mov_b32 v18,-1\nv_mov_b32 v19,-1\nv_lshrrev_b32 v50,1,v119\nv_and_b32 v51,1,v0\n" \
"v_cos_f32 v62,v63\nv_cos_f32 v76,v73\nv_sin_f32 v77,v73\nv_sin_f32 v79,v79\n" \
"v_cos_f32 v84,v72\nv_sin_f32 v85,v72\nv_cvt_f32_u32 v101,v119\nbuffer_store_dwordx4 v[16:19],v2,s[36:39],s35 offen sc1\n" \
"v_cmp_eq_u32 vcc,v50,v51\nv_add_f32 v101,v101,v101\nv_mov_b32 v50,0x3e800000\nv_mul_f32 v101,v101,v100\n" \
"v_sin_f32 v81,v81\nv_sin_f32 v83,v83\nv_cndmask_b32 v52,0,v50,vcc\nv_cos_f32 v56,v49\n" \
"v_sin_f32 v57,v49\nv_sin_f32 v63,v63\nv_cos_f32 v106,v101\nv_sin_f32 v107,v101\n" \
"v_cmp_gt_u32 vcc,8,v118\nv_cvt_pk_f16_f32 v128,v76,-v77\nv_cvt_pk_f16_f32 v132,v77,v76\nv_cvt_pk_f16_f32 v129,v78,-v79\n" \
"v_cvt_pk_f16_f32 v133,v79,v78\nv_pk_mul_f32 v[92:93],v[76:77],v[84:85] op_sel:[1,1] op_sel_hi:[0,1] neg_lo:[1,0]\nv_pk_mul_f32 v[94:95],v[78:79],v[84:85] op_sel:[1,1] op_sel_hi:[0,1] neg_lo:[1,0]\ns_add_u32 s55,s35,4096\n" \
"v_cndmask_b32 v102,0,0.5,vcc\nv_lshlrev_b32 v51,31,v0\nbuffer_store_dwordx4 v[16:19],v2,s[36:39],s55 offen sc1\nv_pk_fma_f32 v[76:77],v[76:77],v[84:85],v[92:93] op_sel_hi:[1,0,1]\n" \
"v_pk_fma_f32 v[78:79],v[78:79],v[84:85],v[94:95] op_sel_hi:[1,0,1]\nv_xor_b32 v54,v51,v52\nv_sub_f32_e64 v104,0.5,v102\nv_cvt_pk_f16_f32 v130,v80,-v81\n" \
"v_cvt_pk_f16_f32 v134,v81,v80\nv_cvt_pk_f16_f32 v131,v82,-v83\nv_cvt_pk_f16_f32 v135,v83,v82\nv_pk_mul_f32 v[92:93],v[80:81],v[84:85] op_sel:[1,1] op_sel_hi:[0,1] neg_lo:[1,0]\n" \
"v_pk_mul_f32 v[94:95],v[82:83],v[84:85] op_sel:[1,1] op_sel_hi:[0,1] neg_lo:[1,0]\nv_pk_mul_f32 v[58:59],v[56:57],v[52:53] op_sel_hi:[1,0]\nv_pk_mul_f32 v[60:61],v[56:57],v[54:55] op_sel_hi:[1,0]\nv_pk_mul_f32 v[64:65],v[62:63],v[52:53] op_sel_hi:[1,0]\n" \
"v_pk_mul_f32 v[66:67],v[62:63],v[54:55] op_sel_hi:[1,0]\nv_pk_mul_f32 v[108:109],v[106:107],v[102:103] op_sel_hi:[1,0]\nv_pk_mul_f32 v[110:111],v[106:107],v[104:105] op_sel_hi:[1,0]\nv_pk_fma_f32 v[80:81],v[80:81],v[84:85],v[92:93] op_sel_hi:[1,0,1]\n" \
"v_pk_fma_f32 v[82:83],v[82:83],v[84:85],v[94:95] op_sel_hi:[1,0,1]\nv_fmamk_f32 v57,v48,0x3f800000,v49\nv_fmamk_f32 v63,v48,0x40a00000,v49\nv_fmamk_f32 v107,v100,0x3f800000,v101\n" \
"v_cvt_pk_f16_f32 v136,v76,-v77\nv_cvt_pk_f16_f32 v140,v77,v76\nv_cvt_pk_f16_f32 v137,v78,-v79\nv_cvt_pk_f16_f32 v141,v79,v78\n" \
"v_pk_mul_f32 v[92:93],v[76:77],v[84:85] op_sel:[1,1] op_sel_hi:[0,1] neg_lo:[1,0]\nv_pk_mul_f32 v[94:95],v[78:79],v[84:85] op_sel:[1,1] op_sel_hi:[0,1] neg_lo:[1,0]\nv_cos_f32 v56,v57\nv_cos_f32 v62,v63\n" \
"v_cos_f32_e64 v106,v107\nbuffer_store_dwordx4 v[16:19],v113,s[36:39],0 offen sc1\nv_pk_fma_f32 v[76:77],v[76:77],v[84:85],v[92:93] op_sel_hi:[1,0,1]\nv_pk_fma_f32 v[78:79],v[78:79],v[84:85],v[94:95] op_sel_hi:[1,0,1]\n" \
"v_cvt_pk_f16_f32 v138,v80,-v81\nv_cvt_pk_f16_f32 v142,v81,v80\nv_cvt_pk_f16_f32 v139,v82,-v83\nv_cvt_pk_f16_f32 v143,v83,v82\n" \
"v_pk_mul_f32 v[92:93],v[80:81],v[84:85] op_sel:[1,1] op_sel_hi:[0,1] neg_lo:[1,0]\nv_pk_mul_f32 v[94:95],v[82:83],v[84:85] op_sel:[1,1] op_sel_hi:[0,1] neg_lo:[1,0]\nv_pk_fma_f32 v[80:81],v[80:81],v[84:85],v[92:93] op_sel_hi:[1,0,1]\nv_pk_fma_f32 v[82:83],v[82:83],v[84:85],v[94:95] op_sel_hi:[1,0,1]\n" \
"v_sin_f32 v57,v57\nv_sin_f32 v63,v63\nv_sin_f32_e64 v107,v107\nv_cvt_pk_f16_f32 v144,v76,-v77\n" \
"v_cvt_pk_f16_f32 v148,v77,v76\nv_cvt_pk_f16_f32 v145,v78,-v79\nv_cvt_pk_f16_f32 v149,v79,v78\nv_pk_mul_f32 v[92:93],v[76:77],v[84:85] op_sel:[1,1] op_sel_hi:[0,1] neg_lo:[1,0]\n" \
"v_pk_mul_f32 v[94:95],v[78:79],v[84:85] op_sel:[1,1] op_sel_hi:[0,1] neg_lo:[1,0]\nv_mov_b32 v20,0\nv_mov_b32 v21,0\nv_mov_b32 v22,0\n" \
"v_mov_b32 v23,0\nbuffer_store_dwordx4 v[20:23],v115,s[36:39],0 offen sc1\nv_pk_fma_f32 v[76:77],v[76:77],v[84:85],v[92:93] op_sel_hi:[1,0,1]\nv_pk_fma_f32 v[78:79],v[78:79],v[84:85],v[94:95] op_sel_hi:[1,0,1]\n" \
"v_cvt_pk_f16_f32 v40,v58,-v61\nv_cvt_pk_f16_f32 v44,v59,v60\nv_cvt_pk_f16_f32 v41,v64,-v67\nv_cvt_pk_f16_f32 v45,v65,v66\n" \
"v_cvt_pk_f16_f32 v216,v108,-v109\nv_cvt_pk_f16_f32 v220,v109,v108\nv_cvt_pk_f16_f32 v217,v110,-v111\nv_cvt_pk_f16_f32 v221,-v111,-v110\n" \
"v_cvt_pk_f16_f32 v146,v80,-v81\nv_cvt_pk_f16_f32 v150,v81,v80\nv_cvt_pk_f16_f32 v147,v82,-v83\nv_cvt_pk_f16_f32 v151,v83,v82\n" \
"v_pk_mul_f32 v[92:93],v[80:81],v[84:85] op_sel:[1,1] op_sel_hi:[0,1] neg_lo:[1,0]\nv_pk_mul_f32 v[94:95],v[82:83],v[84:85] op_sel:[1,1] op_sel_hi:[0,1] neg_lo:[1,0]\nbuffer_load_dword v233,s[8:11],s100 offen lds\nv_pk_mul_f32 v[58:59],v[56:57],v[52:53] op_sel_hi:[1,0]\n" \
"v_pk_mul_f32 v[60:61],v[56:57],v[54:55] op_sel_hi:[1,0]\nv_pk_mul_f32 v[64:65],v[62:63],v[52:53] op_sel_hi:[1,0]\nv_pk_mul_f32 v[66:67],v[62:63],v[54:55] op_sel_hi:[1,0]\nv_pk_mul_f32 v[108:109],v[106:107],v[102:103] op_sel_hi:[1,0]\n" \
"v_pk_mul_f32 v[110:111],v[106:107],v[104:105] op_sel_hi:[1,0]\nv_pk_fma_f32 v[80:81],v[80:81],v[84:85],v[92:93] op_sel_hi:[1,0,1]\nv_pk_fma_f32 v[82:83],v[82:83],v[84:85],v[94:95] op_sel_hi:[1,0,1]\nv_lshlrev_b32 v3,4,v119\n" \
"v_lshrrev_b32 v15,1,v1\nv_lshl_add_u32 v3,v118,6,v3\nv_cvt_pk_f16_f32 v42,v58,-v61\nv_cvt_pk_f16_f32 v46,v59,v60\n" \
"v_cvt_pk_f16_f32 v43,v64,-v67\nv_cvt_pk_f16_f32 v47,v65,v66\nv_cvt_pk_f16_f32 v218,v108,-v109\nv_cvt_pk_f16_f32 v222,v109,v108\n" \
"v_cvt_pk_f16_f32 v219,v110,-v111\nv_cvt_pk_f16_f32 v223,-v111,-v110\nv_cvt_pk_f16_f32 v152,v76,-v77\nv_cvt_pk_f16_f32 v156,v77,v76\n" \
"v_cvt_pk_f16_f32 v153,v78,-v79\nv_cvt_pk_f16_f32 v157,v79,v78\nv_cvt_pk_f16_f32 v154,v80,-v81\nv_cvt_pk_f16_f32 v158,v81,v80\n" \
"v_cvt_pk_f16_f32 v155,v82,-v83\nv_cvt_pk_f16_f32 v159,v83,v82\nv_add_u32 v15,s57,v15\nv_add_u32 v8,s57,v8\n" \
"v_add_u32 v9,s57,v9\nv_add_u32 v10,s57,v10\nv_add_u32 v11,s57,v11\nv_lshrrev_b32 v77,5,v0\n" \
"v_bfe_u32 v113,v0,1,5\nv_and_b32 v115,1,v0\ns_lshl_b32 s63,s3,1\nv_mul_u32_u24 v77,1160,v77\n" \
"v_and_b32 v76,31,v0\nv_and_b32 v114,6,v113\nv_add_u32 v115,s63,v115\nv_xor_b32 v114,v114,v115\n" \
"v_lshlrev_b32 v113,7,v113\nv_lshl_add_u32 v113,v114,4,v113\ns_add_u32 s64,s54,2916352\ns_add_u32 s65,s64,4096\n" \
"v_bfrev_b32 v114,1\ns_lshl1_add_u32 s95,s33,s3\ns_add_u32 s95,s95,74\ns_cmp_lt_u32 s3,2\n" \
"s_cselect_b32 s95,s95,256\ns_and_b32 s100,s95,31\ns_lshl_b32 s100,s100,3\ns_cmp_lt_u32 s95,256\n" \
"s_cselect_b32 s95,s95,0x40000\ns_lshr_b32 s95,s95,5\ns_add_u32 s95,s95,s100\ns_lshl_b32 s95,s95,6\n" \
"s_add_u32 s95,s95,s32\ns_mul_i32 s95,s95,1152\ns_mov_b32 m0,162176\ns_lshl_b32 s101,s33,7\n" \
"s_sub_u32 s101,s101,1280\nv_add_u32 v79,s101,v0\nv_mul_u32_u24 v78,52429,v79\nv_lshrrev_b32 v78,18,v78\n" \
"v_lshlrev_b32 v79,7,v79\ns_mov_b32 s101,6272\nv_mad_u32_u24 v79,v78,s101,v79\ns_mov_b32 s26,3981312\n" \
"s_mov_b32 s27,0x20000\nv_mad_u32_u24 v78,v76,36,v77\nv_cmp_gt_u32 vcc,72,v0\ns_lshl_b32 s67,s32,2\n" \
"s_add_u32 s66,s64,8192\nv_cndmask_b32 v114,v114,v2,vcc\ns_add_u32 s67,s67,s3\ns_lshl_b32 s67,s67,5\n" \
"s_add_u32 s67,s67,s33\ns_lshl_b32 s67,s67,11\ns_add_u32 s67,s67,5308416\ns_lshl2_add_u32 s69,s3,161664\n" \
"s_waitcnt vmcnt(20)\nv_readfirstlane_b32 s28,v126\nv_readfirstlane_b32 s29,v127\ns_mul_i32 s72,s3,2320\n" \
"v_mov_b32 v251,s69\nv_cmp_gt_u32 vcc,32,v0\ns_add_u32 s28,s28,1\ns_addc_u32 s29,s29,0\n" \
"s_lshl_b32 s7,s32,11\nv_cndmask_b32 v74,2.0,0,vcc\ns_mov_b32 s93,128\ns_mov_b32 s73,8192\n" \
"s_mov_b32 s74,12288\ns_mov_b32 s75,17408\ns_mov_b32 s76,21504\ns_mov_b32 s77,16384\n" \
"s_mov_b32 s78,25600\ns_add_u32 s7,s7,40960\ns_mov_b32 s81,0x3d800000\ns_mov_b32 s42,18874368\n" \
"s_mov_b32 s43,0x20000\ns_waitcnt lgkmcnt(0)\ns_mov_b64 s[40:41],s[44:45]\ns_mov_b64 s[44:45],s[46:47]\n" \
"s_mov_b32 s46,2359296\ns_mov_b32 s47,0x20000\ns_waitcnt vmcnt(19)\nv_lshlrev_b32_e64 v48,16,v224\n" \
"v_and_b32 v49,0xffff0000,v224\nv_readlane_b32 s70,v48,0\nv_readlane_b32 s71,v48,32\nv_and_b32 v51,0xffff0000,v225\n" \
"v_mov_b32 v72,s70\nv_mov_b32 v73,s71\nv_lshlrev_b32 v50,16,v225\nv_and_b32 v53,0xffff0000,v226\n" \
"v_permlane32_swap_b32 v72,v73\nv_pk_add_f32 v[48:49],v[48:49],v[72:73] op_sel_hi:[1,0] neg_lo:[0,1] neg_hi:[0,1]\nv_pk_add_f32 v[50:51],v[50:51],v[72:73] op_sel_hi:[1,0] neg_lo:[0,1] neg_hi:[0,1]\nv_pk_mul_f32 v[68:69],v[48:49],v[48:49]\n" \
"v_lshlrev_b32_e64 v52,16,v226\nv_and_b32 v55,0xffff0000,v227\nv_pk_add_f32 v[52:53],v[52:53],v[72:73] op_sel_hi:[1,0] neg_lo:[0,1] neg_hi:[0,1]\nv_pk_add_f32 v[66:67],v[48:49],v[50:51]\n" \
"v_pk_fma_f32 v[68:69],v[50:51],v[50:51],v[68:69]\nv_lshlrev_b32 v54,16,v227\ns_waitcnt vmcnt(18)\nv_and_b32 v57,0xffff0000,v228\n" \
"v_pk_add_f32 v[54:55],v[54:55],v[72:73] op_sel_hi:[1,0] neg_lo:[0,1] neg_hi:[0,1]\nv_pk_add_f32 v[66:67],v[66:67],v[52:53]\nv_pk_fma_f32 v[68:69],v[52:53],v[52:53],v[68:69]\nv_lshlrev_b32_e64 v56,16,v228\n" \
"v_and_b32 v59,0xffff0000,v229\nv_pk_add_f32 v[56:57],v[56:57],v[72:73] op_sel_hi:[1,0] neg_lo:[0,1] neg_hi:[0,1]\nv_pk_add_f32 v[66:67],v[66:67],v[54:55]\nv_pk_fma_f32 v[68:69],v[54:55],v[54:55],v[68:69]\n" \
"v_lshlrev_b32_e64 v58,16,v229\nv_and_b32 v61,0xffff0000,v230\nv_pk_add_f32 v[58:59],v[58:59],v[72:73] op_sel_hi:[1,0] neg_lo:[0,1] neg_hi:[0,1]\nv_pk_add_f32 v[66:67],v[66:67],v[56:57]\n" \
"v_pk_fma_f32 v[68:69],v[56:57],v[56:57],v[68:69]\nv_lshlrev_b32_e64 v60,16,v230\nv_and_b32 v63,0xffff0000,v231\nv_pk_add_f32 v[60:61],v[60:61],v[72:73] op_sel_hi:[1,0] neg_lo:[0,1] neg_hi:[0,1]\n" \
"v_pk_add_f32 v[66:67],v[66:67],v[58:59]\nv_pk_fma_f32 v[68:69],v[58:59],v[58:59],v[68:69]\nv_lshlrev_b32 v62,16,v231\ns_waitcnt vmcnt(17)\n" \
"v_and_b32 v65,0xffff0000,v232\nv_pk_add_f32 v[62:63],v[62:63],v[72:73] op_sel_hi:[1,0] neg_lo:[0,1] neg_hi:[0,1]\nv_pk_add_f32 v[66:67],v[66:67],v[60:61]\nv_pk_fma_f32 v[68:69],v[60:61],v[60:61],v[68:69]\n" \
"v_lshlrev_b32_e64 v64,16,v232\nv_pk_add_f32 v[64:65],v[64:65],v[72:73] op_sel_hi:[1,0] neg_lo:[0,1] neg_hi:[0,1]\nv_pk_add_f32 v[66:67],v[66:67],v[62:63]\nv_pk_fma_f32 v[68:69],v[62:63],v[62:63],v[68:69]\n" \
"v_pk_add_f32 v[66:67],v[66:67],v[64:65]\nv_pk_fma_f32 v[68:69],v[64:65],v[64:65],v[68:69]\nv_add_f32 v66,v66,v67\nv_add_f32 v68,v68,v69\n" \
"s_nop 1\nv_permlane16_swap_b32 v66,v68\nv_add_f32 v66,v66,v68\ns_nop 1\n" \
"v_add_f32_dpp v66,v66,v66 quad_perm:[1,0,3,2] row_mask:0xf bank_mask:0xf bound_ctrl:1\ns_nop 1\nv_add_f32_dpp v66,v66,v66 quad_perm:[2,3,0,1] row_mask:0xf bank_mask:0xf bound_ctrl:1\ns_nop 1\n" \
"v_add_f32_dpp v66,v66,v66 row_half_mirror row_mask:0xf bank_mask:0xf bound_ctrl:1\ns_nop 1\nv_add_f32_dpp v66,v66,v66 row_mirror row_mask:0xf bank_mask:0xf bound_ctrl:1\nv_mul_f32 v70,0x3ae38e39,v66\n" \
"v_mul_f32 v68,0x3ae38e39,v66\ns_nop 1\nv_permlane16_swap_b32 v70,v68\nv_fma_f32 v68,-v70,v70,v68\n" \
"v_max_f32 v68,0,v68\nv_add_f32 v68,0x3727c5ac,v68\nv_rsq_f32 v66,v68\nv_add_f32 v250,v70,v72\n" \
"v_mul_f32 v67,-v70,v66\nv_pk_fma_f32 v[48:49],v[48:49],v[66:67],v[66:67] op_sel:[0,0,1] op_sel_hi:[1,0,1]\nv_pk_fma_f32 v[50:51],v[50:51],v[66:67],v[66:67] op_sel:[0,0,1] op_sel_hi:[1,0,1]\nv_cvt_pk_f16_f32 v80,v48,v49\n" \
"v_cvt_pk_f16_f32 v81,v50,v51\nv_pk_fma_f32 v[52:53],v[52:53],v[66:67],v[66:67] op_sel:[0,0,1] op_sel_hi:[1,0,1]\nv_pk_fma_f32 v[54:55],v[54:55],v[66:67],v[66:67] op_sel:[0,0,1] op_sel_hi:[1,0,1]\nds_write2_b32 v78,v80,v81 offset0:0 offset1:1\n" \
"v_cvt_pk_f16_f32 v82,v52,v53\nv_cvt_pk_f16_f32 v83,v54,v55\nv_pk_fma_f32 v[56:57],v[56:57],v[66:67],v[66:67] op_sel:[0,0,1] op_sel_hi:[1,0,1]\nv_pk_fma_f32 v[58:59],v[58:59],v[66:67],v[66:67] op_sel:[0,0,1] op_sel_hi:[1,0,1]\n" \
"ds_write2_b32 v78,v82,v83 offset0:2 offset1:3\nv_cvt_pk_f16_f32 v84,v56,v57\nv_cvt_pk_f16_f32 v85,v58,v59\nv_pk_fma_f32 v[60:61],v[60:61],v[66:67],v[66:67] op_sel:[0,0,1] op_sel_hi:[1,0,1]\n" \
"v_pk_fma_f32 v[62:63],v[62:63],v[66:67],v[66:67] op_sel:[0,0,1] op_sel_hi:[1,0,1]\nds_write2_b32 v78,v84,v85 offset0:4 offset1:5\nv_cvt_pk_f16_f32 v86,v60,v61\nv_cvt_pk_f16_f32 v87,v62,v63\n" \
"v_pk_fma_f32 v[64:65],v[64:65],v[66:67],v[66:67] op_sel:[0,0,1] op_sel_hi:[1,0,1]\nds_write2_b32 v78,v86,v87 offset0:6 offset1:7\nv_cvt_pk_f16_f32 v88,v64,v65\nds_write_b32 v78,v88 offset:32\n" \
"s_waitcnt vmcnt(11)\ns_barrier\nds_read_u16 v16,v246\nds_read_u16 v17,v246 offset:5216\n" \
"ds_read_u16 v18,v246 offset:578\nds_read_u16 v19,v246 offset:5794\nds_read_u16 v20,v246 offset:1156\nds_read_u16 v21,v246 offset:6372\n" \
"ds_read_u16 v22,v246 offset:1734\nds_read_u16 v23,v246 offset:6950\ns_waitcnt vmcnt(8)\nv_lshrrev_b32 v96,1,v234\n" \
"ds_read_u16 v24,v247\nv_lshrrev_b32 v97,1,v235\nv_lshrrev_b32 v98,1,v236\ns_waitcnt lgkmcnt(7)\n" \
"v_lshl_or_b32 v16,v17,16,v16\nv_lshrrev_b32 v99,1,v237\nds_read_u16 v25,v247 offset:5216\ns_waitcnt vmcnt(7)\n" \
"v_lshrrev_b32 v100,1,v238\nv_lshrrev_b32 v101,1,v239\nv_lshrrev_b32 v102,1,v240\nv_lshrrev_b32 v103,1,v241\n" \
"ds_read_u16 v26,v247 offset:578\nv_add_u32 v96,s72,v96\nv_add_u32 v97,s72,v97\ns_waitcnt lgkmcnt(7)\n" \
"v_lshl_or_b32 v17,v19,16,v18\nv_add_u32 v98,s72,v98\nds_read_u16 v27,v247 offset:5794\nv_add_u32 v99,s72,v99\n" \
"v_add_u32 v100,s72,v100\nv_add_u32 v101,s72,v101\nv_add_u32 v102,s72,v102\nds_read_u16 v28,v247 offset:1156\n" \
"v_add_u32 v103,s72,v103\nv_pk_maximum3_f16 v124,v16,v17,v16\ns_waitcnt lgkmcnt(7)\nv_lshl_or_b32 v18,v21,16,v20\n" \
"v_pk_minimum3_f16 v125,v16,v17,v16\nds_read_u16 v29,v247 offset:6372\nds_read_u16 v30,v247 offset:1734\ns_waitcnt lgkmcnt(7)\n" \
"v_lshl_or_b32 v19,v23,16,v22\nbuffer_store_dwordx4 v[16:19],v2,s[36:39],s64 offen sc1\nds_read_u16 v31,v247 offset:6950\nds_read_u16 v104,v248\n" \
"v_pk_maximum3_f16 v124,v18,v19,v124\nv_pk_minimum3_f16 v125,v18,v19,v125\ns_waitcnt lgkmcnt(7)\nv_lshl_or_b32 v24,v25,16,v24\n" \
"ds_read_u16 v105,v248 offset:5216\nds_read_u16 v106,v248 offset:578\ns_waitcnt lgkmcnt(7)\nv_lshl_or_b32 v25,v27,16,v26\n" \
"v_pk_maximum3_f16 v124,v24,v25,v124\nds_read_u16 v107,v248 offset:5794\nv_pk_minimum3_f16 v125,v24,v25,v125\nds_read_u16 v108,v248 offset:1156\n" \
"s_waitcnt lgkmcnt(7)\nv_lshl_or_b32 v26,v29,16,v28\nds_read_u16 v109,v248 offset:6372\nds_read_u16 v110,v248 offset:1734\n" \
"s_waitcnt lgkmcnt(7)\nv_lshl_or_b32 v27,v31,16,v30\nbuffer_store_dwordx4 v[24:27],v2,s[36:39],s65 offen sc1\nds_read_u16 v111,v248 offset:6950\n" \
"ds_read_b128 v[116:119],v113 offset:20224\nv_pk_maximum3_f16 v124,v26,v27,v124\nv_pk_minimum3_f16 v125,v26,v27,v125\nds_read_b128 v[120:123],v113 offset:24320\n" \
"s_waitcnt lgkmcnt(8)\nv_lshl_or_b32 v104,v105,16,v104\nds_read_b32 v16,v96\nds_read_b32 v24,v96 offset:1160\n" \
"ds_read_b32 v17,v97\nds_read_b32 v25,v97 offset:1160\ns_waitcnt lgkmcnt(10)\nv_lshl_or_b32 v105,v107,16,v106\n" \
"ds_read_b32 v18,v98\nv_pk_maximum3_f16 v124,v104,v105,v124\nds_read_b32 v26,v98 offset:1160\nv_pk_minimum3_f16 v125,v104,v105,v125\n" \
"ds_read_b32 v19,v99\nds_read_b32 v27,v99 offset:1160\nds_read_b32 v20,v100\ns_waitcnt lgkmcnt(13)\n" \
"v_lshl_or_b32 v106,v109,16,v108\nds_read_b32 v28,v100 offset:1160\nds_read_b32 v21,v101\nds_read_b32 v29,v101 offset:1160\n" \
"s_waitcnt lgkmcnt(14)\nv_lshl_or_b32 v107,v111,16,v110\nbuffer_store_dwordx4 v[104:107],v114,s[36:39],s66 offen sc1\ns_waitcnt lgkmcnt(13)\n" \
"buffer_store_dwordx4 v[116:119],v1,s[36:39],s67 offen sc1\ns_waitcnt lgkmcnt(12)\nbuffer_store_dwordx4 v[120:123],v1,s[36:39],s67 offen offset:1024 sc1\nds_read_b32 v22,v102\n" \
"ds_read_b32 v30,v102 offset:1160\nds_read_b32 v23,v103\nds_read_b32 v31,v103 offset:1160\nv_pk_maximum3_f16 v124,v106,v107,v124\n" \
"buffer_load_dword v233,s[8:11],s95 offen lds\nv_and_b32 v124,0x7fff7fff,v124\nv_pk_minimum3_f16 v125,v106,v107,v125\nv_and_b32 v125,0x7fff7fff,v125\n" \
"v_pk_max_u16 v124,v124,v125\nv_lshlrev_b32 v125,16,v124\nv_max_u32 v124,v124,v125\ns_mov_b32 m0,162176\n" \
"s_nop 0\nv_max_u32_dpp v124,v124,v124 quad_perm:[1,0,3,2] row_mask:0xf bank_mask:0xf bound_ctrl:1\ns_nop 1\nv_max_u32_dpp v124,v124,v124 quad_perm:[2,3,0,1] row_mask:0xf bank_mask:0xf bound_ctrl:1\n" \
"s_nop 1\nv_max_u32_dpp v124,v124,v124 row_half_mirror row_mask:0xf bank_mask:0xf bound_ctrl:1\ns_nop 1\nv_max_u32_dpp v124,v124,v124 row_mirror row_mask:0xf bank_mask:0xf bound_ctrl:1\n" \
"s_nop 0\nv_readlane_b32 s68,v124,0\nv_readlane_b32 s31,v124,16\nv_readlane_b32 s96,v124,32\n" \
"v_readlane_b32 s97,v124,48\ns_max_u32 s31,s31,s96\ns_max_u32 s68,s68,s97\ns_max_u32 s68,s68,s31\n" \
"s_mul_i32 s31,s32,576\ns_add_u32 s31,s31,2304\ns_andn2_b32 s31,s31,127\ns_cmp_gt_u32 s3,1\n" \
"s_cselect_b32 s31,s31,2147483648\nbuffer_load_dword v79,s[24:27],s31 offen lds\nv_mov_b32 v124,s68\nds_write_b32 v251,v124 offset:32\n" \
"v_mov_b32 v114,161664\nv_and_b32 v80,15,v0\nv_bfe_u32 v81,v0,4,2\nv_cvt_f32_u32 v80,v80\n" \
"v_cvt_f32_u32 v82,s3\nv_fma_f32 v80,v80,4.0,v82\nv_cvt_f32_u32 v81,v81\nbuffer_load_dwordx4 v[48:51],v2,s[36:39],s73 offen sc1\n" \
"v_mul_f32 v82,0xbc800000,v81\nv_cos_f32 v92,v82\nv_sin_f32 v93,v82\nv_mul_f32 v80,0xbc800000,v80\n" \
"v_mul_f32 v81,4.0,v81\nbuffer_load_dwordx4 v[60:63],v2,s[36:39],s75 offen sc1\ns_mov_b32 s70,0x3f7fb10f\ns_mov_b32 s71,0xbd48fb30\n" \
"v_mul_f32 v81,v81,v80\nv_pk_mul_f32 v[100:101],v[92:93],s[70:71] op_sel:[1,1] op_sel_hi:[0,1] neg_lo:[1,0]\nv_fmamk_f32 v87,v80,0x40000000,v81\nv_pk_fma_f32 v[94:95],v[92:93],s[70:71],v[100:101] op_sel_hi:[1,0,1]\n" \
"v_cos_f32_e64 v86,v87\ns_mov_b32 s70,0x3f7fec43\ns_mov_b32 s71,0xbcc90ab0\nv_fmamk_f32 v89,v80,0x3f800000,v81\n" \
"v_fmamk_f32 v91,v80,0x40400000,v81\nv_pk_mul_f32 v[100:101],v[92:93],s[70:71] op_sel:[1,1] op_sel_hi:[0,1] neg_lo:[1,0]\nbuffer_load_dwordx4 v[52:55],v2,s[36:39],s74 offen sc1\nv_cos_f32 v88,v89\n" \
"v_cos_f32 v90,v91\nv_pk_fma_f32 v[96:97],v[92:93],s[70:71],v[100:101] op_sel_hi:[1,0,1]\nv_cos_f32 v84,v81\nv_sin_f32 v85,v81\n" \
"v_sin_f32 v87,v87\ns_mov_b32 s70,0x3f7f4e6d\ns_mov_b32 s71,0xbd96a905\nv_pk_mul_f32 v[100:101],v[92:93],s[70:71] op_sel:[1,1] op_sel_hi:[0,1] neg_lo:[1,0]\n" \
"v_sin_f32 v89,v89\nv_sin_f32_e64 v91,v91\nv_pk_fma_f32 v[98:99],v[92:93],s[70:71],v[100:101] op_sel_hi:[1,0,1]\nbuffer_load_dwordx4 v[64:67],v2,s[36:39],s76 offen sc1\n" \
"v_cvt_pk_f16_f32 v160,v84,-v85\nv_cvt_pk_f16_f32 v164,v85,v84\nv_cvt_pk_f16_f32 v161,v86,-v87\nv_cvt_pk_f16_f32 v165,v87,v86\n" \
"v_pk_mul_f32 v[100:101],v[84:85],v[92:93] op_sel:[1,1] op_sel_hi:[0,1] neg_lo:[1,0]\nv_pk_mul_f32 v[102:103],v[86:87],v[94:95] op_sel:[1,1] op_sel_hi:[0,1] neg_lo:[1,0]\nv_pk_fma_f32 v[84:85],v[84:85],v[92:93],v[100:101] op_sel_hi:[1,0,1]\nv_pk_fma_f32 v[86:87],v[86:87],v[94:95],v[102:103] op_sel_hi:[1,0,1]\n" \
"v_cvt_pk_f16_f32 v162,v88,-v89\nv_cvt_pk_f16_f32 v166,v89,v88\nv_cvt_pk_f16_f32 v163,v90,-v91\nv_cvt_pk_f16_f32 v167,v91,v90\n" \
"v_pk_mul_f32 v[100:101],v[88:89],v[96:97] op_sel:[1,1] op_sel_hi:[0,1] neg_lo:[1,0]\nv_pk_mul_f32 v[102:103],v[90:91],v[98:99] op_sel:[1,1] op_sel_hi:[0,1] neg_lo:[1,0]\nv_pk_fma_f32 v[88:89],v[88:89],v[96:97],v[100:101] op_sel_hi:[1,0,1]\nv_pk_fma_f32 v[90:91],v[90:91],v[98:99],v[102:103] op_sel_hi:[1,0,1]\n" \
"buffer_load_dwordx4 v[56:59],v1,s[36:39],s77 offen sc1\nv_cvt_pk_f16_f32 v168,v84,-v85\nv_cvt_pk_f16_f32 v172,v85,v84\nv_cvt_pk_f16_f32 v169,v86,-v87\n" \
"v_cvt_pk_f16_f32 v173,v87,v86\nv_pk_mul_f32 v[100:101],v[84:85],v[92:93] op_sel:[1,1] op_sel_hi:[0,1] neg_lo:[1,0]\nv_pk_mul_f32 v[102:103],v[86:87],v[94:95] op_sel:[1,1] op_sel_hi:[0,1] neg_lo:[1,0]\nv_pk_fma_f32 v[84:85],v[84:85],v[92:93],v[100:101] op_sel_hi:[1,0,1]\n" \
"s_waitcnt vmcnt(7) lgkmcnt(0)\ns_barrier\nds_read_b128 v[116:119],v114 offset:32\nv_pk_fma_f32 v[86:87],v[86:87],v[94:95],v[102:103] op_sel_hi:[1,0,1]\n" \
"v_cvt_pk_f16_f32 v170,v88,-v89\nv_cvt_pk_f16_f32 v174,v89,v88\nv_cvt_pk_f16_f32 v171,v90,-v91\nv_cvt_pk_f16_f32 v175,v91,v90\n" \
"v_pk_mul_f32 v[100:101],v[88:89],v[96:97] op_sel:[1,1] op_sel_hi:[0,1] neg_lo:[1,0]\nv_pk_mul_f32 v[102:103],v[90:91],v[98:99] op_sel:[1,1] op_sel_hi:[0,1] neg_lo:[1,0]\nv_pk_fma_f32 v[88:89],v[88:89],v[96:97],v[100:101] op_sel_hi:[1,0,1]\nv_pk_fma_f32 v[90:91],v[90:91],v[98:99],v[102:103] op_sel_hi:[1,0,1]\n" \
"buffer_load_dwordx4 v[68:71],v1,s[36:39],s78 offen sc1\nv_cvt_pk_f16_f32 v176,v84,-v85\nv_cvt_pk_f16_f32 v180,v85,v84\nv_cvt_pk_f16_f32 v177,v86,-v87\n" \
"v_cvt_pk_f16_f32 v181,v87,v86\nv_pk_mul_f32 v[100:101],v[84:85],v[92:93] op_sel:[1,1] op_sel_hi:[0,1] neg_lo:[1,0]\nv_pk_mul_f32 v[102:103],v[86:87],v[94:95] op_sel:[1,1] op_sel_hi:[0,1] neg_lo:[1,0]\ns_mov_b32 s79,0xffffff\n" \
"s_lshl3_add_u32 s80,s2,4294615040\nv_pk_fma_f32 v[84:85],v[84:85],v[92:93],v[100:101] op_sel_hi:[1,0,1]\nv_pk_fma_f32 v[86:87],v[86:87],v[94:95],v[102:103] op_sel_hi:[1,0,1]\ns_waitcnt lgkmcnt(0)\n" \
"v_max3_u32 v116,v116,v117,v118\nv_max_u32 v116,v116,v119\nv_and_b32 v119,0xf8,v0\nv_cmp_ne_u32 vcc,192,v119\n" \
"v_lshlrev_b32 v116,1,v116\nv_mov_b32 v117,s29\nv_lshlrev_b32 v118,11,v0\nv_bfi_b32 v117,s79,v117,v116\n" \
"v_add_u32 v118,s80,v118\nv_bfrev_b32_e64 v119,1\nv_cvt_pk_f16_f32 v178,v88,-v89\nv_cvt_pk_f16_f32 v182,v89,v88\n" \
"v_cvt_pk_f16_f32 v179,v90,-v91\nv_cvt_pk_f16_f32 v183,v91,v90\nv_pk_mul_f32 v[100:101],v[88:89],v[96:97] op_sel:[1,1] op_sel_hi:[0,1] neg_lo:[1,0]\nv_pk_mul_f32 v[102:103],v[90:91],v[98:99] op_sel:[1,1] op_sel_hi:[0,1] neg_lo:[1,0]\n" \
"v_cndmask_b32 v118,v118,v119,vcc\nv_mov_b32 v116,s28\nv_pk_fma_f32 v[88:89],v[88:89],v[96:97],v[100:101] op_sel_hi:[1,0,1]\nv_pk_fma_f32 v[90:91],v[90:91],v[98:99],v[102:103] op_sel_hi:[1,0,1]\n" \
"buffer_store_dwordx2 v[116:117],v118,s[36:39],0 offen sc1\nv_add_u32 v112,28416,v1\nv_cvt_pk_f16_f32 v184,v84,-v85\nv_cvt_pk_f16_f32 v188,v85,v84\n" \
"v_cvt_pk_f16_f32 v185,v86,-v87\nv_cvt_pk_f16_f32 v189,v87,v86\nv_cvt_pk_f16_f32 v186,v88,-v89\nv_cvt_pk_f16_f32 v190,v89,v88\n" \
"v_cvt_pk_f16_f32 v187,v90,-v91\nv_cvt_pk_f16_f32 v191,v91,v90\nds_read_b128 v[192:195],v112\nds_read_b128 v[196:199],v112 offset:1024\n" \
"ds_read_b128 v[200:203],v112 offset:2048\nds_read_b128 v[204:207],v112 offset:3072\nds_read_b128 v[208:211],v112 offset:4096\nds_read_b128 v[212:215],v112 offset:5120\n" \
"ds_read_b128 v[32:35],v112 offset:6144\nds_read_b128 v[36:39],v112 offset:7168\ns_waitcnt vmcnt(6)\nv_cmp_ne_u64 vcc,s[28:29],v[48:49]\n" \
"s_waitcnt vmcnt(4)\nv_cmp_ne_u64 s[84:85],s[28:29],v[52:53]\ns_waitcnt vmcnt(2)\nv_cmp_ne_u64 s[82:83],s[28:29],v[56:57]\n" \
"s_or_b64 vcc,vcc,s[84:85]\nv_cmp_ne_u64 s[84:85],s[28:29],v[60:61]\ns_or_b64 vcc,vcc,s[82:83]\nv_cmp_ne_u64 s[82:83],s[28:29],v[64:65]\n" \
"s_or_b64 vcc,vcc,s[84:85]\ns_waitcnt vmcnt(1)\nv_cmp_ne_u64 s[84:85],s[28:29],v[68:69]\ns_or_b64 vcc,vcc,s[82:83]\n" \
"s_or_b64 vcc,vcc,s[84:85]\ns_cbranch_vccnz .LsmC_%=\n.LbmC_%=:\nv_pk_add_f32 v[50:51],v[50:51],v[62:63]\n" \
"v_pk_add_f32 v[54:55],v[54:55],v[66:67]\nv_pk_mul_f32 v[62:63],v[50:51],2.0 op_sel_hi:[1,0]\nv_pk_add_f32 v[58:59],v[58:59],v[70:71]\nv_max3_f32 v112,|v62|,|v63|,0\n" \
"v_pk_fma_f32 v[66:67],v[54:55],2.0,v[74:75] op_sel_hi:[1,0,0]\nv_max3_f32 v112,|v66|,|v67|,v112\nv_pk_fma_f32 v[70:71],v[58:59],2.0,2.0 op_sel_hi:[1,0,0]\nv_max3_f32 v112,|v70|,|v71|,v112\n" \
"v_cmp_lt_f32 s[82:83],s81,v112\nv_cmp_ngt_f32 vcc,0x41000000,v112\ns_cmp_lg_u64 s[82:83],0\ns_cselect_b32 s82,2,0\n" \
"v_cvt_pk_f16_f32 v62,v62,v63\ns_cmp_lg_u64 vcc,0\ns_addc_u32 s82,s82,0\nds_write_b32 v252,v62 offset:62848\n" \
"v_cvt_pk_f16_f32 v66,v66,v67\nds_write_b32 v253,v66 offset:62848\nv_cvt_pk_f16_f32 v70,v70,v71\nv_mov_b32 v113,s82\n" \
"ds_write_b32 v254,v70 offset:62848\nds_write_b32 v251,v113\ns_waitcnt lgkmcnt(0)\ns_barrier\n" \
"ds_read_b128 v[124:127],v114\nds_read_b64 v[96:97],v234 offset:62848\nds_read_b64 v[98:99],v235 offset:62848\nds_read_b64 v[100:101],v236 offset:62848\n" \
"ds_read_b64 v[102:103],v237 offset:62848\nds_read_b64 v[104:105],v238 offset:62848\nds_read_b64 v[106:107],v239 offset:62848\nds_read_b64 v[108:109],v240 offset:62848\n" \
"ds_read_b64 v[110:111],v241 offset:62848\ns_waitcnt lgkmcnt(8)\nv_or3_b32 v124,v124,v125,v126\nv_or_b32 v124,v124,v127\n" \
"v_cmp_ne_u32 vcc,2,v124\ns_nop 0\ns_cbranch_vccnz .LsxC_%=\n.LbxC_%=:\n" \
"s_waitcnt lgkmcnt(7)\nv_pk_fma_f16 v80,v16,v96,v97\ns_waitcnt lgkmcnt(6)\nv_pk_fma_f16 v81,v17,v98,v99\n" \
"s_waitcnt lgkmcnt(5)\nv_pk_fma_f16 v82,v18,v100,v101\ns_waitcnt lgkmcnt(4)\nv_pk_fma_f16 v83,v19,v102,v103\n" \
"v_pk_fma_f16 v88,v24,v96,v97\ns_waitcnt lgkmcnt(3)\nv_pk_fma_f16 v84,v20,v104,v105\ns_waitcnt lgkmcnt(2)\n" \
"v_pk_fma_f16 v85,v21,v106,v107\nv_pk_fma_f16 v89,v25,v98,v99\ns_waitcnt lgkmcnt(1)\nv_pk_fma_f16 v86,v22,v108,v109\n" \
"v_pk_fma_f16 v90,v26,v100,v101\ns_waitcnt lgkmcnt(0)\nv_pk_fma_f16 v87,v23,v110,v111\nv_pk_fma_f16 v91,v27,v102,v103\n" \
"v_pk_fma_f16 v92,v28,v104,v105\nv_pk_fma_f16 v93,v29,v106,v107\nv_pk_fma_f16 v94,v30,v108,v109\ns_add_u32 s96,s92,0\n" \
"s_add_u32 m0,s57,0\ns_add_u32 s97,s92,9437184\nbuffer_load_dwordx4 v4,s[8:11],s96 offen lds\nv_pk_fma_f16 v95,v31,v110,v111\n" \
"v_mfma_f32_16x16x32_f16 v[48:51],v[80:83],v[192:195],0\nv_and_b32 v112,15,v0\nv_bfrev_b32 v113,1\nv_mfma_f32_16x16x32_f16 v[52:55],v[80:83],v[196:199],0\n" \
"v_cmp_eq_u32 vcc,0,v112\ns_add_u32 s73,s35,s88\nbuffer_load_dwordx4 v5,s[8:11],s96 offen offset:1024 lds\nv_mfma_f32_16x16x32_f16 v[56:59],v[84:87],v[32:35],0\n" \
"v_cndmask_b32 v113,v113,v3,vcc\nv_cmp_gt_u32 vcc,64,v0\nv_mfma_f32_16x16x32_f16 v[60:63],v[84:87],v[36:39],0\ns_add_u32 s75,s73,8192\n" \
"v_cndmask_b32 v113,v113,v3,vcc\nv_mfma_f32_16x16x32_f16 v[64:67],v[88:91],v[192:195],0\nbuffer_load_dwordx4 v6,s[8:11],s96 offen offset:2048 lds\nv_mfma_f32_16x16x32_f16 v[68:71],v[88:91],v[196:199],0\n" \
"v_pk_add_f32 v[56:57],v[48:49],v[56:57] neg_lo:[0,1] neg_hi:[0,1]\nv_sub_f32 v58,v50,v58\nv_mfma_f32_16x16x32_f16 v[72:75],v[92:95],v[32:35],0\nv_pk_add_f32 v[60:61],v[52:53],v[60:61] neg_lo:[0,1] neg_hi:[0,1]\n" \
"v_sub_f32 v62,v54,v62\nv_mfma_f32_16x16x32_f16 v[76:79],v[92:95],v[36:39],0\nbuffer_load_dwordx4 v7,s[8:11],s96 offen offset:3072 lds\nv_pk_fma_f32 v[48:49],v[48:49],2.0,v[56:57] op_sel_hi:[1,0,1] neg_lo:[0,0,1] neg_hi:[0,0,1]\n" \
"v_fma_f32 v50,v50,2.0,-v58\nv_pk_fma_f32 v[52:53],v[52:53],2.0,v[60:61] op_sel_hi:[1,0,1] neg_lo:[0,0,1] neg_hi:[0,0,1]\nv_fma_f32 v54,v54,2.0,-v62\nv_pk_add_f32 v[72:73],v[64:65],v[72:73] neg_lo:[0,1] neg_hi:[0,1]\n" \
"v_sub_f32_e64 v74,v66,v74\nv_pk_fma_f32 v[64:65],v[64:65],2.0,v[72:73] op_sel_hi:[1,0,1] neg_lo:[0,0,1] neg_hi:[0,0,1]\nv_pk_add_f32 v[76:77],v[68:69],v[76:77] neg_lo:[0,1] neg_hi:[0,1]\ns_add_u32 m0,s57,4096\n" \
"s_add_u32 s95,s92,73728\nbuffer_load_dwordx4 v4,s[8:11],s97 offen lds\nv_sub_f32_e64 v78,v70,v78\nv_fma_f32 v66,v66,2.0,-v74\n" \
"v_pk_fma_f32 v[68:69],v[68:69],2.0,v[76:77] op_sel_hi:[1,0,1] neg_lo:[0,0,1] neg_hi:[0,0,1]\nv_fma_f32 v70,v70,2.0,-v78\nv_cvt_pk_f16_f32 v16,v48,v52\nv_cvt_pk_f16_f32 v17,v49,v53\n" \
"v_cvt_pk_f16_f32 v18,v50,v54\nv_cvt_pk_f16_f32 v24,v64,v68\nbuffer_load_dwordx4 v5,s[8:11],s97 offen offset:1024 lds\nv_cvt_pk_f16_f32 v25,v65,v69\n" \
"v_cvt_pk_f16_f32 v26,v66,v70\nv_mfma_f32_16x16x32_f16 v[48:51],v[200:203],v[16:19],0\nv_cvt_pk_f16_f32 v20,v56,v60\nv_cvt_pk_f16_f32 v21,v57,v61\n" \
"v_mfma_f32_16x16x32_f16 v[52:55],v[204:207],v[16:19],0\nbuffer_load_dwordx4 v6,s[8:11],s97 offen offset:2048 lds\nv_mfma_f32_16x16x32_f16 v[64:67],v[200:203],v[24:27],0\nv_cvt_pk_f16_f32 v22,v58,v62\n" \
"v_cvt_pk_f16_f32 v28,v72,v76\nv_mfma_f32_16x16x32_f16 v[68:71],v[204:207],v[24:27],0\nv_cvt_pk_f16_f32 v29,v73,v77\nv_cvt_pk_f16_f32 v30,v74,v78\n" \
"buffer_load_dwordx4 v7,s[8:11],s97 offen offset:3072 lds\nv_mfma_f32_16x16x32_f16 v[56:59],v[200:203],v[20:23],0\nv_cvt_pk_f16_f32 v80,v48,v52\nv_cvt_pk_f16_f32 v82,v49,v53\n" \
"v_mfma_f32_16x16x32_f16 v[60:63],v[204:207],v[20:23],0\nv_cvt_pk_f16_f32 v84,v50,v54\nv_cvt_pk_f16_f32 v81,v64,v68\ns_add_u32 m0,s57,8192\n" \
"s_add_u32 s100,s92,9510912\nbuffer_load_dwordx4 v4,s[8:11],s95 offen lds\nv_mfma_f32_16x16x32_f16 v[72:75],v[200:203],v[28:31],0\nds_write_b64 v255,v[80:81]\n" \
"v_mfma_f32_16x16x32_f16 v[76:79],v[204:207],v[28:31],0\nv_cvt_pk_f16_f32 v83,v65,v69\nds_write_b64 v255,v[82:83] offset:512\nbuffer_load_dwordx4 v5,s[8:11],s95 offen offset:1024 lds\n" \
"v_cvt_pk_f16_f32 v85,v66,v70\nds_write_b64 v255,v[84:85] offset:1024\nv_cvt_pk_f16_f32 v86,v56,v60\nv_cvt_pk_f16_f32 v88,v57,v61\n" \
"v_cvt_pk_f16_f32 v90,v58,v62\nv_cvt_pk_f16_f32 v87,v72,v76\nbuffer_load_dwordx4 v6,s[8:11],s95 offen offset:2048 lds\nds_write_b64 v255,v[86:87] offset:256\n" \
"v_cvt_pk_f16_f32 v89,v73,v77\nds_write_b64 v255,v[88:89] offset:768\nv_cvt_pk_f16_f32 v91,v74,v78\nds_write_b64 v255,v[90:91] offset:1280\n" \
"buffer_load_dwordx4 v7,s[8:11],s95 offen offset:3072 lds\ns_cmp_lt_u32 s3,2\ns_cselect_b32 s75,s75,2147483648\ns_add_u32 s74,s73,4096\n" \
"s_add_u32 m0,s57,12288\ns_add_u32 s101,s92,147456\ns_mul_i32 s76,s32,18\ns_add_u32 s76,s76,s33\n" \
"s_sub_u32 s76,s76,14\ns_cmp_lt_u32 s33,14\ns_cselect_b32 s76,144,s76\nbuffer_load_dwordx4 v4,s[8:11],s100 offen lds\n" \
"s_waitcnt lgkmcnt(0)\ns_barrier\nds_read_b128 v[16:19],v243\nds_read_b128 v[20:23],v244\n" \
"ds_read_b128 v[24:27],v245\nv_lshrrev_b32 v124,2,v0\nv_mul_u32_u24 v124,9344,v124\nv_and_b32 v125,48,v2\n" \
"buffer_load_dwordx4 v5,s[8:11],s100 offen offset:1024 lds\nv_add_u32 v124,v124,v125\nv_xor_b32 v125,32,v124\ns_lshl_b32 s77,s76,6\n" \
"s_add_u32 s66,s77,2916352\ns_add_u32 s67,s77,3514368\ns_add_u32 s68,s77,4112384\ns_add_u32 s69,s77,4710400\n" \
"buffer_load_dwordx4 v6,s[8:11],s100 offen offset:2048 lds\ns_waitcnt lgkmcnt(2)\nv_mfma_f32_16x16x32_f16 v[48:51],v[40:43],v[16:19],0\nv_mfma_f32_16x16x32_f16 v[52:55],v[44:47],v[16:19],0\n" \
"s_waitcnt lgkmcnt(1)\nv_mfma_f32_16x16x32_f16 v[56:59],v[40:43],v[20:23],0\nbuffer_load_dwordx4 v7,s[8:11],s100 offen offset:3072 lds\nbuffer_load_dwordx4 v[104:107],v1,s[36:39],s7 offen sc1\n" \
"buffer_load_dwordx4 v[108:111],v1,s[36:39],s7 offen offset:1024 sc1\ns_add_u32 m0,s57,16384\nv_mfma_f32_16x16x32_f16 v[60:63],v[44:47],v[20:23],0\ns_waitcnt lgkmcnt(0)\n" \
"v_mfma_f32_16x16x32_f16 v[64:67],v[40:43],v[24:27],0\nv_cvt_pk_f16_f32 v80,v48,v52\nv_mfma_f32_16x16x32_f16 v[68:71],v[44:47],v[24:27],0\nv_cvt_pk_f16_f32 v81,v49,v53\n" \
"v_cvt_pk_f16_f32 v82,v50,v54\nbuffer_load_dwordx4 v4,s[8:11],s101 offen lds\nv_cvt_pk_f16_f32 v83,v51,v55\nv_and_b32 v80,-2,v80\n" \
"v_cvt_pk_f16_f32 v84,v56,v60\nv_cvt_pk_f16_f32 v85,v57,v61\nv_cvt_pk_f16_f32 v86,v58,v62\nv_cvt_pk_f16_f32 v88,v64,v68\n" \
"v_cvt_pk_f16_f32 v87,v59,v63\nv_cvt_pk_f16_f32 v89,v65,v69\nbuffer_load_dwordx4 v5,s[8:11],s101 offen offset:1024 lds\nv_cvt_pk_f16_f32 v90,v66,v70\n" \
"v_cvt_pk_f16_f32 v91,v67,v71\nv_and_b32 v84,-2,v84\nv_and_b32 v88,-2,v88\nbuffer_load_dwordx4 v6,s[8:11],s101 offen offset:2048 lds\n" \
"s_waitcnt vmcnt(4)\nv_xor_b32 v116,s29,v105\nv_xor_b32 v117,s29,v107\nbuffer_load_dwordx4 v7,s[8:11],s101 offen offset:3072 lds\n" \
"s_waitcnt vmcnt(4)\nv_xor_b32 v118,s29,v109\nv_or3_b32 v116,v116,v117,v118\nv_xor_b32 v119,s29,v111\n" \
"v_xor_b32 v117,s28,v104\nv_or_b32 v116,v116,v119\nv_lshl_or_b32 v116,v116,8,v117\nv_xor_b32 v118,s28,v106\n" \
"v_xor_b32 v119,s28,v108\nv_or3_b32 v116,v116,v118,v119\nv_xor_b32 v117,s28,v110\nv_or_b32 v116,v116,v117\n" \
"v_cmp_ne_u32 vcc,0,v116\ns_nop 0\ns_cbranch_vccnz .Lsw_%=\nbuffer_store_dwordx4 v[80:83],v3,s[36:39],s73 offen sc1\n" \
"buffer_store_dwordx4 v[84:87],v3,s[36:39],s74 offen sc1\nbuffer_store_dwordx4 v[88:91],v113,s[36:39],s75 offen sc1\ns_add_u32 m0,s88,16384\ns_nop 0\n" \
"buffer_load_dwordx4 v124,s[36:39],s66 offen sc1 lds\ns_add_u32 m0,s88,20480\ns_nop 0\nbuffer_load_dwordx4 v125,s[36:39],s67 offen sc1 lds\n" \
"s_add_u32 m0,s88,24576\ns_nop 0\nbuffer_load_dwordx4 v124,s[36:39],s68 offen sc1 lds\ns_add_u32 m0,s88,28672\n" \
"s_nop 0\nbuffer_load_dwordx4 v125,s[36:39],s69 offen sc1 lds\n.Lbw_%=:\ns_cmp_lt_u32 s3,2\n" \
"s_cselect_b32 s18,s75,s73\ns_mov_b32 s12,s88\ns_mov_b32 s13,s89\ns_mov_b32 s14,s87\n" \
"s_mov_b32 s16,s73\ns_mov_b32 s17,s74\nv_bfe_u32 v86,v0,4,2\nv_and_b32 v88,1,v86\n" \
"v_bfe_u32 v85,v0,3,1\nv_and_b32 v84,7,v0\nv_lshlrev_b32 v88,3,v88\nv_lshl_add_u32 v87,v86,6,v85\n" \
"s_lshl_b32 s84,s3,2\nv_xor_b32 v89,v84,v88\nv_add_u32 v87,s84,v87\nv_lshlrev_b32_e64 v89,2,v89\n" \
"v_lshl_add_u32 v80,v87,6,v89\nv_and_b32 v87,0xf0,v1\nv_add_u32 v80,16384,v80\nv_lshl_add_u32 v246,v86,12,v87\n" \
"v_sub_u32 v82,46076,v80\nv_lshl_add_u32 v87,v84,1,v85\nv_xor_b32 v82,32,v82\nv_lshlrev_b32 v87,4,v87\n" \
"v_add_u32 v81,128,v80\nv_subrev_u32 v83,128,v82\nv_lshl_add_u32 v87,v85,10,v87\nv_lshl_add_u32 v87,v86,8,v87\n" \
"s_lshl_b32 s86,s3,12\nv_add_u32 v252,s86,v87\nv_lshlrev_b32 v87,3,v85\nv_lshl_add_u32 v87,v84,4,v87\n" \
"s_lshr_b32 s84,s76,5\nv_lshl_add_u32 v87,v86,12,v87\ns_lshl_b32 s85,s3,8\ns_add_u32 s84,s84,1\n" \
"v_add_u32 v246,s85,v246\nv_add_u32 v248,s85,v87\ns_cmp_lt_u32 s76,128\ns_cselect_b32 s84,s84,0\n" \
"v_max3_u32 v126,v105,v107,v109\ns_and_b32 s85,s76,31\nv_max_u32 v126,v126,v111\ns_lshl_b32 s84,s84,6\n" \
"s_mul_i32 s85,s85,225\nv_max_u32_dpp v126,v126,v126 quad_perm:[1,0,3,2] row_mask:0xf bank_mask:0xf bound_ctrl:1\ns_add_u32 s84,s84,s85\ns_mul_i32 s85,s84,58255\n" \
"v_max_u32_dpp v126,v126,v126 quad_perm:[2,3,0,1] row_mask:0xf bank_mask:0xf bound_ctrl:1\ns_lshr_b32 s85,s85,24\ns_mul_i32 s85,s85,288\nv_max_u32_dpp v126,v126,v126 row_half_mirror row_mask:0xf bank_mask:0xf bound_ctrl:1\n" \
"s_sub_u32 s84,s84,s85\nv_cvt_f32_u32 v92,s84\nv_max_u32_dpp v126,v126,v126 row_mirror row_mask:0xf bank_mask:0xf bound_ctrl:1\nv_mul_f32 v92,0x3ae38e39,v92\n" \
"v_readlane_b32 s94,v126,0\nv_readlane_b32 s31,v126,16\nv_readlane_b32 s96,v126,32\nv_readlane_b32 s97,v126,48\n" \
"s_max_u32 s31,s31,s96\ns_max_u32 s94,s94,s97\ns_max_u32 s94,s94,s31\nv_cos_f32 v90,v92\n" \
"s_lshr_b32 s94,s94,24\nv_bfe_u32 v87,v0,1,3\nv_and_b32 v88,1,v0\ns_max_u32 s94,s94,64\n" \
"v_lshl_add_u32 v87,v88,3,v87\ns_min_u32 s94,s94,250\nv_lshl_add_u32 v87,v86,4,v87\ns_sub_u32 s85,251,s94\n" \
"v_sin_f32 v92,v92\nv_lshlrev_b32 v87,4,v87\ns_cmp_eq_u32 s76,128\ns_subb_u32 s85,s85,0\n" \
"v_bfe_u32 v120,v0,4,2\nv_add_u32 v254,s86,v87\ns_mul_i32 s80,s3,37376\nv_mul_u32_u24 v120,598016,v120\n" \
"v_and_b32 v121,15,v0\nv_and_b32 v87,15,v0\ns_lshl_b32 s85,s85,23\ns_add_u32 s80,s80,s77\n" \
"v_lshl_add_u32 v120,v121,2,v120\nv_mul_u32_u24 v87,149504,v87\nv_fma_f32 v94,v92,s85,s85\ns_mov_b32 s79,0x34003400\n" \
"s_add_u32 s80,s80,524288\nv_add_u32 v121,149504,v120\nv_add_u32 v122,299008,v120\nv_add_u32 v123,448512,v120\n" \
"v_lshl_add_u32 v255,v86,4,v87\nv_pk_mul_f16 v112,v192,s79\nv_pk_mul_f16 v113,v193,s79\nv_pk_mul_f16 v114,v194,s79\n" \
"v_pk_mul_f16 v115,v195,s79\nv_pk_mul_f16 v116,v196,s79\nv_pk_mul_f16 v117,v197,s79\nv_pk_mul_f16 v118,v198,s79\n" \
"v_pk_mul_f16 v119,v199,s79\nv_fma_f32 v92,-v92,s85,s85\ns_add_u32 s81,s80,9344\ns_add_u32 s82,s80,18688\n" \
"s_add_u32 s83,s80,28032\nv_xor_b32 v247,16,v246\nv_xor_b32 v253,16,v252\nv_mul_f32 v90,s85,v90\n" \
"ds_read_b64_tr_b16 a[0:1],v8\nds_read_b64_tr_b16 a[4:5],v9\nds_read_b64_tr_b16 a[8:9],v10\nds_read_b64_tr_b16 a[12:13],v11\n" \
"ds_read_b64_tr_b16 a[16:17],v8 offset:2048\nds_read_b64_tr_b16 a[20:21],v9 offset:2048\nds_read_b64_tr_b16 a[24:25],v10 offset:2048\nds_read_b64_tr_b16 a[28:29],v11 offset:2048\n" \
"ds_read_b64_tr_b16 a[2:3],v8 offset:4096\nds_read_b64_tr_b16 a[6:7],v9 offset:4096\nds_read_b64_tr_b16 a[10:11],v10 offset:4096\nds_read_b64_tr_b16 a[14:15],v11 offset:4096\n" \
"ds_read_b64_tr_b16 a[18:19],v8 offset:6144\nds_read_b64_tr_b16 a[22:23],v9 offset:6144\nds_read_b64_tr_b16 a[26:27],v10 offset:6144\nds_read_b64_tr_b16 a[30:31],v11 offset:6144\n" \
"ds_read_b64_tr_b16 a[32:33],v8 offset:8192\nds_read_b64_tr_b16 a[36:37],v9 offset:8192\nds_read_b64_tr_b16 a[40:41],v10 offset:8192\nds_read_b64_tr_b16 a[44:45],v11 offset:8192\n" \
"ds_read_b64_tr_b16 a[48:49],v8 offset:10240\nds_read_b64_tr_b16 a[52:53],v9 offset:10240\nds_read_b64_tr_b16 a[56:57],v10 offset:10240\nds_read_b64_tr_b16 a[60:61],v11 offset:10240\n" \
"ds_read_b64_tr_b16 a[34:35],v8 offset:12288\nds_read_b64_tr_b16 a[38:39],v9 offset:12288\nds_read_b64_tr_b16 a[42:43],v10 offset:12288\nds_read_b64_tr_b16 a[46:47],v11 offset:12288\n" \
"ds_read_b64_tr_b16 a[50:51],v8 offset:14336\nds_read_b64_tr_b16 a[54:55],v9 offset:14336\nds_read_b64_tr_b16 a[58:59],v10 offset:14336\nds_read_b64_tr_b16 a[62:63],v11 offset:14336\n" \
"s_waitcnt vmcnt(0)\ns_barrier\nds_read2st64_b32 v[64:65],v80 offset0:0 offset1:4\nds_read2st64_b32 v[72:73],v82 offset0:12 offset1:8\n" \
"ds_read2st64_b32 v[66:67],v80 offset0:8 offset1:12\nds_read2st64_b32 v[74:75],v82 offset0:4 offset1:0\nds_read2st64_b32 v[68:69],v81 offset0:0 offset1:4\nds_read2st64_b32 v[76:77],v83 offset0:12 offset1:8\n" \
"ds_read2st64_b32 v[70:71],v81 offset0:8 offset1:12\nds_read2st64_b32 v[78:79],v83 offset0:4 offset1:0\ns_waitcnt lgkmcnt(7)\nv_and_b32 v20,0xffff0000,v64\n" \
"v_and_b32 v21,0xffff0000,v65\nv_lshlrev_b32 v16,16,v64\nv_lshlrev_b32 v17,16,v65\ns_waitcnt lgkmcnt(6)\n" \
"v_and_b32 v36,0xffff0000,v72\nv_and_b32 v37,0xffff0000,v73\nv_lshlrev_b32 v32,16,v72\ns_waitcnt lgkmcnt(5)\n" \
"v_and_b32 v22,0xffff0000,v66\nv_and_b32 v23,0xffff0000,v67\nv_lshlrev_b32 v33,16,v73\nv_lshlrev_b32 v18,16,v66\n" \
"s_waitcnt lgkmcnt(4)\nv_and_b32 v38,0xffff0000,v74\nv_and_b32 v39,0xffff0000,v75\nv_lshlrev_b32 v34,16,v74\n" \
"s_waitcnt lgkmcnt(3)\nv_and_b32 v44,0xffff0000,v68\nv_and_b32 v45,0xffff0000,v69\nv_lshlrev_b32 v19,16,v67\n" \
"s_waitcnt lgkmcnt(2)\nv_and_b32 v60,0xffff0000,v76\nv_and_b32 v61,0xffff0000,v77\nv_lshlrev_b32 v35,16,v75\n" \
"v_lshlrev_b32 v40,16,v68\ns_waitcnt lgkmcnt(1)\nv_and_b32 v46,0xffff0000,v70\nv_and_b32 v47,0xffff0000,v71\n" \
"v_lshlrev_b32 v56,16,v76\ns_waitcnt lgkmcnt(0)\nv_and_b32 v62,0xffff0000,v78\nv_and_b32 v63,0xffff0000,v79\n" \
"v_lshlrev_b32 v41,16,v69\nv_lshlrev_b32 v57,16,v77\nv_lshlrev_b32 v42,16,v70\nv_lshlrev_b32 v58,16,v78\n" \
"v_lshlrev_b32 v43,16,v71\nv_lshlrev_b32 v59,16,v79\ns_cmp_eq_u32 s76,128\ns_cbranch_scc1 .Lsz_%=\n" \
".Lbz_%=:\nv_pk_mul_f32 v[24:25],v[16:17],v[94:95] op_sel_hi:[1,0]\nv_pk_mul_f32 v[28:29],v[20:21],v[94:95] op_sel_hi:[1,0]\nv_pk_add_f32 v[64:65],v[20:21],v[36:37]\n" \
"v_pk_add_f32 v[66:67],v[16:17],v[32:33] neg_lo:[0,1] neg_hi:[0,1]\nv_pk_mul_f32 v[26:27],v[18:19],v[94:95] op_sel_hi:[1,0]\nv_pk_mul_f32 v[30:31],v[22:23],v[94:95] op_sel_hi:[1,0]\nv_pk_add_f32 v[68:69],v[22:23],v[38:39]\n" \
"v_pk_add_f32 v[70:71],v[18:19],v[34:35] neg_lo:[0,1] neg_hi:[0,1]\nv_pk_mul_f32 v[48:49],v[40:41],v[94:95] op_sel_hi:[1,0]\nv_pk_mul_f32 v[52:53],v[44:45],v[94:95] op_sel_hi:[1,0]\nv_pk_add_f32 v[72:73],v[44:45],v[60:61]\n" \
"v_pk_add_f32 v[74:75],v[40:41],v[56:57] neg_lo:[0,1] neg_hi:[0,1]\nv_pk_mul_f32 v[50:51],v[42:43],v[94:95] op_sel_hi:[1,0]\nv_pk_mul_f32 v[54:55],v[46:47],v[94:95] op_sel_hi:[1,0]\nv_pk_add_f32 v[76:77],v[46:47],v[62:63]\n" \
"v_pk_add_f32 v[78:79],v[42:43],v[58:59] neg_lo:[0,1] neg_hi:[0,1]\nv_pk_mul_f32 v[16:17],v[16:17],v[92:93] op_sel_hi:[1,0]\nv_pk_mul_f32 v[20:21],v[20:21],v[92:93] op_sel_hi:[1,0]\nv_pk_mul_f32 v[18:19],v[18:19],v[92:93] op_sel_hi:[1,0]\n" \
"v_pk_mul_f32 v[22:23],v[22:23],v[92:93] op_sel_hi:[1,0]\nbuffer_load_dword v96,v120,s[36:39],s80 offen sc1\nbuffer_load_dword v97,v121,s[36:39],s80 offen sc1\nbuffer_load_dword v98,v122,s[36:39],s80 offen sc1\n" \
"buffer_load_dword v99,v123,s[36:39],s80 offen sc1\nbuffer_load_dword v100,v120,s[36:39],s81 offen sc1\nbuffer_load_dword v101,v121,s[36:39],s81 offen sc1\nbuffer_load_dword v102,v122,s[36:39],s81 offen sc1\n" \
"buffer_load_dword v103,v123,s[36:39],s81 offen sc1\nbuffer_load_dword v104,v120,s[36:39],s82 offen sc1\nbuffer_load_dword v105,v121,s[36:39],s82 offen sc1\nbuffer_load_dword v106,v122,s[36:39],s82 offen sc1\n" \
"buffer_load_dword v107,v123,s[36:39],s82 offen sc1\nbuffer_load_dword v108,v120,s[36:39],s83 offen sc1\nbuffer_load_dword v109,v121,s[36:39],s83 offen sc1\nbuffer_load_dword v110,v122,s[36:39],s83 offen sc1\n" \
"buffer_load_dword v111,v123,s[36:39],s83 offen sc1\nv_pk_mul_f32 v[40:41],v[40:41],v[92:93] op_sel_hi:[1,0]\nv_pk_mul_f32 v[44:45],v[44:45],v[92:93] op_sel_hi:[1,0]\nv_pk_mul_f32 v[42:43],v[42:43],v[92:93] op_sel_hi:[1,0]\n" \
"v_pk_mul_f32 v[46:47],v[46:47],v[92:93] op_sel_hi:[1,0]\nv_pk_fma_f32 v[24:25],v[32:33],v[92:93],v[24:25] op_sel_hi:[1,0,1]\nv_pk_fma_f32 v[28:29],v[36:37],v[92:93],v[28:29] op_sel_hi:[1,0,1] neg_lo:[1,0,0] neg_hi:[1,0,0]\nv_pk_fma_f32 v[16:17],v[32:33],v[94:95],v[16:17] op_sel_hi:[1,0,1]\n" \
"v_pk_fma_f32 v[20:21],v[36:37],v[94:95],v[20:21] op_sel_hi:[1,0,1] neg_lo:[1,0,0] neg_hi:[1,0,0]\nv_pk_fma_f32 v[26:27],v[34:35],v[92:93],v[26:27] op_sel_hi:[1,0,1]\nv_pk_fma_f32 v[30:31],v[38:39],v[92:93],v[30:31] op_sel_hi:[1,0,1] neg_lo:[1,0,0] neg_hi:[1,0,0]\nv_pk_fma_f32 v[18:19],v[34:35],v[94:95],v[18:19] op_sel_hi:[1,0,1]\n" \
"v_pk_fma_f32 v[22:23],v[38:39],v[94:95],v[22:23] op_sel_hi:[1,0,1] neg_lo:[1,0,0] neg_hi:[1,0,0]\nv_pk_fma_f32 v[48:49],v[56:57],v[92:93],v[48:49] op_sel_hi:[1,0,1]\nv_pk_fma_f32 v[52:53],v[60:61],v[92:93],v[52:53] op_sel_hi:[1,0,1] neg_lo:[1,0,0] neg_hi:[1,0,0]\nv_pk_fma_f32 v[40:41],v[56:57],v[94:95],v[40:41] op_sel_hi:[1,0,1]\n" \
"v_pk_fma_f32 v[44:45],v[60:61],v[94:95],v[44:45] op_sel_hi:[1,0,1] neg_lo:[1,0,0] neg_hi:[1,0,0]\nv_pk_fma_f32 v[50:51],v[58:59],v[92:93],v[50:51] op_sel_hi:[1,0,1]\nv_pk_fma_f32 v[54:55],v[62:63],v[92:93],v[54:55] op_sel_hi:[1,0,1] neg_lo:[1,0,0] neg_hi:[1,0,0]\nv_pk_fma_f32 v[42:43],v[58:59],v[94:95],v[42:43] op_sel_hi:[1,0,1]\n" \
"v_pk_fma_f32 v[46:47],v[62:63],v[94:95],v[46:47] op_sel_hi:[1,0,1] neg_lo:[1,0,0] neg_hi:[1,0,0]\nv_pk_mul_f32 v[32:33],v[64:65],v[90:91] op_sel_hi:[1,0] neg_lo:[1,0] neg_hi:[1,0]\nv_pk_mul_f32 v[36:37],v[66:67],v[90:91] op_sel_hi:[1,0]\nv_pk_mul_f32 v[34:35],v[68:69],v[90:91] op_sel_hi:[1,0] neg_lo:[1,0] neg_hi:[1,0]\n" \
"v_pk_mul_f32 v[38:39],v[70:71],v[90:91] op_sel_hi:[1,0]\nv_pk_mul_f32 v[56:57],v[72:73],v[90:91] op_sel_hi:[1,0] neg_lo:[1,0] neg_hi:[1,0]\nv_pk_mul_f32 v[60:61],v[74:75],v[90:91] op_sel_hi:[1,0]\nv_pk_mul_f32 v[58:59],v[76:77],v[90:91] op_sel_hi:[1,0] neg_lo:[1,0] neg_hi:[1,0]\n" \
"v_pk_mul_f32 v[62:63],v[78:79],v[90:91] op_sel_hi:[1,0]\ns_mov_b32 s78,0x11111111\ns_mov_b32 s79,0x11111111\nds_read_b64_tr_b16 a[64:65],v8 offset:16384\n" \
"ds_read_b64_tr_b16 a[68:69],v9 offset:16384\nds_read_b64_tr_b16 a[72:73],v10 offset:16384\nds_read_b64_tr_b16 a[76:77],v11 offset:16384\nds_read_b64_tr_b16 a[80:81],v8 offset:18432\n" \
"ds_read_b64_tr_b16 a[84:85],v9 offset:18432\nds_read_b64_tr_b16 a[88:89],v10 offset:18432\nds_read_b64_tr_b16 a[92:93],v11 offset:18432\ns_waitcnt vmcnt(13)\n" \
"v_or3_b32 v124,v96,v97,v98\ns_waitcnt vmcnt(11)\nv_or3_b32 v124,v124,v99,v100\ns_waitcnt vmcnt(9)\n" \
"v_or3_b32 v124,v124,v101,v102\ns_waitcnt vmcnt(7)\nv_or3_b32 v124,v124,v103,v104\ns_waitcnt vmcnt(5)\n" \
"v_or3_b32 v124,v124,v105,v106\ns_waitcnt vmcnt(3)\nv_or3_b32 v124,v124,v107,v108\ns_waitcnt vmcnt(1)\n" \
"v_or3_b32 v124,v124,v109,v110\ns_waitcnt vmcnt(0)\nv_or_b32 v124,v124,v111\nv_and_b32 v124,1,v124\n" \
"v_cmp_ne_u32 vcc,0,v124\ns_nop 0\ns_and_b64 vcc,vcc,s[78:79]\ns_cbranch_vccnz .Lst_%=\n" \
".Lbt_%=:\nv_mfma_f32_16x16x32_f16 v[64:67],v[128:131],v[96:99],0\ns_add_u32 s95,s92,9584640\ns_add_u32 m0,s57,20480\n" \
"v_mfma_f32_16x16x32_f16 v[68:71],v[132:135],v[96:99],0\nbuffer_load_dwordx4 v4,s[8:11],s95 offen lds\nv_mfma_f32_16x16x32_f16 v[72:75],v[136:139],v[100:103],0\ns_add_u32 s100,s92,221184\n" \
"s_add_u32 s101,s92,9658368\nv_mfma_f32_16x16x32_f16 v[76:79],v[140:143],v[100:103],0\ns_add_u32 s31,s92,294912\ns_add_u32 s96,s92,9732096\n" \
"v_mfma_f32_16x16x32_f16 v[80:83],v[144:147],v[104:107],0\nbuffer_load_dwordx4 v5,s[8:11],s95 offen offset:1024 lds\nv_mfma_f32_16x16x32_f16 v[84:87],v[148:151],v[104:107],0\nv_cvt_pk_f16_f32 v96,v64,v68\n" \
"v_cvt_pk_f16_f32 v100,v65,v69\nv_mfma_f32_16x16x32_f16 v[88:91],v[152:155],v[108:111],0\nv_cvt_pk_f16_f32 v97,v72,v76\nv_cvt_pk_f16_f32 v101,v73,v77\n" \
"buffer_load_dwordx4 v6,s[8:11],s95 offen offset:2048 lds\nv_mfma_f32_16x16x32_f16 v[92:95],v[156:159],v[108:111],0\nv_cvt_pk_f16_f32 v104,v66,v70\nv_cvt_pk_f16_f32 v98,v80,v84\n" \
"v_cvt_pk_f16_f32 v102,v81,v85\nv_cvt_pk_f16_f32 v105,v74,v78\nv_cvt_pk_f16_f32 v106,v82,v86\nv_cvt_pk_f16_f32 v108,v67,v71\n" \
"buffer_load_dwordx4 v7,s[8:11],s95 offen offset:3072 lds\nv_cvt_pk_f16_f32 v109,v75,v79\nv_cvt_pk_f16_f32 v99,v88,v92\nds_write_b128 v246,v[96:99]\n" \
"v_cvt_pk_f16_f32 v103,v89,v93\ns_add_u32 m0,s57,24576\ns_add_u32 s97,s92,368640\nbuffer_load_dwordx4 v4,s[8:11],s100 offen lds\n" \
"ds_write_b128 v247,v[100:103] offset:1024\nv_cvt_pk_f16_f32 v107,v90,v94\nds_write_b128 v246,v[104:107] offset:2048\nbuffer_load_dwordx4 v5,s[8:11],s100 offen offset:1024 lds\n" \
"v_cvt_pk_f16_f32 v110,v83,v87\nv_cvt_pk_f16_f32 v111,v91,v95\nds_write_b128 v247,v[108:111] offset:3072\ns_add_u32 s95,s92,9805824\n" \
"s_mov_b32 s5,26624\nbuffer_load_dwordx4 v6,s[8:11],s100 offen offset:2048 lds\ns_mov_b32 s6,31232\nbuffer_load_dwordx4 v7,s[8:11],s100 offen offset:3072 lds\n" \
"s_add_u32 m0,s57,0\ns_add_u32 s100,s92,442368\ns_waitcnt lgkmcnt(0)\ns_barrier\n" \
"ds_read_b128 v[96:99],v252\nds_read_b128 v[100:103],v253\nbuffer_load_dwordx4 v4,s[8:11],s101 offen lds\nds_read_b128 v[104:107],v252 offset:2048\n" \
"ds_read_b128 v[108:111],v253 offset:2048\nbuffer_load_dwordx4 v5,s[8:11],s101 offen offset:1024 lds\ns_waitcnt lgkmcnt(3)\nv_mfma_f32_16x16x32_f16 v[64:67],v[112:115],v[96:99],0\n" \
"v_mfma_f32_16x16x32_f16 v[68:71],v[116:119],v[96:99],0\nbuffer_load_dwordx4 v6,s[8:11],s101 offen offset:2048 lds\ns_waitcnt lgkmcnt(2)\nv_mfma_f32_16x16x32_f16 v[72:75],v[112:115],v[100:103],0\n" \
"v_mfma_f32_16x16x32_f16 v[76:79],v[116:119],v[100:103],0\ns_waitcnt lgkmcnt(1)\nv_mfma_f32_16x16x32_f16 v[80:83],v[112:115],v[104:107],0\nbuffer_load_dwordx4 v7,s[8:11],s101 offen offset:3072 lds\n" \
"v_mfma_f32_16x16x32_f16 v[84:87],v[116:119],v[104:107],0\ns_add_u32 m0,s57,4096\nv_pk_mul_f32 v[122:123],v[16:17],v[64:65]\nv_pk_mul_f32 v[120:121],v[16:17],v[68:69]\n" \
"v_pk_mul_f32 v[124:125],v[18:19],v[70:71]\nv_pk_mul_f32 v[126:127],v[18:19],v[66:67]\nv_pk_fma_f32 v[120:121],v[20:21],v[64:65],v[120:121]\nbuffer_load_dwordx4 v4,s[8:11],s31 offen lds\n" \
"v_pk_fma_f32 v[122:123],v[20:21],v[68:69],v[122:123] neg_lo:[1,0,0] neg_hi:[1,0,0]\nv_pk_fma_f32 v[124:125],v[22:23],v[66:67],v[124:125]\nv_pk_fma_f32 v[126:127],v[22:23],v[70:71],v[126:127] neg_lo:[1,0,0] neg_hi:[1,0,0]\nv_pk_fma_f32 v[120:121],v[32:33],v[76:77],v[120:121]\n" \
"v_pk_fma_f32 v[122:123],v[32:33],v[72:73],v[122:123]\nv_pk_fma_f32 v[124:125],v[34:35],v[78:79],v[124:125]\nv_pk_fma_f32 v[126:127],v[34:35],v[74:75],v[126:127]\nbuffer_load_dwordx4 v5,s[8:11],s31 offen offset:1024 lds\n" \
"v_pk_fma_f32 v[120:121],v[36:37],v[72:73],v[120:121]\nv_pk_fma_f32 v[122:123],v[36:37],v[76:77],v[122:123] neg_lo:[1,0,0] neg_hi:[1,0,0]\nv_pk_fma_f32 v[124:125],v[38:39],v[74:75],v[124:125]\nv_pk_fma_f32 v[126:127],v[38:39],v[78:79],v[126:127] neg_lo:[1,0,0] neg_hi:[1,0,0]\n" \
"s_waitcnt lgkmcnt(0)\nv_mfma_f32_16x16x32_f16 v[88:91],v[112:115],v[108:111],0\nv_cvt_pk_f16_f32 v96,v120,v122\nv_cvt_pk_f16_f32 v97,v121,v123\n" \
"buffer_load_dwordx4 v6,s[8:11],s31 offen offset:2048 lds\nv_mfma_f32_16x16x32_f16 v[92:95],v[116:119],v[108:111],0\nv_cvt_pk_f16_f32 v98,v124,v126\nv_cvt_pk_f16_f32 v99,v125,v127\n" \
"v_pk_mul_f32 v[120:121],v[24:25],v[76:77]\nv_pk_mul_f32 v[122:123],v[24:25],v[72:73]\nv_pk_mul_f32 v[124:125],v[26:27],v[78:79]\nv_pk_mul_f32 v[126:127],v[26:27],v[74:75]\n" \
"buffer_load_dwordx4 v7,s[8:11],s31 offen offset:3072 lds\nv_pk_fma_f32 v[120:121],v[28:29],v[72:73],v[120:121]\nv_pk_fma_f32 v[122:123],v[28:29],v[76:77],v[122:123] neg_lo:[1,0,0] neg_hi:[1,0,0]\nv_pk_fma_f32 v[124:125],v[30:31],v[74:75],v[124:125]\n" \
"v_pk_fma_f32 v[126:127],v[30:31],v[78:79],v[126:127] neg_lo:[1,0,0] neg_hi:[1,0,0]\nv_pk_fma_f32 v[120:121],v[32:33],v[68:69],v[120:121] neg_lo:[1,0,0] neg_hi:[1,0,0]\nv_pk_fma_f32 v[122:123],v[32:33],v[64:65],v[122:123] neg_lo:[1,0,0] neg_hi:[1,0,0]\nv_pk_fma_f32 v[124:125],v[34:35],v[70:71],v[124:125] neg_lo:[1,0,0] neg_hi:[1,0,0]\n" \
"s_add_u32 m0,s57,8192\ns_add_u32 s101,s92,9879552\nbuffer_load_dwordx4 v4,s[8:11],s96 offen lds\nv_pk_fma_f32 v[126:127],v[34:35],v[66:67],v[126:127] neg_lo:[1,0,0] neg_hi:[1,0,0]\n" \
"v_pk_fma_f32 v[120:121],v[36:37],v[64:65],v[120:121] neg_lo:[1,0,0] neg_hi:[1,0,0]\nv_pk_fma_f32 v[122:123],v[36:37],v[68:69],v[122:123]\nv_pk_fma_f32 v[124:125],v[38:39],v[66:67],v[124:125] neg_lo:[1,0,0] neg_hi:[1,0,0]\nv_pk_fma_f32 v[126:127],v[38:39],v[70:71],v[126:127]\n" \
"v_cvt_pk_f16_f32 v100,v120,v122\nv_cvt_pk_f16_f32 v101,v121,v123\nbuffer_load_dwordx4 v5,s[8:11],s96 offen offset:1024 lds\nv_cvt_pk_f16_f32 v102,v124,v126\n" \
"v_cvt_pk_f16_f32 v103,v125,v127\nv_pk_mul_f32 v[120:121],v[40:41],v[84:85]\nv_pk_mul_f32 v[122:123],v[40:41],v[80:81]\nv_pk_mul_f32 v[124:125],v[42:43],v[86:87]\n" \
"v_pk_mul_f32 v[126:127],v[42:43],v[82:83]\nv_pk_fma_f32 v[120:121],v[44:45],v[80:81],v[120:121]\nbuffer_load_dwordx4 v6,s[8:11],s96 offen offset:2048 lds\nv_pk_fma_f32 v[122:123],v[44:45],v[84:85],v[122:123] neg_lo:[1,0,0] neg_hi:[1,0,0]\n" \
"v_pk_fma_f32 v[124:125],v[46:47],v[82:83],v[124:125]\nv_pk_fma_f32 v[126:127],v[46:47],v[86:87],v[126:127] neg_lo:[1,0,0] neg_hi:[1,0,0]\nv_pk_fma_f32 v[120:121],v[56:57],v[92:93],v[120:121]\nv_pk_fma_f32 v[122:123],v[56:57],v[88:89],v[122:123]\n" \
"v_pk_fma_f32 v[124:125],v[58:59],v[94:95],v[124:125]\nv_pk_fma_f32 v[126:127],v[58:59],v[90:91],v[126:127]\nbuffer_load_dwordx4 v7,s[8:11],s96 offen offset:3072 lds\nv_pk_fma_f32 v[120:121],v[60:61],v[88:89],v[120:121]\n" \
"v_pk_fma_f32 v[122:123],v[60:61],v[92:93],v[122:123] neg_lo:[1,0,0] neg_hi:[1,0,0]\nv_pk_fma_f32 v[124:125],v[62:63],v[90:91],v[124:125]\nv_pk_fma_f32 v[126:127],v[62:63],v[94:95],v[126:127] neg_lo:[1,0,0] neg_hi:[1,0,0]\nv_cvt_pk_f16_f32 v104,v120,v122\n" \
"v_cvt_pk_f16_f32 v105,v121,v123\nv_cvt_pk_f16_f32 v106,v124,v126\ns_add_u32 m0,s57,12288\nv_cvt_pk_f16_f32 v107,v125,v127\n" \
"buffer_load_dwordx4 v4,s[8:11],s97 offen lds\nv_pk_mul_f32 v[120:121],v[48:49],v[92:93]\nv_pk_mul_f32 v[122:123],v[48:49],v[88:89]\nv_pk_mul_f32 v[124:125],v[50:51],v[94:95]\n" \
"v_pk_mul_f32 v[126:127],v[50:51],v[90:91]\nv_pk_fma_f32 v[120:121],v[52:53],v[88:89],v[120:121]\nv_pk_fma_f32 v[122:123],v[52:53],v[92:93],v[122:123] neg_lo:[1,0,0] neg_hi:[1,0,0]\nv_pk_fma_f32 v[124:125],v[54:55],v[90:91],v[124:125]\n" \
"buffer_load_dwordx4 v5,s[8:11],s97 offen offset:1024 lds\nv_pk_fma_f32 v[126:127],v[54:55],v[94:95],v[126:127] neg_lo:[1,0,0] neg_hi:[1,0,0]\nv_pk_fma_f32 v[120:121],v[56:57],v[84:85],v[120:121] neg_lo:[1,0,0] neg_hi:[1,0,0]\nv_pk_fma_f32 v[122:123],v[56:57],v[80:81],v[122:123] neg_lo:[1,0,0] neg_hi:[1,0,0]\n" \
"v_pk_fma_f32 v[124:125],v[58:59],v[86:87],v[124:125] neg_lo:[1,0,0] neg_hi:[1,0,0]\nv_pk_fma_f32 v[126:127],v[58:59],v[82:83],v[126:127] neg_lo:[1,0,0] neg_hi:[1,0,0]\nv_pk_fma_f32 v[120:121],v[60:61],v[80:81],v[120:121] neg_lo:[1,0,0] neg_hi:[1,0,0]\nv_pk_fma_f32 v[122:123],v[60:61],v[84:85],v[122:123]\n" \
"buffer_load_dwordx4 v6,s[8:11],s97 offen offset:2048 lds\nv_pk_fma_f32 v[124:125],v[62:63],v[82:83],v[124:125] neg_lo:[1,0,0] neg_hi:[1,0,0]\nv_pk_fma_f32 v[126:127],v[62:63],v[86:87],v[126:127]\nv_mfma_f32_16x16x32_f16 v[64:67],v[112:115],v[96:99],0\n" \
"v_cvt_pk_f16_f32 v108,v120,v122\nv_cvt_pk_f16_f32 v109,v121,v123\nv_mfma_f32_16x16x32_f16 v[68:71],v[116:119],v[96:99],0\nbuffer_load_dwordx4 v7,s[8:11],s97 offen offset:3072 lds\n" \
"v_mfma_f32_16x16x32_f16 v[80:83],v[112:115],v[104:107],0\nv_cvt_pk_f16_f32 v110,v124,v126\nv_cvt_pk_f16_f32 v111,v125,v127\nv_mfma_f32_16x16x32_f16 v[84:87],v[116:119],v[104:107],0\n" \
"s_add_u32 m0,s57,16384\ns_waitcnt vmcnt(20)\nds_read_b64_tr_b16 a[66:67],v8 offset:20480\nv_mfma_f32_16x16x32_f16 v[72:75],v[112:115],v[100:103],0\n" \
"buffer_load_dwordx4 v4,s[8:11],s95 offen lds\nv_mfma_f32_16x16x32_f16 v[76:79],v[116:119],v[100:103],0\nv_cvt_pk_f16_f32 v96,v64,v68\nv_cvt_pk_f16_f32 v98,v65,v69\n" \
"v_mfma_f32_16x16x32_f16 v[88:91],v[112:115],v[108:111],0\nv_cvt_pk_f16_f32 v97,v80,v84\nds_write_b64 v248,v[96:97] offset:16384\nbuffer_load_dwordx4 v5,s[8:11],s95 offen offset:1024 lds\n" \
"v_mfma_f32_16x16x32_f16 v[92:95],v[116:119],v[108:111],0\nv_cvt_pk_f16_f32 v99,v81,v85\nds_write_b64 v248,v[98:99] offset:17408\nv_cvt_pk_f16_f32 v100,v66,v70\n" \
"v_cvt_pk_f16_f32 v101,v82,v86\nds_write_b64 v248,v[100:101] offset:18432\nbuffer_load_dwordx4 v6,s[8:11],s95 offen offset:2048 lds\nv_cvt_pk_f16_f32 v102,v67,v71\n" \
"v_cvt_pk_f16_f32 v103,v83,v87\nds_write_b64 v248,v[102:103] offset:19456\nv_cvt_pk_f16_f32 v104,v72,v76\nv_cvt_pk_f16_f32 v105,v88,v92\n" \
"ds_write_b64 v248,v[104:105] offset:16512\nbuffer_load_dwordx4 v7,s[8:11],s95 offen offset:3072 lds\nv_cvt_pk_f16_f32 v106,v73,v77\nv_cvt_pk_f16_f32 v107,v89,v93\n" \
"ds_write_b64 v248,v[106:107] offset:17536\nv_cvt_pk_f16_f32 v108,v74,v78\nv_cvt_pk_f16_f32 v109,v90,v94\nds_write_b64 v248,v[108:109] offset:18560\n" \
"s_add_u32 m0,s57,20480\nv_cvt_pk_f16_f32 v110,v75,v79\nv_cvt_pk_f16_f32 v111,v91,v95\nds_write_b64 v248,v[110:111] offset:19584\n" \
"ds_read_b64_tr_b16 a[70:71],v9 offset:20480\nds_read_b64_tr_b16 a[74:75],v10 offset:20480\nds_read_b64_tr_b16 a[78:79],v11 offset:20480\nds_read_b64_tr_b16 a[82:83],v8 offset:22528\n" \
"ds_read_b64_tr_b16 a[86:87],v9 offset:22528\nds_read_b64_tr_b16 a[90:91],v10 offset:22528\nds_read_b64_tr_b16 a[94:95],v11 offset:22528\ns_waitcnt lgkmcnt(0)\n" \
"buffer_load_dwordx4 v4,s[8:11],s100 offen lds\ns_waitcnt vmcnt(21)\nds_read_b64_tr_b16 a[96:97],v8 offset:24576\nds_read_b64_tr_b16 a[100:101],v9 offset:24576\n" \
"ds_read_b64_tr_b16 a[104:105],v10 offset:24576\nds_read_b64_tr_b16 a[108:109],v11 offset:24576\nds_read_b64_tr_b16 a[112:113],v8 offset:26624\nds_read_b64_tr_b16 a[116:117],v9 offset:26624\n" \
"ds_read_b64_tr_b16 a[120:121],v10 offset:26624\nv_min_u32 v32,2288,v2\nbuffer_load_dwordx4 v5,s[8:11],s100 offen offset:1024 lds\ns_barrier\n" \
"ds_read_b128 v[96:99],v254 offset:16384\nds_read_b128 v[100:103],v254 offset:17408\nds_read_b128 v[104:107],v254 offset:18432\nds_read_b128 v[108:111],v254 offset:19456\n" \
"ds_read_b64_tr_b16 a[124:125],v11 offset:26624\nbuffer_load_dwordx4 v6,s[8:11],s100 offen offset:2048 lds\nbuffer_load_dwordx4 v7,s[8:11],s100 offen offset:3072 lds\ns_add_u32 m0,s57,24576\n" \
"s_waitcnt lgkmcnt(4)\nv_mfma_f32_16x16x32_f16 v[64:67],v[96:99],v[160:163],0\nv_mfma_f32_16x16x32_f16 v[68:71],v[96:99],v[164:167],0\ns_waitcnt lgkmcnt(0)\n" \
"buffer_load_dwordx4 v4,s[8:11],s101 offen lds\nv_mfma_f32_16x16x32_f16 v[72:75],v[100:103],v[168:171],0\nv_mfma_f32_16x16x32_f16 v[76:79],v[100:103],v[172:175],0\nv_mfma_f32_16x16x32_f16 v[80:83],v[104:107],v[176:179],0\n" \
"buffer_load_dwordx4 v5,s[8:11],s101 offen offset:1024 lds\nv_mfma_f32_16x16x32_f16 v[84:87],v[104:107],v[180:183],0\nv_mfma_f32_16x16x32_f16 v[88:91],v[108:111],v[184:187],0\nv_cvt_pk_f16_f32 v96,v68,v64\n" \
"v_cvt_pk_f16_f32 v97,v69,v65\nbuffer_load_dwordx4 v6,s[8:11],s101 offen offset:2048 lds\nv_cvt_pk_f16_f32 v98,v70,v66\nv_cvt_pk_f16_f32 v99,v71,v67\n" \
"v_or_b32_e64 v96,1,v96\nv_mfma_f32_16x16x32_f16 v[92:95],v[108:111],v[188:191],0\nv_cvt_pk_f16_f32 v100,v76,v72\nv_cvt_pk_f16_f32 v101,v77,v73\n" \
"v_cvt_pk_f16_f32 v102,v78,v74\nbuffer_load_dwordx4 v7,s[8:11],s101 offen offset:3072 lds\nv_cvt_pk_f16_f32 v103,v79,v75\nv_or_b32_e64 v100,1,v100\n" \
"v_cvt_pk_f16_f32 v104,v84,v80\nv_cvt_pk_f16_f32 v105,v85,v81\nv_cvt_pk_f16_f32 v106,v86,v82\nv_cvt_pk_f16_f32 v107,v87,v83\n" \
"v_or_b32_e64 v104,1,v104\nv_cvt_pk_f16_f32 v108,v92,v88\nbuffer_load_dwordx4 v[16:19],v32,s[36:39],s5 offen sc1\nv_cvt_pk_f16_f32 v109,v93,v89\n" \
"v_cvt_pk_f16_f32 v110,v94,v90\nv_cvt_pk_f16_f32 v111,v95,v91\nv_or_b32_e64 v108,1,v108\nbuffer_load_dwordx4 v[20:23],v32,s[36:39],s5 offen offset:2304 sc1\n" \
"buffer_load_dwordx4 v[24:27],v32,s[36:39],s6 offen sc1\nbuffer_load_dwordx4 v[28:31],v32,s[36:39],s6 offen offset:2304 sc1\nbuffer_store_dwordx4 v[96:99],v255,s[36:39],s80 offen sc1\nbuffer_store_dwordx4 v[100:103],v255,s[36:39],s81 offen sc1\n" \
"buffer_store_dwordx4 v[104:107],v255,s[36:39],s82 offen sc1\nbuffer_store_dwordx4 v[108:111],v255,s[36:39],s83 offen sc1\ns_barrier\ns_waitcnt vmcnt(32)\n" \
"ds_read_b64_tr_b16 a[98:99],v8\nds_read_b64_tr_b16 a[102:103],v9\nds_read_b64_tr_b16 a[106:107],v10\nds_read_b64_tr_b16 a[110:111],v11\n" \
"ds_read_b64_tr_b16 a[114:115],v8 offset:2048\nds_read_b64_tr_b16 a[118:119],v9 offset:2048\nds_read_b64_tr_b16 a[122:123],v10 offset:2048\nds_read_b64_tr_b16 a[126:127],v11 offset:2048\n" \
"s_waitcnt vmcnt(28)\nds_read_b64_tr_b16 a[128:129],v8 offset:4096\nds_read_b64_tr_b16 a[132:133],v9 offset:4096\nds_read_b64_tr_b16 a[136:137],v10 offset:4096\n" \
"ds_read_b64_tr_b16 a[140:141],v11 offset:4096\nds_read_b64_tr_b16 a[144:145],v8 offset:6144\nds_read_b64_tr_b16 a[148:149],v9 offset:6144\nds_read_b64_tr_b16 a[152:153],v10 offset:6144\n" \
"ds_read_b64_tr_b16 a[156:157],v11 offset:6144\ns_waitcnt vmcnt(24)\nds_read_b64_tr_b16 a[130:131],v8 offset:8192\nds_read_b64_tr_b16 a[134:135],v9 offset:8192\n" \
"ds_read_b64_tr_b16 a[138:139],v10 offset:8192\nds_read_b64_tr_b16 a[142:143],v11 offset:8192\nds_read_b64_tr_b16 a[146:147],v8 offset:10240\nds_read_b64_tr_b16 a[150:151],v9 offset:10240\n" \
"ds_read_b64_tr_b16 a[154:155],v10 offset:10240\nds_read_b64_tr_b16 a[158:159],v11 offset:10240\ns_waitcnt vmcnt(7)\nv_cmp_ne_u64 vcc,s[28:29],v[16:17]\n" \
"s_waitcnt vmcnt(6)\nv_cmp_ne_u64 s[24:25],s[28:29],v[20:21]\ns_waitcnt vmcnt(5)\nv_cmp_ne_u64 s[26:27],s[28:29],v[24:25]\n" \
"s_or_b64 vcc,vcc,s[24:25]\ns_waitcnt vmcnt(4)\nv_cmp_ne_u64 s[24:25],s[28:29],v[28:29]\ns_or_b64 vcc,vcc,s[26:27]\n" \
"s_or_b64 vcc,vcc,s[24:25]\ns_cbranch_vccnz .LsnC_%=\n.LbnC_%=:\nv_and_b32 v97,0xffff0000,v18\n" \
"v_and_b32 v99,0xffff0000,v19\nv_lshlrev_b32 v96,16,v18\nv_lshlrev_b32 v98,16,v19\ns_add_u32 s24,s93,s94\n" \
"v_mul_f32 v96,0x3ee38e39,v96\nv_mul_f32 v97,0x3ee38e39,v97\nv_mul_f32 v98,0x3ee38e39,v98\nv_mul_f32 v99,0x3ee38e39,v99\n" \
"s_sub_u32 s24,s24,253\nv_ldexp_f32 v96,v96,s24\nv_ldexp_f32 v97,v97,s24\nv_ldexp_f32 v98,v98,s24\n" \
"v_ldexp_f32 v99,v99,s24\ns_and_b32 s22,s33,7\ns_and_b32 s24,s32,1\ns_lshl3_add_u32 s22,s24,s22\n" \
"s_bfe_u32 s24,s33,0x10002\ns_lshl1_add_u32 s24,s32,s24\ns_mul_i32 s19,s24,18\nv_bfe_u32 v112,v0,1,5\n" \
"s_and_b32 s24,s33,3\nv_mul_u32_u24 v112,72,v112\nv_lshrrev_b32 v113,6,v0\ns_lshl2_add_u32 s23,s24,s19\n" \
"v_mul_u32_u24 v114,144,v0\nv_lshl_add_u32 v112,v113,5,v112\ns_lshl_b32 s24,s3,5\nv_subrev_u32 v114,18368,v114\n" \
"v_mov_b32 v115,1048576\nv_cmp_gt_u32 vcc,144,v0\nv_and_b32 v113,1,v0\ns_lshr_b32 s20,s33,3\n" \
"s_lshr_b32 s21,s32,1\ns_add_u32 s54,s14,s24\nv_cndmask_b32 v114,v115,v114,vcc\nv_lshl_add_u32 v112,v113,4,v112\n" \
"s_lshr_b32 s25,s33,3\ns_lshl2_add_u32 s24,s20,s21\nv_cmp_gt_u32 vcc,128,v0\nv_add_u32 v113,8,v112\n" \
"s_mul_i32 s25,s25,576\ns_add_u32 s23,s23,s3\ns_lshl2_add_u32 s24,s24,s3\nv_cndmask_b32 v112,v114,v112,vcc\n" \
"v_add_u32 v115,72,v114\ns_sub_u32 s25,163200,s25\ns_add_u32 s23,s23,1\ns_lshl_b32 s24,s24,9\n" \
"v_add_u32 v114,s25,v2\ns_lshl_b32 s23,s23,9\ns_add_u32 s24,s24,262144\nds_write_b64 v112,v[96:97] offset:28800\n" \
"s_sub_u32 s59,s24,s23\ns_lshr_b32 s25,s3,1\ns_mul_i32 s24,s21,1152\ns_mul_i32 s25,s25,576\n" \
"v_cndmask_b32 v113,v115,v113,vcc\ns_add_u32 s24,s24,s25\nds_write_b64 v113,v[98:99] offset:28800\ns_lshl4_add_u32 s24,s24,s22\n" \
"s_and_b32 s25,s3,1\nv_and_b32 v101,0xffff0000,v22\nv_lshlrev_b32 v100,16,v22\ns_lshl_b32 s24,s24,8\n" \
"s_lshl_b32 s65,s25,17\nds_write_b64 v112,v[100:101] offset:31104\ns_add_u32 s65,s65,s24\ns_lshl_b32 s25,s25,20\n" \
"v_and_b32 v103,0xffff0000,v23\nv_lshlrev_b32 v102,16,v23\ns_add_u32 s24,s24,s25\nds_write_b64 v113,v[102:103] offset:31104\n" \
"s_add_u32 s60,s24,655488\ns_add_u32 s61,s24,786432\ns_add_u32 s62,s24,786560\ns_add_u32 s63,s24,917504\n" \
"s_add_u32 s64,s24,917632\ns_mul_i32 s52,s20,4505600\ns_lshl2_add_u32 s24,s21,s3\nv_and_b32 v105,0xffff0000,v26\n" \
"v_lshlrev_b32 v104,16,v26\ns_add_u32 s52,s52,12123648\ns_mul_i32 s24,s24,9216\nds_write_b64 v112,v[104:105] offset:33408\n" \
"s_add_u32 s24,s24,s52\nv_subrev_u32 v115,163200,v114\nv_and_b32 v107,0xffff0000,v27\nv_lshlrev_b32_e64 v106,16,v27\n" \
"s_add_u32 s68,s24,819712\nv_mov_b32 v116,1048576\nv_cmp_gt_u32 vcc,576,v115\nds_write_b64 v113,v[106:107] offset:33408\n" \
"s_lshl_b32 s24,s22,14\nv_cndmask_b32 v114,v116,v114,vcc\nv_and_b32 v109,0xffff0000,v30\nv_and_b32 v111,0xffff0000,v31\n" \
"v_lshlrev_b32 v108,16,v30\nv_lshlrev_b32 v110,16,v31\ns_add_u32 s24,s24,s52\ns_lshl2_add_u32 s25,s21,s3\n" \
"ds_write_b128 v114,v[108:111]\ns_add_u32 s24,s24,967168\ns_lshl_b32 s25,s25,8\ns_add_u32 s79,s25,s24\n" \
"s_add_u32 s25,s21,4\ns_and_b32 s25,s25,3\ns_add_u32 s24,s24,s12\ns_lshl_b32 s25,s25,12\n" \
"s_add_u32 s25,s25,s24\ns_sub_u32 s74,s25,819200\ns_add_u32 s25,s21,3\ns_and_b32 s25,s25,3\n" \
"s_lshl_b32 s25,s25,12\ns_add_u32 s25,s25,s24\ns_sub_u32 s71,s25,2048000\ns_sub_u32 s76,s25,1638400\n" \
"s_add_u32 s25,s21,2\ns_and_b32 s25,s25,3\ns_lshl_b32 s25,s25,12\ns_add_u32 s25,s25,s24\n" \
"s_sub_u32 s72,s25,1638400\ns_sub_u32 s77,s25,1638400\ns_add_u32 s25,s21,1\ns_and_b32 s25,s25,3\n" \
"s_lshl_b32 s25,s25,12\ns_add_u32 s25,s25,s24\ns_sub_u32 s73,s25,1638400\ns_sub_u32 s78,s25,1228800\n" \
"s_lshl2_add_u32 s26,s33,s3\ns_and_b32 s24,s26,15\ns_and_b32 s25,s26,8\ns_lshl2_add_u32 s24,s24,192\n" \
"s_mul_i32 s25,s25,28\nv_and_b32 v112,3,v0\ns_add_u32 s25,s25,s24\ns_mul_i32 s27,s3,4896\n" \
"v_and_b32 v113,12,v0\nv_lshlrev_b32 v112,2,v112\ns_sub_u32 s25,s25,32\ns_add_u32 s27,s27,9216\n" \
"v_max_u32 v112,v112,v113\ns_bitcmp1_b32 s26,4\ns_cselect_b32 s24,s25,s24\nv_mov_b32 v114,1048576\n" \
"v_mov_b32 v113,s27\nv_cmp_gt_u32 vcc,12,v112\ns_waitcnt lgkmcnt(0)\ns_barrier\n" \
"s_and_b32 s25,s26,31\nv_cndmask_b32 v112,v114,v113,vcc\ns_lshl2_add_u32 s25,s25,448\nv_add_u32 v48,v234,v112\n" \
"v_add_u32 v49,v235,v112\nv_add_u32 v50,v236,v112\nv_add_u32 v51,v237,v112\nv_add_u32 v52,v238,v112\n" \
"v_add_u32 v53,v239,v112\nv_add_u32 v54,v240,v112\nv_add_u32 v55,v241,v112\ns_bitcmp1_b32 s26,5\n" \
"s_cselect_b32 s24,s25,s24\nbuffer_load_dwordx4 v[112:115],v3,s[36:39],s16 offen sc1\ns_lshr_b32 s25,s26,6\ns_lshl1_add_u32 s25,s21,s25\n" \
"s_mul_i32 s25,s25,576\nbuffer_load_dwordx4 v[116:119],v3,s[36:39],s17 offen sc1\ns_add_u32 s24,s24,s25\ns_and_b32 s25,s32,1\n" \
"s_lshl1_add_u32 s24,s24,s25\nv_and_b32 v124,48,v0\nv_cmp_gt_u32 vcc,64,v0\ns_lshl_b32 s24,s24,11\n" \
"s_and_b32 s25,s33,3\nv_cndmask_b32 v124,v124,v3,vcc\ns_add_u32 s24,s24,s25\nbuffer_load_dwordx4 v[120:123],v124,s[36:39],s18 offen sc1\n" \
"v_bfe_u32 v127,v0,5,1\nv_and_b32 v13,31,v0\ns_mul_i32 s48,s20,589824\ns_add_u32 s89,s24,1\n" \
"v_mul_u32_u24 v127,2448,v127\nv_mul_u32_u24 v13,72,v13\ns_mul_i32 s27,s3,4896\ns_lshl4_add_u32 s48,s12,s48\n" \
"s_lshl_b32 s86,s21,11\ns_lshl2_add_u32 s24,s2,s3\nv_and_b32 v63,0xffff0000,v224\nv_and_b32 v65,0xffff0000,v225\n" \
"v_and_b32 v67,0xffff0000,v226\nv_and_b32 v69,0xffff0000,v227\nv_and_b32 v71,0xffff0000,v228\nv_and_b32 v73,0xffff0000,v229\n" \
"v_and_b32 v75,0xffff0000,v230\nv_and_b32 v77,0xffff0000,v231\nv_and_b32 v79,0xffff0000,v232\nv_lshlrev_b32 v62,16,v224\n" \
"v_lshlrev_b32 v64,16,v225\nv_lshlrev_b32 v66,16,v226\nv_lshlrev_b32 v68,16,v227\nv_lshlrev_b32 v70,16,v228\n" \
"v_lshlrev_b32 v72,16,v229\nv_lshlrev_b32 v74,16,v230\nv_lshlrev_b32 v76,16,v231\nv_lshlrev_b32 v78,16,v232\n" \
"v_add3_u32 v13,v13,v127,s27\ns_add_u32 s48,s48,9764864\ns_add_u32 s65,s65,2097152\ns_lshl_b32 s67,s21,7\n" \
"s_lshl_b32 s85,s21,17\ns_sub_u32 s86,s14,s86\ns_lshl_b32 s24,s24,8\nv_pk_add_f32 v[62:63],v[62:63],v[250:251] op_sel_hi:[1,0] neg_lo:[0,1] neg_hi:[0,1]\n" \
"v_pk_add_f32 v[64:65],v[64:65],v[250:251] op_sel_hi:[1,0] neg_lo:[0,1] neg_hi:[0,1]\nv_pk_add_f32 v[66:67],v[66:67],v[250:251] op_sel_hi:[1,0] neg_lo:[0,1] neg_hi:[0,1]\nv_pk_add_f32 v[68:69],v[68:69],v[250:251] op_sel_hi:[1,0] neg_lo:[0,1] neg_hi:[0,1]\nv_pk_add_f32 v[70:71],v[70:71],v[250:251] op_sel_hi:[1,0] neg_lo:[0,1] neg_hi:[0,1]\n" \
"v_pk_add_f32 v[72:73],v[72:73],v[250:251] op_sel_hi:[1,0] neg_lo:[0,1] neg_hi:[0,1]\nv_pk_add_f32 v[74:75],v[74:75],v[250:251] op_sel_hi:[1,0] neg_lo:[0,1] neg_hi:[0,1]\nv_pk_add_f32 v[76:77],v[76:77],v[250:251] op_sel_hi:[1,0] neg_lo:[0,1] neg_hi:[0,1]\nv_pk_add_f32 v[78:79],v[78:79],v[250:251] op_sel_hi:[1,0] neg_lo:[0,1] neg_hi:[0,1]\n" \
"v_mov_b64 v[16:17],v[192:193]\nv_mov_b64 v[18:19],v[194:195]\nv_mov_b64 v[20:21],v[196:197]\nv_mov_b64 v[22:23],v[198:199]\n" \
"v_mov_b64 v[24:25],v[208:209]\nv_mov_b64 v[26:27],v[210:211]\nv_mov_b64 v[28:29],v[212:213]\nv_mov_b64 v[30:31],v[214:215]\n" \
"v_mov_b64 v[32:33],v[216:217]\nv_mov_b64 v[34:35],v[218:219]\nv_mov_b64 v[36:37],v[220:221]\nv_mov_b64 v[38:39],v[222:223]\n" \
"v_mov_b64 v[40:41],v[200:201]\nv_mov_b64 v[42:43],v[202:203]\nv_mov_b64 v[44:45],v[204:205]\nv_mov_b64 v[46:47],v[206:207]\n" \
"v_mov_b64 v[56:57],v[242:243]\nv_mov_b64 v[58:59],v[244:245]\nv_mov_b32 v60,v249\nv_mov_b32 v61,v250\n" \
"ds_read_b64_tr_b16 a[160:161],v8 offset:12288\nds_read_b64_tr_b16 a[164:165],v9 offset:12288\nds_read_b64_tr_b16 a[168:169],v10 offset:12288\nds_read_b64_tr_b16 a[172:173],v11 offset:12288\n" \
"ds_read_b64_tr_b16 a[176:177],v8 offset:14336\nds_read_b64_tr_b16 a[180:181],v9 offset:14336\nds_read_b64_tr_b16 a[184:185],v10 offset:14336\nds_read_b64_tr_b16 a[188:189],v11 offset:14336\n" \
"ds_read_b64_tr_b16 a[162:163],v8 offset:16384\nds_read_b64_tr_b16 a[166:167],v9 offset:16384\nds_read_b64_tr_b16 a[170:171],v10 offset:16384\nds_read_b64_tr_b16 a[174:175],v11 offset:16384\n" \
"ds_read_b64_tr_b16 a[178:179],v8 offset:18432\nv_mov_b32_e64 v127,0\nds_read_b64_tr_b16 a[182:183],v9 offset:18432\nds_read_b64_tr_b16 a[186:187],v10 offset:18432\n" \
"s_add_u32 s49,s48,4096\ns_add_u32 s50,s48,8192\ns_add_u32 s51,s48,12288\ns_add_u32 s53,s52,3276800\n" \
"s_add_u32 s83,s52,2048000\ns_mov_b32 s55,0xbdd2d3e7\ns_mul_i32 s56,s3,9568\ns_lshl_b32 s58,s3,13\n" \
"s_add_u32 s75,s59,196608\ns_add_u32 s66,s65,128\nds_read_b64_tr_b16 a[190:191],v11 offset:18432\nds_read_b64_tr_b16 a[192:193],v8 offset:20480\n" \
"s_add_u32 s67,s67,163200\nds_read_b64_tr_b16 a[196:197],v9 offset:20480\ns_add_u32 s69,s68,3072\nds_read_b64_tr_b16 a[200:201],v10 offset:20480\n" \
"s_add_u32 s70,s68,6144\nds_read_b64_tr_b16 a[204:205],v11 offset:20480\ns_add_u32 s80,s79,4096\nds_read_b64_tr_b16 a[208:209],v8 offset:22528\n" \
"s_add_u32 s81,s79,8192\nds_read_b64_tr_b16 a[212:213],v9 offset:22528\ns_add_u32 s82,s79,12288\nds_read_b64_tr_b16 a[216:217],v10 offset:22528\n" \
"ds_read_b64_tr_b16 a[220:221],v11 offset:22528\ns_and_b32 s84,s33,1\nds_read_b64_tr_b16 a[194:195],v8 offset:24576\ns_add_u32 s85,s85,65536\n" \
"ds_read_b64_tr_b16 a[198:199],v9 offset:24576\ns_add_u32 s87,s12,65536\nds_read_b64_tr_b16 a[202:203],v10 offset:24576\ns_add_u32 s88,s74,819200\n" \
"ds_read_b64_tr_b16 a[206:207],v11 offset:24576\ns_add_u32 s86,s86,0x10000\nds_read_b64_tr_b16 a[210:211],v8 offset:26624\ns_mov_b32 s90,7405568\n" \
"ds_read_b64_tr_b16 a[214:215],v9 offset:26624\ns_add_u32 s91,s24,69206016\nds_read_b64_tr_b16 a[218:219],v10 offset:26624\nds_read_b64_tr_b16 a[222:223],v11 offset:26624\n" \
"s_sub_u32 s98,s57,s12\ns_mov_b32 s99,s12\ns_waitcnt vmcnt(1)\nv_and_b32 v125,v112,v116\n" \
"s_waitcnt vmcnt(0)\nv_bitop3_b32 v125,v125,v120,1 bitop3:0x80\nv_cmp_eq_u32 vcc,0,v125\ns_nop 0\n" \
"s_cbranch_vccnz .LsuC_%=\n.LbuC_%=:\nv_mfma_f32_16x16x32_f16 v[80:83],v[32:35],v[112:115],0\nv_and_b32 v3,31,v0\n" \
"v_mul_u32_u24 v3,72,v3\nv_mfma_f32_16x16x32_f16 v[84:87],v[36:39],v[112:115],0\nv_add_u32 v13,9216,v13\nv_mov_b32 v104,-1\n" \
"v_mfma_f32_16x16x32_f16 v[88:91],v[32:35],v[116:119],0\nv_mov_b32 v105,-1\nv_mov_b32 v106,-1\nv_mfma_f32_16x16x32_f16 v[92:95],v[36:39],v[116:119],0\n" \
"v_mov_b32 v107,-1\ns_add_u32 s27,s2,192\nv_mfma_f32_16x16x32_f16 v[96:99],v[32:35],v[120:123],0\ns_lshr_b32 s31,s27,4\n" \
"s_mul_i32 s96,s31,37\ns_lshr_b32 s96,s96,8\ns_lshl2_add_u32 s31,s96,s31\ns_mul_i32 s31,s31,409600\n" \
"s_and_b32 s96,s27,15\ns_lshl_b32 s96,s96,14\ns_add_u32 s31,s31,s96\ns_add_u32 s31,s31,13090816\n" \
"buffer_store_dwordx4 v[104:107],v2,s[36:39],s31 offen sc1\nv_mfma_f32_16x16x32_f16 v[100:103],v[36:39],v[120:123],0\nv_cvt_pk_f16_f32 v112,v84,v80\nv_cvt_pk_f16_f32 v113,v85,v81\n" \
"v_cvt_pk_f16_f32 v114,v86,v82\nv_cvt_pk_f16_f32 v115,v87,v83\nds_write_b128 v57,v[112:115]\nv_cvt_pk_f16_f32 v116,v92,v88\n" \
"v_cvt_pk_f16_f32 v117,v93,v89\ns_add_u32 s31,s31,4096\nbuffer_store_dwordx4 v[104:107],v2,s[36:39],s31 offen sc1\nv_cvt_pk_f16_f32 v118,v94,v90\n" \
"v_cvt_pk_f16_f32 v119,v95,v91\nds_write_b128 v58,v[116:119]\nv_cvt_pk_f16_f32 v120,v100,v96\nv_cvt_pk_f16_f32 v121,v101,v97\n" \
"v_cvt_pk_f16_f32 v122,v102,v98\nv_cvt_pk_f16_f32 v123,v103,v99\nds_write_b128 v60,v[120:123]\ns_add_u32 s31,s31,4096\n" \
"buffer_store_dwordx4 v[104:107],v2,s[36:39],s31 offen sc1\nv_add_u32 v60,31104,v3\nv_add_u32 v3,28800,v3\nds_read2_b64 v[32:35],v3 offset0:0 offset1:1\n" \
"ds_read2_b64 v[36:39],v3 offset0:2 offset1:3\ns_add_u32 s31,s31,4096\ns_add_u32 s27,s2,47\ns_lshr_b32 s97,s27,4\n" \
"s_mul_i32 s95,s97,37\ns_lshr_b32 s95,s95,8\ns_lshl2_add_u32 s97,s95,s97\ns_mul_i32 s97,s97,409600\n" \
"s_and_b32 s95,s27,15\ns_lshl_b32 s95,s95,14\nbuffer_store_dwordx4 v[104:107],v2,s[36:39],s31 offen sc1\ns_add_u32 s97,s97,s95\n" \
"s_add_u32 s97,s97,13090816\ns_add_u32 s100,s92,516096\ns_waitcnt lgkmcnt(2)\ns_barrier\n" \
"ds_read_b64 v[112:113],v56\nds_read_b64 v[116:117],v56 offset:512\nds_read_b64 v[120:121],v56 offset:256\nds_read_b64 v[124:125],v56 offset:768\n" \
"ds_read_b64 v[114:115],v56 offset:1024\nds_read_b64 v[122:123],v56 offset:1280\nds_read_b64 v[56:57],v3 offset:64\ns_add_u32 m0,s57,0\n" \
"buffer_store_dwordx4 v[104:107],v2,s[36:39],s97 offen sc1\ns_add_u32 s97,s97,4096\ns_add_u32 s101,s92,9953280\ns_bfe_u32 s26,s33,0x20001\n" \
"s_lshl2_add_u32 s15,s32,s26\ns_lshl2_add_u32 s15,s15,s3\ns_lshl_b32 s15,s15,14\ns_add_u32 s15,s15,5308416\n" \
"s_add_u32 s31,s15,0\ns_add_u32 s96,s15,4096\ns_add_u32 s95,s15,12288\nv_bfe_u32 v110,v0,1,2\n" \
"v_bfe_u32 v111,v0,4,1\nv_xor_b32 v110,v110,v111\nbuffer_store_dwordx4 v[104:107],v2,s[36:39],s97 offen sc1\ns_waitcnt lgkmcnt(5)\n" \
"v_swap_b32 v113,v116\ns_waitcnt lgkmcnt(3)\nv_swap_b32 v121,v124\ns_waitcnt lgkmcnt(2)\n" \
"v_mov_b32 v118,v115\nv_mfma_f32_16x16x32_f16 v[80:83],v[112:115],v[40:43],0\ns_waitcnt lgkmcnt(1)\nv_mov_b32 v126,v123\n" \
"s_add_u32 s97,s97,4096\nv_mfma_f32_16x16x32_f16 v[84:87],v[112:115],v[44:47],0\nv_and_b32_e64 v111,1,v0\nv_lshl_add_u32 v110,v110,1,v111\n" \
"v_mfma_f32_16x16x32_f16 v[88:91],v[120:123],v[40:43],0\nv_bfe_u32 v111,v0,3,3\nbuffer_store_dwordx4 v[104:107],v2,s[36:39],s97 offen sc1\nv_mfma_f32_16x16x32_f16 v[92:95],v[120:123],v[44:47],0\n" \
"s_add_u32 s97,s97,4096\nv_lshlrev_b32 v111,12,v111\nv_mfma_f32_16x16x32_f16 v[96:99],v[116:119],v[40:43],0\ns_lshr_b32 s26,s32,1\n" \
"s_mul_i32 s92,s26,1152\nv_pk_add_f32 v[88:89],v[80:81],v[88:89] neg_lo:[0,1] neg_hi:[0,1]\nv_pk_add_f32 v[90:91],v[82:83],v[90:91] neg_lo:[0,1] neg_hi:[0,1]\nv_pk_fma_f32 v[80:81],v[80:81],2.0,v[88:89] op_sel_hi:[1,0,1] neg_lo:[0,0,1] neg_hi:[0,0,1]\n" \
"v_pk_add_f32 v[92:93],v[84:85],v[92:93] neg_lo:[0,1] neg_hi:[0,1]\nv_pk_add_f32 v[94:95],v[86:87],v[94:95] neg_lo:[0,1] neg_hi:[0,1]\nbuffer_store_dwordx4 v[104:107],v2,s[36:39],s97 offen sc1\nv_pk_fma_f32 v[82:83],v[82:83],2.0,v[90:91] op_sel_hi:[1,0,1] neg_lo:[0,0,1] neg_hi:[0,0,1]\n" \
"v_pk_fma_f32 v[84:85],v[84:85],2.0,v[92:93] op_sel_hi:[1,0,1] neg_lo:[0,0,1] neg_hi:[0,0,1]\nv_pk_fma_f32 v[86:87],v[86:87],2.0,v[94:95] op_sel_hi:[1,0,1] neg_lo:[0,0,1] neg_hi:[0,0,1]\nv_cvt_pk_f16_f32 v112,v80,v84\nv_cvt_pk_f16_f32 v113,v81,v85\n" \
"v_cvt_pk_f16_f32 v114,v82,v86\nv_cvt_pk_f16_f32 v115,v83,v87\nv_mfma_f32_16x16x32_f16 v[100:103],v[116:119],v[44:47],0\nv_cvt_pk_f16_f32 v116,v88,v92\n" \
"v_cvt_pk_f16_f32 v117,v89,v93\nv_mfma_f32_16x16x32_f16 v[80:83],v[16:19],v[112:115],0\nbuffer_load_dwordx4 v4,s[8:11],s100 offen lds\nv_mfma_f32_16x16x32_f16 v[84:87],v[20:23],v[112:115],0\n" \
"v_cvt_pk_f16_f32 v118,v90,v94\nv_cvt_pk_f16_f32 v119,v91,v95\nv_mfma_f32_16x16x32_f16 v[104:107],v[124:127],v[40:43],0\nds_read2_b64 v[40:43],v3 offset0:4 offset1:5\n" \
"ds_read2_b64 v[112:115],v60 offset0:0 offset1:1\nv_mfma_f32_16x16x32_f16 v[88:91],v[24:27],v[116:119],0\nbuffer_load_dwordx4 v5,s[8:11],s100 offen offset:1024 lds\nv_mfma_f32_16x16x32_f16 v[92:95],v[28:31],v[116:119],0\n" \
"ds_write2_b32 v48,v84,v80 offset0:0 offset1:1\nds_write2_b32 v49,v85,v81 offset0:0 offset1:1\nds_write2_b32 v50,v86,v82 offset0:0 offset1:1\nbuffer_load_dwordx4 v6,s[8:11],s100 offen offset:2048 lds\n" \
"v_pk_add_f32 v[104:105],v[96:97],v[104:105] neg_lo:[0,1] neg_hi:[0,1]\nv_pk_add_f32 v[106:107],v[98:99],v[106:107] neg_lo:[0,1] neg_hi:[0,1]\nv_pk_fma_f32 v[96:97],v[96:97],2.0,v[104:105] op_sel_hi:[1,0,1] neg_lo:[0,0,1] neg_hi:[0,0,1]\nv_pk_fma_f32 v[98:99],v[98:99],2.0,v[106:107] op_sel_hi:[1,0,1] neg_lo:[0,0,1] neg_hi:[0,0,1]\n" \
"ds_write2_b32 v51,v87,v83 offset0:0 offset1:1\nds_write2_b32 v52,v92,v88 offset0:0 offset1:1\nbuffer_load_dwordx4 v7,s[8:11],s100 offen offset:3072 lds\nds_write2_b32 v53,v93,v89 offset0:0 offset1:1\n" \
"ds_write2_b32 v54,v94,v90 offset0:0 offset1:1\nds_write2_b32 v55,v95,v91 offset0:0 offset1:1\nv_add_u32 v48,2448,v48\ns_add_u32 m0,s57,4096\n" \
"s_add_u32 s97,s15,8192\nbuffer_load_dwordx4 v4,s[8:11],s101 offen lds\nv_add_u32 v49,2448,v49\nv_add_u32 v50,2448,v50\n" \
"v_add_u32 v51,2448,v51\nv_add_u32 v52,2448,v52\nv_add_u32 v53,2448,v53\nv_add_u32 v54,2448,v54\n" \
"v_add_u32 v55,2448,v55\nbuffer_load_dwordx4 v5,s[8:11],s101 offen offset:1024 lds\nds_read2_b64 v[116:119],v60 offset0:2 offset1:3\nv_lshl_add_u32 v4,v110,4,v111\n" \
"v_mfma_f32_16x16x32_f16 v[108:111],v[124:127],v[44:47],0\nds_read2_b64 v[44:47],v3 offset0:6 offset1:7\nv_xor_b32 v5,64,v4\nv_add_u32 v5,31744,v5\n" \
"s_lshr_b32 s26,s3,1\nbuffer_load_dwordx4 v6,s[8:11],s101 offen offset:2048 lds\nv_add_u32 v6,63488,v4\ns_lshl_b32 s26,s26,6\n" \
"s_add_u32 s92,s92,s26\nv_pk_add_f32 v[108:109],v[100:101],v[108:109] neg_lo:[0,1] neg_hi:[0,1]\nv_pk_add_f32 v[110:111],v[102:103],v[110:111] neg_lo:[0,1] neg_hi:[0,1]\nv_pk_fma_f32 v[100:101],v[100:101],2.0,v[108:109] op_sel_hi:[1,0,1] neg_lo:[0,0,1] neg_hi:[0,0,1]\n" \
"v_pk_fma_f32 v[102:103],v[102:103],2.0,v[110:111] op_sel_hi:[1,0,1] neg_lo:[0,0,1] neg_hi:[0,0,1]\nv_cvt_pk_f16_f32 v120,v96,v100\nbuffer_load_dwordx4 v7,s[8:11],s101 offen offset:3072 lds\nv_cvt_pk_f16_f32 v121,v97,v101\n" \
"v_cvt_pk_f16_f32 v122,v98,v102\nv_cvt_pk_f16_f32 v123,v99,v103\nv_cvt_pk_f16_f32 v124,v104,v108\nv_cvt_pk_f16_f32 v125,v105,v109\n" \
"v_mfma_f32_16x16x32_f16 v[96:99],v[16:19],v[120:123],0\nv_cvt_pk_f16_f32 v126,v106,v110\ns_add_u32 m0,s57,8192\ns_lshr_b32 s100,s33,3\n" \
"buffer_load_dwordx4 v1,s[36:39],s31 offen sc1 lds\nv_mfma_f32_16x16x32_f16 v[100:103],v[20:23],v[120:123],0\nv_cvt_pk_f16_f32 v127,v107,v111\nds_read2_b64 v[120:123],v60 offset0:4 offset1:5\n" \
"v_add_u32 v7,63488,v5\nv_mfma_f32_16x16x32_f16 v[104:107],v[24:27],v[124:127],0\ns_lshl_b32 s26,s3,8\ns_add_u32 s92,s92,s26\n" \
"buffer_load_dwordx4 v1,s[36:39],s31 offen offset:1024 sc1 lds\nv_mfma_f32_16x16x32_f16 v[108:111],v[28:31],v[124:127],0\nds_write2_b32 v48,v100,v96 offset0:0 offset1:1\nds_write2_b32 v49,v101,v97 offset0:0 offset1:1\n" \
"ds_write2_b32 v50,v102,v98 offset0:0 offset1:1\nbuffer_load_dwordx4 v1,s[36:39],s31 offen offset:2048 sc1 lds\nds_write2_b32 v51,v103,v99 offset0:0 offset1:1\nds_read2_b64 v[124:127],v60 offset0:6 offset1:7\n" \
"ds_read_b64 v[98:99],v60 offset:64\nv_add_u32 v60,2304,v60\nds_write2_b32 v52,v108,v104 offset0:0 offset1:1\nds_write2_b32 v53,v109,v105 offset0:0 offset1:1\n" \
"buffer_load_dwordx4 v1,s[36:39],s31 offen offset:3072 sc1 lds\nds_write2_b32 v54,v110,v106 offset0:0 offset1:1\nds_write2_b32 v55,v111,v107 offset0:0 offset1:1\nds_read2_b64 v[80:83],v13 offset0:0 offset1:1\n" \
"ds_read2_b64 v[84:87],v13 offset0:2 offset1:3\ns_add_u32 m0,s57,12288\ns_lshl_b32 s92,s92,4\nbuffer_load_dwordx4 v1,s[36:39],s96 offen sc1 lds\n" \
"ds_read2_b64 v[88:91],v13 offset0:4 offset1:5\ns_and_b32 s26,s32,1\nds_read2_b64 v[92:95],v13 offset0:6 offset1:7\ns_lshl3_add_u32 s92,s26,s92\n" \
"s_and_b32 s26,s33,7\ns_add_u32 s92,s92,s26\ns_lshl_b32 s92,s92,8\ns_add_u32 s100,s100,0\n" \
"s_sub_u32 s101,s100,4\nbuffer_load_dwordx4 v1,s[36:39],s96 offen offset:1024 sc1 lds\nds_read_b64 v[96:97],v13 offset:64\nds_read2_b64 v[16:19],v60 offset0:0 offset1:1\n" \
"ds_read2_b64 v[20:23],v60 offset0:2 offset1:3\ns_waitcnt lgkmcnt(6)\nv_pk_fma_f32 v[62:63],v[80:81],v[32:33],v[62:63]\nv_pk_fma_f32 v[64:65],v[82:83],v[34:35],v[64:65]\n" \
"v_pk_mul_f32 v[102:103],v[62:63],v[62:63]\nv_pk_add_f32 v[100:101],v[62:63],v[64:65]\nv_pk_fma_f32 v[102:103],v[64:65],v[64:65],v[102:103]\nbuffer_load_dwordx4 v1,s[36:39],s96 offen offset:2048 sc1 lds\n" \
"s_waitcnt lgkmcnt(5)\nv_pk_fma_f32 v[66:67],v[84:85],v[36:37],v[66:67]\nv_pk_fma_f32 v[68:69],v[86:87],v[38:39],v[68:69]\nv_pk_add_f32 v[100:101],v[100:101],v[66:67]\n" \
"v_pk_fma_f32 v[102:103],v[66:67],v[66:67],v[102:103]\ns_waitcnt lgkmcnt(4)\nv_pk_fma_f32 v[70:71],v[88:89],v[40:41],v[70:71]\nv_pk_add_f32 v[100:101],v[100:101],v[68:69]\n" \
"v_pk_fma_f32 v[102:103],v[68:69],v[68:69],v[102:103]\nbuffer_load_dwordx4 v1,s[36:39],s96 offen offset:3072 sc1 lds\nv_pk_fma_f32 v[72:73],v[90:91],v[42:43],v[72:73]\nv_pk_add_f32 v[100:101],v[100:101],v[70:71]\n" \
"v_pk_fma_f32 v[102:103],v[70:71],v[70:71],v[102:103]\ns_waitcnt lgkmcnt(3)\nv_pk_fma_f32 v[74:75],v[92:93],v[44:45],v[74:75]\nv_pk_add_f32 v[100:101],v[100:101],v[72:73]\n" \
"v_pk_fma_f32 v[102:103],v[72:73],v[72:73],v[102:103]\nv_pk_fma_f32 v[76:77],v[94:95],v[46:47],v[76:77]\ns_add_u32 m0,s57,16384\ns_min_u32 s100,s100,s101\n" \
"buffer_load_dwordx4 v1,s[36:39],s97 offen sc1 lds\nv_pk_add_f32 v[100:101],v[100:101],v[74:75]\nv_pk_fma_f32 v[102:103],v[74:75],v[74:75],v[102:103]\ns_waitcnt lgkmcnt(2)\n" \
"v_pk_fma_f32 v[78:79],v[96:97],v[56:57],v[78:79]\nv_pk_add_f32 v[100:101],v[100:101],v[76:77]\nv_pk_fma_f32 v[102:103],v[76:77],v[76:77],v[102:103]\nv_pk_add_f32 v[100:101],v[100:101],v[78:79]\n" \
"v_pk_fma_f32 v[102:103],v[78:79],v[78:79],v[102:103]\nbuffer_load_dwordx4 v1,s[36:39],s97 offen offset:1024 sc1 lds\nds_read2_b64 v[24:27],v60 offset0:4 offset1:5\nv_add_f32 v100,v100,v101\n" \
"v_add_f32 v102,v102,v103\nds_read2_b64 v[28:31],v60 offset0:6 offset1:7\nds_read_b64 v[58:59],v60 offset:64\nv_permlane16_swap_b32 v100,v102\n" \
"v_add_f32 v100,v100,v102\ns_waitcnt vmcnt(14)\nds_read_b64_tr_b16 a[224:225],v8\nbuffer_load_dwordx4 v1,s[36:39],s97 offen offset:2048 sc1 lds\n" \
"v_add_f32_dpp v100,v100,v100 quad_perm:[1,0,3,2] row_mask:0xf bank_mask:0xf bound_ctrl:1\nds_read_b64_tr_b16 a[228:229],v9\nds_read_b64_tr_b16 a[232:233],v10\nv_add_f32_dpp v100,v100,v100 quad_perm:[2,3,0,1] row_mask:0xf bank_mask:0xf bound_ctrl:1\n" \
"ds_read_b64_tr_b16 a[236:237],v11\nds_read_b64_tr_b16 a[240:241],v8 offset:2048\nv_add_f32_dpp v100,v100,v100 row_half_mirror row_mask:0xf bank_mask:0xf bound_ctrl:1\nds_read_b64_tr_b16 a[244:245],v9 offset:2048\n" \
"buffer_load_dwordx4 v1,s[36:39],s97 offen offset:3072 sc1 lds\nv_add_f32_dpp v100,v100,v100 row_mirror row_mask:0xf bank_mask:0xf bound_ctrl:1\nv_mul_f32 v104,0x3ae38e39,v100\nv_mul_f32 v102,0x3ae38e39,v100\n" \
"ds_read_b64_tr_b16 a[248:249],v10 offset:2048\nds_read_b64_tr_b16 a[252:253],v11 offset:2048\nv_permlane16_swap_b32 v104,v102\nv_fma_f32 v102,-v104,v104,v102\n" \
"s_add_u32 m0,s57,20480\ns_lshl_b32 s101,s100,12\nbuffer_load_dwordx4 v1,s[36:39],s95 offen sc1 lds\nv_max_f32 v102,0,v102\n" \
"v_add_f32 v102,0x3727c5ac,v102\nv_rsq_f32 v100,v102\ns_waitcnt vmcnt(13)\nds_read_b64_tr_b16 a[226:227],v8 offset:4096\n" \
"v_mul_f32 v101,-v104,v100\nv_pk_fma_f32 v[80:81],v[62:63],v[100:101],v[100:101] op_sel:[0,0,1] op_sel_hi:[1,0,1]\nv_pk_fma_f32 v[82:83],v[64:65],v[100:101],v[100:101] op_sel:[0,0,1] op_sel_hi:[1,0,1]\nbuffer_load_dwordx4 v1,s[36:39],s95 offen offset:1024 sc1 lds\n" \
"v_pk_fma_f32 v[84:85],v[66:67],v[100:101],v[100:101] op_sel:[0,0,1] op_sel_hi:[1,0,1]\nv_pk_fma_f32 v[86:87],v[68:69],v[100:101],v[100:101] op_sel:[0,0,1] op_sel_hi:[1,0,1]\nv_pk_fma_f32 v[88:89],v[70:71],v[100:101],v[100:101] op_sel:[0,0,1] op_sel_hi:[1,0,1]\nv_pk_fma_f32 v[90:91],v[72:73],v[100:101],v[100:101] op_sel:[0,0,1] op_sel_hi:[1,0,1]\n" \
"v_pk_fma_f32 v[92:93],v[74:75],v[100:101],v[100:101] op_sel:[0,0,1] op_sel_hi:[1,0,1]\nv_pk_fma_f32 v[94:95],v[76:77],v[100:101],v[100:101] op_sel:[0,0,1] op_sel_hi:[1,0,1]\nv_pk_fma_f32 v[96:97],v[78:79],v[100:101],v[100:101] op_sel:[0,0,1] op_sel_hi:[1,0,1]\nbuffer_load_dwordx4 v1,s[36:39],s95 offen offset:2048 sc1 lds\n" \
"ds_read_b64_tr_b16 a[230:231],v9 offset:4096\nds_read_b64_tr_b16 a[234:235],v10 offset:4096\nds_read_b64_tr_b16 a[238:239],v11 offset:4096\nds_read_b64_tr_b16 a[242:243],v8 offset:6144\n" \
"ds_read_b64_tr_b16 a[246:247],v9 offset:6144\nds_read_b64_tr_b16 a[250:251],v10 offset:6144\nds_read_b64_tr_b16 a[254:255],v11 offset:6144\ns_waitcnt vmcnt(11)\n" \
"ds_read_b64_tr_b16 v[192:193],v15 offset:8192\nds_read_b64_tr_b16 v[196:197],v15 offset:8704\nbuffer_load_dwordx4 v1,s[36:39],s95 offen offset:3072 sc1 lds\nds_read_b64_tr_b16 v[194:195],v15 offset:9216\n" \
"ds_read_b64_tr_b16 v[198:199],v15 offset:9728\nds_read_b64_tr_b16 v[200:201],v15 offset:10240\nds_read_b64_tr_b16 v[204:205],v15 offset:10752\nds_read_b64_tr_b16 v[202:203],v15 offset:11264\n" \
"ds_read_b64_tr_b16 v[206:207],v15 offset:11776\ns_add_u32 s101,s101,s57\ns_add_u32 m0,s101,0\ns_and_b32 s101,s100,1\n" \
"s_lshr_b32 s100,s100,1\ns_lshl_b32 s100,s100,10\ns_add_u32 s100,s100,s101\ns_lshl_b32 s100,s100,7\n" \
"s_add_u32 s100,s100,s92\ns_lshr_b32 s31,s33,3\ns_add_u32 s31,s31,1\ns_sub_u32 s96,s31,4\n" \
"s_min_u32 s31,s31,s96\ns_lshl_b32 s96,s31,12\ns_add_u32 s96,s96,s57\ns_lshr_b32 s97,s33,3\n" \
"s_add_u32 s97,s97,2\ns_sub_u32 s95,s97,4\ns_min_u32 s97,s97,s95\ns_lshl_b32 s95,s97,12\n" \
"s_add_u32 s95,s95,s57\nv_readlane_b32 s24,v112,0\nv_mov_b32 v60,v61\ns_mov_b32 s25,s24\n" \
"v_pk_add_f32 v[112:113],v[112:113],s[24:25] neg_lo:[0,1] neg_hi:[0,1]\ns_waitcnt lgkmcnt(15)\nv_pk_fma_f32 v[80:81],v[80:81],v[16:17],v[112:113]\nv_pk_mul_f32 v[102:103],v[80:81],v[80:81]\n" \
"v_pk_add_f32 v[114:115],v[114:115],s[24:25] neg_lo:[0,1] neg_hi:[0,1]\nv_pk_fma_f32 v[82:83],v[82:83],v[18:19],v[114:115]\nv_pk_add_f32 v[100:101],v[80:81],v[82:83]\nv_pk_fma_f32 v[102:103],v[82:83],v[82:83],v[102:103]\n" \
"v_pk_add_f32 v[116:117],v[116:117],s[24:25] neg_lo:[0,1] neg_hi:[0,1]\nv_pk_fma_f32 v[84:85],v[84:85],v[20:21],v[116:117]\nv_pk_add_f32 v[100:101],v[100:101],v[84:85]\nv_pk_fma_f32 v[102:103],v[84:85],v[84:85],v[102:103]\n" \
"v_pk_add_f32 v[118:119],v[118:119],s[24:25] neg_lo:[0,1] neg_hi:[0,1]\nv_pk_fma_f32 v[86:87],v[86:87],v[22:23],v[118:119]\nv_pk_add_f32 v[100:101],v[100:101],v[86:87]\nv_pk_fma_f32 v[102:103],v[86:87],v[86:87],v[102:103]\n" \
"v_pk_add_f32 v[120:121],v[120:121],s[24:25] neg_lo:[0,1] neg_hi:[0,1]\nv_pk_fma_f32 v[88:89],v[88:89],v[24:25],v[120:121]\nv_pk_add_f32 v[100:101],v[100:101],v[88:89]\nv_pk_fma_f32 v[102:103],v[88:89],v[88:89],v[102:103]\n" \
"v_pk_add_f32 v[122:123],v[122:123],s[24:25] neg_lo:[0,1] neg_hi:[0,1]\nv_pk_fma_f32 v[90:91],v[90:91],v[26:27],v[122:123]\nv_pk_add_f32 v[100:101],v[100:101],v[90:91]\nv_pk_fma_f32 v[102:103],v[90:91],v[90:91],v[102:103]\n" \
"s_waitcnt vmcnt(8)\nds_read_b64_tr_b16 v[208:209],v15 offset:12288\nds_read_b64_tr_b16 v[212:213],v15 offset:12800\nds_read_b64_tr_b16 v[210:211],v15 offset:13312\n" \
"ds_read_b64_tr_b16 v[214:215],v15 offset:13824\nds_read_b64_tr_b16 v[216:217],v15 offset:14336\nds_read_b64_tr_b16 v[220:221],v15 offset:14848\nds_read_b64_tr_b16 v[218:219],v15 offset:15360\n" \
"ds_read_b64_tr_b16 v[222:223],v15 offset:15872\ns_waitcnt lgkmcnt(0)\nbuffer_load_dwordx4 v4,s[40:43],s100 offen lds\nv_pk_add_f32 v[124:125],v[124:125],s[24:25] neg_lo:[0,1] neg_hi:[0,1]\n" \
"v_pk_fma_f32 v[92:93],v[92:93],v[28:29],v[124:125]\nv_pk_add_f32 v[100:101],v[100:101],v[92:93]\nv_pk_fma_f32 v[102:103],v[92:93],v[92:93],v[102:103]\nv_pk_add_f32 v[126:127],v[126:127],s[24:25] neg_lo:[0,1] neg_hi:[0,1]\n" \
"v_pk_fma_f32 v[94:95],v[94:95],v[30:31],v[126:127]\nv_pk_add_f32 v[100:101],v[100:101],v[94:95]\nbuffer_load_dwordx4 v5,s[40:43],s100 offen offset:1024 lds\nv_pk_fma_f32 v[102:103],v[94:95],v[94:95],v[102:103]\n" \
"v_pk_add_f32 v[98:99],v[98:99],s[24:25] neg_lo:[0,1] neg_hi:[0,1]\nv_pk_fma_f32 v[96:97],v[96:97],v[58:59],v[98:99]\nv_pk_add_f32 v[100:101],v[100:101],v[96:97]\nv_pk_fma_f32 v[102:103],v[96:97],v[96:97],v[102:103]\n" \
"v_add_f32 v100,v100,v101\nv_add_f32 v102,v102,v103\nv_pk_add_f32 v[62:63],v[62:63],v[60:61] op_sel_hi:[1,0]\nbuffer_load_dwordx4 v6,s[40:43],s100 offen offset:2048 lds\n" \
"v_permlane16_swap_b32 v100,v102\nv_add_f32_e64 v100,v100,v102\nv_cvt_pk_bf16_f32 v32,v62,v63\nv_pk_add_f32 v[64:65],v[64:65],v[60:61] op_sel_hi:[1,0]\n" \
"v_add_f32_dpp v100,v100,v100 quad_perm:[1,0,3,2] row_mask:0xf bank_mask:0xf bound_ctrl:1\nv_cvt_pk_bf16_f32 v33,v64,v65\nv_pk_add_f32 v[66:67],v[66:67],v[60:61] op_sel_hi:[1,0]\nbuffer_load_dwordx4 v7,s[40:43],s100 offen offset:3072 lds\n" \
"v_add_f32_dpp v100,v100,v100 quad_perm:[2,3,0,1] row_mask:0xf bank_mask:0xf bound_ctrl:1\ns_add_u32 m0,s96,0\ns_and_b32 s96,s31,1\nv_add_f32_dpp v100,v100,v100 row_half_mirror row_mask:0xf bank_mask:0xf bound_ctrl:1\n" \
"s_lshr_b32 s31,s31,1\ns_lshl_b32 s31,s31,10\nv_add_f32_dpp v100,v100,v100 row_mirror row_mask:0xf bank_mask:0xf bound_ctrl:1\nv_mul_f32 v104,0x3ae38e39,v100\n" \
"s_add_u32 s31,s31,s96\ns_lshl_b32 s31,s31,7\ns_add_u32 s31,s31,s92\nbuffer_load_dwordx4 v4,s[40:43],s31 offen lds\n" \
"v_mul_f32 v102,0x3ae38e39,v100\ns_lshr_b32 s100,s33,3\ns_add_u32 s100,s100,3\nv_permlane16_swap_b32 v104,v102\n" \
"v_fma_f32 v102,-v104,v104,v102\nv_max_f32 v102,0,v102\nv_add_f32 v102,0x3727c5ac,v102\nbuffer_load_dwordx4 v5,s[40:43],s31 offen offset:1024 lds\n" \
"v_rsq_f32 v100,v102\ns_sub_u32 s101,s100,4\nv_mul_f32 v101,-v104,v100\nv_pk_fma_f32 v[80:81],v[80:81],v[100:101],v[100:101] op_sel:[0,0,1] op_sel_hi:[1,0,1]\n" \
"v_pk_fma_f32 v[82:83],v[82:83],v[100:101],v[100:101] op_sel:[0,0,1] op_sel_hi:[1,0,1]\nv_pk_fma_f32 v[84:85],v[84:85],v[100:101],v[100:101] op_sel:[0,0,1] op_sel_hi:[1,0,1]\nv_pk_fma_f32 v[86:87],v[86:87],v[100:101],v[100:101] op_sel:[0,0,1] op_sel_hi:[1,0,1]\nbuffer_load_dwordx4 v6,s[40:43],s31 offen offset:2048 lds\n" \
"v_cvt_pk_bf16_f32 v16,v80,v81\nv_cvt_pk_bf16_f32 v17,v82,v83\nv_cvt_pk_bf16_f32 v18,v84,v85\nv_cvt_pk_bf16_f32 v19,v86,v87\n" \
"v_pk_fma_f32 v[88:89],v[88:89],v[100:101],v[100:101] op_sel:[0,0,1] op_sel_hi:[1,0,1]\nv_pk_fma_f32 v[90:91],v[90:91],v[100:101],v[100:101] op_sel:[0,0,1] op_sel_hi:[1,0,1]\nv_pk_fma_f32 v[92:93],v[92:93],v[100:101],v[100:101] op_sel:[0,0,1] op_sel_hi:[1,0,1]\nbuffer_load_dwordx4 v7,s[40:43],s31 offen offset:3072 lds\n" \
"v_pk_fma_f32 v[94:95],v[94:95],v[100:101],v[100:101] op_sel:[0,0,1] op_sel_hi:[1,0,1]\nv_cvt_pk_bf16_f32 v20,v88,v89\nv_cvt_pk_bf16_f32 v21,v90,v91\nv_cvt_pk_bf16_f32 v22,v92,v93\n" \
"v_cvt_pk_bf16_f32 v23,v94,v95\nv_pk_fma_f32 v[96:97],v[96:97],v[100:101],v[100:101] op_sel:[0,0,1] op_sel_hi:[1,0,1]\nv_cvt_pk_bf16_f32 v24,v96,v97\ns_add_u32 m0,s95,0\n" \
"s_and_b32 s95,s97,1\ns_lshr_b32 s97,s97,1\ns_lshl_b32 s97,s97,10\ns_add_u32 s97,s97,s95\n" \
"s_lshl_b32 s97,s97,7\ns_add_u32 s97,s97,s92\nbuffer_load_dwordx4 v4,s[40:43],s97 offen lds\ns_min_u32 s100,s100,s101\n" \
"s_lshl_b32 s101,s100,12\ns_add_u32 s101,s101,s57\nv_cvt_pk_bf16_f32 v34,v66,v67\nv_pk_add_f32 v[68:69],v[68:69],v[60:61] op_sel_hi:[1,0]\n" \
"v_cvt_pk_bf16_f32 v35,v68,v69\nv_pk_add_f32 v[70:71],v[70:71],v[60:61] op_sel_hi:[1,0]\nv_cvt_pk_bf16_f32 v36,v70,v71\nbuffer_load_dwordx4 v5,s[40:43],s97 offen offset:1024 lds\n" \
"v_pk_add_f32 v[72:73],v[72:73],v[60:61] op_sel_hi:[1,0]\nv_cvt_pk_bf16_f32 v37,v72,v73\nv_pk_add_f32 v[74:75],v[74:75],v[60:61] op_sel_hi:[1,0]\nv_cvt_pk_bf16_f32 v38,v74,v75\n" \
"v_pk_add_f32 v[76:77],v[76:77],v[60:61] op_sel_hi:[1,0]\nv_cvt_pk_bf16_f32 v39,v76,v77\nv_pk_add_f32 v[78:79],v[78:79],v[60:61] op_sel_hi:[1,0]\nbuffer_load_dwordx4 v6,s[40:43],s97 offen offset:2048 lds\n" \
"v_cvt_pk_bf16_f32 v40,v78,v79\nv_and_b32 v48,31,v0\nv_mad_u32_u24 v49,v48,56,0\nv_sub_u32 v48,v13,v49\n" \
"ds_write_b128 v48,v[16:19]\nbuffer_load_dwordx4 v7,s[40:43],s97 offen offset:3072 lds\nds_write_b128 v48,v[20:23] offset:512\ns_add_u32 m0,s101,0\n" \
"s_and_b32 s101,s100,1\ns_lshr_b32 s100,s100,1\ns_lshl_b32 s100,s100,10\ns_add_u32 s100,s100,s101\n" \
"s_lshl_b32 s100,s100,7\ns_add_u32 s100,s100,s92\nbuffer_load_dwordx4 v4,s[40:43],s100 offen lds\nv_and_b32 v49,31,v0\n" \
"v_mul_u32_u24 v49,68,v49\nv_sub_u32 v49,v13,v49\nds_write_b32 v49,v24 offset:1024\nv_bfe_u32 v50,v0,2,2\n" \
"v_mul_u32_u24 v50,4896,v50\nv_bfe_u32 v51,v0,4,2\nv_lshl_add_u32 v51,v51,2,s3\nbuffer_load_dwordx4 v5,s[40:43],s100 offen offset:1024 lds\n" \
"v_lshl_add_u32 v50,v51,5,v50\nv_and_b32 v52,3,v0\nv_lshl_add_u32 v50,v52,3,v50\nv_and_b32 v52,0xf0,v1\n" \
"v_lshl_add_u32 v51,v51,16,v52\nv_bfrev_b32 v52,1\nv_cmp_gt_u32 vcc,256,v1\nv_and_b32 v53,31,v0\n" \
"buffer_load_dwordx4 v6,s[40:43],s100 offen offset:2048 lds\ns_waitcnt lgkmcnt(0)\ns_barrier\nds_read_b64_tr_b16 v[112:113],v50 offset:9216\n" \
"ds_read_b64_tr_b16 v[114:115],v50 offset:11664\nds_read_b64_tr_b16 v[116:117],v50 offset:9728\nds_read_b64_tr_b16 v[118:119],v50 offset:12176\nds_read_b64_tr_b16 v[120:121],v50 offset:10240\n" \
"ds_read_b64_tr_b16 v[122:123],v50 offset:12688\nbuffer_load_dwordx4 v7,s[40:43],s100 offen offset:3072 lds\nv_cndmask_b32 v52,v52,v51,vcc\nv_mul_u32_u24 v53,12,v53\n" \
"v_sub_u32 v53,v12,v53\ns_lshl_b32 s24,s34,8\ns_add_u32 s24,s24,9764864\ns_add_u32 s25,s24,1048576\n" \
"s_add_u32 s26,s24,2097152\ns_add_u32 s13,s13,7405568\ns_load_dwordx2 s[4:5],s[0:1],56\ns_load_dwordx2 s[16:17],s[0:1],64\n" \
"s_waitcnt lgkmcnt(0)\nbuffer_store_dwordx4 v[112:115],v51,s[36:39],s24 offen sc1\nbuffer_store_dwordx4 v[116:119],v51,s[36:39],s25 offen sc1\nbuffer_store_dwordx4 v[120:123],v52,s[36:39],s26 offen sc1\n" \
"buffer_store_dwordx4 v[32:35],v12,s[36:39],s13 offen sc1\nbuffer_store_dwordx4 v[36:39],v12,s[36:39],s13 offen offset:512 sc1\nbuffer_store_dword v40,v53,s[36:39],s13 offen offset:1024 sc1\ns_lshr_b32 s31,s33,3\n" \
"s_lshl1_add_u32 s31,s31,0\ns_sub_u32 s96,s31,7\ns_min_u32 s31,s31,s96\ns_lshl_b32 s96,s31,12\n" \
"s_add_u32 s96,s96,s57\ns_add_u32 m0,s96,0\ns_and_b32 s96,s31,1\ns_lshr_b32 s31,s31,1\n" \
"s_lshl_b32 s31,s31,10\ns_add_u32 s31,s31,s96\ns_lshl_b32 s31,s31,7\ns_add_u32 s31,s31,s92\n" \
"s_add_u32 s31,s31,262144\ns_lshr_b32 s97,s33,3\ns_lshl1_add_u32 s97,s97,1\ns_sub_u32 s95,s97,7\n" \
"s_min_u32 s97,s97,s95\ns_lshl_b32 s95,s97,12\ns_add_u32 s95,s95,s57\ns_lshr_b32 s100,s33,3\n" \
"s_lshl1_add_u32 s100,s100,2\ns_sub_u32 s101,s100,7\ns_min_u32 s100,s100,s101\ns_lshl_b32 s101,s100,12\n" \
"s_add_u32 s101,s101,s57\nv_and_b32 v64,63,v0\ns_and_b32 s25,s33,7\nv_lshrrev_b32 v66,5,v64\n" \
"v_and_b32 v67,1,v0\ns_lshl2_add_u32 s25,s25,s3\ns_and_b32 s26,s33,6\ns_lshr_b32 s27,s3,1\n" \
"v_lshl_add_u32 v66,v66,1,v67\ns_mul_i32 s24,s32,1152\ns_lshl_b32 s25,s25,5\ns_add_u32 s26,s26,s27\n" \
"s_mov_b32 s6,9216\ns_mov_b32 s7,0x20000\nv_lshlrev_b32 v68,3,v66\ns_add_u32 s25,s25,s24\n" \
"v_bfe_u32 v72,v0,1,1\nv_lshrrev_b32 v74,2,v64\ns_lshl4_add_u32 s26,s26,s24\nv_and_b32 v73,12,v0\n" \
"v_lshrrev_b32 v71,4,v64\nv_and_b32 v69,7,v0\nbuffer_load_dwordx2 v[80:81],v68,s[4:7],s25 offen\nv_lshl_add_u32 v72,v72,2,s21\n" \
"v_and_b32 v74,12,v74\nv_add_u32 v71,v71,v73\ns_lshl_b32 s24,s3,4\nv_lshlrev_b32 v70,1,v69\n" \
"v_lshl_add_u32 v72,v72,4,v74\ns_mov_b32 s15,s19\nv_add_u32 v71,s24,v71\nbuffer_load_ushort v82,v70,s[4:7],s26 offen offset:1024\n" \
"s_mov_b32 s18,4096\ns_mov_b32 s19,0x20000\nv_lshlrev_b32 v74,1,v71\ns_lshl_b32 s24,s22,8\n" \
"buffer_load_ushort v83,v74,s[16:19],s24 offen\nv_add_u32 v72,s3,v72\nbuffer_load_ushort v84,v74,s[16:19],s24 offen offset:128\nv_lshlrev_b32_e64 v75,1,v72\n" \
"buffer_load_ushort v85,v75,s[16:19],s24 offen\nv_lshl_add_u32 v73,v67,1,v73\nv_mul_u32_u24 v91,1152,v72\nv_lshl_add_u32 v91,v73,1,v91\n" \
"v_lshlrev_b32_e64 v73,2,v73\nv_add_u32 v73,163712,v73\nds_read_b64 v[86:87],v73\ns_mul_i32 s24,s22,147456\n" \
"s_mul_i32 s25,s20,288\nv_bfe_u32 v58,v0,1,1\ns_add_u32 s24,s24,s25\nv_mul_u32_u24 v58,544,v58\n" \
"v_bfe_u32 v75,v0,2,1\ns_lshl_b32 s25,s21,6\nv_lshl_add_u32 v58,v75,8,v58\ns_add_u32 s25,s25,s24\n" \
"v_bfe_u32 v75,v0,3,2\ns_add_u32 s24,s24,256\nv_lshl_add_u32 v58,v75,6,v58\nv_add_u32 v91,s24,v91\n" \
"v_mul_u32_u24 v90,1152,v71\nv_and_b32 v74,3,v0\nv_lshl_add_u32 v58,v66,4,v58\ns_mul_i32 s24,s3,1056\n" \
"v_lshl_add_u32 v90,v74,3,v90\nv_add_u32 v58,s24,v58\nv_add_u32 v90,s25,v90\ns_and_b32 s24,s3,1\n" \
"v_mul_u32_u24 v60,288,v75\ns_lshl_b32 s24,s24,2\ns_lshr_b32 s25,s3,1\nv_lshrrev_b32 v75,5,v64\n" \
"s_add_u32 s24,s24,s25\nv_lshl_add_u32 v60,v75,6,v60\ns_lshl_b32 s24,s24,5\nv_lshl_add_u32 v60,v69,2,v60\n" \
"s_add_u32 s24,s24,4224\nv_add_u32 v60,s24,v60\ns_bfe_u32 s24,s33,0x10001\ns_add_u32 s24,s24,s15\n" \
"s_add_u32 s24,s24,17\ns_lshl2_add_u32 s24,s24,s3\ns_waitcnt lgkmcnt(0)\nv_lshrrev_b32 v86,16,v86\n" \
"s_lshl_b32 s24,s24,7\nv_mov_b32 v92,v4\nv_mov_b32 v93,v5\nv_add_u32 v74,s57,v1\n" \
"v_or_b32 v95,v86,v87\nv_lshl_add_u32 v62,v64,1,s24\nv_bfrev_b32 v76,1\nv_cmp_eq_u32 vcc,0,v64\n" \
"s_lshl2_add_u32 s24,s34,s3\ns_lshl2_add_u32 s24,s24,65536\nv_cndmask_b32 v76,v76,0,vcc\nv_mov_b32 v77,1\n" \
"s_barrier\nv_mov_b32 v57,0xc0135761\nv_add_u32 v59,0x80000,v1\nv_lshl_add_u32 v61,v64,3,s23\n" \
"v_mov_b32 v63,0\ns_waitcnt vmcnt(31)\nds_read_b64_tr_b16 v[224:225],v15 offset:16384\nds_read_b64_tr_b16 v[228:229],v15 offset:16896\n" \
"ds_read_b64_tr_b16 v[226:227],v15 offset:17408\nds_read_b64_tr_b16 v[230:231],v15 offset:17920\nds_read_b64_tr_b16 v[232:233],v15 offset:18432\nds_read_b64_tr_b16 v[236:237],v15 offset:18944\n" \
"ds_read_b64_tr_b16 v[234:235],v15 offset:19456\nds_read_b64_tr_b16 v[238:239],v15 offset:19968\ns_waitcnt vmcnt(27)\nds_read_b64_tr_b16 v[240:241],v15 offset:20480\n" \
"ds_read_b64_tr_b16 v[244:245],v15 offset:20992\nds_read_b64_tr_b16 v[242:243],v15 offset:21504\nds_read_b64_tr_b16 v[246:247],v15 offset:22016\nds_read_b64_tr_b16 v[248:249],v15 offset:22528\n" \
"v_bfe_u32 v100,v0,4,2\nds_read_b64_tr_b16 v[252:253],v15 offset:23040\nds_read_b64_tr_b16 v[250:251],v15 offset:23552\nv_bfe_u32 v101,v0,2,2\n" \
"v_lshl_add_u32 v100,v100,3,v101\nv_bfe_u32 v101,v0,3,2\nv_lshl_add_u32 v100,v100,2,v101\nv_and_b32 v101,3,v0\n" \
"v_lshl_add_u32 v100,v100,2,v101\nv_lshlrev_b32 v100,3,v100\nv_xor_b32 v8,0,v100\nv_add_u32 v8,s57,v8\n" \
"v_mov_b32 v88,v8\ns_waitcnt vmcnt(5)\nbuffer_store_dword v77,v76,s[36:39],s24 offen sc1\nds_read_b64_tr_b16 v[254:255],v15 offset:24064\n" \
"v_xor_b32 v9,32,v100\nv_add_u32 v9,s57,v9\nv_xor_b32 v10,64,v100\nv_add_u32 v10,s57,v10\n" \
"v_xor_b32 v11,96,v100\nv_add_u32 v11,s57,v11\ns_waitcnt vmcnt(5)\nv_and_b32 v53,0xffff0000,v80\n" \
"v_and_b32 v55,0xffff0000,v81\nv_lshlrev_b32 v52,16,v80\nv_lshlrev_b32 v54,16,v81\ns_waitcnt vmcnt(4)\n" \
"v_lshlrev_b32 v56,16,v82\ns_waitcnt vmcnt(2)\nv_lshl_or_b32 v89,v84,16,v83\nds_write_b128 v74,v[88:91] offset:28672\n" \
"s_waitcnt vmcnt(1)\nv_lshlrev_b32_e64 v94,16,v85\nds_write_b128 v74,v[92:95] offset:29696\nds_read_b64_tr_b16 v[128:129],v8\n" \
"ds_read_b64_tr_b16 v[130:131],v8 offset:512\nds_read_b64_tr_b16 v[132:133],v9\nds_read_b64_tr_b16 v[134:135],v9 offset:512\nds_read_b64_tr_b16 v[136:137],v10\n" \
"ds_read_b64_tr_b16 v[138:139],v10 offset:512\nds_read_b64_tr_b16 v[140:141],v11\nds_read_b64_tr_b16 v[142:143],v11 offset:512\nds_read_b64_tr_b16 v[144:145],v8 offset:4096\n" \
"ds_read_b64_tr_b16 v[146:147],v8 offset:4608\nds_read_b64_tr_b16 v[148:149],v9 offset:4096\nds_read_b64_tr_b16 v[150:151],v9 offset:4608\nds_read_b64_tr_b16 v[152:153],v10 offset:4096\n" \
"ds_read_b64_tr_b16 v[154:155],v10 offset:4608\nds_read_b64_tr_b16 v[156:157],v11 offset:4096\nds_read_b64_tr_b16 v[158:159],v11 offset:4608\nds_read_b64_tr_b16 v[160:161],v8 offset:8192\n" \
"ds_read_b64_tr_b16 v[162:163],v8 offset:8704\nds_read_b64_tr_b16 v[164:165],v9 offset:8192\nds_read_b64_tr_b16 v[166:167],v9 offset:8704\nds_read_b64_tr_b16 v[168:169],v10 offset:8192\n" \
"ds_read_b64_tr_b16 v[170:171],v10 offset:8704\nds_read_b64_tr_b16 v[172:173],v11 offset:8192\nds_read_b64_tr_b16 v[174:175],v11 offset:8704\nds_read_b64_tr_b16 v[176:177],v8 offset:12288\n" \
"ds_read_b64_tr_b16 v[178:179],v8 offset:12800\nds_read_b64_tr_b16 v[180:181],v9 offset:12288\nds_read_b64_tr_b16 v[182:183],v9 offset:12800\nds_read_b64_tr_b16 v[184:185],v10 offset:12288\n" \
"ds_read_b64_tr_b16 v[186:187],v10 offset:12800\nds_read_b64_tr_b16 v[188:189],v11 offset:12288\nds_read_b64_tr_b16 v[190:191],v11 offset:12800\ns_waitcnt lgkmcnt(0)\n" \
"buffer_load_dwordx4 v4,s[40:43],s31 offen lds\nbuffer_load_dwordx4 v5,s[40:43],s31 offen offset:1024 lds\nbuffer_load_dwordx4 v6,s[40:43],s31 offen offset:2048 lds\nbuffer_load_dwordx4 v7,s[40:43],s31 offen offset:3072 lds\n" \
"s_add_u32 m0,s95,0\ns_and_b32 s95,s97,1\ns_lshr_b32 s97,s97,1\ns_lshl_b32 s97,s97,10\n" \
"s_add_u32 s97,s97,s95\ns_lshl_b32 s97,s97,7\ns_add_u32 s97,s97,s92\ns_add_u32 s97,s97,262144\n" \
"buffer_load_dwordx4 v4,s[40:43],s97 offen lds\nbuffer_load_dwordx4 v5,s[40:43],s97 offen offset:1024 lds\nbuffer_load_dwordx4 v6,s[40:43],s97 offen offset:2048 lds\nbuffer_load_dwordx4 v7,s[40:43],s97 offen offset:3072 lds\n" \
"s_add_u32 m0,s101,0\ns_and_b32 s101,s100,1\ns_lshr_b32 s100,s100,1\ns_lshl_b32 s100,s100,10\n" \
"s_add_u32 s100,s100,s101\ns_lshl_b32 s100,s100,7\ns_add_u32 s100,s100,s92\ns_add_u32 s100,s100,262144\n" \
"buffer_load_dwordx4 v4,s[40:43],s100 offen lds\nbuffer_load_dwordx4 v5,s[40:43],s100 offen offset:1024 lds\nbuffer_load_dwordx4 v6,s[40:43],s100 offen offset:2048 lds\nbuffer_load_dwordx4 v7,s[40:43],s100 offen offset:3072 lds\n" \
"s_lshr_b32 s31,s33,3\ns_lshl1_add_u32 s31,s31,3\ns_sub_u32 s96,s31,7\ns_min_u32 s31,s31,s96\n" \
"s_lshl_b32 s96,s31,12\ns_add_u32 s96,s96,s57\ns_add_u32 m0,s96,0\ns_and_b32 s96,s31,1\n" \
"s_lshr_b32 s31,s31,1\ns_lshl_b32 s31,s31,10\ns_add_u32 s31,s31,s96\ns_lshl_b32 s31,s31,7\n" \
"s_add_u32 s31,s31,s92\ns_add_u32 s31,s31,262144\nbuffer_load_dwordx4 v4,s[40:43],s31 offen lds\nbuffer_load_dwordx4 v5,s[40:43],s31 offen offset:1024 lds\n" \
"buffer_load_dwordx4 v6,s[40:43],s31 offen offset:2048 lds\nbuffer_load_dwordx4 v7,s[40:43],s31 offen offset:3072 lds\ns_lshr_b32 s97,s33,3\ns_lshl1_add_u32 s97,s97,4\n" \
"s_sub_u32 s95,s97,7\ns_min_u32 s97,s97,s95\ns_lshl_b32 s95,s97,12\ns_add_u32 s95,s95,s57\n" \
"s_add_u32 m0,s95,0\ns_and_b32 s95,s97,1\ns_lshr_b32 s97,s97,1\ns_lshl_b32 s97,s97,10\n" \
"s_add_u32 s97,s97,s95\ns_lshl_b32 s97,s97,7\ns_add_u32 s97,s97,s92\ns_add_u32 s97,s97,262144\n" \
"buffer_load_dwordx4 v4,s[40:43],s97 offen lds\nbuffer_load_dwordx4 v5,s[40:43],s97 offen offset:1024 lds\nbuffer_load_dwordx4 v6,s[40:43],s97 offen offset:2048 lds\nbuffer_load_dwordx4 v7,s[40:43],s97 offen offset:3072 lds\n" \
"s_lshr_b32 s100,s33,3\ns_lshl1_add_u32 s100,s100,5\ns_sub_u32 s101,s100,7\ns_min_u32 s100,s100,s101\n" \
"s_lshl_b32 s101,s100,12\ns_add_u32 s101,s101,s57\ns_add_u32 m0,s101,0\ns_and_b32 s101,s100,1\n" \
"s_lshr_b32 s100,s100,1\ns_lshl_b32 s100,s100,10\ns_add_u32 s100,s100,s101\ns_lshl_b32 s100,s100,7\n" \
"s_add_u32 s100,s100,s92\ns_add_u32 s100,s100,262144\nbuffer_load_dwordx4 v4,s[40:43],s100 offen lds\nbuffer_load_dwordx4 v5,s[40:43],s100 offen offset:1024 lds\n" \
"buffer_load_dwordx4 v6,s[40:43],s100 offen offset:2048 lds\nbuffer_load_dwordx4 v7,s[40:43],s100 offen offset:3072 lds\ns_lshr_b32 s31,s33,3\ns_lshl1_add_u32 s31,s31,6\n" \
"s_sub_u32 s96,s31,7\ns_min_u32 s31,s31,s96\ns_lshl_b32 s96,s31,12\ns_add_u32 s96,s96,s57\n" \
"s_add_u32 m0,s96,0\ns_and_b32 s96,s31,1\ns_lshr_b32 s31,s31,1\ns_lshl_b32 s31,s31,10\n" \
"s_add_u32 s31,s31,s96\ns_lshl_b32 s31,s31,7\ns_add_u32 s31,s31,s92\ns_add_u32 s31,s31,262144\n" \
"buffer_load_dwordx4 v4,s[40:43],s31 offen lds\nbuffer_load_dwordx4 v5,s[40:43],s31 offen offset:1024 lds\nbuffer_load_dwordx4 v6,s[40:43],s31 offen offset:2048 lds\nbuffer_load_dwordx4 v7,s[40:43],s31 offen offset:3072 lds\n" \
".Lgemm_%=:\ns_memrealtime s[94:95]\nbuffer_load_dwordx4 v[64:67],v59,s[36:39],s48 offen sc1\nbuffer_load_dwordx4 v[68:71],v59,s[36:39],s48 offen offset:1024 sc1\n" \
"buffer_load_dwordx4 v[72:75],v59,s[36:39],s48 offen offset:2048 sc1\nbuffer_load_dwordx4 v[76:79],v59,s[36:39],s48 offen offset:3072 sc1\nbuffer_load_dwordx4 v[80:83],v59,s[36:39],s49 offen sc1\nbuffer_load_dwordx4 v[84:87],v59,s[36:39],s49 offen offset:1024 sc1\n" \
"buffer_load_dwordx4 v[88:91],v59,s[36:39],s49 offen offset:2048 sc1\nbuffer_load_dwordx4 v[92:95],v59,s[36:39],s49 offen offset:3072 sc1\nbuffer_load_dwordx4 v[96:99],v59,s[36:39],s50 offen sc1\nbuffer_load_dwordx4 v[100:103],v59,s[36:39],s50 offen offset:1024 sc1\n" \
"buffer_load_dwordx4 v[104:107],v59,s[36:39],s50 offen offset:2048 sc1\nbuffer_load_dwordx4 v[108:111],v59,s[36:39],s50 offen offset:3072 sc1\nbuffer_load_dwordx4 v[112:115],v59,s[36:39],s51 offen sc1\nbuffer_load_dwordx4 v[116:119],v59,s[36:39],s51 offen offset:1024 sc1\n" \
"buffer_load_dwordx4 v[120:123],v59,s[36:39],s51 offen offset:2048 sc1\nbuffer_load_dwordx4 v[124:127],v59,s[36:39],s51 offen offset:3072 sc1\ns_branch .Lz0_%=\n.Lzr_%=:\n" \
"v_and_b32 v0,0x3ff,v59\nv_lshlrev_b32 v0,0,v0\nv_bfrev_b32 v1,1\ns_waitcnt lgkmcnt(0)\n" \
".Lh_%=:\nbuffer_load_dwordx4 v[4:7],v0,s[36:39],s87 offen sc1\ns_memrealtime s[96:97]\ns_waitcnt vmcnt(0) lgkmcnt(0)\n" \
"v_bitop3_b32 v4,v4,v5,v6 bitop3:0x80\nv_and_b32 v4,v4,v7\nv_cmp_ne_u32 vcc,0,v4\nv_max3_u32 v2,v64,v68,v72\n" \
"v_max3_u32 v2,v2,v76,v80\nv_max3_u32 v2,v2,v84,v88\nv_max3_u32 v2,v2,v92,v96\nv_max3_u32 v2,v2,v100,v104\n" \
"v_max3_u32 v2,v2,v108,v112\nv_max3_u32 v2,v2,v116,v120\nv_max_u32 v2,v2,v124\nv_cndmask_b32 v0,v0,v1,vcc\n" \
"v_cmp_eq_u32 vcc,-1,v2\nv_cmp_ne_u32 s[100:101],v0,v1\ns_sub_u32 s96,s96,s94\ns_cbranch_vccz .Lj_%=\n" \
"v_cmpx_eq_u32 vcc,-1,v64\nbuffer_load_dwordx4 v[64:67],v59,s[36:39],s48 offen sc1\ns_mov_b64 exec,-1\nv_cmpx_eq_u32 vcc,-1,v68\n" \
"buffer_load_dwordx4 v[68:71],v59,s[36:39],s48 offen offset:1024 sc1\ns_mov_b64 exec,-1\nv_cmpx_eq_u32 vcc,-1,v72\nbuffer_load_dwordx4 v[72:75],v59,s[36:39],s48 offen offset:2048 sc1\n" \
"s_mov_b64 exec,-1\nv_cmpx_eq_u32 vcc,-1,v76\nbuffer_load_dwordx4 v[76:79],v59,s[36:39],s48 offen offset:3072 sc1\ns_mov_b64 exec,-1\n" \
"v_cmpx_eq_u32 vcc,-1,v80\nbuffer_load_dwordx4 v[80:83],v59,s[36:39],s49 offen sc1\ns_mov_b64 exec,-1\nv_cmpx_eq_u32 vcc,-1,v84\n" \
"buffer_load_dwordx4 v[84:87],v59,s[36:39],s49 offen offset:1024 sc1\ns_mov_b64 exec,-1\nv_cmpx_eq_u32 vcc,-1,v88\nbuffer_load_dwordx4 v[88:91],v59,s[36:39],s49 offen offset:2048 sc1\n" \
"s_mov_b64 exec,-1\nv_cmpx_eq_u32 vcc,-1,v92\nbuffer_load_dwordx4 v[92:95],v59,s[36:39],s49 offen offset:3072 sc1\ns_mov_b64 exec,-1\n" \
"v_cmpx_eq_u32 vcc,-1,v96\nbuffer_load_dwordx4 v[96:99],v59,s[36:39],s50 offen sc1\ns_mov_b64 exec,-1\nv_cmpx_eq_u32 vcc,-1,v100\n" \
"buffer_load_dwordx4 v[100:103],v59,s[36:39],s50 offen offset:1024 sc1\ns_mov_b64 exec,-1\nv_cmpx_eq_u32 vcc,-1,v104\nbuffer_load_dwordx4 v[104:107],v59,s[36:39],s50 offen offset:2048 sc1\n" \
"s_mov_b64 exec,-1\nv_cmpx_eq_u32 vcc,-1,v108\nbuffer_load_dwordx4 v[108:111],v59,s[36:39],s50 offen offset:3072 sc1\ns_mov_b64 exec,-1\n" \
"v_cmpx_eq_u32 vcc,-1,v112\nbuffer_load_dwordx4 v[112:115],v59,s[36:39],s51 offen sc1\ns_mov_b64 exec,-1\nv_cmpx_eq_u32 vcc,-1,v116\n" \
"buffer_load_dwordx4 v[116:119],v59,s[36:39],s51 offen offset:1024 sc1\ns_mov_b64 exec,-1\nv_cmpx_eq_u32 vcc,-1,v120\nbuffer_load_dwordx4 v[120:123],v59,s[36:39],s51 offen offset:2048 sc1\n" \
"s_mov_b64 exec,-1\nv_cmpx_eq_u32 vcc,-1,v124\nbuffer_load_dwordx4 v[124:127],v59,s[36:39],s51 offen offset:3072 sc1\ns_mov_b64 exec,-1\n" \
".Lj_%=:\ns_cmp_gt_u32 s96,0x4c4b40\ns_cbranch_scc1 .Lg_%=\ns_cmp_lg_u64 s[100:101],0\n" \
"s_cbranch_scc1 .Lh_%=\n.Lg_%=:\ns_waitcnt vmcnt(0)\ns_mov_b32 s33,s86\n" \
"s_nop 0\ns_mov_b32 m0,s56\ns_mov_b32 s94,s86\ns_and_b32 s95,s89,7\n" \
"s_andn2_b32 s101,s89,7\ns_mul_i32 s95,s95,409600\ns_add_u32 s95,s95,s52\ns_mov_b32 s86,0x11111111\n" \
"s_mov_b32 s87,0x11111111\nv_add_u32 v59,s85,v59\ns_add_u32 s100,s90,73728\ns_mov_b32 s35,73728\n" \
"s_mov_b32 s34,s91\ns_waitcnt lgkmcnt(0)\ns_cmp_eq_u32 s84,0\ns_cbranch_scc0 .Ls_%=\n" \
".Lf_%=:\ns_waitcnt vmcnt(16)\nv_mfma_f32_16x16x32_bf16 v[0:3],v[64:67],a[0:3],0\nds_read_b32 v44,v60\n" \
"v_add_f32 v42,1.0,v42\nv_mfma_f32_16x16x32_bf16 v[4:7],v[64:67],a[4:7],0\nds_read_b32 v45,v60 offset:9568\nv_add_f32 v43,1.0,v43\n" \
"v_mfma_f32_16x16x32_bf16 v[8:11],v[64:67],a[8:11],0\nds_read_b32 v46,v60 offset:19136\nv_add_u32 v59,0x10000,v59\nv_mfma_f32_16x16x32_bf16 v[12:15],v[64:67],a[12:15],0\n" \
"ds_read_b32 v47,v60 offset:28704\nv_and_b32 v59,0x7ffff,v59\nv_mfma_f32_16x16x32_bf16 v[16:19],v[64:67],v[192:195],0\nbuffer_load_dwordx4 v[64:67],v59,s[36:39],s48 offen sc1\n" \
"s_waitcnt vmcnt(16)\nv_mfma_f32_16x16x32_bf16 v[0:3],v[68:71],a[16:19],v[0:3]\nv_rcp_f32 v40,v40\nv_mfma_f32_16x16x32_bf16 v[4:7],v[68:71],a[20:23],v[4:7]\n" \
"v_rcp_f32 v41,v41\nv_mfma_f32_16x16x32_bf16 v[8:11],v[68:71],a[24:27],v[8:11]\nv_rcp_f32 v42,v42\nv_mfma_f32_16x16x32_bf16 v[12:15],v[68:71],a[28:31],v[12:15]\n" \
"v_rcp_f32 v43,v43\nv_mfma_f32_16x16x32_bf16 v[16:19],v[68:71],v[196:199],v[16:19]\nbuffer_load_dwordx4 v[68:71],v59,s[36:39],s48 offen offset:1024 sc1\ns_waitcnt vmcnt(15)\n" \
"v_mfma_f32_16x16x32_bf16 v[0:3],v[72:75],a[32:35],v[0:3]\nv_mul_f32 v36,v36,v40\nv_mul_f32 v37,v37,v41\nv_mfma_f32_16x16x32_bf16 v[4:7],v[72:75],a[36:39],v[4:7]\n" \
"v_mul_f32 v38,v38,v42\nv_mul_f32 v39,v39,v43\nv_mfma_f32_16x16x32_bf16 v[8:11],v[72:75],a[40:43],v[8:11]\nv_cvt_pk_bf16_f32 v40,v36,v37\n" \
"v_cvt_pk_bf16_f32 v41,v38,v39\nv_mfma_f32_16x16x32_bf16 v[12:15],v[72:75],a[44:47],v[12:15]\nbuffer_store_dwordx2 v[40:41],v61,s[36:39],s52 offen sc1\nds_write_addtid_b32 v20 offset:5344\n" \
"v_mfma_f32_16x16x32_bf16 v[16:19],v[72:75],v[200:203],v[16:19]\nbuffer_load_dwordx4 v[72:75],v59,s[36:39],s48 offen offset:2048 sc1\ns_waitcnt vmcnt(16)\nv_mfma_f32_16x16x32_bf16 v[0:3],v[76:79],a[48:51],v[0:3]\n" \
"ds_write_addtid_b32 v21 offset:5888\ns_waitcnt lgkmcnt(2)\nv_mfma_f32_16x16x32_bf16 v[4:7],v[76:79],a[52:55],v[4:7]\nds_write_addtid_b32 v22 offset:5600\n" \
"v_add_f32 v44,v44,v45\nv_mfma_f32_16x16x32_bf16 v[8:11],v[76:79],a[56:59],v[8:11]\nds_write_addtid_b32 v23 offset:6144\nv_add_f32 v46,v46,v47\n" \
"v_mfma_f32_16x16x32_bf16 v[12:15],v[76:79],a[60:63],v[12:15]\nds_write_addtid_b32 v24 offset:6400\nv_add_f32 v44,v44,v56\nv_mfma_f32_16x16x32_bf16 v[16:19],v[76:79],v[204:207],v[16:19]\n" \
"buffer_load_dwordx4 v[76:79],v59,s[36:39],s48 offen offset:3072 sc1\ns_waitcnt vmcnt(16)\nv_mfma_f32_16x16x32_bf16 v[0:3],v[80:83],a[64:67],v[0:3]\nds_write_addtid_b32 v25 offset:6944\n" \
"v_add_f32 v44,v44,v46\nv_mfma_f32_16x16x32_bf16 v[4:7],v[80:83],a[68:71],v[4:7]\nds_write_addtid_b32 v26 offset:6656\nv_mul_f32 v45,v44,v44\n" \
"v_mfma_f32_16x16x32_bf16 v[8:11],v[80:83],a[72:75],v[8:11]\nds_write_addtid_b32 v27 offset:7200\nv_fma_f32 v45,v45,s55,v57\nv_mfma_f32_16x16x32_bf16 v[12:15],v[80:83],a[76:79],v[12:15]\n" \
"ds_write_addtid_b32 v28 offset:7456\nv_mul_f32 v45,v44,v45\nv_mfma_f32_16x16x32_bf16 v[16:19],v[80:83],v[208:211],v[16:19]\nbuffer_load_dwordx4 v[80:83],v59,s[36:39],s49 offen sc1\n" \
"s_waitcnt vmcnt(16)\nv_mfma_f32_16x16x32_bf16 v[0:3],v[84:87],a[80:83],v[0:3]\nds_write_addtid_b32 v29 offset:8000\nv_mfma_f32_16x16x32_bf16 v[4:7],v[84:87],a[84:87],v[4:7]\n" \
"ds_write_addtid_b32 v30 offset:7712\nv_mfma_f32_16x16x32_bf16 v[8:11],v[84:87],a[88:91],v[8:11]\nds_write_addtid_b32 v31 offset:8256\nv_mfma_f32_16x16x32_bf16 v[12:15],v[84:87],a[92:95],v[12:15]\n" \
"ds_write_addtid_b32 v32 offset:8512\nv_mfma_f32_16x16x32_bf16 v[16:19],v[84:87],v[212:215],v[16:19]\nbuffer_load_dwordx4 v[84:87],v59,s[36:39],s49 offen offset:1024 sc1\ns_waitcnt vmcnt(16)\n" \
"v_mfma_f32_16x16x32_bf16 v[0:3],v[88:91],a[96:99],v[0:3]\nds_write_addtid_b32 v33 offset:9056\nv_mfma_f32_16x16x32_bf16 v[4:7],v[88:91],a[100:103],v[4:7]\nds_write_addtid_b32 v34 offset:8768\n" \
"v_mfma_f32_16x16x32_bf16 v[8:11],v[88:91],a[104:107],v[8:11]\nds_write_addtid_b32 v35 offset:9312\nv_mfma_f32_16x16x32_bf16 v[12:15],v[88:91],a[108:111],v[12:15]\nv_exp_f32 v45,v45\n" \
"v_mfma_f32_16x16x32_bf16 v[16:19],v[88:91],v[216:219],v[16:19]\nbuffer_load_dwordx4 v[88:91],v59,s[36:39],s49 offen offset:2048 sc1\ns_waitcnt vmcnt(16)\nv_mfma_f32_16x16x32_bf16 v[0:3],v[92:95],a[112:115],v[0:3]\n" \
"v_add_f32 v45,1.0,v45\nv_mfma_f32_16x16x32_bf16 v[4:7],v[92:95],a[116:119],v[4:7]\nv_rcp_f32 v45,v45\nv_mfma_f32_16x16x32_bf16 v[8:11],v[92:95],a[120:123],v[8:11]\n" \
"v_mul_f32 v44,v44,v45\nv_cvt_pk_bf16_f32 v45,v44,v44\nv_mfma_f32_16x16x32_bf16 v[12:15],v[92:95],a[124:127],v[12:15]\ns_waitcnt lgkmcnt(0)\n" \
"s_barrier\nv_mfma_f32_16x16x32_bf16 v[16:19],v[92:95],v[220:223],v[16:19]\nbuffer_load_dwordx4 v[92:95],v59,s[36:39],s49 offen offset:3072 sc1\ns_waitcnt vmcnt(16)\n" \
"v_mfma_f32_16x16x32_bf16 v[0:3],v[96:99],a[128:131],v[0:3]\nbuffer_store_short v45,v62,s[36:39],s52 offen sc1\nds_read_b128 v[36:39],v58 offset:5344\nv_mfma_f32_16x16x32_bf16 v[4:7],v[96:99],a[132:135],v[4:7]\n" \
"ds_read_b128 v[40:43],v58 offset:14912\nv_mfma_f32_16x16x32_bf16 v[8:11],v[96:99],a[136:139],v[8:11]\nds_read_b128 v[44:47],v58 offset:24480\nv_mfma_f32_16x16x32_bf16 v[12:15],v[96:99],a[140:143],v[12:15]\n" \
"ds_read_b128 v[48:51],v58 offset:34048\nv_mfma_f32_16x16x32_bf16 v[16:19],v[96:99],v[224:227],v[16:19]\nbuffer_load_dwordx4 v[96:99],v59,s[36:39],s50 offen sc1\ns_waitcnt vmcnt(17)\n" \
"v_mfma_f32_16x16x32_bf16 v[0:3],v[100:103],a[144:147],v[0:3]\ns_cmp_eq_u32 s52,s83\ns_cbranch_scc1 .Lxq5_%=\n.Lyq5_%=:\n" \
"v_mfma_f32_16x16x32_bf16 v[4:7],v[100:103],a[148:151],v[4:7]\nv_mfma_f32_16x16x32_bf16 v[8:11],v[100:103],a[152:155],v[8:11]\nv_mfma_f32_16x16x32_bf16 v[12:15],v[100:103],a[156:159],v[12:15]\nv_mfma_f32_16x16x32_bf16 v[16:19],v[100:103],v[228:231],v[16:19]\n" \
"buffer_load_dwordx4 v[100:103],v59,s[36:39],s50 offen offset:1024 sc1\ns_waitcnt vmcnt(17)\nv_mfma_f32_16x16x32_bf16 v[0:3],v[104:107],a[160:163],v[0:3]\ns_waitcnt lgkmcnt(2)\n" \
"v_add_f32 v36,v36,v40\nv_mfma_f32_16x16x32_bf16 v[4:7],v[104:107],a[164:167],v[4:7]\nv_add_f32 v37,v37,v41\nv_add_f32 v38,v38,v42\n" \
"v_mfma_f32_16x16x32_bf16 v[8:11],v[104:107],a[168:171],v[8:11]\nv_add_f32 v39,v39,v43\nv_add_f32 v36,v36,v52\nv_mfma_f32_16x16x32_bf16 v[12:15],v[104:107],a[172:175],v[12:15]\n" \
"v_add_f32 v37,v37,v53\nv_add_f32 v38,v38,v54\nv_mfma_f32_16x16x32_bf16 v[16:19],v[104:107],v[232:235],v[16:19]\nbuffer_load_dwordx4 v[104:107],v59,s[36:39],s50 offen offset:2048 sc1\n" \
"s_waitcnt vmcnt(17)\nv_mfma_f32_16x16x32_bf16 v[0:3],v[108:111],a[176:179],v[0:3]\nv_add_f32 v39,v39,v55\ns_waitcnt lgkmcnt(0)\n" \
"v_mfma_f32_16x16x32_bf16 v[4:7],v[108:111],a[180:183],v[4:7]\nv_add_f32 v44,v44,v48\nv_add_f32 v45,v45,v49\nv_mfma_f32_16x16x32_bf16 v[8:11],v[108:111],a[184:187],v[8:11]\n" \
"v_add_f32 v46,v46,v50\nv_add_f32 v47,v47,v51\nv_mfma_f32_16x16x32_bf16 v[12:15],v[108:111],a[188:191],v[12:15]\nv_add_f32 v36,v36,v44\n" \
"v_add_f32 v37,v37,v45\nv_mfma_f32_16x16x32_bf16 v[16:19],v[108:111],v[236:239],v[16:19]\nbuffer_load_dwordx4 v[108:111],v59,s[36:39],s50 offen offset:3072 sc1\ns_waitcnt vmcnt(17)\n" \
"v_mfma_f32_16x16x32_bf16 v[0:3],v[112:115],a[192:195],v[0:3]\nv_add_f32 v38,v38,v46\nv_add_f32 v39,v39,v47\nv_mfma_f32_16x16x32_bf16 v[4:7],v[112:115],a[196:199],v[4:7]\n" \
"v_mul_f32 v40,v36,v36\nv_mul_f32 v41,v37,v37\nv_mfma_f32_16x16x32_bf16 v[8:11],v[112:115],a[200:203],v[8:11]\nv_mul_f32 v42,v38,v38\n" \
"v_mul_f32 v43,v39,v39\nv_mfma_f32_16x16x32_bf16 v[12:15],v[112:115],a[204:207],v[12:15]\nv_fma_f32 v40,v40,s55,v57\nv_fma_f32 v41,v41,s55,v57\n" \
"v_mfma_f32_16x16x32_bf16 v[16:19],v[112:115],v[240:243],v[16:19]\nbuffer_load_dwordx4 v[112:115],v59,s[36:39],s51 offen sc1\ns_waitcnt vmcnt(17)\nv_mfma_f32_16x16x32_bf16 v[0:3],v[116:119],a[208:211],v[0:3]\n" \
"v_fma_f32 v42,v42,s55,v57\nv_fma_f32 v43,v43,s55,v57\nv_mfma_f32_16x16x32_bf16 v[4:7],v[116:119],a[212:215],v[4:7]\nv_mul_f32 v40,v36,v40\n" \
"v_mul_f32 v41,v37,v41\nv_mfma_f32_16x16x32_bf16 v[8:11],v[116:119],a[216:219],v[8:11]\nv_mul_f32 v42,v38,v42\nv_mul_f32 v43,v39,v43\n" \
"v_mfma_f32_16x16x32_bf16 v[12:15],v[116:119],a[220:223],v[12:15]\nv_exp_f32 v40,v40\nv_mfma_f32_16x16x32_bf16 v[16:19],v[116:119],v[244:247],v[16:19]\nbuffer_load_dwordx4 v[116:119],v59,s[36:39],s51 offen offset:1024 sc1\n" \
"s_waitcnt vmcnt(17)\nv_mfma_f32_16x16x32_bf16 v[0:3],v[120:123],a[224:227],v[0:3]\nv_exp_f32 v41,v41\nv_mfma_f32_16x16x32_bf16 v[4:7],v[120:123],a[228:231],v[4:7]\n" \
"v_exp_f32 v42,v42\nv_mfma_f32_16x16x32_bf16 v[8:11],v[120:123],a[232:235],v[8:11]\nv_exp_f32 v43,v43\nv_mfma_f32_16x16x32_bf16 v[12:15],v[120:123],a[236:239],v[12:15]\n" \
"v_add_f32 v40,1.0,v40\nv_add_f32 v41,1.0,v41\nv_mfma_f32_16x16x32_bf16 v[16:19],v[120:123],v[248:251],v[16:19]\nbuffer_load_dwordx4 v[120:123],v59,s[36:39],s51 offen offset:2048 sc1\n" \
"s_waitcnt vmcnt(17)\nv_mfma_f32_16x16x32_bf16 v[0:3],v[124:127],a[240:243],v[0:3]\nv_add_f32 v42,1.0,v42\nv_add_f32 v43,1.0,v43\n" \
"v_mfma_f32_16x16x32_bf16 v[4:7],v[124:127],a[244:247],v[4:7]\ns_cmp_eq_u32 s52,s95\ns_cbranch_scc1 .Lxt5_%=\n.Lyt5_%=:\n" \
"v_mfma_f32_16x16x32_bf16 v[8:11],v[124:127],a[248:251],v[8:11]\nv_rcp_f32 v40,v40\nv_mfma_f32_16x16x32_bf16 v[12:15],v[124:127],a[252:255],v[12:15]\nv_rcp_f32 v41,v41\n" \
"v_mfma_f32_16x16x32_bf16 v[16:19],v[124:127],v[252:255],v[16:19]\nbuffer_load_dwordx4 v[124:127],v59,s[36:39],s51 offen offset:3072 sc1\ns_add_u32 s52,s52,409600\ns_cmp_eq_u32 s52,s53\n" \
"s_cbranch_scc1 .Lls_%=\n.Ls_%=:\ns_waitcnt vmcnt(17)\nv_mfma_f32_16x16x32_bf16 v[20:23],v[64:67],a[0:3],0\n" \
"v_rcp_f32 v42,v42\nv_mfma_f32_16x16x32_bf16 v[24:27],v[64:67],a[4:7],0\nv_add_u32 v59,0x10000,v59\nds_write_addtid_b32 v0\n" \
"v_mfma_f32_16x16x32_bf16 v[28:31],v[64:67],a[8:11],0\nv_and_b32 v59,0x7ffff,v59\nds_write_addtid_b32 v1 offset:544\nv_mfma_f32_16x16x32_bf16 v[32:35],v[64:67],a[12:15],0\n" \
"buffer_load_dwordx4 v[64:67],v59,s[36:39],s48 offen sc1\ns_waitcnt vmcnt(17)\nv_mfma_f32_16x16x32_bf16 v[20:23],v[68:71],a[16:19],v[20:23]\nv_rcp_f32 v43,v43\n" \
"v_mfma_f32_16x16x32_bf16 v[24:27],v[68:71],a[20:23],v[24:27]\nv_mul_f32 v36,v36,v40\nv_mul_f32 v37,v37,v41\nv_mfma_f32_16x16x32_bf16 v[28:31],v[68:71],a[24:27],v[28:31]\n" \
"v_mul_f32 v38,v38,v42\nv_mul_f32 v39,v39,v43\nv_mfma_f32_16x16x32_bf16 v[32:35],v[68:71],a[28:31],v[32:35]\nbuffer_load_dwordx4 v[68:71],v59,s[36:39],s48 offen offset:1024 sc1\n" \
"s_waitcnt vmcnt(16)\nv_mfma_f32_16x16x32_bf16 v[20:23],v[72:75],a[32:35],v[20:23]\nds_write_addtid_b32 v2 offset:256\nv_cvt_pk_bf16_f32 v40,v36,v37\n" \
"v_mfma_f32_16x16x32_bf16 v[24:27],v[72:75],a[36:39],v[24:27]\nds_write_addtid_b32 v3 offset:800\nv_cvt_pk_bf16_f32 v41,v38,v39\nv_mfma_f32_16x16x32_bf16 v[28:31],v[72:75],a[40:43],v[28:31]\n" \
"ds_write_addtid_b32 v4 offset:1056\nbuffer_store_dwordx2 v[40:41],v61,s[36:39],s52 offen sc1\nv_mfma_f32_16x16x32_bf16 v[32:35],v[72:75],a[44:47],v[32:35]\nbuffer_load_dwordx4 v[72:75],v59,s[36:39],s48 offen offset:2048 sc1\n" \
"s_waitcnt vmcnt(17)\nv_mfma_f32_16x16x32_bf16 v[20:23],v[76:79],a[48:51],v[20:23]\nds_write_addtid_b32 v5 offset:1600\nv_mfma_f32_16x16x32_bf16 v[24:27],v[76:79],a[52:55],v[24:27]\n" \
"ds_write_addtid_b32 v6 offset:1312\nv_mfma_f32_16x16x32_bf16 v[28:31],v[76:79],a[56:59],v[28:31]\nds_write_addtid_b32 v7 offset:1856\nv_mfma_f32_16x16x32_bf16 v[32:35],v[76:79],a[60:63],v[32:35]\n" \
"buffer_load_dwordx4 v[76:79],v59,s[36:39],s48 offen offset:3072 sc1\ns_waitcnt vmcnt(17)\nv_mfma_f32_16x16x32_bf16 v[20:23],v[80:83],a[64:67],v[20:23]\nds_write_addtid_b32 v8 offset:2112\n" \
"v_mfma_f32_16x16x32_bf16 v[24:27],v[80:83],a[68:71],v[24:27]\nds_write_addtid_b32 v9 offset:2656\nv_mfma_f32_16x16x32_bf16 v[28:31],v[80:83],a[72:75],v[28:31]\nds_write_addtid_b32 v10 offset:2368\n" \
"v_mfma_f32_16x16x32_bf16 v[32:35],v[80:83],a[76:79],v[32:35]\nbuffer_load_dwordx4 v[80:83],v59,s[36:39],s49 offen sc1\ns_waitcnt vmcnt(17)\nv_mfma_f32_16x16x32_bf16 v[20:23],v[84:87],a[80:83],v[20:23]\n" \
"ds_write_addtid_b32 v11 offset:2912\nv_mfma_f32_16x16x32_bf16 v[24:27],v[84:87],a[84:87],v[24:27]\nds_write_addtid_b32 v12 offset:3168\nv_mfma_f32_16x16x32_bf16 v[28:31],v[84:87],a[88:91],v[28:31]\n" \
"ds_write_addtid_b32 v13 offset:3712\nv_mfma_f32_16x16x32_bf16 v[32:35],v[84:87],a[92:95],v[32:35]\nbuffer_load_dwordx4 v[84:87],v59,s[36:39],s49 offen offset:1024 sc1\ns_waitcnt vmcnt(17)\n" \
"v_mfma_f32_16x16x32_bf16 v[20:23],v[88:91],a[96:99],v[20:23]\nds_write_addtid_b32 v14 offset:3424\nv_mfma_f32_16x16x32_bf16 v[24:27],v[88:91],a[100:103],v[24:27]\nds_write_addtid_b32 v15 offset:3968\n" \
"v_mfma_f32_16x16x32_bf16 v[28:31],v[88:91],a[104:107],v[28:31]\nds_write_addtid_b32 v16 offset:4224\nv_mfma_f32_16x16x32_bf16 v[32:35],v[88:91],a[108:111],v[32:35]\nbuffer_load_dwordx4 v[88:91],v59,s[36:39],s49 offen offset:2048 sc1\n" \
"s_waitcnt vmcnt(17)\nv_mfma_f32_16x16x32_bf16 v[20:23],v[92:95],a[112:115],v[20:23]\nds_write_addtid_b32 v17 offset:4512\nv_mfma_f32_16x16x32_bf16 v[24:27],v[92:95],a[116:119],v[24:27]\n" \
"ds_write_addtid_b32 v18 offset:4800\nv_mfma_f32_16x16x32_bf16 v[28:31],v[92:95],a[120:123],v[28:31]\nds_write_addtid_b32 v19 offset:5088\nv_mfma_f32_16x16x32_bf16 v[32:35],v[92:95],a[124:127],v[32:35]\n" \
"buffer_load_dwordx4 v[92:95],v59,s[36:39],s49 offen offset:3072 sc1\ns_waitcnt vmcnt(16)\nv_mfma_f32_16x16x32_bf16 v[20:23],v[96:99],a[128:131],v[20:23]\nv_mfma_f32_16x16x32_bf16 v[24:27],v[96:99],a[132:135],v[24:27]\n" \
"v_mfma_f32_16x16x32_bf16 v[28:31],v[96:99],a[136:139],v[28:31]\nv_mfma_f32_16x16x32_bf16 v[32:35],v[96:99],a[140:143],v[32:35]\nbuffer_load_dwordx4 v[96:99],v59,s[36:39],s50 offen sc1\ns_waitcnt vmcnt(16)\n" \
"v_mfma_f32_16x16x32_bf16 v[20:23],v[100:103],a[144:147],v[20:23]\ns_waitcnt lgkmcnt(0)\ns_barrier\nv_mfma_f32_16x16x32_bf16 v[24:27],v[100:103],a[148:151],v[24:27]\n" \
"ds_read_b128 v[36:39],v58\nv_mfma_f32_16x16x32_bf16 v[28:31],v[100:103],a[152:155],v[28:31]\nds_read_b128 v[40:43],v58 offset:9568\nv_mfma_f32_16x16x32_bf16 v[32:35],v[100:103],a[156:159],v[32:35]\n" \
"buffer_load_dwordx4 v[100:103],v59,s[36:39],s50 offen offset:1024 sc1\ns_waitcnt vmcnt(16)\nv_mfma_f32_16x16x32_bf16 v[20:23],v[104:107],a[160:163],v[20:23]\nds_read_b128 v[44:47],v58 offset:19136\n" \
"v_mfma_f32_16x16x32_bf16 v[24:27],v[104:107],a[164:167],v[24:27]\nds_read_b128 v[48:51],v58 offset:28704\nv_mfma_f32_16x16x32_bf16 v[28:31],v[104:107],a[168:171],v[28:31]\ns_cmp_eq_u32 s52,s83\n" \
"s_cbranch_scc1 .Lxq4_%=\n.Lyq4_%=:\nv_mfma_f32_16x16x32_bf16 v[32:35],v[104:107],a[172:175],v[32:35]\nbuffer_load_dwordx4 v[104:107],v59,s[36:39],s50 offen offset:2048 sc1\n" \
"s_waitcnt vmcnt(16)\nv_mfma_f32_16x16x32_bf16 v[20:23],v[108:111],a[176:179],v[20:23]\nv_mfma_f32_16x16x32_bf16 v[24:27],v[108:111],a[180:183],v[24:27]\nv_mfma_f32_16x16x32_bf16 v[28:31],v[108:111],a[184:187],v[28:31]\n" \
"s_waitcnt lgkmcnt(2)\nv_add_f32 v36,v36,v40\nv_add_f32 v37,v37,v41\nv_mfma_f32_16x16x32_bf16 v[32:35],v[108:111],a[188:191],v[32:35]\n" \
"buffer_load_dwordx4 v[108:111],v59,s[36:39],s50 offen offset:3072 sc1\nv_add_f32 v38,v38,v42\ns_waitcnt vmcnt(16)\nv_mfma_f32_16x16x32_bf16 v[20:23],v[112:115],a[192:195],v[20:23]\n" \
"v_add_f32 v39,v39,v43\nv_add_f32 v36,v36,v52\nv_add_f32 v37,v37,v53\nv_mfma_f32_16x16x32_bf16 v[24:27],v[112:115],a[196:199],v[24:27]\n" \
"v_add_f32 v38,v38,v54\nv_add_f32 v39,v39,v55\ns_waitcnt lgkmcnt(0)\nv_mfma_f32_16x16x32_bf16 v[28:31],v[112:115],a[200:203],v[28:31]\n" \
"v_add_f32 v44,v44,v48\nv_add_f32 v45,v45,v49\nv_add_f32 v46,v46,v50\nv_mfma_f32_16x16x32_bf16 v[32:35],v[112:115],a[204:207],v[32:35]\n" \
"buffer_load_dwordx4 v[112:115],v59,s[36:39],s51 offen sc1\nv_add_f32 v47,v47,v51\ns_waitcnt vmcnt(16)\nv_mfma_f32_16x16x32_bf16 v[20:23],v[116:119],a[208:211],v[20:23]\n" \
"v_add_f32 v36,v36,v44\nv_add_f32 v37,v37,v45\nv_add_f32 v38,v38,v46\nv_mfma_f32_16x16x32_bf16 v[24:27],v[116:119],a[212:215],v[24:27]\n" \
"v_add_f32 v39,v39,v47\nv_mul_f32 v40,v36,v36\nv_mul_f32 v41,v37,v37\nv_mfma_f32_16x16x32_bf16 v[28:31],v[116:119],a[216:219],v[28:31]\n" \
"v_mul_f32 v42,v38,v38\nv_mul_f32 v43,v39,v39\nv_fma_f32 v40,v40,s55,v57\nv_mfma_f32_16x16x32_bf16 v[32:35],v[116:119],a[220:223],v[32:35]\n" \
"buffer_load_dwordx4 v[116:119],v59,s[36:39],s51 offen offset:1024 sc1\nv_fma_f32 v41,v41,s55,v57\ns_waitcnt vmcnt(16)\nv_mfma_f32_16x16x32_bf16 v[20:23],v[120:123],a[224:227],v[20:23]\n" \
"v_fma_f32 v42,v42,s55,v57\nv_fma_f32 v43,v43,s55,v57\nv_mul_f32 v40,v36,v40\nv_mfma_f32_16x16x32_bf16 v[24:27],v[120:123],a[228:231],v[24:27]\n" \
"v_mul_f32 v41,v37,v41\nv_mul_f32 v42,v38,v42\nv_mfma_f32_16x16x32_bf16 v[28:31],v[120:123],a[232:235],v[28:31]\nv_mul_f32 v43,v39,v43\n" \
"v_mfma_f32_16x16x32_bf16 v[32:35],v[120:123],a[236:239],v[32:35]\nbuffer_load_dwordx4 v[120:123],v59,s[36:39],s51 offen offset:2048 sc1\ns_waitcnt vmcnt(16)\nv_mfma_f32_16x16x32_bf16 v[20:23],v[124:127],a[240:243],v[20:23]\n" \
"s_cmp_eq_u32 s52,s95\ns_cbranch_scc1 .Lxt4_%=\n.Lyt4_%=:\nv_mfma_f32_16x16x32_bf16 v[24:27],v[124:127],a[244:247],v[24:27]\n" \
"v_exp_f32 v40,v40\nv_mfma_f32_16x16x32_bf16 v[28:31],v[124:127],a[248:251],v[28:31]\nv_exp_f32 v41,v41\nv_mfma_f32_16x16x32_bf16 v[32:35],v[124:127],a[252:255],v[32:35]\n" \
"buffer_load_dwordx4 v[124:127],v59,s[36:39],s51 offen offset:3072 sc1\nv_exp_f32 v42,v42\nv_exp_f32 v43,v43\nv_add_f32 v40,1.0,v40\n" \
"s_add_u32 s52,s52,409600\ns_cmp_eq_u32 s52,s53\nv_add_f32 v41,1.0,v41\ns_cbranch_scc0 .Lf_%=\n" \
"v_mfma_f32_16x16x32_bf16 v[0:3],v[64:67],a[0:3],0\nds_read_b32 v44,v60\nv_and_b32 v59,0x3ff,v59\ns_add_u32 s48,s68,409600\n" \
"v_mfma_f32_16x16x32_bf16 v[4:7],v[64:67],a[4:7],0\nds_read_b32 v45,v60 offset:9568\ns_add_u32 s49,s69,409600\ns_add_u32 s50,s70,409600\n" \
"v_mfma_f32_16x16x32_bf16 v[8:11],v[64:67],a[8:11],0\nds_read_b32 v46,v60 offset:19136\nds_write_addtid_b32 v20 offset:5344\nv_add_f32 v42,1.0,v42\n" \
"v_mfma_f32_16x16x32_bf16 v[12:15],v[64:67],a[12:15],0\nds_read_b32 v47,v60 offset:28704\nds_write_addtid_b32 v21 offset:5888\nv_add_f32 v43,1.0,v43\n" \
"v_mfma_f32_16x16x32_bf16 v[16:19],v[64:67],v[192:195],0\nds_write_addtid_b32 v22 offset:5600\nds_write_addtid_b32 v23 offset:6144\nv_mfma_f32_16x16x32_bf16 v[0:3],v[68:71],a[16:19],v[0:3]\n" \
"ds_write_addtid_b32 v24 offset:6400\nds_write_addtid_b32 v25 offset:6944\nv_mfma_f32_16x16x32_bf16 v[4:7],v[68:71],a[20:23],v[4:7]\nds_write_addtid_b32 v26 offset:6656\n" \
"ds_write_addtid_b32 v27 offset:7200\nv_mfma_f32_16x16x32_bf16 v[8:11],v[68:71],a[24:27],v[8:11]\nds_write_addtid_b32 v28 offset:7456\nds_write_addtid_b32 v29 offset:8000\n" \
"v_mfma_f32_16x16x32_bf16 v[12:15],v[68:71],a[28:31],v[12:15]\nds_write_addtid_b32 v30 offset:7712\nds_write_addtid_b32 v31 offset:8256\nv_mfma_f32_16x16x32_bf16 v[16:19],v[68:71],v[196:199],v[16:19]\n" \
"ds_write_addtid_b32 v32 offset:8512\nds_write_addtid_b32 v33 offset:9056\ns_waitcnt vmcnt(15)\nv_mfma_f32_16x16x32_bf16 v[0:3],v[72:75],a[32:35],v[0:3]\n" \
"s_waitcnt vmcnt(6)\nv_cmp_eq_u32 vcc,0,v63\nv_add_u32 v71,s57,v59\nv_mfma_f32_16x16x32_bf16 v[4:7],v[72:75],a[36:39],v[4:7]\n" \
"ds_read_b32 v64,v71 offset:28672\nds_read_b64 v[68:69],v71 offset:29696\ns_waitcnt lgkmcnt(15)\nv_mfma_f32_16x16x32_bf16 v[8:11],v[72:75],a[40:43],v[8:11]\n" \
"s_and_b64 vcc,vcc,s[86:87]\ns_cbranch_scc1 .LsaF_%=\n.LbaF_%=:\nds_write_addtid_b32 v34 offset:8768\n" \
"v_mfma_f32_16x16x32_bf16 v[12:15],v[72:75],a[44:47],v[12:15]\nds_write_addtid_b32 v35 offset:9312\nv_rcp_f32 v40,v40\nv_mfma_f32_16x16x32_bf16 v[16:19],v[72:75],v[200:203],v[16:19]\n" \
"v_add_f32 v44,v44,v45\nv_add_f32 v46,v46,v47\ns_waitcnt vmcnt(14)\nv_mfma_f32_16x16x32_bf16 v[0:3],v[76:79],a[48:51],v[0:3]\n" \
"buffer_load_dwordx4 v[72:75],v59,s[36:39],s68 offen sc1\ns_waitcnt lgkmcnt(2)\nv_xor_b32 v65,32,v64\nv_mfma_f32_16x16x32_bf16 v[4:7],v[76:79],a[52:55],v[4:7]\n" \
"v_xor_b32 v66,64,v64\nv_xor_b32 v67,96,v64\nv_add_u32 v70,0xf800,v68\nv_mfma_f32_16x16x32_bf16 v[8:11],v[76:79],a[56:59],v[8:11]\n" \
"v_add_u32 v71,0xf800,v69\nds_read_b64_tr_b16 a[0:1],v64\nds_read_b64_tr_b16 a[2:3],v64 offset:512\nv_mfma_f32_16x16x32_bf16 v[12:15],v[76:79],a[60:63],v[12:15]\n" \
"ds_read_b64_tr_b16 a[4:5],v65\nds_read_b64_tr_b16 a[6:7],v65 offset:512\nv_add_f32 v44,v44,v56\nv_mfma_f32_16x16x32_bf16 v[16:19],v[76:79],v[204:207],v[16:19]\n" \
"s_waitcnt lgkmcnt(0)\ns_barrier\nv_mfma_f32_16x16x32_bf16 v[0:3],v[80:83],a[64:67],v[0:3]\nbuffer_load_dwordx4 v[76:79],v59,s[36:39],s68 offen offset:1024 sc1\n" \
"ds_read_b64_tr_b16 a[8:9],v66\nds_read_b64_tr_b16 a[10:11],v66 offset:512\nv_mfma_f32_16x16x32_bf16 v[4:7],v[80:83],a[68:71],v[4:7]\nds_read_b64_tr_b16 a[12:13],v67\n" \
"ds_read_b64_tr_b16 a[14:15],v67 offset:512\nv_add_f32 v44,v44,v46\nv_mfma_f32_16x16x32_bf16 v[8:11],v[80:83],a[72:75],v[8:11]\nv_rcp_f32 v41,v41\n" \
"v_mul_f32 v45,v44,v44\nv_mfma_f32_16x16x32_bf16 v[12:15],v[80:83],a[76:79],v[12:15]\nv_rcp_f32 v42,v42\nv_fma_f32 v45,v45,s55,v57\n" \
"v_mfma_f32_16x16x32_bf16 v[16:19],v[80:83],v[208:211],v[16:19]\nds_read_b64_tr_b16 a[16:17],v64 offset:4096\nds_read_b64_tr_b16 a[18:19],v64 offset:4608\nv_mfma_f32_16x16x32_bf16 v[0:3],v[84:87],a[80:83],v[0:3]\n" \
"buffer_load_dwordx4 v[80:83],v59,s[36:39],s68 offen offset:2048 sc1\ns_waitcnt lgkmcnt(2)\ns_add_u32 m0,s57,0\nv_mfma_f32_16x16x32_bf16 v[4:7],v[84:87],a[84:87],v[4:7]\n" \
"ds_read_b64_tr_b16 a[20:21],v65 offset:4096\nds_read_b64_tr_b16 a[22:23],v65 offset:4608\nv_mul_f32 v45,v44,v45\nv_mfma_f32_16x16x32_bf16 v[8:11],v[84:87],a[88:91],v[8:11]\n" \
"ds_read_b64_tr_b16 a[24:25],v66 offset:4096\nds_read_b64_tr_b16 a[26:27],v66 offset:4608\nv_mfma_f32_16x16x32_bf16 v[12:15],v[84:87],a[92:95],v[12:15]\nbuffer_load_dwordx4 v68,s[40:43],s60 offen lds\n" \
"ds_read_b64_tr_b16 a[28:29],v67 offset:4096\nds_read_b64_tr_b16 a[30:31],v67 offset:4608\nv_mfma_f32_16x16x32_bf16 v[16:19],v[84:87],v[212:215],v[16:19]\nv_rcp_f32 v43,v43\n" \
"v_mfma_f32_16x16x32_bf16 v[0:3],v[88:91],a[96:99],v[0:3]\nv_exp_f32 v45,v45\nv_mul_f32 v36,v36,v40\nv_mfma_f32_16x16x32_bf16 v[4:7],v[88:91],a[100:103],v[4:7]\n" \
"buffer_load_dwordx4 v69,s[40:43],s60 offen offset:1024 lds\nv_mul_f32 v37,v37,v41\nv_add_f32 v45,1.0,v45\nv_mfma_f32_16x16x32_bf16 v[8:11],v[88:91],a[104:107],v[8:11]\n" \
"v_mul_f32 v38,v38,v42\nv_rcp_f32 v45,v45\nv_mfma_f32_16x16x32_bf16 v[12:15],v[88:91],a[108:111],v[12:15]\nv_mul_f32 v39,v39,v43\n" \
"v_cvt_pk_bf16_f32 v40,v36,v37\nv_mul_f32 v44,v44,v45\nv_mfma_f32_16x16x32_bf16 v[16:19],v[88:91],v[216:219],v[16:19]\nbuffer_load_dwordx4 v70,s[40:43],s60 offen offset:2048 lds\n" \
"ds_read_b64_tr_b16 a[32:33],v64 offset:8192\nv_mfma_f32_16x16x32_bf16 v[0:3],v[92:95],a[112:115],v[0:3]\nds_read_b64_tr_b16 a[34:35],v64 offset:8704\nds_read_b64_tr_b16 a[36:37],v65 offset:8192\n" \
"v_cvt_pk_bf16_f32 v41,v38,v39\nv_mfma_f32_16x16x32_bf16 v[4:7],v[92:95],a[116:119],v[4:7]\nds_read_b64_tr_b16 a[38:39],v65 offset:8704\nds_read_b64_tr_b16 a[40:41],v66 offset:8192\n" \
"v_cvt_pk_bf16_f32 v45,v44,v44\nv_mfma_f32_16x16x32_bf16 v[8:11],v[92:95],a[120:123],v[8:11]\nbuffer_load_dwordx4 v71,s[40:43],s60 offen offset:3072 lds\nds_read_b64_tr_b16 a[42:43],v66 offset:8704\n" \
"ds_read_b64_tr_b16 a[44:45],v67 offset:8192\nv_mfma_f32_16x16x32_bf16 v[12:15],v[92:95],a[124:127],v[12:15]\nds_read_b64_tr_b16 a[46:47],v67 offset:8704\nbuffer_store_dwordx2 v[40:41],v61,s[36:39],s52 offen sc1\n" \
"buffer_store_short v45,v62,s[36:39],s52 offen sc1\nv_mfma_f32_16x16x32_bf16 v[16:19],v[92:95],v[220:223],v[16:19]\nds_read_b128 v[36:39],v58 offset:5344\nds_read_b128 v[40:43],v58 offset:14912\n" \
"s_waitcnt vmcnt(18)\nv_mfma_f32_16x16x32_bf16 v[0:3],v[96:99],a[128:131],v[0:3]\nbuffer_load_dwordx4 v[84:87],v59,s[36:39],s69 offen sc1\nds_read_b128 v[44:47],v58 offset:24480\n" \
"ds_read_b128 v[48:51],v58 offset:34048\nv_mfma_f32_16x16x32_bf16 v[4:7],v[96:99],a[132:135],v[4:7]\ns_add_u32 s52,s52,409600\nv_mfma_f32_16x16x32_bf16 v[8:11],v[96:99],a[136:139],v[8:11]\n" \
"v_mfma_f32_16x16x32_bf16 v[12:15],v[96:99],a[140:143],v[12:15]\nbuffer_load_dwordx4 v[88:91],v59,s[36:39],s69 offen offset:1024 sc1\nv_mfma_f32_16x16x32_bf16 v[16:19],v[96:99],v[224:227],v[16:19]\nds_read_b64_tr_b16 a[48:49],v64 offset:12288\n" \
"ds_read_b64_tr_b16 a[50:51],v64 offset:12800\ns_waitcnt vmcnt(19)\nv_mfma_f32_16x16x32_bf16 v[0:3],v[100:103],a[144:147],v[0:3]\nds_read_b64_tr_b16 a[52:53],v65 offset:12288\n" \
"ds_read_b64_tr_b16 a[54:55],v65 offset:12800\nv_mfma_f32_16x16x32_bf16 v[4:7],v[100:103],a[148:151],v[4:7]\nbuffer_load_dwordx4 v[92:95],v59,s[36:39],s69 offen offset:2048 sc1\ns_waitcnt lgkmcnt(15)\n" \
"s_add_u32 m0,s57,4096\nv_mfma_f32_16x16x32_bf16 v[8:11],v[100:103],a[152:155],v[8:11]\nds_read_b64_tr_b16 a[56:57],v66 offset:12288\nds_read_b64_tr_b16 a[58:59],v66 offset:12800\n" \
"s_waitcnt lgkmcnt(8)\nv_mfma_f32_16x16x32_bf16 v[12:15],v[100:103],a[156:159],v[12:15]\nds_read_b64_tr_b16 a[60:61],v67 offset:12288\nds_read_b64_tr_b16 a[62:63],v67 offset:12800\n" \
"v_add_f32 v36,v36,v40\nv_mfma_f32_16x16x32_bf16 v[16:19],v[100:103],v[228:231],v[16:19]\nbuffer_load_dwordx4 v68,s[40:43],s61 offen lds\nv_add_f32 v37,v37,v41\n" \
"s_waitcnt vmcnt(18)\nv_mfma_f32_16x16x32_bf16 v[0:3],v[104:107],a[160:163],v[0:3]\nv_add_f32 v38,v38,v42\nv_add_f32 v39,v39,v43\n" \
"v_add_f32 v36,v36,v52\nv_mfma_f32_16x16x32_bf16 v[4:7],v[104:107],a[164:167],v[4:7]\nv_add_f32 v37,v37,v53\nv_add_f32 v38,v38,v54\n" \
"v_add_f32 v39,v39,v55\nv_mfma_f32_16x16x32_bf16 v[8:11],v[104:107],a[168:171],v[8:11]\nbuffer_load_dwordx4 v69,s[40:43],s61 offen offset:1024 lds\ns_waitcnt lgkmcnt(8)\n" \
"v_add_f32 v44,v44,v48\nv_mfma_f32_16x16x32_bf16 v[12:15],v[104:107],a[172:175],v[12:15]\nv_add_f32 v45,v45,v49\nv_add_f32 v46,v46,v50\n" \
"v_add_f32 v47,v47,v51\nv_mfma_f32_16x16x32_bf16 v[16:19],v[104:107],v[232:235],v[16:19]\nds_read_b64_tr_b16 a[64:65],v64 offset:16384\nds_read_b64_tr_b16 a[66:67],v64 offset:16896\n" \
"s_waitcnt vmcnt(18)\nv_mfma_f32_16x16x32_bf16 v[0:3],v[108:111],a[176:179],v[0:3]\nbuffer_load_dwordx4 v70,s[40:43],s61 offen offset:2048 lds\nds_read_b64_tr_b16 a[68:69],v65 offset:16384\n" \
"ds_read_b64_tr_b16 a[70:71],v65 offset:16896\nv_mfma_f32_16x16x32_bf16 v[4:7],v[108:111],a[180:183],v[4:7]\nds_read_b64_tr_b16 a[72:73],v66 offset:16384\nds_read_b64_tr_b16 a[74:75],v66 offset:16896\n" \
"v_add_f32 v36,v36,v44\nv_mfma_f32_16x16x32_bf16 v[8:11],v[108:111],a[184:187],v[8:11]\nds_read_b64_tr_b16 a[76:77],v67 offset:16384\nds_read_b64_tr_b16 a[78:79],v67 offset:16896\n" \
"v_add_f32 v37,v37,v45\nv_mfma_f32_16x16x32_bf16 v[12:15],v[108:111],a[188:191],v[12:15]\nbuffer_load_dwordx4 v71,s[40:43],s61 offen offset:3072 lds\nv_add_f32 v38,v38,v46\n" \
"v_add_f32 v39,v39,v47\nv_mfma_f32_16x16x32_bf16 v[16:19],v[108:111],v[236:239],v[16:19]\nv_mul_f32 v40,v36,v36\nv_mul_f32 v41,v37,v37\n" \
"s_waitcnt vmcnt(19)\nv_mfma_f32_16x16x32_bf16 v[0:3],v[112:115],a[192:195],v[0:3]\nv_mul_f32 v42,v38,v38\nv_mul_f32 v43,v39,v39\n" \
"v_fma_f32 v40,v40,s55,v57\nv_mfma_f32_16x16x32_bf16 v[4:7],v[112:115],a[196:199],v[4:7]\nbuffer_load_dwordx4 v[108:111],v59,s[36:39],s48 offen sc1\ns_add_u32 m0,s57,8192\n" \
"v_mfma_f32_16x16x32_bf16 v[8:11],v[112:115],a[200:203],v[8:11]\nv_fma_f32 v41,v41,s55,v57\nv_fma_f32 v42,v42,s55,v57\nv_fma_f32 v43,v43,s55,v57\n" \
"v_mfma_f32_16x16x32_bf16 v[12:15],v[112:115],a[204:207],v[12:15]\nv_mul_f32 v40,v36,v40\nv_mul_f32 v41,v37,v41\nv_mul_f32 v42,v38,v42\n" \
"v_mfma_f32_16x16x32_bf16 v[16:19],v[112:115],v[240:243],v[16:19]\nbuffer_load_dwordx4 v68,s[40:43],s62 offen lds\nds_read_b64_tr_b16 a[80:81],v64 offset:20480\ns_waitcnt vmcnt(20)\n" \
"v_mfma_f32_16x16x32_bf16 v[0:3],v[116:119],a[208:211],v[0:3]\nds_read_b64_tr_b16 a[82:83],v64 offset:20992\nds_read_b64_tr_b16 a[84:85],v65 offset:20480\nv_mul_f32 v43,v39,v43\n" \
"v_mfma_f32_16x16x32_bf16 v[4:7],v[116:119],a[212:215],v[4:7]\nds_read_b64_tr_b16 a[86:87],v65 offset:20992\nds_read_b64_tr_b16 a[88:89],v66 offset:20480\nv_mfma_f32_16x16x32_bf16 v[8:11],v[116:119],a[216:219],v[8:11]\n" \
"buffer_load_dwordx4 v69,s[40:43],s62 offen offset:1024 lds\nds_read_b64_tr_b16 a[90:91],v66 offset:20992\nds_read_b64_tr_b16 a[92:93],v67 offset:20480\nv_mfma_f32_16x16x32_bf16 v[12:15],v[116:119],a[220:223],v[12:15]\n" \
"ds_read_b64_tr_b16 a[94:95],v67 offset:20992\nv_exp_f32 v40,v40\nv_mfma_f32_16x16x32_bf16 v[16:19],v[116:119],v[244:247],v[16:19]\nv_exp_f32 v41,v41\n" \
"s_waitcnt vmcnt(20)\nv_mfma_f32_16x16x32_bf16 v[0:3],v[120:123],a[224:227],v[0:3]\nbuffer_load_dwordx4 v70,s[40:43],s62 offen offset:2048 lds\nv_exp_f32 v42,v42\n" \
"v_mfma_f32_16x16x32_bf16 v[4:7],v[120:123],a[228:231],v[4:7]\nv_exp_f32 v43,v43\nv_add_f32 v40,1.0,v40\nv_mfma_f32_16x16x32_bf16 v[8:11],v[120:123],a[232:235],v[8:11]\n" \
"v_add_f32 v41,1.0,v41\nv_add_f32 v42,1.0,v42\nv_add_f32 v43,1.0,v43\nv_mfma_f32_16x16x32_bf16 v[12:15],v[120:123],a[236:239],v[12:15]\n" \
"buffer_load_dwordx4 v71,s[40:43],s62 offen offset:3072 lds\nv_rcp_f32 v40,v40\nv_mfma_f32_16x16x32_bf16 v[16:19],v[120:123],v[248:251],v[16:19]\nds_read_b64_tr_b16 a[96:97],v64 offset:24576\n" \
"ds_read_b64_tr_b16 a[98:99],v64 offset:25088\ns_waitcnt vmcnt(21)\nv_mfma_f32_16x16x32_bf16 v[0:3],v[124:127],a[240:243],v[0:3]\nds_read_b64_tr_b16 a[100:101],v65 offset:24576\n" \
"ds_read_b64_tr_b16 a[102:103],v65 offset:25088\nv_mfma_f32_16x16x32_bf16 v[4:7],v[124:127],a[244:247],v[4:7]\nbuffer_load_dwordx4 v[112:115],v59,s[36:39],s48 offen offset:1024 sc1\nds_read_b64_tr_b16 a[104:105],v66 offset:24576\n" \
"ds_read_b64_tr_b16 a[106:107],v66 offset:25088\nv_mfma_f32_16x16x32_bf16 v[8:11],v[124:127],a[248:251],v[8:11]\nds_read_b64_tr_b16 a[108:109],v67 offset:24576\nds_read_b64_tr_b16 a[110:111],v67 offset:25088\n" \
"v_mfma_f32_16x16x32_bf16 v[12:15],v[124:127],a[252:255],v[12:15]\nv_rcp_f32 v41,v41\nv_mfma_f32_16x16x32_bf16 v[16:19],v[124:127],v[252:255],v[16:19]\nbuffer_load_dwordx4 v[116:119],v59,s[36:39],s48 offen offset:2048 sc1\n" \
"s_mov_b32 m0,s56\ns_waitcnt vmcnt(22)\nv_mfma_f32_16x16x32_bf16 v[208:211],v[128:131],v[72:75],0\nv_rcp_f32 v42,v42\n" \
"v_mfma_f32_16x16x32_bf16 v[212:215],v[132:135],v[72:75],0\nds_write_addtid_b32 v0\nds_write_addtid_b32 v1 offset:544\nv_mfma_f32_16x16x32_bf16 v[216:219],v[136:139],v[72:75],0\n" \
"buffer_load_dwordx4 v[120:123],v59,s[36:39],s49 offen sc1\nds_write_addtid_b32 v2 offset:256\nds_write_addtid_b32 v3 offset:800\nv_mfma_f32_16x16x32_bf16 v[220:223],v[140:143],v[72:75],0\n" \
"ds_write_addtid_b32 v4 offset:1056\nds_write_addtid_b32 v5 offset:1600\nv_mfma_f32_16x16x32_bf16 v[224:227],v[144:147],v[72:75],0\nds_write_addtid_b32 v6 offset:1312\n" \
"ds_write_addtid_b32 v7 offset:1856\nv_mfma_f32_16x16x32_bf16 v[228:231],v[148:151],v[72:75],0\nbuffer_load_dwordx4 v[124:127],v59,s[36:39],s49 offen offset:1024 sc1\nds_write_addtid_b32 v8 offset:2112\n" \
"ds_write_addtid_b32 v9 offset:2656\nv_mfma_f32_16x16x32_bf16 v[232:235],v[152:155],v[72:75],0\nds_write_addtid_b32 v10 offset:2368\nds_write_addtid_b32 v11 offset:2912\n" \
"v_rcp_f32 v43,v43\nv_mfma_f32_16x16x32_bf16 v[236:239],v[156:159],v[72:75],0\nds_write_addtid_b32 v12 offset:3168\nds_write_addtid_b32 v13 offset:3712\n" \
"v_mul_f32 v36,v36,v40\ns_waitcnt vmcnt(23)\nv_mfma_f32_16x16x32_bf16 v[208:211],v[160:163],v[76:79],v[208:211]\nv_mul_f32 v37,v37,v41\n" \
"v_mul_f32 v38,v38,v42\nv_mul_f32 v39,v39,v43\nv_cvt_pk_bf16_f32 v40,v36,v37\nv_cvt_pk_bf16_f32 v41,v38,v39\n" \
"buffer_store_dwordx2 v[40:41],v61,s[36:39],s52 offen sc1\nv_mfma_f32_16x16x32_bf16 v[212:215],v[164:167],v[76:79],v[212:215]\nbuffer_load_dwordx4 v[192:195],v59,s[36:39],s49 offen offset:2048 sc1\nds_write_addtid_b32 v14 offset:3424\n" \
"ds_write_addtid_b32 v15 offset:3968\nv_mfma_f32_16x16x32_bf16 v[216:219],v[168:171],v[76:79],v[216:219]\nds_write_addtid_b32 v16 offset:4224\nds_write_addtid_b32 v17 offset:4512\n" \
"s_add_u32 s52,s52,409600\nv_mfma_f32_16x16x32_bf16 v[220:223],v[172:175],v[76:79],v[220:223]\nds_write_addtid_b32 v18 offset:4800\nds_write_addtid_b32 v19 offset:5088\n" \
"s_waitcnt vmcnt(20)\nv_mfma_f32_16x16x32_bf16 v[224:227],v[176:179],v[76:79],v[224:227]\nbuffer_load_dwordx4 v[96:99],v59,s[36:39],s70 offen sc1\ns_add_u32 m0,s57,12288\n" \
"v_mfma_f32_16x16x32_bf16 v[228:231],v[180:183],v[76:79],v[228:231]\nds_read_b64_tr_b16 a[112:113],v64\nds_read_b64_tr_b16 a[114:115],v64 offset:512\nv_mfma_f32_16x16x32_bf16 v[232:235],v[184:187],v[76:79],v[232:235]\n" \
"ds_read_b64_tr_b16 a[116:117],v65\nds_read_b64_tr_b16 a[118:119],v65 offset:512\nv_mfma_f32_16x16x32_bf16 v[236:239],v[188:191],v[76:79],v[236:239]\nbuffer_load_dwordx4 v68,s[40:43],s63 offen lds\n" \
"s_waitcnt lgkmcnt(15)\nv_mfma_f32_16x16x32_bf16 v[208:211],a[0:3],v[80:83],v[208:211]\nds_read_b64_tr_b16 a[120:121],v66\nds_read_b64_tr_b16 a[122:123],v66 offset:512\n" \
"v_mfma_f32_16x16x32_bf16 v[212:215],a[4:7],v[80:83],v[212:215]\ns_waitcnt lgkmcnt(0)\ns_barrier\nds_read_b128 v[36:39],v58\n" \
"v_mfma_f32_16x16x32_bf16 v[216:219],a[8:11],v[80:83],v[216:219]\nbuffer_load_dwordx4 v69,s[40:43],s63 offen offset:1024 lds\nds_read_b128 v[40:43],v58 offset:9568\nds_read_b128 v[44:47],v58 offset:19136\n" \
"v_mfma_f32_16x16x32_bf16 v[220:223],a[12:15],v[80:83],v[220:223]\nds_read_b128 v[48:51],v58 offset:28704\nds_read_b64_tr_b16 a[124:125],v67\nv_mfma_f32_16x16x32_bf16 v[224:227],a[16:19],v[80:83],v[224:227]\n" \
"ds_read_b64_tr_b16 a[126:127],v67 offset:512\nds_read_b32 v252,v60\nv_mfma_f32_16x16x32_bf16 v[228:231],a[20:23],v[80:83],v[228:231]\nbuffer_load_dwordx4 v70,s[40:43],s63 offen offset:2048 lds\n" \
"ds_read_b32 v253,v60 offset:9568\nds_read_b32 v254,v60 offset:19136\nv_mfma_f32_16x16x32_bf16 v[232:235],a[24:27],v[80:83],v[232:235]\nds_read_b32 v255,v60 offset:28704\n" \
"v_mfma_f32_16x16x32_bf16 v[236:239],a[28:31],v[80:83],v[236:239]\ns_waitcnt vmcnt(21)\nv_mfma_f32_16x16x32_bf16 v[208:211],a[32:35],v[84:87],v[208:211]\nbuffer_load_dwordx4 v71,s[40:43],s63 offen offset:3072 lds\n" \
"s_add_u32 m0,s57,16384\nv_mfma_f32_16x16x32_bf16 v[212:215],a[36:39],v[84:87],v[212:215]\nv_mfma_f32_16x16x32_bf16 v[216:219],a[40:43],v[84:87],v[216:219]\ns_waitcnt lgkmcnt(8)\n" \
"v_add_f32 v36,v36,v40\nv_add_f32 v37,v37,v41\nv_mfma_f32_16x16x32_bf16 v[220:223],a[44:47],v[84:87],v[220:223]\nbuffer_load_dwordx4 v68,s[40:43],s64 offen lds\n" \
"v_add_f32 v38,v38,v42\nv_mfma_f32_16x16x32_bf16 v[224:227],a[48:51],v[84:87],v[224:227]\ns_waitcnt vmcnt(17)\nds_read_b64_tr_b16 a[128:129],v64 offset:4096\n" \
"ds_read_b64_tr_b16 a[130:131],v64 offset:4608\nv_mfma_f32_16x16x32_bf16 v[228:231],a[52:55],v[84:87],v[228:231]\nds_read_b64_tr_b16 a[132:133],v65 offset:4096\nds_read_b64_tr_b16 a[134:135],v65 offset:4608\n" \
"v_add_f32 v39,v39,v43\nv_mfma_f32_16x16x32_bf16 v[232:235],a[56:59],v[84:87],v[232:235]\nbuffer_load_dwordx4 v69,s[40:43],s64 offen offset:1024 lds\nds_read_b64_tr_b16 a[136:137],v66 offset:4096\n" \
"ds_read_b64_tr_b16 a[138:139],v66 offset:4608\nv_mfma_f32_16x16x32_bf16 v[236:239],a[60:63],v[84:87],v[236:239]\nds_read_b64_tr_b16 a[140:141],v67 offset:4096\nv_mfma_f32_16x16x32_bf16 v[208:211],a[64:67],v[88:91],v[208:211]\n" \
"ds_read_b64_tr_b16 a[142:143],v67 offset:4608\nv_add_f32 v36,v36,v52\nv_add_f32 v37,v37,v53\nv_mfma_f32_16x16x32_bf16 v[212:215],a[68:71],v[88:91],v[212:215]\n" \
"buffer_load_dwordx4 v70,s[40:43],s64 offen offset:2048 lds\nv_add_f32 v38,v38,v54\nv_add_f32 v39,v39,v55\nv_mfma_f32_16x16x32_bf16 v[216:219],a[72:75],v[88:91],v[216:219]\n" \
"s_waitcnt lgkmcnt(14)\nv_add_f32 v44,v44,v48\nv_add_f32 v45,v45,v49\nv_mfma_f32_16x16x32_bf16 v[220:223],a[76:79],v[88:91],v[220:223]\n" \
"v_add_f32 v46,v46,v50\nv_add_f32 v47,v47,v51\nv_mfma_f32_16x16x32_bf16 v[224:227],a[80:83],v[88:91],v[224:227]\nbuffer_load_dwordx4 v71,s[40:43],s64 offen offset:3072 lds\n" \
"v_add_f32 v36,v36,v44\nv_add_f32 v37,v37,v45\nv_mfma_f32_16x16x32_bf16 v[228:231],a[84:87],v[88:91],v[228:231]\nv_add_f32 v38,v38,v46\n" \
"v_add_f32 v39,v39,v47\nv_mul_f32 v40,v36,v36\nv_mfma_f32_16x16x32_bf16 v[232:235],a[88:91],v[88:91],v[232:235]\nv_mul_f32 v41,v37,v37\n" \
"v_mul_f32 v42,v38,v38\nv_mul_f32 v43,v39,v39\nv_mfma_f32_16x16x32_bf16 v[236:239],a[92:95],v[88:91],v[236:239]\nbuffer_load_dwordx4 v[100:103],v59,s[36:39],s70 offen offset:1024 sc1\n" \
"v_mfma_f32_16x16x32_bf16 v[208:211],a[96:99],v[92:95],v[208:211]\ns_add_u32 m0,s57,20480\nv_fma_f32 v40,v40,s55,v57\nv_mfma_f32_16x16x32_bf16 v[212:215],a[100:103],v[92:95],v[212:215]\n" \
"v_fma_f32 v41,v41,s55,v57\nv_fma_f32 v42,v42,s55,v57\nv_fma_f32 v43,v43,s55,v57\nv_mfma_f32_16x16x32_bf16 v[216:219],a[104:107],v[92:95],v[216:219]\n" \
"buffer_load_dwordx4 v68,s[40:43],s65 offen lds\nv_mul_f32 v40,v36,v40\nv_mul_f32 v41,v37,v41\nv_mfma_f32_16x16x32_bf16 v[220:223],a[108:111],v[92:95],v[220:223]\n" \
"s_waitcnt vmcnt(17)\nds_read_b64_tr_b16 a[144:145],v64 offset:8192\nv_mfma_f32_16x16x32_bf16 v[0:3],v[128:131],v[108:111],0\nds_read_b64_tr_b16 a[146:147],v64 offset:8704\n" \
"ds_read_b64_tr_b16 a[148:149],v65 offset:8192\nv_mul_f32 v42,v38,v42\nv_mfma_f32_16x16x32_bf16 v[4:7],v[132:135],v[108:111],0\nbuffer_load_dwordx4 v69,s[40:43],s65 offen offset:1024 lds\n" \
"ds_read_b64_tr_b16 a[150:151],v65 offset:8704\nds_read_b64_tr_b16 a[152:153],v66 offset:8192\nv_mfma_f32_16x16x32_bf16 v[8:11],v[136:139],v[108:111],0\nds_read_b64_tr_b16 a[154:155],v66 offset:8704\n" \
"ds_read_b64_tr_b16 a[156:157],v67 offset:8192\nv_mul_f32 v43,v39,v43\nv_mfma_f32_16x16x32_bf16 v[12:15],v[140:143],v[108:111],0\nds_read_b64_tr_b16 a[158:159],v67 offset:8704\n" \
"v_exp_f32 v40,v40\nv_mfma_f32_16x16x32_bf16 v[16:19],v[144:147],v[108:111],0\nbuffer_load_dwordx4 v70,s[40:43],s65 offen offset:2048 lds\nv_exp_f32 v41,v41\n" \
"v_mfma_f32_16x16x32_bf16 v[20:23],v[148:151],v[108:111],0\nv_exp_f32 v42,v42\ns_waitcnt lgkmcnt(15)\nv_mfma_f32_16x16x32_bf16 v[24:27],v[152:155],v[108:111],0\n" \
"v_exp_f32 v43,v43\nv_add_f32 v40,1.0,v40\nv_mfma_f32_16x16x32_bf16 v[28:31],v[156:159],v[108:111],0\nbuffer_load_dwordx4 v71,s[40:43],s65 offen offset:3072 lds\n" \
"v_add_f32 v41,1.0,v41\ns_waitcnt vmcnt(19)\nv_mfma_f32_16x16x32_bf16 v[0:3],v[160:163],v[112:115],v[0:3]\nv_add_f32 v42,1.0,v42\n" \
"v_add_f32 v43,1.0,v43\nv_add_f32 v252,v252,v253\nv_mfma_f32_16x16x32_bf16 v[4:7],v[164:167],v[112:115],v[4:7]\nv_rcp_f32 v40,v40\n" \
"v_add_f32 v254,v254,v255\nv_mfma_f32_16x16x32_bf16 v[8:11],v[168:171],v[112:115],v[8:11]\nbuffer_load_dwordx4 v[104:107],v59,s[36:39],s70 offen offset:2048 sc1\ns_add_u32 m0,s57,24576\n" \
"v_mfma_f32_16x16x32_bf16 v[12:15],v[172:175],v[112:115],v[12:15]\nv_rcp_f32 v41,v41\nv_add_f32 v252,v252,v56\nv_mfma_f32_16x16x32_bf16 v[16:19],v[176:179],v[112:115],v[16:19]\n" \
"v_rcp_f32 v42,v42\nv_add_f32 v252,v252,v254\nv_mfma_f32_16x16x32_bf16 v[20:23],v[180:183],v[112:115],v[20:23]\nbuffer_load_dwordx4 v68,s[40:43],s66 offen lds\n" \
"v_rcp_f32 v43,v43\nv_mfma_f32_16x16x32_bf16 v[24:27],v[184:187],v[112:115],v[24:27]\nv_mul_f32 v36,v36,v40\nv_mul_f32 v37,v37,v41\n" \
"v_mul_f32 v38,v38,v42\nv_mfma_f32_16x16x32_bf16 v[28:31],v[188:191],v[112:115],v[28:31]\nv_mul_f32 v39,v39,v43\nv_cvt_pk_bf16_f32 v40,v36,v37\n" \
"s_waitcnt vmcnt(20)\nv_mfma_f32_16x16x32_bf16 v[0:3],a[0:3],v[116:119],v[0:3]\nbuffer_load_dwordx4 v69,s[40:43],s66 offen offset:1024 lds\nv_cvt_pk_bf16_f32 v41,v38,v39\n" \
"v_mul_f32 v253,v252,v252\nv_mfma_f32_16x16x32_bf16 v[4:7],a[4:7],v[116:119],v[4:7]\nv_fma_f32 v253,v253,s55,v57\nv_mul_f32 v253,v252,v253\n" \
"v_mfma_f32_16x16x32_bf16 v[8:11],a[8:11],v[116:119],v[8:11]\nv_exp_f32 v253,v253\nv_mfma_f32_16x16x32_bf16 v[12:15],a[12:15],v[116:119],v[12:15]\nbuffer_load_dwordx4 v70,s[40:43],s66 offen offset:2048 lds\n" \
"v_add_f32 v253,1.0,v253\nv_mfma_f32_16x16x32_bf16 v[16:19],a[16:19],v[116:119],v[16:19]\nv_rcp_f32 v253,v253\nv_mfma_f32_16x16x32_bf16 v[20:23],a[20:23],v[116:119],v[20:23]\n" \
"v_mul_f32 v252,v252,v253\nv_cvt_pk_bf16_f32 v253,v252,v252\nv_mfma_f32_16x16x32_bf16 v[24:27],a[24:27],v[116:119],v[24:27]\nbuffer_load_dwordx4 v71,s[40:43],s66 offen offset:3072 lds\n" \
"v_mfma_f32_16x16x32_bf16 v[28:31],a[28:31],v[116:119],v[28:31]\ns_waitcnt vmcnt(22)\nv_mfma_f32_16x16x32_bf16 v[0:3],a[32:35],v[120:123],v[0:3]\nv_mfma_f32_16x16x32_bf16 v[4:7],a[36:39],v[120:123],v[4:7]\n" \
"buffer_load_dwordx4 v[196:199],v59,s[36:39],s50 offen sc1\nv_mfma_f32_16x16x32_bf16 v[8:11],a[40:43],v[120:123],v[8:11]\nv_mfma_f32_16x16x32_bf16 v[12:15],a[44:47],v[120:123],v[12:15]\nv_mfma_f32_16x16x32_bf16 v[16:19],a[48:51],v[120:123],v[16:19]\n" \
"buffer_load_dwordx4 v[200:203],v59,s[36:39],s50 offen offset:1024 sc1\nv_mfma_f32_16x16x32_bf16 v[20:23],a[52:55],v[120:123],v[20:23]\nv_mfma_f32_16x16x32_bf16 v[24:27],a[56:59],v[120:123],v[24:27]\ns_waitcnt vmcnt(16)\n" \
"ds_read_b64_tr_b16 a[160:161],v64 offset:12288\nds_read_b64_tr_b16 a[162:163],v64 offset:12800\nv_mfma_f32_16x16x32_bf16 v[28:31],a[60:63],v[120:123],v[28:31]\nbuffer_load_dwordx4 v[204:207],v59,s[36:39],s50 offen offset:2048 sc1\n" \
"ds_read_b64_tr_b16 a[164:165],v65 offset:12288\ns_waitcnt vmcnt(24)\nv_mfma_f32_16x16x32_bf16 v[0:3],a[64:67],v[124:127],v[0:3]\ns_add_u32 s59,s59,0x10000\n" \
"buffer_load_dword v63,v61,s[36:39],s59 offen sc1\nv_add_u32 v59,819200,v59\nv_mfma_f32_16x16x32_bf16 v[4:7],a[68:71],v[124:127],v[4:7]\nds_read_b64_tr_b16 a[166:167],v65 offset:12800\n" \
"ds_read_b64_tr_b16 a[168:169],v66 offset:12288\nbuffer_store_short v253,v62,s[36:39],s52 offen sc1\nv_mfma_f32_16x16x32_bf16 v[8:11],a[72:75],v[124:127],v[8:11]\nv_and_b32 v58,0x3ff,v59\n" \
"v_add_u32 v58,s99,v58\nv_add_u32 v46,s98,v58\nv_mfma_f32_16x16x32_bf16 v[12:15],a[76:79],v[124:127],v[12:15]\nds_read_b64 v[56:57],v46 offset:28680\n" \
"ds_read_b64_tr_b16 a[170:171],v66 offset:12800\nbuffer_store_dwordx2 v[40:41],v61,s[36:39],s52 offen sc1\nv_mfma_f32_16x16x32_bf16 v[16:19],a[80:83],v[124:127],v[16:19]\nbuffer_load_dwordx4 v[72:75],v59,s[36:39],s68 offen sc1\n" \
"ds_read_b64_tr_b16 a[172:173],v67 offset:12288\nds_read_b64_tr_b16 a[174:175],v67 offset:12800\nv_mfma_f32_16x16x32_bf16 v[20:23],a[84:87],v[124:127],v[20:23]\nv_mfma_f32_16x16x32_bf16 v[24:27],a[88:91],v[124:127],v[24:27]\n" \
"v_mfma_f32_16x16x32_bf16 v[28:31],a[92:95],v[124:127],v[28:31]\nbuffer_load_dwordx4 v[76:79],v59,s[36:39],s68 offen offset:1024 sc1\ns_waitcnt vmcnt(27)\nv_mfma_f32_16x16x32_bf16 v[0:3],a[96:99],v[192:195],v[0:3]\n" \
"s_waitcnt lgkmcnt(0)\ns_barrier\ns_mov_b32 m0,s58\nv_mfma_f32_16x16x32_bf16 v[4:7],a[100:103],v[192:195],v[4:7]\n" \
"v_mfma_f32_16x16x32_bf16 v[8:11],a[104:107],v[192:195],v[8:11]\nbuffer_load_dwordx4 v[80:83],v59,s[36:39],s68 offen offset:2048 sc1\ns_waitcnt vmcnt(19)\nds_read_b64_tr_b16 a[176:177],v64 offset:16384\n" \
"v_mfma_f32_16x16x32_bf16 v[12:15],a[108:111],v[192:195],v[12:15]\nds_read_b64_tr_b16 a[178:179],v64 offset:16896\nds_read_b64_tr_b16 a[180:181],v65 offset:16384\nv_mfma_f32_16x16x32_bf16 v[224:227],a[112:115],v[92:95],v[224:227]\n" \
"ds_read_b64_tr_b16 a[182:183],v65 offset:16896\nds_read_b64_tr_b16 a[184:185],v66 offset:16384\nv_mfma_f32_16x16x32_bf16 v[228:231],a[116:119],v[92:95],v[228:231]\nbuffer_load_dwordx4 v[84:87],v59,s[36:39],s69 offen sc1\n" \
"ds_read_b64_tr_b16 a[186:187],v66 offset:16896\nds_read_b64_tr_b16 a[188:189],v67 offset:16384\nv_mfma_f32_16x16x32_bf16 v[232:235],a[120:123],v[92:95],v[232:235]\nds_read_b64_tr_b16 a[190:191],v67 offset:16896\n" \
"v_mfma_f32_16x16x32_bf16 v[236:239],a[124:127],v[92:95],v[236:239]\nv_mfma_f32_16x16x32_bf16 v[208:211],a[128:131],v[96:99],v[208:211]\nbuffer_load_dwordx4 v[88:91],v59,s[36:39],s69 offen offset:1024 sc1\nv_mfma_f32_16x16x32_bf16 v[212:215],a[132:135],v[96:99],v[212:215]\n" \
"v_mfma_f32_16x16x32_bf16 v[216:219],a[136:139],v[96:99],v[216:219]\nv_mfma_f32_16x16x32_bf16 v[220:223],a[140:143],v[96:99],v[220:223]\nbuffer_load_dwordx4 v[92:95],v59,s[36:39],s69 offen offset:2048 sc1\nv_mfma_f32_16x16x32_bf16 v[224:227],a[144:147],v[96:99],v[224:227]\n" \
"v_mfma_f32_16x16x32_bf16 v[228:231],a[148:151],v[96:99],v[228:231]\nv_mfma_f32_16x16x32_bf16 v[232:235],a[152:155],v[96:99],v[232:235]\nv_mfma_f32_16x16x32_bf16 v[236:239],a[156:159],v[96:99],v[236:239]\nbuffer_load_dwordx4 v[96:99],v59,s[36:39],s70 offen sc1\n" \
"s_waitcnt vmcnt(22)\nv_mfma_f32_16x16x32_bf16 v[208:211],a[160:163],v[100:103],v[208:211]\ns_waitcnt vmcnt(18)\nds_read_b64_tr_b16 a[192:193],v64 offset:20480\n" \
"ds_read_b64_tr_b16 a[194:195],v64 offset:20992\nv_mfma_f32_16x16x32_bf16 v[212:215],a[164:167],v[100:103],v[212:215]\nds_read_b64_tr_b16 a[196:197],v65 offset:20480\nds_read_b64_tr_b16 a[198:199],v65 offset:20992\n" \
"v_mfma_f32_16x16x32_bf16 v[216:219],a[168:171],v[100:103],v[216:219]\nds_read_b64_tr_b16 a[200:201],v66 offset:20480\nds_read_b64_tr_b16 a[202:203],v66 offset:20992\nv_mfma_f32_16x16x32_bf16 v[220:223],a[172:175],v[100:103],v[220:223]\n" \
"ds_read_b64_tr_b16 a[204:205],v67 offset:20480\nds_read_b64_tr_b16 a[206:207],v67 offset:20992\ns_waitcnt lgkmcnt(8)\nv_mfma_f32_16x16x32_bf16 v[224:227],a[176:179],v[100:103],v[224:227]\n" \
"s_waitcnt vmcnt(13)\nds_read_b64_tr_b16 a[208:209],v64 offset:24576\nds_read_b64_tr_b16 a[210:211],v64 offset:25088\nv_mfma_f32_16x16x32_bf16 v[228:231],a[180:183],v[100:103],v[228:231]\n" \
"ds_read_b64_tr_b16 a[212:213],v65 offset:24576\nds_read_b64_tr_b16 a[214:215],v65 offset:25088\nv_mfma_f32_16x16x32_bf16 v[232:235],a[184:187],v[100:103],v[232:235]\nds_read_b64_tr_b16 a[216:217],v66 offset:24576\n" \
"ds_read_b64_tr_b16 a[218:219],v66 offset:25088\nv_mfma_f32_16x16x32_bf16 v[236:239],a[188:191],v[100:103],v[236:239]\nds_read_b64_tr_b16 a[220:221],v67 offset:24576\nds_read_b64_tr_b16 a[222:223],v67 offset:25088\n" \
"s_waitcnt lgkmcnt(8)\nv_mfma_f32_16x16x32_bf16 v[208:211],a[192:195],v[104:107],v[208:211]\nbuffer_load_dwordx4 v[100:103],v59,s[36:39],s70 offen offset:1024 sc1\nv_mfma_f32_16x16x32_bf16 v[212:215],a[196:199],v[104:107],v[212:215]\n" \
"v_mfma_f32_16x16x32_bf16 v[216:219],a[200:203],v[104:107],v[216:219]\nv_mfma_f32_16x16x32_bf16 v[220:223],a[204:207],v[104:107],v[220:223]\ns_waitcnt lgkmcnt(0)\nv_mfma_f32_16x16x32_bf16 v[224:227],a[208:211],v[104:107],v[224:227]\n" \
"v_mfma_f32_16x16x32_bf16 v[228:231],a[212:215],v[104:107],v[228:231]\nv_mfma_f32_16x16x32_bf16 v[232:235],a[216:219],v[104:107],v[232:235]\nv_mfma_f32_16x16x32_bf16 v[236:239],a[220:223],v[104:107],v[236:239]\nbuffer_load_dwordx4 v[104:107],v59,s[36:39],s70 offen offset:2048 sc1\n" \
"v_mfma_f32_16x16x32_bf16 v[16:19],a[112:115],v[192:195],v[16:19]\nv_mfma_f32_16x16x32_bf16 v[20:23],a[116:119],v[192:195],v[20:23]\nds_write_addtid_b32 v208\nds_write_addtid_b32 v209 offset:256\n" \
"v_mfma_f32_16x16x32_bf16 v[24:27],a[120:123],v[192:195],v[24:27]\nds_write_addtid_b32 v210 offset:512\nds_write_addtid_b32 v211 offset:768\nv_mfma_f32_16x16x32_bf16 v[28:31],a[124:127],v[192:195],v[28:31]\n" \
"ds_write_addtid_b32 v212 offset:1024\nds_write_addtid_b32 v213 offset:1280\ns_waitcnt vmcnt(14)\nv_mfma_f32_16x16x32_bf16 v[0:3],a[128:131],v[196:199],v[0:3]\n" \
"ds_write_addtid_b32 v214 offset:1536\nds_write_addtid_b32 v215 offset:1792\nv_mfma_f32_16x16x32_bf16 v[4:7],a[132:135],v[196:199],v[4:7]\nds_write_addtid_b32 v216 offset:2048\n" \
"ds_write_addtid_b32 v217 offset:2304\nv_mfma_f32_16x16x32_bf16 v[8:11],a[136:139],v[196:199],v[8:11]\nds_write_addtid_b32 v218 offset:2560\nds_write_addtid_b32 v219 offset:2816\n" \
"v_mfma_f32_16x16x32_bf16 v[12:15],a[140:143],v[196:199],v[12:15]\nds_write_addtid_b32 v220 offset:3072\nds_write_addtid_b32 v221 offset:3328\nv_mfma_f32_16x16x32_bf16 v[16:19],a[144:147],v[196:199],v[16:19]\n" \
"ds_write_addtid_b32 v222 offset:3584\nds_write_addtid_b32 v223 offset:3840\nv_mfma_f32_16x16x32_bf16 v[20:23],a[148:151],v[196:199],v[20:23]\nds_write_addtid_b32 v224 offset:4096\n" \
"ds_write_addtid_b32 v225 offset:4352\nv_mfma_f32_16x16x32_bf16 v[24:27],a[152:155],v[196:199],v[24:27]\nds_write_addtid_b32 v226 offset:4608\nds_write_addtid_b32 v227 offset:4864\n" \
"v_mfma_f32_16x16x32_bf16 v[28:31],a[156:159],v[196:199],v[28:31]\nds_write_addtid_b32 v228 offset:5120\nds_write_addtid_b32 v229 offset:5376\ns_waitcnt vmcnt(13)\n" \
"v_mfma_f32_16x16x32_bf16 v[0:3],a[160:163],v[200:203],v[0:3]\nds_write_addtid_b32 v230 offset:5632\nds_write_addtid_b32 v231 offset:5888\nv_mfma_f32_16x16x32_bf16 v[4:7],a[164:167],v[200:203],v[4:7]\n" \
"ds_write_addtid_b32 v232 offset:6144\nds_write_addtid_b32 v233 offset:6400\nv_mfma_f32_16x16x32_bf16 v[8:11],a[168:171],v[200:203],v[8:11]\nds_write_addtid_b32 v234 offset:6656\n" \
"ds_write_addtid_b32 v235 offset:6912\nv_mfma_f32_16x16x32_bf16 v[12:15],a[172:175],v[200:203],v[12:15]\nds_write_addtid_b32 v236 offset:7168\nds_write_addtid_b32 v237 offset:7424\n" \
"v_mfma_f32_16x16x32_bf16 v[16:19],a[176:179],v[200:203],v[16:19]\nds_write_addtid_b32 v238 offset:7680\nds_write_addtid_b32 v239 offset:7936\nv_mfma_f32_16x16x32_bf16 v[20:23],a[180:183],v[200:203],v[20:23]\n" \
"v_mfma_f32_16x16x32_bf16 v[24:27],a[184:187],v[200:203],v[24:27]\nv_mfma_f32_16x16x32_bf16 v[28:31],a[188:191],v[200:203],v[28:31]\ns_waitcnt vmcnt(12)\nv_mfma_f32_16x16x32_bf16 v[0:3],a[192:195],v[204:207],v[0:3]\n" \
"v_mfma_f32_16x16x32_bf16 v[4:7],a[196:199],v[204:207],v[4:7]\nv_mfma_f32_16x16x32_bf16 v[8:11],a[200:203],v[204:207],v[8:11]\ns_waitcnt lgkmcnt(0)\ns_barrier\n" \
"ds_read_b128 v[32:35],v58\nv_mfma_f32_16x16x32_bf16 v[12:15],a[204:207],v[204:207],v[12:15]\nds_read_b128 v[36:39],v58 offset:8192\nds_read_b128 v[40:43],v58 offset:16384\n" \
"v_mfma_f32_16x16x32_bf16 v[16:19],a[208:211],v[204:207],v[16:19]\nds_read_b128 v[44:47],v58 offset:24576\nds_read_b128 v[48:51],v58 offset:4096\nv_mfma_f32_16x16x32_bf16 v[20:23],a[212:215],v[204:207],v[20:23]\n" \
"ds_read_b128 v[240:243],v58 offset:12288\nds_read_b128 v[244:247],v58 offset:20480\nv_mfma_f32_16x16x32_bf16 v[24:27],a[216:219],v[204:207],v[24:27]\nds_read_b128 v[248:251],v58 offset:28672\n" \
"v_mfma_f32_16x16x32_bf16 v[28:31],a[220:223],v[204:207],v[28:31]\ns_waitcnt vmcnt(7)\nv_mfma_f32_16x16x32_bf16 v[208:211],v[128:131],v[72:75],0\ns_mov_b64 exec,1\n" \
"buffer_store_dword v61,off,s[36:39],s54 sc1\ns_mov_b64 exec,-1\nv_mfma_f32_16x16x32_bf16 v[212:215],v[132:135],v[72:75],0\nv_cmp_eq_u32 vcc,0,v63\n" \
"ds_write_addtid_b32 v0 offset:32768\nv_mfma_f32_16x16x32_bf16 v[216:219],v[136:139],v[72:75],0\nds_write_addtid_b32 v1 offset:33024\nds_write_addtid_b32 v2 offset:33280\n" \
"v_mfma_f32_16x16x32_bf16 v[220:223],v[140:143],v[72:75],0\ns_and_b64 vcc,vcc,s[86:87]\ns_cbranch_scc1 .LsbF_%=\n.LbbF_%=:\n" \
"buffer_load_dwordx4 v[108:111],v59,s[36:39],s48 offen sc1\nv_mfma_f32_16x16x32_bf16 v[224:227],v[144:147],v[72:75],0\nds_write_addtid_b32 v3 offset:33536\nds_write_addtid_b32 v4 offset:33792\n" \
"s_waitcnt lgkmcnt(9)\nv_mfma_f32_16x16x32_bf16 v[228:231],v[148:151],v[72:75],0\nds_write_addtid_b32 v5 offset:34048\nds_write_addtid_b32 v6 offset:34304\n" \
"v_add_f32 v32,v32,v36\nv_mfma_f32_16x16x32_bf16 v[232:235],v[152:155],v[72:75],0\nbuffer_load_dwordx4 v[112:115],v59,s[36:39],s48 offen offset:1024 sc1\nds_write_addtid_b32 v7 offset:34560\n" \
"ds_write_addtid_b32 v8 offset:34816\nv_mfma_f32_16x16x32_bf16 v[236:239],v[156:159],v[72:75],0\nds_write_addtid_b32 v9 offset:35072\nds_write_addtid_b32 v10 offset:35328\n" \
"v_add_f32 v33,v33,v37\nv_mfma_f32_16x16x32_bf16 v[208:211],v[160:163],v[76:79],v[208:211]\nds_write_addtid_b32 v11 offset:35584\nds_write_addtid_b32 v12 offset:35840\n" \
"v_add_f32 v34,v34,v38\nv_mfma_f32_16x16x32_bf16 v[212:215],v[164:167],v[76:79],v[212:215]\nbuffer_load_dwordx4 v[116:119],v59,s[36:39],s48 offen offset:2048 sc1\nds_write_addtid_b32 v13 offset:36096\n" \
"ds_write_addtid_b32 v14 offset:36352\nv_mfma_f32_16x16x32_bf16 v[216:219],v[168:171],v[76:79],v[216:219]\nds_write_addtid_b32 v15 offset:36608\nds_write_addtid_b32 v16 offset:36864\n" \
"v_add_f32 v35,v35,v39\nv_mfma_f32_16x16x32_bf16 v[220:223],v[172:175],v[76:79],v[220:223]\nds_write_addtid_b32 v17 offset:37120\nds_write_addtid_b32 v18 offset:37376\n" \
"v_add_f32 v40,v40,v44\nv_mfma_f32_16x16x32_bf16 v[224:227],v[176:179],v[76:79],v[224:227]\nbuffer_load_dwordx4 v[120:123],v59,s[36:39],s49 offen sc1\nds_write_addtid_b32 v19 offset:37632\n" \
"ds_write_addtid_b32 v20 offset:37888\nv_mfma_f32_16x16x32_bf16 v[228:231],v[180:183],v[76:79],v[228:231]\nds_write_addtid_b32 v21 offset:38144\nds_write_addtid_b32 v22 offset:38400\n" \
"v_add_f32 v41,v41,v45\nv_mfma_f32_16x16x32_bf16 v[232:235],v[184:187],v[76:79],v[232:235]\nds_write_addtid_b32 v23 offset:38656\nds_write_addtid_b32 v24 offset:38912\n" \
"v_add_f32 v42,v42,v46\nv_mfma_f32_16x16x32_bf16 v[236:239],v[188:191],v[76:79],v[236:239]\nbuffer_load_dwordx4 v[124:127],v59,s[36:39],s49 offen offset:1024 sc1\nds_write_addtid_b32 v25 offset:39168\n" \
"s_waitcnt vmcnt(11)\nv_mfma_f32_16x16x32_bf16 v[208:211],a[0:3],v[80:83],v[208:211]\nds_write_addtid_b32 v26 offset:39424\nds_write_addtid_b32 v27 offset:39680\n" \
"v_add_f32 v43,v43,v47\nv_mfma_f32_16x16x32_bf16 v[212:215],a[4:7],v[80:83],v[212:215]\nds_write_addtid_b32 v28 offset:39936\nds_write_addtid_b32 v29 offset:40192\n" \
"v_add_f32 v32,v32,v40\nv_mfma_f32_16x16x32_bf16 v[216:219],a[8:11],v[80:83],v[216:219]\nbuffer_load_dwordx4 v[192:195],v59,s[36:39],s49 offen offset:2048 sc1\nds_write_addtid_b32 v30 offset:40448\n" \
"ds_write_addtid_b32 v31 offset:40704\nv_mfma_f32_16x16x32_bf16 v[220:223],a[12:15],v[80:83],v[220:223]\nv_add_f32 v33,v33,v41\nv_add_f32 v34,v34,v42\n" \
"v_add_f32 v35,v35,v43\nv_mfma_f32_16x16x32_bf16 v[224:227],a[16:19],v[80:83],v[224:227]\nv_cvt_pk_bf16_f32 v36,v32,v33\nv_cvt_pk_bf16_f32 v37,v34,v35\n" \
"s_waitcnt lgkmcnt(15)\nv_mfma_f32_16x16x32_bf16 v[228:231],a[20:23],v[80:83],v[228:231]\nbuffer_load_dwordx4 v[196:199],v59,s[36:39],s50 offen sc1\nv_add_f32 v48,v48,v240\n" \
"v_add_f32 v49,v49,v241\nv_mfma_f32_16x16x32_bf16 v[232:235],a[24:27],v[80:83],v[232:235]\nv_add_f32 v50,v50,v242\nv_add_f32 v51,v51,v243\n" \
"v_add_f32 v244,v244,v248\nv_mfma_f32_16x16x32_bf16 v[236:239],a[28:31],v[80:83],v[236:239]\nv_add_f32 v245,v245,v249\nv_add_f32 v246,v246,v250\n" \
"v_add_f32 v247,v247,v251\nv_mfma_f32_16x16x32_bf16 v[208:211],a[32:35],v[84:87],v[208:211]\nbuffer_load_dwordx4 v[200:203],v59,s[36:39],s50 offen offset:1024 sc1\ns_waitcnt lgkmcnt(0)\n" \
"s_barrier\nv_mfma_f32_16x16x32_bf16 v[212:215],a[36:39],v[84:87],v[212:215]\nv_add_f32 v48,v48,v244\nv_add_f32 v49,v49,v245\n" \
"v_add_f32 v50,v50,v246\nv_mfma_f32_16x16x32_bf16 v[216:219],a[40:43],v[84:87],v[216:219]\nv_add_f32 v51,v51,v247\nv_cvt_pk_bf16_f32 v38,v48,v49\n" \
"v_cvt_pk_bf16_f32 v39,v50,v51\nv_mfma_f32_16x16x32_bf16 v[220:223],a[44:47],v[84:87],v[220:223]\nbuffer_load_dwordx4 v[204:207],v59,s[36:39],s50 offen offset:2048 sc1\nv_mfma_f32_16x16x32_bf16 v[224:227],a[48:51],v[84:87],v[224:227]\n" \
"v_mfma_f32_16x16x32_bf16 v[228:231],a[52:55],v[84:87],v[228:231]\nv_mfma_f32_16x16x32_bf16 v[232:235],a[56:59],v[84:87],v[232:235]\nbuffer_store_dwordx4 v[36:39],v59,s[36:39],s74 offen sc1\nds_read_b128 v[32:35],v58 offset:32768\n" \
"s_nop 0\nds_read_b128 v[36:39],v58 offset:40960\nv_mfma_f32_16x16x32_bf16 v[236:239],a[60:63],v[84:87],v[236:239]\nds_read_b128 v[40:43],v58 offset:49152\n" \
"ds_read_b128 v[44:47],v58 offset:57344\ns_waitcnt vmcnt(14)\nv_mfma_f32_16x16x32_bf16 v[208:211],a[64:67],v[88:91],v[208:211]\nds_read_b128 v[48:51],v58 offset:36864\n" \
"ds_read_b128 v[240:243],v58 offset:45056\nv_mfma_f32_16x16x32_bf16 v[212:215],a[68:71],v[88:91],v[212:215]\nds_read_b128 v[244:247],v58 offset:53248\nds_read_b128 v[248:251],v58 offset:61440\n" \
"v_mfma_f32_16x16x32_bf16 v[216:219],a[72:75],v[88:91],v[216:219]\nv_mfma_f32_16x16x32_bf16 v[220:223],a[76:79],v[88:91],v[220:223]\nv_mfma_f32_16x16x32_bf16 v[224:227],a[80:83],v[88:91],v[224:227]\nv_mfma_f32_16x16x32_bf16 v[228:231],a[84:87],v[88:91],v[228:231]\n" \
"v_mfma_f32_16x16x32_bf16 v[232:235],a[88:91],v[88:91],v[232:235]\nv_mfma_f32_16x16x32_bf16 v[236:239],a[92:95],v[88:91],v[236:239]\ns_waitcnt lgkmcnt(4)\nv_add_f32 v32,v32,v36\n" \
"v_add_f32 v33,v33,v37\nv_mfma_f32_16x16x32_bf16 v[208:211],a[96:99],v[92:95],v[208:211]\nv_add_f32 v34,v34,v38\nv_add_f32 v35,v35,v39\n" \
"v_add_f32 v40,v40,v44\nv_mfma_f32_16x16x32_bf16 v[212:215],a[100:103],v[92:95],v[212:215]\nv_add_f32 v41,v41,v45\nv_add_f32 v42,v42,v46\n" \
"v_add_f32 v43,v43,v47\nv_mfma_f32_16x16x32_bf16 v[216:219],a[104:107],v[92:95],v[216:219]\nv_add_f32 v32,v32,v40\nv_add_f32 v33,v33,v41\n" \
"v_add_f32 v34,v34,v42\nv_mfma_f32_16x16x32_bf16 v[220:223],a[108:111],v[92:95],v[220:223]\nv_add_f32 v35,v35,v43\nv_cvt_pk_bf16_f32 v36,v32,v33\n" \
"v_cvt_pk_bf16_f32 v37,v34,v35\nv_mfma_f32_16x16x32_bf16 v[224:227],a[112:115],v[92:95],v[224:227]\ns_waitcnt lgkmcnt(0)\nv_add_f32 v48,v48,v240\n" \
"v_add_f32 v49,v49,v241\nv_mfma_f32_16x16x32_bf16 v[228:231],a[116:119],v[92:95],v[228:231]\nv_add_f32 v50,v50,v242\nv_add_f32 v51,v51,v243\n" \
"v_add_f32 v244,v244,v248\nv_mfma_f32_16x16x32_bf16 v[232:235],a[120:123],v[92:95],v[232:235]\nv_add_f32 v245,v245,v249\nv_add_f32 v246,v246,v250\n" \
"v_add_f32 v247,v247,v251\nv_mfma_f32_16x16x32_bf16 v[236:239],a[124:127],v[92:95],v[236:239]\nv_add_f32 v48,v48,v244\nv_add_f32 v49,v49,v245\n" \
"s_waitcnt vmcnt(12)\nv_mfma_f32_16x16x32_bf16 v[208:211],a[128:131],v[96:99],v[208:211]\nv_add_f32 v50,v50,v246\nv_add_f32 v51,v51,v247\n" \
"v_cvt_pk_bf16_f32 v38,v48,v49\nv_mfma_f32_16x16x32_bf16 v[212:215],a[132:135],v[96:99],v[212:215]\nv_cvt_pk_bf16_f32 v39,v50,v51\nv_add_u32 v59,409600,v59\n" \
"buffer_store_dwordx4 v[36:39],v59,s[36:39],s74 offen sc1\nv_mfma_f32_16x16x32_bf16 v[216:219],a[136:139],v[96:99],v[216:219]\nv_mfma_f32_16x16x32_bf16 v[220:223],a[140:143],v[96:99],v[220:223]\nv_mfma_f32_16x16x32_bf16 v[224:227],a[144:147],v[96:99],v[224:227]\n" \
"v_mfma_f32_16x16x32_bf16 v[228:231],a[148:151],v[96:99],v[228:231]\nv_mfma_f32_16x16x32_bf16 v[232:235],a[152:155],v[96:99],v[232:235]\nv_mfma_f32_16x16x32_bf16 v[236:239],a[156:159],v[96:99],v[236:239]\nv_mfma_f32_16x16x32_bf16 v[208:211],a[160:163],v[100:103],v[208:211]\n" \
"v_mfma_f32_16x16x32_bf16 v[212:215],a[164:167],v[100:103],v[212:215]\nv_mfma_f32_16x16x32_bf16 v[216:219],a[168:171],v[100:103],v[216:219]\nv_mfma_f32_16x16x32_bf16 v[220:223],a[172:175],v[100:103],v[220:223]\nv_mfma_f32_16x16x32_bf16 v[224:227],a[176:179],v[100:103],v[224:227]\n" \
"v_mfma_f32_16x16x32_bf16 v[228:231],a[180:183],v[100:103],v[228:231]\nv_mfma_f32_16x16x32_bf16 v[232:235],a[184:187],v[100:103],v[232:235]\nv_mfma_f32_16x16x32_bf16 v[236:239],a[188:191],v[100:103],v[236:239]\ns_waitcnt vmcnt(12)\n" \
"v_mfma_f32_16x16x32_bf16 v[208:211],a[192:195],v[104:107],v[208:211]\nv_mfma_f32_16x16x32_bf16 v[212:215],a[196:199],v[104:107],v[212:215]\nv_mfma_f32_16x16x32_bf16 v[216:219],a[200:203],v[104:107],v[216:219]\nv_mfma_f32_16x16x32_bf16 v[220:223],a[204:207],v[104:107],v[220:223]\n" \
"v_mfma_f32_16x16x32_bf16 v[224:227],a[208:211],v[104:107],v[224:227]\nv_mfma_f32_16x16x32_bf16 v[228:231],a[212:215],v[104:107],v[228:231]\nv_mfma_f32_16x16x32_bf16 v[232:235],a[216:219],v[104:107],v[232:235]\nv_mfma_f32_16x16x32_bf16 v[236:239],a[220:223],v[104:107],v[236:239]\n" \
"s_branch .Le_%=\n.Lzl_%=:\ns_branch .Lzr_%=\n.Lls_%=:\n" \
"v_mfma_f32_16x16x32_bf16 v[20:23],v[64:67],a[0:3],0\nv_and_b32 v59,0x3ff,v59\ns_add_u32 s48,s68,409600\ns_add_u32 s49,s69,409600\n" \
"v_mfma_f32_16x16x32_bf16 v[24:27],v[64:67],a[4:7],0\ns_add_u32 s50,s70,409600\nds_write_addtid_b32 v0\nds_write_addtid_b32 v1 offset:544\n" \
"v_mfma_f32_16x16x32_bf16 v[28:31],v[64:67],a[8:11],0\nds_write_addtid_b32 v2 offset:256\nds_write_addtid_b32 v3 offset:800\nv_mfma_f32_16x16x32_bf16 v[32:35],v[64:67],a[12:15],0\n" \
"ds_write_addtid_b32 v4 offset:1056\nds_write_addtid_b32 v5 offset:1600\nv_mfma_f32_16x16x32_bf16 v[20:23],v[68:71],a[16:19],v[20:23]\nds_write_addtid_b32 v6 offset:1312\n" \
"ds_write_addtid_b32 v7 offset:1856\nv_mfma_f32_16x16x32_bf16 v[24:27],v[68:71],a[20:23],v[24:27]\nds_write_addtid_b32 v8 offset:2112\nds_write_addtid_b32 v9 offset:2656\n" \
"v_mfma_f32_16x16x32_bf16 v[28:31],v[68:71],a[24:27],v[28:31]\nds_write_addtid_b32 v10 offset:2368\nds_write_addtid_b32 v11 offset:2912\nv_mfma_f32_16x16x32_bf16 v[32:35],v[68:71],a[28:31],v[32:35]\n" \
"ds_write_addtid_b32 v12 offset:3168\nds_write_addtid_b32 v13 offset:3712\ns_waitcnt vmcnt(16)\nv_mfma_f32_16x16x32_bf16 v[20:23],v[72:75],a[32:35],v[20:23]\n" \
"s_waitcnt vmcnt(7)\nv_cmp_eq_u32 vcc,0,v63\nv_add_u32 v71,s57,v59\nv_mfma_f32_16x16x32_bf16 v[24:27],v[72:75],a[36:39],v[24:27]\n" \
"ds_read_b32 v64,v71 offset:28672\nds_read_b64 v[68:69],v71 offset:29696\nv_mfma_f32_16x16x32_bf16 v[28:31],v[72:75],a[40:43],v[28:31]\ns_and_b64 vcc,vcc,s[86:87]\n" \
"s_cbranch_scc1 .LsaS_%=\n.LbaS_%=:\nds_write_addtid_b32 v14 offset:3424\nv_mfma_f32_16x16x32_bf16 v[32:35],v[72:75],a[44:47],v[32:35]\n" \
"ds_write_addtid_b32 v15 offset:3968\nds_write_addtid_b32 v16 offset:4224\ns_waitcnt vmcnt(15)\nv_mfma_f32_16x16x32_bf16 v[20:23],v[76:79],a[48:51],v[20:23]\n" \
"buffer_load_dwordx4 v[72:75],v59,s[36:39],s68 offen sc1\nds_write_addtid_b32 v17 offset:4512\nds_write_addtid_b32 v18 offset:4800\nv_mfma_f32_16x16x32_bf16 v[24:27],v[76:79],a[52:55],v[24:27]\n" \
"s_waitcnt lgkmcnt(5)\nv_xor_b32 v65,32,v64\nv_xor_b32 v66,64,v64\nv_mfma_f32_16x16x32_bf16 v[28:31],v[76:79],a[56:59],v[28:31]\n" \
"v_xor_b32 v67,96,v64\nv_add_u32 v70,0xf800,v68\nv_add_u32 v71,0xf800,v69\nv_mfma_f32_16x16x32_bf16 v[32:35],v[76:79],a[60:63],v[32:35]\n" \
"ds_write_addtid_b32 v19 offset:5088\nds_read_b64_tr_b16 a[0:1],v64\nv_mfma_f32_16x16x32_bf16 v[20:23],v[80:83],a[64:67],v[20:23]\nbuffer_load_dwordx4 v[76:79],v59,s[36:39],s68 offen offset:1024 sc1\n" \
"ds_read_b64_tr_b16 a[2:3],v64 offset:512\nds_read_b64_tr_b16 a[4:5],v65\nv_mfma_f32_16x16x32_bf16 v[24:27],v[80:83],a[68:71],v[24:27]\nds_read_b64_tr_b16 a[6:7],v65 offset:512\n" \
"ds_read_b64_tr_b16 a[8:9],v66\nv_mfma_f32_16x16x32_bf16 v[28:31],v[80:83],a[72:75],v[28:31]\nds_read_b64_tr_b16 a[10:11],v66 offset:512\nds_read_b64_tr_b16 a[12:13],v67\n" \
"v_mfma_f32_16x16x32_bf16 v[32:35],v[80:83],a[76:79],v[32:35]\nds_read_b64_tr_b16 a[14:15],v67 offset:512\nv_mfma_f32_16x16x32_bf16 v[20:23],v[84:87],a[80:83],v[20:23]\nbuffer_load_dwordx4 v[80:83],v59,s[36:39],s68 offen offset:2048 sc1\n" \
"v_rcp_f32 v42,v42\nv_mfma_f32_16x16x32_bf16 v[24:27],v[84:87],a[84:87],v[24:27]\ns_waitcnt lgkmcnt(0)\ns_barrier\n" \
"v_mfma_f32_16x16x32_bf16 v[28:31],v[84:87],a[88:91],v[28:31]\nv_rcp_f32 v43,v43\nv_mul_f32 v36,v36,v40\nv_mfma_f32_16x16x32_bf16 v[32:35],v[84:87],a[92:95],v[32:35]\n" \
"v_mul_f32 v37,v37,v41\nv_mul_f32 v38,v38,v42\nv_mfma_f32_16x16x32_bf16 v[20:23],v[88:91],a[96:99],v[20:23]\nbuffer_load_dwordx4 v[84:87],v59,s[36:39],s69 offen sc1\n" \
"s_add_u32 m0,s57,0\nv_mfma_f32_16x16x32_bf16 v[24:27],v[88:91],a[100:103],v[24:27]\nds_read_b64_tr_b16 a[16:17],v64 offset:4096\nds_read_b64_tr_b16 a[18:19],v64 offset:4608\n" \
"v_mul_f32 v39,v39,v43\nv_mfma_f32_16x16x32_bf16 v[28:31],v[88:91],a[104:107],v[28:31]\nds_read_b64_tr_b16 a[20:21],v65 offset:4096\nds_read_b64_tr_b16 a[22:23],v65 offset:4608\n" \
"v_cvt_pk_bf16_f32 v40,v36,v37\nv_mfma_f32_16x16x32_bf16 v[32:35],v[88:91],a[108:111],v[32:35]\nbuffer_load_dwordx4 v68,s[40:43],s60 offen lds\nds_read_b64_tr_b16 a[24:25],v66 offset:4096\n" \
"v_mfma_f32_16x16x32_bf16 v[20:23],v[92:95],a[112:115],v[20:23]\nds_read_b64_tr_b16 a[26:27],v66 offset:4608\nds_read_b64_tr_b16 a[28:29],v67 offset:4096\nv_cvt_pk_bf16_f32 v41,v38,v39\n" \
"v_mfma_f32_16x16x32_bf16 v[24:27],v[92:95],a[116:119],v[24:27]\nds_read_b64_tr_b16 a[30:31],v67 offset:4608\nbuffer_store_dwordx2 v[40:41],v61,s[36:39],s52 offen sc1\nds_read_b128 v[36:39],v58\n" \
"v_mfma_f32_16x16x32_bf16 v[28:31],v[92:95],a[120:123],v[28:31]\nbuffer_load_dwordx4 v69,s[40:43],s60 offen offset:1024 lds\nds_read_b128 v[40:43],v58 offset:9568\nds_read_b128 v[44:47],v58 offset:19136\n" \
"v_mfma_f32_16x16x32_bf16 v[32:35],v[92:95],a[124:127],v[32:35]\nds_read_b128 v[48:51],v58 offset:28704\ns_add_u32 s52,s52,409600\nv_mfma_f32_16x16x32_bf16 v[20:23],v[96:99],a[128:131],v[20:23]\n" \
"v_mfma_f32_16x16x32_bf16 v[24:27],v[96:99],a[132:135],v[24:27]\nbuffer_load_dwordx4 v70,s[40:43],s60 offen offset:2048 lds\nv_mfma_f32_16x16x32_bf16 v[28:31],v[96:99],a[136:139],v[28:31]\nds_read_b64_tr_b16 a[32:33],v64 offset:8192\n" \
"ds_read_b64_tr_b16 a[34:35],v64 offset:8704\nv_mfma_f32_16x16x32_bf16 v[32:35],v[96:99],a[140:143],v[32:35]\nds_read_b64_tr_b16 a[36:37],v65 offset:8192\nds_read_b64_tr_b16 a[38:39],v65 offset:8704\n" \
"s_waitcnt vmcnt(14)\nv_mfma_f32_16x16x32_bf16 v[20:23],v[100:103],a[144:147],v[20:23]\nbuffer_load_dwordx4 v71,s[40:43],s60 offen offset:3072 lds\nds_read_b64_tr_b16 a[40:41],v66 offset:8192\n" \
"ds_read_b64_tr_b16 a[42:43],v66 offset:8704\nv_mfma_f32_16x16x32_bf16 v[24:27],v[100:103],a[148:151],v[24:27]\nds_read_b64_tr_b16 a[44:45],v67 offset:8192\nds_read_b64_tr_b16 a[46:47],v67 offset:8704\n" \
"v_mfma_f32_16x16x32_bf16 v[28:31],v[100:103],a[152:155],v[28:31]\ns_waitcnt lgkmcnt(10)\nv_add_f32 v36,v36,v40\nv_add_f32 v37,v37,v41\n" \
"v_mfma_f32_16x16x32_bf16 v[32:35],v[100:103],a[156:159],v[32:35]\nbuffer_load_dwordx4 v[88:91],v59,s[36:39],s69 offen offset:1024 sc1\nv_add_f32 v38,v38,v42\ns_waitcnt vmcnt(15)\n" \
"v_mfma_f32_16x16x32_bf16 v[20:23],v[104:107],a[160:163],v[20:23]\nv_add_f32 v39,v39,v43\nv_add_f32 v36,v36,v52\nv_add_f32 v37,v37,v53\n" \
"v_mfma_f32_16x16x32_bf16 v[24:27],v[104:107],a[164:167],v[24:27]\nv_add_f32 v38,v38,v54\nv_add_f32 v39,v39,v55\ns_waitcnt lgkmcnt(8)\n" \
"v_mfma_f32_16x16x32_bf16 v[28:31],v[104:107],a[168:171],v[28:31]\nbuffer_load_dwordx4 v[92:95],v59,s[36:39],s69 offen offset:2048 sc1\ns_add_u32 m0,s57,4096\nv_mfma_f32_16x16x32_bf16 v[32:35],v[104:107],a[172:175],v[32:35]\n" \
"v_add_f32 v44,v44,v48\nv_add_f32 v45,v45,v49\ns_waitcnt vmcnt(15)\nv_mfma_f32_16x16x32_bf16 v[20:23],v[108:111],a[176:179],v[20:23]\n" \
"ds_read_b64_tr_b16 a[48:49],v64 offset:12288\nds_read_b64_tr_b16 a[50:51],v64 offset:12800\nv_add_f32 v46,v46,v50\nv_mfma_f32_16x16x32_bf16 v[24:27],v[108:111],a[180:183],v[24:27]\n" \
"buffer_load_dwordx4 v68,s[40:43],s61 offen lds\nds_read_b64_tr_b16 a[52:53],v65 offset:12288\nds_read_b64_tr_b16 a[54:55],v65 offset:12800\nv_mfma_f32_16x16x32_bf16 v[28:31],v[108:111],a[184:187],v[28:31]\n" \
"ds_read_b64_tr_b16 a[56:57],v66 offset:12288\nds_read_b64_tr_b16 a[58:59],v66 offset:12800\nv_add_f32 v47,v47,v51\nv_mfma_f32_16x16x32_bf16 v[32:35],v[108:111],a[188:191],v[32:35]\n" \
"ds_read_b64_tr_b16 a[60:61],v67 offset:12288\nds_read_b64_tr_b16 a[62:63],v67 offset:12800\ns_waitcnt vmcnt(15)\nv_mfma_f32_16x16x32_bf16 v[20:23],v[112:115],a[192:195],v[20:23]\n" \
"buffer_load_dwordx4 v69,s[40:43],s61 offen offset:1024 lds\nv_add_f32 v36,v36,v44\nv_add_f32 v37,v37,v45\nv_mfma_f32_16x16x32_bf16 v[24:27],v[112:115],a[196:199],v[24:27]\n" \
"v_add_f32 v38,v38,v46\nv_add_f32 v39,v39,v47\nv_mul_f32 v40,v36,v36\nv_mfma_f32_16x16x32_bf16 v[28:31],v[112:115],a[200:203],v[28:31]\n" \
"v_mul_f32 v41,v37,v37\nv_mul_f32 v42,v38,v38\nv_mul_f32 v43,v39,v39\nv_mfma_f32_16x16x32_bf16 v[32:35],v[112:115],a[204:207],v[32:35]\n" \
"buffer_load_dwordx4 v70,s[40:43],s61 offen offset:2048 lds\nv_fma_f32 v40,v40,s55,v57\ns_waitcnt vmcnt(16)\nv_mfma_f32_16x16x32_bf16 v[20:23],v[116:119],a[208:211],v[20:23]\n" \
"v_fma_f32 v41,v41,s55,v57\nv_fma_f32 v42,v42,s55,v57\nv_fma_f32 v43,v43,s55,v57\nv_mfma_f32_16x16x32_bf16 v[24:27],v[116:119],a[212:215],v[24:27]\n" \
"v_mul_f32 v40,v36,v40\nv_mul_f32 v41,v37,v41\nv_mul_f32 v42,v38,v42\nv_mfma_f32_16x16x32_bf16 v[28:31],v[116:119],a[216:219],v[28:31]\n" \
"buffer_load_dwordx4 v71,s[40:43],s61 offen offset:3072 lds\nds_read_b64_tr_b16 a[64:65],v64 offset:16384\nds_read_b64_tr_b16 a[66:67],v64 offset:16896\nv_mfma_f32_16x16x32_bf16 v[32:35],v[116:119],a[220:223],v[32:35]\n" \
"ds_read_b64_tr_b16 a[68:69],v65 offset:16384\nds_read_b64_tr_b16 a[70:71],v65 offset:16896\ns_waitcnt vmcnt(16)\nv_mfma_f32_16x16x32_bf16 v[20:23],v[120:123],a[224:227],v[20:23]\n" \
"ds_read_b64_tr_b16 a[72:73],v66 offset:16384\nds_read_b64_tr_b16 a[74:75],v66 offset:16896\nv_mul_f32 v43,v39,v43\nv_mfma_f32_16x16x32_bf16 v[24:27],v[120:123],a[228:231],v[24:27]\n" \
"buffer_load_dwordx4 v[108:111],v59,s[36:39],s48 offen sc1\nds_read_b64_tr_b16 a[76:77],v67 offset:16384\nds_read_b64_tr_b16 a[78:79],v67 offset:16896\nv_mfma_f32_16x16x32_bf16 v[28:31],v[120:123],a[232:235],v[28:31]\n" \
"v_exp_f32 v40,v40\nds_read_b32 v48,v60\nv_mfma_f32_16x16x32_bf16 v[32:35],v[120:123],a[236:239],v[32:35]\nv_exp_f32 v41,v41\n" \
"s_waitcnt vmcnt(16)\nv_mfma_f32_16x16x32_bf16 v[20:23],v[124:127],a[240:243],v[20:23]\nbuffer_load_dwordx4 v[112:115],v59,s[36:39],s48 offen offset:1024 sc1\nv_exp_f32 v42,v42\n" \
"v_mfma_f32_16x16x32_bf16 v[24:27],v[124:127],a[244:247],v[24:27]\nv_exp_f32 v43,v43\nv_add_f32 v40,1.0,v40\nv_mfma_f32_16x16x32_bf16 v[28:31],v[124:127],a[248:251],v[28:31]\n" \
"v_add_f32 v41,1.0,v41\nv_add_f32 v42,1.0,v42\nv_add_f32 v43,1.0,v43\nv_mfma_f32_16x16x32_bf16 v[32:35],v[124:127],a[252:255],v[32:35]\n" \
"buffer_load_dwordx4 v[116:119],v59,s[36:39],s48 offen offset:2048 sc1\ns_mov_b32 m0,s56\ns_waitcnt vmcnt(17)\nv_mfma_f32_16x16x32_bf16 v[208:211],v[128:131],v[72:75],0\n" \
"ds_read_b64_tr_b16 a[80:81],v64 offset:20480\nds_read_b64_tr_b16 a[82:83],v64 offset:20992\nv_mfma_f32_16x16x32_bf16 v[212:215],v[132:135],v[72:75],0\nds_write_addtid_b32 v20 offset:5344\n" \
"ds_write_addtid_b32 v21 offset:5888\nv_mfma_f32_16x16x32_bf16 v[216:219],v[136:139],v[72:75],0\nbuffer_load_dwordx4 v[120:123],v59,s[36:39],s49 offen sc1\nds_write_addtid_b32 v22 offset:5600\n" \
"ds_write_addtid_b32 v23 offset:6144\nv_mfma_f32_16x16x32_bf16 v[220:223],v[140:143],v[72:75],0\nds_write_addtid_b32 v24 offset:6400\nds_write_addtid_b32 v25 offset:6944\n" \
"v_mfma_f32_16x16x32_bf16 v[224:227],v[144:147],v[72:75],0\nds_write_addtid_b32 v26 offset:6656\nds_write_addtid_b32 v27 offset:7200\nv_rcp_f32 v40,v40\n" \
"v_mfma_f32_16x16x32_bf16 v[228:231],v[148:151],v[72:75],0\nbuffer_load_dwordx4 v[124:127],v59,s[36:39],s49 offen offset:1024 sc1\nds_write_addtid_b32 v28 offset:7456\nds_write_addtid_b32 v29 offset:8000\n" \
"v_mfma_f32_16x16x32_bf16 v[232:235],v[152:155],v[72:75],0\nds_write_addtid_b32 v30 offset:7712\nds_write_addtid_b32 v31 offset:8256\nv_rcp_f32 v41,v41\n" \
"v_mfma_f32_16x16x32_bf16 v[236:239],v[156:159],v[72:75],0\nds_write_addtid_b32 v32 offset:8512\nds_write_addtid_b32 v33 offset:9056\ns_waitcnt vmcnt(18)\n" \
"v_mfma_f32_16x16x32_bf16 v[208:211],v[160:163],v[76:79],v[208:211]\nv_rcp_f32 v42,v42\nv_rcp_f32 v43,v43\nv_mul_f32 v36,v36,v40\n" \
"v_mul_f32 v37,v37,v41\nv_mul_f32 v38,v38,v42\nv_mul_f32 v39,v39,v43\nv_cvt_pk_bf16_f32 v40,v36,v37\n" \
"v_cvt_pk_bf16_f32 v41,v38,v39\nbuffer_store_dwordx2 v[40:41],v61,s[36:39],s52 offen sc1\nv_mfma_f32_16x16x32_bf16 v[212:215],v[164:167],v[76:79],v[212:215]\nbuffer_load_dwordx4 v[192:195],v59,s[36:39],s49 offen offset:2048 sc1\n" \
"ds_write_addtid_b32 v34 offset:8768\nds_write_addtid_b32 v35 offset:9312\nv_mfma_f32_16x16x32_bf16 v[216:219],v[168:171],v[76:79],v[216:219]\nds_read_b64_tr_b16 a[84:85],v65 offset:20480\n" \
"ds_read_b64_tr_b16 a[86:87],v65 offset:20992\nv_mfma_f32_16x16x32_bf16 v[220:223],v[172:175],v[76:79],v[220:223]\nds_read_b64_tr_b16 a[88:89],v66 offset:20480\nds_read_b64_tr_b16 a[90:91],v66 offset:20992\n" \
"v_mfma_f32_16x16x32_bf16 v[224:227],v[176:179],v[76:79],v[224:227]\nbuffer_load_dwordx4 v[96:99],v59,s[36:39],s70 offen sc1\ns_waitcnt lgkmcnt(15)\ns_add_u32 m0,s57,8192\n" \
"v_mfma_f32_16x16x32_bf16 v[228:231],v[180:183],v[76:79],v[228:231]\nds_read_b64_tr_b16 a[92:93],v67 offset:20480\nds_read_b64_tr_b16 a[94:95],v67 offset:20992\nv_mfma_f32_16x16x32_bf16 v[232:235],v[184:187],v[76:79],v[232:235]\n" \
"ds_read_b64_tr_b16 a[96:97],v64 offset:24576\nds_read_b64_tr_b16 a[98:99],v64 offset:25088\nv_mfma_f32_16x16x32_bf16 v[236:239],v[188:191],v[76:79],v[236:239]\nbuffer_load_dwordx4 v68,s[40:43],s62 offen lds\n" \
"s_waitcnt lgkmcnt(15)\ns_waitcnt vmcnt(21)\nv_mfma_f32_16x16x32_bf16 v[208:211],a[0:3],v[80:83],v[208:211]\ns_waitcnt lgkmcnt(0)\n" \
"s_barrier\nds_read_b64_tr_b16 a[100:101],v65 offset:24576\nv_mfma_f32_16x16x32_bf16 v[212:215],a[4:7],v[80:83],v[212:215]\nds_read_b64_tr_b16 a[102:103],v65 offset:25088\n" \
"ds_read_b64_tr_b16 a[104:105],v66 offset:24576\nv_mfma_f32_16x16x32_bf16 v[216:219],a[8:11],v[80:83],v[216:219]\nbuffer_load_dwordx4 v69,s[40:43],s62 offen offset:1024 lds\nds_read_b64_tr_b16 a[106:107],v66 offset:25088\n" \
"ds_read_b64_tr_b16 a[108:109],v67 offset:24576\nv_mfma_f32_16x16x32_bf16 v[220:223],a[12:15],v[80:83],v[220:223]\nds_read_b64_tr_b16 a[110:111],v67 offset:25088\nds_read_b32 v49,v60 offset:9568\n" \
"v_mfma_f32_16x16x32_bf16 v[224:227],a[16:19],v[80:83],v[224:227]\nds_read_b32 v50,v60 offset:19136\nds_read_b32 v51,v60 offset:28704\nv_mfma_f32_16x16x32_bf16 v[228:231],a[20:23],v[80:83],v[228:231]\n" \
"buffer_load_dwordx4 v70,s[40:43],s62 offen offset:2048 lds\nv_mfma_f32_16x16x32_bf16 v[232:235],a[24:27],v[80:83],v[232:235]\ns_waitcnt vmcnt(17)\nds_read_b64_tr_b16 a[112:113],v64\n" \
"ds_read_b64_tr_b16 a[114:115],v64 offset:512\nv_mfma_f32_16x16x32_bf16 v[236:239],a[28:31],v[80:83],v[236:239]\nds_read_b64_tr_b16 a[116:117],v65\nv_mfma_f32_16x16x32_bf16 v[208:211],a[32:35],v[84:87],v[208:211]\n" \
"buffer_load_dwordx4 v71,s[40:43],s62 offen offset:3072 lds\ns_add_u32 m0,s57,12288\nv_mfma_f32_16x16x32_bf16 v[212:215],a[36:39],v[84:87],v[212:215]\nds_read_b64_tr_b16 a[118:119],v65 offset:512\n" \
"ds_read_b64_tr_b16 a[120:121],v66\nv_mfma_f32_16x16x32_bf16 v[216:219],a[40:43],v[84:87],v[216:219]\nds_read_b64_tr_b16 a[122:123],v66 offset:512\nds_read_b64_tr_b16 a[124:125],v67\n" \
"s_waitcnt lgkmcnt(7)\nv_mfma_f32_16x16x32_bf16 v[220:223],a[44:47],v[84:87],v[220:223]\nbuffer_load_dwordx4 v68,s[40:43],s63 offen lds\nds_read_b64_tr_b16 a[126:127],v67 offset:512\n" \
"v_mfma_f32_16x16x32_bf16 v[224:227],a[48:51],v[84:87],v[224:227]\nv_add_f32 v48,v48,v49\nv_add_f32 v50,v50,v51\nv_add_f32 v48,v48,v56\n" \
"v_mfma_f32_16x16x32_bf16 v[228:231],a[52:55],v[84:87],v[228:231]\nv_add_f32 v48,v48,v50\nv_mul_f32 v49,v48,v48\nv_fma_f32 v49,v49,s55,v57\n" \
"v_mfma_f32_16x16x32_bf16 v[232:235],a[56:59],v[84:87],v[232:235]\nbuffer_load_dwordx4 v69,s[40:43],s63 offen offset:1024 lds\nv_mul_f32 v49,v48,v49\nv_mfma_f32_16x16x32_bf16 v[236:239],a[60:63],v[84:87],v[236:239]\n" \
"s_waitcnt vmcnt(19)\nv_mfma_f32_16x16x32_bf16 v[208:211],a[64:67],v[88:91],v[208:211]\nv_exp_f32 v49,v49\nv_mfma_f32_16x16x32_bf16 v[212:215],a[68:71],v[88:91],v[212:215]\n" \
"buffer_load_dwordx4 v70,s[40:43],s63 offen offset:2048 lds\nv_add_f32 v49,1.0,v49\nv_mfma_f32_16x16x32_bf16 v[216:219],a[72:75],v[88:91],v[216:219]\nv_rcp_f32 v49,v49\n" \
"v_mfma_f32_16x16x32_bf16 v[220:223],a[76:79],v[88:91],v[220:223]\nv_mul_f32 v48,v48,v49\nv_cvt_pk_bf16_f32 v49,v48,v48\nv_mfma_f32_16x16x32_bf16 v[224:227],a[80:83],v[88:91],v[224:227]\n" \
"buffer_load_dwordx4 v71,s[40:43],s63 offen offset:3072 lds\nbuffer_store_short v49,v62,s[36:39],s52 offen sc1\nds_read_b128 v[36:39],v58 offset:5344\nv_mfma_f32_16x16x32_bf16 v[228:231],a[84:87],v[88:91],v[228:231]\n" \
"ds_read_b128 v[40:43],v58 offset:14912\nds_read_b128 v[44:47],v58 offset:24480\ns_add_u32 s52,s52,409600\nv_mfma_f32_16x16x32_bf16 v[232:235],a[88:91],v[88:91],v[232:235]\n" \
"ds_read_b128 v[48:51],v58 offset:34048\nv_mfma_f32_16x16x32_bf16 v[236:239],a[92:95],v[88:91],v[236:239]\nbuffer_load_dwordx4 v[100:103],v59,s[36:39],s70 offen offset:1024 sc1\ns_waitcnt vmcnt(22)\n" \
"v_mfma_f32_16x16x32_bf16 v[208:211],a[96:99],v[92:95],v[208:211]\ns_add_u32 m0,s57,16384\ns_waitcnt vmcnt(18)\nv_mfma_f32_16x16x32_bf16 v[212:215],a[100:103],v[92:95],v[212:215]\n" \
"ds_read_b64_tr_b16 a[128:129],v64 offset:4096\nds_read_b64_tr_b16 a[130:131],v64 offset:4608\nv_mfma_f32_16x16x32_bf16 v[216:219],a[104:107],v[92:95],v[216:219]\nbuffer_load_dwordx4 v68,s[40:43],s64 offen lds\n" \
"ds_read_b64_tr_b16 a[132:133],v65 offset:4096\nds_read_b64_tr_b16 a[134:135],v65 offset:4608\nv_mfma_f32_16x16x32_bf16 v[220:223],a[108:111],v[92:95],v[220:223]\nds_read_b64_tr_b16 a[136:137],v66 offset:4096\n" \
"ds_read_b64_tr_b16 a[138:139],v66 offset:4608\ns_waitcnt vmcnt(18)\nv_mfma_f32_16x16x32_bf16 v[0:3],v[128:131],v[108:111],0\nds_read_b64_tr_b16 a[140:141],v67 offset:4096\n" \
"ds_read_b64_tr_b16 a[142:143],v67 offset:4608\nv_mfma_f32_16x16x32_bf16 v[4:7],v[132:135],v[108:111],0\nbuffer_load_dwordx4 v69,s[40:43],s64 offen offset:1024 lds\ns_waitcnt lgkmcnt(10)\n" \
"v_add_f32 v36,v36,v40\nv_mfma_f32_16x16x32_bf16 v[8:11],v[136:139],v[108:111],0\nv_add_f32 v37,v37,v41\nv_add_f32 v38,v38,v42\n" \
"v_add_f32 v39,v39,v43\nv_mfma_f32_16x16x32_bf16 v[12:15],v[140:143],v[108:111],0\nv_add_f32 v36,v36,v52\nv_add_f32 v37,v37,v53\n" \
"v_add_f32 v38,v38,v54\nv_mfma_f32_16x16x32_bf16 v[16:19],v[144:147],v[108:111],0\nbuffer_load_dwordx4 v70,s[40:43],s64 offen offset:2048 lds\nv_add_f32 v39,v39,v55\n" \
"s_waitcnt lgkmcnt(8)\nv_mfma_f32_16x16x32_bf16 v[20:23],v[148:151],v[108:111],0\nv_add_f32 v44,v44,v48\nv_add_f32 v45,v45,v49\n" \
"v_add_f32 v46,v46,v50\nv_mfma_f32_16x16x32_bf16 v[24:27],v[152:155],v[108:111],0\nv_add_f32 v47,v47,v51\nv_add_f32 v36,v36,v44\n" \
"v_add_f32 v37,v37,v45\nv_mfma_f32_16x16x32_bf16 v[28:31],v[156:159],v[108:111],0\nbuffer_load_dwordx4 v71,s[40:43],s64 offen offset:3072 lds\nv_add_f32 v38,v38,v46\n" \
"s_waitcnt vmcnt(20)\nv_mfma_f32_16x16x32_bf16 v[0:3],v[160:163],v[112:115],v[0:3]\ns_add_u32 m0,s57,20480\nv_add_f32 v39,v39,v47\n" \
"v_mfma_f32_16x16x32_bf16 v[4:7],v[164:167],v[112:115],v[4:7]\nv_mul_f32 v40,v36,v36\nv_mul_f32 v41,v37,v37\nv_mul_f32 v42,v38,v38\n" \
"v_mfma_f32_16x16x32_bf16 v[8:11],v[168:171],v[112:115],v[8:11]\nbuffer_load_dwordx4 v68,s[40:43],s65 offen lds\nv_mul_f32 v43,v39,v39\nv_fma_f32 v40,v40,s55,v57\n" \
"v_mfma_f32_16x16x32_bf16 v[12:15],v[172:175],v[112:115],v[12:15]\nv_fma_f32 v41,v41,s55,v57\nv_fma_f32 v42,v42,s55,v57\nv_fma_f32 v43,v43,s55,v57\n" \
"v_mfma_f32_16x16x32_bf16 v[16:19],v[176:179],v[112:115],v[16:19]\nv_mul_f32 v40,v36,v40\nv_mul_f32 v41,v37,v41\nv_mul_f32 v42,v38,v42\n" \
"v_mfma_f32_16x16x32_bf16 v[20:23],v[180:183],v[112:115],v[20:23]\nbuffer_load_dwordx4 v69,s[40:43],s65 offen offset:1024 lds\nv_mul_f32 v43,v39,v43\nv_mfma_f32_16x16x32_bf16 v[24:27],v[184:187],v[112:115],v[24:27]\n" \
"v_exp_f32 v40,v40\nv_mfma_f32_16x16x32_bf16 v[28:31],v[188:191],v[112:115],v[28:31]\nv_exp_f32 v41,v41\ns_waitcnt vmcnt(21)\n" \
"v_mfma_f32_16x16x32_bf16 v[0:3],a[0:3],v[116:119],v[0:3]\nbuffer_load_dwordx4 v70,s[40:43],s65 offen offset:2048 lds\nv_exp_f32 v42,v42\nv_mfma_f32_16x16x32_bf16 v[4:7],a[4:7],v[116:119],v[4:7]\n" \
"v_exp_f32 v43,v43\nv_add_f32 v40,1.0,v40\nv_add_f32 v41,1.0,v41\nv_mfma_f32_16x16x32_bf16 v[8:11],a[8:11],v[116:119],v[8:11]\n" \
"v_add_f32 v42,1.0,v42\nv_add_f32 v43,1.0,v43\nv_rcp_f32 v40,v40\nv_mfma_f32_16x16x32_bf16 v[12:15],a[12:15],v[116:119],v[12:15]\n" \
"buffer_load_dwordx4 v71,s[40:43],s65 offen offset:3072 lds\nv_rcp_f32 v41,v41\nv_mfma_f32_16x16x32_bf16 v[16:19],a[16:19],v[116:119],v[16:19]\nv_rcp_f32 v42,v42\n" \
"v_mfma_f32_16x16x32_bf16 v[20:23],a[20:23],v[116:119],v[20:23]\nv_rcp_f32 v43,v43\nv_mul_f32 v36,v36,v40\nv_mfma_f32_16x16x32_bf16 v[24:27],a[24:27],v[116:119],v[24:27]\n" \
"v_mul_f32 v37,v37,v41\nv_mul_f32 v38,v38,v42\nv_mul_f32 v39,v39,v43\nv_cvt_pk_bf16_f32 v40,v36,v37\n" \
"v_cvt_pk_bf16_f32 v41,v38,v39\nv_mfma_f32_16x16x32_bf16 v[28:31],a[28:31],v[116:119],v[28:31]\nbuffer_load_dwordx4 v[104:107],v59,s[36:39],s70 offen offset:2048 sc1\nv_and_b32 v58,0x3ff,v59\n" \
"s_waitcnt vmcnt(23)\nv_mfma_f32_16x16x32_bf16 v[0:3],a[32:35],v[120:123],v[0:3]\ns_add_u32 m0,s57,24576\nv_add_u32 v58,s99,v58\n" \
"v_mfma_f32_16x16x32_bf16 v[4:7],a[36:39],v[120:123],v[4:7]\nv_add_u32 v46,s98,v58\nds_read_b64 v[56:57],v46 offset:28680\nv_mfma_f32_16x16x32_bf16 v[8:11],a[40:43],v[120:123],v[8:11]\n" \
"buffer_load_dwordx4 v68,s[40:43],s66 offen lds\nv_mfma_f32_16x16x32_bf16 v[12:15],a[44:47],v[120:123],v[12:15]\nv_mfma_f32_16x16x32_bf16 v[16:19],a[48:51],v[120:123],v[16:19]\nv_mfma_f32_16x16x32_bf16 v[20:23],a[52:55],v[120:123],v[20:23]\n" \
"buffer_load_dwordx4 v69,s[40:43],s66 offen offset:1024 lds\nv_mfma_f32_16x16x32_bf16 v[24:27],a[56:59],v[120:123],v[24:27]\ns_waitcnt vmcnt(17)\nds_read_b64_tr_b16 a[144:145],v64 offset:8192\n" \
"ds_read_b64_tr_b16 a[146:147],v64 offset:8704\nv_mfma_f32_16x16x32_bf16 v[28:31],a[60:63],v[120:123],v[28:31]\nds_read_b64_tr_b16 a[148:149],v65 offset:8192\nds_read_b64_tr_b16 a[150:151],v65 offset:8704\n" \
"v_mfma_f32_16x16x32_bf16 v[0:3],a[64:67],v[124:127],v[0:3]\nbuffer_load_dwordx4 v70,s[40:43],s66 offen offset:2048 lds\nds_read_b64_tr_b16 a[152:153],v66 offset:8192\nds_read_b64_tr_b16 a[154:155],v66 offset:8704\n" \
"v_mfma_f32_16x16x32_bf16 v[4:7],a[68:71],v[124:127],v[4:7]\nds_read_b64_tr_b16 a[156:157],v67 offset:8192\nds_read_b64_tr_b16 a[158:159],v67 offset:8704\nv_mfma_f32_16x16x32_bf16 v[8:11],a[72:75],v[124:127],v[8:11]\n" \
"v_mfma_f32_16x16x32_bf16 v[12:15],a[76:79],v[124:127],v[12:15]\nbuffer_load_dwordx4 v71,s[40:43],s66 offen offset:3072 lds\nv_mfma_f32_16x16x32_bf16 v[16:19],a[80:83],v[124:127],v[16:19]\ns_waitcnt lgkmcnt(0)\n" \
"s_barrier\ns_mov_b32 m0,s58\nv_mfma_f32_16x16x32_bf16 v[20:23],a[84:87],v[124:127],v[20:23]\nv_mfma_f32_16x16x32_bf16 v[24:27],a[88:91],v[124:127],v[24:27]\n" \
"buffer_load_dwordx4 v[196:199],v59,s[36:39],s50 offen sc1\nv_mfma_f32_16x16x32_bf16 v[28:31],a[92:95],v[124:127],v[28:31]\nv_mfma_f32_16x16x32_bf16 v[0:3],a[96:99],v[192:195],v[0:3]\nv_mfma_f32_16x16x32_bf16 v[4:7],a[100:103],v[192:195],v[4:7]\n" \
"buffer_load_dwordx4 v[200:203],v59,s[36:39],s50 offen offset:1024 sc1\nv_mfma_f32_16x16x32_bf16 v[8:11],a[104:107],v[192:195],v[8:11]\ns_waitcnt vmcnt(17)\nds_read_b64_tr_b16 a[160:161],v64 offset:12288\n" \
"ds_read_b64_tr_b16 a[162:163],v64 offset:12800\nv_mfma_f32_16x16x32_bf16 v[12:15],a[108:111],v[192:195],v[12:15]\nds_read_b64_tr_b16 a[164:165],v65 offset:12288\nds_read_b64_tr_b16 a[166:167],v65 offset:12800\n" \
"v_mfma_f32_16x16x32_bf16 v[224:227],a[112:115],v[92:95],v[224:227]\nbuffer_load_dwordx4 v[204:207],v59,s[36:39],s50 offen offset:2048 sc1\ns_add_u32 s59,s59,0x10000\nbuffer_load_dword v63,v61,s[36:39],s59 offen sc1\n" \
"v_mfma_f32_16x16x32_bf16 v[228:231],a[116:119],v[92:95],v[228:231]\nv_add_u32 v59,819200,v59\nds_read_b64_tr_b16 a[168:169],v66 offset:12288\nds_read_b64_tr_b16 a[170:171],v66 offset:12800\n" \
"v_mfma_f32_16x16x32_bf16 v[232:235],a[120:123],v[92:95],v[232:235]\nds_read_b64_tr_b16 a[172:173],v67 offset:12288\nds_read_b64_tr_b16 a[174:175],v67 offset:12800\nbuffer_store_dwordx2 v[40:41],v61,s[36:39],s52 offen sc1\n" \
"v_mfma_f32_16x16x32_bf16 v[236:239],a[124:127],v[92:95],v[236:239]\nbuffer_load_dwordx4 v[72:75],v59,s[36:39],s68 offen sc1\nv_mfma_f32_16x16x32_bf16 v[208:211],a[128:131],v[96:99],v[208:211]\nv_mfma_f32_16x16x32_bf16 v[212:215],a[132:135],v[96:99],v[212:215]\n" \
"v_mfma_f32_16x16x32_bf16 v[216:219],a[136:139],v[96:99],v[216:219]\nbuffer_load_dwordx4 v[76:79],v59,s[36:39],s68 offen offset:1024 sc1\nv_mfma_f32_16x16x32_bf16 v[220:223],a[140:143],v[96:99],v[220:223]\nv_mfma_f32_16x16x32_bf16 v[224:227],a[144:147],v[96:99],v[224:227]\n" \
"s_waitcnt vmcnt(16)\nds_read_b64_tr_b16 a[176:177],v64 offset:16384\nds_read_b64_tr_b16 a[178:179],v64 offset:16896\nv_mfma_f32_16x16x32_bf16 v[228:231],a[148:151],v[96:99],v[228:231]\n" \
"ds_read_b64_tr_b16 a[180:181],v65 offset:16384\nds_read_b64_tr_b16 a[182:183],v65 offset:16896\nbuffer_load_dwordx4 v[80:83],v59,s[36:39],s68 offen offset:2048 sc1\nv_mfma_f32_16x16x32_bf16 v[232:235],a[152:155],v[96:99],v[232:235]\n" \
"ds_read_b64_tr_b16 a[184:185],v66 offset:16384\nds_read_b64_tr_b16 a[186:187],v66 offset:16896\nv_mfma_f32_16x16x32_bf16 v[236:239],a[156:159],v[96:99],v[236:239]\nds_read_b64_tr_b16 a[188:189],v67 offset:16384\n" \
"ds_read_b64_tr_b16 a[190:191],v67 offset:16896\ns_waitcnt lgkmcnt(8)\nv_mfma_f32_16x16x32_bf16 v[208:211],a[160:163],v[100:103],v[208:211]\ns_waitcnt vmcnt(13)\n" \
"ds_read_b64_tr_b16 a[192:193],v64 offset:20480\nds_read_b64_tr_b16 a[194:195],v64 offset:20992\nv_mfma_f32_16x16x32_bf16 v[212:215],a[164:167],v[100:103],v[212:215]\nds_read_b64_tr_b16 a[196:197],v65 offset:20480\n" \
"ds_read_b64_tr_b16 a[198:199],v65 offset:20992\nbuffer_load_dwordx4 v[84:87],v59,s[36:39],s69 offen sc1\nv_mfma_f32_16x16x32_bf16 v[216:219],a[168:171],v[100:103],v[216:219]\nds_read_b64_tr_b16 a[200:201],v66 offset:20480\n" \
"ds_read_b64_tr_b16 a[202:203],v66 offset:20992\nv_mfma_f32_16x16x32_bf16 v[220:223],a[172:175],v[100:103],v[220:223]\nds_read_b64_tr_b16 a[204:205],v67 offset:20480\nds_read_b64_tr_b16 a[206:207],v67 offset:20992\n" \
"s_waitcnt lgkmcnt(8)\nv_mfma_f32_16x16x32_bf16 v[224:227],a[176:179],v[100:103],v[224:227]\ns_waitcnt vmcnt(9)\nds_read_b64_tr_b16 a[208:209],v64 offset:24576\n" \
"ds_read_b64_tr_b16 a[210:211],v64 offset:25088\nv_mfma_f32_16x16x32_bf16 v[228:231],a[180:183],v[100:103],v[228:231]\nds_read_b64_tr_b16 a[212:213],v65 offset:24576\nds_read_b64_tr_b16 a[214:215],v65 offset:25088\n" \
"buffer_load_dwordx4 v[88:91],v59,s[36:39],s69 offen offset:1024 sc1\nv_mfma_f32_16x16x32_bf16 v[232:235],a[184:187],v[100:103],v[232:235]\nds_read_b64_tr_b16 a[216:217],v66 offset:24576\nds_read_b64_tr_b16 a[218:219],v66 offset:25088\n" \
"v_mfma_f32_16x16x32_bf16 v[236:239],a[188:191],v[100:103],v[236:239]\nds_read_b64_tr_b16 a[220:221],v67 offset:24576\nds_read_b64_tr_b16 a[222:223],v67 offset:25088\ns_waitcnt lgkmcnt(8)\n" \
"v_mfma_f32_16x16x32_bf16 v[208:211],a[192:195],v[104:107],v[208:211]\nbuffer_load_dwordx4 v[92:95],v59,s[36:39],s69 offen offset:2048 sc1\nv_mfma_f32_16x16x32_bf16 v[212:215],a[196:199],v[104:107],v[212:215]\nv_mfma_f32_16x16x32_bf16 v[216:219],a[200:203],v[104:107],v[216:219]\n" \
"v_mfma_f32_16x16x32_bf16 v[220:223],a[204:207],v[104:107],v[220:223]\nbuffer_load_dwordx4 v[96:99],v59,s[36:39],s70 offen sc1\ns_waitcnt lgkmcnt(0)\nv_mfma_f32_16x16x32_bf16 v[224:227],a[208:211],v[104:107],v[224:227]\n" \
"v_mfma_f32_16x16x32_bf16 v[228:231],a[212:215],v[104:107],v[228:231]\nv_mfma_f32_16x16x32_bf16 v[232:235],a[216:219],v[104:107],v[232:235]\nbuffer_load_dwordx4 v[100:103],v59,s[36:39],s70 offen offset:1024 sc1\nv_mfma_f32_16x16x32_bf16 v[236:239],a[220:223],v[104:107],v[236:239]\n" \
"v_mfma_f32_16x16x32_bf16 v[16:19],a[112:115],v[192:195],v[16:19]\nv_mfma_f32_16x16x32_bf16 v[20:23],a[116:119],v[192:195],v[20:23]\nbuffer_load_dwordx4 v[104:107],v59,s[36:39],s70 offen offset:2048 sc1\nds_write_addtid_b32 v208\n" \
"ds_write_addtid_b32 v209 offset:256\nv_mfma_f32_16x16x32_bf16 v[24:27],a[120:123],v[192:195],v[24:27]\nds_write_addtid_b32 v210 offset:512\nds_write_addtid_b32 v211 offset:768\n" \
"v_mfma_f32_16x16x32_bf16 v[28:31],a[124:127],v[192:195],v[28:31]\nds_write_addtid_b32 v212 offset:1024\nds_write_addtid_b32 v213 offset:1280\ns_waitcnt vmcnt(13)\n" \
"v_mfma_f32_16x16x32_bf16 v[0:3],a[128:131],v[196:199],v[0:3]\nds_write_addtid_b32 v214 offset:1536\nds_write_addtid_b32 v215 offset:1792\nv_mfma_f32_16x16x32_bf16 v[4:7],a[132:135],v[196:199],v[4:7]\n" \
"ds_write_addtid_b32 v216 offset:2048\nds_write_addtid_b32 v217 offset:2304\nv_mfma_f32_16x16x32_bf16 v[8:11],a[136:139],v[196:199],v[8:11]\nds_write_addtid_b32 v218 offset:2560\n" \
"ds_write_addtid_b32 v219 offset:2816\nv_mfma_f32_16x16x32_bf16 v[12:15],a[140:143],v[196:199],v[12:15]\nds_write_addtid_b32 v220 offset:3072\nds_write_addtid_b32 v221 offset:3328\n" \
"v_mfma_f32_16x16x32_bf16 v[16:19],a[144:147],v[196:199],v[16:19]\nds_write_addtid_b32 v222 offset:3584\nds_write_addtid_b32 v223 offset:3840\nv_mfma_f32_16x16x32_bf16 v[20:23],a[148:151],v[196:199],v[20:23]\n" \
"ds_write_addtid_b32 v224 offset:4096\nds_write_addtid_b32 v225 offset:4352\nv_mfma_f32_16x16x32_bf16 v[24:27],a[152:155],v[196:199],v[24:27]\nds_write_addtid_b32 v226 offset:4608\n" \
"ds_write_addtid_b32 v227 offset:4864\nv_mfma_f32_16x16x32_bf16 v[28:31],a[156:159],v[196:199],v[28:31]\nds_write_addtid_b32 v228 offset:5120\nds_write_addtid_b32 v229 offset:5376\n" \
"s_waitcnt vmcnt(12)\nv_mfma_f32_16x16x32_bf16 v[0:3],a[160:163],v[200:203],v[0:3]\nds_write_addtid_b32 v230 offset:5632\nds_write_addtid_b32 v231 offset:5888\n" \
"v_mfma_f32_16x16x32_bf16 v[4:7],a[164:167],v[200:203],v[4:7]\nds_write_addtid_b32 v232 offset:6144\nds_write_addtid_b32 v233 offset:6400\nv_mfma_f32_16x16x32_bf16 v[8:11],a[168:171],v[200:203],v[8:11]\n" \
"ds_write_addtid_b32 v234 offset:6656\nds_write_addtid_b32 v235 offset:6912\nv_mfma_f32_16x16x32_bf16 v[12:15],a[172:175],v[200:203],v[12:15]\nds_write_addtid_b32 v236 offset:7168\n" \
"ds_write_addtid_b32 v237 offset:7424\nv_mfma_f32_16x16x32_bf16 v[16:19],a[176:179],v[200:203],v[16:19]\nds_write_addtid_b32 v238 offset:7680\nds_write_addtid_b32 v239 offset:7936\n" \
"v_mfma_f32_16x16x32_bf16 v[20:23],a[180:183],v[200:203],v[20:23]\nv_mfma_f32_16x16x32_bf16 v[24:27],a[184:187],v[200:203],v[24:27]\nv_mfma_f32_16x16x32_bf16 v[28:31],a[188:191],v[200:203],v[28:31]\ns_waitcnt vmcnt(11)\n" \
"v_mfma_f32_16x16x32_bf16 v[0:3],a[192:195],v[204:207],v[0:3]\nv_mfma_f32_16x16x32_bf16 v[4:7],a[196:199],v[204:207],v[4:7]\nv_mfma_f32_16x16x32_bf16 v[8:11],a[200:203],v[204:207],v[8:11]\ns_waitcnt lgkmcnt(0)\n" \
"s_barrier\nds_read_b128 v[32:35],v58\nv_mfma_f32_16x16x32_bf16 v[12:15],a[204:207],v[204:207],v[12:15]\nds_read_b128 v[36:39],v58 offset:8192\n" \
"ds_read_b128 v[40:43],v58 offset:16384\nv_mfma_f32_16x16x32_bf16 v[16:19],a[208:211],v[204:207],v[16:19]\nds_read_b128 v[44:47],v58 offset:24576\nds_read_b128 v[48:51],v58 offset:4096\n" \
"v_mfma_f32_16x16x32_bf16 v[20:23],a[212:215],v[204:207],v[20:23]\nds_read_b128 v[240:243],v58 offset:12288\nds_read_b128 v[244:247],v58 offset:20480\nv_mfma_f32_16x16x32_bf16 v[24:27],a[216:219],v[204:207],v[24:27]\n" \
"ds_read_b128 v[248:251],v58 offset:28672\nv_mfma_f32_16x16x32_bf16 v[28:31],a[220:223],v[204:207],v[28:31]\ns_waitcnt vmcnt(7)\nv_mfma_f32_16x16x32_bf16 v[208:211],v[128:131],v[72:75],0\n" \
"s_mov_b64 exec,1\nbuffer_store_dword v61,off,s[36:39],s54 sc1\ns_mov_b64 exec,-1\nv_mfma_f32_16x16x32_bf16 v[212:215],v[132:135],v[72:75],0\n" \
"v_cmp_eq_u32 vcc,0,v63\nds_write_addtid_b32 v0 offset:32768\nv_mfma_f32_16x16x32_bf16 v[216:219],v[136:139],v[72:75],0\nds_write_addtid_b32 v1 offset:33024\n" \
"ds_write_addtid_b32 v2 offset:33280\nv_mfma_f32_16x16x32_bf16 v[220:223],v[140:143],v[72:75],0\ns_and_b64 vcc,vcc,s[86:87]\ns_cbranch_scc1 .LsbS_%=\n" \
".LbbS_%=:\nbuffer_load_dwordx4 v[108:111],v59,s[36:39],s48 offen sc1\nv_mfma_f32_16x16x32_bf16 v[224:227],v[144:147],v[72:75],0\nds_write_addtid_b32 v3 offset:33536\n" \
"ds_write_addtid_b32 v4 offset:33792\ns_waitcnt lgkmcnt(9)\nv_mfma_f32_16x16x32_bf16 v[228:231],v[148:151],v[72:75],0\nds_write_addtid_b32 v5 offset:34048\n" \
"ds_write_addtid_b32 v6 offset:34304\nv_add_f32 v32,v32,v36\nv_mfma_f32_16x16x32_bf16 v[232:235],v[152:155],v[72:75],0\nbuffer_load_dwordx4 v[112:115],v59,s[36:39],s48 offen offset:1024 sc1\n" \
"ds_write_addtid_b32 v7 offset:34560\nds_write_addtid_b32 v8 offset:34816\nv_mfma_f32_16x16x32_bf16 v[236:239],v[156:159],v[72:75],0\nds_write_addtid_b32 v9 offset:35072\n" \
"ds_write_addtid_b32 v10 offset:35328\nv_add_f32 v33,v33,v37\nv_mfma_f32_16x16x32_bf16 v[208:211],v[160:163],v[76:79],v[208:211]\nds_write_addtid_b32 v11 offset:35584\n" \
"ds_write_addtid_b32 v12 offset:35840\nv_add_f32 v34,v34,v38\nv_mfma_f32_16x16x32_bf16 v[212:215],v[164:167],v[76:79],v[212:215]\nbuffer_load_dwordx4 v[116:119],v59,s[36:39],s48 offen offset:2048 sc1\n" \
"ds_write_addtid_b32 v13 offset:36096\nds_write_addtid_b32 v14 offset:36352\nv_mfma_f32_16x16x32_bf16 v[216:219],v[168:171],v[76:79],v[216:219]\nds_write_addtid_b32 v15 offset:36608\n" \
"ds_write_addtid_b32 v16 offset:36864\nv_add_f32 v35,v35,v39\nv_mfma_f32_16x16x32_bf16 v[220:223],v[172:175],v[76:79],v[220:223]\nds_write_addtid_b32 v17 offset:37120\n" \
"ds_write_addtid_b32 v18 offset:37376\nv_add_f32 v40,v40,v44\nv_mfma_f32_16x16x32_bf16 v[224:227],v[176:179],v[76:79],v[224:227]\nbuffer_load_dwordx4 v[120:123],v59,s[36:39],s49 offen sc1\n" \
"ds_write_addtid_b32 v19 offset:37632\nds_write_addtid_b32 v20 offset:37888\nv_mfma_f32_16x16x32_bf16 v[228:231],v[180:183],v[76:79],v[228:231]\nds_write_addtid_b32 v21 offset:38144\n" \
"ds_write_addtid_b32 v22 offset:38400\nv_add_f32 v41,v41,v45\nv_mfma_f32_16x16x32_bf16 v[232:235],v[184:187],v[76:79],v[232:235]\nds_write_addtid_b32 v23 offset:38656\n" \
"ds_write_addtid_b32 v24 offset:38912\nv_add_f32 v42,v42,v46\nv_mfma_f32_16x16x32_bf16 v[236:239],v[188:191],v[76:79],v[236:239]\nbuffer_load_dwordx4 v[124:127],v59,s[36:39],s49 offen offset:1024 sc1\n" \
"ds_write_addtid_b32 v25 offset:39168\ns_waitcnt vmcnt(11)\nv_mfma_f32_16x16x32_bf16 v[208:211],a[0:3],v[80:83],v[208:211]\nds_write_addtid_b32 v26 offset:39424\n" \
"ds_write_addtid_b32 v27 offset:39680\nv_add_f32 v43,v43,v47\nv_mfma_f32_16x16x32_bf16 v[212:215],a[4:7],v[80:83],v[212:215]\nds_write_addtid_b32 v28 offset:39936\n" \
"ds_write_addtid_b32 v29 offset:40192\nv_add_f32 v32,v32,v40\nv_mfma_f32_16x16x32_bf16 v[216:219],a[8:11],v[80:83],v[216:219]\nbuffer_load_dwordx4 v[192:195],v59,s[36:39],s49 offen offset:2048 sc1\n" \
"ds_write_addtid_b32 v30 offset:40448\nds_write_addtid_b32 v31 offset:40704\nv_mfma_f32_16x16x32_bf16 v[220:223],a[12:15],v[80:83],v[220:223]\nv_add_f32 v33,v33,v41\n" \
"v_add_f32 v34,v34,v42\nv_add_f32 v35,v35,v43\nv_mfma_f32_16x16x32_bf16 v[224:227],a[16:19],v[80:83],v[224:227]\nv_cvt_pk_bf16_f32 v36,v32,v33\n" \
"v_cvt_pk_bf16_f32 v37,v34,v35\ns_waitcnt lgkmcnt(15)\nv_mfma_f32_16x16x32_bf16 v[228:231],a[20:23],v[80:83],v[228:231]\nbuffer_load_dwordx4 v[196:199],v59,s[36:39],s50 offen sc1\n" \
"v_add_f32 v48,v48,v240\nv_add_f32 v49,v49,v241\nv_mfma_f32_16x16x32_bf16 v[232:235],a[24:27],v[80:83],v[232:235]\nv_add_f32 v50,v50,v242\n" \
"v_add_f32 v51,v51,v243\nv_add_f32 v244,v244,v248\nv_mfma_f32_16x16x32_bf16 v[236:239],a[28:31],v[80:83],v[236:239]\nv_add_f32 v245,v245,v249\n" \
"v_add_f32 v246,v246,v250\nv_add_f32 v247,v247,v251\nv_mfma_f32_16x16x32_bf16 v[208:211],a[32:35],v[84:87],v[208:211]\nbuffer_load_dwordx4 v[200:203],v59,s[36:39],s50 offen offset:1024 sc1\n" \
"s_waitcnt lgkmcnt(0)\ns_barrier\nv_mfma_f32_16x16x32_bf16 v[212:215],a[36:39],v[84:87],v[212:215]\nv_add_f32 v48,v48,v244\n" \
"v_add_f32 v49,v49,v245\nv_add_f32 v50,v50,v246\nv_mfma_f32_16x16x32_bf16 v[216:219],a[40:43],v[84:87],v[216:219]\nv_add_f32 v51,v51,v247\n" \
"v_cvt_pk_bf16_f32 v38,v48,v49\nv_cvt_pk_bf16_f32 v39,v50,v51\nv_mfma_f32_16x16x32_bf16 v[220:223],a[44:47],v[84:87],v[220:223]\nbuffer_load_dwordx4 v[204:207],v59,s[36:39],s50 offen offset:2048 sc1\n" \
"v_mfma_f32_16x16x32_bf16 v[224:227],a[48:51],v[84:87],v[224:227]\nv_mfma_f32_16x16x32_bf16 v[228:231],a[52:55],v[84:87],v[228:231]\nv_mfma_f32_16x16x32_bf16 v[232:235],a[56:59],v[84:87],v[232:235]\nbuffer_store_dwordx4 v[36:39],v59,s[36:39],s74 offen sc1\n" \
"ds_read_b128 v[32:35],v58 offset:32768\ns_nop 0\nds_read_b128 v[36:39],v58 offset:40960\nv_mfma_f32_16x16x32_bf16 v[236:239],a[60:63],v[84:87],v[236:239]\n" \
"ds_read_b128 v[40:43],v58 offset:49152\nds_read_b128 v[44:47],v58 offset:57344\ns_waitcnt vmcnt(14)\nv_mfma_f32_16x16x32_bf16 v[208:211],a[64:67],v[88:91],v[208:211]\n" \
"ds_read_b128 v[48:51],v58 offset:36864\nds_read_b128 v[240:243],v58 offset:45056\nv_mfma_f32_16x16x32_bf16 v[212:215],a[68:71],v[88:91],v[212:215]\nds_read_b128 v[244:247],v58 offset:53248\n" \
"ds_read_b128 v[248:251],v58 offset:61440\nv_mfma_f32_16x16x32_bf16 v[216:219],a[72:75],v[88:91],v[216:219]\nv_mfma_f32_16x16x32_bf16 v[220:223],a[76:79],v[88:91],v[220:223]\nv_mfma_f32_16x16x32_bf16 v[224:227],a[80:83],v[88:91],v[224:227]\n" \
"v_mfma_f32_16x16x32_bf16 v[228:231],a[84:87],v[88:91],v[228:231]\nv_mfma_f32_16x16x32_bf16 v[232:235],a[88:91],v[88:91],v[232:235]\nv_mfma_f32_16x16x32_bf16 v[236:239],a[92:95],v[88:91],v[236:239]\ns_waitcnt lgkmcnt(4)\n" \
"v_add_f32 v32,v32,v36\nv_add_f32 v33,v33,v37\nv_mfma_f32_16x16x32_bf16 v[208:211],a[96:99],v[92:95],v[208:211]\nv_add_f32 v34,v34,v38\n" \
"v_add_f32 v35,v35,v39\nv_add_f32 v40,v40,v44\nv_mfma_f32_16x16x32_bf16 v[212:215],a[100:103],v[92:95],v[212:215]\nv_add_f32 v41,v41,v45\n" \
"v_add_f32 v42,v42,v46\nv_add_f32 v43,v43,v47\nv_mfma_f32_16x16x32_bf16 v[216:219],a[104:107],v[92:95],v[216:219]\nv_add_f32 v32,v32,v40\n" \
"v_add_f32 v33,v33,v41\nv_add_f32 v34,v34,v42\nv_mfma_f32_16x16x32_bf16 v[220:223],a[108:111],v[92:95],v[220:223]\nv_add_f32 v35,v35,v43\n" \
"v_cvt_pk_bf16_f32 v36,v32,v33\nv_cvt_pk_bf16_f32 v37,v34,v35\nv_mfma_f32_16x16x32_bf16 v[224:227],a[112:115],v[92:95],v[224:227]\ns_waitcnt lgkmcnt(0)\n" \
"v_add_f32 v48,v48,v240\nv_add_f32 v49,v49,v241\nv_mfma_f32_16x16x32_bf16 v[228:231],a[116:119],v[92:95],v[228:231]\nv_add_f32 v50,v50,v242\n" \
"v_add_f32 v51,v51,v243\nv_add_f32 v244,v244,v248\nv_mfma_f32_16x16x32_bf16 v[232:235],a[120:123],v[92:95],v[232:235]\nv_add_f32 v245,v245,v249\n" \
"v_add_f32 v246,v246,v250\nv_add_f32 v247,v247,v251\nv_mfma_f32_16x16x32_bf16 v[236:239],a[124:127],v[92:95],v[236:239]\nv_add_f32 v48,v48,v244\n" \
"v_add_f32 v49,v49,v245\ns_waitcnt vmcnt(12)\nv_mfma_f32_16x16x32_bf16 v[208:211],a[128:131],v[96:99],v[208:211]\nv_add_f32 v50,v50,v246\n" \
"v_add_f32 v51,v51,v247\nv_cvt_pk_bf16_f32 v38,v48,v49\nv_mfma_f32_16x16x32_bf16 v[212:215],a[132:135],v[96:99],v[212:215]\nv_cvt_pk_bf16_f32 v39,v50,v51\n" \
"v_add_u32 v59,409600,v59\nbuffer_store_dwordx4 v[36:39],v59,s[36:39],s74 offen sc1\nv_mfma_f32_16x16x32_bf16 v[216:219],a[136:139],v[96:99],v[216:219]\nv_mfma_f32_16x16x32_bf16 v[220:223],a[140:143],v[96:99],v[220:223]\n" \
"v_mfma_f32_16x16x32_bf16 v[224:227],a[144:147],v[96:99],v[224:227]\nv_mfma_f32_16x16x32_bf16 v[228:231],a[148:151],v[96:99],v[228:231]\nv_mfma_f32_16x16x32_bf16 v[232:235],a[152:155],v[96:99],v[232:235]\nv_mfma_f32_16x16x32_bf16 v[236:239],a[156:159],v[96:99],v[236:239]\n" \
"v_mfma_f32_16x16x32_bf16 v[208:211],a[160:163],v[100:103],v[208:211]\nv_mfma_f32_16x16x32_bf16 v[212:215],a[164:167],v[100:103],v[212:215]\nv_mfma_f32_16x16x32_bf16 v[216:219],a[168:171],v[100:103],v[216:219]\nv_mfma_f32_16x16x32_bf16 v[220:223],a[172:175],v[100:103],v[220:223]\n" \
"v_mfma_f32_16x16x32_bf16 v[224:227],a[176:179],v[100:103],v[224:227]\nv_mfma_f32_16x16x32_bf16 v[228:231],a[180:183],v[100:103],v[228:231]\nv_mfma_f32_16x16x32_bf16 v[232:235],a[184:187],v[100:103],v[232:235]\nv_mfma_f32_16x16x32_bf16 v[236:239],a[188:191],v[100:103],v[236:239]\n" \
"s_waitcnt vmcnt(12)\nv_mfma_f32_16x16x32_bf16 v[208:211],a[192:195],v[104:107],v[208:211]\nv_mfma_f32_16x16x32_bf16 v[212:215],a[196:199],v[104:107],v[212:215]\nv_mfma_f32_16x16x32_bf16 v[216:219],a[200:203],v[104:107],v[216:219]\n" \
"v_mfma_f32_16x16x32_bf16 v[220:223],a[204:207],v[104:107],v[220:223]\nv_mfma_f32_16x16x32_bf16 v[224:227],a[208:211],v[104:107],v[224:227]\nv_mfma_f32_16x16x32_bf16 v[228:231],a[212:215],v[104:107],v[228:231]\nv_mfma_f32_16x16x32_bf16 v[232:235],a[216:219],v[104:107],v[232:235]\n" \
"v_mfma_f32_16x16x32_bf16 v[236:239],a[220:223],v[104:107],v[236:239]\n.Le_%=:\ns_waitcnt vmcnt(9)\nv_mfma_f32_16x16x32_bf16 v[0:3],v[128:131],v[108:111],0\n" \
"v_add_u32 v59,409600,v59\nv_mfma_f32_16x16x32_bf16 v[4:7],v[132:135],v[108:111],0\nds_write_addtid_b32 v208\nv_mfma_f32_16x16x32_bf16 v[8:11],v[136:139],v[108:111],0\n" \
"buffer_load_dwordx4 v[72:75],v59,s[36:39],s68 offen sc1\nds_write_addtid_b32 v209 offset:256\nv_mfma_f32_16x16x32_bf16 v[12:15],v[140:143],v[108:111],0\nds_write_addtid_b32 v210 offset:512\n" \
"v_mfma_f32_16x16x32_bf16 v[16:19],v[144:147],v[108:111],0\nds_write_addtid_b32 v211 offset:768\nv_mfma_f32_16x16x32_bf16 v[20:23],v[148:151],v[108:111],0\nds_write_addtid_b32 v212 offset:1024\n" \
"v_mfma_f32_16x16x32_bf16 v[24:27],v[152:155],v[108:111],0\nds_write_addtid_b32 v213 offset:1280\nv_mfma_f32_16x16x32_bf16 v[28:31],v[156:159],v[108:111],0\nds_write_addtid_b32 v214 offset:1536\n" \
"v_mfma_f32_16x16x32_bf16 v[0:3],v[160:163],v[112:115],v[0:3]\nbuffer_load_dwordx4 v[76:79],v59,s[36:39],s68 offen offset:1024 sc1\nds_write_addtid_b32 v215 offset:1792\nv_mfma_f32_16x16x32_bf16 v[4:7],v[164:167],v[112:115],v[4:7]\n" \
"ds_write_addtid_b32 v216 offset:2048\nv_mfma_f32_16x16x32_bf16 v[8:11],v[168:171],v[112:115],v[8:11]\nds_write_addtid_b32 v217 offset:2304\nv_mfma_f32_16x16x32_bf16 v[12:15],v[172:175],v[112:115],v[12:15]\n" \
"ds_write_addtid_b32 v218 offset:2560\nv_mfma_f32_16x16x32_bf16 v[16:19],v[176:179],v[112:115],v[16:19]\nds_write_addtid_b32 v219 offset:2816\nv_mfma_f32_16x16x32_bf16 v[20:23],v[180:183],v[112:115],v[20:23]\n" \
"ds_write_addtid_b32 v220 offset:3072\nv_mfma_f32_16x16x32_bf16 v[24:27],v[184:187],v[112:115],v[24:27]\nbuffer_load_dwordx4 v[80:83],v59,s[36:39],s68 offen offset:2048 sc1\nds_write_addtid_b32 v221 offset:3328\n" \
"v_mfma_f32_16x16x32_bf16 v[28:31],v[188:191],v[112:115],v[28:31]\nds_write_addtid_b32 v222 offset:3584\ns_waitcnt vmcnt(10)\nv_mfma_f32_16x16x32_bf16 v[0:3],a[0:3],v[116:119],v[0:3]\n" \
"ds_write_addtid_b32 v223 offset:3840\nv_bfe_u32 v69,v59,2,8\nv_mfma_f32_16x16x32_bf16 v[4:7],a[4:7],v[116:119],v[4:7]\nds_write_addtid_b32 v224 offset:4096\n" \
"buffer_load_dword v64,v69,s[36:39],s79 offen sc1\nv_mfma_f32_16x16x32_bf16 v[8:11],a[8:11],v[116:119],v[8:11]\nds_write_addtid_b32 v225 offset:4352\nbuffer_load_dword v65,v69,s[36:39],s80 offen sc1\n" \
"v_mfma_f32_16x16x32_bf16 v[12:15],a[12:15],v[116:119],v[12:15]\nds_write_addtid_b32 v226 offset:4608\nbuffer_load_dword v66,v69,s[36:39],s81 offen sc1\nv_mfma_f32_16x16x32_bf16 v[16:19],a[16:19],v[116:119],v[16:19]\n" \
"buffer_load_dwordx4 v[84:87],v59,s[36:39],s69 offen sc1\nds_write_addtid_b32 v227 offset:4864\nv_mfma_f32_16x16x32_bf16 v[20:23],a[20:23],v[116:119],v[20:23]\nds_write_addtid_b32 v228 offset:5120\n" \
"buffer_load_dword v67,v69,s[36:39],s82 offen sc1\nv_mfma_f32_16x16x32_bf16 v[24:27],a[24:27],v[116:119],v[24:27]\nds_write_addtid_b32 v229 offset:5376\nbuffer_load_dword v68,v57,s[36:39],s90 offen sc1\n" \
"v_mfma_f32_16x16x32_bf16 v[28:31],a[28:31],v[116:119],v[28:31]\nds_write_addtid_b32 v230 offset:5632\nv_add_u32 v70,s98,v58\nv_mfma_f32_16x16x32_bf16 v[0:3],a[32:35],v[120:123],v[0:3]\n" \
"s_add_u32 s59,s59,0x10000\nbuffer_load_dword v63,v61,s[36:39],s59 offen sc1\nv_mfma_f32_16x16x32_bf16 v[4:7],a[36:39],v[120:123],v[4:7]\nds_write_addtid_b32 v231 offset:5888\n" \
"v_mfma_f32_16x16x32_bf16 v[8:11],a[40:43],v[120:123],v[8:11]\nbuffer_load_dwordx4 v[88:91],v59,s[36:39],s69 offen offset:1024 sc1\nds_write_addtid_b32 v232 offset:6144\nv_mfma_f32_16x16x32_bf16 v[12:15],a[44:47],v[120:123],v[12:15]\n" \
"ds_write_addtid_b32 v233 offset:6400\nv_mfma_f32_16x16x32_bf16 v[16:19],a[48:51],v[120:123],v[16:19]\nds_write_addtid_b32 v234 offset:6656\nv_mfma_f32_16x16x32_bf16 v[20:23],a[52:55],v[120:123],v[20:23]\n" \
"ds_write_addtid_b32 v235 offset:6912\nv_mfma_f32_16x16x32_bf16 v[24:27],a[56:59],v[120:123],v[24:27]\nds_write_addtid_b32 v236 offset:7168\nv_mfma_f32_16x16x32_bf16 v[28:31],a[60:63],v[120:123],v[28:31]\n" \
"ds_write_addtid_b32 v237 offset:7424\ns_waitcnt vmcnt(16)\nv_mfma_f32_16x16x32_bf16 v[0:3],a[64:67],v[124:127],v[0:3]\nbuffer_load_dwordx4 v[92:95],v59,s[36:39],s69 offen offset:2048 sc1\n" \
"ds_write_addtid_b32 v238 offset:7680\nv_mfma_f32_16x16x32_bf16 v[4:7],a[68:71],v[124:127],v[4:7]\nds_write_addtid_b32 v239 offset:7936\nv_mfma_f32_16x16x32_bf16 v[8:11],a[72:75],v[124:127],v[8:11]\n" \
"ds_read_b64 v[70:71],v70 offset:29704\nv_mfma_f32_16x16x32_bf16 v[12:15],a[76:79],v[124:127],v[12:15]\nv_mfma_f32_16x16x32_bf16 v[16:19],a[80:83],v[124:127],v[16:19]\nv_mfma_f32_16x16x32_bf16 v[20:23],a[84:87],v[124:127],v[20:23]\n" \
"v_mfma_f32_16x16x32_bf16 v[24:27],a[88:91],v[124:127],v[24:27]\nbuffer_load_dwordx4 v[96:99],v59,s[36:39],s70 offen sc1\nv_mfma_f32_16x16x32_bf16 v[28:31],a[92:95],v[124:127],v[28:31]\ns_waitcnt lgkmcnt(0)\n" \
"s_barrier\nv_mfma_f32_16x16x32_bf16 v[0:3],a[96:99],v[192:195],v[0:3]\nds_read_b128 v[32:35],v58\nv_mfma_f32_16x16x32_bf16 v[4:7],a[100:103],v[192:195],v[4:7]\n" \
"ds_read_b128 v[36:39],v58 offset:8192\nv_mfma_f32_16x16x32_bf16 v[8:11],a[104:107],v[192:195],v[8:11]\nds_read_b128 v[40:43],v58 offset:16384\nv_mfma_f32_16x16x32_bf16 v[12:15],a[108:111],v[192:195],v[12:15]\n" \
"ds_read_b128 v[44:47],v58 offset:24576\nv_mfma_f32_16x16x32_bf16 v[16:19],a[112:115],v[192:195],v[16:19]\nbuffer_load_dwordx4 v[100:103],v59,s[36:39],s70 offen offset:1024 sc1\nds_read_b128 v[48:51],v58 offset:4096\n" \
"v_mfma_f32_16x16x32_bf16 v[20:23],a[116:119],v[192:195],v[20:23]\nds_read_b128 v[240:243],v58 offset:12288\nv_mfma_f32_16x16x32_bf16 v[24:27],a[120:123],v[192:195],v[24:27]\nds_read_b128 v[244:247],v58 offset:20480\n" \
"v_mfma_f32_16x16x32_bf16 v[28:31],a[124:127],v[192:195],v[28:31]\nds_read_b128 v[248:251],v58 offset:28672\ns_waitcnt vmcnt(17)\nv_mfma_f32_16x16x32_bf16 v[0:3],a[128:131],v[196:199],v[0:3]\n" \
"v_mfma_f32_16x16x32_bf16 v[4:7],a[132:135],v[196:199],v[4:7]\nv_mfma_f32_16x16x32_bf16 v[8:11],a[136:139],v[196:199],v[8:11]\nbuffer_load_dwordx4 v[104:107],v59,s[36:39],s70 offen offset:2048 sc1\nv_mfma_f32_16x16x32_bf16 v[12:15],a[140:143],v[196:199],v[12:15]\n" \
"s_waitcnt lgkmcnt(4)\nv_add_f32 v32,v32,v36\nv_mfma_f32_16x16x32_bf16 v[16:19],a[144:147],v[196:199],v[16:19]\nv_add_f32 v33,v33,v37\n" \
"v_add_f32 v34,v34,v38\nv_mfma_f32_16x16x32_bf16 v[20:23],a[148:151],v[196:199],v[20:23]\nv_add_f32 v35,v35,v39\nv_add_f32 v40,v40,v44\n" \
"v_mfma_f32_16x16x32_bf16 v[24:27],a[152:155],v[196:199],v[24:27]\nv_add_f32 v41,v41,v45\nv_add_f32 v42,v42,v46\nv_mfma_f32_16x16x32_bf16 v[28:31],a[156:159],v[196:199],v[28:31]\n" \
"v_add_f32 v43,v43,v47\nv_add_f32 v32,v32,v40\nv_mfma_f32_16x16x32_bf16 v[0:3],a[160:163],v[200:203],v[0:3]\nv_add_f32 v33,v33,v41\n" \
"v_add_f32 v34,v34,v42\nv_mfma_f32_16x16x32_bf16 v[4:7],a[164:167],v[200:203],v[4:7]\nv_add_f32 v35,v35,v43\nv_cvt_pk_bf16_f32 v36,v32,v33\n" \
"v_mfma_f32_16x16x32_bf16 v[8:11],a[168:171],v[200:203],v[8:11]\nv_cvt_pk_bf16_f32 v37,v34,v35\ns_waitcnt lgkmcnt(0)\nv_mfma_f32_16x16x32_bf16 v[12:15],a[172:175],v[200:203],v[12:15]\n" \
"v_add_f32 v48,v48,v240\nv_add_f32 v49,v49,v241\nv_mfma_f32_16x16x32_bf16 v[16:19],a[176:179],v[200:203],v[16:19]\nv_add_f32 v50,v50,v242\n" \
"v_add_f32 v51,v51,v243\nv_mfma_f32_16x16x32_bf16 v[20:23],a[180:183],v[200:203],v[20:23]\nv_add_f32 v244,v244,v248\nv_add_f32 v245,v245,v249\n" \
"v_mfma_f32_16x16x32_bf16 v[24:27],a[184:187],v[200:203],v[24:27]\nv_add_f32 v246,v246,v250\nv_add_f32 v247,v247,v251\nv_mfma_f32_16x16x32_bf16 v[28:31],a[188:191],v[200:203],v[28:31]\n" \
"v_add_f32 v48,v48,v244\ns_waitcnt vmcnt(17)\nv_mfma_f32_16x16x32_bf16 v[0:3],a[192:195],v[204:207],v[0:3]\nv_add_f32 v49,v49,v245\n" \
"v_add_f32 v50,v50,v246\nv_mfma_f32_16x16x32_bf16 v[4:7],a[196:199],v[204:207],v[4:7]\nv_add_f32 v51,v51,v247\nv_cvt_pk_bf16_f32 v38,v48,v49\n" \
"v_mfma_f32_16x16x32_bf16 v[8:11],a[200:203],v[204:207],v[8:11]\nv_cvt_pk_bf16_f32 v39,v50,v51\nbuffer_store_dwordx4 v[36:39],v59,s[36:39],s74 offen sc1\nv_mfma_f32_16x16x32_bf16 v[12:15],a[204:207],v[204:207],v[12:15]\n" \
"v_mfma_f32_16x16x32_bf16 v[16:19],a[208:211],v[204:207],v[16:19]\nv_mfma_f32_16x16x32_bf16 v[20:23],a[212:215],v[204:207],v[20:23]\nv_mfma_f32_16x16x32_bf16 v[24:27],a[216:219],v[204:207],v[24:27]\nv_mfma_f32_16x16x32_bf16 v[28:31],a[220:223],v[204:207],v[28:31]\n" \
"s_waitcnt vmcnt(14)\nv_mfma_f32_16x16x32_bf16 v[208:211],v[128:131],v[72:75],0\nv_add_u32 v59,409600,v59\nv_mfma_f32_16x16x32_bf16 v[212:215],v[132:135],v[72:75],0\n" \
"s_waitcnt vmcnt(6)\nv_cmp_eq_u32 vcc,0,v63\nv_mfma_f32_16x16x32_bf16 v[216:219],v[136:139],v[72:75],0\nds_write_addtid_b32 v0 offset:32768\n" \
"v_mfma_f32_16x16x32_bf16 v[220:223],v[140:143],v[72:75],0\ns_and_b64 vcc,vcc,s[86:87]\ns_cbranch_scc1 .Lsg4_%=\n.Lbg4_%=:\n" \
"v_mfma_f32_16x16x32_bf16 v[224:227],v[144:147],v[72:75],0\nbuffer_load_dwordx4 v[108:111],v59,s[36:39],s68 offen sc1\nds_write_addtid_b32 v1 offset:33024\nv_mfma_f32_16x16x32_bf16 v[228:231],v[148:151],v[72:75],0\n" \
"ds_write_addtid_b32 v2 offset:33280\nv_mfma_f32_16x16x32_bf16 v[232:235],v[152:155],v[72:75],0\nds_write_addtid_b32 v3 offset:33536\nv_max3_u32 v52,v64,v65,v66\n" \
"v_mfma_f32_16x16x32_bf16 v[236:239],v[156:159],v[72:75],0\nds_write_addtid_b32 v4 offset:33792\nv_max_u32 v52,v52,v67\nv_mfma_f32_16x16x32_bf16 v[208:211],v[160:163],v[76:79],v[208:211]\n" \
"buffer_load_dwordx4 v[112:115],v59,s[36:39],s68 offen offset:1024 sc1\nds_write_addtid_b32 v5 offset:34048\nv_mfma_f32_16x16x32_bf16 v[212:215],v[164:167],v[76:79],v[212:215]\nds_write_addtid_b32 v6 offset:34304\n" \
"v_cmp_eq_u32 vcc,-1,v52\nv_mfma_f32_16x16x32_bf16 v[216:219],v[168:171],v[76:79],v[216:219]\nds_write_addtid_b32 v7 offset:34560\nv_mfma_f32_16x16x32_bf16 v[220:223],v[172:175],v[76:79],v[220:223]\n" \
"ds_write_addtid_b32 v8 offset:34816\ns_cbranch_vccnz .Lse_%=\n.Lbe_%=:\nv_mfma_f32_16x16x32_bf16 v[224:227],v[176:179],v[76:79],v[224:227]\n" \
"ds_write_addtid_b32 v9 offset:35072\nv_lshlrev_b32 v52,16,v64\nv_mfma_f32_16x16x32_bf16 v[228:231],v[180:183],v[76:79],v[228:231]\nds_write_addtid_b32 v10 offset:35328\n" \
"v_and_b32 v53,0xffff0000,v64\nv_mfma_f32_16x16x32_bf16 v[232:235],v[184:187],v[76:79],v[232:235]\nbuffer_load_dwordx4 v[116:119],v59,s[36:39],s68 offen offset:2048 sc1\nds_write_addtid_b32 v11 offset:35584\n" \
"v_mfma_f32_16x16x32_bf16 v[236:239],v[188:191],v[76:79],v[236:239]\nds_write_addtid_b32 v12 offset:35840\nv_mfma_f32_16x16x32_bf16 v[208:211],a[0:3],v[80:83],v[208:211]\nds_write_addtid_b32 v13 offset:36096\n" \
"v_lshlrev_b32 v54,16,v65\nv_mfma_f32_16x16x32_bf16 v[212:215],a[4:7],v[80:83],v[212:215]\nds_write_addtid_b32 v14 offset:36352\nv_and_b32 v55,0xffff0000,v65\n" \
"v_mfma_f32_16x16x32_bf16 v[216:219],a[8:11],v[80:83],v[216:219]\nds_write_addtid_b32 v15 offset:36608\nv_lshlrev_b32 v252,16,v66\nv_mfma_f32_16x16x32_bf16 v[220:223],a[12:15],v[80:83],v[220:223]\n" \
"ds_write_addtid_b32 v16 offset:36864\nv_and_b32 v253,0xffff0000,v66\nv_mfma_f32_16x16x32_bf16 v[224:227],a[16:19],v[80:83],v[224:227]\nbuffer_load_dwordx4 v[120:123],v59,s[36:39],s69 offen sc1\n" \
"ds_write_addtid_b32 v17 offset:37120\nv_mfma_f32_16x16x32_bf16 v[228:231],a[20:23],v[80:83],v[228:231]\nds_write_addtid_b32 v18 offset:37376\nv_lshlrev_b32 v254,16,v67\n" \
"v_mfma_f32_16x16x32_bf16 v[232:235],a[24:27],v[80:83],v[232:235]\nds_write_addtid_b32 v19 offset:37632\nv_and_b32 v255,0xffff0000,v67\nv_mfma_f32_16x16x32_bf16 v[236:239],a[28:31],v[80:83],v[236:239]\n" \
"ds_write_addtid_b32 v20 offset:37888\nv_add_f32 v52,v52,v54\nv_mfma_f32_16x16x32_bf16 v[208:211],a[32:35],v[84:87],v[208:211]\nds_write_addtid_b32 v21 offset:38144\n" \
"v_add_f32 v53,v53,v55\nv_mfma_f32_16x16x32_bf16 v[212:215],a[36:39],v[84:87],v[212:215]\nds_write_addtid_b32 v22 offset:38400\nv_add_f32 v252,v252,v254\n" \
"v_mfma_f32_16x16x32_bf16 v[216:219],a[40:43],v[84:87],v[216:219]\nbuffer_load_dwordx4 v[124:127],v59,s[36:39],s69 offen offset:1024 sc1\nds_write_addtid_b32 v23 offset:38656\nv_mfma_f32_16x16x32_bf16 v[220:223],a[44:47],v[84:87],v[220:223]\n" \
"ds_write_addtid_b32 v24 offset:38912\nv_add_f32 v253,v253,v255\nv_mfma_f32_16x16x32_bf16 v[224:227],a[48:51],v[84:87],v[224:227]\nds_write_addtid_b32 v25 offset:39168\n" \
"s_waitcnt lgkmcnt(15)\nv_mfma_f32_16x16x32_bf16 v[228:231],a[52:55],v[84:87],v[228:231]\nds_write_addtid_b32 v26 offset:39424\nv_add_f32 v52,v52,v70\n" \
"v_mfma_f32_16x16x32_bf16 v[232:235],a[56:59],v[84:87],v[232:235]\nds_write_addtid_b32 v27 offset:39680\nv_add_f32 v53,v53,v70\nv_mfma_f32_16x16x32_bf16 v[236:239],a[60:63],v[84:87],v[236:239]\n" \
"ds_write_addtid_b32 v28 offset:39936\ns_waitcnt vmcnt(9)\nv_mfma_f32_16x16x32_bf16 v[208:211],a[64:67],v[88:91],v[208:211]\nbuffer_load_dwordx4 v[192:195],v59,s[36:39],s69 offen offset:2048 sc1\n" \
"ds_write_addtid_b32 v29 offset:40192\nv_mfma_f32_16x16x32_bf16 v[212:215],a[68:71],v[88:91],v[212:215]\nds_write_addtid_b32 v30 offset:40448\nv_lshlrev_b32 v54,16,v71\n" \
"v_mfma_f32_16x16x32_bf16 v[216:219],a[72:75],v[88:91],v[216:219]\nds_write_addtid_b32 v31 offset:40704\nv_and_b32 v55,0xffff0000,v71\nv_mfma_f32_16x16x32_bf16 v[220:223],a[76:79],v[88:91],v[220:223]\n" \
"v_lshlrev_b32 v254,16,v68\nv_and_b32 v255,0xffff0000,v68\nv_mfma_f32_16x16x32_bf16 v[224:227],a[80:83],v[88:91],v[224:227]\nv_add_f32 v52,v52,v252\n" \
"v_add_f32 v53,v53,v253\nv_mfma_f32_16x16x32_bf16 v[228:231],a[84:87],v[88:91],v[228:231]\nv_fma_f32 v52,v54,v52,v254\nv_fma_f32 v53,v55,v53,v255\n" \
"v_mfma_f32_16x16x32_bf16 v[232:235],a[88:91],v[88:91],v[232:235]\nbuffer_load_dwordx4 v[196:199],v59,s[36:39],s70 offen sc1\nv_cvt_pk_bf16_f32 v52,v52,v53\nv_mfma_f32_16x16x32_bf16 v[236:239],a[92:95],v[88:91],v[236:239]\n" \
"buffer_store_dword v52,v57,s[44:47],0 offen sc1\nv_mfma_f32_16x16x32_bf16 v[208:211],a[96:99],v[92:95],v[208:211]\ns_waitcnt lgkmcnt(0)\ns_barrier\n" \
"v_mfma_f32_16x16x32_bf16 v[212:215],a[100:103],v[92:95],v[212:215]\nds_read_b128 v[32:35],v58 offset:32768\nv_mfma_f32_16x16x32_bf16 v[216:219],a[104:107],v[92:95],v[216:219]\nds_read_b128 v[36:39],v58 offset:40960\n" \
"v_mfma_f32_16x16x32_bf16 v[220:223],a[108:111],v[92:95],v[220:223]\nds_read_b128 v[40:43],v58 offset:49152\nv_mfma_f32_16x16x32_bf16 v[224:227],a[112:115],v[92:95],v[224:227]\nbuffer_load_dwordx4 v[200:203],v59,s[36:39],s70 offen offset:1024 sc1\n" \
"ds_read_b128 v[44:47],v58 offset:57344\nv_mfma_f32_16x16x32_bf16 v[228:231],a[116:119],v[92:95],v[228:231]\nds_read_b128 v[48:51],v58 offset:36864\nv_mfma_f32_16x16x32_bf16 v[232:235],a[120:123],v[92:95],v[232:235]\n" \
"ds_read_b128 v[240:243],v58 offset:45056\nv_mfma_f32_16x16x32_bf16 v[236:239],a[124:127],v[92:95],v[236:239]\nds_read_b128 v[244:247],v58 offset:53248\ns_waitcnt vmcnt(11)\n" \
"v_mfma_f32_16x16x32_bf16 v[208:211],a[128:131],v[96:99],v[208:211]\nds_read_b128 v[248:251],v58 offset:61440\nv_mfma_f32_16x16x32_bf16 v[212:215],a[132:135],v[96:99],v[212:215]\nv_mfma_f32_16x16x32_bf16 v[216:219],a[136:139],v[96:99],v[216:219]\n" \
"buffer_load_dwordx4 v[204:207],v59,s[36:39],s70 offen offset:2048 sc1\nv_mfma_f32_16x16x32_bf16 v[220:223],a[140:143],v[96:99],v[220:223]\nv_mfma_f32_16x16x32_bf16 v[224:227],a[144:147],v[96:99],v[224:227]\ns_waitcnt lgkmcnt(4)\n" \
"v_add_f32 v32,v32,v36\nv_mfma_f32_16x16x32_bf16 v[228:231],a[148:151],v[96:99],v[228:231]\nv_add_f32 v33,v33,v37\nv_add_f32 v34,v34,v38\n" \
"v_mfma_f32_16x16x32_bf16 v[232:235],a[152:155],v[96:99],v[232:235]\nv_add_f32 v35,v35,v39\nv_add_f32 v40,v40,v44\nv_mfma_f32_16x16x32_bf16 v[236:239],a[156:159],v[96:99],v[236:239]\n" \
"v_add_f32 v41,v41,v45\nv_add_f32 v42,v42,v46\nv_mfma_f32_16x16x32_bf16 v[208:211],a[160:163],v[100:103],v[208:211]\nv_add_f32 v43,v43,v47\n" \
"v_add_f32 v32,v32,v40\nv_mfma_f32_16x16x32_bf16 v[212:215],a[164:167],v[100:103],v[212:215]\nv_add_f32 v33,v33,v41\nv_add_f32 v34,v34,v42\n" \
"v_mfma_f32_16x16x32_bf16 v[216:219],a[168:171],v[100:103],v[216:219]\nv_add_f32 v35,v35,v43\nv_cvt_pk_bf16_f32 v36,v32,v33\nv_mfma_f32_16x16x32_bf16 v[220:223],a[172:175],v[100:103],v[220:223]\n" \
"v_cvt_pk_bf16_f32 v37,v34,v35\ns_waitcnt lgkmcnt(0)\nv_mfma_f32_16x16x32_bf16 v[224:227],a[176:179],v[100:103],v[224:227]\nv_add_f32 v48,v48,v240\n" \
"v_add_f32 v49,v49,v241\nv_mfma_f32_16x16x32_bf16 v[228:231],a[180:183],v[100:103],v[228:231]\nv_add_f32 v50,v50,v242\nv_add_f32 v51,v51,v243\n" \
"v_mfma_f32_16x16x32_bf16 v[232:235],a[184:187],v[100:103],v[232:235]\nv_add_f32 v244,v244,v248\nv_add_f32 v245,v245,v249\nv_mfma_f32_16x16x32_bf16 v[236:239],a[188:191],v[100:103],v[236:239]\n" \
"v_add_f32 v246,v246,v250\ns_waitcnt vmcnt(11)\nv_mfma_f32_16x16x32_bf16 v[208:211],a[192:195],v[104:107],v[208:211]\nv_add_f32 v247,v247,v251\n" \
"v_add_f32 v48,v48,v244\nv_mfma_f32_16x16x32_bf16 v[212:215],a[196:199],v[104:107],v[212:215]\nv_add_f32 v49,v49,v245\nv_add_f32 v50,v50,v246\n" \
"v_mfma_f32_16x16x32_bf16 v[216:219],a[200:203],v[104:107],v[216:219]\nv_add_f32 v51,v51,v247\nv_cvt_pk_bf16_f32 v38,v48,v49\nv_mfma_f32_16x16x32_bf16 v[220:223],a[204:207],v[104:107],v[220:223]\n" \
"v_cvt_pk_bf16_f32 v39,v50,v51\nbuffer_store_dwordx4 v[36:39],v59,s[36:39],s74 offen sc1\nv_mfma_f32_16x16x32_bf16 v[224:227],a[208:211],v[104:107],v[224:227]\nv_mfma_f32_16x16x32_bf16 v[228:231],a[212:215],v[104:107],v[228:231]\n" \
"v_mfma_f32_16x16x32_bf16 v[232:235],a[216:219],v[104:107],v[232:235]\nv_mfma_f32_16x16x32_bf16 v[236:239],a[220:223],v[104:107],v[236:239]\ns_waitcnt vmcnt(9)\nv_mfma_f32_16x16x32_bf16 v[0:3],v[128:131],v[108:111],0\n" \
"v_add_u32 v59,409600,v59\nv_add_u32 v60,s98,v58\nv_mfma_f32_16x16x32_bf16 v[4:7],v[132:135],v[108:111],0\nds_write_addtid_b32 v208\n" \
"v_mfma_f32_16x16x32_bf16 v[8:11],v[136:139],v[108:111],0\nbuffer_load_dwordx4 v[72:75],v59,s[36:39],s68 offen sc1\nds_write_addtid_b32 v209 offset:256\nv_mfma_f32_16x16x32_bf16 v[12:15],v[140:143],v[108:111],0\n" \
"ds_write_addtid_b32 v210 offset:512\nv_mfma_f32_16x16x32_bf16 v[16:19],v[144:147],v[108:111],0\nds_write_addtid_b32 v211 offset:768\nv_mfma_f32_16x16x32_bf16 v[20:23],v[148:151],v[108:111],0\n" \
"buffer_load_dwordx4 v[64:67],v59,s[36:39],s71 offen sc1\nds_write_addtid_b32 v212 offset:1024\nv_mfma_f32_16x16x32_bf16 v[24:27],v[152:155],v[108:111],0\nds_write_addtid_b32 v213 offset:1280\n" \
"v_mfma_f32_16x16x32_bf16 v[28:31],v[156:159],v[108:111],0\nds_write_addtid_b32 v214 offset:1536\nv_mfma_f32_16x16x32_bf16 v[0:3],v[160:163],v[112:115],v[0:3]\nbuffer_load_dwordx4 v[76:79],v59,s[36:39],s68 offen offset:1024 sc1\n" \
"ds_write_addtid_b32 v215 offset:1792\nv_mfma_f32_16x16x32_bf16 v[4:7],v[164:167],v[112:115],v[4:7]\nds_write_addtid_b32 v216 offset:2048\nv_mfma_f32_16x16x32_bf16 v[8:11],v[168:171],v[112:115],v[8:11]\n" \
"ds_write_addtid_b32 v217 offset:2304\nv_mfma_f32_16x16x32_bf16 v[12:15],v[172:175],v[112:115],v[12:15]\nds_write_addtid_b32 v218 offset:2560\nv_mfma_f32_16x16x32_bf16 v[16:19],v[176:179],v[112:115],v[16:19]\n" \
"ds_write_addtid_b32 v219 offset:2816\nv_mfma_f32_16x16x32_bf16 v[20:23],v[180:183],v[112:115],v[20:23]\nds_write_addtid_b32 v220 offset:3072\nv_mfma_f32_16x16x32_bf16 v[24:27],v[184:187],v[112:115],v[24:27]\n" \
"buffer_load_dwordx4 v[80:83],v59,s[36:39],s68 offen offset:2048 sc1\nds_write_addtid_b32 v221 offset:3328\nv_mfma_f32_16x16x32_bf16 v[28:31],v[188:191],v[112:115],v[28:31]\nds_write_addtid_b32 v222 offset:3584\n" \
"s_waitcnt vmcnt(11)\nv_mfma_f32_16x16x32_bf16 v[0:3],a[0:3],v[116:119],v[0:3]\nds_write_addtid_b32 v223 offset:3840\nv_mfma_f32_16x16x32_bf16 v[4:7],a[4:7],v[116:119],v[4:7]\n" \
"buffer_load_dwordx4 v[52:55],v59,s[36:39],s76 offen sc1\nds_write_addtid_b32 v224 offset:4096\nv_mfma_f32_16x16x32_bf16 v[8:11],a[8:11],v[116:119],v[8:11]\nds_write_addtid_b32 v225 offset:4352\n" \
"v_mfma_f32_16x16x32_bf16 v[12:15],a[12:15],v[116:119],v[12:15]\nds_write_addtid_b32 v226 offset:4608\nv_mfma_f32_16x16x32_bf16 v[16:19],a[16:19],v[116:119],v[16:19]\nbuffer_load_dwordx4 v[84:87],v59,s[36:39],s69 offen sc1\n" \
"ds_write_addtid_b32 v227 offset:4864\nv_mfma_f32_16x16x32_bf16 v[20:23],a[20:23],v[116:119],v[20:23]\nds_write_addtid_b32 v228 offset:5120\nv_mfma_f32_16x16x32_bf16 v[24:27],a[24:27],v[116:119],v[24:27]\n" \
"ds_write_addtid_b32 v229 offset:5376\nv_mfma_f32_16x16x32_bf16 v[28:31],a[28:31],v[116:119],v[28:31]\nds_write_addtid_b32 v230 offset:5632\nv_mfma_f32_16x16x32_bf16 v[0:3],a[32:35],v[120:123],v[0:3]\n" \
"ds_write_addtid_b32 v231 offset:5888\nv_mfma_f32_16x16x32_bf16 v[4:7],a[36:39],v[120:123],v[4:7]\nds_write_addtid_b32 v232 offset:6144\nv_mfma_f32_16x16x32_bf16 v[8:11],a[40:43],v[120:123],v[8:11]\n" \
"buffer_load_dwordx4 v[88:91],v59,s[36:39],s69 offen offset:1024 sc1\nds_write_addtid_b32 v233 offset:6400\nv_mfma_f32_16x16x32_bf16 v[12:15],a[44:47],v[120:123],v[12:15]\nds_write_addtid_b32 v234 offset:6656\n" \
"v_mfma_f32_16x16x32_bf16 v[16:19],a[48:51],v[120:123],v[16:19]\nds_write_addtid_b32 v235 offset:6912\nv_mfma_f32_16x16x32_bf16 v[20:23],a[52:55],v[120:123],v[20:23]\nds_write_addtid_b32 v236 offset:7168\n" \
"v_mfma_f32_16x16x32_bf16 v[24:27],a[56:59],v[120:123],v[24:27]\nds_write_addtid_b32 v237 offset:7424\nv_mfma_f32_16x16x32_bf16 v[28:31],a[60:63],v[120:123],v[28:31]\nds_write_addtid_b32 v238 offset:7680\n" \
"s_waitcnt vmcnt(12)\nv_mfma_f32_16x16x32_bf16 v[0:3],a[64:67],v[124:127],v[0:3]\nbuffer_load_dwordx4 v[92:95],v59,s[36:39],s69 offen offset:2048 sc1\nds_write_addtid_b32 v239 offset:7936\n" \
"v_mfma_f32_16x16x32_bf16 v[4:7],a[68:71],v[124:127],v[4:7]\nds_read_b32 v61,v60 offset:28676\nv_mfma_f32_16x16x32_bf16 v[8:11],a[72:75],v[124:127],v[8:11]\nv_mfma_f32_16x16x32_bf16 v[12:15],a[76:79],v[124:127],v[12:15]\n" \
"v_mfma_f32_16x16x32_bf16 v[16:19],a[80:83],v[124:127],v[16:19]\nv_mfma_f32_16x16x32_bf16 v[20:23],a[84:87],v[124:127],v[20:23]\nv_mfma_f32_16x16x32_bf16 v[24:27],a[88:91],v[124:127],v[24:27]\nbuffer_load_dwordx4 v[96:99],v59,s[36:39],s70 offen sc1\n" \
"v_mfma_f32_16x16x32_bf16 v[28:31],a[92:95],v[124:127],v[28:31]\ns_waitcnt lgkmcnt(0)\ns_barrier\nv_mfma_f32_16x16x32_bf16 v[0:3],a[96:99],v[192:195],v[0:3]\n" \
"ds_read_b128 v[32:35],v58\nv_mfma_f32_16x16x32_bf16 v[4:7],a[100:103],v[192:195],v[4:7]\nds_read_b128 v[36:39],v58 offset:8192\nv_mfma_f32_16x16x32_bf16 v[8:11],a[104:107],v[192:195],v[8:11]\n" \
"ds_read_b128 v[40:43],v58 offset:16384\nv_lshlrev_b32 v60,16,v61\nv_mfma_f32_16x16x32_bf16 v[12:15],a[108:111],v[192:195],v[12:15]\nds_read_b128 v[44:47],v58 offset:24576\n" \
"v_and_b32 v61,0xffff0000,v61\nv_mfma_f32_16x16x32_bf16 v[16:19],a[112:115],v[192:195],v[16:19]\nbuffer_load_dwordx4 v[100:103],v59,s[36:39],s70 offen offset:1024 sc1\nds_read_b128 v[48:51],v58 offset:4096\n" \
"v_mfma_f32_16x16x32_bf16 v[20:23],a[116:119],v[192:195],v[20:23]\nds_read_b128 v[240:243],v58 offset:12288\nv_mfma_f32_16x16x32_bf16 v[24:27],a[120:123],v[192:195],v[24:27]\nds_read_b128 v[244:247],v58 offset:20480\n" \
"v_mfma_f32_16x16x32_bf16 v[28:31],a[124:127],v[192:195],v[28:31]\nds_read_b128 v[248:251],v58 offset:28672\ns_waitcnt vmcnt(12)\nv_mfma_f32_16x16x32_bf16 v[0:3],a[128:131],v[196:199],v[0:3]\n" \
"v_mfma_f32_16x16x32_bf16 v[4:7],a[132:135],v[196:199],v[4:7]\nv_mfma_f32_16x16x32_bf16 v[8:11],a[136:139],v[196:199],v[8:11]\nbuffer_load_dwordx4 v[104:107],v59,s[36:39],s70 offen offset:2048 sc1\nv_mfma_f32_16x16x32_bf16 v[12:15],a[140:143],v[196:199],v[12:15]\n" \
"s_waitcnt lgkmcnt(4)\nv_add_f32 v32,v32,v36\nv_mfma_f32_16x16x32_bf16 v[16:19],a[144:147],v[196:199],v[16:19]\nv_add_f32 v33,v33,v37\n" \
"v_add_f32 v34,v34,v38\nv_mfma_f32_16x16x32_bf16 v[20:23],a[148:151],v[196:199],v[20:23]\nv_add_f32 v35,v35,v39\nv_add_f32 v40,v40,v44\n" \
"v_mfma_f32_16x16x32_bf16 v[24:27],a[152:155],v[196:199],v[24:27]\nv_add_f32 v41,v41,v45\nv_add_f32 v42,v42,v46\nv_mfma_f32_16x16x32_bf16 v[28:31],a[156:159],v[196:199],v[28:31]\n" \
"v_add_f32 v43,v43,v47\nv_add_f32 v32,v32,v40\nv_mfma_f32_16x16x32_bf16 v[0:3],a[160:163],v[200:203],v[0:3]\nv_add_f32 v33,v33,v41\n" \
"v_add_f32 v34,v34,v42\nv_mfma_f32_16x16x32_bf16 v[4:7],a[164:167],v[200:203],v[4:7]\nv_add_f32 v35,v35,v43\nv_cvt_pk_bf16_f32 v36,v32,v33\n" \
"v_mfma_f32_16x16x32_bf16 v[8:11],a[168:171],v[200:203],v[8:11]\nv_cvt_pk_bf16_f32 v37,v34,v35\ns_waitcnt lgkmcnt(0)\nv_mfma_f32_16x16x32_bf16 v[12:15],a[172:175],v[200:203],v[12:15]\n" \
"v_add_f32 v48,v48,v240\nv_add_f32 v49,v49,v241\nv_mfma_f32_16x16x32_bf16 v[16:19],a[176:179],v[200:203],v[16:19]\nv_add_f32 v50,v50,v242\n" \
"v_add_f32 v51,v51,v243\nv_mfma_f32_16x16x32_bf16 v[20:23],a[180:183],v[200:203],v[20:23]\nv_add_f32 v244,v244,v248\nv_add_f32 v245,v245,v249\n" \
"v_mfma_f32_16x16x32_bf16 v[24:27],a[184:187],v[200:203],v[24:27]\nv_add_f32 v246,v246,v250\nv_add_f32 v247,v247,v251\nv_mfma_f32_16x16x32_bf16 v[28:31],a[188:191],v[200:203],v[28:31]\n" \
"v_add_f32 v48,v48,v244\ns_waitcnt vmcnt(12)\nv_mfma_f32_16x16x32_bf16 v[0:3],a[192:195],v[204:207],v[0:3]\nv_add_f32 v49,v49,v245\n" \
"v_add_f32 v50,v50,v246\nv_mfma_f32_16x16x32_bf16 v[4:7],a[196:199],v[204:207],v[4:7]\nv_add_f32 v51,v51,v247\nv_cvt_pk_bf16_f32 v38,v48,v49\n" \
"v_mfma_f32_16x16x32_bf16 v[8:11],a[200:203],v[204:207],v[8:11]\nv_cvt_pk_bf16_f32 v39,v50,v51\nbuffer_store_dwordx4 v[36:39],v59,s[36:39],s74 offen sc1\nv_mfma_f32_16x16x32_bf16 v[12:15],a[204:207],v[204:207],v[12:15]\n" \
"v_mfma_f32_16x16x32_bf16 v[16:19],a[208:211],v[204:207],v[16:19]\nv_mfma_f32_16x16x32_bf16 v[20:23],a[212:215],v[204:207],v[20:23]\nv_mfma_f32_16x16x32_bf16 v[24:27],a[216:219],v[204:207],v[24:27]\nv_mfma_f32_16x16x32_bf16 v[28:31],a[220:223],v[204:207],v[28:31]\n" \
"s_waitcnt vmcnt(9)\nv_mfma_f32_16x16x32_bf16 v[208:211],v[128:131],v[72:75],0\nv_add_u32 v59,409600,v59\ns_waitcnt vmcnt(7)\n" \
"v_mfma_f32_16x16x32_bf16 v[212:215],v[132:135],v[72:75],0\nds_write_addtid_b32 v0 offset:32768\nv_max_u32 v62,v64,v52\nv_mfma_f32_16x16x32_bf16 v[216:219],v[136:139],v[72:75],0\n" \
"buffer_load_dwordx4 v[108:111],v59,s[36:39],s68 offen sc1\nds_write_addtid_b32 v1 offset:33024\nv_mfma_f32_16x16x32_bf16 v[220:223],v[140:143],v[72:75],0\nds_write_addtid_b32 v2 offset:33280\n" \
"v_cmp_eq_u32 vcc,-1,v62\nv_mfma_f32_16x16x32_bf16 v[224:227],v[144:147],v[72:75],0\nds_write_addtid_b32 v3 offset:33536\nv_mfma_f32_16x16x32_bf16 v[228:231],v[148:151],v[72:75],0\n" \
"ds_write_addtid_b32 v4 offset:33792\ns_cbranch_vccnz .Lsp1_%=\n.Lbp1_%=:\nv_mfma_f32_16x16x32_bf16 v[232:235],v[152:155],v[72:75],0\n" \
"ds_write_addtid_b32 v5 offset:34048\nv_and_b32 v71,0xffff0000,v67\nv_mfma_f32_16x16x32_bf16 v[236:239],v[156:159],v[72:75],0\nds_write_addtid_b32 v6 offset:34304\n" \
"v_lshlrev_b32 v70,16,v67\nv_mfma_f32_16x16x32_bf16 v[208:211],v[160:163],v[76:79],v[208:211]\nbuffer_load_dwordx4 v[112:115],v59,s[36:39],s68 offen offset:1024 sc1\nds_write_addtid_b32 v7 offset:34560\n" \
"v_mfma_f32_16x16x32_bf16 v[212:215],v[164:167],v[76:79],v[212:215]\nds_write_addtid_b32 v8 offset:34816\nv_and_b32 v69,0xffff0000,v66\nv_mfma_f32_16x16x32_bf16 v[216:219],v[168:171],v[76:79],v[216:219]\n" \
"ds_write_addtid_b32 v9 offset:35072\nv_lshlrev_b32 v68,16,v66\nv_mfma_f32_16x16x32_bf16 v[220:223],v[172:175],v[76:79],v[220:223]\nds_write_addtid_b32 v10 offset:35328\n" \
"v_and_b32 v67,0xffff0000,v65\nv_mfma_f32_16x16x32_bf16 v[224:227],v[176:179],v[76:79],v[224:227]\nds_write_addtid_b32 v11 offset:35584\nv_lshlrev_b32 v66,16,v65\n" \
"v_mfma_f32_16x16x32_bf16 v[228:231],v[180:183],v[76:79],v[228:231]\nds_write_addtid_b32 v12 offset:35840\nv_and_b32 v65,0xffff0000,v64\nv_mfma_f32_16x16x32_bf16 v[232:235],v[184:187],v[76:79],v[232:235]\n" \
"buffer_load_dwordx4 v[116:119],v59,s[36:39],s68 offen offset:2048 sc1\nds_write_addtid_b32 v13 offset:36096\nv_mfma_f32_16x16x32_bf16 v[236:239],v[188:191],v[76:79],v[236:239]\nds_write_addtid_b32 v14 offset:36352\n" \
"s_waitcnt vmcnt(9)\nv_mfma_f32_16x16x32_bf16 v[208:211],a[0:3],v[80:83],v[208:211]\nds_write_addtid_b32 v15 offset:36608\nv_lshlrev_b32 v64,16,v64\n" \
"v_mfma_f32_16x16x32_bf16 v[212:215],a[4:7],v[80:83],v[212:215]\nds_write_addtid_b32 v16 offset:36864\nv_add_f32 v64,v64,v60\nv_mfma_f32_16x16x32_bf16 v[216:219],a[8:11],v[80:83],v[216:219]\n" \
"ds_write_addtid_b32 v17 offset:37120\nv_add_f32 v65,v65,v60\nv_mfma_f32_16x16x32_bf16 v[220:223],a[12:15],v[80:83],v[220:223]\nds_write_addtid_b32 v18 offset:37376\n" \
"v_add_f32 v66,v66,v60\nv_mfma_f32_16x16x32_bf16 v[224:227],a[16:19],v[80:83],v[224:227]\nbuffer_load_dwordx4 v[120:123],v59,s[36:39],s69 offen sc1\nds_write_addtid_b32 v19 offset:37632\n" \
"v_mfma_f32_16x16x32_bf16 v[228:231],a[20:23],v[80:83],v[228:231]\nds_write_addtid_b32 v20 offset:37888\nv_add_f32 v67,v67,v60\nv_mfma_f32_16x16x32_bf16 v[232:235],a[24:27],v[80:83],v[232:235]\n" \
"ds_write_addtid_b32 v21 offset:38144\nv_add_f32 v68,v68,v61\nv_mfma_f32_16x16x32_bf16 v[236:239],a[28:31],v[80:83],v[236:239]\nds_write_addtid_b32 v22 offset:38400\n" \
"v_add_f32 v69,v69,v61\nv_mfma_f32_16x16x32_bf16 v[208:211],a[32:35],v[84:87],v[208:211]\nds_write_addtid_b32 v23 offset:38656\nv_add_f32 v70,v70,v61\n" \
"v_mfma_f32_16x16x32_bf16 v[212:215],a[36:39],v[84:87],v[212:215]\nds_write_addtid_b32 v24 offset:38912\nv_add_f32 v71,v71,v61\nv_mfma_f32_16x16x32_bf16 v[216:219],a[40:43],v[84:87],v[216:219]\n" \
"buffer_load_dwordx4 v[124:127],v59,s[36:39],s69 offen offset:1024 sc1\nds_write_addtid_b32 v25 offset:39168\nv_mfma_f32_16x16x32_bf16 v[220:223],a[44:47],v[84:87],v[220:223]\nds_write_addtid_b32 v26 offset:39424\n" \
"v_and_b32 v255,0xffff0000,v55\nv_mfma_f32_16x16x32_bf16 v[224:227],a[48:51],v[84:87],v[224:227]\nds_write_addtid_b32 v27 offset:39680\nv_lshlrev_b32 v254,16,v55\n" \
"v_mfma_f32_16x16x32_bf16 v[228:231],a[52:55],v[84:87],v[228:231]\nds_write_addtid_b32 v28 offset:39936\nv_and_b32 v253,0xffff0000,v54\nv_mfma_f32_16x16x32_bf16 v[232:235],a[56:59],v[84:87],v[232:235]\n" \
"ds_write_addtid_b32 v29 offset:40192\nv_lshlrev_b32 v252,16,v54\nv_mfma_f32_16x16x32_bf16 v[236:239],a[60:63],v[84:87],v[236:239]\nds_write_addtid_b32 v30 offset:40448\n" \
"s_waitcnt vmcnt(9)\nv_mfma_f32_16x16x32_bf16 v[208:211],a[64:67],v[88:91],v[208:211]\nbuffer_load_dwordx4 v[192:195],v59,s[36:39],s69 offen offset:2048 sc1\nds_write_addtid_b32 v31 offset:40704\n" \
"v_mfma_f32_16x16x32_bf16 v[212:215],a[68:71],v[88:91],v[212:215]\nv_and_b32 v55,0xffff0000,v53\nv_lshlrev_b32 v54,16,v53\nv_mfma_f32_16x16x32_bf16 v[216:219],a[72:75],v[88:91],v[216:219]\n" \
"v_and_b32 v53,0xffff0000,v52\nv_lshlrev_b32 v52,16,v52\nv_mfma_f32_16x16x32_bf16 v[220:223],a[76:79],v[88:91],v[220:223]\nv_add_f32 v52,v52,v60\n" \
"v_add_f32 v53,v53,v60\nv_mfma_f32_16x16x32_bf16 v[224:227],a[80:83],v[88:91],v[224:227]\nv_add_f32 v54,v54,v60\nv_add_f32 v55,v55,v60\n" \
"v_mfma_f32_16x16x32_bf16 v[228:231],a[84:87],v[88:91],v[228:231]\nv_add_f32 v252,v252,v61\nv_add_f32 v253,v253,v61\nv_mfma_f32_16x16x32_bf16 v[232:235],a[88:91],v[88:91],v[232:235]\n" \
"buffer_load_dwordx4 v[196:199],v59,s[36:39],s70 offen sc1\nv_add_f32 v254,v254,v61\nv_mfma_f32_16x16x32_bf16 v[236:239],a[92:95],v[88:91],v[236:239]\ns_waitcnt lgkmcnt(0)\n" \
"s_barrier\nv_mfma_f32_16x16x32_bf16 v[208:211],a[96:99],v[92:95],v[208:211]\nds_read_b128 v[32:35],v58 offset:32768\nv_add_f32 v255,v255,v61\n" \
"v_mfma_f32_16x16x32_bf16 v[212:215],a[100:103],v[92:95],v[212:215]\nds_read_b128 v[36:39],v58 offset:40960\nv_mfma_f32_16x16x32_bf16 v[216:219],a[104:107],v[92:95],v[216:219]\nds_read_b128 v[40:43],v58 offset:49152\n" \
"v_mfma_f32_16x16x32_bf16 v[220:223],a[108:111],v[92:95],v[220:223]\nds_read_b128 v[44:47],v58 offset:57344\nv_mfma_f32_16x16x32_bf16 v[224:227],a[112:115],v[92:95],v[224:227]\nbuffer_load_dwordx4 v[200:203],v59,s[36:39],s70 offen offset:1024 sc1\n" \
"ds_read_b128 v[48:51],v58 offset:36864\nv_mfma_f32_16x16x32_bf16 v[228:231],a[116:119],v[92:95],v[228:231]\nds_read_b128 v[240:243],v58 offset:45056\nv_mfma_f32_16x16x32_bf16 v[232:235],a[120:123],v[92:95],v[232:235]\n" \
"ds_read_b128 v[244:247],v58 offset:53248\nv_mfma_f32_16x16x32_bf16 v[236:239],a[124:127],v[92:95],v[236:239]\nbuffer_load_dwordx4 v[60:63],v59,s[36:39],s72 offen sc1\ns_waitcnt vmcnt(11)\n" \
"v_mfma_f32_16x16x32_bf16 v[208:211],a[128:131],v[96:99],v[208:211]\nds_read_b128 v[248:251],v58 offset:61440\nv_mfma_f32_16x16x32_bf16 v[212:215],a[132:135],v[96:99],v[212:215]\nv_mfma_f32_16x16x32_bf16 v[216:219],a[136:139],v[96:99],v[216:219]\n" \
"buffer_load_dwordx4 v[204:207],v59,s[36:39],s70 offen offset:2048 sc1\nv_mfma_f32_16x16x32_bf16 v[220:223],a[140:143],v[96:99],v[220:223]\ns_waitcnt lgkmcnt(4)\nv_add_f32 v32,v32,v36\n" \
"v_mfma_f32_16x16x32_bf16 v[224:227],a[144:147],v[96:99],v[224:227]\nv_add_f32 v33,v33,v37\nv_add_f32 v34,v34,v38\nv_mfma_f32_16x16x32_bf16 v[228:231],a[148:151],v[96:99],v[228:231]\n" \
"v_add_f32 v35,v35,v39\nv_add_f32 v40,v40,v44\nv_mfma_f32_16x16x32_bf16 v[232:235],a[152:155],v[96:99],v[232:235]\nv_add_f32 v41,v41,v45\n" \
"v_add_f32 v42,v42,v46\nv_mfma_f32_16x16x32_bf16 v[236:239],a[156:159],v[96:99],v[236:239]\nv_add_f32 v43,v43,v47\nv_add_f32 v32,v32,v40\n" \
"v_mfma_f32_16x16x32_bf16 v[208:211],a[160:163],v[100:103],v[208:211]\nv_add_f32 v33,v33,v41\nv_add_f32 v34,v34,v42\nv_mfma_f32_16x16x32_bf16 v[212:215],a[164:167],v[100:103],v[212:215]\n" \
"v_add_f32 v35,v35,v43\nv_cvt_pk_bf16_f32 v36,v32,v33\nv_mfma_f32_16x16x32_bf16 v[216:219],a[168:171],v[100:103],v[216:219]\nv_cvt_pk_bf16_f32 v37,v34,v35\n" \
"s_waitcnt lgkmcnt(0)\nv_mfma_f32_16x16x32_bf16 v[220:223],a[172:175],v[100:103],v[220:223]\nv_add_f32 v48,v48,v240\nv_add_f32 v49,v49,v241\n" \
"v_mfma_f32_16x16x32_bf16 v[224:227],a[176:179],v[100:103],v[224:227]\nv_add_f32 v50,v50,v242\nv_add_f32 v51,v51,v243\nv_mfma_f32_16x16x32_bf16 v[228:231],a[180:183],v[100:103],v[228:231]\n" \
"v_add_f32 v244,v244,v248\nv_add_f32 v245,v245,v249\nv_mfma_f32_16x16x32_bf16 v[232:235],a[184:187],v[100:103],v[232:235]\nv_add_f32 v246,v246,v250\n" \
"v_add_f32 v247,v247,v251\nv_mfma_f32_16x16x32_bf16 v[236:239],a[188:191],v[100:103],v[236:239]\nv_add_f32 v48,v48,v244\ns_waitcnt vmcnt(11)\n" \
"v_mfma_f32_16x16x32_bf16 v[208:211],a[192:195],v[104:107],v[208:211]\nv_add_f32 v49,v49,v245\nv_add_f32 v50,v50,v246\nv_mfma_f32_16x16x32_bf16 v[212:215],a[196:199],v[104:107],v[212:215]\n" \
"v_add_f32 v51,v51,v247\nv_cvt_pk_bf16_f32 v38,v48,v49\nv_mfma_f32_16x16x32_bf16 v[216:219],a[200:203],v[104:107],v[216:219]\nv_cvt_pk_bf16_f32 v39,v50,v51\n" \
"buffer_store_dwordx4 v[36:39],v59,s[36:39],s74 offen sc1\nv_mfma_f32_16x16x32_bf16 v[220:223],a[204:207],v[104:107],v[220:223]\nv_mfma_f32_16x16x32_bf16 v[224:227],a[208:211],v[104:107],v[224:227]\nv_mfma_f32_16x16x32_bf16 v[228:231],a[212:215],v[104:107],v[228:231]\n" \
"v_mfma_f32_16x16x32_bf16 v[232:235],a[216:219],v[104:107],v[232:235]\nv_mfma_f32_16x16x32_bf16 v[236:239],a[220:223],v[104:107],v[236:239]\ns_waitcnt vmcnt(9)\nv_mfma_f32_16x16x32_bf16 v[0:3],v[128:131],v[108:111],0\n" \
"v_add_u32 v59,409600,v59\nv_mfma_f32_16x16x32_bf16 v[4:7],v[132:135],v[108:111],0\nds_write_addtid_b32 v208\nds_write_addtid_b32 v209 offset:256\n" \
"v_mfma_f32_16x16x32_bf16 v[8:11],v[136:139],v[108:111],0\nbuffer_load_dwordx4 v[72:75],v59,s[36:39],s68 offen sc1\nds_write_addtid_b32 v210 offset:512\nv_mfma_f32_16x16x32_bf16 v[12:15],v[140:143],v[108:111],0\n" \
"ds_write_addtid_b32 v211 offset:768\nds_write_addtid_b32 v212 offset:1024\nv_mfma_f32_16x16x32_bf16 v[16:19],v[144:147],v[108:111],0\nds_write_addtid_b32 v213 offset:1280\n" \
"ds_write_addtid_b32 v214 offset:1536\nv_mfma_f32_16x16x32_bf16 v[20:23],v[148:151],v[108:111],0\nds_write_addtid_b32 v215 offset:1792\nds_write_addtid_b32 v216 offset:2048\n" \
"v_mfma_f32_16x16x32_bf16 v[24:27],v[152:155],v[108:111],0\nds_write_addtid_b32 v217 offset:2304\nds_write_addtid_b32 v218 offset:2560\nv_mfma_f32_16x16x32_bf16 v[28:31],v[156:159],v[108:111],0\n" \
"ds_write_addtid_b32 v219 offset:2816\nds_write_addtid_b32 v220 offset:3072\nv_mfma_f32_16x16x32_bf16 v[0:3],v[160:163],v[112:115],v[0:3]\nbuffer_load_dwordx4 v[76:79],v59,s[36:39],s68 offen offset:1024 sc1\n" \
"ds_write_addtid_b32 v221 offset:3328\nv_mfma_f32_16x16x32_bf16 v[4:7],v[164:167],v[112:115],v[4:7]\nds_write_addtid_b32 v222 offset:3584\nds_write_addtid_b32 v223 offset:3840\n" \
"v_mfma_f32_16x16x32_bf16 v[8:11],v[168:171],v[112:115],v[8:11]\nds_write_addtid_b32 v224 offset:4096\nds_write_addtid_b32 v225 offset:4352\nv_mfma_f32_16x16x32_bf16 v[12:15],v[172:175],v[112:115],v[12:15]\n" \
"buffer_load_dwordx4 v[108:111],v59,s[36:39],s77 offen sc1\nds_write_addtid_b32 v226 offset:4608\nv_mfma_f32_16x16x32_bf16 v[16:19],v[176:179],v[112:115],v[16:19]\nds_write_addtid_b32 v227 offset:4864\n" \
"ds_write_addtid_b32 v228 offset:5120\nv_mfma_f32_16x16x32_bf16 v[20:23],v[180:183],v[112:115],v[20:23]\nds_write_addtid_b32 v229 offset:5376\nds_write_addtid_b32 v230 offset:5632\n" \
"v_mfma_f32_16x16x32_bf16 v[24:27],v[184:187],v[112:115],v[24:27]\nbuffer_load_dwordx4 v[80:83],v59,s[36:39],s68 offen offset:2048 sc1\nds_write_addtid_b32 v231 offset:5888\nv_mfma_f32_16x16x32_bf16 v[28:31],v[188:191],v[112:115],v[28:31]\n" \
"ds_write_addtid_b32 v232 offset:6144\ns_waitcnt vmcnt(11)\nv_mfma_f32_16x16x32_bf16 v[0:3],a[0:3],v[116:119],v[0:3]\nds_write_addtid_b32 v233 offset:6400\n" \
"ds_write_addtid_b32 v234 offset:6656\nv_mfma_f32_16x16x32_bf16 v[4:7],a[4:7],v[116:119],v[4:7]\nds_write_addtid_b32 v235 offset:6912\nds_write_addtid_b32 v236 offset:7168\n" \
"v_mfma_f32_16x16x32_bf16 v[8:11],a[8:11],v[116:119],v[8:11]\nds_write_addtid_b32 v237 offset:7424\nds_write_addtid_b32 v238 offset:7680\nv_mfma_f32_16x16x32_bf16 v[12:15],a[12:15],v[116:119],v[12:15]\n" \
"ds_write_addtid_b32 v239 offset:7936\nv_mfma_f32_16x16x32_bf16 v[16:19],a[16:19],v[116:119],v[16:19]\nbuffer_load_dwordx4 v[84:87],v59,s[36:39],s69 offen sc1\nv_mfma_f32_16x16x32_bf16 v[20:23],a[20:23],v[116:119],v[20:23]\n" \
"v_mfma_f32_16x16x32_bf16 v[24:27],a[24:27],v[116:119],v[24:27]\nv_mfma_f32_16x16x32_bf16 v[28:31],a[28:31],v[116:119],v[28:31]\nv_mfma_f32_16x16x32_bf16 v[0:3],a[32:35],v[120:123],v[0:3]\nv_mfma_f32_16x16x32_bf16 v[4:7],a[36:39],v[120:123],v[4:7]\n" \
"s_waitcnt lgkmcnt(0)\ns_barrier\nv_mfma_f32_16x16x32_bf16 v[8:11],a[40:43],v[120:123],v[8:11]\nbuffer_load_dwordx4 v[88:91],v59,s[36:39],s69 offen offset:1024 sc1\n" \
"ds_read_b128 v[32:35],v58\nv_mfma_f32_16x16x32_bf16 v[12:15],a[44:47],v[120:123],v[12:15]\nds_read_b128 v[36:39],v58 offset:8192\nds_read_b128 v[40:43],v58 offset:16384\n" \
"v_mfma_f32_16x16x32_bf16 v[16:19],a[48:51],v[120:123],v[16:19]\nds_read_b128 v[44:47],v58 offset:24576\nds_read_b128 v[48:51],v58 offset:4096\nv_mfma_f32_16x16x32_bf16 v[20:23],a[52:55],v[120:123],v[20:23]\n" \
"ds_read_b128 v[240:243],v58 offset:12288\nds_read_b128 v[244:247],v58 offset:20480\nv_mfma_f32_16x16x32_bf16 v[24:27],a[56:59],v[120:123],v[24:27]\nds_read_b128 v[248:251],v58 offset:28672\n" \
"s_waitcnt vmcnt(8)\nv_mfma_f32_16x16x32_bf16 v[28:31],a[60:63],v[120:123],v[28:31]\nv_cmp_eq_u32 vcc,-1,v60\nv_mfma_f32_16x16x32_bf16 v[0:3],a[64:67],v[124:127],v[0:3]\n" \
"buffer_load_dwordx4 v[92:95],v59,s[36:39],s69 offen offset:2048 sc1\nv_mfma_f32_16x16x32_bf16 v[4:7],a[68:71],v[124:127],v[4:7]\nbuffer_load_dwordx2 v[116:117],v56,s[36:39],s90 offen sc1\ns_cbranch_vccnz .Lsp2A_%=\n" \
".Lbp2A_%=:\nv_mfma_f32_16x16x32_bf16 v[8:11],a[72:75],v[124:127],v[8:11]\nbuffer_load_dwordx2 v[118:119],v56,s[36:39],s90 offen offset:32 sc1\nv_lshlrev_b32 v112,16,v60\n" \
"v_mfma_f32_16x16x32_bf16 v[12:15],a[76:79],v[124:127],v[12:15]\nv_and_b32 v113,0xffff0000,v60\nv_add_f32 v64,v64,v112\nv_mfma_f32_16x16x32_bf16 v[16:19],a[80:83],v[124:127],v[16:19]\n" \
"s_waitcnt lgkmcnt(4)\nv_add_f32 v32,v32,v36\nv_mfma_f32_16x16x32_bf16 v[20:23],a[84:87],v[124:127],v[20:23]\nv_add_f32 v33,v33,v37\n" \
"v_add_f32 v34,v34,v38\nv_mfma_f32_16x16x32_bf16 v[24:27],a[88:91],v[124:127],v[24:27]\nbuffer_load_dwordx4 v[96:99],v59,s[36:39],s70 offen sc1\nv_add_f32 v35,v35,v39\n" \
"v_mfma_f32_16x16x32_bf16 v[28:31],a[92:95],v[124:127],v[28:31]\nbuffer_load_dwordx2 v[120:121],v56,s[36:39],s100 offen sc1\nv_add_f32 v40,v40,v44\nv_mfma_f32_16x16x32_bf16 v[0:3],a[96:99],v[192:195],v[0:3]\n" \
"buffer_load_dwordx2 v[122:123],v56,s[36:39],s100 offen offset:32 sc1\nv_add_f32 v41,v41,v45\nv_mfma_f32_16x16x32_bf16 v[4:7],a[100:103],v[192:195],v[4:7]\nv_add_f32 v42,v42,v46\n" \
"v_add_f32 v43,v43,v47\nv_mfma_f32_16x16x32_bf16 v[8:11],a[104:107],v[192:195],v[8:11]\nv_add_f32 v32,v32,v40\nv_add_f32 v33,v33,v41\n" \
"v_mfma_f32_16x16x32_bf16 v[12:15],a[108:111],v[192:195],v[12:15]\nv_add_f32 v34,v34,v42\nv_add_f32 v35,v35,v43\nv_mfma_f32_16x16x32_bf16 v[16:19],a[112:115],v[192:195],v[16:19]\n" \
"buffer_load_dwordx4 v[100:103],v59,s[36:39],s70 offen offset:1024 sc1\nv_cvt_pk_bf16_f32 v36,v32,v33\nv_mfma_f32_16x16x32_bf16 v[20:23],a[116:119],v[192:195],v[20:23]\nv_cvt_pk_bf16_f32 v37,v34,v35\n" \
"s_waitcnt lgkmcnt(0)\nv_mfma_f32_16x16x32_bf16 v[24:27],a[120:123],v[192:195],v[24:27]\nv_add_f32 v48,v48,v240\nv_add_f32 v49,v49,v241\n" \
"v_mfma_f32_16x16x32_bf16 v[28:31],a[124:127],v[192:195],v[28:31]\nv_add_f32 v50,v50,v242\nv_mfma_f32_16x16x32_bf16 v[0:3],a[128:131],v[196:199],v[0:3]\nv_add_f32 v65,v65,v113\n" \
"v_add_f32 v51,v51,v243\nv_mfma_f32_16x16x32_bf16 v[4:7],a[132:135],v[196:199],v[4:7]\nv_lshlrev_b32 v112,16,v61\nv_add_f32 v244,v244,v248\n" \
"v_mfma_f32_16x16x32_bf16 v[8:11],a[136:139],v[196:199],v[8:11]\nbuffer_load_dwordx4 v[104:107],v59,s[36:39],s70 offen offset:2048 sc1\nv_and_b32 v113,0xffff0000,v61\nv_mfma_f32_16x16x32_bf16 v[12:15],a[140:143],v[196:199],v[12:15]\n" \
"v_add_f32 v245,v245,v249\nv_add_f32 v66,v66,v112\nv_mfma_f32_16x16x32_bf16 v[16:19],a[144:147],v[196:199],v[16:19]\nv_add_f32 v246,v246,v250\n" \
"v_add_f32 v67,v67,v113\nv_mfma_f32_16x16x32_bf16 v[20:23],a[148:151],v[196:199],v[20:23]\nv_add_f32 v247,v247,v251\nv_lshlrev_b32 v112,16,v62\n" \
"v_mfma_f32_16x16x32_bf16 v[24:27],a[152:155],v[196:199],v[24:27]\nv_add_f32 v48,v48,v244\nv_and_b32 v113,0xffff0000,v62\nv_mfma_f32_16x16x32_bf16 v[28:31],a[156:159],v[196:199],v[28:31]\n" \
"v_add_f32 v49,v49,v245\nv_add_f32 v68,v68,v112\nv_mfma_f32_16x16x32_bf16 v[0:3],a[160:163],v[200:203],v[0:3]\nv_add_f32 v50,v50,v246\n" \
"v_add_f32 v69,v69,v113\nv_mfma_f32_16x16x32_bf16 v[4:7],a[164:167],v[200:203],v[4:7]\nv_add_f32 v51,v51,v247\nv_lshlrev_b32 v112,16,v63\n" \
"v_mfma_f32_16x16x32_bf16 v[8:11],a[168:171],v[200:203],v[8:11]\nv_cvt_pk_bf16_f32 v38,v48,v49\nv_and_b32 v113,0xffff0000,v63\nv_mfma_f32_16x16x32_bf16 v[12:15],a[172:175],v[200:203],v[12:15]\n" \
"v_cvt_pk_bf16_f32 v39,v50,v51\nv_add_f32 v70,v70,v112\nv_mfma_f32_16x16x32_bf16 v[16:19],a[176:179],v[200:203],v[16:19]\nbuffer_store_dwordx4 v[36:39],v59,s[36:39],s74 offen sc1\n" \
"v_add_f32 v71,v71,v113\nv_mfma_f32_16x16x32_bf16 v[20:23],a[180:183],v[200:203],v[20:23]\nv_mfma_f32_16x16x32_bf16 v[24:27],a[184:187],v[200:203],v[24:27]\nv_mfma_f32_16x16x32_bf16 v[28:31],a[188:191],v[200:203],v[28:31]\n" \
"s_waitcnt vmcnt(16)\nv_mfma_f32_16x16x32_bf16 v[0:3],a[192:195],v[204:207],v[0:3]\nv_mfma_f32_16x16x32_bf16 v[4:7],a[196:199],v[204:207],v[4:7]\nv_mfma_f32_16x16x32_bf16 v[8:11],a[200:203],v[204:207],v[8:11]\n" \
"v_mfma_f32_16x16x32_bf16 v[12:15],a[204:207],v[204:207],v[12:15]\nv_mfma_f32_16x16x32_bf16 v[16:19],a[208:211],v[204:207],v[16:19]\nv_mfma_f32_16x16x32_bf16 v[20:23],a[212:215],v[204:207],v[20:23]\nv_mfma_f32_16x16x32_bf16 v[24:27],a[216:219],v[204:207],v[24:27]\n" \
"v_mfma_f32_16x16x32_bf16 v[28:31],a[220:223],v[204:207],v[28:31]\ns_waitcnt vmcnt(13)\nv_mfma_f32_16x16x32_bf16 v[208:211],v[128:131],v[72:75],0\nv_add_u32 v59,409600,v59\n" \
"s_waitcnt vmcnt(12)\nv_mfma_f32_16x16x32_bf16 v[212:215],v[132:135],v[72:75],0\nds_write_addtid_b32 v0 offset:32768\nds_write_addtid_b32 v1 offset:33024\n" \
"v_mfma_f32_16x16x32_bf16 v[216:219],v[136:139],v[72:75],0\nds_write_addtid_b32 v2 offset:33280\nds_write_addtid_b32 v3 offset:33536\nv_mfma_f32_16x16x32_bf16 v[220:223],v[140:143],v[72:75],0\n" \
"buffer_load_dwordx4 v[112:115],v59,s[36:39],s73 offen sc1\nds_write_addtid_b32 v4 offset:33792\nv_mfma_f32_16x16x32_bf16 v[208:211],v[160:163],v[76:79],v[208:211]\nds_write_addtid_b32 v5 offset:34048\n" \
"ds_write_addtid_b32 v6 offset:34304\nv_mfma_f32_16x16x32_bf16 v[212:215],v[164:167],v[76:79],v[212:215]\nds_write_addtid_b32 v7 offset:34560\nds_write_addtid_b32 v8 offset:34816\n" \
"v_mfma_f32_16x16x32_bf16 v[216:219],v[168:171],v[76:79],v[216:219]\nds_write_addtid_b32 v9 offset:35072\nds_write_addtid_b32 v10 offset:35328\nv_mfma_f32_16x16x32_bf16 v[220:223],v[172:175],v[76:79],v[220:223]\n" \
"ds_write_addtid_b32 v11 offset:35584\ns_waitcnt vmcnt(11)\nv_mfma_f32_16x16x32_bf16 v[208:211],a[0:3],v[80:83],v[208:211]\nds_write_addtid_b32 v12 offset:35840\n" \
"ds_write_addtid_b32 v13 offset:36096\nv_mfma_f32_16x16x32_bf16 v[212:215],a[4:7],v[80:83],v[212:215]\nds_write_addtid_b32 v14 offset:36352\nds_write_addtid_b32 v15 offset:36608\n" \
"v_mfma_f32_16x16x32_bf16 v[216:219],a[8:11],v[80:83],v[216:219]\nds_write_addtid_b32 v16 offset:36864\nds_write_addtid_b32 v17 offset:37120\nv_mfma_f32_16x16x32_bf16 v[220:223],a[12:15],v[80:83],v[220:223]\n" \
"ds_write_addtid_b32 v18 offset:37376\nds_write_addtid_b32 v19 offset:37632\nv_mfma_f32_16x16x32_bf16 v[208:211],a[32:35],v[84:87],v[208:211]\nds_write_addtid_b32 v20 offset:37888\n" \
"ds_write_addtid_b32 v21 offset:38144\nv_mfma_f32_16x16x32_bf16 v[212:215],a[36:39],v[84:87],v[212:215]\nds_write_addtid_b32 v22 offset:38400\nds_write_addtid_b32 v23 offset:38656\n" \
"v_mfma_f32_16x16x32_bf16 v[216:219],a[40:43],v[84:87],v[216:219]\nds_write_addtid_b32 v24 offset:38912\nds_write_addtid_b32 v25 offset:39168\nv_mfma_f32_16x16x32_bf16 v[220:223],a[44:47],v[84:87],v[220:223]\n" \
"ds_write_addtid_b32 v26 offset:39424\ns_waitcnt vmcnt(9)\nv_mfma_f32_16x16x32_bf16 v[208:211],a[64:67],v[88:91],v[208:211]\nds_write_addtid_b32 v27 offset:39680\n" \
"ds_write_addtid_b32 v28 offset:39936\nv_mfma_f32_16x16x32_bf16 v[212:215],a[68:71],v[88:91],v[212:215]\nds_write_addtid_b32 v29 offset:40192\nds_write_addtid_b32 v30 offset:40448\n" \
"v_mfma_f32_16x16x32_bf16 v[216:219],a[72:75],v[88:91],v[216:219]\nds_write_addtid_b32 v31 offset:40704\nv_cmp_eq_u32 vcc,-1,v108\nv_mfma_f32_16x16x32_bf16 v[220:223],a[76:79],v[88:91],v[220:223]\n" \
"v_and_b32 v62,0x30,v59\nv_add_u32 v62,s67,v62\nv_mfma_f32_16x16x32_bf16 v[208:211],a[96:99],v[92:95],v[208:211]\ns_cbranch_vccnz .Lsp2B_%=\n" \
".Lbp2B_%=:\nv_lshlrev_b32 v60,16,v108\nv_mfma_f32_16x16x32_bf16 v[212:215],a[100:103],v[92:95],v[212:215]\nv_and_b32 v61,0xffff0000,v108\n" \
"v_add_f32 v52,v52,v60\nv_mfma_f32_16x16x32_bf16 v[216:219],a[104:107],v[92:95],v[216:219]\nv_add_f32 v53,v53,v61\nv_lshlrev_b32 v60,16,v109\n" \
"v_mfma_f32_16x16x32_bf16 v[220:223],a[108:111],v[92:95],v[220:223]\nv_and_b32 v61,0xffff0000,v109\ns_waitcnt vmcnt(3)\nv_mfma_f32_16x16x32_bf16 v[208:211],a[128:131],v[96:99],v[208:211]\n" \
"s_waitcnt lgkmcnt(0)\ns_barrier\nv_mfma_f32_16x16x32_bf16 v[212:215],a[132:135],v[96:99],v[212:215]\nds_read_b128 v[32:35],v58 offset:32768\n" \
"ds_read_b128 v[36:39],v58 offset:40960\nv_mfma_f32_16x16x32_bf16 v[216:219],a[136:139],v[96:99],v[216:219]\nds_read_b128 v[40:43],v58 offset:49152\nds_read_b128 v[44:47],v58 offset:57344\n" \
"v_mfma_f32_16x16x32_bf16 v[220:223],a[140:143],v[96:99],v[220:223]\nds_read_b128 v[48:51],v58 offset:36864\nds_read_b128 v[240:243],v58 offset:45056\nv_mfma_f32_16x16x32_bf16 v[208:211],a[160:163],v[100:103],v[208:211]\n" \
"ds_read_b128 v[244:247],v58 offset:53248\nds_read_b128 v[248:251],v58 offset:61440\nv_mfma_f32_16x16x32_bf16 v[212:215],a[164:167],v[100:103],v[212:215]\nv_add_f32 v54,v54,v60\n" \
"v_add_f32 v55,v55,v61\nv_mfma_f32_16x16x32_bf16 v[216:219],a[168:171],v[100:103],v[216:219]\nv_lshlrev_b32 v60,16,v110\nv_and_b32 v61,0xffff0000,v110\n" \
"v_mfma_f32_16x16x32_bf16 v[220:223],a[172:175],v[100:103],v[220:223]\nv_add_f32 v252,v252,v60\ns_waitcnt vmcnt(2)\nv_mfma_f32_16x16x32_bf16 v[208:211],a[192:195],v[104:107],v[208:211]\n" \
"v_add_f32 v253,v253,v61\nv_lshlrev_b32 v60,16,v111\nv_mfma_f32_16x16x32_bf16 v[212:215],a[196:199],v[104:107],v[212:215]\nv_and_b32 v61,0xffff0000,v111\n" \
"v_add_f32 v254,v254,v60\nv_mfma_f32_16x16x32_bf16 v[216:219],a[200:203],v[104:107],v[216:219]\ns_waitcnt lgkmcnt(4)\nv_add_f32 v32,v32,v36\n" \
"v_mfma_f32_16x16x32_bf16 v[220:223],a[204:207],v[104:107],v[220:223]\nv_add_f32 v33,v33,v37\nv_add_f32 v34,v34,v38\nv_mfma_f32_16x16x32_bf16 v[224:227],v[144:147],v[72:75],0\n" \
"v_add_f32 v35,v35,v39\nv_add_f32 v40,v40,v44\nv_mfma_f32_16x16x32_bf16 v[228:231],v[148:151],v[72:75],0\nv_add_f32 v41,v41,v45\n" \
"v_add_f32 v42,v42,v46\nv_mfma_f32_16x16x32_bf16 v[232:235],v[152:155],v[72:75],0\nds_write_addtid_b32 v208\nds_write_addtid_b32 v209 offset:256\n" \
"v_mfma_f32_16x16x32_bf16 v[236:239],v[156:159],v[72:75],0\nds_write_addtid_b32 v210 offset:512\nds_write_addtid_b32 v211 offset:768\nv_mfma_f32_16x16x32_bf16 v[224:227],v[176:179],v[76:79],v[224:227]\n" \
"ds_write_addtid_b32 v212 offset:1024\nds_write_addtid_b32 v213 offset:1280\nv_mfma_f32_16x16x32_bf16 v[228:231],v[180:183],v[76:79],v[228:231]\nds_write_addtid_b32 v214 offset:1536\n" \
"ds_write_addtid_b32 v215 offset:1792\nv_mfma_f32_16x16x32_bf16 v[232:235],v[184:187],v[76:79],v[232:235]\nds_write_addtid_b32 v216 offset:2048\nds_write_addtid_b32 v217 offset:2304\n" \
"v_mfma_f32_16x16x32_bf16 v[236:239],v[188:191],v[76:79],v[236:239]\nds_write_addtid_b32 v218 offset:2560\nds_write_addtid_b32 v219 offset:2816\nv_mfma_f32_16x16x32_bf16 v[224:227],a[16:19],v[80:83],v[224:227]\n" \
"ds_write_addtid_b32 v220 offset:3072\nds_write_addtid_b32 v221 offset:3328\nv_mfma_f32_16x16x32_bf16 v[228:231],a[20:23],v[80:83],v[228:231]\nds_write_addtid_b32 v222 offset:3584\n" \
"ds_write_addtid_b32 v223 offset:3840\nv_mfma_f32_16x16x32_bf16 v[232:235],a[24:27],v[80:83],v[232:235]\nv_add_f32 v43,v43,v47\nv_add_f32 v32,v32,v40\n" \
"v_mfma_f32_16x16x32_bf16 v[236:239],a[28:31],v[80:83],v[236:239]\nv_add_f32 v33,v33,v41\nv_add_f32 v34,v34,v42\nv_mfma_f32_16x16x32_bf16 v[224:227],a[48:51],v[84:87],v[224:227]\n" \
"v_add_f32 v255,v255,v61\nv_add_f32 v35,v35,v43\nv_mfma_f32_16x16x32_bf16 v[228:231],a[52:55],v[84:87],v[228:231]\ns_waitcnt vmcnt(0)\n" \
"v_mfma_f32_16x16x32_bf16 v[232:235],a[56:59],v[84:87],v[232:235]\nbuffer_load_dwordx4 v[124:127],v59,s[36:39],s78 offen sc1\nv_cmp_eq_u32 vcc,-1,v112\nv_mfma_f32_16x16x32_bf16 v[236:239],a[60:63],v[84:87],v[236:239]\n" \
"s_waitcnt lgkmcnt(0)\ns_barrier\nv_mfma_f32_16x16x32_bf16 v[224:227],a[80:83],v[88:91],v[224:227]\nds_read_b128 v[16:19],v58\n" \
"ds_read_b128 v[20:23],v58 offset:8192\nv_mfma_f32_16x16x32_bf16 v[228:231],a[84:87],v[88:91],v[228:231]\nds_read_b128 v[24:27],v58 offset:16384\nds_read_b128 v[28:31],v58 offset:24576\n" \
"v_mfma_f32_16x16x32_bf16 v[232:235],a[88:91],v[88:91],v[232:235]\nv_add_f32 v48,v48,v240\ns_cbranch_vccnz .Lsp3A_%=\n.Lbp3A_%=:\n" \
"v_mfma_f32_16x16x32_bf16 v[236:239],a[92:95],v[88:91],v[236:239]\nv_add_f32 v49,v49,v241\nv_lshlrev_b32 v60,16,v112\nv_mfma_f32_16x16x32_bf16 v[224:227],a[112:115],v[92:95],v[224:227]\n" \
"v_add_f32 v50,v50,v242\nv_and_b32 v61,0xffff0000,v112\nv_mfma_f32_16x16x32_bf16 v[228:231],a[116:119],v[92:95],v[228:231]\nv_add_f32 v51,v51,v243\n" \
"v_add_f32 v64,v64,v60\nv_mfma_f32_16x16x32_bf16 v[232:235],a[120:123],v[92:95],v[232:235]\nv_add_f32 v244,v244,v248\nv_add_f32 v65,v65,v61\n" \
"v_mfma_f32_16x16x32_bf16 v[236:239],a[124:127],v[92:95],v[236:239]\nv_add_f32 v245,v245,v249\nv_lshlrev_b32 v60,16,v113\nv_mfma_f32_16x16x32_bf16 v[224:227],a[144:147],v[96:99],v[224:227]\n" \
"v_add_f32 v246,v246,v250\nv_and_b32 v61,0xffff0000,v113\nv_mfma_f32_16x16x32_bf16 v[228:231],a[148:151],v[96:99],v[228:231]\nv_add_f32 v247,v247,v251\n" \
"v_add_f32 v66,v66,v60\nv_mfma_f32_16x16x32_bf16 v[232:235],a[152:155],v[96:99],v[232:235]\nv_add_f32 v48,v48,v244\nv_add_f32 v67,v67,v61\n" \
"v_mfma_f32_16x16x32_bf16 v[236:239],a[156:159],v[96:99],v[236:239]\nv_add_f32 v49,v49,v245\nv_lshlrev_b32 v60,16,v114\nv_add_f32 v50,v50,v246\n" \
"v_mfma_f32_16x16x32_bf16 v[224:227],a[176:179],v[100:103],v[224:227]\nv_and_b32 v61,0xffff0000,v114\nv_add_f32 v51,v51,v247\nv_add_f32 v68,v68,v60\n" \
"v_mfma_f32_16x16x32_bf16 v[228:231],a[180:183],v[100:103],v[228:231]\nv_add_f32 v69,v69,v61\nv_lshlrev_b32 v60,16,v115\nv_and_b32 v61,0xffff0000,v115\n" \
"v_mfma_f32_16x16x32_bf16 v[232:235],a[184:187],v[100:103],v[232:235]\nv_add_f32 v70,v70,v60\nds_read_b128 v[8:11],v62\nv_mfma_f32_16x16x32_bf16 v[236:239],a[188:191],v[100:103],v[236:239]\n" \
"v_add_f32 v71,v71,v61\nds_read_b128 v[12:15],v62 offset:64\nv_mfma_f32_16x16x32_bf16 v[224:227],a[208:211],v[104:107],v[224:227]\nv_lshlrev_b32 v0,16,v116\n" \
"v_and_b32 v1,0xffff0000,v116\nv_mfma_f32_16x16x32_bf16 v[228:231],a[212:215],v[104:107],v[228:231]\nv_lshlrev_b32 v2,16,v117\nv_and_b32 v3,0xffff0000,v117\n" \
"v_mfma_f32_16x16x32_bf16 v[232:235],a[216:219],v[104:107],v[232:235]\nv_lshlrev_b32 v4,16,v120\nv_and_b32 v5,0xffff0000,v120\nv_mfma_f32_16x16x32_bf16 v[236:239],a[220:223],v[104:107],v[236:239]\n" \
"v_lshlrev_b32 v6,16,v121\nv_and_b32 v7,0xffff0000,v121\ns_waitcnt lgkmcnt(0)\nv_fma_f32 v0,v8,v64,v0\n" \
"v_fma_f32 v1,v9,v65,v1\nv_fma_f32 v2,v10,v66,v2\nv_fma_f32 v3,v11,v67,v3\nv_fma_f32 v4,v8,v68,v4\n" \
"v_fma_f32 v5,v9,v69,v5\nv_fma_f32 v6,v10,v70,v6\nv_fma_f32 v7,v11,v71,v7\nv_fma_f32 v0,v8,v32,v0\n" \
"v_fma_f32 v1,v9,v33,v1\nv_fma_f32 v2,v10,v34,v2\nv_fma_f32 v3,v11,v35,v3\nv_cvt_pk_bf16_f32 v0,v0,v1\n" \
"v_cvt_pk_bf16_f32 v1,v2,v3\nbuffer_store_dwordx2 v[0:1],v56,s[44:47],0 offen sc1\nv_fma_f32 v4,v8,v48,v4\nv_fma_f32 v5,v9,v49,v5\n" \
"v_fma_f32 v6,v10,v50,v6\nv_fma_f32 v7,v11,v51,v7\nv_cvt_pk_bf16_f32 v4,v4,v5\nv_cvt_pk_bf16_f32 v5,v6,v7\n" \
"buffer_store_dwordx2 v[4:5],v56,s[44:47],s35 offen sc1\nv_lshlrev_b32 v64,16,v118\nv_and_b32 v65,0xffff0000,v118\nv_lshlrev_b32 v66,16,v119\n" \
"v_and_b32 v67,0xffff0000,v119\nds_write_addtid_b32 v224 offset:4096\nds_write_addtid_b32 v225 offset:4352\nds_write_addtid_b32 v226 offset:4608\n" \
"ds_write_addtid_b32 v227 offset:4864\nds_write_addtid_b32 v228 offset:5120\nds_write_addtid_b32 v229 offset:5376\nds_write_addtid_b32 v230 offset:5632\n" \
"ds_write_addtid_b32 v231 offset:5888\nds_write_addtid_b32 v232 offset:6144\nds_write_addtid_b32 v233 offset:6400\nds_write_addtid_b32 v234 offset:6656\n" \
"ds_write_addtid_b32 v235 offset:6912\nds_write_addtid_b32 v236 offset:7168\nds_write_addtid_b32 v237 offset:7424\nds_write_addtid_b32 v238 offset:7680\n" \
"ds_write_addtid_b32 v239 offset:7936\nv_lshlrev_b32 v68,16,v122\nv_and_b32 v69,0xffff0000,v122\nv_lshlrev_b32 v70,16,v123\n" \
"v_and_b32 v71,0xffff0000,v123\ns_waitcnt lgkmcnt(15)\nv_pk_add_f32 v[16:17],v[16:17],v[20:21]\nv_pk_add_f32 v[18:19],v[18:19],v[22:23]\n" \
"v_pk_add_f32 v[24:25],v[24:25],v[28:29]\nv_pk_add_f32 v[26:27],v[26:27],v[30:31]\nv_pk_add_f32 v[16:17],v[16:17],v[24:25]\nv_pk_add_f32 v[18:19],v[18:19],v[26:27]\n" \
"s_waitcnt lgkmcnt(0)\ns_barrier\nds_read_b128 v[32:35],v58 offset:4096\nds_read_b128 v[36:39],v58 offset:12288\n" \
"ds_read_b128 v[40:43],v58 offset:20480\nds_read_b128 v[44:47],v58 offset:28672\ns_waitcnt vmcnt(2)\nv_cmp_eq_u32 vcc,-1,v124\n" \
"s_cbranch_vccnz .Lsp3B_%=\n.Lbp3B_%=:\nv_lshlrev_b32 v20,16,v124\nv_and_b32 v21,0xffff0000,v124\n" \
"v_lshlrev_b32 v22,16,v125\nv_and_b32 v23,0xffff0000,v125\nv_lshlrev_b32 v24,16,v126\nv_and_b32 v25,0xffff0000,v126\n" \
"v_lshlrev_b32 v26,16,v127\nv_and_b32 v27,0xffff0000,v127\nv_pk_add_f32 v[52:53],v[52:53],v[20:21]\nv_pk_add_f32 v[54:55],v[54:55],v[22:23]\n" \
"v_pk_add_f32 v[252:253],v[252:253],v[24:25]\nv_pk_add_f32 v[254:255],v[254:255],v[26:27]\nv_pk_fma_f32 v[64:65],v[12:13],v[52:53],v[64:65]\nv_pk_fma_f32 v[66:67],v[14:15],v[54:55],v[66:67]\n" \
"v_pk_fma_f32 v[68:69],v[12:13],v[252:253],v[68:69]\nv_pk_fma_f32 v[70:71],v[14:15],v[254:255],v[70:71]\nv_pk_fma_f32 v[64:65],v[12:13],v[16:17],v[64:65]\nv_pk_fma_f32 v[66:67],v[14:15],v[18:19],v[66:67]\n" \
"v_cvt_pk_bf16_f32 v64,v64,v65\nv_cvt_pk_bf16_f32 v65,v66,v67\nbuffer_store_dwordx2 v[64:65],v56,s[44:47],0 offen offset:32 sc1\ns_waitcnt lgkmcnt(0)\n" \
"v_pk_add_f32 v[32:33],v[32:33],v[36:37]\nv_pk_add_f32 v[34:35],v[34:35],v[38:39]\nv_pk_add_f32 v[40:41],v[40:41],v[44:45]\nv_pk_add_f32 v[42:43],v[42:43],v[46:47]\n" \
"v_pk_add_f32 v[32:33],v[32:33],v[40:41]\nv_pk_add_f32 v[34:35],v[34:35],v[42:43]\nv_pk_fma_f32 v[68:69],v[12:13],v[32:33],v[68:69]\nv_pk_fma_f32 v[70:71],v[14:15],v[34:35],v[70:71]\n" \
"v_cvt_pk_bf16_f32 v68,v68,v69\nv_cvt_pk_bf16_f32 v69,v70,v71\nbuffer_store_dwordx2 v[68:69],v56,s[44:47],s35 offen offset:32 sc1\ns_branch .Lend_%=\n" \
".LsaF_%=:\ns_memrealtime s[94:95]\ns_waitcnt lgkmcnt(0)\n.LpaF_%=:\n" \
"s_sleep 2\nbuffer_load_dword v63,v61,s[36:39],s59 offen sc1\ns_memrealtime s[96:97]\ns_waitcnt vmcnt(0) lgkmcnt(0)\n" \
"s_sub_u32 s96,s96,s94\ns_cmp_gt_u32 s96,0x4c4b40\ns_cbranch_scc1 .LbaF_%=\nv_cmp_eq_u32 vcc,0,v63\n" \
"s_and_b64 vcc,vcc,s[86:87]\ns_cbranch_scc1 .LpaF_%=\ns_branch .LbaF_%=\n.LsbF_%=:\n" \
"s_memrealtime s[94:95]\ns_waitcnt lgkmcnt(0)\n.LpbF_%=:\ns_sleep 2\n" \
"buffer_load_dword v63,v61,s[36:39],s59 offen sc1\ns_memrealtime s[96:97]\ns_waitcnt vmcnt(0) lgkmcnt(0)\ns_sub_u32 s96,s96,s94\n" \
"s_cmp_gt_u32 s96,0x4c4b40\ns_cbranch_scc1 .LbbF_%=\nv_cmp_eq_u32 vcc,0,v63\ns_and_b64 vcc,vcc,s[86:87]\n" \
"s_cbranch_scc1 .LpbF_%=\ns_branch .LbbF_%=\n.LsaS_%=:\ns_memrealtime s[94:95]\n" \
"s_waitcnt lgkmcnt(0)\n.LpaS_%=:\ns_sleep 2\nbuffer_load_dword v63,v61,s[36:39],s59 offen sc1\n" \
"s_memrealtime s[96:97]\ns_waitcnt vmcnt(0) lgkmcnt(0)\ns_sub_u32 s96,s96,s94\ns_cmp_gt_u32 s96,0x4c4b40\n" \
"s_cbranch_scc1 .LbaS_%=\nv_cmp_eq_u32 vcc,0,v63\ns_and_b64 vcc,vcc,s[86:87]\ns_cbranch_scc1 .LpaS_%=\n" \
"s_branch .LbaS_%=\n.LsbS_%=:\ns_memrealtime s[94:95]\ns_waitcnt lgkmcnt(0)\n" \
".LpbS_%=:\ns_sleep 2\nbuffer_load_dword v63,v61,s[36:39],s59 offen sc1\ns_memrealtime s[96:97]\n" \
"s_waitcnt vmcnt(0) lgkmcnt(0)\ns_sub_u32 s96,s96,s94\ns_cmp_gt_u32 s96,0x4c4b40\ns_cbranch_scc1 .LbbS_%=\n" \
"v_cmp_eq_u32 vcc,0,v63\ns_and_b64 vcc,vcc,s[86:87]\ns_cbranch_scc1 .LpbS_%=\ns_branch .LbbS_%=\n" \
".Lsp3B_%=:\ns_memrealtime s[94:95]\ns_waitcnt lgkmcnt(0)\n.Lpp3B_%=:\n" \
"s_sub_u32 s101,s78,0\nbuffer_load_dwordx4 v[124:127],v59,s[36:39],s101 offen sc1\ns_memrealtime s[96:97]\ns_waitcnt vmcnt(0) lgkmcnt(0)\n" \
"s_sub_u32 s96,s96,s94\ns_cmp_gt_u32 s96,0x4c4b40\ns_cbranch_scc1 .Lbp3B_%=\nv_cmp_eq_u32 vcc,-1,v124\n" \
"s_cbranch_vccnz .Lpp3B_%=\ns_branch .Lbp3B_%=\n.Lsg4_%=:\ns_memrealtime s[94:95]\n" \
"s_waitcnt lgkmcnt(0)\n.Lpg4_%=:\ns_sleep 2\nbuffer_load_dword v63,v61,s[36:39],s59 offen sc1\n" \
"s_memrealtime s[96:97]\ns_waitcnt vmcnt(0) lgkmcnt(0)\ns_sub_u32 s96,s96,s94\ns_cmp_gt_u32 s96,0x4c4b40\n" \
"s_cbranch_scc1 .Lbg4_%=\nv_cmp_eq_u32 vcc,0,v63\ns_and_b64 vcc,vcc,s[86:87]\ns_cbranch_scc1 .Lpg4_%=\n" \
"s_branch .Lbg4_%=\n.Lse_%=:\ns_memrealtime s[94:95]\ns_waitcnt lgkmcnt(0)\n" \
".Lpe_%=:\nbuffer_load_dword v64,v69,s[36:39],s79 offen sc1\nbuffer_load_dword v65,v69,s[36:39],s80 offen sc1\nbuffer_load_dword v66,v69,s[36:39],s81 offen sc1\n" \
"buffer_load_dword v67,v69,s[36:39],s82 offen sc1\ns_memrealtime s[96:97]\ns_waitcnt vmcnt(0) lgkmcnt(0)\ns_sub_u32 s96,s96,s94\n" \
"s_cmp_gt_u32 s96,0x4c4b40\ns_cbranch_scc1 .Lbe_%=\nv_max3_u32 v52,v64,v65,v66\nv_max_u32 v52,v52,v67\n" \
"v_cmp_eq_u32 vcc,-1,v52\ns_cbranch_vccnz .Lpe_%=\ns_branch .Lbe_%=\n.Lsp1_%=:\n" \
"s_memrealtime s[94:95]\ns_waitcnt lgkmcnt(0)\n.Lpp1_%=:\ns_sub_u32 s101,s71,409600\n" \
"buffer_load_dwordx4 v[64:67],v59,s[36:39],s101 offen sc1\ns_sub_u32 s101,s76,409600\nbuffer_load_dwordx4 v[52:55],v59,s[36:39],s101 offen sc1\ns_memrealtime s[96:97]\n" \
"s_waitcnt vmcnt(0) lgkmcnt(0)\ns_sub_u32 s96,s96,s94\ns_cmp_gt_u32 s96,0x4c4b40\ns_cbranch_scc1 .Lbp1_%=\n" \
"v_max_u32 v62,v64,v52\nv_cmp_eq_u32 vcc,-1,v62\ns_cbranch_vccnz .Lpp1_%=\ns_branch .Lbp1_%=\n" \
".Lsp2A_%=:\ns_memrealtime s[94:95]\ns_waitcnt lgkmcnt(0)\n.Lpp2A_%=:\n" \
"s_sub_u32 s101,s72,409600\nbuffer_load_dwordx4 v[60:63],v59,s[36:39],s101 offen sc1\ns_memrealtime s[96:97]\ns_waitcnt vmcnt(0) lgkmcnt(0)\n" \
"s_sub_u32 s96,s96,s94\ns_cmp_gt_u32 s96,0x4c4b40\ns_cbranch_scc1 .Lbp2A_%=\nv_cmp_eq_u32 vcc,-1,v60\n" \
"s_cbranch_vccnz .Lpp2A_%=\ns_branch .Lbp2A_%=\n.Lsp2B_%=:\ns_memrealtime s[94:95]\n" \
"s_waitcnt lgkmcnt(0)\n.Lpp2B_%=:\ns_sub_u32 s101,s77,409600\nbuffer_load_dwordx4 v[108:111],v59,s[36:39],s101 offen sc1\n" \
"s_memrealtime s[96:97]\ns_waitcnt vmcnt(0) lgkmcnt(0)\ns_sub_u32 s96,s96,s94\ns_cmp_gt_u32 s96,0x4c4b40\n" \
"s_cbranch_scc1 .Lbp2B_%=\nv_cmp_eq_u32 vcc,-1,v108\ns_cbranch_vccnz .Lpp2B_%=\ns_branch .Lbp2B_%=\n" \
".Lsp3A_%=:\ns_memrealtime s[94:95]\ns_waitcnt lgkmcnt(0)\n.Lpp3A_%=:\n" \
"s_sub_u32 s101,s73,0\nbuffer_load_dwordx4 v[112:115],v59,s[36:39],s101 offen sc1\ns_memrealtime s[96:97]\ns_waitcnt vmcnt(0) lgkmcnt(0)\n" \
"s_sub_u32 s96,s96,s94\ns_cmp_gt_u32 s96,0x4c4b40\ns_cbranch_scc1 .Lbp3A_%=\nv_cmp_eq_u32 vcc,-1,v112\n" \
"s_cbranch_vccnz .Lpp3A_%=\ns_branch .Lbp3A_%=\n.Lz0_%=:\ns_branch .Lz1_%=\n" \
".Lxq5_%=:\ns_mov_b64 exec,1\nbuffer_store_dword v61,off,s[36:39],s54 sc1\ns_mov_b64 exec,-1\n" \
"buffer_load_dword v63,v61,s[36:39],s75 offen sc1\ns_mov_b32 s75,s59\ns_add_u32 s54,s54,0x10000\ns_add_u32 s83,s83,819200\n" \
"s_branch .Lyq5_%=\n.Lz1_%=:\ns_branch .Lz2_%=\n.Lxt5_%=:\n" \
"v_and_b32 v20,0x300,v59\nv_and_b32 v21,0xf0,v59\nv_lshlrev_b32 v20,4,v20\ns_mov_b32 m0,162176\n" \
"v_lshl_or_b32 v20,v21,3,v20\nbuffer_load_dword v20,s[40:43],s101 offen lds\ns_mov_b32 m0,s56\ns_branch .Lyt5_%=\n" \
".Lz2_%=:\ns_branch .Lz3_%=\n.Lxq4_%=:\ns_mov_b64 exec,1\n" \
"buffer_store_dword v61,off,s[36:39],s54 sc1\ns_mov_b64 exec,-1\nbuffer_load_dword v63,v61,s[36:39],s75 offen sc1\ns_mov_b32 s75,s59\n" \
"s_add_u32 s54,s54,0x10000\ns_add_u32 s83,s83,819200\ns_branch .Lyq4_%=\n.Lz3_%=:\n" \
"s_branch .Lz4_%=\n.Lxt4_%=:\nv_and_b32 v0,0x300,v59\nv_and_b32 v1,0xf0,v59\n" \
"v_lshlrev_b32 v0,4,v0\ns_mov_b32 m0,162176\nv_lshl_or_b32 v0,v1,3,v0\nbuffer_load_dword v0,s[40:43],s101 offen lds\n" \
"s_mov_b32 m0,s56\ns_branch .Lyt4_%=\n.Lz4_%=:\ns_branch .Lzl_%=\n" \
".LsmP_%=:\ns_sleep 2\nbuffer_load_dwordx4 v[48:51],v2,s[36:39],s73 offen sc1\nbuffer_load_dwordx4 v[60:63],v2,s[36:39],s75 offen sc1\n" \
"buffer_load_dwordx4 v[52:55],v2,s[36:39],s74 offen sc1\nbuffer_load_dwordx4 v[64:67],v2,s[36:39],s76 offen sc1\nbuffer_load_dwordx4 v[56:59],v1,s[36:39],s77 offen sc1\nbuffer_load_dwordx4 v[68:71],v1,s[36:39],s78 offen sc1\n" \
"s_memrealtime s[96:97]\ns_waitcnt vmcnt(0) lgkmcnt(0)\ns_sub_u32 s96,s96,s30\ns_cmp_gt_u32 s96,0x4c4b40\n" \
"s_cbranch_scc1 .LbmP_%=\nv_cmp_ne_u64 vcc,s[28:29],v[48:49]\nv_cmp_ne_u64 s[84:85],s[28:29],v[52:53]\nv_cmp_ne_u64 s[82:83],s[28:29],v[56:57]\n" \
"s_or_b64 vcc,vcc,s[84:85]\nv_cmp_ne_u64 s[84:85],s[28:29],v[60:61]\ns_or_b64 vcc,vcc,s[82:83]\nv_cmp_ne_u64 s[82:83],s[28:29],v[64:65]\n" \
"s_or_b64 vcc,vcc,s[84:85]\nv_cmp_ne_u64 s[84:85],s[28:29],v[68:69]\ns_or_b64 vcc,vcc,s[82:83]\ns_or_b64 vcc,vcc,s[84:85]\n" \
"s_cbranch_vccnz .LsmP_%=\ns_branch .LbmP_%=\n.LsxP_%=:\ns_waitcnt lgkmcnt(0)\n" \
"v_mul_f32 v74,0.5,v74\nv_pk_add_f32 v[124:125],v[54:55],v[74:75] op_sel_hi:[1,0]\nv_pk_add_f32 v[126:127],v[58:59],1.0 op_sel_hi:[1,0]\nv_max3_f32 v112,|v50|,|v51|,|v124|\n" \
"v_max3_f32 v112,|v125|,|v126|,v112\nv_max_f32 v112,|v127|,v112\ns_nop 1\nv_max_u32_dpp v112,v112,v112 quad_perm:[1,0,3,2] row_mask:0xf bank_mask:0xf bound_ctrl:1\n" \
"s_nop 1\nv_max_u32_dpp v112,v112,v112 quad_perm:[2,3,0,1] row_mask:0xf bank_mask:0xf bound_ctrl:1\ns_nop 1\nv_max_u32_dpp v112,v112,v112 row_half_mirror row_mask:0xf bank_mask:0xf bound_ctrl:1\n" \
"s_nop 1\nv_max_u32_dpp v112,v112,v112 row_mirror row_mask:0xf bank_mask:0xf bound_ctrl:1\ns_nop 0\nv_readlane_b32 s82,v112,0\n" \
"v_readlane_b32 s100,v112,16\nv_readlane_b32 s101,v112,32\nv_readlane_b32 s31,v112,48\ns_max_u32 s100,s100,s101\n" \
"s_max_u32 s82,s82,s31\ns_max_u32 s82,s82,s100\nv_mov_b32 v113,s82\nds_write_b32 v251,v113 offset:16\n" \
"s_waitcnt lgkmcnt(0)\ns_barrier\nds_read_b128 v[124:127],v114 offset:16\ns_waitcnt lgkmcnt(0)\n" \
"v_max3_u32 v124,v124,v125,v126\nv_max_u32 v124,v124,v127\ns_nop 0\nv_readfirstlane_b32 s93,v124\n" \
"s_nop 0\ns_lshr_b32 s93,s93,23\ns_max_u32 s93,s93,64\ns_min_u32 s93,s93,254\n" \
"s_sub_u32 s82,256,s93\ns_lshl_b32 s82,s82,23\nv_mov_b32 v124,s82\nv_mul_f32 v74,v74,v124\n" \
"v_pk_mul_f32 v[62:63],v[50:51],v[124:125] op_sel_hi:[1,0]\nv_max3_f32 v112,|v62|,|v63|,0\nv_cvt_pk_f16_f32 v62,v62,v63\nds_write_b32 v252,v62 offset:62848\n" \
"v_pk_fma_f32 v[66:67],v[54:55],v[124:125],v[74:75] op_sel_hi:[1,0,0]\nv_max3_f32 v112,|v66|,|v67|,v112\nv_cvt_pk_f16_f32 v66,v66,v67\nds_write_b32 v253,v66 offset:62848\n" \
"v_pk_fma_f32 v[70:71],v[58:59],v[124:125],v[124:125] op_sel_hi:[1,0,0]\nv_max3_f32 v112,|v70|,|v71|,v112\nv_cvt_pk_f16_f32 v70,v70,v71\nds_write_b32 v254,v70 offset:62848\n" \
"s_waitcnt lgkmcnt(0)\ns_barrier\nds_read_b64 v[96:97],v234 offset:62848\nds_read_b64 v[98:99],v235 offset:62848\n" \
"ds_read_b64 v[100:101],v236 offset:62848\nds_read_b64 v[102:103],v237 offset:62848\nds_read_b64 v[104:105],v238 offset:62848\nds_read_b64 v[106:107],v239 offset:62848\n" \
"ds_read_b64 v[108:109],v240 offset:62848\nds_read_b64 v[110:111],v241 offset:62848\ns_waitcnt lgkmcnt(0)\ns_branch .LbxP_%=\n" \
".LsrP_%=:\ns_sleep 2\nbuffer_load_dwordx4 v[104:107],v1,s[36:39],s7 offen sc1\nbuffer_load_dwordx4 v[108:111],v1,s[36:39],s7 offen offset:1024 sc1\n" \
"s_memrealtime s[96:97]\ns_waitcnt vmcnt(0) lgkmcnt(0)\ns_sub_u32 s96,s96,s30\ns_cmp_gt_u32 s96,0x4c4b40\n" \
"s_cbranch_scc1 .LbrP_%=\nv_xor_b32 v112,s29,v105\nv_xor_b32 v113,s29,v107\nv_xor_b32 v114,s29,v109\n" \
"v_xor_b32 v115,s29,v111\nv_or3_b32 v112,v112,v113,v114\nv_xor_b32 v113,s28,v104\nv_xor_b32 v114,s28,v106\n" \
"v_or_b32 v112,v112,v115\nv_xor_b32 v115,s28,v108\nv_lshl_or_b32 v112,v112,8,v113\nv_xor_b32 v113,s28,v110\n" \
"v_or3_b32 v112,v112,v114,v115\nv_or_b32 v112,v112,v113\nv_cmp_ne_u32 vcc,0,v112\ns_nop 0\n" \
"s_cbranch_vccnz .LsrP_%=\ns_branch .LbrP_%=\n.LsnP_%=:\ns_sleep 2\n" \
"buffer_load_dwordx4 v[80:83],v120,s[36:39],s5 offen sc1\nbuffer_load_dwordx4 v[84:87],v120,s[36:39],s5 offen offset:2304 sc1\nbuffer_load_dwordx4 v[88:91],v120,s[36:39],s6 offen sc1\nbuffer_load_dwordx4 v[92:95],v120,s[36:39],s6 offen offset:2304 sc1\n" \
"s_memrealtime s[96:97]\ns_waitcnt vmcnt(0) lgkmcnt(0)\ns_sub_u32 s96,s96,s30\ns_cmp_gt_u32 s96,0x4c4b40\n" \
"s_cbranch_scc1 .LbnP_%=\nv_cmp_ne_u64 vcc,s[28:29],v[80:81]\nv_cmp_ne_u64 s[24:25],s[28:29],v[84:85]\nv_cmp_ne_u64 s[26:27],s[28:29],v[88:89]\n" \
"s_or_b64 vcc,vcc,s[24:25]\nv_cmp_ne_u64 s[24:25],s[28:29],v[92:93]\ns_or_b64 vcc,vcc,s[26:27]\ns_or_b64 vcc,vcc,s[24:25]\n" \
"s_cbranch_vccnz .LsnP_%=\ns_branch .LbnP_%=\n.LsuP_%=:\nv_and_b32 v125,1,v112\n" \
"v_cmpx_eq_u32 vcc,0,v125\nbuffer_load_dwordx4 v[112:115],v3,s[36:39],s16 offen sc1\ns_mov_b64 exec,-1\nv_and_b32 v125,1,v116\n" \
"v_cmpx_eq_u32 vcc,0,v125\nbuffer_load_dwordx4 v[116:119],v3,s[36:39],s17 offen sc1\ns_mov_b64 exec,-1\nv_and_b32 v125,1,v120\n" \
"v_cmpx_eq_u32 vcc,0,v125\nbuffer_load_dwordx4 v[120:123],v124,s[36:39],s18 offen sc1\ns_mov_b64 exec,-1\ns_memrealtime s[96:97]\n" \
"s_waitcnt vmcnt(0) lgkmcnt(0)\ns_sub_u32 s96,s96,s30\ns_cmp_gt_u32 s96,0x4c4b40\ns_cbranch_scc1 .LbuP_%=\n" \
"v_and_b32 v125,v112,v116\nv_bitop3_b32 v125,v125,v120,1 bitop3:0x80\nv_cmp_eq_u32 vcc,0,v125\ns_nop 0\n" \
"s_cbranch_vccnz .LsuP_%=\ns_branch .LbuP_%=\n.LsmN_%=:\ns_sleep 2\n" \
"buffer_load_dwordx4 v[48:51],v2,s[36:39],s73 offen sc1\nbuffer_load_dwordx4 v[60:63],v2,s[36:39],s75 offen sc1\nbuffer_load_dwordx4 v[52:55],v2,s[36:39],s74 offen sc1\nbuffer_load_dwordx4 v[64:67],v2,s[36:39],s76 offen sc1\n" \
"buffer_load_dwordx4 v[56:59],v1,s[36:39],s77 offen sc1\nbuffer_load_dwordx4 v[68:71],v1,s[36:39],s78 offen sc1\ns_memrealtime s[96:97]\ns_waitcnt vmcnt(0) lgkmcnt(0)\n" \
"s_sub_u32 s96,s96,s30\ns_cmp_gt_u32 s96,0x4c4b40\ns_cbranch_scc1 .LbmN_%=\nv_cmp_ne_u64 vcc,s[28:29],v[48:49]\n" \
"v_cmp_ne_u64 s[84:85],s[28:29],v[52:53]\nv_cmp_ne_u64 s[82:83],s[28:29],v[56:57]\ns_or_b64 vcc,vcc,s[84:85]\nv_cmp_ne_u64 s[84:85],s[28:29],v[60:61]\n" \
"s_or_b64 vcc,vcc,s[82:83]\nv_cmp_ne_u64 s[82:83],s[28:29],v[64:65]\ns_or_b64 vcc,vcc,s[84:85]\nv_cmp_ne_u64 s[84:85],s[28:29],v[68:69]\n" \
"s_or_b64 vcc,vcc,s[82:83]\ns_or_b64 vcc,vcc,s[84:85]\ns_cbranch_vccnz .LsmN_%=\ns_branch .LbmN_%=\n" \
".LsxN_%=:\ns_waitcnt lgkmcnt(0)\nv_mul_f32 v74,0.5,v74\nv_pk_add_f32 v[124:125],v[54:55],v[74:75] op_sel_hi:[1,0]\n" \
"v_pk_add_f32 v[126:127],v[58:59],1.0 op_sel_hi:[1,0]\nv_max3_f32 v112,|v50|,|v51|,|v124|\nv_max3_f32 v112,|v125|,|v126|,v112\nv_max_f32 v112,|v127|,v112\n" \
"s_nop 1\nv_max_u32_dpp v112,v112,v112 quad_perm:[1,0,3,2] row_mask:0xf bank_mask:0xf bound_ctrl:1\ns_nop 1\nv_max_u32_dpp v112,v112,v112 quad_perm:[2,3,0,1] row_mask:0xf bank_mask:0xf bound_ctrl:1\n" \
"s_nop 1\nv_max_u32_dpp v112,v112,v112 row_half_mirror row_mask:0xf bank_mask:0xf bound_ctrl:1\ns_nop 1\nv_max_u32_dpp v112,v112,v112 row_mirror row_mask:0xf bank_mask:0xf bound_ctrl:1\n" \
"s_nop 0\nv_readlane_b32 s82,v112,0\nv_readlane_b32 s100,v112,16\nv_readlane_b32 s101,v112,32\n" \
"v_readlane_b32 s31,v112,48\ns_max_u32 s100,s100,s101\ns_max_u32 s82,s82,s31\ns_max_u32 s82,s82,s100\n" \
"v_mov_b32 v113,s82\nds_write_b32 v251,v113 offset:16\ns_waitcnt lgkmcnt(0)\ns_barrier\n" \
"ds_read_b128 v[124:127],v114 offset:16\ns_waitcnt lgkmcnt(0)\nv_max3_u32 v124,v124,v125,v126\nv_max_u32 v124,v124,v127\n" \
"s_nop 0\nv_readfirstlane_b32 s93,v124\ns_nop 0\ns_lshr_b32 s93,s93,23\n" \
"s_max_u32 s93,s93,64\ns_min_u32 s93,s93,254\ns_sub_u32 s82,256,s93\ns_lshl_b32 s82,s82,23\n" \
"v_mov_b32 v124,s82\nv_mul_f32 v74,v74,v124\nv_pk_mul_f32 v[62:63],v[50:51],v[124:125] op_sel_hi:[1,0]\nv_max3_f32 v112,|v62|,|v63|,0\n" \
"v_cvt_pk_f16_f32 v62,v62,v63\nds_write_b32 v252,v62 offset:62848\nv_pk_fma_f32 v[66:67],v[54:55],v[124:125],v[74:75] op_sel_hi:[1,0,0]\nv_max3_f32 v112,|v66|,|v67|,v112\n" \
"v_cvt_pk_f16_f32 v66,v66,v67\nds_write_b32 v253,v66 offset:62848\nv_pk_fma_f32 v[70:71],v[58:59],v[124:125],v[124:125] op_sel_hi:[1,0,0]\nv_max3_f32 v112,|v70|,|v71|,v112\n" \
"v_cvt_pk_f16_f32 v70,v70,v71\nds_write_b32 v254,v70 offset:62848\ns_waitcnt lgkmcnt(0)\ns_barrier\n" \
"ds_read_b64 v[96:97],v234 offset:62848\nds_read_b64 v[98:99],v235 offset:62848\nds_read_b64 v[100:101],v236 offset:62848\nds_read_b64 v[102:103],v237 offset:62848\n" \
"ds_read_b64 v[104:105],v238 offset:62848\nds_read_b64 v[106:107],v239 offset:62848\nds_read_b64 v[108:109],v240 offset:62848\nds_read_b64 v[110:111],v241 offset:62848\n" \
"s_waitcnt lgkmcnt(0)\ns_branch .LbxN_%=\n.LsrN_%=:\ns_sleep 2\n" \
"buffer_load_dwordx4 v[104:107],v1,s[36:39],s7 offen sc1\nbuffer_load_dwordx4 v[108:111],v1,s[36:39],s7 offen offset:1024 sc1\ns_memrealtime s[96:97]\ns_waitcnt vmcnt(0) lgkmcnt(0)\n" \
"s_sub_u32 s96,s96,s30\ns_cmp_gt_u32 s96,0x4c4b40\ns_cbranch_scc1 .LbrN_%=\nv_xor_b32 v112,s29,v105\n" \
"v_xor_b32 v113,s29,v107\nv_xor_b32 v114,s29,v109\nv_xor_b32 v115,s29,v111\nv_or3_b32 v112,v112,v113,v114\n" \
"v_xor_b32 v113,s28,v104\nv_xor_b32 v114,s28,v106\nv_or_b32 v112,v112,v115\nv_xor_b32 v115,s28,v108\n" \
"v_lshl_or_b32 v112,v112,8,v113\nv_xor_b32 v113,s28,v110\nv_or3_b32 v112,v112,v114,v115\nv_or_b32 v112,v112,v113\n" \
"v_cmp_ne_u32 vcc,0,v112\ns_nop 0\ns_cbranch_vccnz .LsrN_%=\ns_branch .LbrN_%=\n" \
".LsnN_%=:\ns_sleep 2\nbuffer_load_dwordx4 v[80:83],v120,s[36:39],s5 offen sc1\nbuffer_load_dwordx4 v[84:87],v120,s[36:39],s5 offen offset:2304 sc1\n" \
"buffer_load_dwordx4 v[88:91],v120,s[36:39],s6 offen sc1\nbuffer_load_dwordx4 v[92:95],v120,s[36:39],s6 offen offset:2304 sc1\ns_memrealtime s[96:97]\ns_waitcnt vmcnt(0) lgkmcnt(0)\n" \
"s_sub_u32 s96,s96,s30\ns_cmp_gt_u32 s96,0x4c4b40\ns_cbranch_scc1 .LbnN_%=\nv_cmp_ne_u64 vcc,s[28:29],v[80:81]\n" \
"v_cmp_ne_u64 s[24:25],s[28:29],v[84:85]\nv_cmp_ne_u64 s[26:27],s[28:29],v[88:89]\ns_or_b64 vcc,vcc,s[24:25]\nv_cmp_ne_u64 s[24:25],s[28:29],v[92:93]\n" \
"s_or_b64 vcc,vcc,s[26:27]\ns_or_b64 vcc,vcc,s[24:25]\ns_cbranch_vccnz .LsnN_%=\ns_branch .LbnN_%=\n" \
".LsuN_%=:\nv_and_b32 v125,1,v112\nv_cmpx_eq_u32 vcc,0,v125\nbuffer_load_dwordx4 v[112:115],v3,s[36:39],s16 offen sc1\n" \
"s_mov_b64 exec,-1\nv_and_b32 v125,1,v116\nv_cmpx_eq_u32 vcc,0,v125\nbuffer_load_dwordx4 v[116:119],v3,s[36:39],s17 offen sc1\n" \
"s_mov_b64 exec,-1\nv_and_b32 v125,1,v120\nv_cmpx_eq_u32 vcc,0,v125\nbuffer_load_dwordx4 v[120:123],v124,s[36:39],s18 offen sc1\n" \
"s_mov_b64 exec,-1\ns_memrealtime s[96:97]\ns_waitcnt vmcnt(0) lgkmcnt(0)\ns_sub_u32 s96,s96,s30\n" \
"s_cmp_gt_u32 s96,0x4c4b40\ns_cbranch_scc1 .LbuN_%=\nv_and_b32 v125,v112,v116\nv_bitop3_b32 v125,v125,v120,1 bitop3:0x80\n" \
"v_cmp_eq_u32 vcc,0,v125\ns_nop 0\ns_cbranch_vccnz .LsuN_%=\ns_branch .LbuN_%=\n" \
".LsmC_%=:\ns_sleep 2\nbuffer_load_dwordx4 v[48:51],v2,s[36:39],s73 offen sc1\nbuffer_load_dwordx4 v[60:63],v2,s[36:39],s75 offen sc1\n" \
"buffer_load_dwordx4 v[52:55],v2,s[36:39],s74 offen sc1\nbuffer_load_dwordx4 v[64:67],v2,s[36:39],s76 offen sc1\nbuffer_load_dwordx4 v[56:59],v1,s[36:39],s77 offen sc1\nbuffer_load_dwordx4 v[68:71],v1,s[36:39],s78 offen sc1\n" \
"s_memrealtime s[96:97]\ns_waitcnt vmcnt(0) lgkmcnt(0)\ns_sub_u32 s96,s96,s30\ns_cmp_gt_u32 s96,0x4c4b40\n" \
"s_cbranch_scc1 .LbmC_%=\nv_cmp_ne_u64 vcc,s[28:29],v[48:49]\nv_cmp_ne_u64 s[84:85],s[28:29],v[52:53]\nv_cmp_ne_u64 s[82:83],s[28:29],v[56:57]\n" \
"s_or_b64 vcc,vcc,s[84:85]\nv_cmp_ne_u64 s[84:85],s[28:29],v[60:61]\ns_or_b64 vcc,vcc,s[82:83]\nv_cmp_ne_u64 s[82:83],s[28:29],v[64:65]\n" \
"s_or_b64 vcc,vcc,s[84:85]\nv_cmp_ne_u64 s[84:85],s[28:29],v[68:69]\ns_or_b64 vcc,vcc,s[82:83]\ns_or_b64 vcc,vcc,s[84:85]\n" \
"s_cbranch_vccnz .LsmC_%=\ns_branch .LbmC_%=\n.LsxC_%=:\ns_waitcnt lgkmcnt(0)\n" \
"v_mul_f32 v74,0.5,v74\nv_pk_add_f32 v[124:125],v[54:55],v[74:75] op_sel_hi:[1,0]\nv_pk_add_f32 v[126:127],v[58:59],1.0 op_sel_hi:[1,0]\nv_max3_f32 v112,|v50|,|v51|,|v124|\n" \
"v_max3_f32 v112,|v125|,|v126|,v112\nv_max_f32 v112,|v127|,v112\ns_nop 1\nv_max_u32_dpp v112,v112,v112 quad_perm:[1,0,3,2] row_mask:0xf bank_mask:0xf bound_ctrl:1\n" \
"s_nop 1\nv_max_u32_dpp v112,v112,v112 quad_perm:[2,3,0,1] row_mask:0xf bank_mask:0xf bound_ctrl:1\ns_nop 1\nv_max_u32_dpp v112,v112,v112 row_half_mirror row_mask:0xf bank_mask:0xf bound_ctrl:1\n" \
"s_nop 1\nv_max_u32_dpp v112,v112,v112 row_mirror row_mask:0xf bank_mask:0xf bound_ctrl:1\ns_nop 0\nv_readlane_b32 s82,v112,0\n" \
"v_readlane_b32 s100,v112,16\nv_readlane_b32 s101,v112,32\nv_readlane_b32 s31,v112,48\ns_max_u32 s100,s100,s101\n" \
"s_max_u32 s82,s82,s31\ns_max_u32 s82,s82,s100\nv_mov_b32 v113,s82\nds_write_b32 v251,v113 offset:16\n" \
"s_waitcnt lgkmcnt(0)\ns_barrier\nds_read_b128 v[124:127],v114 offset:16\ns_waitcnt lgkmcnt(0)\n" \
"v_max3_u32 v124,v124,v125,v126\nv_max_u32 v124,v124,v127\ns_nop 0\nv_readfirstlane_b32 s93,v124\n" \
"s_nop 0\ns_lshr_b32 s93,s93,23\ns_max_u32 s93,s93,64\ns_min_u32 s93,s93,254\n" \
"s_sub_u32 s82,256,s93\ns_lshl_b32 s82,s82,23\nv_mov_b32 v124,s82\nv_mul_f32 v74,v74,v124\n" \
"v_pk_mul_f32 v[62:63],v[50:51],v[124:125] op_sel_hi:[1,0]\nv_max3_f32 v112,|v62|,|v63|,0\nv_cvt_pk_f16_f32 v62,v62,v63\nds_write_b32 v252,v62 offset:62848\n" \
"v_pk_fma_f32 v[66:67],v[54:55],v[124:125],v[74:75] op_sel_hi:[1,0,0]\nv_max3_f32 v112,|v66|,|v67|,v112\nv_cvt_pk_f16_f32 v66,v66,v67\nds_write_b32 v253,v66 offset:62848\n" \
"v_pk_fma_f32 v[70:71],v[58:59],v[124:125],v[124:125] op_sel_hi:[1,0,0]\nv_max3_f32 v112,|v70|,|v71|,v112\nv_cvt_pk_f16_f32 v70,v70,v71\nds_write_b32 v254,v70 offset:62848\n" \
"s_waitcnt lgkmcnt(0)\ns_barrier\nds_read_b64 v[96:97],v234 offset:62848\nds_read_b64 v[98:99],v235 offset:62848\n" \
"ds_read_b64 v[100:101],v236 offset:62848\nds_read_b64 v[102:103],v237 offset:62848\nds_read_b64 v[104:105],v238 offset:62848\nds_read_b64 v[106:107],v239 offset:62848\n" \
"ds_read_b64 v[108:109],v240 offset:62848\nds_read_b64 v[110:111],v241 offset:62848\ns_waitcnt lgkmcnt(0)\ns_branch .LbxC_%=\n" \
".Lsw_%=:\nbuffer_store_dwordx4 v[80:83],v3,s[36:39],s73 offen sc1\nbuffer_store_dwordx4 v[84:87],v3,s[36:39],s74 offen sc1\nbuffer_store_dwordx4 v[88:91],v113,s[36:39],s75 offen sc1\n" \
".Lpw_%=:\nbuffer_load_dwordx4 v[104:107],v1,s[36:39],s7 offen sc1\nbuffer_load_dwordx4 v[108:111],v1,s[36:39],s7 offen offset:1024 sc1\ns_memrealtime s[96:97]\n" \
"s_waitcnt vmcnt(0) lgkmcnt(0)\ns_sub_u32 s96,s96,s30\ns_cmp_gt_u32 s96,0x4c4b40\ns_cbranch_scc1 .Lqw_%=\n" \
"v_xor_b32 v116,s29,v105\nv_xor_b32 v117,s29,v107\nv_xor_b32 v118,s29,v109\nv_xor_b32 v119,s29,v111\n" \
"v_or3_b32 v116,v116,v117,v118\nv_xor_b32 v117,s28,v104\nv_xor_b32 v118,s28,v106\nv_or_b32 v116,v116,v119\n" \
"v_xor_b32 v119,s28,v108\nv_lshl_or_b32 v116,v116,8,v117\nv_xor_b32 v117,s28,v110\nv_or3_b32 v116,v116,v118,v119\n" \
"v_or_b32 v116,v116,v117\nv_cmp_ne_u32 vcc,0,v116\ns_nop 0\ns_cbranch_vccnz .Lpw_%=\n" \
".Lqw_%=:\ns_add_u32 m0,s88,16384\ns_nop 0\nbuffer_load_dwordx4 v124,s[36:39],s66 offen sc1 lds\n" \
"s_add_u32 m0,s88,20480\ns_nop 0\nbuffer_load_dwordx4 v125,s[36:39],s67 offen sc1 lds\ns_add_u32 m0,s88,24576\n" \
"s_nop 0\nbuffer_load_dwordx4 v124,s[36:39],s68 offen sc1 lds\ns_add_u32 m0,s88,28672\ns_nop 0\n" \
"buffer_load_dwordx4 v125,s[36:39],s69 offen sc1 lds\ns_waitcnt vmcnt(0)\ns_branch .Lbw_%=\n.Lsz_%=:\n" \
"v_subrev_u32 v124,16384,v80\nv_lshrrev_b32 v124,6,v124\nv_mul_u32_u24 v125,9344,v124\nv_lshl_add_u32 v124,v84,2,v125\n" \
"v_sub_u32 v125,2382780,v124\ns_mov_b32 s79,2925632\nbuffer_load_dword v64,v124,s[36:39],s79 offen sc1\nv_subrev_u32 v126,0,v125\n" \
"s_mov_b32 s79,2925632\nbuffer_load_dword v72,v126,s[36:39],s79 offen sc1\ns_mov_b32 s79,3075136\nbuffer_load_dword v65,v124,s[36:39],s79 offen sc1\n" \
"v_subrev_u32 v126,149504,v125\ns_mov_b32 s79,2925632\nbuffer_load_dword v73,v126,s[36:39],s79 offen sc1\ns_mov_b32 s79,3224640\n" \
"buffer_load_dword v66,v124,s[36:39],s79 offen sc1\nv_subrev_u32 v126,299008,v125\ns_mov_b32 s79,2925632\nbuffer_load_dword v74,v126,s[36:39],s79 offen sc1\n" \
"s_mov_b32 s79,3374144\nbuffer_load_dword v67,v124,s[36:39],s79 offen sc1\nv_subrev_u32 v126,448512,v125\ns_mov_b32 s79,2925632\n" \
"buffer_load_dword v75,v126,s[36:39],s79 offen sc1\ns_mov_b32 s79,2944320\nbuffer_load_dword v68,v124,s[36:39],s79 offen sc1\nv_subrev_u32 v126,18688,v125\n" \
"s_mov_b32 s79,2925632\nbuffer_load_dword v76,v126,s[36:39],s79 offen sc1\ns_mov_b32 s79,3093824\nbuffer_load_dword v69,v124,s[36:39],s79 offen sc1\n" \
"v_subrev_u32 v126,168192,v125\ns_mov_b32 s79,2925632\nbuffer_load_dword v77,v126,s[36:39],s79 offen sc1\ns_mov_b32 s79,3243328\n" \
"buffer_load_dword v70,v124,s[36:39],s79 offen sc1\nv_subrev_u32 v126,317696,v125\ns_mov_b32 s79,2925632\nbuffer_load_dword v78,v126,s[36:39],s79 offen sc1\n" \
"s_mov_b32 s79,3392832\nbuffer_load_dword v71,v124,s[36:39],s79 offen sc1\nv_subrev_u32 v126,467200,v125\ns_mov_b32 s79,2925632\n" \
"buffer_load_dword v79,v126,s[36:39],s79 offen sc1\ns_waitcnt vmcnt(0)\nv_lshlrev_b32 v124,16,v72\nv_and_b32 v125,0xffff0000,v72\n" \
"v_add_f32 v16,v16,v124\nv_sub_f32 v20,v20,v125\nv_lshlrev_b32 v124,16,v64\nv_and_b32 v125,0xffff0000,v64\n" \
"v_add_f32 v32,v32,v124\nv_sub_f32 v36,v36,v125\nv_lshlrev_b32 v124,16,v73\nv_and_b32 v125,0xffff0000,v73\n" \
"v_add_f32 v17,v17,v124\nv_sub_f32 v21,v21,v125\nv_lshlrev_b32 v124,16,v65\nv_and_b32 v125,0xffff0000,v65\n" \
"v_add_f32 v33,v33,v124\nv_sub_f32 v37,v37,v125\nv_lshlrev_b32 v124,16,v74\nv_and_b32 v125,0xffff0000,v74\n" \
"v_add_f32 v18,v18,v124\nv_sub_f32 v22,v22,v125\nv_lshlrev_b32 v124,16,v66\nv_and_b32 v125,0xffff0000,v66\n" \
"v_add_f32 v34,v34,v124\nv_sub_f32 v38,v38,v125\nv_lshlrev_b32 v124,16,v75\nv_and_b32 v125,0xffff0000,v75\n" \
"v_add_f32 v19,v19,v124\nv_sub_f32 v23,v23,v125\nv_lshlrev_b32 v124,16,v67\nv_and_b32 v125,0xffff0000,v67\n" \
"v_add_f32 v35,v35,v124\nv_sub_f32 v39,v39,v125\nv_lshlrev_b32 v124,16,v76\nv_and_b32 v125,0xffff0000,v76\n" \
"v_add_f32 v40,v40,v124\nv_sub_f32 v44,v44,v125\nv_lshlrev_b32 v124,16,v68\nv_and_b32 v125,0xffff0000,v68\n" \
"v_add_f32 v56,v56,v124\nv_sub_f32 v60,v60,v125\nv_lshlrev_b32 v124,16,v77\nv_and_b32 v125,0xffff0000,v77\n" \
"v_add_f32 v41,v41,v124\nv_sub_f32 v45,v45,v125\nv_lshlrev_b32 v124,16,v69\nv_and_b32 v125,0xffff0000,v69\n" \
"v_add_f32 v57,v57,v124\nv_sub_f32 v61,v61,v125\nv_lshlrev_b32 v124,16,v78\nv_and_b32 v125,0xffff0000,v78\n" \
"v_add_f32 v42,v42,v124\nv_sub_f32 v46,v46,v125\nv_lshlrev_b32 v124,16,v70\nv_and_b32 v125,0xffff0000,v70\n" \
"v_add_f32 v58,v58,v124\nv_sub_f32 v62,v62,v125\nv_lshlrev_b32 v124,16,v79\nv_and_b32 v125,0xffff0000,v79\n" \
"v_add_f32 v43,v43,v124\nv_sub_f32 v47,v47,v125\nv_lshlrev_b32 v124,16,v71\nv_and_b32 v125,0xffff0000,v71\n" \
"v_add_f32 v59,v59,v124\nv_sub_f32 v63,v63,v125\ns_branch .Lbz_%=\n.Lst_%=:\n" \
"s_sleep 1\nbuffer_load_dword v96,v120,s[36:39],s80 offen sc1\nbuffer_load_dword v97,v121,s[36:39],s80 offen sc1\nbuffer_load_dword v98,v122,s[36:39],s80 offen sc1\n" \
"buffer_load_dword v99,v123,s[36:39],s80 offen sc1\nbuffer_load_dword v100,v120,s[36:39],s81 offen sc1\nbuffer_load_dword v101,v121,s[36:39],s81 offen sc1\nbuffer_load_dword v102,v122,s[36:39],s81 offen sc1\n" \
"buffer_load_dword v103,v123,s[36:39],s81 offen sc1\nbuffer_load_dword v104,v120,s[36:39],s82 offen sc1\nbuffer_load_dword v105,v121,s[36:39],s82 offen sc1\nbuffer_load_dword v106,v122,s[36:39],s82 offen sc1\n" \
"buffer_load_dword v107,v123,s[36:39],s82 offen sc1\nbuffer_load_dword v108,v120,s[36:39],s83 offen sc1\nbuffer_load_dword v109,v121,s[36:39],s83 offen sc1\nbuffer_load_dword v110,v122,s[36:39],s83 offen sc1\n" \
"buffer_load_dword v111,v123,s[36:39],s83 offen sc1\ns_memrealtime s[96:97]\ns_waitcnt vmcnt(0) lgkmcnt(0)\ns_sub_u32 s96,s96,s30\n" \
"s_cmp_gt_u32 s96,0x4c4b40\ns_cbranch_scc1 .Lbt_%=\nv_or3_b32 v124,v96,v97,v98\nv_or3_b32 v124,v124,v99,v100\n" \
"v_or3_b32 v124,v124,v101,v102\nv_or3_b32 v124,v124,v103,v104\nv_or3_b32 v124,v124,v105,v106\nv_or3_b32 v124,v124,v107,v108\n" \
"v_or3_b32 v124,v124,v109,v110\nv_or_b32 v124,v124,v111\nv_and_b32 v124,1,v124\nv_cmp_ne_u32 vcc,0,v124\n" \
"s_nop 0\ns_and_b64 vcc,vcc,s[78:79]\ns_cbranch_vccnz .Lst_%=\ns_branch .Lbt_%=\n" \
".LsnC_%=:\ns_sleep 2\nbuffer_load_dwordx4 v[16:19],v32,s[36:39],s5 offen sc1\nbuffer_load_dwordx4 v[20:23],v32,s[36:39],s5 offen offset:2304 sc1\n" \
"buffer_load_dwordx4 v[24:27],v32,s[36:39],s6 offen sc1\nbuffer_load_dwordx4 v[28:31],v32,s[36:39],s6 offen offset:2304 sc1\ns_memrealtime s[96:97]\ns_waitcnt vmcnt(0) lgkmcnt(0)\n" \
"s_sub_u32 s96,s96,s30\ns_cmp_gt_u32 s96,0x4c4b40\ns_cbranch_scc1 .LbnC_%=\nv_cmp_ne_u64 vcc,s[28:29],v[16:17]\n" \
"v_cmp_ne_u64 s[24:25],s[28:29],v[20:21]\nv_cmp_ne_u64 s[26:27],s[28:29],v[24:25]\ns_or_b64 vcc,vcc,s[24:25]\nv_cmp_ne_u64 s[24:25],s[28:29],v[28:29]\n" \
"s_or_b64 vcc,vcc,s[26:27]\ns_or_b64 vcc,vcc,s[24:25]\ns_cbranch_vccnz .LsnC_%=\ns_branch .LbnC_%=\n" \
".LsuC_%=:\nv_and_b32 v125,1,v112\nv_cmpx_eq_u32 vcc,0,v125\nbuffer_load_dwordx4 v[112:115],v3,s[36:39],s16 offen sc1\n" \
"s_mov_b64 exec,-1\nv_and_b32 v125,1,v116\nv_cmpx_eq_u32 vcc,0,v125\nbuffer_load_dwordx4 v[116:119],v3,s[36:39],s17 offen sc1\n" \
"s_mov_b64 exec,-1\nv_and_b32 v125,1,v120\nv_cmpx_eq_u32 vcc,0,v125\nbuffer_load_dwordx4 v[120:123],v124,s[36:39],s18 offen sc1\n" \
"s_mov_b64 exec,-1\ns_memrealtime s[96:97]\ns_waitcnt vmcnt(0) lgkmcnt(0)\ns_sub_u32 s96,s96,s30\n" \
"s_cmp_gt_u32 s96,0x4c4b40\ns_cbranch_scc1 .LbuC_%=\nv_and_b32 v125,v112,v116\nv_bitop3_b32 v125,v125,v120,1 bitop3:0x80\n" \
"v_cmp_eq_u32 vcc,0,v125\ns_nop 0\ns_cbranch_vccnz .LsuC_%=\ns_branch .LbuC_%=\n" \
".Lend_%=:\n"
#define ASM_CLOB "v1", "v2", "v3", "v4", "v5", "v6", "v7", "v8", "v9", "v10", "v11", "v12", "v13", "v14", "v15", "v16", "v17", "v18", "v19", "v20", "v21", "v22", "v23", "v24", "v25", "v26", "v27", "v28", "v29", "v30", "v31", "v32", "v33", "v34", "v35", "v36", "v37", "v38", "v39", "v40", "v41", "v42", "v43", "v44", "v45", "v46", "v47", "v48", "v49", "v50", "v51", "v52", "v53", "v54", "v55", "v56", "v57", "v58", "v59", "v60", "v61", "v62", "v63", "v64", "v65", "v66", "v67", "v68", "v69", "v70", "v71", "v72", "v73", "v74", "v75", "v76", "v77", "v78", "v79", "v80", "v81", "v82", "v83", "v84", "v85", "v86", "v87", "v88", "v89", "v90", "v91", "v92", "v93", "v94", "v95", "v96", "v97", "v98", "v99", "v100", "v101", "v102", "v103", "v104", "v105", "v106", "v107", "v108", "v109", "v110", "v111", "v112", "v113", "v114", "v115", "v116", "v117", "v118", "v119", "v120", "v121", "v122", "v123", "v124", "v125", "v126", "v127", "v128", "v129", "v130", "v131", "v132", "v133", "v134", "v135", "v136", "v137", "v138", "v139", "v140", "v141", "v142", "v143", "v144", "v145", "v146", "v147", "v148", "v149", "v150", "v151", "v152", "v153", "v154", "v155", "v156", "v157", "v158", "v159", "v160", "v161", "v162", "v163", "v164", "v165", "v166", "v167", "v168", "v169", "v170", "v171", "v172", "v173", "v174", "v175", "v176", "v177", "v178", "v179", "v180", "v181", "v182", "v183", "v184", "v185", "v186", "v187", "v188", "v189", "v190", "v191", "v192", "v193", "v194", "v195", "v196", "v197", "v198", "v199", "v200", "v201", "v202", "v203", "v204", "v205", "v206", "v207", "v208", "v209", "v210", "v211", "v212", "v213", "v214", "v215", "v216", "v217", "v218", "v219", "v220", "v221", "v222", "v223", "v224", "v225", "v226", "v227", "v228", "v229", "v230", "v231", "v232", "v233", "v234", "v235", "v236", "v237", "v238", "v239", "v240", "v241", "v242", "v243", "v244", "v245", "v246", "v247", "v248", "v249", "v250", "v251", "v252", "v253", "v254", "v255", "a0", "a1", "a2", "a3", "a4", "a5", "a6", "a7", "a8", "a9", "a10", "a11", "a12", "a13", "a14", "a15", "a16", "a17", "a18", "a19", "a20", "a21", "a22", "a23", "a24", "a25", "a26", "a27", "a28", "a29", "a30", "a31", "a32", "a33", "a34", "a35", "a36", "a37", "a38", "a39", "a40", "a41", "a42", "a43", "a44", "a45", "a46", "a47", "a48", "a49", "a50", "a51", "a52", "a53", "a54", "a55", "a56", "a57", "a58", "a59", "a60", "a61", "a62", "a63", "a64", "a65", "a66", "a67", "a68", "a69", "a70", "a71", "a72", "a73", "a74", "a75", "a76", "a77", "a78", "a79", "a80", "a81", "a82", "a83", "a84", "a85", "a86", "a87", "a88", "a89", "a90", "a91", "a92", "a93", "a94", "a95", "a96", "a97", "a98", "a99", "a100", "a101", "a102", "a103", "a104", "a105", "a106", "a107", "a108", "a109", "a110", "a111", "a112", "a113", "a114", "a115", "a116", "a117", "a118", "a119", "a120", "a121", "a122", "a123", "a124", "a125", "a126", "a127", "a128", "a129", "a130", "a131", "a132", "a133", "a134", "a135", "a136", "a137", "a138", "a139", "a140", "a141", "a142", "a143", "a144", "a145", "a146", "a147", "a148", "a149", "a150", "a151", "a152", "a153", "a154", "a155", "a156", "a157", "a158", "a159", "a160", "a161", "a162", "a163", "a164", "a165", "a166", "a167", "a168", "a169", "a170", "a171", "a172", "a173", "a174", "a175", "a176", "a177", "a178", "a179", "a180", "a181", "a182", "a183", "a184", "a185", "a186", "a187", "a188", "a189", "a190", "a191", "a192", "a193", "a194", "a195", "a196", "a197", "a198", "a199", "a200", "a201", "a202", "a203", "a204", "a205", "a206", "a207", "a208", "a209", "a210", "a211", "a212", "a213", "a214", "a215", "a216", "a217", "a218", "a219", "a220", "a221", "a222", "a223", "a224", "a225", "a226", "a227", "a228", "a229", "a230", "a231", "a232", "a233", "a234", "a235", "a236", "a237", "a238", "a239", "a240", "a241", "a242", "a243", "a244", "a245", "a246", "a247", "a248", "a249", "a250", "a251", "a252", "a253", "a254", "a255", "s3", "s6", "s7", "s8", "s9", "s10", "s11", "s12", "s13", "s14", "s15", "s16", "s17", "s18", "s19", "s20", "s21", "s22", "s23", "s24", "s25", "s26", "s27", "s28", "s29", "s30", "s31", "s32", "s33", "s34", "s35", "s36", "s37", "s38", "s39", "s40", "s41", "s42", "s43", "s44", "s45", "s46", "s47", "s48", "s49", "s50", "s51", "s52", "s53", "s54", "s55", "s56", "s57", "s58", "s59", "s60", "s61", "s62", "s63", "s64", "s65", "s66", "s67", "s68", "s69", "s70", "s71", "s72", "s73", "s74", "s75", "s76", "s77", "s78", "s79", "s80", "s81", "s82", "s83", "s84", "s85", "s86", "s87", "s88", "s89", "s90", "s91", "s92", "s93", "s94", "s95", "s96", "s97", "s98", "s99", "s100", "s101"
extern "C" __global__ __launch_bounds__(256, 1) void adm(
    const u16* x, const u16* wr, const u16* wi, const u16* w1, const u16* cvec, const u16* wmod, char* wsb, const u16* b1, const u16* b2, const u16* w2, u16* outp) {
  unsigned long long ka = (unsigned long long)__builtin_amdgcn_kernarg_segment_ptr(), tab = (unsigned long long)TAB;
  unsigned bid = blockIdx.x, tid = threadIdx.x;
  asm volatile(ASM_ALL : "+{s[0:1]}"(ka), "+{s2}"(bid), "+{s[4:5]}"(tab), "+{v0}"(tid) : : "vcc", "scc", "memory", ASM_CLOB);
}

extern "C" void kernel_launch(void* const* d_in, const int* in_sizes, int n_in, void* d_out, int out_size, void* d_ws,
                              size_t ws_size, hipStream_t stream) {
  (void)hipFuncSetAttribute((const void*)adm, hipFuncAttributeMaxDynamicSharedMemorySize, 163840);
  adm<<<256, 256, 163840, stream>>>((const u16*)d_in[0], (const u16*)d_in[1], (const u16*)d_in[2], (const u16*)d_in[4], (const u16*)d_in[8], (const u16*)d_in[9],
                                    (char*)d_ws, (const u16*)d_in[5], (const u16*)d_in[7], (const u16*)d_in[6], (u16*)d_out);
}
```

```cpp
#include <hip/hip_runtime.h>
typedef unsigned short u16;
static __device__ const unsigned __attribute__((aligned(128))) TAB[5632] = {
13312,13312,13312,13312,13312,0x2e1f3364,0x31a831a8,0x33642e1f,13312,0x31a831a8,0x34000000,0x31a8b1a8,13312,0x33642e1f,0x31a8b1a8,0xae1fb364,
13312,0x34000000,46080,0xb4000000,13312,0x3364ae1f,0xb1a8b1a8,0xae1f3364,13312,0x31a8b1a8,0xb4000000,0x31a831a8,13312,0x2e1fb364,0xb1a831a8,0x3364ae1f,
13312,46080,13312,46080,13312,0xae1fb364,0x31a831a8,0xb364ae1f,13312,0xb1a8b1a8,0x34000000,0xb1a831a8,13312,0xb364ae1f,0x31a8b1a8,0x2e1f3364,
13312,0xb4000000,46080,0x34000000,13312,0xb3642e1f,0xb1a8b1a8,0x2e1fb364,13312,0xb1a831a8,0xb4000000,0xb1a8b1a8,13312,0xae1f3364,0xb1a831a8,0xb3642e1f,
13312,13312,13312,13312,0x34000000,0x3364ae1f,0x31a8b1a8,0x2e1fb364,46080,0xb1a8b1a8,0xb4000000,0xb1a831a8,0xb4000000,0xae1f3364,0x31a831a8,0x3364ae1f,
13312,0x34000000,46080,0xb4000000,0x34000000,0xae1fb364,0xb1a831a8,0x33642e1f,46080,0xb1a831a8,0x34000000,0xb1a8b1a8,0xb4000000,0x33642e1f,0xb1a8b1a8,0x2e1f3364,
13312,46080,13312,46080,0x34000000,0xb3642e1f,0x31a8b1a8,0xae1f3364,46080,0x31a831a8,0xb4000000,0x31a8b1a8,0xb4000000,0x2e1fb364,0x31a831a8,0xb3642e1f,
13312,0xb4000000,46080,0x34000000,0x34000000,0x2e1f3364,0xb1a831a8,0xb364ae1f,46080,0x31a8b1a8,0x34000000,0x31a831a8,0xb4000000,0xb364ae1f,0xb1a8b1a8,0xae1fb364,
13312,13312,13312,13312,46080,0xae1fb364,0xb1a8b1a8,0xb364ae1f,13312,0x31a831a8,0x34000000,0x31a8b1a8,46080,0xb364ae1f,0xb1a831a8,0x2e1f3364,
13312,0x34000000,46080,0xb4000000,46080,0xb3642e1f,0x31a831a8,0x2e1fb364,13312,0x31a8b1a8,0xb4000000,0x31a831a8,46080,0xae1f3364,0x31a8b1a8,0xb3642e1f,
13312,46080,13312,46080,46080,0x2e1f3364,0xb1a8b1a8,0x33642e1f,13312,0xb1a8b1a8,0x34000000,0xb1a831a8,46080,0x33642e1f,0xb1a831a8,0xae1fb364,
13312,0xb4000000,46080,0x34000000,46080,0x3364ae1f,0x31a831a8,0xae1f3364,13312,0xb1a831a8,0xb4000000,0xb1a8b1a8,46080,0x2e1fb364,0x31a8b1a8,0x3364ae1f,
13312,13312,13312,13312,0xb4000000,0xb3642e1f,0xb1a831a8,0xae1f3364,46080,0xb1a8b1a8,0xb4000000,0xb1a831a8,0x34000000,0x2e1fb364,0xb1a8b1a8,0xb3642e1f,
13312,0x34000000,46080,0xb4000000,0xb4000000,0x2e1f3364,0x31a8b1a8,0xb364ae1f,46080,0xb1a831a8,0x34000000,0xb1a8b1a8,0x34000000,0xb364ae1f,0x31a831a8,0xae1fb364,
13312,46080,13312,46080,0xb4000000,0x3364ae1f,0xb1a831a8,0x2e1fb364,46080,0x31a831a8,0xb4000000,0x31a8b1a8,0x34000000,0xae1f3364,0xb1a8b1a8,0x3364ae1f,
13312,0xb4000000,46080,0x34000000,0xb4000000,0xae1fb364,0x31a8b1a8,0x33642e1f,46080,0x31a8b1a8,0x34000000,0x31a831a8,0x34000000,0x33642e1f,0x31a831a8,0x2e1f3364,
0x34000000,0x34000000,0x34000000,0x34000000,0x34000000,0x3364ae1f,0x31a8b1a8,0x2e1fb364,0x34000000,0x31a8b1a8,46080,0xb1a8b1a8,0x34000000,0x2e1fb364,0xb1a8b1a8,0xb3642e1f,
0x34000000,46080,0xb4000000,13312,0x34000000,0xae1fb364,0xb1a831a8,0x33642e1f,0x34000000,0xb1a8b1a8,13312,0x31a8b1a8,0x34000000,0xb364ae1f,0x31a831a8,0xae1fb364,
0x34000000,0xb4000000,0x34000000,0xb4000000,0x34000000,0xb3642e1f,0x31a8b1a8,0xae1f3364,0x34000000,0xb1a831a8,46080,0x31a831a8,0x34000000,0xae1f3364,0xb1a8b1a8,0x3364ae1f,
0x34000000,13312,0xb4000000,46080,0x34000000,0x2e1f3364,0xb1a831a8,0xb364ae1f,0x34000000,0x31a831a8,13312,0xb1a831a8,0x34000000,0x33642e1f,0x31a831a8,0x2e1f3364,
0x34000000,0x34000000,0x34000000,0x34000000,46080,0xae1fb364,0xb1a8b1a8,0xb364ae1f,0xb4000000,0xb1a831a8,13312,0x31a831a8,13312,0x33642e1f,0x31a8b1a8,0xae1fb364,
0x34000000,46080,0xb4000000,13312,46080,0xb3642e1f,0x31a831a8,0x2e1fb364,0xb4000000,0x31a831a8,46080,0xb1a831a8,13312,0x2e1fb364,0xb1a831a8,0x3364ae1f,
0x34000000,0xb4000000,0x34000000,0xb4000000,46080,0x2e1f3364,0xb1a8b1a8,0x33642e1f,0xb4000000,0x31a8b1a8,13312,0xb1a8b1a8,13312,0xb364ae1f,0x31a8b1a8,0x2e1f3364,
0x34000000,13312,0xb4000000,46080,46080,0x3364ae1f,0x31a831a8,0xae1f3364,0xb4000000,0xb1a8b1a8,46080,0x31a8b1a8,13312,0xae1f3364,0xb1a831a8,0xb3642e1f,
0x34000000,0x34000000,0x34000000,0x34000000,0xb4000000,0xb3642e1f,0xb1a831a8,0xae1f3364,0x34000000,0x31a8b1a8,46080,0xb1a8b1a8,0xb4000000,0xae1f3364,0x31a831a8,0x3364ae1f,
0x34000000,46080,0xb4000000,13312,0xb4000000,0x2e1f3364,0x31a8b1a8,0xb364ae1f,0x34000000,0xb1a8b1a8,13312,0x31a8b1a8,0xb4000000,0x33642e1f,0xb1a8b1a8,0x2e1f3364,
0x34000000,0xb4000000,0x34000000,0xb4000000,0xb4000000,0x3364ae1f,0xb1a831a8,0x2e1fb364,0x34000000,0xb1a831a8,46080,0x31a831a8,0xb4000000,0x2e1fb364,0x31a831a8,0xb3642e1f,
0x34000000,13312,0xb4000000,46080,0xb4000000,0xae1fb364,0x31a8b1a8,0x33642e1f,0x34000000,0x31a831a8,13312,0xb1a831a8,0xb4000000,0xb364ae1f,0xb1a8b1a8,0xae1fb364,
0x34000000,0x34000000,0x34000000,0x34000000,13312,0x2e1f3364,0x31a831a8,0x33642e1f,0xb4000000,0xb1a831a8,13312,0x31a831a8,46080,0xb364ae1f,0xb1a831a8,0x2e1f3364,
0x34000000,46080,0xb4000000,13312,13312,0x3364ae1f,0xb1a8b1a8,0xae1f3364,0xb4000000,0x31a831a8,46080,0xb1a831a8,46080,0xae1f3364,0x31a8b1a8,0xb3642e1f,
0x34000000,0xb4000000,0x34000000,0xb4000000,13312,0xae1fb364,0x31a831a8,0xb364ae1f,0xb4000000,0x31a8b1a8,13312,0xb1a8b1a8,46080,0x33642e1f,0xb1a831a8,0xae1fb364,
0x34000000,13312,0xb4000000,46080,13312,0xb3642e1f,0xb1a8b1a8,0x2e1fb364,0xb4000000,0xb1a8b1a8,46080,0x31a8b1a8,46080,0x2e1fb364,0x31a8b1a8,0x3364ae1f,
13312,13312,13312,0,13312,0x31243221,0x33e1298f,0,13312,0x33e1298f,0x2d79b384,0,0,0,0,0,
13312,0x32eeb000,0xb2eeb000,0,13312,0x2d79b384,0xb1243221,0,13312,0xad79b384,0x31243221,0,0,0,0,0,
13312,0xb2eeb000,0x32eeb000,0,13312,0xb3e1298f,0xad79b384,0,13312,0xb1243221,0xb3e1298f,0,0,0,0,0,
0,0,0,0,0,0,0,0,0,0,0,0,0,0,0,0,
13312,13312,13312,0,0x32eeb000,0x2d79b384,0xad79b384,0,0xb2eeb000,0xb1243221,0x31243221,0,0,0,0,0,
13312,0x32eeb000,0xb2eeb000,0,0x32eeb000,0xb3e1298f,0x33e1298f,0,0xb2eeb000,0x33e1298f,0xb3e1298f,0,0,0,0,0,
13312,0xb2eeb000,0x32eeb000,0,0x32eeb000,0x31243221,0xb1243221,0,0xb2eeb000,0xad79b384,0x2d79b384,0,0,0,0,0,
0,0,0,0,0,0,0,0,0,0,0,0,0,0,0,0,
13312,13312,13312,0,0xb2eeb000,0xb3e1298f,0xb1243221,0,0x32eeb000,0xad79b384,0xb3e1298f,0,0,0,0,0,
13312,0x32eeb000,0xb2eeb000,0,0xb2eeb000,0x31243221,0xad79b384,0,0x32eeb000,0xb1243221,0x2d79b384,0,0,0,0,0,
13312,0xb2eeb000,0x32eeb000,0,0xb2eeb000,0x2d79b384,0x33e1298f,0,0x32eeb000,0x33e1298f,0x31243221,0,0,0,0,0,
0,0,0,0,0,0,0,0,0,0,0,0,0,0,0,0,
0,0,0,0,0,0,0,0,0,0,0,0,0,0,0,0,
0,0,0,0,0,0,0,0,0,0,0,0,0,0,0,0,
0,0,0,0,0,0,0,0,0,0,0,0,0,0,0,0,
0,0,0,0,0,0,0,0,0,0,0,0,0,0,0,0,
0x34000000,0x34000000,0x34000000,0,0x34000000,0x3221b124,0x298fb3e1,0,0x34000000,0x298fb3e1,0xb384ad79,0,0,0,0,0,
0x34000000,0xb000b2ee,0xb00032ee,0,0x34000000,0xb384ad79,0x32213124,0,0x34000000,0xb3842d79,0x3221b124,0,0,0,0,0,
0x34000000,0xb00032ee,0xb000b2ee,0,0x34000000,0x298f33e1,0xb3842d79,0,0x34000000,0x32213124,0x298f33e1,0,0,0,0,0,
0,0,0,0,0,0,0,0,0,0,0,0,0,0,0,0,
0x34000000,0x34000000,0x34000000,0,0xb000b2ee,0xb384ad79,0xb3842d79,0,0xb00032ee,0x32213124,0x3221b124,0,0,0,0,0,
0x34000000,0xb000b2ee,0xb00032ee,0,0xb000b2ee,0x298f33e1,0x298fb3e1,0,0xb00032ee,0x298fb3e1,0x298f33e1,0,0,0,0,0,
0x34000000,0xb00032ee,0xb000b2ee,0,0xb000b2ee,0x3221b124,0x32213124,0,0xb00032ee,0xb3842d79,0xb384ad79,0,0,0,0,0,
0,0,0,0,0,0,0,0,0,0,0,0,0,0,0,0,
0x34000000,0x34000000,0x34000000,0,0xb00032ee,0x298f33e1,0x32213124,0,0xb000b2ee,0xb3842d79,0x298f33e1,0,0,0,0,0,
0x34000000,0xb000b2ee,0xb00032ee,0,0xb00032ee,0x3221b124,0xb3842d79,0,0xb000b2ee,0x32213124,0xb384ad79,0,0,0,0,0,
0x34000000,0xb00032ee,0xb000b2ee,0,0xb00032ee,0xb384ad79,0x298fb3e1,0,0xb000b2ee,0x298fb3e1,0x3221b124,0,0,0,0,0,
0,0,0,0,0,0,0,0,0,0,0,0,0,0,0,0,
0,0,0,0,0,0,0,0,0,0,0,0,0,0,0,0,
0,0,0,0,0,0,0,0,0,0,0,0,0,0,0,0,
0,0,0,0,0,0,0,0,0,0,0,0,0,0,0,0,
0,0,0,0,0,0,0,0,0,0,0,0,0,0,0,0,
13312,0x2a3e33d9,0x2e1f3364,0x307232a7,13312,0x307232a7,0x33642e1f,0x33d9aa3e,13312,0x32a73072,0x3364ae1f,0x2a3eb3d9,13312,0x33d92a3e,0x2e1fb364,0xb2a7b072,
13312,0x33d9aa3e,0xae1fb364,0xb2a73072,13312,0x32a7b072,0xb364ae1f,0x2a3e33d9,13312,0x3072b2a7,0xb3642e1f,0x33d92a3e,13312,0x2a3eb3d9,0xae1f3364,0x3072b2a7,
13312,0xaa3eb3d9,0x2e1f3364,0xb072b2a7,13312,0xb072b2a7,0x33642e1f,0xb3d92a3e,13312,0xb2a7b072,0x3364ae1f,0xaa3e33d9,13312,0xb3d9aa3e,0x2e1fb364,0x32a73072,
13312,0xb3d92a3e,0xae1fb364,0x32a7b072,13312,0xb2a73072,0xb364ae1f,0xaa3eb3d9,13312,0xb07232a7,0xb3642e1f,0xb3d9aa3e,13312,0xaa3e33d9,0xae1f3364,0xb07232a7,
0x31a831a8,0x32a73072,0x33642e1f,0x33d92a3e,0x31a8b1a8,0x2a3eb3d9,0xae1fb364,0xb2a7b072,0xb1a8b1a8,0xb3d92a3e,0xae1f3364,0x307232a7,0xb1a831a8,0x307232a7,0x3364ae1f,0xaa3eb3d9,
0x31a831a8,0x3072b2a7,0xb364ae1f,0xaa3e33d9,0x31a8b1a8,0xb3d9aa3e,0x2e1f3364,0x3072b2a7,0xb1a8b1a8,0x2a3e33d9,0x2e1fb364,0xb2a73072,0xb1a831a8,0x32a7b072,0xb3642e1f,0x33d9aa3e,
0x31a831a8,0xb2a7b072,0x33642e1f,0xb3d9aa3e,0x31a8b1a8,0xaa3e33d9,0xae1fb364,0x32a73072,0xb1a8b1a8,0x33d9aa3e,0xae1f3364,0xb072b2a7,0xb1a831a8,0xb072b2a7,0x3364ae1f,0x2a3e33d9,
0x31a831a8,0xb07232a7,0xb364ae1f,0x2a3eb3d9,0x31a8b1a8,0x33d92a3e,0x2e1f3364,0xb07232a7,0xb1a8b1a8,0xaa3eb3d9,0x2e1fb364,0x32a7b072,0xb1a831a8,0xb2a73072,0xb3642e1f,0xb3d92a3e,
0x34000000,0x33d9aa3e,0x3364ae1f,0x32a7b072,0xb4000000,0xb2a73072,0xae1f3364,0x2a3e33d9,0x34000000,0x3072b2a7,0xae1fb364,0xb3d9aa3e,0xb4000000,0xaa3e33d9,0x33642e1f,0x3072b2a7,
0x34000000,0xaa3eb3d9,0xb3642e1f,0x307232a7,0xb4000000,0x307232a7,0x2e1fb364,0xb3d92a3e,0x34000000,0xb2a7b072,0x2e1f3364,0x2a3eb3d9,0xb4000000,0x33d92a3e,0xb364ae1f,0x32a73072,
0x34000000,0xb3d92a3e,0x3364ae1f,0xb2a73072,0xb4000000,0x32a7b072,0xae1f3364,0xaa3eb3d9,0x34000000,0xb07232a7,0xae1fb364,0x33d92a3e,0xb4000000,0x2a3eb3d9,0x33642e1f,0xb07232a7,
0x34000000,0x2a3e33d9,0xb3642e1f,0xb072b2a7,0xb4000000,0xb072b2a7,0x2e1fb364,0x33d9aa3e,0x34000000,0x32a73072,0x2e1f3364,0xaa3e33d9,0xb4000000,0xb3d9aa3e,0xb364ae1f,0xb2a7b072,
0x31a8b1a8,0x3072b2a7,0x2e1fb364,0x2a3eb3d9,0x31a831a8,0x33d92a3e,0x3364ae1f,0x3072b2a7,0xb1a831a8,0x2a3e33d9,0x33642e1f,0x32a7b072,0xb1a8b1a8,0xb2a73072,0x2e1f3364,0x33d9aa3e,
0x31a8b1a8,0xb2a7b072,0xae1f3364,0x33d92a3e,0x31a831a8,0x2a3eb3d9,0xb3642e1f,0x32a73072,0xb1a831a8,0x33d9aa3e,0xb364ae1f,0x307232a7,0xb1a8b1a8,0x307232a7,0xae1fb364,0x2a3e33d9,
0x31a8b1a8,0xb07232a7,0x2e1fb364,0xaa3e33d9,0x31a831a8,0xb3d9aa3e,0x3364ae1f,0xb07232a7,0xb1a831a8,0xaa3eb3d9,0x33642e1f,0xb2a73072,0xb1a8b1a8,0x32a7b072,0x2e1f3364,0xb3d92a3e,
0x31a8b1a8,0x32a73072,0xae1f3364,0xb3d9aa3e,0x31a831a8,0xaa3e33d9,0xb3642e1f,0xb2a7b072,0xb1a831a8,0xb3d92a3e,0xb364ae1f,0xb072b2a7,0xb1a8b1a8,0xb072b2a7,0xae1fb364,0xaa3eb3d9,
0x34000000,0x33d9aa3e,0x3364ae1f,0x32a7b072,0x34000000,0x32a7b072,0x2e1fb364,0xaa3eb3d9,0x34000000,0x3072b2a7,0xae1fb364,0xb3d9aa3e,0x34000000,0x2a3eb3d9,0xb364ae1f,0xb07232a7,
0x34000000,0xaa3eb3d9,0xb3642e1f,0x307232a7,0x34000000,0xb072b2a7,0xae1f3364,0x33d9aa3e,0x34000000,0xb2a7b072,0x2e1f3364,0x2a3eb3d9,0x34000000,0xb3d9aa3e,0x33642e1f,0xb2a7b072,
0x34000000,0xb3d92a3e,0x3364ae1f,0xb2a73072,0x34000000,0xb2a73072,0x2e1fb364,0x2a3e33d9,0x34000000,0xb07232a7,0xae1fb364,0x33d92a3e,0x34000000,0xaa3e33d9,0xb364ae1f,0x3072b2a7,
0x34000000,0x2a3e33d9,0xb3642e1f,0xb072b2a7,0x34000000,0x307232a7,0xae1f3364,0xb3d92a3e,0x34000000,0x32a73072,0x2e1f3364,0xaa3e33d9,0x34000000,0x33d92a3e,0x33642e1f,0x32a73072,
0x31a8b1a8,0x3072b2a7,0x2e1fb364,0x2a3eb3d9,0xb1a8b1a8,0xb3d9aa3e,0xb3642e1f,0xb07232a7,0xb1a831a8,0x2a3e33d9,0x33642e1f,0x32a7b072,0x31a831a8,0x32a7b072,0xae1fb364,0xb3d92a3e,
0x31a8b1a8,0xb2a7b072,0xae1f3364,0x33d92a3e,0xb1a8b1a8,0xaa3e33d9,0x3364ae1f,0xb2a7b072,0xb1a831a8,0x33d9aa3e,0xb364ae1f,0x307232a7,0x31a831a8,0xb072b2a7,0x2e1f3364,0xaa3eb3d9,
0x31a8b1a8,0xb07232a7,0x2e1fb364,0xaa3e33d9,0xb1a8b1a8,0x33d92a3e,0xb3642e1f,0x3072b2a7,0xb1a831a8,0xaa3eb3d9,0x33642e1f,0xb2a73072,0x31a831a8,0xb2a73072,0xae1fb364,0x33d9aa3e,
0x31a8b1a8,0x32a73072,0xae1f3364,0xb3d9aa3e,0xb1a8b1a8,0x2a3eb3d9,0x3364ae1f,0x32a73072,0xb1a831a8,0xb3d92a3e,0xb364ae1f,0xb072b2a7,0x31a831a8,0x307232a7,0x2e1f3364,0x2a3e33d9,
46080,0xaa3eb3d9,0xae1fb364,0xb072b2a7,13312,0x307232a7,0x33642e1f,0x33d9aa3e,46080,0xb2a7b072,0xb3642e1f,0xaa3e33d9,13312,0x33d92a3e,0x2e1fb364,0xb2a7b072,
46080,0xb3d92a3e,0x2e1f3364,0x32a7b072,13312,0x32a7b072,0xb364ae1f,0x2a3e33d9,46080,0xb07232a7,0x3364ae1f,0xb3d9aa3e,13312,0x2a3eb3d9,0xae1f3364,0x3072b2a7,
46080,0x2a3e33d9,0xae1fb364,0x307232a7,13312,0xb072b2a7,0x33642e1f,0xb3d92a3e,46080,0x32a73072,0xb3642e1f,0x2a3eb3d9,13312,0xb3d9aa3e,0x2e1fb364,0x32a73072,
46080,0x33d9aa3e,0x2e1f3364,0xb2a73072,13312,0xb2a73072,0xb364ae1f,0xaa3eb3d9,46080,0x3072b2a7,0x3364ae1f,0x33d92a3e,13312,0xaa3e33d9,0xae1f3364,0xb07232a7,
0xb1a8b1a8,0xb2a7b072,0xb364ae1f,0xb3d9aa3e,0x31a8b1a8,0x2a3eb3d9,0xae1fb364,0xb2a7b072,0x31a831a8,0x33d9aa3e,0x2e1fb364,0xb072b2a7,0xb1a831a8,0x307232a7,0x3364ae1f,0xaa3eb3d9,
0xb1a8b1a8,0xb07232a7,0x33642e1f,0x2a3eb3d9,0x31a8b1a8,0xb3d9aa3e,0x2e1f3364,0x3072b2a7,0x31a831a8,0xaa3eb3d9,0xae1f3364,0x32a7b072,0xb1a831a8,0x32a7b072,0xb3642e1f,0x33d9aa3e,
0xb1a8b1a8,0x32a73072,0xb364ae1f,0x33d92a3e,0x31a8b1a8,0xaa3e33d9,0xae1fb364,0x32a73072,0x31a831a8,0xb3d92a3e,0x2e1fb364,0x307232a7,0xb1a831a8,0xb072b2a7,0x3364ae1f,0x2a3e33d9,
0xb1a8b1a8,0x3072b2a7,0x33642e1f,0xaa3e33d9,0x31a8b1a8,0x33d92a3e,0x2e1f3364,0xb07232a7,0x31a831a8,0x2a3e33d9,0xae1f3364,0xb2a73072,0xb1a831a8,0xb2a73072,0xb3642e1f,0xb3d92a3e,
13312,13312,13312,13312,0x2a3e33d9,0x307232a7,0x32a73072,0x33d92a3e,0x2e1f3364,0x33642e1f,0x3364ae1f,0x2e1fb364,0x307232a7,0x33d9aa3e,0x2a3eb3d9,0xb2a7b072,
0x31a831a8,0x31a8b1a8,0xb1a8b1a8,0xb1a831a8,0x32a73072,0x2a3eb3d9,0xb3d92a3e,0x307232a7,0x33642e1f,0xae1fb364,0xae1f3364,0x3364ae1f,0x33d92a3e,0xb2a7b072,0x307232a7,0xaa3eb3d9,
0x34000000,0xb4000000,0x34000000,0xb4000000,0x33d9aa3e,0xb2a73072,0x3072b2a7,0xaa3e33d9,0x3364ae1f,0xae1f3364,0xae1fb364,0x33642e1f,0x32a7b072,0x2a3e33d9,0xb3d9aa3e,0x3072b2a7,
0x31a8b1a8,0x31a831a8,0xb1a831a8,0xb1a8b1a8,0x3072b2a7,0x33d92a3e,0x2a3e33d9,0xb2a73072,0x2e1fb364,0x3364ae1f,0x33642e1f,0x2e1f3364,0x2a3eb3d9,0x3072b2a7,0x32a7b072,0x33d9aa3e,
13312,13312,13312,13312,0x33d9aa3e,0x32a7b072,0x3072b2a7,0x2a3eb3d9,0xae1fb364,0xb364ae1f,0xb3642e1f,0xae1f3364,0xb2a73072,0x2a3e33d9,0x33d92a3e,0x3072b2a7,
0x31a831a8,0x31a8b1a8,0xb1a8b1a8,0xb1a831a8,0x3072b2a7,0xb3d9aa3e,0x2a3e33d9,0x32a7b072,0xb364ae1f,0x2e1f3364,0x2e1fb364,0xb3642e1f,0xaa3e33d9,0x3072b2a7,0xb2a73072,0x33d9aa3e,
0x34000000,0xb4000000,0x34000000,0xb4000000,0xaa3eb3d9,0x307232a7,0xb2a7b072,0x33d92a3e,0xb3642e1f,0x2e1fb364,0x2e1f3364,0xb364ae1f,0x307232a7,0xb3d92a3e,0x2a3eb3d9,0x32a73072,
0x31a8b1a8,0x31a831a8,0xb1a831a8,0xb1a8b1a8,0xb2a7b072,0x2a3eb3d9,0x33d9aa3e,0x307232a7,0xae1f3364,0xb3642e1f,0xb364ae1f,0xae1fb364,0x33d92a3e,0x32a73072,0x307232a7,0x2a3e33d9,
13312,13312,13312,13312,0xaa3eb3d9,0xb072b2a7,0xb2a7b072,0xb3d9aa3e,0x2e1f3364,0x33642e1f,0x3364ae1f,0x2e1fb364,0xb072b2a7,0xb3d92a3e,0xaa3e33d9,0x32a73072,
0x31a831a8,0x31a8b1a8,0xb1a8b1a8,0xb1a831a8,0xb2a7b072,0xaa3e33d9,0x33d9aa3e,0xb072b2a7,0x33642e1f,0xae1fb364,0xae1f3364,0x3364ae1f,0xb3d9aa3e,0x32a73072,0xb072b2a7,0x2a3e33d9,
0x34000000,0xb4000000,0x34000000,0xb4000000,0xb3d92a3e,0x32a7b072,0xb07232a7,0x2a3eb3d9,0x3364ae1f,0xae1f3364,0xae1fb364,0x33642e1f,0xb2a73072,0xaa3eb3d9,0x33d92a3e,0xb07232a7,
0x31a8b1a8,0x31a831a8,0xb1a831a8,0xb1a8b1a8,0xb07232a7,0xb3d9aa3e,0xaa3eb3d9,0x32a7b072,0x2e1fb364,0x3364ae1f,0x33642e1f,0x2e1f3364,0xaa3e33d9,0xb07232a7,0xb2a73072,0xb3d92a3e,
13312,13312,13312,13312,0xb3d92a3e,0xb2a73072,0xb07232a7,0xaa3e33d9,0xae1fb364,0xb364ae1f,0xb3642e1f,0xae1f3364,0x32a7b072,0xaa3eb3d9,0xb3d9aa3e,0xb07232a7,
0x31a831a8,0x31a8b1a8,0xb1a8b1a8,0xb1a831a8,0xb07232a7,0x33d92a3e,0xaa3eb3d9,0xb2a73072,0xb364ae1f,0x2e1f3364,0x2e1fb364,0xb3642e1f,0x2a3eb3d9,0xb07232a7,0x32a7b072,0xb3d92a3e,
0x34000000,0xb4000000,0x34000000,0xb4000000,0x2a3e33d9,0xb072b2a7,0x32a73072,0xb3d9aa3e,0xb3642e1f,0x2e1fb364,0x2e1f3364,0xb364ae1f,0xb072b2a7,0x33d9aa3e,0xaa3e33d9,0xb2a7b072,
0x31a8b1a8,0x31a831a8,0xb1a831a8,0xb1a8b1a8,0x32a73072,0xaa3e33d9,0xb3d92a3e,0xb072b2a7,0xae1f3364,0xb3642e1f,0xb364ae1f,0xae1fb364,0xb3d9aa3e,0xb2a7b072,0xb072b2a7,0xaa3eb3d9,
0x34000000,0x34000000,0x34000000,0x34000000,0x33d9aa3e,0x32a7b072,0x3072b2a7,0x2a3eb3d9,0x3364ae1f,0x2e1fb364,0xae1fb364,0xb364ae1f,0x32a7b072,0xaa3eb3d9,0xb3d9aa3e,0xb07232a7,
0x31a8b1a8,0xb1a8b1a8,0xb1a831a8,0x31a831a8,0x3072b2a7,0xb3d9aa3e,0x2a3e33d9,0x32a7b072,0x2e1fb364,0xb3642e1f,0x33642e1f,0xae1fb364,0x2a3eb3d9,0xb07232a7,0x32a7b072,0xb3d92a3e,
46080,13312,46080,13312,0xaa3eb3d9,0x307232a7,0xb2a7b072,0x33d92a3e,0xae1fb364,0x33642e1f,0xb3642e1f,0x2e1fb364,0xb072b2a7,0x33d9aa3e,0xaa3e33d9,0xb2a7b072,
0xb1a8b1a8,0x31a8b1a8,0x31a831a8,0xb1a831a8,0xb2a7b072,0x2a3eb3d9,0x33d9aa3e,0x307232a7,0xb364ae1f,0xae1fb364,0x2e1fb364,0x3364ae1f,0xb3d9aa3e,0xb2a7b072,0xb072b2a7,0xaa3eb3d9,
0x34000000,0x34000000,0x34000000,0x34000000,0xaa3eb3d9,0xb072b2a7,0xb2a7b072,0xb3d9aa3e,0xb3642e1f,0xae1f3364,0x2e1f3364,0x33642e1f,0x307232a7,0x33d9aa3e,0x2a3eb3d9,0xb2a7b072,
0x31a8b1a8,0xb1a8b1a8,0xb1a831a8,0x31a831a8,0xb2a7b072,0xaa3e33d9,0x33d9aa3e,0xb072b2a7,0xae1f3364,0x3364ae1f,0xb364ae1f,0x2e1f3364,0x33d92a3e,0xb2a7b072,0x307232a7,0xaa3eb3d9,
46080,13312,46080,13312,0xb3d92a3e,0x32a7b072,0xb07232a7,0x2a3eb3d9,0x2e1f3364,0xb364ae1f,0x3364ae1f,0xae1f3364,0x32a7b072,0x2a3e33d9,0xb3d9aa3e,0x3072b2a7,
0xb1a8b1a8,0x31a8b1a8,0x31a831a8,0xb1a831a8,0xb07232a7,0xb3d9aa3e,0xaa3eb3d9,0x32a7b072,0x33642e1f,0x2e1f3364,0xae1f3364,0xb3642e1f,0x2a3eb3d9,0x3072b2a7,0x32a7b072,0x33d9aa3e,
0x34000000,0x34000000,0x34000000,0x34000000,0xb3d92a3e,0xb2a73072,0xb07232a7,0xaa3e33d9,0x3364ae1f,0x2e1fb364,0xae1fb364,0xb364ae1f,0xb2a73072,0x2a3e33d9,0x33d92a3e,0x3072b2a7,
0x31a8b1a8,0xb1a8b1a8,0xb1a831a8,0x31a831a8,0xb07232a7,0x33d92a3e,0xaa3eb3d9,0xb2a73072,0x2e1fb364,0xb3642e1f,0x33642e1f,0xae1fb364,0xaa3e33d9,0x3072b2a7,0xb2a73072,0x33d9aa3e,
46080,13312,46080,13312,0x2a3e33d9,0xb072b2a7,0x32a73072,0xb3d9aa3e,0xae1fb364,0x33642e1f,0xb3642e1f,0x2e1fb364,0x307232a7,0xb3d92a3e,0x2a3eb3d9,0x32a73072,
0xb1a8b1a8,0x31a8b1a8,0x31a831a8,0xb1a831a8,0x32a73072,0xaa3e33d9,0xb3d92a3e,0xb072b2a7,0xb364ae1f,0xae1fb364,0x2e1fb364,0x3364ae1f,0x33d92a3e,0x32a73072,0x307232a7,0x2a3e33d9,
0x34000000,0x34000000,0x34000000,0x34000000,0x2a3e33d9,0x307232a7,0x32a73072,0x33d92a3e,0xb3642e1f,0xae1f3364,0x2e1f3364,0x33642e1f,0xb072b2a7,0xb3d92a3e,0xaa3e33d9,0x32a73072,
0x31a8b1a8,0xb1a8b1a8,0xb1a831a8,0x31a831a8,0x32a73072,0x2a3eb3d9,0xb3d92a3e,0x307232a7,0xae1f3364,0x3364ae1f,0xb364ae1f,0x2e1f3364,0xb3d9aa3e,0x32a73072,0xb072b2a7,0x2a3e33d9,
46080,13312,46080,13312,0x33d9aa3e,0xb2a73072,0x3072b2a7,0xaa3e33d9,0x2e1f3364,0xb364ae1f,0x3364ae1f,0xae1f3364,0xb2a73072,0xaa3eb3d9,0x33d92a3e,0xb07232a7,
0xb1a8b1a8,0x31a8b1a8,0x31a831a8,0xb1a831a8,0x3072b2a7,0x33d92a3e,0x2a3e33d9,0xb2a73072,0x33642e1f,0x2e1f3364,0xae1f3364,0xb3642e1f,0xaa3e33d9,0xb07232a7,0xb2a73072,0xb3d92a3e,
0,304,648,952,576,880,1224,1528,1152,1456,1800,2104,1728,2032,104,408,
1728,2032,104,408,32,336,680,984,608,912,1256,1560,1184,1488,1832,2136,
1184,1488,1832,2136,1760,2064,352,1648,64,1360,72,376,64,1360,72,376,
64,1360,72,376,64,1360,72,376,64,1360,72,376,64,1360,72,376,
1296,1600,1944,2248,1872,2176,248,552,176,480,824,1128,752,1056,1400,1704,
752,1056,1400,1704,1328,1632,1976,2280,1904,2208,928,2224,640,1936,216,520,
640,1936,216,520,144,448,792,1096,720,1024,1368,1672,720,1024,1368,1672,
720,1024,1368,1672,720,1024,1368,1672,720,1024,1368,1672,720,1024,1368,1672,
320,624,968,1272,896,1200,1544,1848,1472,1776,2120,496,2048,208,1504,88,
2048,208,1504,88,1216,16,360,664,288,592,936,1240,864,1168,1512,1816,
864,1168,1512,1816,1440,1744,2088,120,2016,48,392,696,2016,48,392,696,
2016,48,392,696,2016,48,392,696,2016,48,392,696,2016,48,392,696,
1616,1920,2264,1072,2192,784,2080,232,1792,160,504,808,432,736,1080,1384,
432,736,1080,1384,1008,1312,1656,1960,1584,1888,2232,264,2160,192,536,840,
2160,192,536,840,464,768,1112,1416,1040,1344,1688,1992,1040,1344,1688,1992,
1040,1344,1688,1992,1040,1344,1688,1992,1040,1344,1688,1992,1040,1344,1688,1992,
152,456,800,1104,728,1032,1376,1680,1304,1608,1952,2256,1880,2184,256,560,
1880,2184,256,560,184,488,832,1136,760,1064,1408,1712,1336,1640,1984,2288,
1336,1640,1984,2288,1912,2216,1000,2296,712,2008,224,528,712,2008,224,528,
712,2008,224,528,712,2008,224,528,712,2008,224,528,712,2008,224,528,
1448,1752,2096,128,2024,56,400,704,328,632,976,1280,904,1208,1552,1856,
904,1208,1552,1856,1480,1784,2128,568,2056,280,1576,96,1288,24,368,672,
1288,24,368,672,296,600,944,1248,872,1176,1520,1824,872,1176,1520,1824,
872,1176,1520,1824,872,1176,1520,1824,872,1176,1520,1824,872,1176,1520,1824,
472,776,1120,1424,1048,1352,1696,2000,1624,1928,2272,1144,2200,856,2152,240,
2200,856,2152,240,1864,168,512,816,440,744,1088,1392,1016,1320,1664,1968,
1016,1320,1664,1968,1592,1896,2240,272,2168,200,544,848,2168,200,544,848,
2168,200,544,848,2168,200,544,848,2168,200,544,848,2168,200,544,848,
1768,2072,424,1720,136,1432,80,384,8,312,656,960,584,888,1232,1536,
584,888,1232,1536,1160,1464,1808,2112,1736,2040,112,416,40,344,688,992,
40,344,688,992,616,920,1264,1568,1192,1496,1840,2144,1192,1496,1840,2144,
1192,1496,1840,2144,1192,1496,1840,2144,1192,1496,1840,2144,1192,1496,1840,2144,
9920,10176,9792,0,12232,12488,12104,16,10818,10562,10946,32,13130,12874,13258,48,
9794,10050,10242,64,12106,12362,12554,80,10944,10688,10496,96,13256,13000,12808,112,
10244,9924,10116,128,12556,12236,12428,144,10494,10814,10622,160,12806,13126,12934,176,
10118,9798,9990,192,12430,12110,12302,208,10620,10940,10748,224,12932,13252,13060,240,
9992,10248,9864,4608,12304,12560,12176,4624,10746,10490,10874,4640,13058,12802,13186,4656,
9866,10122,10314,4672,12178,12434,12626,4688,10872,10616,10424,4704,13184,12928,12736,4720,
10316,9996,10188,4736,12628,12308,12500,4752,10422,10742,10550,4768,12734,13054,12862,4784,
10190,9870,10062,4800,12502,12182,12374,4816,10548,10868,10676,4832,12860,13180,12988,4848,
10064,10320,9936,1048576,12376,12632,12248,496,10674,10418,10802,480,12986,12730,13114,464,
9938,10194,9810,448,12250,12506,12122,432,10800,10544,10928,416,13112,12856,13240,400,
9812,10068,10260,384,12124,12380,12572,368,10926,10670,10478,352,13238,12982,12790,336,
10262,9942,10134,320,12574,12254,12446,304,10476,10796,10604,288,12788,13108,12916,272,
10136,9816,10008,1048576,12448,12128,12320,5104,10602,10922,10730,5088,12914,13234,13042,5072,
10010,10266,9882,5056,12322,12578,12194,5040,10728,10472,10856,5024,13040,12784,13168,5008,
9884,10140,10332,4992,12196,12452,12644,4976,10854,10598,10406,4960,13166,12910,12718,4944,
10334,10014,10206,4928,12646,12326,12518,4912,10404,10724,10532,4896,12716,13036,12844,4880,
10208,9888,10080,256,12520,12200,12392,1048576,10530,10850,10658,1048576,12842,13162,12970,1048576,
10082,10338,10368,1048576,12394,12650,12680,1048576,10656,10400,10370,1048576,12968,12712,12682,1048576,
9956,10212,10368,1048576,12268,12524,12680,1048576,10782,10526,10370,1048576,13094,12838,12682,1048576,
9830,10086,10368,1048576,12142,12398,12680,1048576,10908,10652,10370,1048576,13220,12964,12682,1048576,
10280,9960,10368,4864,12592,12272,12680,1048576,10458,10778,10370,1048576,12770,13090,12682,1048576,
10154,9834,10368,1048576,12466,12146,12680,1048576,10584,10904,10370,1048576,12896,13216,12682,1048576,
10028,10284,10368,1048576,12340,12596,12680,1048576,10710,10454,10370,1048576,13022,12766,12682,1048576,
9902,10158,10368,1048576,12214,12470,12680,1048576,10836,10580,10370,1048576,13148,12892,12682,1048576,
10352,10032,10368,1048576,12664,12344,12680,1048576,10386,10706,10370,1048576,12698,13018,12682,1048576,
10226,9906,10368,1048576,12538,12218,12680,1048576,10512,10832,10370,1048576,12824,13144,12682,1048576,
10100,10356,10368,1048576,12412,12668,12680,1048576,10638,10382,10370,1048576,12950,12694,12682,1048576,
9974,10230,10368,1048576,12286,12542,12680,1048576,10764,10508,10370,1048576,13076,12820,12682,1048576,
9848,10104,10368,1048576,12160,12416,12680,1048576,10890,10634,10370,1048576,13202,12946,12682,1048576,
10298,9978,10368,1048576,12610,12290,12680,1048576,10440,10760,10370,1048576,12752,13072,12682,1048576,
10172,9852,10368,1048576,12484,12164,12680,1048576,10566,10886,10370,1048576,12878,13198,12682,1048576,
10046,10302,10368,1048576,12358,12614,12680,1048576,10692,10436,10370,1048576,13004,12748,12682,1048576,
10048,10304,10368,1048576,12360,12616,12680,1048576,10690,10434,10370,1048576,13002,12746,12682,1048576,
9922,10178,10368,1048576,12234,12490,12680,1048576,10816,10560,10370,1048576,13128,12872,12682,1048576,
9796,10052,10368,1048576,12108,12364,12680,1048576,10942,10686,10370,1048576,13254,12998,12682,1048576,
10246,9926,10368,1048576,12558,12238,12680,1048576,10492,10812,10370,1048576,12804,13124,12682,1048576,
10120,9800,10368,1048576,12432,12112,12680,1048576,10618,10938,10370,1048576,12930,13250,12682,1048576,
9994,10250,10368,1048576,12306,12562,12680,1048576,10744,10488,10370,1048576,13056,12800,12682,1048576,
9868,10124,10368,1048576,12180,12436,12680,1048576,10870,10614,10370,1048576,13182,12926,12682,1048576,
10318,9998,10368,1048576,12630,12310,12680,1048576,10420,10740,10370,1048576,12732,13052,12682,1048576,
10192,9872,10368,1048576,12504,12184,12680,1048576,10546,10866,10370,1048576,12858,13178,12682,1048576,
10066,10322,10368,1048576,12378,12634,12680,1048576,10672,10416,10370,1048576,12984,12728,12682,1048576,
9940,10196,10368,1048576,12252,12508,12680,1048576,10798,10542,10370,1048576,13110,12854,12682,1048576,
9814,10070,10368,1048576,12126,12382,12680,1048576,10924,10668,10370,1048576,13236,12980,12682,1048576,
10264,9944,10368,1048576,12576,12256,12680,1048576,10474,10794,10370,1048576,12786,13106,12682,1048576,
10138,9818,10368,1048576,12450,12130,12680,1048576,10600,10920,10370,1048576,12912,13232,12682,1048576,
10012,10268,10368,1048576,12324,12580,12680,1048576,10726,10470,10370,1048576,13038,12782,12682,1048576,
9886,10142,10368,1048576,12198,12454,12680,1048576,10852,10596,10370,1048576,13164,12908,12682,1048576,
10336,10016,10368,1048576,12648,12328,12680,1048576,10402,10722,10370,1048576,12714,13034,12682,1048576,
10210,9890,10368,1048576,12522,12202,12680,1048576,10528,10848,10370,1048576,12840,13160,12682,1048576,
10084,10340,10368,1048576,12396,12652,12680,1048576,10654,10398,10370,1048576,12966,12710,12682,1048576,
9958,10214,10368,1048576,12270,12526,12680,1048576,10780,10524,10370,1048576,13092,12836,12682,1048576,
9832,10088,10368,1048576,12144,12400,12680,1048576,10906,10650,10370,1048576,13218,12962,12682,1048576,
10282,9962,10368,1048576,12594,12274,12680,1048576,10456,10776,10370,1048576,12768,13088,12682,1048576,
10156,9836,10368,1048576,12468,12148,12680,1048576,10582,10902,10370,1048576,12894,13214,12682,1048576,
10030,10286,10368,1048576,12342,12598,12680,1048576,10708,10452,10370,1048576,13020,12764,12682,1048576,
9904,10160,10368,1048576,12216,12472,12680,1048576,10834,10578,10370,1048576,13146,12890,12682,1048576,
10354,10034,10368,1048576,12666,12346,12680,1048576,10384,10704,10370,1048576,12696,13016,12682,1048576,
10228,9908,10368,1048576,12540,12220,12680,1048576,10510,10830,10370,1048576,12822,13142,12682,1048576,
10102,10358,10368,1048576,12414,12670,12680,1048576,10636,10380,10370,1048576,12948,12692,12682,1048576,
9976,10232,10368,1048576,12288,12544,12680,1048576,10762,10506,10370,1048576,13074,12818,12682,1048576,
9850,10106,10368,1048576,12162,12418,12680,1048576,10888,10632,10370,1048576,13200,12944,12682,1048576,
10300,9980,10368,1048576,12612,12292,12680,1048576,10438,10758,10370,1048576,12750,13070,12682,1048576,
10174,9854,10368,1048576,12486,12166,12680,1048576,10564,10884,10370,1048576,12876,13196,12682,1048576,
0,512,1536,0,16,528,1552,16,32,544,1568,32,48,560,1584,48,
64,576,1600,64,80,592,1616,80,96,608,1632,96,112,624,1648,112,
128,640,1664,128,144,656,1680,144,160,672,1696,160,176,688,1712,176,
192,704,1728,192,208,720,1744,208,224,736,1760,224,240,752,1776,240,
1536,5120,6144,4608,1552,5136,6160,4624,1568,5152,6176,4640,1584,5168,6192,4656,
1600,5184,6208,4672,1616,5200,6224,4688,1632,5216,6240,4704,1648,5232,6256,4720,
1664,5248,6272,4736,1680,5264,6288,4752,1696,5280,6304,4768,1712,5296,6320,4784,
1728,5312,6336,4800,1744,5328,6352,4816,1760,5344,6368,4832,1776,5360,6384,4848,
3072,4096,3072,0,3088,4592,3568,496,3104,4576,3552,480,3120,4560,3536,464,
3136,4544,3520,448,3152,4528,3504,432,3168,4512,3488,416,3184,4496,3472,400,
3200,4480,3456,384,3216,4464,3440,368,3232,4448,3424,352,3248,4432,3408,336,
3264,4416,3392,320,3280,4400,3376,304,3296,4384,3360,288,3312,4368,3344,272,
3072,8704,7680,4608,3088,9200,8176,5104,3104,9184,8160,5088,3120,9168,8144,5072,
3136,9152,8128,5056,3152,9136,8112,5040,3168,9120,8096,5024,3184,9104,8080,5008,
3200,9088,8064,4992,3216,9072,8048,4976,3232,9056,8032,4960,3248,9040,8016,4944,
3264,9024,8000,4928,3280,9008,7984,4912,3296,8992,7968,4896,3312,8976,7952,4880,
8,768,1792,256,24,784,1808,272,40,800,1824,288,56,816,1840,304,
72,832,1856,320,88,848,1872,336,104,864,1888,352,120,880,1904,368,
136,896,1920,384,152,912,1936,400,168,928,1952,416,184,944,1968,432,
200,960,1984,448,216,976,2000,464,232,992,2016,480,248,1008,2032,496,
1544,5376,6400,4864,1560,5392,6416,4880,1576,5408,6432,4896,1592,5424,6448,4912,
1608,5440,6464,4928,1624,5456,6480,4944,1640,5472,6496,4960,1656,5488,6512,4976,
1672,5504,6528,4992,1688,5520,6544,5008,1704,5536,6560,5024,1720,5552,6576,5040,
1736,5568,6592,5056,1752,5584,6608,5072,1768,5600,6624,5088,1784,5616,6640,5104,
3080,4352,3328,256,3096,4336,3312,240,3112,4320,3296,224,3128,4304,3280,208,
3144,4288,3264,192,3160,4272,3248,176,3176,4256,3232,160,3192,4240,3216,144,
3208,4224,3200,128,3224,4208,3184,112,3240,4192,3168,96,3256,4176,3152,80,
3272,4160,3136,64,3288,4144,3120,48,3304,4128,3104,32,3320,4112,3088,16,
3080,8960,7936,4864,3096,8944,7920,4848,3112,8928,7904,4832,3128,8912,7888,4816,
3144,8896,7872,4800,3160,8880,7856,4784,3176,8864,7840,4768,3192,8848,7824,4752,
3208,8832,7808,4736,3224,8816,7792,4720,3240,8800,7776,4704,3256,8784,7760,4688,
3272,8768,7744,4672,3288,8752,7728,4656,3304,8736,7712,4640,3320,8720,7696,4624,
4608,1024,2048,0,4624,1040,2064,16,4640,1056,2080,32,4656,1072,2096,48,
4672,1088,2112,64,4688,1104,2128,80,4704,1120,2144,96,4720,1136,2160,112,
4736,1152,2176,128,4752,1168,2192,144,4768,1184,2208,160,4784,1200,2224,176,
4800,1216,2240,192,4816,1232,2256,208,4832,1248,2272,224,4848,1264,2288,240,
6144,5632,6656,4608,6160,5648,6672,4624,6176,5664,6688,4640,6192,5680,6704,4656,
6208,5696,6720,4672,6224,5712,6736,4688,6240,5728,6752,4704,6256,5744,6768,4720,
6272,5760,6784,4736,6288,5776,6800,4752,6304,5792,6816,4768,6320,5808,6832,4784,
6336,5824,6848,4800,6352,5840,6864,4816,6368,5856,6880,4832,6384,5872,6896,4848,
7680,3584,2560,0,7696,4080,3056,496,7712,4064,3040,480,7728,4048,3024,464,
7744,4032,3008,448,7760,4016,2992,432,7776,4000,2976,416,7792,3984,2960,400,
7808,3968,2944,384,7824,3952,2928,368,7840,3936,2912,352,7856,3920,2896,336,
7872,3904,2880,320,7888,3888,2864,304,7904,3872,2848,288,7920,3856,2832,272,
7680,8192,7168,4608,7696,8688,7664,5104,7712,8672,7648,5088,7728,8656,7632,5072,
7744,8640,7616,5056,7760,8624,7600,5040,7776,8608,7584,5024,7792,8592,7568,5008,
7808,8576,7552,4992,7824,8560,7536,4976,7840,8544,7520,4960,7856,8528,7504,4944,
7872,8512,7488,4928,7888,8496,7472,4912,7904,8480,7456,4896,7920,8464,7440,4880,
4616,1280,2304,256,4632,1296,2320,272,4648,1312,2336,288,4664,1328,2352,304,
4680,1344,2368,320,4696,1360,2384,336,4712,1376,2400,352,4728,1392,2416,368,
4744,1408,2432,384,4760,1424,2448,400,4776,1440,2464,416,4792,1456,2480,432,
4808,1472,2496,448,4824,1488,2512,464,4840,1504,2528,480,4856,1520,2544,496,
6152,5888,6912,4864,6168,5904,6928,4880,6184,5920,6944,4896,6200,5936,6960,4912,
6216,5952,6976,4928,6232,5968,6992,4944,6248,5984,7008,4960,6264,6000,7024,4976,
6280,6016,7040,4992,6296,6032,7056,5008,6312,6048,7072,5024,6328,6064,7088,5040,
6344,6080,7104,5056,6360,6096,7120,5072,6376,6112,7136,5088,6392,6128,7152,5104,
7688,3840,2816,256,7704,3824,2800,240,7720,3808,2784,224,7736,3792,2768,208,
7752,3776,2752,192,7768,3760,2736,176,7784,3744,2720,160,7800,3728,2704,144,
7816,3712,2688,128,7832,3696,2672,112,7848,3680,2656,96,7864,3664,2640,80,
7880,3648,2624,64,7896,3632,2608,48,7912,3616,2592,32,7928,3600,2576,16,
7688,8448,7424,4864,7704,8432,7408,4848,7720,8416,7392,4832,7736,8400,7376,4816,
7752,8384,7360,4800,7768,8368,7344,4784,7784,8352,7328,4768,7800,8336,7312,4752,
7816,8320,7296,4736,7832,8304,7280,4720,7848,8288,7264,4704,7864,8272,7248,4688,
7880,8256,7232,4672,7896,8240,7216,4656,7912,8224,7200,4640,7928,8208,7184,4624,
4,68,1760,0,12,140,1768,16,20,212,1776,32,28,284,1784,48,
76,356,1832,64,84,428,1840,80,92,500,1848,96,100,572,1856,112,
148,644,1904,128,156,716,1912,144,164,788,1920,160,172,860,1928,176,
220,932,1976,192,228,1004,1984,208,236,1076,1992,224,244,1148,2000,240,
292,1220,2048,1536,300,1292,2056,1552,308,1364,2064,1568,316,1436,2072,1584,
364,1508,2120,1600,372,1580,2128,1616,380,1652,2136,1632,388,1724,2144,1648,
436,1796,2192,1664,444,1868,2200,1680,452,1940,2208,1696,460,2012,2216,1712,
508,2084,2264,1728,516,2156,2272,1744,524,2228,2280,1760,532,2300,2288,1776,
580,0,64,3072,588,8,136,3088,596,16,208,3104,604,24,280,3120,
652,72,352,3136,660,80,424,3152,668,88,496,3168,676,96,568,3184,
724,144,640,3200,732,152,712,3216,740,160,784,3232,748,168,856,3248,
796,216,928,3264,804,224,1000,3280,812,232,1072,3296,820,240,1144,3312,
868,288,1216,1048576,876,296,1288,1048576,884,304,1360,1048576,892,312,1432,1048576,
940,360,1504,1048576,948,368,1576,1048576,956,376,1648,1048576,964,384,1720,1048576,
1012,432,1792,1048576,1020,440,1864,1048576,1028,448,1936,1048576,1036,456,2008,1048576,
1084,504,2080,1048576,1092,512,2152,1048576,1100,520,2224,1048576,1108,528,2296,1048576,
1156,576,1760,8,1164,584,1768,24,1172,592,1776,40,1180,600,1784,56,
1228,648,1832,72,1236,656,1840,88,1244,664,1848,104,1252,672,1856,120,
1300,720,1904,136,1308,728,1912,152,1316,736,1920,168,1324,744,1928,184,
1372,792,1976,200,1380,800,1984,216,1388,808,1992,232,1396,816,2000,248,
1444,864,2048,1544,1452,872,2056,1560,1460,880,2064,1576,1468,888,2072,1592,
1516,936,2120,1608,1524,944,2128,1624,1532,952,2136,1640,1540,960,2144,1656,
1588,1008,2192,1672,1596,1016,2200,1688,1604,1024,2208,1704,1612,1032,2216,1720,
1660,1080,2264,1736,1668,1088,2272,1752,1676,1096,2280,1768,1684,1104,2288,1784,
1732,1152,64,3080,1740,1160,136,3096,1748,1168,208,3112,1756,1176,280,3128,
1804,1224,352,3144,1812,1232,424,3160,1820,1240,496,3176,1828,1248,568,3192,
1876,1296,640,3208,1884,1304,712,3224,1892,1312,784,3240,1900,1320,856,3256,
1948,1368,928,3272,1956,1376,1000,3288,1964,1384,1072,3304,1972,1392,1144,3320,
2020,1440,1216,1048576,2028,1448,1288,1048576,2036,1456,1360,1048576,2044,1464,1432,1048576,
2092,1512,1504,1048576,2100,1520,1576,1048576,2108,1528,1648,1048576,2116,1536,1720,1048576,
2164,1584,1792,1048576,2172,1592,1864,1048576,2180,1600,1936,1048576,2188,1608,2008,1048576,
2236,1656,2080,1048576,2244,1664,2152,1048576,2252,1672,2224,1048576,2260,1680,2296,1048576,
36,1728,1760,4608,44,1736,1768,4624,52,1744,1776,4640,60,1752,1784,4656,
108,1800,1832,4672,116,1808,1840,4688,124,1816,1848,4704,132,1824,1856,4720,
180,1872,1904,4736,188,1880,1912,4752,196,1888,1920,4768,204,1896,1928,4784,
252,1944,1976,4800,260,1952,1984,4816,268,1960,1992,4832,276,1968,2000,4848,
324,2016,2048,6144,332,2024,2056,6160,340,2032,2064,6176,348,2040,2072,6192,
396,2088,2120,6208,404,2096,2128,6224,412,2104,2136,6240,420,2112,2144,6256,
468,2160,2192,6272,476,2168,2200,6288,484,2176,2208,6304,492,2184,2216,6320,
540,2232,2264,6336,548,2240,2272,6352,556,2248,2280,6368,564,2256,2288,6384,
612,32,64,7680,620,40,136,7696,628,48,208,7712,636,56,280,7728,
684,104,352,7744,692,112,424,7760,700,120,496,7776,708,128,568,7792,
756,176,640,7808,764,184,712,7824,772,192,784,7840,780,200,856,7856,
828,248,928,7872,836,256,1000,7888,844,264,1072,7904,852,272,1144,7920,
900,320,1216,1048576,908,328,1288,1048576,916,336,1360,1048576,924,344,1432,1048576,
972,392,1504,1048576,980,400,1576,1048576,988,408,1648,1048576,996,416,1720,1048576,
1044,464,1792,1048576,1052,472,1864,1048576,1060,480,1936,1048576,1068,488,2008,1048576,
1116,536,2080,1048576,1124,544,2152,1048576,1132,552,2224,1048576,1140,560,2296,1048576,
1188,608,1760,4616,1196,616,1768,4632,1204,624,1776,4648,1212,632,1784,4664,
1260,680,1832,4680,1268,688,1840,4696,1276,696,1848,4712,1284,704,1856,4728,
1332,752,1904,4744,1340,760,1912,4760,1348,768,1920,4776,1356,776,1928,4792,
1404,824,1976,4808,1412,832,1984,4824,1420,840,1992,4840,1428,848,2000,4856,
1476,896,2048,6152,1484,904,2056,6168,1492,912,2064,6184,1500,920,2072,6200,
1548,968,2120,6216,1556,976,2128,6232,1564,984,2136,6248,1572,992,2144,6264,
1620,1040,2192,6280,1628,1048,2200,6296,1636,1056,2208,6312,1644,1064,2216,6328,
1692,1112,2264,6344,1700,1120,2272,6360,1708,1128,2280,6376,1716,1136,2288,6392,
1764,1184,64,7688,1772,1192,136,7704,1780,1200,208,7720,1788,1208,280,7736,
1836,1256,352,7752,1844,1264,424,7768,1852,1272,496,7784,1860,1280,568,7800,
1908,1328,640,7816,1916,1336,712,7832,1924,1344,784,7848,1932,1352,856,7864,
1980,1400,928,7880,1988,1408,1000,7896,1996,1416,1072,7912,2004,1424,1144,7928,
2052,1472,1216,1048576,2060,1480,1288,1048576,2068,1488,1360,1048576,2076,1496,1432,1048576,
2124,1544,1504,1048576,2132,1552,1576,1048576,2140,1560,1648,1048576,2148,1568,1720,1048576,
2196,1616,1792,1048576,2204,1624,1864,1048576,2212,1632,1936,1048576,2220,1640,2008,1048576,
2268,1688,2080,1048576,2276,1696,2152,1048576,2284,1704,2224,1048576,2292,1712,2296,1048576,
};
#define ASM_ALL \
"s_load_dwordx2 s[12:13],s[0:1],0\ns_load_dwordx4 s[16:19],s[0:1],8\ns_load_dwordx2 s[36:37],s[0:1],48\ns_load_dwordx2 s[8:9],s[0:1],24\n" \
"s_load_dwordx2 s[20:21],s[0:1],32\ns_load_dwordx2 s[24:25],s[0:1],40\ns_getpc_b64 s[96:97]\n.Lc0_%=:\n" \
"v_and_b32 v1,0xe0,v0\nv_lshlrev_b32 v2,7,v0\ns_lshr_b32 s33,s2,3\ns_mov_b32 s98,.Lend_%=-.Lc0_%=\n" \
"s_mov_b32 s99,0x20000\ns_sub_u32 s95,s33,9\nv_lshl_or_b32 v1,v1,24,v2\ns_lshl_b32 s95,s95,12\n" \
"s_mov_b32 m0,162176\nv_readfirstlane_b32 s3,v0\nbuffer_load_dword v1,s[96:99],s95 offen lds\ns_and_b32 s32,s2,7\n" \
"s_mov_b32 s15,0x20000\ns_mov_b32 s14,2359296\ns_lshr_b32 s3,s3,6\ns_lshl_b32 s34,s32,5\n" \
"v_and_b32 v13,31,v0\nv_bfe_u32 v12,v0,5,1\ns_or_b32 s34,s34,s33\nv_mul_u32_u24 v12,1179648,v12\n" \
"s_lshl_b32 s100,s3,8\nv_lshlrev_b32 v2,4,v0\ns_add_u32 s100,s100,s34\nv_lshl_add_u32 v15,v13,4,v12\n" \
"s_mul_i32 s89,s100,1152\nv_lshl_add_u32 v13,v13,2,v12\nv_mov_b32 v12,v15\nv_and_b32 v1,0x3f0,v2\n" \
"s_mov_b32 s38,134217728\ns_mov_b32 s39,0x20000\ns_cmp_gt_u32 s2,71\ns_cbranch_scc1 .Lnp_%=\n" \
"s_and_b32 s95,s32,3\ns_lshr_b32 s100,s32,2\ns_mul_i32 s95,s95,9\ns_mul_i32 s101,s100,576\n" \
"s_add_u32 s95,s95,s33\ns_mul_i32 s100,s100,1990656\ns_lshl_b32 s95,s95,6\ns_mov_b32 s22,576\n" \
"s_mov_b32 s23,0x20000\ns_mov_b32 s26,1990656\ns_mov_b32 s27,0x20000\ns_waitcnt lgkmcnt(0)\n" \
"s_add_u32 s20,s20,s101\ns_addc_u32 s21,s21,0\ns_add_u32 s24,s24,s100\ns_addc_u32 s25,s25,0\n" \
"buffer_load_dwordx2 v[126:127],off,s[36:39],0 sc1\nv_lshrrev_b32 v120,2,v0\nv_and_b32 v121,3,v0\nv_mul_u32_u24 v122,6912,v120\n" \
"v_lshlrev_b32 v123,3,v120\nv_lshl_add_u32 v121,v121,4,s95\nbuffer_load_dwordx2 v[24:25],v123,s[20:23],0 offen\nv_lshlrev_b32 v120,1,v120\n" \
"v_lshl_add_u32 v123,v122,2,v121\nbuffer_load_ushort v28,v120,s[20:23],0 offen offset:512\nv_add_u32 v121,v121,v122\nbuffer_load_dwordx4 v[80:83],v123,s[24:27],0 offen\n" \
"s_mov_b32 s95,6912\nbuffer_load_dwordx4 v[84:87],v123,s[24:27],s95 offen\ns_mov_b32 s95,13824\nbuffer_load_dwordx4 v[88:91],v123,s[24:27],s95 offen\n" \
"s_mov_b32 s95,20736\nbuffer_load_dwordx4 v[92:95],v123,s[24:27],s95 offen\ns_mov_b32 s95,1769472\nbuffer_load_dwordx4 v[96:99],v121,s[24:27],s95 offen\n" \
"s_branch .Lnq_%=\n.Lnp_%=:\ns_waitcnt lgkmcnt(0)\nbuffer_load_dwordx2 v[126:127],off,s[36:39],0 sc1\n" \
".Lnq_%=:\ns_memrealtime s[30:31]\nbuffer_load_dwordx4 v[224:227],v12,s[12:15],s89 offen\nbuffer_load_dwordx4 v[228:231],v12,s[12:15],s89 offen offset:512\n" \
"buffer_load_dword v232,v13,s[12:15],s89 offen offset:1024\ns_cmp_lt_u32 s3,2\ns_cselect_b64 s[16:17],s[16:17],s[18:19]\ns_mov_b32 s18,1183744\n" \
"s_mov_b32 s19,0x20000\ns_mul_i32 s100,s34,4624\nv_and_b32 v16,0x7f0,v2\ns_lshr_b32 s95,s3,1\n" \
"s_mul_i32 s95,s95,3168\ns_lshl_b32 s88,s3,10\ns_add_u32 s95,s95,s88\ns_add_u32 m0,s95,9792\n" \
"v_add_u32 v17,s100,v16\nbuffer_load_dwordx4 v16,s[16:19],s100 offen lds\nbuffer_load_dwordx4 v16,s[16:19],s100 offen offset:2048 lds\nv_add_u32 v17,4096,v17\n" \
"v_subrev_u32 v18,1183744,v17\ns_add_u32 m0,s95,13888\nv_min_u32 v17,v17,v18\ns_bitcmp1_b32 s3,0\n" \
"s_cselect_b64 exec,63,-1\nbuffer_load_dwordx4 v17,s[16:19],0 offen lds\ns_mov_b64 exec,-1\ns_mov_b32 s6,22528\n" \
"s_mov_b32 s7,0x20000\ns_mov_b32 s95,10240\nbuffer_load_dwordx4 v[246:249],v2,s[4:7],s95 offen\ns_cmp_gt_u32 s2,110\n" \
"s_cbranch_scc1 .LC_%=\ns_cmp_gt_u32 s2,71\ns_cbranch_scc1 .LN_%=\n.LP_%=:\n" \
"v_bfe_u32 v112,v0,3,2\nv_bfe_u32 v113,v0,5,1\nv_lshl_add_u32 v112,v112,8,v113\ns_lshl_b32 s48,s33,2\n" \
"v_lshrrev_b32 v113,3,v0\ns_add_u32 s48,s48,s3\nv_and_b32 v113,6,v113\nv_and_b32 v114,7,v0\n" \
"s_mul_i32 s48,s48,18432\ns_mul_i32 s49,s32,1152\nv_mul_u32_u24 v112,9216,v112\nv_xor_b32 v113,v113,v114\n" \
"s_add_u32 s48,s48,s49\nv_lshl_add_u32 v4,v113,4,v112\ns_mov_b32 s10,18874368\ns_mov_b32 s11,0x20000\n" \
"s_add_u32 s48,s48,1024\ns_add_u32 m0,s88,20224\ns_add_u32 s50,s48,9437184\nbuffer_load_dwordx4 v4,s[8:11],s48 offen lds\n" \
"s_add_u32 m0,s88,24320\nv_add_u32 v5,17408,v4\nbuffer_load_dwordx4 v4,s[8:11],s50 offen lds\nv_add_u32 v6,34816,v4\n" \
"v_add_u32 v7,52224,v4\nv_lshlrev_b32 v158,3,v0\nv_add_u32 v158,161152,v158\ns_mul_i32 s63,s32,9\n" \
"s_add_u32 s63,s63,s33\nv_bfrev_b32 v159,1\nv_cmp_gt_u32 s[64:65],16,v0\ns_lshl_b32 s86,s63,8\n" \
"s_add_u32 s63,s86,8192\nv_cndmask_b32 v159,v159,v2,s[64:65]\ns_mov_b32 s51,4096\ns_add_u32 m0,s88,28416\n" \
"s_waitcnt vmcnt(15)\nv_lshlrev_b32 v128,16,v24\nv_mul_f32 v129,0xbfb8aa3b,v128\nv_exp_f32 v129,v129\n" \
"v_and_b32 v130,0xffff0000,v24\nv_add_f32 v129,1.0,v129\nv_mul_f32 v131,0xbfb8aa3b,v130\nv_lshlrev_b32 v132,16,v25\n" \
"v_rcp_f32 v129,v129\nv_exp_f32 v131,v131\nv_mul_f32 v133,0xbfb8aa3b,v132\nv_exp_f32 v133,v133\n" \
"v_mul_f32 v128,v128,v129\nv_add_f32 v131,1.0,v131\nv_and_b32 v134,0xffff0000,v25\nv_cvt_pk_bf16_f32 v128,v128,v128\n" \
"v_rcp_f32 v131,v131\nv_add_f32 v133,1.0,v133\nv_mul_f32 v135,0xbfb8aa3b,v134\nv_lshlrev_b32 v128,16,v128\n" \
"s_waitcnt vmcnt(14)\nv_lshlrev_b32 v136,16,v28\nv_rcp_f32 v133,v133\nv_exp_f32 v135,v135\n" \
"v_mul_f32 v137,0xbfb8aa3b,v136\nv_mul_f32 v130,v130,v131\nv_exp_f32 v137,v137\nv_cvt_pk_bf16_f32 v130,v130,v130\n" \
"v_mul_f32 v132,v132,v133\nv_add_f32 v135,1.0,v135\nv_lshlrev_b32 v130,16,v130\nv_cvt_pk_bf16_f32 v132,v132,v132\n" \
"v_rcp_f32 v135,v135\nv_add_f32 v137,1.0,v137\nv_lshlrev_b32 v132,16,v132\nv_rcp_f32 v137,v137\n" \
"v_mul_f32 v134,v134,v135\nv_cvt_pk_bf16_f32 v134,v134,v134\nv_mul_f32 v136,v136,v137\nv_lshlrev_b32 v134,16,v134\n" \
"v_cvt_pk_bf16_f32 v136,v136,v136\nv_lshlrev_b32 v136,16,v136\ns_mov_b32 s52,8192\ns_mov_b32 s53,14336\n" \
"s_mul_i32 s57,s3,30720\ns_add_u32 s57,s57,38272\ns_and_b32 s60,s33,7\ns_mul_i32 s92,s3,589824\n" \
"s_add_u32 s92,s92,s49\ns_lshl_b32 s62,s60,7\ns_add_u32 s92,s92,s62\ns_load_dwordx4 s[44:47],s[0:1],72\n" \
"v_mov_b32 v16,-1\nv_mov_b32 v20,0\nv_mov_b32 v17,-1\nv_mov_b32 v21,0\n" \
"v_mov_b32 v18,-1\nv_mov_b32 v22,0\nv_mov_b32 v19,-1\nv_mov_b32 v23,0\n" \
"s_mul_i32 s54,s34,9344\ns_add_u32 s35,s54,524288\ns_add_u32 s55,s35,4096\nv_and_b32 v112,15,v0\n" \
"v_bfe_u32 v113,v0,4,2\nv_mul_u32_u24 v113,589824,v113\nv_lshl_add_u32 v113,v112,4,v113\ns_lshl_b32 s56,s34,8\n" \
"s_add_u32 s56,s56,10289152\ns_cmp_eq_u32 s3,2\ns_cselect_b32 s56,s56,2147483648\nv_add_u32 v113,s56,v113\n" \
"s_add_u32 s58,s35,8192\nv_add_u32 v114,s58,v2\nv_cmp_gt_u32 vcc,68,v0\ns_lshr_b32 s59,s33,3\n" \
"s_lshl_b32 s59,s59,3\nv_cndmask_b32 v113,v113,v114,vcc\ns_add_u32 s59,s59,s32\ns_lshl_b32 s59,s59,3\n" \
"s_add_u32 s59,s59,s60\ns_lshl_b32 s59,s59,7\ns_add_u32 s87,s59,262144\nv_lshrrev_b32 v115,3,v0\n" \
"v_and_b32 v116,0x70,v2\nv_lshl_add_u32 v115,v115,16,v116\nv_add_u32 v115,s87,v115\ns_lshl_b32 s61,s34,4\n" \
"s_add_u32 s61,s61,65536\nv_mov_b32 v116,s61\nv_cmp_gt_u32 vcc,24,v0\nv_and_b32 v118,15,v0\n" \
"v_bfe_u32 v119,v0,4,2\nv_cndmask_b32 v115,v116,v115,vcc\nv_bfrev_b32 v116,1\nv_cmp_gt_u32 vcc,25,v0\n" \
"v_lshlrev_b32 v3,4,v119\nv_lshl_add_u32 v3,v118,6,v3\nv_cndmask_b32 v115,v116,v115,vcc\nv_lshrrev_b32 v15,1,v1\n" \
"v_add_u32 v15,s57,v15\nv_and_b32 v120,0x3c0,v1\nv_bfe_u32 v121,v0,3,2\nv_and_b32 v122,3,v0\n" \
"v_lshlrev_b32 v120,1,v120\nv_lshl_add_u32 v120,v121,5,v120\nv_lshl_add_u32 v120,v122,3,v120\nv_xor_b32 v8,0,v120\n" \
"v_add_u32 v8,s57,v8\nv_xor_b32 v9,32,v120\nv_add_u32 v9,s57,v9\nv_xor_b32 v10,64,v120\n" \
"v_add_u32 v10,s57,v10\nv_xor_b32 v11,96,v120\nv_add_u32 v11,s57,v11\nv_cvt_f32_u32 v120,s34\n" \
"v_lshrrev_b32 v48,1,v118\nv_cvt_f32_u32 v48,v48\nv_and_b32 v49,1,v119\nv_cvt_f32_u32 v49,v49\n" \
"v_mul_f32 v48,0xbe000000,v48\nv_mul_f32 v50,0x3b800000,v120\nv_fma_f32 v49,v49,2.0,v50\nv_mul_f32 v49,v48,v49\n" \
"v_lshrrev_b32 v50,1,v119\nv_and_b32 v51,1,v0\nv_cmp_eq_u32 vcc,v50,v51\nv_mov_b32 v50,0x3e800000\n" \
"v_lshlrev_b32 v51,31,v0\nv_cndmask_b32 v52,0,v50,vcc\nv_xor_b32 v54,v51,v52\nv_cos_f32 v56,v49\n" \
"v_sin_f32 v57,v49\nv_fmamk_f32 v63,v48,0x40800000,v49\nv_pk_mul_f32 v[58:59],v[56:57],v[52:53] op_sel_hi:[1,0]\nv_pk_mul_f32 v[60:61],v[56:57],v[54:55] op_sel_hi:[1,0]\n" \
"v_cvt_pk_f16_f32 v40,v58,-v61\nv_cvt_pk_f16_f32 v44,v59,v60\nv_cos_f32 v62,v63\nv_sin_f32 v63,v63\n" \
"v_fmamk_f32 v57,v48,0x3f800000,v49\nv_pk_mul_f32 v[64:65],v[62:63],v[52:53] op_sel_hi:[1,0]\nv_pk_mul_f32 v[66:67],v[62:63],v[54:55] op_sel_hi:[1,0]\nv_cvt_pk_f16_f32 v41,v64,-v67\n" \
"v_cvt_pk_f16_f32 v45,v65,v66\nv_cos_f32 v56,v57\nv_sin_f32 v57,v57\nv_fmamk_f32 v63,v48,0x40a00000,v49\n" \
"v_pk_mul_f32 v[58:59],v[56:57],v[52:53] op_sel_hi:[1,0]\nv_pk_mul_f32 v[60:61],v[56:57],v[54:55] op_sel_hi:[1,0]\nv_cvt_pk_f16_f32 v42,v58,-v61\nv_cvt_pk_f16_f32 v46,v59,v60\n" \
"v_cos_f32 v62,v63\nv_sin_f32 v63,v63\nv_bfe_u32 v100,v0,1,2\nv_pk_mul_f32 v[64:65],v[62:63],v[52:53] op_sel_hi:[1,0]\n" \
"v_pk_mul_f32 v[66:67],v[62:63],v[54:55] op_sel_hi:[1,0]\nv_cvt_pk_f16_f32 v43,v64,-v67\nv_cvt_pk_f16_f32 v47,v65,v66\nv_and_b32 v101,1,v0\n" \
"v_lshl_add_u32 v100,v101,2,v100\nv_cvt_f32_u32 v100,v100\nv_mul_f32 v101,0x3a000000,v120\nv_fmamk_f32 v100,v100,0x3e000000,v101\n" \
"v_cvt_f32_u32 v101,v119\nv_add_f32 v101,v101,v101\nv_mul_f32 v101,v101,v100\nv_cmp_gt_u32 vcc,8,v118\n" \
"v_cos_f32 v106,v101\nv_sin_f32 v107,v101\nv_cndmask_b32 v102,0,0.5,vcc\nv_sub_f32_e64 v104,0.5,v102\n" \
"v_pk_mul_f32 v[108:109],v[106:107],v[102:103] op_sel_hi:[1,0]\nv_pk_mul_f32 v[110:111],v[106:107],v[104:105] op_sel_hi:[1,0]\nv_cvt_pk_f16_f32 v216,v108,-v109\nv_cvt_pk_f16_f32 v220,v109,v108\n" \
"v_cvt_pk_f16_f32 v217,v110,-v111\nv_cvt_pk_f16_f32 v221,-v111,-v110\nv_fmamk_f32 v107,v100,0x3f800000,v101\nv_cos_f32 v106,v107\n" \
"v_sin_f32 v107,v107\ns_nop 0\nv_pk_mul_f32 v[108:109],v[106:107],v[102:103] op_sel_hi:[1,0]\nv_pk_mul_f32 v[110:111],v[106:107],v[104:105] op_sel_hi:[1,0]\n" \
"v_cvt_pk_f16_f32 v218,v108,-v109\nv_cvt_pk_f16_f32 v222,v109,v108\nv_cvt_pk_f16_f32 v219,v110,-v111\nv_cvt_pk_f16_f32 v223,-v111,-v110\n" \
"v_readfirstlane_b32 s28,v126\nv_readfirstlane_b32 s29,v127\ns_add_u32 s28,s28,1\ns_addc_u32 s29,s29,0\n" \
"v_mov_b32 v168,s28\nv_mov_b32 v169,s29\ns_waitcnt vmcnt(13)\nv_and_b32 v149,0xffff0000,v80\n" \
"v_and_b32 v151,0xffff0000,v81\nv_lshlrev_b32 v148,16,v80\nv_lshlrev_b32 v150,16,v81\nv_pk_mul_f32 v[140:141],v[148:149],v[128:129] op_sel_hi:[1,0]\n" \
"v_pk_mul_f32 v[142:143],v[150:151],v[128:129] op_sel_hi:[1,0]\nv_and_b32 v149,0xffff0000,v82\nv_and_b32 v151,0xffff0000,v83\nv_lshlrev_b32 v148,16,v82\n" \
"v_lshlrev_b32 v150,16,v83\nv_pk_mul_f32 v[144:145],v[148:149],v[128:129] op_sel_hi:[1,0]\nv_pk_mul_f32 v[146:147],v[150:151],v[128:129] op_sel_hi:[1,0]\ns_waitcnt vmcnt(12)\n" \
"v_and_b32 v153,0xffff0000,v84\nv_and_b32 v155,0xffff0000,v85\nv_lshlrev_b32 v152,16,v84\nv_lshlrev_b32 v154,16,v85\n" \
"v_pk_fma_f32 v[140:141],v[152:153],v[130:131],v[140:141] op_sel_hi:[1,0,1]\nv_pk_fma_f32 v[142:143],v[154:155],v[130:131],v[142:143] op_sel_hi:[1,0,1]\ns_waitcnt vmcnt(11)\nv_and_b32 v149,0xffff0000,v88\n" \
"v_and_b32 v151,0xffff0000,v89\nv_lshlrev_b32 v148,16,v88\nv_lshlrev_b32 v150,16,v89\nv_and_b32 v153,0xffff0000,v86\n" \
"v_and_b32 v155,0xffff0000,v87\nv_pk_fma_f32 v[140:141],v[148:149],v[132:133],v[140:141] op_sel_hi:[1,0,1]\nv_pk_fma_f32 v[142:143],v[150:151],v[132:133],v[142:143] op_sel_hi:[1,0,1]\nv_lshlrev_b32 v152,16,v86\n" \
"v_lshlrev_b32 v154,16,v87\nv_pk_fma_f32 v[144:145],v[152:153],v[130:131],v[144:145] op_sel_hi:[1,0,1]\nv_pk_fma_f32 v[146:147],v[154:155],v[130:131],v[146:147] op_sel_hi:[1,0,1]\nv_and_b32 v149,0xffff0000,v90\n" \
"v_and_b32 v151,0xffff0000,v91\nv_lshlrev_b32 v148,16,v90\nv_lshlrev_b32 v150,16,v91\nv_pk_fma_f32 v[144:145],v[148:149],v[132:133],v[144:145] op_sel_hi:[1,0,1]\n" \
"v_pk_fma_f32 v[146:147],v[150:151],v[132:133],v[146:147] op_sel_hi:[1,0,1]\ns_waitcnt vmcnt(10)\nv_and_b32 v153,0xffff0000,v92\nv_and_b32 v155,0xffff0000,v93\n" \
"v_lshlrev_b32 v152,16,v92\nv_lshlrev_b32 v154,16,v93\nv_pk_fma_f32 v[140:141],v[152:153],v[134:135],v[140:141] op_sel_hi:[1,0,1]\nv_pk_fma_f32 v[142:143],v[154:155],v[134:135],v[142:143] op_sel_hi:[1,0,1]\n" \
"s_waitcnt vmcnt(9)\nv_and_b32 v149,0xffff0000,v96\nv_and_b32 v151,0xffff0000,v97\nv_lshlrev_b32 v148,16,v96\n" \
"v_lshlrev_b32 v150,16,v97\nv_and_b32 v153,0xffff0000,v94\nv_and_b32 v155,0xffff0000,v95\nv_pk_fma_f32 v[140:141],v[148:149],v[136:137],v[140:141] op_sel_hi:[1,0,1]\n" \
"v_pk_fma_f32 v[142:143],v[150:151],v[136:137],v[142:143] op_sel_hi:[1,0,1]\nv_lshlrev_b32 v152,16,v94\nv_lshlrev_b32 v154,16,v95\nv_pk_fma_f32 v[144:145],v[152:153],v[134:135],v[144:145] op_sel_hi:[1,0,1]\n" \
"v_pk_fma_f32 v[146:147],v[154:155],v[134:135],v[146:147] op_sel_hi:[1,0,1]\nv_and_b32 v149,0xffff0000,v98\nv_and_b32 v151,0xffff0000,v99\nv_lshlrev_b32 v148,16,v98\n" \
"v_lshlrev_b32 v150,16,v99\nv_pk_fma_f32 v[144:145],v[148:149],v[136:137],v[144:145] op_sel_hi:[1,0,1]\nv_pk_fma_f32 v[146:147],v[150:151],v[136:137],v[146:147] op_sel_hi:[1,0,1]\nv_and_b32 v148,3,v0\n" \
"v_permlane32_swap_b32 v140,v144\nv_permlane32_swap_b32 v141,v145\nv_permlane32_swap_b32 v142,v146\nv_permlane32_swap_b32 v143,v147\n" \
"v_pk_add_f32 v[140:141],v[140:141],v[144:145]\nv_pk_add_f32 v[142:143],v[142:143],v[146:147]\nv_lshrrev_b32 v149,4,v0\nv_and_b32 v150,12,v149\n" \
"v_permlane16_swap_b32 v140,v142\nv_permlane16_swap_b32 v141,v143\nv_lshlrev_b32 v148,5,v148\nv_pk_add_f32 v[140:141],v[140:141],v[142:143]\n" \
"v_mad_u32_u24 v148,v150,24,v148\nv_lshl_add_u32 v148,v149,3,v148\nv_add_f32_dpp v140,v140,v140 row_ror:8 row_mask:0xf bank_mask:0xf bound_ctrl:1\nv_add_f32_dpp v141,v141,v141 row_ror:8 row_mask:0xf bank_mask:0xf bound_ctrl:1\n" \
"v_add_u32 v148,161152,v148\nv_add_f32_dpp v140,v140,v140 row_ror:4 row_mask:0xf bank_mask:0xf bound_ctrl:1\nv_add_f32_dpp v141,v141,v141 row_ror:4 row_mask:0xf bank_mask:0xf bound_ctrl:1\nds_write_b64 v148,v[140:141]\n" \
"s_waitcnt lgkmcnt(0)\ns_barrier\nds_read_b64 v[160:161],v158\nds_read_b64 v[162:163],v158 offset:128\n" \
"ds_read_b64 v[164:165],v158 offset:256\nds_read_b64 v[166:167],v158 offset:384\ns_waitcnt lgkmcnt(2)\nv_pk_add_f32 v[160:161],v[160:161],v[162:163]\n" \
"s_waitcnt lgkmcnt(0)\nv_pk_add_f32 v[164:165],v[164:165],v[166:167]\nv_pk_add_f32 v[170:171],v[160:161],v[164:165]\nbuffer_store_dwordx4 v[168:171],v159,s[36:39],s63 offen sc1\n" \
"buffer_load_dwordx4 v2,s[4:7],0 offen lds\ns_add_u32 m0,s88,32512\ns_nop 0\nbuffer_load_dwordx4 v2,s[4:7],s51 offen lds\n" \
"buffer_load_dwordx4 v[234:237],v1,s[4:7],s52 offen\nbuffer_load_dwordx4 v[238:241],v1,s[4:7],s52 offen offset:1024\nbuffer_load_dwordx4 v[242:245],v2,s[4:7],s53 offen\ns_mov_b32 s52,18432\n" \
"buffer_load_dwordx4 v[252:255],v2,s[4:7],s52 offen\nbuffer_store_dwordx4 v[16:19],v2,s[36:39],s35 offen sc1\nbuffer_store_dwordx4 v[16:19],v2,s[36:39],s55 offen sc1\nbuffer_store_dwordx4 v[16:19],v113,s[36:39],0 offen sc1\n" \
"buffer_store_dwordx4 v[20:23],v115,s[36:39],0 offen sc1\nv_lshrrev_b32 v77,5,v0\nv_bfe_u32 v113,v0,1,5\nv_and_b32 v115,1,v0\n" \
"s_lshl_b32 s63,s3,1\nv_mul_u32_u24 v77,1160,v77\nv_and_b32 v76,31,v0\nv_and_b32 v114,6,v113\n" \
"v_add_u32 v115,s63,v115\nv_mad_u32_u24 v78,v76,36,v77\nv_xor_b32 v114,v114,v115\nv_lshlrev_b32 v113,7,v113\n" \
"v_lshl_add_u32 v113,v114,4,v113\ns_add_u32 s64,s54,2916352\ns_add_u32 s65,s64,4096\nv_bfrev_b32 v114,1\n" \
"v_cmp_gt_u32 vcc,72,v0\ns_lshl_b32 s67,s32,2\ns_add_u32 s66,s64,8192\nv_cndmask_b32 v114,v114,v2,vcc\n" \
"s_add_u32 s67,s67,s3\ns_lshl_b32 s67,s67,5\ns_add_u32 s67,s67,s33\ns_lshl_b32 s67,s67,11\n" \
"s_add_u32 s67,s67,5308416\ns_lshl2_add_u32 s69,s3,161664\ns_mul_i32 s72,s3,2320\nv_mov_b32 v251,s69\n" \
"v_cmp_gt_u32 vcc,32,v0\ns_lshl_b32 s7,s32,11\ns_mov_b32 s93,128\nv_cndmask_b32 v74,2.0,0,vcc\n" \
"s_mov_b32 s73,8192\ns_mov_b32 s74,12288\ns_mov_b32 s75,17408\ns_mov_b32 s76,21504\n" \
"s_mov_b32 s77,16384\ns_mov_b32 s78,25600\ns_add_u32 s7,s7,40960\ns_mov_b32 s81,0x3d800000\n" \
"s_mov_b32 s42,18874368\ns_mov_b32 s43,0x20000\ns_waitcnt vmcnt(7)\nv_lshrrev_b32 v96,1,v234\n" \
"v_lshrrev_b32 v97,1,v235\nv_lshrrev_b32 v98,1,v236\nv_lshrrev_b32 v99,1,v237\nv_add_u32 v96,s72,v96\n" \
"s_waitcnt vmcnt(6)\nv_lshrrev_b32 v100,1,v238\nv_lshrrev_b32 v101,1,v239\nv_lshrrev_b32 v102,1,v240\n" \
"v_lshrrev_b32 v103,1,v241\nv_add_u32 v97,s72,v97\nv_add_u32 v98,s72,v98\nv_add_u32 v99,s72,v99\n" \
"v_add_u32 v100,s72,v100\nv_add_u32 v101,s72,v101\nv_add_u32 v102,s72,v102\nv_add_u32 v103,s72,v103\n" \
"v_lshlrev_b32 v48,16,v224\nv_and_b32 v49,0xffff0000,v224\nv_readlane_b32 s70,v48,0\nv_readlane_b32 s71,v48,32\n" \
"v_and_b32 v51,0xffff0000,v225\nv_mov_b32 v72,s70\nv_mov_b32 v73,s71\nv_lshlrev_b32 v50,16,v225\n" \
"v_and_b32 v53,0xffff0000,v226\nv_permlane32_swap_b32 v72,v73\nv_pk_add_f32 v[48:49],v[48:49],v[72:73] op_sel_hi:[1,0] neg_lo:[0,1] neg_hi:[0,1]\nv_pk_add_f32 v[50:51],v[50:51],v[72:73] op_sel_hi:[1,0] neg_lo:[0,1] neg_hi:[0,1]\n" \
"v_pk_mul_f32 v[68:69],v[48:49],v[48:49]\nv_lshlrev_b32_e64 v52,16,v226\nv_and_b32 v55,0xffff0000,v227\nv_pk_add_f32 v[52:53],v[52:53],v[72:73] op_sel_hi:[1,0] neg_lo:[0,1] neg_hi:[0,1]\n" \
"v_pk_add_f32 v[66:67],v[48:49],v[50:51]\nv_pk_fma_f32 v[68:69],v[50:51],v[50:51],v[68:69]\nv_lshlrev_b32_e64 v54,16,v227\nv_and_b32 v57,0xffff0000,v228\n" \
"v_pk_add_f32 v[54:55],v[54:55],v[72:73] op_sel_hi:[1,0] neg_lo:[0,1] neg_hi:[0,1]\nv_pk_add_f32 v[66:67],v[66:67],v[52:53]\nv_pk_fma_f32 v[68:69],v[52:53],v[52:53],v[68:69]\nv_lshlrev_b32_e64 v56,16,v228\n" \
"v_and_b32 v59,0xffff0000,v229\nv_pk_add_f32 v[56:57],v[56:57],v[72:73] op_sel_hi:[1,0] neg_lo:[0,1] neg_hi:[0,1]\nv_pk_add_f32 v[66:67],v[66:67],v[54:55]\nv_pk_fma_f32 v[68:69],v[54:55],v[54:55],v[68:69]\n" \
"v_lshlrev_b32_e64 v58,16,v229\nv_and_b32 v61,0xffff0000,v230\nv_pk_add_f32 v[58:59],v[58:59],v[72:73] op_sel_hi:[1,0] neg_lo:[0,1] neg_hi:[0,1]\nv_pk_add_f32 v[66:67],v[66:67],v[56:57]\n" \
"v_pk_fma_f32 v[68:69],v[56:57],v[56:57],v[68:69]\nv_lshlrev_b32_e64 v60,16,v230\nv_and_b32 v63,0xffff0000,v231\nv_pk_add_f32 v[60:61],v[60:61],v[72:73] op_sel_hi:[1,0] neg_lo:[0,1] neg_hi:[0,1]\n" \
"v_pk_add_f32 v[66:67],v[66:67],v[58:59]\nv_pk_fma_f32 v[68:69],v[58:59],v[58:59],v[68:69]\nv_lshlrev_b32_e64 v62,16,v231\nv_and_b32 v65,0xffff0000,v232\n" \
"v_pk_add_f32 v[62:63],v[62:63],v[72:73] op_sel_hi:[1,0] neg_lo:[0,1] neg_hi:[0,1]\nv_pk_add_f32 v[66:67],v[66:67],v[60:61]\nv_pk_fma_f32 v[68:69],v[60:61],v[60:61],v[68:69]\nv_lshlrev_b32_e64 v64,16,v232\n" \
"v_pk_add_f32 v[64:65],v[64:65],v[72:73] op_sel_hi:[1,0] neg_lo:[0,1] neg_hi:[0,1]\nv_pk_add_f32 v[66:67],v[66:67],v[62:63]\nv_pk_fma_f32 v[68:69],v[62:63],v[62:63],v[68:69]\nv_pk_add_f32 v[66:67],v[66:67],v[64:65]\n" \
"v_pk_fma_f32 v[68:69],v[64:65],v[64:65],v[68:69]\nv_add_f32 v66,v66,v67\nv_add_f32 v68,v68,v69\ns_mov_b64 s[40:41],s[44:45]\n" \
"s_mov_b64 s[44:45],s[46:47]\nv_permlane16_swap_b32 v66,v68\nv_add_f32 v66,v66,v68\ns_mov_b32 s46,2359296\n" \
"s_mov_b32 s47,0x20000\nv_add_f32_dpp v66,v66,v66 quad_perm:[1,0,3,2] row_mask:0xf bank_mask:0xf bound_ctrl:1\ns_nop 1\nv_add_f32_dpp v66,v66,v66 quad_perm:[2,3,0,1] row_mask:0xf bank_mask:0xf bound_ctrl:1\n" \
"s_nop 1\nv_add_f32_dpp v66,v66,v66 row_half_mirror row_mask:0xf bank_mask:0xf bound_ctrl:1\ns_nop 1\nv_add_f32_dpp v66,v66,v66 row_mirror row_mask:0xf bank_mask:0xf bound_ctrl:1\n" \
"v_mul_f32 v70,0x3ae38e39,v66\nv_mul_f32 v68,0x3ae38e39,v66\ns_nop 1\nv_permlane16_swap_b32 v70,v68\n" \
"v_fma_f32 v68,-v70,v70,v68\nv_max_f32 v68,0,v68\nv_add_f32 v68,0x3727c5ac,v68\nv_rsq_f32 v66,v68\n" \
"v_add_f32 v250,v70,v72\nv_mul_f32 v67,-v70,v66\nv_pk_fma_f32 v[48:49],v[48:49],v[66:67],v[66:67] op_sel:[0,0,1] op_sel_hi:[1,0,1]\nv_pk_fma_f32 v[50:51],v[50:51],v[66:67],v[66:67] op_sel:[0,0,1] op_sel_hi:[1,0,1]\n" \
"v_cvt_pk_f16_f32 v80,v48,v49\nv_cvt_pk_f16_f32 v81,v50,v51\nv_pk_fma_f32 v[52:53],v[52:53],v[66:67],v[66:67] op_sel:[0,0,1] op_sel_hi:[1,0,1]\nv_pk_fma_f32 v[54:55],v[54:55],v[66:67],v[66:67] op_sel:[0,0,1] op_sel_hi:[1,0,1]\n" \
"ds_write2_b32 v78,v80,v81 offset0:0 offset1:1\nv_cvt_pk_f16_f32 v82,v52,v53\nv_cvt_pk_f16_f32 v83,v54,v55\nv_pk_fma_f32 v[56:57],v[56:57],v[66:67],v[66:67] op_sel:[0,0,1] op_sel_hi:[1,0,1]\n" \
"v_pk_fma_f32 v[58:59],v[58:59],v[66:67],v[66:67] op_sel:[0,0,1] op_sel_hi:[1,0,1]\nds_write2_b32 v78,v82,v83 offset0:2 offset1:3\nv_cvt_pk_f16_f32 v84,v56,v57\nv_cvt_pk_f16_f32 v85,v58,v59\n" \
"v_pk_fma_f32 v[60:61],v[60:61],v[66:67],v[66:67] op_sel:[0,0,1] op_sel_hi:[1,0,1]\nv_pk_fma_f32 v[62:63],v[62:63],v[66:67],v[66:67] op_sel:[0,0,1] op_sel_hi:[1,0,1]\nds_write2_b32 v78,v84,v85 offset0:4 offset1:5\nv_cvt_pk_f16_f32 v86,v60,v61\n" \
"v_cvt_pk_f16_f32 v87,v62,v63\nv_pk_fma_f32 v[64:65],v[64:65],v[66:67],v[66:67] op_sel:[0,0,1] op_sel_hi:[1,0,1]\nds_write2_b32 v78,v86,v87 offset0:6 offset1:7\nv_cvt_pk_f16_f32 v88,v64,v65\n" \
"ds_write_b32 v78,v88 offset:32\ns_barrier\nds_read_u16 v16,v246\nds_read_u16 v17,v246 offset:5216\n" \
"ds_read_u16 v18,v246 offset:578\nds_read_u16 v19,v246 offset:5794\nds_read_u16 v20,v246 offset:1156\nds_read_u16 v21,v246 offset:6372\n" \
"ds_read_u16 v22,v246 offset:1734\nds_read_u16 v23,v246 offset:6950\nds_read_u16 v24,v247\ns_waitcnt lgkmcnt(7)\n" \
"v_lshl_or_b32 v16,v17,16,v16\nds_read_u16 v25,v247 offset:5216\nds_read_u16 v26,v247 offset:578\ns_waitcnt lgkmcnt(7)\n" \
"v_lshl_or_b32 v17,v19,16,v18\nv_pk_maximum3_f16 v124,v16,v17,v16\nds_read_u16 v27,v247 offset:5794\nv_pk_minimum3_f16 v125,v16,v17,v16\n" \
"ds_read_u16 v28,v247 offset:1156\ns_waitcnt lgkmcnt(7)\nv_lshl_or_b32 v18,v21,16,v20\nds_read_u16 v29,v247 offset:6372\n" \
"ds_read_u16 v30,v247 offset:1734\ns_waitcnt lgkmcnt(7)\nv_lshl_or_b32 v19,v23,16,v22\nbuffer_store_dwordx4 v[16:19],v2,s[36:39],s64 offen sc1\n" \
"ds_read_u16 v31,v247 offset:6950\nds_read_u16 v104,v248\nv_pk_maximum3_f16 v124,v18,v19,v124\nv_pk_minimum3_f16 v125,v18,v19,v125\n" \
"s_waitcnt lgkmcnt(7)\nv_lshl_or_b32 v24,v25,16,v24\nds_read_u16 v105,v248 offset:5216\nds_read_u16 v106,v248 offset:578\n" \
"s_waitcnt lgkmcnt(7)\nv_lshl_or_b32 v25,v27,16,v26\nv_pk_maximum3_f16 v124,v24,v25,v124\nds_read_u16 v107,v248 offset:5794\n" \
"v_pk_minimum3_f16 v125,v24,v25,v125\nds_read_u16 v108,v248 offset:1156\ns_waitcnt lgkmcnt(7)\nv_lshl_or_b32 v26,v29,16,v28\n" \
"ds_read_u16 v109,v248 offset:6372\nds_read_u16 v110,v248 offset:1734\ns_waitcnt lgkmcnt(7)\nv_lshl_or_b32 v27,v31,16,v30\n" \
"buffer_store_dwordx4 v[24:27],v2,s[36:39],s65 offen sc1\nds_read_u16 v111,v248 offset:6950\nds_read_b128 v[116:119],v113 offset:20224\nv_pk_maximum3_f16 v124,v26,v27,v124\n" \
"v_pk_minimum3_f16 v125,v26,v27,v125\nds_read_b128 v[120:123],v113 offset:24320\ns_waitcnt lgkmcnt(8)\nv_lshl_or_b32 v104,v105,16,v104\n" \
"ds_read_b32 v16,v96\nds_read_b32 v24,v96 offset:1160\nds_read_b32 v17,v97\nds_read_b32 v25,v97 offset:1160\n" \
"s_waitcnt lgkmcnt(10)\nv_lshl_or_b32 v105,v107,16,v106\nds_read_b32 v18,v98\nv_pk_maximum3_f16 v124,v104,v105,v124\n" \
"ds_read_b32 v26,v98 offset:1160\nv_pk_minimum3_f16 v125,v104,v105,v125\nds_read_b32 v19,v99\nds_read_b32 v27,v99 offset:1160\n" \
"ds_read_b32 v20,v100\ns_waitcnt lgkmcnt(13)\nv_lshl_or_b32 v106,v109,16,v108\nds_read_b32 v28,v100 offset:1160\n" \
"ds_read_b32 v21,v101\nds_read_b32 v29,v101 offset:1160\ns_waitcnt lgkmcnt(14)\nv_lshl_or_b32 v107,v111,16,v110\n" \
"buffer_store_dwordx4 v[104:107],v114,s[36:39],s66 offen sc1\ns_waitcnt lgkmcnt(13)\nbuffer_store_dwordx4 v[116:119],v1,s[36:39],s67 offen sc1\ns_waitcnt lgkmcnt(12)\n" \
"buffer_store_dwordx4 v[120:123],v1,s[36:39],s67 offen offset:1024 sc1\nds_read_b32 v22,v102\nds_read_b32 v30,v102 offset:1160\nds_read_b32 v23,v103\n" \
"ds_read_b32 v31,v103 offset:1160\nv_pk_maximum3_f16 v124,v106,v107,v124\nv_and_b32 v124,0x7fff7fff,v124\nv_pk_minimum3_f16 v125,v106,v107,v125\n" \
"v_and_b32 v125,0x7fff7fff,v125\nv_pk_max_u16 v124,v124,v125\nv_lshlrev_b32 v125,16,v124\nv_max_u32 v124,v124,v125\n" \
"s_nop 1\nv_max_u32_dpp v124,v124,v124 quad_perm:[1,0,3,2] row_mask:0xf bank_mask:0xf bound_ctrl:1\ns_nop 1\nv_max_u32_dpp v124,v124,v124 quad_perm:[2,3,0,1] row_mask:0xf bank_mask:0xf bound_ctrl:1\n" \
"s_nop 1\nv_max_u32_dpp v124,v124,v124 row_half_mirror row_mask:0xf bank_mask:0xf bound_ctrl:1\ns_nop 1\nv_max_u32_dpp v124,v124,v124 row_mirror row_mask:0xf bank_mask:0xf bound_ctrl:1\n" \
"s_nop 0\nv_readlane_b32 s68,v124,0\nv_readlane_b32 s100,v124,16\nv_readlane_b32 s101,v124,32\n" \
"v_readlane_b32 s31,v124,48\ns_max_u32 s100,s100,s101\ns_max_u32 s68,s68,s31\ns_max_u32 s68,s68,s100\n" \
"v_mov_b32 v124,s68\nds_write_b32 v251,v124 offset:32\nv_mov_b32 v114,161664\nbuffer_load_dwordx4 v[48:51],v2,s[36:39],s73 offen sc1\n" \
"buffer_load_dwordx4 v[60:63],v2,s[36:39],s75 offen sc1\nbuffer_load_dwordx4 v[52:55],v2,s[36:39],s74 offen sc1\nbuffer_load_dwordx4 v[64:67],v2,s[36:39],s76 offen sc1\nbuffer_load_dwordx4 v[56:59],v1,s[36:39],s77 offen sc1\n" \
"buffer_load_dwordx4 v[68:71],v1,s[36:39],s78 offen sc1\ns_mov_b32 s79,0xffffff\ns_lshl3_add_u32 s80,s2,4294615040\nv_add_u32 v112,28416,v1\n" \
"s_waitcnt vmcnt(6) lgkmcnt(0)\ns_barrier\nds_read_b128 v[116:119],v114 offset:32\nds_read_b128 v[192:195],v112\n" \
"ds_read_b128 v[196:199],v112 offset:1024\nds_read_b128 v[200:203],v112 offset:2048\nds_read_b128 v[204:207],v112 offset:3072\nds_read_b128 v[208:211],v112 offset:4096\n" \
"ds_read_b128 v[212:215],v112 offset:5120\nds_read_b128 v[32:35],v112 offset:6144\nds_read_b128 v[36:39],v112 offset:7168\ns_waitcnt lgkmcnt(8)\n" \
"v_max3_u32 v116,v116,v117,v118\nv_max_u32 v116,v116,v119\nv_and_b32 v119,0xf8,v0\nv_cmp_ne_u32 vcc,192,v119\n" \
"v_lshlrev_b32 v116,1,v116\nv_mov_b32 v117,s29\nv_lshlrev_b32 v118,11,v0\nv_bfi_b32 v117,s79,v117,v116\n" \
"v_add_u32 v118,s80,v118\nv_bfrev_b32 v119,1\nv_cndmask_b32 v118,v118,v119,vcc\nv_mov_b32 v116,s28\n" \
"buffer_store_dwordx2 v[116:117],v118,s[36:39],0 offen sc1\ns_waitcnt vmcnt(6)\nv_cmp_ne_u64 vcc,s[28:29],v[48:49]\ns_waitcnt vmcnt(4)\n" \
"v_cmp_ne_u64 s[84:85],s[28:29],v[52:53]\ns_waitcnt vmcnt(2)\nv_cmp_ne_u64 s[82:83],s[28:29],v[56:57]\ns_or_b64 vcc,vcc,s[84:85]\n" \
"v_cmp_ne_u64 s[84:85],s[28:29],v[60:61]\ns_or_b64 vcc,vcc,s[82:83]\nv_cmp_ne_u64 s[82:83],s[28:29],v[64:65]\ns_or_b64 vcc,vcc,s[84:85]\n" \
"s_waitcnt vmcnt(1)\nv_cmp_ne_u64 s[84:85],s[28:29],v[68:69]\ns_or_b64 vcc,vcc,s[82:83]\ns_or_b64 vcc,vcc,s[84:85]\n" \
"s_cbranch_vccnz .LsmP_%=\n.LbmP_%=:\nv_pk_add_f32 v[50:51],v[50:51],v[62:63]\nv_pk_add_f32 v[54:55],v[54:55],v[66:67]\n" \
"v_pk_mul_f32 v[62:63],v[50:51],2.0 op_sel_hi:[1,0]\nv_pk_add_f32 v[58:59],v[58:59],v[70:71]\nv_max3_f32 v112,|v62|,|v63|,0\nv_pk_fma_f32 v[66:67],v[54:55],2.0,v[74:75] op_sel_hi:[1,0,0]\n" \
"v_max3_f32 v112,|v66|,|v67|,v112\nv_pk_fma_f32 v[70:71],v[58:59],2.0,2.0 op_sel_hi:[1,0,0]\nv_max3_f32 v112,|v70|,|v71|,v112\nv_cmp_lt_f32 s[82:83],s81,v112\n" \
"v_cmp_ngt_f32 vcc,0x41000000,v112\ns_cmp_lg_u64 s[82:83],0\ns_cselect_b32 s82,2,0\nv_cvt_pk_f16_f32 v62,v62,v63\n" \
"s_cmp_lg_u64 vcc,0\ns_addc_u32 s82,s82,0\nds_write_b32 v252,v62 offset:62848\nv_cvt_pk_f16_f32 v66,v66,v67\n" \
"ds_write_b32 v253,v66 offset:62848\nv_cvt_pk_f16_f32 v70,v70,v71\nv_mov_b32 v113,s82\nds_write_b32 v254,v70 offset:62848\n" \
"ds_write_b32 v251,v113\ns_waitcnt lgkmcnt(0)\ns_barrier\nds_read_b128 v[124:127],v114\n" \
"ds_read_b64 v[96:97],v234 offset:62848\nds_read_b64 v[98:99],v235 offset:62848\nds_read_b64 v[100:101],v236 offset:62848\nds_read_b64 v[102:103],v237 offset:62848\n" \
"ds_read_b64 v[104:105],v238 offset:62848\nds_read_b64 v[106:107],v239 offset:62848\nds_read_b64 v[108:109],v240 offset:62848\nds_read_b64 v[110:111],v241 offset:62848\n" \
"s_waitcnt lgkmcnt(8)\nv_or3_b32 v124,v124,v125,v126\nv_or_b32 v124,v124,v127\nv_cmp_ne_u32 vcc,2,v124\n" \
"s_nop 0\ns_cbranch_vccnz .LsxP_%=\n.LbxP_%=:\ns_waitcnt lgkmcnt(7)\n" \
"v_pk_fma_f16 v80,v16,v96,v97\ns_waitcnt lgkmcnt(6)\nv_pk_fma_f16 v81,v17,v98,v99\ns_waitcnt lgkmcnt(5)\n" \
"v_pk_fma_f16 v82,v18,v100,v101\ns_waitcnt lgkmcnt(4)\nv_pk_fma_f16 v83,v19,v102,v103\nv_pk_fma_f16 v88,v24,v96,v97\n" \
"s_waitcnt lgkmcnt(3)\nv_pk_fma_f16 v84,v20,v104,v105\ns_waitcnt lgkmcnt(2)\nv_pk_fma_f16 v85,v21,v106,v107\n" \
"v_pk_fma_f16 v89,v25,v98,v99\ns_waitcnt lgkmcnt(1)\nv_pk_fma_f16 v86,v22,v108,v109\nv_pk_fma_f16 v90,v26,v100,v101\n" \
"s_waitcnt lgkmcnt(0)\nv_pk_fma_f16 v87,v23,v110,v111\nv_pk_fma_f16 v91,v27,v102,v103\nv_pk_fma_f16 v92,v28,v104,v105\n" \
"v_pk_fma_f16 v93,v29,v106,v107\nv_pk_fma_f16 v94,v30,v108,v109\nv_bfe_u32 v233,v0,3,3\nv_mul_u32_u24 v233,8192,v233\n" \
"v_lshl_add_u32 v233,v1,3,v233\ns_lshl2_add_u32 s96,s33,s3\ns_add_u32 s96,s96,138\ns_and_b32 s97,s96,31\n" \
"s_lshl_b32 s97,s97,3\ns_cmp_lt_u32 s96,256\ns_cselect_b32 s96,s96,0x40000\ns_lshr_b32 s96,s96,5\n" \
"s_add_u32 s96,s96,s97\ns_lshl_b32 s96,s96,6\ns_add_u32 s96,s96,s32\ns_mul_i32 s96,s96,1152\n" \
"s_mov_b32 m0,162176\nv_pk_fma_f16 v95,v31,v110,v111\nbuffer_load_dword v233,s[8:11],s96 offen lds\nv_mfma_f32_16x16x32_f16 v[48:51],v[80:83],v[192:195],0\n" \
"s_add_u32 s73,s35,s88\ns_add_u32 s74,s73,4096\nv_mfma_f32_16x16x32_f16 v[52:55],v[80:83],v[196:199],0\nv_and_b32 v112,15,v0\n" \
"v_bfrev_b32 v113,1\nv_mfma_f32_16x16x32_f16 v[56:59],v[84:87],v[32:35],0\nv_cmp_eq_u32 vcc,0,v112\ns_add_u32 s75,s73,8192\n" \
"v_mfma_f32_16x16x32_f16 v[60:63],v[84:87],v[36:39],0\nv_cndmask_b32 v113,v113,v3,vcc\nv_cmp_gt_u32 vcc,64,v0\nv_mfma_f32_16x16x32_f16 v[64:67],v[88:91],v[192:195],0\n" \
"s_cmp_lt_u32 s3,2\ns_cselect_b32 s75,s75,2147483648\nv_mfma_f32_16x16x32_f16 v[68:71],v[88:91],v[196:199],0\nv_pk_add_f32 v[56:57],v[48:49],v[56:57] neg_lo:[0,1] neg_hi:[0,1]\n" \
"v_sub_f32 v58,v50,v58\nv_mfma_f32_16x16x32_f16 v[72:75],v[92:95],v[32:35],0\nv_pk_add_f32 v[60:61],v[52:53],v[60:61] neg_lo:[0,1] neg_hi:[0,1]\nv_sub_f32 v62,v54,v62\n" \
"v_mfma_f32_16x16x32_f16 v[76:79],v[92:95],v[36:39],0\nv_pk_fma_f32 v[48:49],v[48:49],2.0,v[56:57] op_sel_hi:[1,0,1] neg_lo:[0,0,1] neg_hi:[0,0,1]\nv_fma_f32 v50,v50,2.0,-v58\nv_pk_fma_f32 v[52:53],v[52:53],2.0,v[60:61] op_sel_hi:[1,0,1] neg_lo:[0,0,1] neg_hi:[0,0,1]\n" \
"v_fma_f32 v54,v54,2.0,-v62\nv_cvt_pk_f16_f32 v16,v48,v52\nv_pk_add_f32 v[72:73],v[64:65],v[72:73] neg_lo:[0,1] neg_hi:[0,1]\nv_sub_f32 v74,v66,v74\n" \
"v_pk_fma_f32 v[64:65],v[64:65],2.0,v[72:73] op_sel_hi:[1,0,1] neg_lo:[0,0,1] neg_hi:[0,0,1]\nv_pk_add_f32 v[76:77],v[68:69],v[76:77] neg_lo:[0,1] neg_hi:[0,1]\nv_sub_f32 v78,v70,v78\nv_fma_f32 v66,v66,2.0,-v74\n" \
"v_pk_fma_f32 v[68:69],v[68:69],2.0,v[76:77] op_sel_hi:[1,0,1] neg_lo:[0,0,1] neg_hi:[0,0,1]\nv_fma_f32 v70,v70,2.0,-v78\nv_cvt_pk_f16_f32 v17,v49,v53\nv_cvt_pk_f16_f32 v18,v50,v54\n" \
"v_cvt_pk_f16_f32 v24,v64,v68\nv_cvt_pk_f16_f32 v25,v65,v69\nv_cvt_pk_f16_f32 v26,v66,v70\nv_mfma_f32_16x16x32_f16 v[48:51],v[200:203],v[16:19],0\n" \
"v_cvt_pk_f16_f32 v20,v56,v60\nv_cvt_pk_f16_f32 v21,v57,v61\nv_mfma_f32_16x16x32_f16 v[52:55],v[204:207],v[16:19],0\nv_cvt_pk_f16_f32 v22,v58,v62\n" \
"v_cvt_pk_f16_f32 v28,v72,v76\nv_mfma_f32_16x16x32_f16 v[64:67],v[200:203],v[24:27],0\nv_cvt_pk_f16_f32 v29,v73,v77\nv_cvt_pk_f16_f32 v30,v74,v78\n" \
"v_mfma_f32_16x16x32_f16 v[68:71],v[204:207],v[24:27],0\nv_cndmask_b32_e64 v113,v113,v3,vcc\nv_mfma_f32_16x16x32_f16 v[56:59],v[200:203],v[20:23],0\nv_cvt_pk_f16_f32 v80,v48,v52\n" \
"v_cvt_pk_f16_f32 v82,v49,v53\nv_mfma_f32_16x16x32_f16 v[60:63],v[204:207],v[20:23],0\nv_cvt_pk_f16_f32 v84,v50,v54\nv_mfma_f32_16x16x32_f16 v[72:75],v[200:203],v[28:31],0\n" \
"s_nop 0\nv_cvt_pk_f16_f32 v81,v64,v68\nv_mfma_f32_16x16x32_f16 v[76:79],v[204:207],v[28:31],0\nds_write_b64 v255,v[80:81]\n" \
"v_cvt_pk_f16_f32 v83,v65,v69\nds_write_b64 v255,v[82:83] offset:512\nv_cvt_pk_f16_f32 v85,v66,v70\nds_write_b64 v255,v[84:85] offset:1024\n" \
"v_cvt_pk_f16_f32 v86,v56,v60\nv_cvt_pk_f16_f32 v88,v57,v61\nv_cvt_pk_f16_f32 v90,v58,v62\nv_cvt_pk_f16_f32 v87,v72,v76\n" \
"ds_write_b64 v255,v[86:87] offset:256\nv_cvt_pk_f16_f32 v89,v73,v77\nds_write_b64 v255,v[88:89] offset:768\nv_cvt_pk_f16_f32 v91,v74,v78\n" \
"ds_write_b64 v255,v[90:91] offset:1280\ns_waitcnt lgkmcnt(0)\ns_barrier\nds_read_b128 v[16:19],v243\n" \
"ds_read_b128 v[20:23],v244\nds_read_b128 v[24:27],v245\ns_waitcnt lgkmcnt(2)\nv_mfma_f32_16x16x32_f16 v[48:51],v[40:43],v[16:19],0\n" \
"v_mfma_f32_16x16x32_f16 v[52:55],v[44:47],v[16:19],0\ns_waitcnt lgkmcnt(1)\nv_mfma_f32_16x16x32_f16 v[56:59],v[40:43],v[20:23],0\nv_mfma_f32_16x16x32_f16 v[60:63],v[44:47],v[20:23],0\n" \
"s_waitcnt lgkmcnt(0)\nv_mfma_f32_16x16x32_f16 v[64:67],v[40:43],v[24:27],0\nv_mfma_f32_16x16x32_f16 v[68:71],v[44:47],v[24:27],0\ns_nop 1\n" \
"v_cvt_pk_f16_f32 v80,v48,v52\nv_and_b32 v80,-2,v80\nv_cvt_pk_f16_f32 v81,v49,v53\nv_cvt_pk_f16_f32 v82,v50,v54\n" \
"v_cvt_pk_f16_f32 v83,v51,v55\nbuffer_store_dwordx4 v[80:83],v3,s[36:39],s73 offen sc1\nv_cvt_pk_f16_f32 v84,v56,v60\nv_cvt_pk_f16_f32 v85,v57,v61\n" \
"v_cvt_pk_f16_f32 v86,v58,v62\nv_cvt_pk_f16_f32 v87,v59,v63\nv_and_b32_e64 v84,-2,v84\nbuffer_store_dwordx4 v[84:87],v3,s[36:39],s74 offen sc1\n" \
"v_cvt_pk_f16_f32 v88,v64,v68\nv_cvt_pk_f16_f32 v89,v65,v69\nv_cvt_pk_f16_f32 v90,v66,v70\nv_cvt_pk_f16_f32 v91,v67,v71\n" \
"v_and_b32 v88,-2,v88\nbuffer_store_dwordx4 v[88:91],v113,s[36:39],s75 offen sc1\ns_cmp_lt_u32 s3,2\ns_cselect_b32 s18,s75,s73\n" \
"s_mov_b32 s12,s88\ns_mov_b32 s13,s89\ns_mov_b32 s14,s87\ns_mov_b32 s16,s73\n" \
"s_mov_b32 s17,s74\ns_load_dwordx2 s[20:21],s[0:1],32\ns_load_dwordx2 s[24:25],s[0:1],40\ns_mov_b32 s22,1152\n" \
"s_mov_b32 s26,3981312\ns_lshr_b32 s95,s86,2\ns_add_u32 s95,s95,2304\ns_waitcnt lgkmcnt(0)\n" \
"v_lshrrev_b32 v120,2,v0\nv_and_b32 v121,3,v0\nv_mul_u32_u24 v122,6912,v120\nv_lshlrev_b32 v123,4,v120\n" \
"v_lshl_add_u32 v121,v121,4,s95\nbuffer_load_dwordx4 v[112:115],v123,s[20:23],0 offen\nv_lshlrev_b32 v120,1,v120\nv_lshl_add_u32 v123,v122,3,v121\n" \
"buffer_load_ushort v116,v120,s[20:23],0 offen offset:1024\nv_add_u32_e64 v121,v121,v122\nbuffer_load_dwordx4 v[16:19],v123,s[24:27],0 offen\ns_mov_b32 s95,6912\n" \
"buffer_load_dwordx4 v[20:23],v123,s[24:27],s95 offen\ns_mov_b32 s95,13824\nbuffer_load_dwordx4 v[24:27],v123,s[24:27],s95 offen\ns_mov_b32 s95,20736\n" \
"buffer_load_dwordx4 v[28:31],v123,s[24:27],s95 offen\ns_mov_b32 s95,27648\nbuffer_load_dwordx4 v[32:35],v123,s[24:27],s95 offen\ns_mov_b32 s95,34560\n" \
"buffer_load_dwordx4 v[36:39],v123,s[24:27],s95 offen\ns_mov_b32 s95,41472\nbuffer_load_dwordx4 v[40:43],v123,s[24:27],s95 offen\ns_mov_b32 s95,48384\n" \
"buffer_load_dwordx4 v[44:47],v123,s[24:27],s95 offen\ns_mov_b32 s95,3538944\nbuffer_load_dwordx4 v[48:51],v121,s[24:27],s95 offen\ns_lshr_b32 s26,s32,1\n" \
"s_cmp_eq_u32 s26,2\ns_cselect_b32 s26,1.0,0\nv_cmp_gt_u32 vcc,8,v0\nv_mov_b32 v100,s28\n" \
"v_mov_b32 v101,s29\ns_waitcnt vmcnt(10)\nv_lshlrev_b32 v52,16,v112\nv_mul_f32 v53,0xbfb8aa3b,v52\n" \
"v_exp_f32 v53,v53\nv_and_b32 v54,0xffff0000,v112\nv_add_f32 v53,1.0,v53\nv_mul_f32 v55,0xbfb8aa3b,v54\n" \
"v_lshlrev_b32 v56,16,v113\nv_rcp_f32 v53,v53\nv_exp_f32 v55,v55\nv_mul_f32 v57,0xbfb8aa3b,v56\n" \
"v_exp_f32 v57,v57\nv_mul_f32 v52,v52,v53\nv_add_f32 v55,1.0,v55\nv_and_b32 v58,0xffff0000,v113\n" \
"v_cvt_pk_bf16_f32 v52,v52,v52\nv_rcp_f32 v55,v55\nv_add_f32 v57,1.0,v57\nv_mul_f32 v59,0xbfb8aa3b,v58\n" \
"v_lshlrev_b32 v52,16,v52\nv_lshlrev_b32 v60,16,v114\nv_rcp_f32 v57,v57\nv_exp_f32 v59,v59\n" \
"v_mul_f32 v61,0xbfb8aa3b,v60\nv_mul_f32 v54,v54,v55\nv_exp_f32 v61,v61\nv_cvt_pk_bf16_f32 v54,v54,v54\n" \
"v_mul_f32 v56,v56,v57\nv_add_f32 v59,1.0,v59\nv_and_b32 v62,0xffff0000,v114\nv_lshlrev_b32 v54,16,v54\n" \
"v_cvt_pk_bf16_f32 v56,v56,v56\nv_rcp_f32 v59,v59\nv_add_f32 v61,1.0,v61\nv_mul_f32 v63,0xbfb8aa3b,v62\n" \
"v_lshlrev_b32 v56,16,v56\nv_lshlrev_b32 v64,16,v115\nv_rcp_f32 v61,v61\nv_exp_f32 v63,v63\n" \
"v_mul_f32 v65,0xbfb8aa3b,v64\nv_mul_f32 v58,v58,v59\nv_exp_f32 v65,v65\nv_cvt_pk_bf16_f32 v58,v58,v58\n" \
"v_mul_f32 v60,v60,v61\nv_add_f32 v63,1.0,v63\nv_and_b32 v66,0xffff0000,v115\nv_lshlrev_b32 v58,16,v58\n" \
"v_cvt_pk_bf16_f32 v60,v60,v60\nv_rcp_f32 v63,v63\nv_add_f32 v65,1.0,v65\nv_mul_f32 v67,0xbfb8aa3b,v66\n" \
"v_lshlrev_b32 v60,16,v60\ns_waitcnt vmcnt(9)\nv_lshlrev_b32 v68,16,v116\nv_rcp_f32 v65,v65\n" \
"v_exp_f32 v67,v67\nv_mul_f32 v69,0xbfb8aa3b,v68\nv_mul_f32 v62,v62,v63\nv_exp_f32 v69,v69\n" \
"v_cvt_pk_bf16_f32 v62,v62,v62\nv_mul_f32 v64,v64,v65\nv_add_f32 v67,1.0,v67\nv_lshlrev_b32 v62,16,v62\n" \
"v_cvt_pk_bf16_f32 v64,v64,v64\nv_rcp_f32 v67,v67\nv_add_f32 v69,1.0,v69\nv_lshlrev_b32 v64,16,v64\n" \
"v_rcp_f32 v69,v69\nv_mul_f32 v66,v66,v67\nv_cvt_pk_bf16_f32 v66,v66,v66\nv_mul_f32 v68,v68,v69\n" \
"v_lshlrev_b32 v66,16,v66\nv_cvt_pk_bf16_f32 v68,v68,v68\nv_lshlrev_b32 v68,16,v68\ns_waitcnt vmcnt(8)\n" \
"v_and_b32 v81,0xffff0000,v16\nv_and_b32 v83,0xffff0000,v17\nv_lshlrev_b32 v80,16,v16\nv_lshlrev_b32_e64 v82,16,v17\n" \
"v_pk_mul_f32 v[70:71],v[80:81],v[52:53] op_sel_hi:[1,0]\nv_pk_mul_f32 v[72:73],v[82:83],v[52:53] op_sel_hi:[1,0]\nv_and_b32 v81,0xffff0000,v18\nv_and_b32 v83,0xffff0000,v19\n" \
"v_lshlrev_b32 v80,16,v18\nv_lshlrev_b32 v82,16,v19\nv_pk_mul_f32 v[74:75],v[80:81],v[52:53] op_sel_hi:[1,0]\nv_pk_mul_f32 v[76:77],v[82:83],v[52:53] op_sel_hi:[1,0]\n" \
"s_waitcnt vmcnt(7)\nv_and_b32 v85,0xffff0000,v20\nv_and_b32 v87,0xffff0000,v21\nv_lshlrev_b32 v84,16,v20\n" \
"v_lshlrev_b32 v86,16,v21\nv_pk_fma_f32 v[70:71],v[84:85],v[54:55],v[70:71] op_sel_hi:[1,0,1]\nv_pk_fma_f32 v[72:73],v[86:87],v[54:55],v[72:73] op_sel_hi:[1,0,1]\ns_waitcnt vmcnt(6)\n" \
"v_and_b32 v81,0xffff0000,v24\nv_and_b32 v83,0xffff0000,v25\nv_lshlrev_b32 v80,16,v24\nv_lshlrev_b32 v82,16,v25\n" \
"v_and_b32 v85,0xffff0000,v22\nv_and_b32 v87,0xffff0000,v23\nv_pk_fma_f32 v[70:71],v[80:81],v[56:57],v[70:71] op_sel_hi:[1,0,1]\nv_pk_fma_f32 v[72:73],v[82:83],v[56:57],v[72:73] op_sel_hi:[1,0,1]\n" \
"v_lshlrev_b32 v84,16,v22\nv_lshlrev_b32 v86,16,v23\nv_pk_fma_f32 v[74:75],v[84:85],v[54:55],v[74:75] op_sel_hi:[1,0,1]\nv_pk_fma_f32 v[76:77],v[86:87],v[54:55],v[76:77] op_sel_hi:[1,0,1]\n" \
"v_and_b32 v81,0xffff0000,v26\nv_and_b32 v83,0xffff0000,v27\nv_lshlrev_b32 v80,16,v26\nv_lshlrev_b32 v82,16,v27\n" \
"v_pk_fma_f32 v[74:75],v[80:81],v[56:57],v[74:75] op_sel_hi:[1,0,1]\nv_pk_fma_f32 v[76:77],v[82:83],v[56:57],v[76:77] op_sel_hi:[1,0,1]\ns_waitcnt vmcnt(5)\nv_and_b32 v85,0xffff0000,v28\n" \
"v_and_b32 v87,0xffff0000,v29\nv_lshlrev_b32 v84,16,v28\nv_lshlrev_b32 v86,16,v29\nv_pk_fma_f32 v[70:71],v[84:85],v[58:59],v[70:71] op_sel_hi:[1,0,1]\n" \
"v_pk_fma_f32 v[72:73],v[86:87],v[58:59],v[72:73] op_sel_hi:[1,0,1]\ns_waitcnt vmcnt(4)\nv_and_b32 v81,0xffff0000,v32\nv_and_b32 v83,0xffff0000,v33\n" \
"v_lshlrev_b32 v80,16,v32\nv_lshlrev_b32 v82,16,v33\nv_and_b32 v85,0xffff0000,v30\nv_and_b32 v87,0xffff0000,v31\n" \
"v_pk_fma_f32 v[70:71],v[80:81],v[60:61],v[70:71] op_sel_hi:[1,0,1]\nv_pk_fma_f32 v[72:73],v[82:83],v[60:61],v[72:73] op_sel_hi:[1,0,1]\nv_lshlrev_b32 v84,16,v30\nv_lshlrev_b32 v86,16,v31\n" \
"v_pk_fma_f32 v[74:75],v[84:85],v[58:59],v[74:75] op_sel_hi:[1,0,1]\nv_pk_fma_f32 v[76:77],v[86:87],v[58:59],v[76:77] op_sel_hi:[1,0,1]\nv_and_b32 v81,0xffff0000,v34\nv_and_b32 v83,0xffff0000,v35\n" \
"v_lshlrev_b32 v80,16,v34\nv_lshlrev_b32 v82,16,v35\nv_pk_fma_f32 v[74:75],v[80:81],v[60:61],v[74:75] op_sel_hi:[1,0,1]\nv_pk_fma_f32 v[76:77],v[82:83],v[60:61],v[76:77] op_sel_hi:[1,0,1]\n" \
"s_waitcnt vmcnt(3)\nv_and_b32 v85,0xffff0000,v36\nv_and_b32 v87,0xffff0000,v37\nv_lshlrev_b32 v84,16,v36\n" \
"v_lshlrev_b32 v86,16,v37\nv_pk_fma_f32 v[70:71],v[84:85],v[62:63],v[70:71] op_sel_hi:[1,0,1]\nv_pk_fma_f32 v[72:73],v[86:87],v[62:63],v[72:73] op_sel_hi:[1,0,1]\ns_waitcnt vmcnt(2)\n" \
"v_and_b32 v81,0xffff0000,v40\nv_and_b32 v83,0xffff0000,v41\nv_lshlrev_b32 v80,16,v40\nv_lshlrev_b32 v82,16,v41\n" \
"v_and_b32 v85,0xffff0000,v38\nv_and_b32 v87,0xffff0000,v39\nv_pk_fma_f32 v[70:71],v[80:81],v[64:65],v[70:71] op_sel_hi:[1,0,1]\nv_pk_fma_f32 v[72:73],v[82:83],v[64:65],v[72:73] op_sel_hi:[1,0,1]\n" \
"v_lshlrev_b32 v84,16,v38\nv_lshlrev_b32 v86,16,v39\nv_pk_fma_f32 v[74:75],v[84:85],v[62:63],v[74:75] op_sel_hi:[1,0,1]\nv_pk_fma_f32 v[76:77],v[86:87],v[62:63],v[76:77] op_sel_hi:[1,0,1]\n" \
"v_and_b32 v81,0xffff0000,v42\nv_and_b32 v83,0xffff0000,v43\nv_lshlrev_b32 v80,16,v42\nv_lshlrev_b32 v82,16,v43\n" \
"v_pk_fma_f32 v[74:75],v[80:81],v[64:65],v[74:75] op_sel_hi:[1,0,1]\nv_pk_fma_f32 v[76:77],v[82:83],v[64:65],v[76:77] op_sel_hi:[1,0,1]\ns_waitcnt vmcnt(1)\nv_and_b32 v85,0xffff0000,v44\n" \
"v_and_b32 v87,0xffff0000,v45\nv_lshlrev_b32 v84,16,v44\nv_lshlrev_b32 v86,16,v45\nv_pk_fma_f32 v[70:71],v[84:85],v[66:67],v[70:71] op_sel_hi:[1,0,1]\n" \
"v_pk_fma_f32 v[72:73],v[86:87],v[66:67],v[72:73] op_sel_hi:[1,0,1]\ns_waitcnt vmcnt(0)\nv_and_b32 v81,0xffff0000,v48\nv_and_b32 v83,0xffff0000,v49\n" \
"v_lshlrev_b32 v80,16,v48\nv_lshlrev_b32 v82,16,v49\nv_and_b32 v85,0xffff0000,v46\nv_and_b32 v87,0xffff0000,v47\n" \
"v_pk_fma_f32 v[70:71],v[80:81],v[68:69],v[70:71] op_sel_hi:[1,0,1]\nv_pk_fma_f32 v[72:73],v[82:83],v[68:69],v[72:73] op_sel_hi:[1,0,1]\nv_lshlrev_b32 v84,16,v46\nv_lshlrev_b32 v86,16,v47\n" \
"v_pk_fma_f32 v[74:75],v[84:85],v[66:67],v[74:75] op_sel_hi:[1,0,1]\nv_pk_fma_f32 v[76:77],v[86:87],v[66:67],v[76:77] op_sel_hi:[1,0,1]\nv_and_b32 v81,0xffff0000,v50\nv_and_b32 v83,0xffff0000,v51\n" \
"v_lshlrev_b32 v80,16,v50\nv_lshlrev_b32 v82,16,v51\nv_pk_fma_f32 v[74:75],v[80:81],v[68:69],v[74:75] op_sel_hi:[1,0,1]\nv_pk_fma_f32 v[76:77],v[82:83],v[68:69],v[76:77] op_sel_hi:[1,0,1]\n" \
"v_and_b32 v80,3,v0\nv_permlane32_swap_b32 v70,v74\nv_permlane32_swap_b32 v71,v75\nv_permlane32_swap_b32 v72,v76\n" \
"v_permlane32_swap_b32 v73,v77\nv_pk_add_f32 v[70:71],v[70:71],v[74:75]\nv_pk_add_f32 v[72:73],v[72:73],v[76:77]\nv_lshrrev_b32 v81,4,v0\n" \
"v_and_b32 v82,12,v81\nv_permlane16_swap_b32 v70,v72\nv_permlane16_swap_b32 v71,v73\nv_lshlrev_b32 v80,5,v80\n" \
"v_pk_add_f32 v[70:71],v[70:71],v[72:73]\nv_mad_u32_u24 v80,v82,24,v80\nv_lshl_add_u32 v80,v81,3,v80\nv_add_f32_dpp v70,v70,v70 row_ror:8 row_mask:0xf bank_mask:0xf bound_ctrl:1\n" \
"v_add_f32_dpp v71,v71,v71 row_ror:8 row_mask:0xf bank_mask:0xf bound_ctrl:1\nv_add_u32 v80,161152,v80\nv_add_f32_dpp v70,v70,v70 row_ror:4 row_mask:0xf bank_mask:0xf bound_ctrl:1\nv_add_f32_dpp v71,v71,v71 row_ror:4 row_mask:0xf bank_mask:0xf bound_ctrl:1\n" \
"ds_write_b64 v80,v[70:71]\nv_and_b32 v80,7,v0\nv_lshlrev_b32 v80,4,v80\nv_add_u32 v80,161152,v80\n" \
"v_bfrev_b32 v81,1\nv_cndmask_b32 v81,v81,v2,vcc\ns_waitcnt lgkmcnt(0)\ns_barrier\n" \
"ds_read_b128 v[84:87],v80\nds_read_b128 v[88:91],v80 offset:128\nds_read_b128 v[92:95],v80 offset:256\nds_read_b128 v[96:99],v80 offset:384\n" \
"s_waitcnt lgkmcnt(2)\nv_pk_add_f32 v[84:85],v[84:85],v[88:89]\nv_pk_add_f32 v[86:87],v[86:87],v[90:91]\ns_waitcnt lgkmcnt(0)\n" \
"v_pk_add_f32 v[92:93],v[92:93],v[96:97]\nv_pk_add_f32 v[94:95],v[94:95],v[98:99]\nv_pk_add_f32 v[84:85],v[84:85],v[92:93]\nv_pk_add_f32 v[86:87],v[86:87],v[94:95]\n" \
"v_cvt_pk_bf16_f32 v88,v84,v85\nv_cvt_pk_bf16_f32 v90,v86,v87\nv_and_b32 v85,0xffff0000,v88\nv_and_b32 v87,0xffff0000,v90\n" \
"v_lshlrev_b32 v84,16,v88\nv_lshlrev_b32 v86,16,v90\nv_add_f32 v84,s26,v84\nv_add_f32 v85,s26,v85\n" \
"v_add_f32 v86,s26,v86\nv_add_f32 v87,s26,v87\ns_lshr_b32 s26,s86,1\nv_cvt_pk_bf16_f32 v102,v84,v85\n" \
"v_cvt_pk_bf16_f32 v103,v86,v87\ns_add_u32 s26,s26,26624\nbuffer_store_dwordx4 v[100:103],v81,s[36:39],s26 offen sc1\ns_and_b32 s22,s33,7\n" \
"s_and_b32 s24,s32,1\ns_lshl3_add_u32 s22,s24,s22\ns_bfe_u32 s24,s33,0x10002\ns_lshl1_add_u32 s24,s32,s24\n" \
"s_mul_i32 s19,s24,18\ns_and_b32 s24,s33,3\ns_lshl2_add_u32 s23,s24,s19\ns_lshl_b32 s24,s3,5\n" \
"s_lshr_b32 s20,s33,3\ns_lshr_b32 s21,s32,1\ns_add_u32 s54,s14,s24\ns_lshl2_add_u32 s24,s20,s21\n" \
"s_add_u32 s23,s23,s3\ns_lshl2_add_u32 s24,s24,s3\ns_add_u32 s23,s23,1\ns_lshl_b32 s24,s24,9\n" \
"s_lshl_b32 s23,s23,9\nv_bfe_u32 v127,v0,3,3\nv_mul_u32_u24 v127,8192,v127\nv_lshl_add_u32 v127,v1,3,v127\n" \
"s_lshl2_add_u32 s95,s33,s3\ns_add_u32 s95,s95,190\ns_and_b32 s100,s95,31\ns_lshl_b32 s100,s100,3\n" \
"s_cmp_lt_u32 s95,256\ns_cselect_b32 s95,s95,0x40000\ns_lshr_b32 s95,s95,5\ns_add_u32 s95,s95,s100\n" \
"s_lshl_b32 s95,s95,6\ns_add_u32 s95,s95,s32\ns_mul_i32 s95,s95,1152\ns_mov_b32 m0,162176\n" \
"s_add_u32 s101,s92,0\nbuffer_load_dword v127,s[8:11],s95 offen lds\ns_add_u32 s24,s24,262144\ns_sub_u32 s59,s24,s23\n" \
"s_lshr_b32 s25,s3,1\ns_mul_i32 s24,s21,1152\ns_mul_i32 s25,s25,576\ns_add_u32 s24,s24,s25\n" \
"s_lshl4_add_u32 s24,s24,s22\ns_and_b32 s25,s3,1\ns_lshl_b32 s24,s24,8\ns_lshl_b32 s65,s25,17\n" \
"s_add_u32 s65,s65,s24\ns_lshl_b32 s25,s25,20\ns_add_u32 s24,s24,s25\ns_add_u32 s60,s24,655488\n" \
"s_add_u32 s61,s24,786432\ns_add_u32 s62,s24,786560\ns_add_u32 m0,s57,0\ns_add_u32 s31,s92,9437184\n" \
"buffer_load_dwordx4 v4,s[8:11],s101 offen lds\ns_add_u32 s63,s24,917504\ns_add_u32 s64,s24,917632\ns_mul_i32 s52,s20,4505600\n" \
"s_lshl2_add_u32 s24,s21,s3\ns_add_u32 s52,s52,12123648\ns_mul_i32 s24,s24,9216\ns_add_u32 s24,s24,s52\n" \
"s_add_u32 s68,s24,819712\ns_lshl_b32 s24,s22,14\nbuffer_load_dwordx4 v5,s[8:11],s101 offen offset:1024 lds\ns_add_u32 s24,s24,s52\n" \
"s_lshl2_add_u32 s25,s21,s3\ns_add_u32 s24,s24,967168\ns_lshl_b32 s25,s25,8\ns_add_u32 s79,s25,s24\n" \
"s_add_u32 s25,s21,4\ns_and_b32 s25,s25,3\ns_add_u32 s24,s24,s12\ns_lshl_b32 s25,s25,12\n" \
"buffer_load_dwordx4 v6,s[8:11],s101 offen offset:2048 lds\ns_add_u32 s25,s25,s24\ns_sub_u32 s74,s25,819200\ns_add_u32 s25,s21,3\n" \
"s_and_b32 s25,s25,3\ns_lshl_b32 s25,s25,12\ns_add_u32 s25,s25,s24\ns_sub_u32 s71,s25,2048000\n" \
"s_sub_u32 s76,s25,1638400\ns_add_u32 s25,s21,2\nbuffer_load_dwordx4 v7,s[8:11],s101 offen offset:3072 lds\ns_and_b32 s25,s25,3\n" \
"s_lshl_b32 s25,s25,12\ns_add_u32 s25,s25,s24\ns_sub_u32 s72,s25,1638400\ns_sub_u32 s77,s25,1638400\n" \
"s_add_u32 s25,s21,1\ns_and_b32 s25,s25,3\ns_lshl_b32 s25,s25,12\ns_add_u32 s25,s25,s24\n" \
"s_add_u32 m0,s57,4096\ns_add_u32 s96,s92,73728\nbuffer_load_dwordx4 v4,s[8:11],s31 offen lds\ns_sub_u32 s73,s25,1638400\n" \
"s_sub_u32 s78,s25,1228800\ns_lshl2_add_u32 s26,s33,s3\ns_and_b32 s24,s26,15\ns_and_b32 s25,s26,8\n" \
"s_lshl2_add_u32 s24,s24,192\ns_mul_i32 s25,s25,28\ns_add_u32 s25,s25,s24\ns_sub_u32 s25,s25,32\n" \
"buffer_load_dwordx4 v5,s[8:11],s31 offen offset:1024 lds\ns_bitcmp1_b32 s26,4\ns_cselect_b32 s24,s25,s24\ns_and_b32 s25,s26,31\n" \
"s_lshl2_add_u32 s25,s25,448\ns_bitcmp1_b32 s26,5\ns_cselect_b32 s24,s25,s24\ns_lshr_b32 s25,s26,6\n" \
"s_lshl1_add_u32 s25,s21,s25\ns_mul_i32 s25,s25,576\nbuffer_load_dwordx4 v6,s[8:11],s31 offen offset:2048 lds\ns_add_u32 s24,s24,s25\n" \
"s_and_b32 s25,s32,1\ns_lshl1_add_u32 s24,s24,s25\nv_and_b32 v112,3,v0\ns_lshl_b32 s24,s24,11\n" \
"s_and_b32 s25,s33,3\ns_mul_i32 s27,s3,4896\nv_and_b32 v113,12,v0\nv_lshlrev_b32 v112,2,v112\n" \
"buffer_load_dwordx4 v7,s[8:11],s31 offen offset:3072 lds\ns_add_u32 s24,s24,s25\ns_add_u32 s27,s27,9216\nv_max_u32 v112,v112,v113\n" \
"s_mul_i32 s48,s20,589824\ns_add_u32 s89,s24,1\nv_mov_b32 v114,1048576\nv_mov_b32 v113,s27\n" \
"v_cmp_gt_u32 vcc,12,v112\ns_add_u32 m0,s57,8192\ns_add_u32 s97,s92,9510912\nbuffer_load_dwordx4 v4,s[8:11],s96 offen lds\n" \
"s_lshl4_add_u32 s48,s12,s48\ns_lshl_b32 s86,s21,11\ns_lshl2_add_u32 s24,s2,s3\nv_and_b32 v63,0xffff0000,v224\n" \
"v_and_b32 v65,0xffff0000,v225\nv_and_b32 v67,0xffff0000,v226\nv_and_b32 v69,0xffff0000,v227\nv_and_b32 v71,0xffff0000,v228\n" \
"buffer_load_dwordx4 v5,s[8:11],s96 offen offset:1024 lds\nv_and_b32 v73,0xffff0000,v229\nv_and_b32 v75,0xffff0000,v230\nv_and_b32 v77,0xffff0000,v231\n" \
"v_and_b32 v79,0xffff0000,v232\nv_cndmask_b32 v112,v114,v113,vcc\nv_lshlrev_b32 v62,16,v224\nv_lshlrev_b32 v64,16,v225\n" \
"v_lshlrev_b32 v66,16,v226\nbuffer_load_dwordx4 v6,s[8:11],s96 offen offset:2048 lds\nv_lshlrev_b32 v68,16,v227\nv_lshlrev_b32 v70,16,v228\n" \
"v_lshlrev_b32 v72,16,v229\nv_lshlrev_b32 v74,16,v230\nv_lshlrev_b32 v76,16,v231\nv_lshlrev_b32 v78,16,v232\n" \
"s_add_u32 s48,s48,9764864\ns_add_u32 s65,s65,2097152\ns_lshl_b32 s67,s21,7\nbuffer_load_dwordx4 v7,s[8:11],s96 offen offset:3072 lds\n" \
"s_lshl_b32 s85,s21,17\ns_sub_u32 s86,s14,s86\ns_lshl_b32 s24,s24,8\nv_pk_add_f32 v[62:63],v[62:63],v[250:251] op_sel_hi:[1,0] neg_lo:[0,1] neg_hi:[0,1]\n" \
"v_pk_add_f32 v[64:65],v[64:65],v[250:251] op_sel_hi:[1,0] neg_lo:[0,1] neg_hi:[0,1]\nv_pk_add_f32 v[66:67],v[66:67],v[250:251] op_sel_hi:[1,0] neg_lo:[0,1] neg_hi:[0,1]\nv_pk_add_f32 v[68:69],v[68:69],v[250:251] op_sel_hi:[1,0] neg_lo:[0,1] neg_hi:[0,1]\nv_pk_add_f32 v[70:71],v[70:71],v[250:251] op_sel_hi:[1,0] neg_lo:[0,1] neg_hi:[0,1]\n" \
"s_add_u32 m0,s57,12288\ns_add_u32 s100,s92,9584640\nbuffer_load_dwordx4 v4,s[8:11],s97 offen lds\nv_pk_add_f32 v[72:73],v[72:73],v[250:251] op_sel_hi:[1,0] neg_lo:[0,1] neg_hi:[0,1]\n" \
"v_pk_add_f32 v[74:75],v[74:75],v[250:251] op_sel_hi:[1,0] neg_lo:[0,1] neg_hi:[0,1]\nv_pk_add_f32 v[76:77],v[76:77],v[250:251] op_sel_hi:[1,0] neg_lo:[0,1] neg_hi:[0,1]\nv_pk_add_f32 v[78:79],v[78:79],v[250:251] op_sel_hi:[1,0] neg_lo:[0,1] neg_hi:[0,1]\nv_mov_b64 v[16:17],v[192:193]\n" \
"v_mov_b64 v[18:19],v[194:195]\nv_mov_b64 v[20:21],v[196:197]\nv_mov_b64 v[22:23],v[198:199]\nbuffer_load_dwordx4 v5,s[8:11],s97 offen offset:1024 lds\n" \
"v_mov_b64 v[24:25],v[208:209]\nv_mov_b64 v[26:27],v[210:211]\nv_mov_b64 v[28:29],v[212:213]\nv_mov_b64 v[30:31],v[214:215]\n" \
"v_mov_b64 v[32:33],v[216:217]\nv_mov_b64 v[34:35],v[218:219]\nv_mov_b64 v[36:37],v[220:221]\nv_mov_b64 v[38:39],v[222:223]\n" \
"v_mov_b64 v[40:41],v[200:201]\nbuffer_load_dwordx4 v6,s[8:11],s97 offen offset:2048 lds\nv_mov_b64 v[42:43],v[202:203]\nv_mov_b64 v[44:45],v[204:205]\n" \
"v_mov_b64 v[46:47],v[206:207]\nv_mov_b64 v[56:57],v[242:243]\nv_mov_b64 v[58:59],v[244:245]\nv_mov_b32 v60,v249\n" \
"v_mov_b32 v61,v250\nv_add_u32 v48,v234,v112\nv_add_u32 v49,v235,v112\nbuffer_load_dwordx4 v7,s[8:11],s97 offen offset:3072 lds\n" \
"v_add_u32 v50,v236,v112\nv_add_u32 v51,v237,v112\nv_add_u32 v52,v238,v112\nv_add_u32 v53,v239,v112\n" \
"v_add_u32 v54,v240,v112\nv_add_u32 v55,v241,v112\ns_add_u32 s49,s48,4096\ns_add_u32 s50,s48,8192\n" \
"s_add_u32 s51,s48,12288\ns_add_u32 m0,s57,16384\ns_add_u32 s53,s52,3276800\ns_add_u32 s83,s52,2048000\n" \
"s_mov_b32 s55,0xbdd2d3e7\ns_mul_i32 s56,s3,9568\ns_lshl_b32 s58,s3,13\ns_add_u32 s75,s59,196608\n" \
"s_add_u32 s66,s65,128\ns_add_u32 s67,s67,163200\ns_add_u32 s69,s68,3072\ns_add_u32 s70,s68,6144\n" \
"s_add_u32 s80,s79,4096\ns_add_u32 s81,s79,8192\ns_add_u32 s82,s79,12288\ns_and_b32 s84,s33,1\n" \
"s_add_u32 s85,s85,65536\ns_add_u32 s87,s12,65536\ns_add_u32 s88,s74,819200\ns_add_u32 s86,s86,0x10000\n" \
"s_mov_b32 s90,7405568\ns_add_u32 s91,s24,69206016\ns_sub_u32 s98,s57,s12\ns_mov_b32 s99,s12\n" \
"s_add_u32 s95,s92,147456\nbuffer_load_dwordx4 v4,s[8:11],s95 offen lds\ns_add_u32 s101,s92,221184\nbuffer_load_dwordx4 v5,s[8:11],s95 offen offset:1024 lds\n" \
"buffer_load_dwordx4 v6,s[8:11],s95 offen offset:2048 lds\nbuffer_load_dwordx4 v7,s[8:11],s95 offen offset:3072 lds\ns_add_u32 m0,s57,20480\ns_nop 0\n" \
"buffer_load_dwordx4 v4,s[8:11],s100 offen lds\nbuffer_load_dwordx4 v5,s[8:11],s100 offen offset:1024 lds\nbuffer_load_dwordx4 v6,s[8:11],s100 offen offset:2048 lds\nbuffer_load_dwordx4 v7,s[8:11],s100 offen offset:3072 lds\n" \
"s_add_u32 m0,s57,24576\ns_nop 0\nbuffer_load_dwordx4 v4,s[8:11],s101 offen lds\nbuffer_load_dwordx4 v5,s[8:11],s101 offen offset:1024 lds\n" \
"buffer_load_dwordx4 v6,s[8:11],s101 offen offset:2048 lds\nbuffer_load_dwordx4 v7,s[8:11],s101 offen offset:3072 lds\nbuffer_load_dwordx4 v[104:107],v1,s[36:39],s7 offen sc1\nbuffer_load_dwordx4 v[108:111],v1,s[36:39],s7 offen offset:1024 sc1\n" \
"s_waitcnt vmcnt(1)\nv_xor_b32 v112,s29,v105\nv_xor_b32 v113,s29,v107\ns_waitcnt vmcnt(0)\n" \
"v_xor_b32 v114,s29,v109\nv_xor_b32 v115,s29,v111\nv_or3_b32 v112,v112,v113,v114\nv_xor_b32 v113,s28,v104\n" \
"v_xor_b32 v114,s28,v106\nv_or_b32 v112,v112,v115\nv_xor_b32 v115,s28,v108\nv_lshl_or_b32 v112,v112,8,v113\n" \
"v_xor_b32 v113,s28,v110\nv_or3_b32 v112,v112,v114,v115\nv_or_b32 v112,v112,v113\nv_cmp_ne_u32 vcc,0,v112\n" \
"s_nop 0\ns_cbranch_vccnz .LsrP_%=\n.LbrP_%=:\nv_min_u32 v120,2288,v2\n" \
"s_mov_b32 s5,26624\nv_max3_u32 v112,v105,v107,v109\nv_max_u32 v112,v112,v111\ns_mov_b32 s6,31232\n" \
"v_or_b32 v102,s2,v0\nv_max_u32_dpp v112,v112,v112 quad_perm:[1,0,3,2] row_mask:0xf bank_mask:0xf bound_ctrl:1\nv_bfrev_b32 v103,1\nv_cmp_eq_u32 vcc,0,v102\n" \
"v_max_u32_dpp v112,v112,v112 quad_perm:[2,3,0,1] row_mask:0xf bank_mask:0xf bound_ctrl:1\nv_mov_b32 v100,s28\nv_cndmask_b32 v102,v103,v102,vcc\nv_max_u32_dpp v112,v112,v112 row_half_mirror row_mask:0xf bank_mask:0xf bound_ctrl:1\n" \
"v_mov_b32 v101,s29\nbuffer_store_dwordx2 v[100:101],v102,s[36:39],0 offen sc1\nv_max_u32_dpp v112,v112,v112 row_mirror row_mask:0xf bank_mask:0xf bound_ctrl:1\nds_read_b64_tr_b16 a[0:1],v8\n" \
"v_readlane_b32 s94,v112,0\nv_readlane_b32 s31,v112,16\nv_readlane_b32 s96,v112,32\nv_readlane_b32 s97,v112,48\n" \
"s_max_u32 s31,s31,s96\ns_max_u32 s94,s94,s97\ns_max_u32 s94,s94,s31\ns_lshr_b32 s94,s94,24\n" \
"s_max_u32 s94,s94,64\ns_min_u32 s94,s94,250\nds_read_b64_tr_b16 a[4:5],v9\nds_read_b64_tr_b16 a[8:9],v10\n" \
"ds_read_b64_tr_b16 a[12:13],v11\nds_read_b64_tr_b16 a[16:17],v8 offset:2048\nds_read_b64_tr_b16 a[20:21],v9 offset:2048\nds_read_b64_tr_b16 a[24:25],v10 offset:2048\n" \
"ds_read_b64_tr_b16 a[28:29],v11 offset:2048\ns_add_u32 s95,s92,9658368\ns_add_u32 m0,s57,0\nds_read_b64_tr_b16 a[2:3],v8 offset:4096\n" \
"s_waitcnt lgkmcnt(1)\nbuffer_load_dwordx4 v4,s[8:11],s95 offen lds\nbuffer_load_dwordx4 v5,s[8:11],s95 offen offset:1024 lds\nbuffer_load_dwordx4 v6,s[8:11],s95 offen offset:2048 lds\n" \
"buffer_load_dwordx4 v7,s[8:11],s95 offen offset:3072 lds\nds_read_b64_tr_b16 a[6:7],v9 offset:4096\nds_read_b64_tr_b16 a[10:11],v10 offset:4096\nds_read_b64_tr_b16 a[14:15],v11 offset:4096\n" \
"ds_read_b64_tr_b16 a[18:19],v8 offset:6144\nds_read_b64_tr_b16 a[22:23],v9 offset:6144\nds_read_b64_tr_b16 a[26:27],v10 offset:6144\nds_read_b64_tr_b16 a[30:31],v11 offset:6144\n" \
"s_add_u32 s100,s92,294912\ns_add_u32 m0,s57,4096\nds_read_b64_tr_b16 a[32:33],v8 offset:8192\ns_waitcnt lgkmcnt(1)\n" \
"buffer_load_dwordx4 v4,s[8:11],s100 offen lds\nbuffer_load_dwordx4 v5,s[8:11],s100 offen offset:1024 lds\nbuffer_load_dwordx4 v6,s[8:11],s100 offen offset:2048 lds\nbuffer_load_dwordx4 v7,s[8:11],s100 offen offset:3072 lds\n" \
"ds_read_b64_tr_b16 a[36:37],v9 offset:8192\nds_read_b64_tr_b16 a[40:41],v10 offset:8192\nds_read_b64_tr_b16 a[44:45],v11 offset:8192\nds_read_b64_tr_b16 a[48:49],v8 offset:10240\n" \
"ds_read_b64_tr_b16 a[52:53],v9 offset:10240\nds_read_b64_tr_b16 a[56:57],v10 offset:10240\nds_read_b64_tr_b16 a[60:61],v11 offset:10240\ns_add_u32 s101,s92,9732096\n" \
"s_add_u32 m0,s57,8192\nds_read_b64_tr_b16 a[34:35],v8 offset:12288\ns_waitcnt lgkmcnt(1)\nbuffer_load_dwordx4 v4,s[8:11],s101 offen lds\n" \
"buffer_load_dwordx4 v5,s[8:11],s101 offen offset:1024 lds\nbuffer_load_dwordx4 v6,s[8:11],s101 offen offset:2048 lds\nbuffer_load_dwordx4 v7,s[8:11],s101 offen offset:3072 lds\nds_read_b64_tr_b16 a[38:39],v9 offset:12288\n" \
"ds_read_b64_tr_b16 a[42:43],v10 offset:12288\nds_read_b64_tr_b16 a[46:47],v11 offset:12288\nds_read_b64_tr_b16 a[50:51],v8 offset:14336\nds_read_b64_tr_b16 a[54:55],v9 offset:14336\n" \
"ds_read_b64_tr_b16 a[58:59],v10 offset:14336\nds_read_b64_tr_b16 a[62:63],v11 offset:14336\ns_add_u32 s31,s92,368640\ns_add_u32 m0,s57,12288\n" \
"ds_read_b64_tr_b16 a[64:65],v8 offset:16384\ns_waitcnt lgkmcnt(1)\nbuffer_load_dwordx4 v4,s[8:11],s31 offen lds\nbuffer_load_dwordx4 v5,s[8:11],s31 offen offset:1024 lds\n" \
"buffer_load_dwordx4 v6,s[8:11],s31 offen offset:2048 lds\nbuffer_load_dwordx4 v7,s[8:11],s31 offen offset:3072 lds\nbuffer_load_dwordx4 v[80:83],v120,s[36:39],s5 offen sc1\nbuffer_load_dwordx4 v[84:87],v120,s[36:39],s5 offen offset:2304 sc1\n" \
"buffer_load_dwordx4 v[88:91],v120,s[36:39],s6 offen sc1\nbuffer_load_dwordx4 v[92:95],v120,s[36:39],s6 offen offset:2304 sc1\nds_read_b64_tr_b16 a[68:69],v9 offset:16384\nds_read_b64_tr_b16 a[72:73],v10 offset:16384\n" \
"ds_read_b64_tr_b16 a[76:77],v11 offset:16384\nds_read_b64_tr_b16 a[80:81],v8 offset:18432\nds_read_b64_tr_b16 a[84:85],v9 offset:18432\nds_read_b64_tr_b16 a[88:89],v10 offset:18432\n" \
"ds_read_b64_tr_b16 a[92:93],v11 offset:18432\ns_add_u32 s96,s92,9805824\ns_add_u32 m0,s57,16384\nds_read_b64_tr_b16 a[66:67],v8 offset:20480\n" \
"s_waitcnt lgkmcnt(1)\nbuffer_load_dwordx4 v4,s[8:11],s96 offen lds\nbuffer_load_dwordx4 v5,s[8:11],s96 offen offset:1024 lds\nbuffer_load_dwordx4 v6,s[8:11],s96 offen offset:2048 lds\n" \
"buffer_load_dwordx4 v7,s[8:11],s96 offen offset:3072 lds\nds_read_b64_tr_b16 a[70:71],v9 offset:20480\nds_read_b64_tr_b16 a[74:75],v10 offset:20480\nds_read_b64_tr_b16 a[78:79],v11 offset:20480\n" \
"ds_read_b64_tr_b16 a[82:83],v8 offset:22528\nds_read_b64_tr_b16 a[86:87],v9 offset:22528\nds_read_b64_tr_b16 a[90:91],v10 offset:22528\nds_read_b64_tr_b16 a[94:95],v11 offset:22528\n" \
"s_add_u32 s97,s92,442368\ns_add_u32 m0,s57,20480\nds_read_b64_tr_b16 a[96:97],v8 offset:24576\ns_waitcnt lgkmcnt(1)\n" \
"buffer_load_dwordx4 v4,s[8:11],s97 offen lds\nbuffer_load_dwordx4 v5,s[8:11],s97 offen offset:1024 lds\nbuffer_load_dwordx4 v6,s[8:11],s97 offen offset:2048 lds\nbuffer_load_dwordx4 v7,s[8:11],s97 offen offset:3072 lds\n" \
"ds_read_b64_tr_b16 a[100:101],v9 offset:24576\nds_read_b64_tr_b16 a[104:105],v10 offset:24576\nds_read_b64_tr_b16 a[108:109],v11 offset:24576\nds_read_b64_tr_b16 a[112:113],v8 offset:26624\n" \
"ds_read_b64_tr_b16 a[116:117],v9 offset:26624\nds_read_b64_tr_b16 a[120:121],v10 offset:26624\nds_read_b64_tr_b16 a[124:125],v11 offset:26624\ns_add_u32 s95,s92,9879552\n" \
"s_add_u32 m0,s57,24576\ns_waitcnt vmcnt(24)\nds_read_b64_tr_b16 a[98:99],v8\ns_waitcnt lgkmcnt(1)\n" \
"buffer_load_dwordx4 v4,s[8:11],s95 offen lds\nbuffer_load_dwordx4 v5,s[8:11],s95 offen offset:1024 lds\nbuffer_load_dwordx4 v6,s[8:11],s95 offen offset:2048 lds\nbuffer_load_dwordx4 v7,s[8:11],s95 offen offset:3072 lds\n" \
"ds_read_b64_tr_b16 a[102:103],v9\nds_read_b64_tr_b16 a[106:107],v10\nds_read_b64_tr_b16 a[110:111],v11\nds_read_b64_tr_b16 a[114:115],v8 offset:2048\n" \
"ds_read_b64_tr_b16 a[118:119],v9 offset:2048\nds_read_b64_tr_b16 a[122:123],v10 offset:2048\nds_read_b64_tr_b16 a[126:127],v11 offset:2048\ns_add_u32 s100,s92,516096\n" \
"s_add_u32 m0,s57,0\ns_waitcnt vmcnt(24)\nds_read_b64_tr_b16 a[128:129],v8 offset:4096\ns_waitcnt lgkmcnt(1)\n" \
"buffer_load_dwordx4 v4,s[8:11],s100 offen lds\nbuffer_load_dwordx4 v5,s[8:11],s100 offen offset:1024 lds\nbuffer_load_dwordx4 v6,s[8:11],s100 offen offset:2048 lds\nbuffer_load_dwordx4 v7,s[8:11],s100 offen offset:3072 lds\n" \
"ds_read_b64_tr_b16 a[132:133],v9 offset:4096\nds_read_b64_tr_b16 a[136:137],v10 offset:4096\nds_read_b64_tr_b16 a[140:141],v11 offset:4096\nds_read_b64_tr_b16 a[144:145],v8 offset:6144\n" \
"ds_read_b64_tr_b16 a[148:149],v9 offset:6144\nds_read_b64_tr_b16 a[152:153],v10 offset:6144\nds_read_b64_tr_b16 a[156:157],v11 offset:6144\ns_add_u32 s101,s92,9953280\n" \
"s_add_u32 m0,s57,4096\ns_waitcnt vmcnt(24)\nds_read_b64_tr_b16 a[130:131],v8 offset:8192\ns_waitcnt lgkmcnt(1)\n" \
"buffer_load_dwordx4 v4,s[8:11],s101 offen lds\nbuffer_load_dwordx4 v5,s[8:11],s101 offen offset:1024 lds\nbuffer_load_dwordx4 v6,s[8:11],s101 offen offset:2048 lds\nbuffer_load_dwordx4 v7,s[8:11],s101 offen offset:3072 lds\n" \
"ds_read_b64_tr_b16 a[134:135],v9 offset:8192\nds_read_b64_tr_b16 a[138:139],v10 offset:8192\nds_read_b64_tr_b16 a[142:143],v11 offset:8192\nds_read_b64_tr_b16 a[146:147],v8 offset:10240\n" \
"ds_read_b64_tr_b16 a[150:151],v9 offset:10240\nds_read_b64_tr_b16 a[154:155],v10 offset:10240\nds_read_b64_tr_b16 a[158:159],v11 offset:10240\ns_bfe_u32 s26,s33,0x20001\n" \
"s_lshl2_add_u32 s15,s32,s26\ns_lshl2_add_u32 s15,s15,s3\ns_lshl_b32 s15,s15,14\ns_add_u32 s15,s15,5308416\n" \
"s_add_u32 s31,s15,0\ns_add_u32 m0,s57,8192\ns_waitcnt vmcnt(24)\nds_read_b64_tr_b16 a[160:161],v8 offset:12288\n" \
"s_waitcnt lgkmcnt(1)\nbuffer_load_dwordx4 v1,s[36:39],s31 offen sc1 lds\nbuffer_load_dwordx4 v1,s[36:39],s31 offen offset:1024 sc1 lds\nbuffer_load_dwordx4 v1,s[36:39],s31 offen offset:2048 sc1 lds\n" \
"buffer_load_dwordx4 v1,s[36:39],s31 offen offset:3072 sc1 lds\nds_read_b64_tr_b16 a[164:165],v9 offset:12288\nds_read_b64_tr_b16 a[168:169],v10 offset:12288\nds_read_b64_tr_b16 a[172:173],v11 offset:12288\n" \
"ds_read_b64_tr_b16 a[176:177],v8 offset:14336\nds_read_b64_tr_b16 a[180:181],v9 offset:14336\nds_read_b64_tr_b16 a[184:185],v10 offset:14336\nds_read_b64_tr_b16 a[188:189],v11 offset:14336\n" \
"s_add_u32 s96,s15,4096\ns_add_u32 m0,s57,12288\ns_waitcnt vmcnt(20)\nds_read_b64_tr_b16 a[162:163],v8 offset:16384\n" \
"s_waitcnt lgkmcnt(1)\nbuffer_load_dwordx4 v1,s[36:39],s96 offen sc1 lds\nbuffer_load_dwordx4 v1,s[36:39],s96 offen offset:1024 sc1 lds\nbuffer_load_dwordx4 v1,s[36:39],s96 offen offset:2048 sc1 lds\n" \
"buffer_load_dwordx4 v1,s[36:39],s96 offen offset:3072 sc1 lds\nds_read_b64_tr_b16 a[166:167],v9 offset:16384\nds_read_b64_tr_b16 a[170:171],v10 offset:16384\nds_read_b64_tr_b16 a[174:175],v11 offset:16384\n" \
"ds_read_b64_tr_b16 a[178:179],v8 offset:18432\nds_read_b64_tr_b16 a[182:183],v9 offset:18432\nds_read_b64_tr_b16 a[186:187],v10 offset:18432\nds_read_b64_tr_b16 a[190:191],v11 offset:18432\n" \
"s_add_u32 s97,s15,8192\ns_add_u32 m0,s57,16384\ns_waitcnt vmcnt(20)\nds_read_b64_tr_b16 a[192:193],v8 offset:20480\n" \
"s_waitcnt lgkmcnt(1)\nbuffer_load_dwordx4 v1,s[36:39],s97 offen sc1 lds\nbuffer_load_dwordx4 v1,s[36:39],s97 offen offset:1024 sc1 lds\nbuffer_load_dwordx4 v1,s[36:39],s97 offen offset:2048 sc1 lds\n" \
"buffer_load_dwordx4 v1,s[36:39],s97 offen offset:3072 sc1 lds\nds_read_b64_tr_b16 a[196:197],v9 offset:20480\nds_read_b64_tr_b16 a[200:201],v10 offset:20480\nds_read_b64_tr_b16 a[204:205],v11 offset:20480\n" \
"ds_read_b64_tr_b16 a[208:209],v8 offset:22528\nds_read_b64_tr_b16 a[212:213],v9 offset:22528\nds_read_b64_tr_b16 a[216:217],v10 offset:22528\nds_read_b64_tr_b16 a[220:221],v11 offset:22528\n" \
"s_add_u32 s95,s15,12288\ns_add_u32 m0,s57,20480\ns_waitcnt lgkmcnt(0)\nbuffer_load_dwordx4 v1,s[36:39],s95 offen sc1 lds\n" \
"buffer_load_dwordx4 v1,s[36:39],s95 offen offset:1024 sc1 lds\nbuffer_load_dwordx4 v1,s[36:39],s95 offen offset:2048 sc1 lds\nbuffer_load_dwordx4 v1,s[36:39],s95 offen offset:3072 sc1 lds\nv_cmp_ne_u64 vcc,s[28:29],v[80:81]\n" \
"v_cmp_ne_u64 s[24:25],s[28:29],v[84:85]\nv_cmp_ne_u64 s[26:27],s[28:29],v[88:89]\ns_or_b64 vcc,vcc,s[24:25]\nv_cmp_ne_u64 s[24:25],s[28:29],v[92:93]\n" \
"s_or_b64 vcc,vcc,s[26:27]\ns_or_b64 vcc,vcc,s[24:25]\ns_cbranch_vccnz .LsnP_%=\n.LbnP_%=:\n" \
"v_bfe_u32 v112,v0,1,5\nv_mul_u32_u24 v112,72,v112\nv_lshrrev_b32 v113,6,v0\nv_mul_u32_u24 v114,144,v0\n" \
"v_lshl_add_u32 v112,v113,5,v112\nv_subrev_u32 v114,18368,v114\nv_mov_b32 v115,1048576\nv_cmp_gt_u32 vcc,144,v0\n" \
"v_and_b32 v113,1,v0\nv_lshl_add_u32 v112,v113,4,v112\nv_cndmask_b32 v114,v115,v114,vcc\nv_and_b32 v97,0xffff0000,v82\n" \
"v_lshlrev_b32 v96,16,v82\ns_add_u32 s24,s93,s94\nv_cmp_gt_u32 vcc,128,v0\nv_add_u32 v113,8,v112\n" \
"v_mul_f32 v96,0x3ee38e39,v96\nv_mul_f32 v97,0x3ee38e39,v97\ns_sub_u32 s24,s24,253\nv_cndmask_b32 v112,v114,v112,vcc\n" \
"v_and_b32 v99,0xffff0000,v83\nv_ldexp_f32 v96,v96,s24\nv_ldexp_f32 v97,v97,s24\nv_lshlrev_b32_e64 v98,16,v83\n" \
"ds_write_b64 v112,v[96:97] offset:28800\nv_add_u32 v115,72,v114\nv_mul_f32 v98,0x3ee38e39,v98\nv_mul_f32 v99,0x3ee38e39,v99\n" \
"v_cndmask_b32_e64 v113,v115,v113,vcc\nv_ldexp_f32 v98,v98,s24\nv_ldexp_f32 v99,v99,s24\nds_write_b64 v113,v[98:99] offset:28800\n" \
"v_and_b32 v101,0xffff0000,v86\nv_lshlrev_b32 v100,16,v86\nds_write_b64 v112,v[100:101] offset:31104\nv_and_b32 v103,0xffff0000,v87\n" \
"v_lshlrev_b32 v102,16,v87\ns_lshr_b32 s25,s33,3\nds_write_b64 v113,v[102:103] offset:31104\ns_mul_i32 s25,s25,576\n" \
"v_and_b32 v105,0xffff0000,v90\nv_lshlrev_b32 v104,16,v90\ns_sub_u32 s25,163200,s25\nds_write_b64 v112,v[104:105] offset:33408\n" \
"v_add_u32 v114,s25,v2\nv_subrev_u32 v115,163200,v114\nv_and_b32 v107,0xffff0000,v91\nv_lshlrev_b32 v106,16,v91\n" \
"v_mov_b32 v116,1048576\nv_cmp_gt_u32 vcc,576,v115\nds_write_b64 v113,v[106:107] offset:33408\nv_and_b32 v109,0xffff0000,v94\n" \
"v_cndmask_b32 v114,v116,v114,vcc\nv_and_b32 v111,0xffff0000,v95\nv_lshlrev_b32 v108,16,v94\nv_lshlrev_b32 v110,16,v95\n" \
"ds_write_b128 v114,v[108:111]\nv_and_b32 v124,48,v0\nv_cmp_gt_u32 vcc,64,v0\nv_bfe_u32 v127,v0,5,1\n" \
"v_and_b32 v13,31,v0\nv_cndmask_b32_e64 v124,v124,v3,vcc\nv_mul_u32_u24 v127,2448,v127\nv_mul_u32_u24 v13,72,v13\n" \
"s_mul_i32 s27,s3,4896\nv_add3_u32 v13,v13,v127,s27\nv_mov_b32 v127,0\ns_waitcnt vmcnt(24)\n" \
"ds_read_b64_tr_b16 a[194:195],v8 offset:24576\nds_read_b64_tr_b16 a[198:199],v9 offset:24576\nds_read_b64_tr_b16 a[202:203],v10 offset:24576\nds_read_b64_tr_b16 a[206:207],v11 offset:24576\n" \
"ds_read_b64_tr_b16 a[210:211],v8 offset:26624\nds_read_b64_tr_b16 a[214:215],v9 offset:26624\nds_read_b64_tr_b16 a[218:219],v10 offset:26624\nds_read_b64_tr_b16 a[222:223],v11 offset:26624\n" \
"s_waitcnt vmcnt(20)\nds_read_b64_tr_b16 a[224:225],v8\nds_read_b64_tr_b16 a[228:229],v9\nds_read_b64_tr_b16 a[232:233],v10\n" \
"ds_read_b64_tr_b16 a[236:237],v11\nds_read_b64_tr_b16 a[240:241],v8 offset:2048\nds_read_b64_tr_b16 a[244:245],v9 offset:2048\ns_waitcnt lgkmcnt(14)\n" \
"s_barrier\nbuffer_load_dwordx4 v[112:115],v3,s[36:39],s16 offen sc1\nbuffer_load_dwordx4 v[116:119],v3,s[36:39],s17 offen sc1\nbuffer_load_dwordx4 v[120:123],v124,s[36:39],s18 offen sc1\n" \
"ds_read_b64_tr_b16 a[248:249],v10 offset:2048\nds_read_b64_tr_b16 a[252:253],v11 offset:2048\ns_waitcnt vmcnt(19)\nds_read_b64_tr_b16 a[226:227],v8 offset:4096\n" \
"ds_read_b64_tr_b16 a[230:231],v9 offset:4096\nds_read_b64_tr_b16 a[234:235],v10 offset:4096\nds_read_b64_tr_b16 a[238:239],v11 offset:4096\nds_read_b64_tr_b16 a[242:243],v8 offset:6144\n" \
"ds_read_b64_tr_b16 a[246:247],v9 offset:6144\nds_read_b64_tr_b16 a[250:251],v10 offset:6144\nds_read_b64_tr_b16 a[254:255],v11 offset:6144\ns_waitcnt vmcnt(15)\n" \
"ds_read_b64_tr_b16 v[192:193],v15 offset:8192\nds_read_b64_tr_b16 v[196:197],v15 offset:8704\nds_read_b64_tr_b16 v[194:195],v15 offset:9216\nds_read_b64_tr_b16 v[198:199],v15 offset:9728\n" \
"ds_read_b64_tr_b16 v[200:201],v15 offset:10240\nds_read_b64_tr_b16 v[204:205],v15 offset:10752\nds_read_b64_tr_b16 v[202:203],v15 offset:11264\nds_read_b64_tr_b16 v[206:207],v15 offset:11776\n" \
"s_waitcnt vmcnt(11)\nds_read_b64_tr_b16 v[208:209],v15 offset:12288\nds_read_b64_tr_b16 v[212:213],v15 offset:12800\nds_read_b64_tr_b16 v[210:211],v15 offset:13312\n" \
"ds_read_b64_tr_b16 v[214:215],v15 offset:13824\nds_read_b64_tr_b16 v[216:217],v15 offset:14336\nds_read_b64_tr_b16 v[220:221],v15 offset:14848\nds_read_b64_tr_b16 v[218:219],v15 offset:15360\n" \
"ds_read_b64_tr_b16 v[222:223],v15 offset:15872\ns_waitcnt vmcnt(7)\nds_read_b64_tr_b16 v[224:225],v15 offset:16384\nds_read_b64_tr_b16 v[228:229],v15 offset:16896\n" \
"ds_read_b64_tr_b16 v[226:227],v15 offset:17408\nds_read_b64_tr_b16 v[230:231],v15 offset:17920\nds_read_b64_tr_b16 v[232:233],v15 offset:18432\nds_read_b64_tr_b16 v[236:237],v15 offset:18944\n" \
"ds_read_b64_tr_b16 v[234:235],v15 offset:19456\nds_read_b64_tr_b16 v[238:239],v15 offset:19968\ns_waitcnt vmcnt(3)\nds_read_b64_tr_b16 v[240:241],v15 offset:20480\n" \
"ds_read_b64_tr_b16 v[244:245],v15 offset:20992\nds_read_b64_tr_b16 v[242:243],v15 offset:21504\nds_read_b64_tr_b16 v[246:247],v15 offset:22016\nds_read_b64_tr_b16 v[248:249],v15 offset:22528\n" \
"ds_read_b64_tr_b16 v[252:253],v15 offset:23040\nds_read_b64_tr_b16 v[250:251],v15 offset:23552\nds_read_b64_tr_b16 v[254:255],v15 offset:24064\ns_waitcnt vmcnt(1)\n" \
"v_and_b32 v125,v112,v116\ns_waitcnt vmcnt(0)\nv_bitop3_b32 v125,v125,v120,1 bitop3:0x80\nv_cmp_eq_u32 vcc,0,v125\n" \
"s_nop 0\ns_cbranch_vccnz .LsuP_%=\n.LbuP_%=:\nv_mfma_f32_16x16x32_f16 v[80:83],v[32:35],v[112:115],0\n" \
"v_and_b32 v3,31,v0\nv_mul_u32_u24 v3,72,v3\nv_mfma_f32_16x16x32_f16 v[84:87],v[36:39],v[112:115],0\nv_add_u32 v13,9216,v13\n" \
"v_mov_b32 v104,-1\nv_mfma_f32_16x16x32_f16 v[88:91],v[32:35],v[116:119],0\nv_mov_b32 v105,-1\nv_mov_b32 v106,-1\n" \
"v_mfma_f32_16x16x32_f16 v[92:95],v[36:39],v[116:119],0\nv_mov_b32 v107,-1\ns_add_u32 s27,s2,0\nv_mfma_f32_16x16x32_f16 v[96:99],v[32:35],v[120:123],0\n" \
"s_lshr_b32 s100,s27,4\ns_mul_i32 s101,s100,37\ns_lshr_b32 s101,s101,8\ns_lshl2_add_u32 s100,s101,s100\n" \
"s_mul_i32 s100,s100,409600\ns_and_b32 s101,s27,15\ns_lshl_b32 s101,s101,14\ns_add_u32 s100,s100,s101\n" \
"s_add_u32 s100,s100,13090816\ns_cmp_lt_u32 s27,158\ns_cselect_b32 s100,s100,2147483648\nbuffer_store_dwordx4 v[104:107],v2,s[36:39],s100 offen sc1\n" \
"v_mfma_f32_16x16x32_f16 v[100:103],v[36:39],v[120:123],0\nv_cvt_pk_f16_f32 v112,v84,v80\nv_cvt_pk_f16_f32 v113,v85,v81\nv_cvt_pk_f16_f32 v114,v86,v82\n" \
"v_cvt_pk_f16_f32 v115,v87,v83\nds_write_b128 v57,v[112:115]\nv_cvt_pk_f16_f32 v116,v92,v88\nv_cvt_pk_f16_f32 v117,v93,v89\n" \
"s_add_u32 s100,s100,4096\nbuffer_store_dwordx4 v[104:107],v2,s[36:39],s100 offen sc1\nv_cvt_pk_f16_f32 v118,v94,v90\nv_cvt_pk_f16_f32 v119,v95,v91\n" \
"ds_write_b128 v58,v[116:119]\nv_cvt_pk_f16_f32 v120,v100,v96\nv_cvt_pk_f16_f32 v121,v101,v97\nv_cvt_pk_f16_f32 v122,v102,v98\n" \
"v_cvt_pk_f16_f32 v123,v103,v99\nds_write_b128 v60,v[120:123]\ns_add_u32 s100,s100,4096\nbuffer_store_dwordx4 v[104:107],v2,s[36:39],s100 offen sc1\n" \
"v_add_u32 v60,31104,v3\nv_add_u32 v3,28800,v3\nds_read2_b64 v[32:35],v3 offset0:0 offset1:1\nds_read2_b64 v[36:39],v3 offset0:2 offset1:3\n" \
"s_add_u32 s100,s100,4096\ns_add_u32 s27,s2,111\ns_lshr_b32 s31,s27,4\ns_mul_i32 s96,s31,37\n" \
"s_lshr_b32 s96,s96,8\ns_lshl2_add_u32 s31,s96,s31\ns_mul_i32 s31,s31,409600\ns_and_b32 s96,s27,15\n" \
"s_lshl_b32 s96,s96,14\nbuffer_store_dwordx4 v[104:107],v2,s[36:39],s100 offen sc1\ns_add_u32 s31,s31,s96\ns_add_u32 s31,s31,13090816\n" \
"s_cmp_lt_u32 s27,158\ns_cselect_b32 s31,s31,2147483648\ns_waitcnt lgkmcnt(2)\ns_barrier\n" \
"ds_read_b64 v[112:113],v56\nds_read_b64 v[116:117],v56 offset:512\nds_read_b64 v[120:121],v56 offset:256\nds_read_b64 v[124:125],v56 offset:768\n" \
"ds_read_b64 v[114:115],v56 offset:1024\nds_read_b64 v[122:123],v56 offset:1280\nds_read_b64 v[56:57],v3 offset:64\nbuffer_store_dwordx4 v[104:107],v2,s[36:39],s31 offen sc1\n" \
"s_add_u32 s31,s31,4096\nv_bfe_u32 v110,v0,1,2\nv_bfe_u32 v111,v0,4,1\nv_xor_b32 v110,v110,v111\n" \
"v_and_b32 v111,1,v0\nv_lshl_add_u32 v110,v110,1,v111\nv_bfe_u32 v111,v0,3,3\nv_lshlrev_b32 v111,12,v111\n" \
"v_lshl_add_u32 v4,v110,4,v111\nv_xor_b32 v5,64,v4\nv_add_u32 v5,31744,v5\nv_add_u32 v6,63488,v4\n" \
"buffer_store_dwordx4 v[104:107],v2,s[36:39],s31 offen sc1\ns_waitcnt lgkmcnt(5)\nv_swap_b32 v113,v116\ns_add_u32 s31,s31,4096\n" \
"s_waitcnt lgkmcnt(3)\nv_swap_b32 v121,v124\nv_add_u32 v7,63488,v5\ns_waitcnt lgkmcnt(2)\n" \
"v_mfma_f32_16x16x32_f16 v[80:83],v[112:115],v[40:43],0\nv_mov_b32 v118,v115\ns_waitcnt lgkmcnt(1)\nv_mov_b32 v126,v123\n" \
"v_mfma_f32_16x16x32_f16 v[84:87],v[112:115],v[44:47],0\ns_lshr_b32 s26,s32,1\ns_mul_i32 s92,s26,1152\nv_mfma_f32_16x16x32_f16 v[88:91],v[120:123],v[40:43],0\n" \
"buffer_store_dwordx4 v[104:107],v2,s[36:39],s31 offen sc1\nv_mfma_f32_16x16x32_f16 v[92:95],v[120:123],v[44:47],0\ns_add_u32 s31,s31,4096\ns_lshr_b32 s26,s3,1\n" \
"v_mfma_f32_16x16x32_f16 v[96:99],v[116:119],v[40:43],0\ns_lshl_b32 s26,s26,6\ns_add_u32 s92,s92,s26\nv_mfma_f32_16x16x32_f16 v[100:103],v[116:119],v[44:47],0\n" \
"v_pk_add_f32 v[88:89],v[80:81],v[88:89] neg_lo:[0,1] neg_hi:[0,1]\nv_pk_add_f32 v[90:91],v[82:83],v[90:91] neg_lo:[0,1] neg_hi:[0,1]\nv_pk_add_f32 v[92:93],v[84:85],v[92:93] neg_lo:[0,1] neg_hi:[0,1]\nbuffer_store_dwordx4 v[104:107],v2,s[36:39],s31 offen sc1\n" \
"v_pk_add_f32 v[94:95],v[86:87],v[94:95] neg_lo:[0,1] neg_hi:[0,1]\nv_pk_fma_f32 v[80:81],v[80:81],2.0,v[88:89] op_sel_hi:[1,0,1] neg_lo:[0,0,1] neg_hi:[0,0,1]\nv_pk_fma_f32 v[82:83],v[82:83],2.0,v[90:91] op_sel_hi:[1,0,1] neg_lo:[0,0,1] neg_hi:[0,0,1]\nv_pk_fma_f32 v[84:85],v[84:85],2.0,v[92:93] op_sel_hi:[1,0,1] neg_lo:[0,0,1] neg_hi:[0,0,1]\n" \
"v_pk_fma_f32 v[86:87],v[86:87],2.0,v[94:95] op_sel_hi:[1,0,1] neg_lo:[0,0,1] neg_hi:[0,0,1]\nv_cvt_pk_f16_f32 v112,v80,v84\nv_cvt_pk_f16_f32 v113,v81,v85\nv_cvt_pk_f16_f32 v114,v82,v86\n" \
"v_cvt_pk_f16_f32 v115,v83,v87\nv_mfma_f32_16x16x32_f16 v[104:107],v[124:127],v[40:43],0\nv_cvt_pk_f16_f32 v116,v88,v92\ns_lshr_b32 s97,s33,3\n" \
"s_add_u32 s97,s97,0\ns_sub_u32 s95,s97,4\ns_min_u32 s97,s97,s95\ns_lshl_b32 s95,s97,12\n" \
"s_add_u32 s95,s95,s57\ns_add_u32 m0,s95,0\ns_and_b32 s95,s97,1\ns_lshr_b32 s97,s97,1\n" \
"s_lshl_b32 s97,s97,10\ns_add_u32 s97,s97,s95\ns_lshl_b32 s97,s97,7\ns_lshr_b32 s100,s33,3\n" \
"s_add_u32 s100,s100,1\ns_sub_u32 s101,s100,4\ns_min_u32 s100,s100,s101\ns_lshl_b32 s101,s100,12\n" \
"s_add_u32 s101,s101,s57\ns_lshr_b32 s31,s33,3\ns_add_u32 s31,s31,2\ns_sub_u32 s96,s31,4\n" \
"s_min_u32 s31,s31,s96\ns_lshl_b32 s96,s31,12\ns_add_u32 s96,s96,s57\nv_mfma_f32_16x16x32_f16 v[80:83],v[16:19],v[112:115],0\n" \
"v_cvt_pk_f16_f32 v117,v89,v93\nv_cvt_pk_f16_f32 v118,v90,v94\nv_mfma_f32_16x16x32_f16 v[84:87],v[20:23],v[112:115],0\nv_cvt_pk_f16_f32 v119,v91,v95\n" \
"v_pk_add_f32 v[104:105],v[96:97],v[104:105] neg_lo:[0,1] neg_hi:[0,1]\nv_mfma_f32_16x16x32_f16 v[108:111],v[124:127],v[44:47],0\nv_pk_add_f32 v[106:107],v[98:99],v[106:107] neg_lo:[0,1] neg_hi:[0,1]\nv_pk_fma_f32 v[96:97],v[96:97],2.0,v[104:105] op_sel_hi:[1,0,1] neg_lo:[0,0,1] neg_hi:[0,0,1]\n" \
"v_mfma_f32_16x16x32_f16 v[88:91],v[24:27],v[116:119],0\nv_pk_fma_f32 v[98:99],v[98:99],2.0,v[106:107] op_sel_hi:[1,0,1] neg_lo:[0,0,1] neg_hi:[0,0,1]\nds_read2_b64 v[40:43],v3 offset0:4 offset1:5\nv_mfma_f32_16x16x32_f16 v[92:95],v[28:31],v[116:119],0\n" \
"ds_write2_b32 v48,v84,v80 offset0:0 offset1:1\nds_write2_b32 v49,v85,v81 offset0:0 offset1:1\nds_write2_b32 v50,v86,v82 offset0:0 offset1:1\nv_pk_add_f32 v[108:109],v[100:101],v[108:109] neg_lo:[0,1] neg_hi:[0,1]\n" \
"v_pk_add_f32 v[110:111],v[102:103],v[110:111] neg_lo:[0,1] neg_hi:[0,1]\nv_pk_fma_f32 v[100:101],v[100:101],2.0,v[108:109] op_sel_hi:[1,0,1] neg_lo:[0,0,1] neg_hi:[0,0,1]\nv_pk_fma_f32 v[102:103],v[102:103],2.0,v[110:111] op_sel_hi:[1,0,1] neg_lo:[0,0,1] neg_hi:[0,0,1]\nds_write2_b32 v51,v87,v83 offset0:0 offset1:1\n" \
"v_cvt_pk_f16_f32 v120,v96,v100\nv_cvt_pk_f16_f32 v121,v97,v101\nv_cvt_pk_f16_f32 v122,v98,v102\nv_cvt_pk_f16_f32 v123,v99,v103\n" \
"ds_write2_b32 v52,v92,v88 offset0:0 offset1:1\nds_write2_b32 v53,v93,v89 offset0:0 offset1:1\nv_mfma_f32_16x16x32_f16 v[96:99],v[16:19],v[120:123],0\nds_write2_b32 v54,v94,v90 offset0:0 offset1:1\n" \
"v_mfma_f32_16x16x32_f16 v[100:103],v[20:23],v[120:123],0\nds_write2_b32 v55,v95,v91 offset0:0 offset1:1\nv_cvt_pk_f16_f32 v124,v104,v108\nv_cvt_pk_f16_f32 v125,v105,v109\n" \
"v_cvt_pk_f16_f32 v126,v106,v110\nv_cvt_pk_f16_f32 v127,v107,v111\nv_add_u32 v48,2448,v48\nv_add_u32 v49,2448,v49\n" \
"v_mfma_f32_16x16x32_f16 v[104:107],v[24:27],v[124:127],0\nds_write2_b32 v48,v100,v96 offset0:0 offset1:1\nv_mfma_f32_16x16x32_f16 v[108:111],v[28:31],v[124:127],0\nds_write2_b32 v49,v101,v97 offset0:0 offset1:1\n" \
"v_add_u32 v50,2448,v50\nds_write2_b32 v50,v102,v98 offset0:0 offset1:1\nv_add_u32 v51,2448,v51\nds_write2_b32 v51,v103,v99 offset0:0 offset1:1\n" \
"v_add_u32 v52,2448,v52\nv_add_u32 v53,2448,v53\nv_add_u32 v54,2448,v54\nds_write2_b32 v52,v108,v104 offset0:0 offset1:1\n" \
"ds_write2_b32 v53,v109,v105 offset0:0 offset1:1\nds_write2_b32 v54,v110,v106 offset0:0 offset1:1\nv_add_u32 v55,2448,v55\nds_write2_b32 v55,v111,v107 offset0:0 offset1:1\n" \
"ds_read2_b64 v[80:83],v13 offset0:0 offset1:1\nds_read2_b64 v[84:87],v13 offset0:2 offset1:3\ns_lshl_b32 s26,s3,8\ns_add_u32 s92,s92,s26\n" \
"s_lshl_b32 s92,s92,4\ns_and_b32 s26,s32,1\ns_lshl3_add_u32 s92,s26,s92\ns_and_b32 s26,s33,7\n" \
"s_add_u32 s92,s92,s26\nds_read2_b64 v[88:91],v13 offset0:4 offset1:5\ns_lshl_b32 s92,s92,8\ns_add_u32 s97,s97,s92\n" \
"buffer_load_dwordx4 v4,s[40:43],s97 offen lds\nds_read2_b64 v[44:47],v3 offset0:6 offset1:7\nv_and_b32 v48,31,v0\nds_read2_b64 v[92:95],v13 offset0:6 offset1:7\n" \
"v_mad_u32_u24 v49,v48,56,0\nv_sub_u32 v48,v13,v49\nv_and_b32 v49,31,v0\nv_mul_u32_u24 v49,68,v49\n" \
"v_sub_u32 v49,v13,v49\nbuffer_load_dwordx4 v5,s[40:43],s97 offen offset:1024 lds\nds_read_b64 v[96:97],v13 offset:64\ns_waitcnt lgkmcnt(5)\n" \
"v_pk_fma_f32 v[62:63],v[80:81],v[32:33],v[62:63]\nv_pk_fma_f32 v[64:65],v[82:83],v[34:35],v[64:65]\nv_pk_mul_f32 v[102:103],v[62:63],v[62:63]\ns_waitcnt lgkmcnt(4)\n" \
"v_pk_fma_f32 v[66:67],v[84:85],v[36:37],v[66:67]\nv_pk_add_f32 v[100:101],v[62:63],v[64:65]\nv_pk_fma_f32 v[102:103],v[64:65],v[64:65],v[102:103]\nv_pk_fma_f32 v[68:69],v[86:87],v[38:39],v[68:69]\n" \
"buffer_load_dwordx4 v6,s[40:43],s97 offen offset:2048 lds\nv_pk_add_f32 v[100:101],v[100:101],v[66:67]\nv_pk_fma_f32 v[102:103],v[66:67],v[66:67],v[102:103]\nds_read2_b64 v[112:115],v60 offset0:0 offset1:1\n" \
"ds_read2_b64 v[116:119],v60 offset0:2 offset1:3\ns_waitcnt lgkmcnt(5)\nv_pk_fma_f32 v[70:71],v[88:89],v[40:41],v[70:71]\nv_pk_add_f32 v[100:101],v[100:101],v[68:69]\n" \
"ds_read2_b64 v[120:123],v60 offset0:4 offset1:5\nv_pk_fma_f32 v[102:103],v[68:69],v[68:69],v[102:103]\nbuffer_load_dwordx4 v7,s[40:43],s97 offen offset:3072 lds\nds_read2_b64 v[124:127],v60 offset0:6 offset1:7\n" \
"v_pk_fma_f32 v[72:73],v[90:91],v[42:43],v[72:73]\nv_pk_add_f32 v[100:101],v[100:101],v[70:71]\nds_read_b64 v[98:99],v60 offset:64\nv_add_u32 v60,2304,v60\n" \
"v_pk_fma_f32 v[102:103],v[70:71],v[70:71],v[102:103]\nds_read2_b64 v[16:19],v60 offset0:0 offset1:1\ns_waitcnt lgkmcnt(7)\nv_pk_fma_f32 v[74:75],v[92:93],v[44:45],v[74:75]\n" \
"s_add_u32 m0,s101,0\ns_and_b32 s101,s100,1\ns_lshr_b32 s100,s100,1\ns_lshl_b32 s100,s100,10\n" \
"s_add_u32 s100,s100,s101\ns_lshl_b32 s100,s100,7\ns_add_u32 s100,s100,s92\nbuffer_load_dwordx4 v4,s[40:43],s100 offen lds\n" \
"v_pk_add_f32 v[100:101],v[100:101],v[72:73]\nv_pk_fma_f32 v[102:103],v[72:73],v[72:73],v[102:103]\nv_pk_fma_f32 v[76:77],v[94:95],v[46:47],v[76:77]\nv_pk_add_f32 v[100:101],v[100:101],v[74:75]\n" \
"v_pk_fma_f32 v[102:103],v[74:75],v[74:75],v[102:103]\nds_read2_b64 v[20:23],v60 offset0:2 offset1:3\ns_waitcnt lgkmcnt(7)\nv_pk_fma_f32 v[78:79],v[96:97],v[56:57],v[78:79]\n" \
"v_pk_add_f32 v[100:101],v[100:101],v[76:77]\nbuffer_load_dwordx4 v5,s[40:43],s100 offen offset:1024 lds\nv_pk_fma_f32 v[102:103],v[76:77],v[76:77],v[102:103]\nv_pk_add_f32 v[100:101],v[100:101],v[78:79]\n" \
"v_pk_fma_f32 v[102:103],v[78:79],v[78:79],v[102:103]\nds_read2_b64 v[24:27],v60 offset0:4 offset1:5\nv_add_f32 v100,v100,v101\nv_add_f32 v102,v102,v103\n" \
"ds_read2_b64 v[28:31],v60 offset0:6 offset1:7\nds_read_b64 v[58:59],v60 offset:64\nbuffer_load_dwordx4 v6,s[40:43],s100 offen offset:2048 lds\nv_permlane16_swap_b32 v100,v102\n" \
"v_add_f32 v100,v100,v102\ns_lshr_b32 s97,s33,3\ns_add_u32 s97,s97,3\nv_add_f32_dpp v100,v100,v100 quad_perm:[1,0,3,2] row_mask:0xf bank_mask:0xf bound_ctrl:1\n" \
"s_sub_u32 s95,s97,4\ns_min_u32 s97,s97,s95\nv_add_f32_dpp v100,v100,v100 quad_perm:[2,3,0,1] row_mask:0xf bank_mask:0xf bound_ctrl:1\nbuffer_load_dwordx4 v7,s[40:43],s100 offen offset:3072 lds\n" \
"s_add_u32 m0,s96,0\nv_add_f32_dpp v100,v100,v100 row_half_mirror row_mask:0xf bank_mask:0xf bound_ctrl:1\ns_and_b32 s96,s31,1\ns_lshr_b32 s31,s31,1\n" \
"v_add_f32_dpp v100,v100,v100 row_mirror row_mask:0xf bank_mask:0xf bound_ctrl:1\nv_mul_f32 v104,0x3ae38e39,v100\nv_mul_f32 v102,0x3ae38e39,v100\ns_lshl_b32 s31,s31,10\n" \
"s_add_u32 s31,s31,s96\ns_lshl_b32 s31,s31,7\ns_add_u32 s31,s31,s92\nbuffer_load_dwordx4 v4,s[40:43],s31 offen lds\n" \
"v_permlane16_swap_b32 v104,v102\nv_fma_f32 v102,-v104,v104,v102\nv_max_f32 v102,0,v102\nv_add_f32 v102,0x3727c5ac,v102\n" \
"v_rsq_f32 v100,v102\ns_lshl_b32 s95,s97,12\nbuffer_load_dwordx4 v5,s[40:43],s31 offen offset:1024 lds\nv_mul_f32 v101,-v104,v100\n" \
"v_pk_fma_f32 v[80:81],v[62:63],v[100:101],v[100:101] op_sel:[0,0,1] op_sel_hi:[1,0,1]\nv_pk_fma_f32 v[82:83],v[64:65],v[100:101],v[100:101] op_sel:[0,0,1] op_sel_hi:[1,0,1]\nv_pk_fma_f32 v[84:85],v[66:67],v[100:101],v[100:101] op_sel:[0,0,1] op_sel_hi:[1,0,1]\nv_pk_fma_f32 v[86:87],v[68:69],v[100:101],v[100:101] op_sel:[0,0,1] op_sel_hi:[1,0,1]\n" \
"v_pk_fma_f32 v[88:89],v[70:71],v[100:101],v[100:101] op_sel:[0,0,1] op_sel_hi:[1,0,1]\nv_pk_fma_f32 v[90:91],v[72:73],v[100:101],v[100:101] op_sel:[0,0,1] op_sel_hi:[1,0,1]\nv_pk_fma_f32 v[92:93],v[74:75],v[100:101],v[100:101] op_sel:[0,0,1] op_sel_hi:[1,0,1]\nbuffer_load_dwordx4 v6,s[40:43],s31 offen offset:2048 lds\n" \
"v_pk_fma_f32 v[94:95],v[76:77],v[100:101],v[100:101] op_sel:[0,0,1] op_sel_hi:[1,0,1]\nv_pk_fma_f32 v[96:97],v[78:79],v[100:101],v[100:101] op_sel:[0,0,1] op_sel_hi:[1,0,1]\ns_add_u32 s95,s95,s57\ns_waitcnt lgkmcnt(9)\n" \
"v_readlane_b32 s24,v112,0\nv_mov_b32 v60,v61\ns_mov_b32 s25,s24\nv_pk_add_f32 v[112:113],v[112:113],s[24:25] neg_lo:[0,1] neg_hi:[0,1]\n" \
"s_waitcnt lgkmcnt(4)\nv_pk_fma_f32 v[80:81],v[80:81],v[16:17],v[112:113]\nbuffer_load_dwordx4 v7,s[40:43],s31 offen offset:3072 lds\nv_pk_mul_f32 v[102:103],v[80:81],v[80:81]\n" \
"s_add_u32 m0,s95,0\ns_and_b32 s95,s97,1\ns_lshr_b32 s97,s97,1\ns_lshl_b32 s97,s97,10\n" \
"s_add_u32 s97,s97,s95\ns_lshl_b32 s97,s97,7\ns_add_u32 s97,s97,s92\nbuffer_load_dwordx4 v4,s[40:43],s97 offen lds\n" \
"v_pk_add_f32 v[114:115],v[114:115],s[24:25] neg_lo:[0,1] neg_hi:[0,1]\nv_pk_fma_f32 v[82:83],v[82:83],v[18:19],v[114:115]\nv_pk_add_f32 v[100:101],v[80:81],v[82:83]\nv_pk_fma_f32 v[102:103],v[82:83],v[82:83],v[102:103]\n" \
"v_pk_add_f32 v[116:117],v[116:117],s[24:25] neg_lo:[0,1] neg_hi:[0,1]\ns_waitcnt lgkmcnt(3)\nv_pk_fma_f32 v[84:85],v[84:85],v[20:21],v[116:117]\nv_pk_add_f32 v[100:101],v[100:101],v[84:85]\n" \
"buffer_load_dwordx4 v5,s[40:43],s97 offen offset:1024 lds\nv_pk_fma_f32 v[102:103],v[84:85],v[84:85],v[102:103]\nv_pk_add_f32 v[118:119],v[118:119],s[24:25] neg_lo:[0,1] neg_hi:[0,1]\nv_pk_fma_f32 v[86:87],v[86:87],v[22:23],v[118:119]\n" \
"v_pk_add_f32 v[100:101],v[100:101],v[86:87]\nv_pk_fma_f32 v[102:103],v[86:87],v[86:87],v[102:103]\nv_pk_add_f32 v[120:121],v[120:121],s[24:25] neg_lo:[0,1] neg_hi:[0,1]\ns_waitcnt lgkmcnt(2)\n" \
"v_pk_fma_f32 v[88:89],v[88:89],v[24:25],v[120:121]\nbuffer_load_dwordx4 v6,s[40:43],s97 offen offset:2048 lds\nv_pk_add_f32 v[100:101],v[100:101],v[88:89]\nv_pk_fma_f32 v[102:103],v[88:89],v[88:89],v[102:103]\n" \
"v_pk_add_f32 v[122:123],v[122:123],s[24:25] neg_lo:[0,1] neg_hi:[0,1]\nv_pk_fma_f32 v[90:91],v[90:91],v[26:27],v[122:123]\nv_pk_add_f32 v[100:101],v[100:101],v[90:91]\nv_pk_fma_f32 v[102:103],v[90:91],v[90:91],v[102:103]\n" \
"v_pk_add_f32 v[124:125],v[124:125],s[24:25] neg_lo:[0,1] neg_hi:[0,1]\nbuffer_load_dwordx4 v7,s[40:43],s97 offen offset:3072 lds\ns_waitcnt lgkmcnt(1)\nv_pk_fma_f32 v[92:93],v[92:93],v[28:29],v[124:125]\n" \
"v_pk_add_f32 v[100:101],v[100:101],v[92:93]\nv_pk_fma_f32 v[102:103],v[92:93],v[92:93],v[102:103]\nv_pk_add_f32 v[126:127],v[126:127],s[24:25] neg_lo:[0,1] neg_hi:[0,1]\nv_pk_fma_f32 v[94:95],v[94:95],v[30:31],v[126:127]\n" \
"v_pk_add_f32 v[100:101],v[100:101],v[94:95]\nv_pk_fma_f32 v[102:103],v[94:95],v[94:95],v[102:103]\nv_pk_add_f32 v[98:99],v[98:99],s[24:25] neg_lo:[0,1] neg_hi:[0,1]\ns_waitcnt lgkmcnt(0)\n" \
"v_pk_fma_f32 v[96:97],v[96:97],v[58:59],v[98:99]\nv_pk_add_f32 v[100:101],v[100:101],v[96:97]\nv_pk_fma_f32 v[102:103],v[96:97],v[96:97],v[102:103]\nv_add_f32 v100,v100,v101\n" \
"v_add_f32 v102,v102,v103\nv_pk_add_f32 v[62:63],v[62:63],v[60:61] op_sel_hi:[1,0]\nv_cvt_pk_bf16_f32 v32,v62,v63\nv_permlane16_swap_b32 v100,v102\n" \
"v_add_f32 v100,v100,v102\nv_pk_add_f32 v[64:65],v[64:65],v[60:61] op_sel_hi:[1,0]\nv_cvt_pk_bf16_f32 v33,v64,v65\nv_add_f32_dpp v100,v100,v100 quad_perm:[1,0,3,2] row_mask:0xf bank_mask:0xf bound_ctrl:1\n" \
"v_pk_add_f32 v[66:67],v[66:67],v[60:61] op_sel_hi:[1,0]\nv_cvt_pk_bf16_f32 v34,v66,v67\nv_add_f32_dpp v100,v100,v100 quad_perm:[2,3,0,1] row_mask:0xf bank_mask:0xf bound_ctrl:1\nv_pk_add_f32 v[68:69],v[68:69],v[60:61] op_sel_hi:[1,0]\n" \
"v_cvt_pk_bf16_f32 v35,v68,v69\nv_add_f32_dpp v100,v100,v100 row_half_mirror row_mask:0xf bank_mask:0xf bound_ctrl:1\nv_pk_add_f32 v[70:71],v[70:71],v[60:61] op_sel_hi:[1,0]\nv_cvt_pk_bf16_f32 v36,v70,v71\n" \
"v_add_f32_dpp v100,v100,v100 row_mirror row_mask:0xf bank_mask:0xf bound_ctrl:1\nv_mul_f32 v104,0x3ae38e39,v100\nv_mul_f32 v102,0x3ae38e39,v100\nv_pk_add_f32 v[72:73],v[72:73],v[60:61] op_sel_hi:[1,0]\n" \
"v_cvt_pk_bf16_f32 v37,v72,v73\nv_permlane16_swap_b32 v104,v102\nv_fma_f32 v102,-v104,v104,v102\nv_max_f32 v102,0,v102\n" \
"v_add_f32 v102,0x3727c5ac,v102\nv_rsq_f32_e64 v100,v102\nv_pk_add_f32 v[74:75],v[74:75],v[60:61] op_sel_hi:[1,0]\nv_mul_f32 v101,-v104,v100\n" \
"v_pk_fma_f32 v[80:81],v[80:81],v[100:101],v[100:101] op_sel:[0,0,1] op_sel_hi:[1,0,1]\nv_pk_fma_f32 v[82:83],v[82:83],v[100:101],v[100:101] op_sel:[0,0,1] op_sel_hi:[1,0,1]\nv_pk_fma_f32 v[84:85],v[84:85],v[100:101],v[100:101] op_sel:[0,0,1] op_sel_hi:[1,0,1]\nv_pk_fma_f32 v[86:87],v[86:87],v[100:101],v[100:101] op_sel:[0,0,1] op_sel_hi:[1,0,1]\n" \
"v_cvt_pk_bf16_f32 v16,v80,v81\nv_cvt_pk_bf16_f32 v17,v82,v83\nv_cvt_pk_bf16_f32 v18,v84,v85\nv_cvt_pk_bf16_f32 v19,v86,v87\n" \
"ds_write_b128 v48,v[16:19]\nv_pk_fma_f32 v[88:89],v[88:89],v[100:101],v[100:101] op_sel:[0,0,1] op_sel_hi:[1,0,1]\nv_pk_fma_f32 v[90:91],v[90:91],v[100:101],v[100:101] op_sel:[0,0,1] op_sel_hi:[1,0,1]\nv_pk_fma_f32 v[92:93],v[92:93],v[100:101],v[100:101] op_sel:[0,0,1] op_sel_hi:[1,0,1]\n" \
"v_pk_fma_f32 v[94:95],v[94:95],v[100:101],v[100:101] op_sel:[0,0,1] op_sel_hi:[1,0,1]\nv_cvt_pk_bf16_f32 v20,v88,v89\nv_cvt_pk_bf16_f32 v21,v90,v91\nv_cvt_pk_bf16_f32 v22,v92,v93\n" \
"v_cvt_pk_bf16_f32 v23,v94,v95\nds_write_b128 v48,v[20:23] offset:512\nv_pk_fma_f32 v[96:97],v[96:97],v[100:101],v[100:101] op_sel:[0,0,1] op_sel_hi:[1,0,1]\nv_cvt_pk_bf16_f32 v24,v96,v97\n" \
"ds_write_b32 v49,v24 offset:1024\nv_cvt_pk_bf16_f32 v38,v74,v75\nv_pk_add_f32 v[76:77],v[76:77],v[60:61] op_sel_hi:[1,0]\nv_cvt_pk_bf16_f32 v39,v76,v77\n" \
"v_pk_add_f32 v[78:79],v[78:79],v[60:61] op_sel_hi:[1,0]\nv_cvt_pk_bf16_f32 v40,v78,v79\nv_bfe_u32 v50,v0,2,2\nv_mul_u32_u24 v50,4896,v50\n" \
"v_bfe_u32 v51,v0,4,2\nv_lshl_add_u32 v51,v51,2,s3\nv_lshl_add_u32 v50,v51,5,v50\nv_and_b32 v52,3,v0\n" \
"v_lshl_add_u32 v50,v52,3,v50\nv_and_b32 v52,0xf0,v1\nv_lshl_add_u32 v51,v51,16,v52\nv_bfrev_b32 v52,1\n" \
"v_cmp_gt_u32 vcc,256,v1\nv_and_b32 v53,31,v0\nv_mul_u32_u24 v53,12,v53\nv_cndmask_b32 v52,v52,v51,vcc\n" \
"v_sub_u32 v53,v12,v53\ns_lshl_b32 s24,s34,8\ns_add_u32 s24,s24,9764864\ns_waitcnt lgkmcnt(0)\n" \
"s_barrier\nds_read_b64_tr_b16 v[112:113],v50 offset:9216\nds_read_b64_tr_b16 v[114:115],v50 offset:11664\nds_read_b64_tr_b16 v[116:117],v50 offset:9728\n" \
"ds_read_b64_tr_b16 v[118:119],v50 offset:12176\nds_read_b64_tr_b16 v[120:121],v50 offset:10240\nds_read_b64_tr_b16 v[122:123],v50 offset:12688\ns_add_u32 s25,s24,1048576\n" \
"s_add_u32 s26,s24,2097152\ns_add_u32 s13,s13,7405568\ns_load_dwordx2 s[4:5],s[0:1],56\ns_load_dwordx2 s[16:17],s[0:1],64\n" \
"s_waitcnt lgkmcnt(0)\nbuffer_store_dwordx4 v[112:115],v51,s[36:39],s24 offen sc1\nbuffer_store_dwordx4 v[116:119],v51,s[36:39],s25 offen sc1\nbuffer_store_dwordx4 v[120:123],v52,s[36:39],s26 offen sc1\n" \
"buffer_store_dwordx4 v[32:35],v12,s[36:39],s13 offen sc1\nbuffer_store_dwordx4 v[36:39],v12,s[36:39],s13 offen offset:512 sc1\nbuffer_store_dword v40,v53,s[36:39],s13 offen offset:1024 sc1\ns_lshr_b32 s100,s33,3\n" \
"s_lshl1_add_u32 s100,s100,0\ns_sub_u32 s101,s100,7\ns_min_u32 s100,s100,s101\ns_lshl_b32 s101,s100,12\n" \
"s_add_u32 s101,s101,s57\ns_add_u32 m0,s101,0\ns_and_b32 s101,s100,1\ns_lshr_b32 s100,s100,1\n" \
"s_lshl_b32 s100,s100,10\ns_add_u32 s100,s100,s101\ns_lshl_b32 s100,s100,7\ns_add_u32 s100,s100,s92\n" \
"s_add_u32 s100,s100,262144\ns_lshr_b32 s31,s33,3\ns_lshl1_add_u32 s31,s31,1\ns_sub_u32 s96,s31,7\n" \
"s_min_u32 s31,s31,s96\ns_lshl_b32 s96,s31,12\ns_add_u32 s96,s96,s57\ns_lshr_b32 s97,s33,3\n" \
"s_lshl1_add_u32 s97,s97,2\ns_sub_u32 s95,s97,7\ns_min_u32 s97,s97,s95\ns_lshl_b32 s95,s97,12\n" \
"s_add_u32 s95,s95,s57\nv_and_b32 v64,63,v0\ns_and_b32 s25,s33,7\nv_lshrrev_b32 v66,5,v64\n" \
"v_and_b32 v67,1,v0\ns_lshl2_add_u32 s25,s25,s3\ns_and_b32 s26,s33,6\ns_lshr_b32 s27,s3,1\n" \
"v_lshl_add_u32 v66,v66,1,v67\ns_mul_i32 s24,s32,1152\ns_lshl_b32 s25,s25,5\ns_add_u32 s26,s26,s27\n" \
"s_mov_b32 s6,9216\ns_mov_b32 s7,0x20000\nv_lshlrev_b32 v68,3,v66\ns_add_u32 s25,s25,s24\n" \
"v_bfe_u32 v72,v0,1,1\nv_lshrrev_b32 v74,2,v64\ns_lshl4_add_u32 s26,s26,s24\nv_and_b32 v73,12,v0\n" \
"v_lshrrev_b32 v71,4,v64\nv_and_b32 v69,7,v0\nbuffer_load_dwordx2 v[80:81],v68,s[4:7],s25 offen\nv_lshl_add_u32 v72,v72,2,s21\n" \
"v_and_b32 v74,12,v74\nv_add_u32 v71,v71,v73\ns_lshl_b32 s24,s3,4\nv_lshlrev_b32 v70,1,v69\n" \
"v_lshl_add_u32 v72,v72,4,v74\ns_mov_b32 s15,s19\nv_add_u32 v71,s24,v71\nbuffer_load_ushort v82,v70,s[4:7],s26 offen offset:1024\n" \
"s_mov_b32 s18,4096\ns_mov_b32 s19,0x20000\nv_lshlrev_b32 v74,1,v71\ns_lshl_b32 s24,s22,8\n" \
"buffer_load_ushort v83,v74,s[16:19],s24 offen\nv_add_u32 v72,s3,v72\nbuffer_load_ushort v84,v74,s[16:19],s24 offen offset:128\nv_lshlrev_b32_e64 v75,1,v72\n" \
"buffer_load_ushort v85,v75,s[16:19],s24 offen\nv_lshl_add_u32 v73,v67,1,v73\nv_mul_u32_u24 v91,1152,v72\nv_lshl_add_u32 v91,v73,1,v91\n" \
"v_lshlrev_b32_e64 v73,2,v73\nv_add_u32 v73,163712,v73\nds_read_b64 v[86:87],v73\ns_mul_i32 s24,s22,147456\n" \
"s_mul_i32 s25,s20,288\nv_bfe_u32 v58,v0,1,1\ns_add_u32 s24,s24,s25\nv_mul_u32_u24 v58,544,v58\n" \
"v_bfe_u32 v75,v0,2,1\ns_lshl_b32 s25,s21,6\nv_lshl_add_u32 v58,v75,8,v58\ns_add_u32 s25,s25,s24\n" \
"v_bfe_u32 v75,v0,3,2\ns_add_u32 s24,s24,256\nv_lshl_add_u32 v58,v75,6,v58\nv_add_u32 v91,s24,v91\n" \
"v_mul_u32_u24 v90,1152,v71\nv_and_b32 v74,3,v0\nv_lshl_add_u32 v58,v66,4,v58\ns_mul_i32 s24,s3,1056\n" \
"v_lshl_add_u32 v90,v74,3,v90\nv_add_u32 v58,s24,v58\nv_add_u32 v90,s25,v90\ns_and_b32 s24,s3,1\n" \
"v_mul_u32_u24 v60,288,v75\ns_lshl_b32 s24,s24,2\ns_lshr_b32 s25,s3,1\nv_lshrrev_b32 v75,5,v64\n" \
"s_add_u32 s24,s24,s25\nv_lshl_add_u32 v60,v75,6,v60\ns_lshl_b32 s24,s24,5\nv_lshl_add_u32 v60,v69,2,v60\n" \
"s_add_u32 s24,s24,4224\nv_add_u32 v60,s24,v60\ns_bfe_u32 s24,s33,0x10001\ns_add_u32 s24,s24,s15\n" \
"s_add_u32 s24,s24,17\ns_lshl2_add_u32 s24,s24,s3\ns_waitcnt lgkmcnt(0)\nv_lshrrev_b32 v86,16,v86\n" \
"s_lshl_b32 s24,s24,7\nv_mov_b32 v92,v4\nv_mov_b32 v93,v5\nv_add_u32 v74,s57,v1\n" \
"v_or_b32 v95,v86,v87\nv_lshl_add_u32 v62,v64,1,s24\nv_bfrev_b32 v76,1\nv_cmp_eq_u32 vcc,0,v64\n" \
"s_lshl2_add_u32 s24,s34,s3\ns_lshl2_add_u32 s24,s24,65536\nv_cndmask_b32 v76,v76,0,vcc\nv_mov_b32 v77,1\n" \
"s_barrier\nv_mov_b32 v57,0xc0135761\nv_add_u32 v59,0x80000,v1\nv_lshl_add_u32 v61,v64,3,s23\n" \
"v_mov_b32 v63,0\nv_bfe_u32 v100,v0,4,2\nv_bfe_u32 v101,v0,2,2\nv_lshl_add_u32 v100,v100,3,v101\n" \
"v_bfe_u32 v101,v0,3,2\nv_lshl_add_u32 v100,v100,2,v101\nv_and_b32 v101,3,v0\nv_lshl_add_u32 v100,v100,2,v101\n" \
"v_lshlrev_b32 v100,3,v100\nv_xor_b32 v8,0,v100\nv_add_u32 v8,s57,v8\nv_mov_b32 v88,v8\n" \
"v_xor_b32 v9,32,v100\nv_add_u32 v9,s57,v9\nv_xor_b32 v10,64,v100\nv_add_u32 v10,s57,v10\n" \
"v_xor_b32 v11,96,v100\nv_add_u32 v11,s57,v11\ns_waitcnt vmcnt(5)\nbuffer_store_dword v77,v76,s[36:39],s24 offen sc1\n" \
"ds_read_b64_tr_b16 v[128:129],v8\nds_read_b64_tr_b16 v[130:131],v8 offset:512\nds_read_b64_tr_b16 v[132:133],v9\nds_read_b64_tr_b16 v[134:135],v9 offset:512\n" \
"ds_read_b64_tr_b16 v[136:137],v10\nds_read_b64_tr_b16 v[138:139],v10 offset:512\nds_read_b64_tr_b16 v[140:141],v11\nds_read_b64_tr_b16 v[142:143],v11 offset:512\n" \
"ds_read_b64_tr_b16 v[144:145],v8 offset:4096\nds_read_b64_tr_b16 v[146:147],v8 offset:4608\nds_read_b64_tr_b16 v[148:149],v9 offset:4096\nds_read_b64_tr_b16 v[150:151],v9 offset:4608\n" \
"ds_read_b64_tr_b16 v[152:153],v10 offset:4096\nds_read_b64_tr_b16 v[154:155],v10 offset:4608\nds_read_b64_tr_b16 v[156:157],v11 offset:4096\nds_read_b64_tr_b16 v[158:159],v11 offset:4608\n" \
"ds_read_b64_tr_b16 v[160:161],v8 offset:8192\nds_read_b64_tr_b16 v[162:163],v8 offset:8704\ns_waitcnt vmcnt(5)\nv_and_b32 v53,0xffff0000,v80\n" \
"v_and_b32 v55,0xffff0000,v81\nv_lshlrev_b32 v52,16,v80\nv_lshlrev_b32 v54,16,v81\nds_read_b64_tr_b16 v[164:165],v9 offset:8192\n" \
"ds_read_b64_tr_b16 v[166:167],v9 offset:8704\nds_read_b64_tr_b16 v[168:169],v10 offset:8192\nds_read_b64_tr_b16 v[170:171],v10 offset:8704\nds_read_b64_tr_b16 v[172:173],v11 offset:8192\n" \
"s_waitcnt vmcnt(4)\nv_lshlrev_b32 v56,16,v82\nds_read_b64_tr_b16 v[174:175],v11 offset:8704\nds_read_b64_tr_b16 v[176:177],v8 offset:12288\n" \
"ds_read_b64_tr_b16 v[178:179],v8 offset:12800\nds_read_b64_tr_b16 v[180:181],v9 offset:12288\nds_read_b64_tr_b16 v[182:183],v9 offset:12800\ns_waitcnt vmcnt(2)\n" \
"v_lshl_or_b32 v89,v84,16,v83\nds_write_b128 v74,v[88:91] offset:28672\ns_waitcnt vmcnt(1)\nv_lshlrev_b32_e64 v94,16,v85\n" \
"ds_write_b128 v74,v[92:95] offset:29696\nds_read_b64_tr_b16 v[184:185],v10 offset:12288\nds_read_b64_tr_b16 v[186:187],v10 offset:12800\nds_read_b64_tr_b16 v[188:189],v11 offset:12288\n" \
"ds_read_b64_tr_b16 v[190:191],v11 offset:12800\ns_waitcnt lgkmcnt(0)\nbuffer_load_dwordx4 v4,s[40:43],s100 offen lds\nbuffer_load_dwordx4 v5,s[40:43],s100 offen offset:1024 lds\n" \
"buffer_load_dwordx4 v6,s[40:43],s100 offen offset:2048 lds\nbuffer_load_dwordx4 v7,s[40:43],s100 offen offset:3072 lds\ns_add_u32 m0,s96,0\ns_and_b32 s96,s31,1\n" \
"s_lshr_b32 s31,s31,1\ns_lshl_b32 s31,s31,10\ns_add_u32 s31,s31,s96\ns_lshl_b32 s31,s31,7\n" \
"s_add_u32 s31,s31,s92\ns_add_u32 s31,s31,262144\nbuffer_load_dwordx4 v4,s[40:43],s31 offen lds\nbuffer_load_dwordx4 v5,s[40:43],s31 offen offset:1024 lds\n" \
"buffer_load_dwordx4 v6,s[40:43],s31 offen offset:2048 lds\nbuffer_load_dwordx4 v7,s[40:43],s31 offen offset:3072 lds\ns_add_u32 m0,s95,0\ns_and_b32 s95,s97,1\n" \
"s_lshr_b32 s97,s97,1\ns_lshl_b32 s97,s97,10\ns_add_u32 s97,s97,s95\ns_lshl_b32 s97,s97,7\n" \
"s_add_u32 s97,s97,s92\ns_add_u32 s97,s97,262144\nbuffer_load_dwordx4 v4,s[40:43],s97 offen lds\nbuffer_load_dwordx4 v5,s[40:43],s97 offen offset:1024 lds\n" \
"buffer_load_dwordx4 v6,s[40:43],s97 offen offset:2048 lds\nbuffer_load_dwordx4 v7,s[40:43],s97 offen offset:3072 lds\ns_lshr_b32 s100,s33,3\ns_lshl1_add_u32 s100,s100,3\n" \
"s_sub_u32 s101,s100,7\ns_min_u32 s100,s100,s101\ns_lshl_b32 s101,s100,12\ns_add_u32 s101,s101,s57\n" \
"s_add_u32 m0,s101,0\ns_and_b32 s101,s100,1\ns_lshr_b32 s100,s100,1\ns_lshl_b32 s100,s100,10\n" \
"s_add_u32 s100,s100,s101\ns_lshl_b32 s100,s100,7\ns_add_u32 s100,s100,s92\ns_add_u32 s100,s100,262144\n" \
"buffer_load_dwordx4 v4,s[40:43],s100 offen lds\nbuffer_load_dwordx4 v5,s[40:43],s100 offen offset:1024 lds\nbuffer_load_dwordx4 v6,s[40:43],s100 offen offset:2048 lds\nbuffer_load_dwordx4 v7,s[40:43],s100 offen offset:3072 lds\n" \
"s_lshr_b32 s31,s33,3\ns_lshl1_add_u32 s31,s31,4\ns_sub_u32 s96,s31,7\ns_min_u32 s31,s31,s96\n" \
"s_lshl_b32 s96,s31,12\ns_add_u32 s96,s96,s57\ns_add_u32 m0,s96,0\ns_and_b32 s96,s31,1\n" \
"s_lshr_b32 s31,s31,1\ns_lshl_b32 s31,s31,10\ns_add_u32 s31,s31,s96\ns_lshl_b32 s31,s31,7\n" \
"s_add_u32 s31,s31,s92\ns_add_u32 s31,s31,262144\nbuffer_load_dwordx4 v4,s[40:43],s31 offen lds\nbuffer_load_dwordx4 v5,s[40:43],s31 offen offset:1024 lds\n" \
"buffer_load_dwordx4 v6,s[40:43],s31 offen offset:2048 lds\nbuffer_load_dwordx4 v7,s[40:43],s31 offen offset:3072 lds\ns_lshr_b32 s97,s33,3\ns_lshl1_add_u32 s97,s97,5\n" \
"s_sub_u32 s95,s97,7\ns_min_u32 s97,s97,s95\ns_lshl_b32 s95,s97,12\ns_add_u32 s95,s95,s57\n" \
"s_add_u32 m0,s95,0\ns_and_b32 s95,s97,1\ns_lshr_b32 s97,s97,1\ns_lshl_b32 s97,s97,10\n" \
"s_add_u32 s97,s97,s95\ns_lshl_b32 s97,s97,7\ns_add_u32 s97,s97,s92\ns_add_u32 s97,s97,262144\n" \
"buffer_load_dwordx4 v4,s[40:43],s97 offen lds\nbuffer_load_dwordx4 v5,s[40:43],s97 offen offset:1024 lds\nbuffer_load_dwordx4 v6,s[40:43],s97 offen offset:2048 lds\nbuffer_load_dwordx4 v7,s[40:43],s97 offen offset:3072 lds\n" \
"s_lshr_b32 s100,s33,3\ns_lshl1_add_u32 s100,s100,6\ns_sub_u32 s101,s100,7\ns_min_u32 s100,s100,s101\n" \
"s_lshl_b32 s101,s100,12\ns_add_u32 s101,s101,s57\ns_add_u32 m0,s101,0\ns_and_b32 s101,s100,1\n" \
"s_lshr_b32 s100,s100,1\ns_lshl_b32 s100,s100,10\ns_add_u32 s100,s100,s101\ns_lshl_b32 s100,s100,7\n" \
"s_add_u32 s100,s100,s92\ns_add_u32 s100,s100,262144\nbuffer_load_dwordx4 v4,s[40:43],s100 offen lds\nbuffer_load_dwordx4 v5,s[40:43],s100 offen offset:1024 lds\n" \
"buffer_load_dwordx4 v6,s[40:43],s100 offen offset:2048 lds\nbuffer_load_dwordx4 v7,s[40:43],s100 offen offset:3072 lds\ns_branch .Lgemm_%=\n.LN_%=:\n" \
"v_bfe_u32 v112,v0,3,2\nv_bfe_u32 v113,v0,5,1\nv_lshl_add_u32 v112,v112,8,v113\ns_lshl_b32 s48,s33,2\n" \
"v_lshrrev_b32 v113,3,v0\ns_add_u32 s48,s48,s3\nv_and_b32 v113,6,v113\nv_and_b32 v114,7,v0\n" \
"s_mul_i32 s48,s48,18432\ns_mul_i32 s49,s32,1152\nv_mul_u32_u24 v112,9216,v112\nv_xor_b32 v113,v113,v114\n" \
"s_add_u32 s48,s48,s49\nv_lshl_add_u32 v4,v113,4,v112\ns_mov_b32 s10,18874368\ns_mov_b32 s11,0x20000\n" \
"s_add_u32 s48,s48,1024\ns_add_u32 m0,s88,20224\ns_add_u32 s50,s48,9437184\nbuffer_load_dwordx4 v4,s[8:11],s48 offen lds\n" \
"s_add_u32 m0,s88,24320\ns_mov_b32 s51,4096\nbuffer_load_dwordx4 v4,s[8:11],s50 offen lds\ns_add_u32 m0,s88,28416\n" \
"s_mov_b32 s52,8192\nbuffer_load_dwordx4 v2,s[4:7],0 offen lds\ns_add_u32 m0,s88,32512\ns_mov_b32 s53,14336\n" \
"buffer_load_dwordx4 v2,s[4:7],s51 offen lds\nbuffer_load_dwordx4 v[234:237],v1,s[4:7],s52 offen\nbuffer_load_dwordx4 v[238:241],v1,s[4:7],s52 offen offset:1024\nbuffer_load_dwordx4 v[242:245],v2,s[4:7],s53 offen\n" \
"s_mov_b32 s52,18432\nbuffer_load_dwordx4 v[252:255],v2,s[4:7],s52 offen\ns_and_b32 s60,s33,7\ns_mul_i32 s92,s3,589824\n" \
"s_mul_i32 s57,s3,30720\ns_add_u32 s92,s92,s49\ns_lshl_b32 s62,s60,7\nv_add_u32 v5,17408,v4\n" \
"v_add_u32 v6,34816,v4\nv_add_u32 v7,52224,v4\ns_add_u32 s57,s57,38272\ns_add_u32 s92,s92,s62\n" \
"s_load_dwordx4 s[44:47],s[0:1],72\nv_and_b32 v120,0x3c0,v1\ns_lshr_b32 s59,s33,3\nv_bfe_u32 v121,v0,3,2\n" \
"v_lshlrev_b32 v120,1,v120\ns_lshl_b32 s56,s34,8\ns_lshl_b32 s59,s59,3\nv_bfe_u32 v113,v0,4,2\n" \
"v_lshl_add_u32 v120,v121,5,v120\nv_and_b32 v122,3,v0\ns_mul_i32 s54,s34,9344\ns_add_u32 s56,s56,10289152\n" \
"s_add_u32 s59,s59,s32\nv_mul_u32_u24 v113,589824,v113\nv_and_b32 v112,15,v0\nv_lshl_add_u32 v120,v122,3,v120\n" \
"s_add_u32 s35,s54,524288\ns_cmp_eq_u32 s3,2\ns_cselect_b32 s56,s56,2147483648\ns_lshl_b32 s59,s59,3\n" \
"v_lshl_add_u32 v113,v112,4,v113\ns_add_u32 s58,s35,8192\nv_bfe_u32 v119,v0,4,2\nv_bfe_u32 v233,v0,3,3\n" \
"v_mul_u32_u24 v233,8192,v233\nv_lshl_add_u32 v233,v1,3,v233\ns_lshl2_add_u32 s100,s33,s3\ns_add_u32 s100,s100,4294967260\n" \
"s_and_b32 s101,s100,31\ns_lshl_b32 s101,s101,3\ns_cmp_lt_u32 s100,256\ns_cselect_b32 s100,s100,0x40000\n" \
"s_lshr_b32 s100,s100,5\ns_add_u32 s100,s100,s101\ns_lshl_b32 s100,s100,6\ns_add_u32 s100,s100,s32\n" \
"s_mul_i32 s100,s100,1152\ns_mov_b32 m0,162176\nv_xor_b32 v8,0,v120\nv_xor_b32 v9,32,v120\n" \
"v_xor_b32 v10,64,v120\nv_xor_b32 v11,96,v120\ns_add_u32 s59,s59,s60\nv_and_b32 v118,15,v0\n" \
"v_and_b32 v116,0x70,v2\nv_add_u32 v113,s56,v113\nv_add_u32 v114,s58,v2\nv_cmp_gt_u32 vcc,68,v0\n" \
"v_lshrrev_b32 v115,3,v0\nv_cvt_f32_u32 v120,s34\ns_lshl_b32 s59,s59,7\ns_lshl_b32 s61,s34,4\n" \
"v_lshrrev_b32 v48,1,v118\nv_and_b32 v49,1,v119\nv_cndmask_b32_e64 v113,v113,v114,vcc\nv_lshl_add_u32 v115,v115,16,v116\n" \
"v_mul_f32 v50,0x3b800000,v120\ns_add_u32 s87,s59,262144\ns_add_u32 s61,s61,65536\nv_cvt_f32_u32 v48,v48\n" \
"v_cvt_f32_u32 v49,v49\nv_bfe_u32 v100,v0,1,2\nv_and_b32 v101,1,v0\nv_add_u32 v115,s87,v115\n" \
"v_mov_b32 v116,s61\nv_cmp_gt_u32 vcc,24,v0\nv_mul_f32 v48,0xbe000000,v48\nv_fma_f32 v49,v49,2.0,v50\n" \
"v_lshl_add_u32 v100,v101,2,v100\nv_cndmask_b32 v115,v116,v115,vcc\nv_mul_f32 v49,v48,v49\nv_mul_f32 v101,0x3a000000,v120\n" \
"v_cvt_f32_u32 v100,v100\nv_bfrev_b32 v116,1\nv_cmp_gt_u32 vcc,25,v0\nv_fmamk_f32 v63,v48,0x40800000,v49\n" \
"v_fmamk_f32 v100,v100,0x3e000000,v101\nv_cndmask_b32 v115,v116,v115,vcc\nv_mov_b32 v16,-1\nv_mov_b32 v17,-1\n" \
"v_mov_b32 v18,-1\nv_mov_b32 v19,-1\nv_lshrrev_b32 v50,1,v119\nv_and_b32 v51,1,v0\n" \
"v_cos_f32 v62,v63\nv_cvt_f32_u32 v101,v119\nbuffer_store_dwordx4 v[16:19],v2,s[36:39],s35 offen sc1\nv_cmp_eq_u32 vcc,v50,v51\n" \
"v_add_f32 v101,v101,v101\nv_mov_b32 v50,0x3e800000\nv_mul_f32 v101,v101,v100\nv_cndmask_b32 v52,0,v50,vcc\n" \
"v_cos_f32 v56,v49\nv_sin_f32 v57,v49\nv_sin_f32 v63,v63\nv_cos_f32 v106,v101\n" \
"v_sin_f32 v107,v101\nv_cmp_gt_u32 vcc,8,v118\ns_add_u32 s55,s35,4096\nv_lshlrev_b32 v51,31,v0\n" \
"v_cndmask_b32 v102,0,0.5,vcc\nbuffer_store_dwordx4 v[16:19],v2,s[36:39],s55 offen sc1\nv_xor_b32 v54,v51,v52\nv_sub_f32_e64 v104,0.5,v102\n" \
"v_pk_mul_f32 v[58:59],v[56:57],v[52:53] op_sel_hi:[1,0]\nv_pk_mul_f32 v[60:61],v[56:57],v[54:55] op_sel_hi:[1,0]\nv_pk_mul_f32 v[64:65],v[62:63],v[52:53] op_sel_hi:[1,0]\nv_pk_mul_f32 v[66:67],v[62:63],v[54:55] op_sel_hi:[1,0]\n" \
"v_pk_mul_f32 v[108:109],v[106:107],v[102:103] op_sel_hi:[1,0]\nv_pk_mul_f32 v[110:111],v[106:107],v[104:105] op_sel_hi:[1,0]\nv_fmamk_f32 v57,v48,0x3f800000,v49\nv_fmamk_f32 v63,v48,0x40a00000,v49\n" \
"v_fmamk_f32 v107,v100,0x3f800000,v101\nv_cos_f32 v56,v57\nv_cos_f32 v62,v63\nv_cos_f32 v106,v107\n" \
"buffer_store_dwordx4 v[16:19],v113,s[36:39],0 offen sc1\nv_sin_f32 v57,v57\nv_sin_f32 v63,v63\nv_sin_f32 v107,v107\n" \
"v_mov_b32 v20,0\nv_mov_b32 v21,0\nv_mov_b32 v22,0\nv_mov_b32 v23,0\n" \
"buffer_store_dwordx4 v[20:23],v115,s[36:39],0 offen sc1\nv_cvt_pk_f16_f32 v40,v58,-v61\nv_cvt_pk_f16_f32 v44,v59,v60\nv_cvt_pk_f16_f32 v41,v64,-v67\n" \
"v_cvt_pk_f16_f32 v45,v65,v66\nv_cvt_pk_f16_f32 v216,v108,-v109\nv_cvt_pk_f16_f32 v220,v109,v108\nv_cvt_pk_f16_f32 v217,v110,-v111\n" \
"v_cvt_pk_f16_f32 v221,-v111,-v110\nv_pk_mul_f32 v[58:59],v[56:57],v[52:53] op_sel_hi:[1,0]\nv_pk_mul_f32 v[60:61],v[56:57],v[54:55] op_sel_hi:[1,0]\nv_pk_mul_f32 v[64:65],v[62:63],v[52:53] op_sel_hi:[1,0]\n" \
"v_pk_mul_f32 v[66:67],v[62:63],v[54:55] op_sel_hi:[1,0]\nv_pk_mul_f32 v[108:109],v[106:107],v[102:103] op_sel_hi:[1,0]\nv_pk_mul_f32 v[110:111],v[106:107],v[104:105] op_sel_hi:[1,0]\nv_lshlrev_b32 v3,4,v119\n" \
"v_lshrrev_b32 v15,1,v1\nv_lshl_add_u32 v3,v118,6,v3\nbuffer_load_dword v233,s[8:11],s100 offen lds\nv_cvt_pk_f16_f32 v42,v58,-v61\n" \
"v_cvt_pk_f16_f32 v46,v59,v60\nv_cvt_pk_f16_f32 v43,v64,-v67\nv_cvt_pk_f16_f32 v47,v65,v66\nv_cvt_pk_f16_f32 v218,v108,-v109\n" \
"v_cvt_pk_f16_f32 v222,v109,v108\nv_cvt_pk_f16_f32 v219,v110,-v111\nv_cvt_pk_f16_f32 v223,-v111,-v110\nv_add_u32 v15,s57,v15\n" \
"v_add_u32 v8,s57,v8\nv_add_u32 v9,s57,v9\nv_add_u32 v10,s57,v10\nv_add_u32 v11,s57,v11\n" \
"v_lshrrev_b32 v77,5,v0\nv_bfe_u32 v113,v0,1,5\nv_and_b32 v115,1,v0\ns_lshl_b32 s63,s3,1\n" \
"v_mul_u32_u24 v77,1160,v77\nv_and_b32 v76,31,v0\nv_and_b32 v114,6,v113\nv_add_u32 v115,s63,v115\n" \
"v_xor_b32 v114,v114,v115\nv_lshlrev_b32 v113,7,v113\nv_lshl_add_u32 v113,v114,4,v113\ns_add_u32 s64,s54,2916352\n" \
"s_add_u32 s65,s64,4096\nv_bfrev_b32 v114,1\ns_lshl1_add_u32 s95,s33,s3\ns_add_u32 s95,s95,74\n" \
"s_cmp_lt_u32 s3,2\ns_cselect_b32 s95,s95,256\ns_and_b32 s100,s95,31\ns_lshl_b32 s100,s100,3\n" \
"s_cmp_lt_u32 s95,256\ns_cselect_b32 s95,s95,0x40000\ns_lshr_b32 s95,s95,5\ns_add_u32 s95,s95,s100\n" \
"s_lshl_b32 s95,s95,6\ns_add_u32 s95,s95,s32\ns_mul_i32 s95,s95,1152\ns_mov_b32 m0,162176\n" \
"s_lshl_b32 s101,s33,7\ns_sub_u32 s101,s101,1280\nv_add_u32 v79,s101,v0\nv_mul_u32_u24 v78,52429,v79\n" \
"v_lshrrev_b32 v78,18,v78\nv_lshlrev_b32 v79,7,v79\ns_mov_b32 s101,6272\nv_mad_u32_u24 v79,v78,s101,v79\n" \
"s_mov_b32 s26,3981312\ns_mov_b32 s27,0x20000\nv_mad_u32_u24 v78,v76,36,v77\nv_cmp_gt_u32 vcc,72,v0\n" \
"s_lshl_b32 s67,s32,2\ns_add_u32 s66,s64,8192\nv_cndmask_b32 v114,v114,v2,vcc\ns_add_u32 s67,s67,s3\n" \
"s_lshl_b32 s67,s67,5\ns_add_u32 s67,s67,s33\ns_lshl_b32 s67,s67,11\ns_add_u32 s67,s67,5308416\n" \
"s_lshl2_add_u32 s69,s3,161664\ns_waitcnt vmcnt(20)\nv_readfirstlane_b32 s28,v126\nv_readfirstlane_b32 s29,v127\n" \
"s_mul_i32 s72,s3,2320\nv_mov_b32 v251,s69\nv_cmp_gt_u32 vcc,32,v0\ns_add_u32 s28,s28,1\n" \
"s_addc_u32 s29,s29,0\ns_lshl_b32 s7,s32,11\nv_cndmask_b32 v74,2.0,0,vcc\ns_mov_b32 s93,128\n" \
"s_mov_b32 s73,8192\ns_mov_b32 s74,12288\ns_mov_b32 s75,17408\ns_mov_b32 s76,21504\n" \
"s_mov_b32 s77,16384\ns_mov_b32 s78,25600\ns_add_u32 s7,s7,40960\ns_mov_b32 s81,0x3d800000\n" \
"s_mov_b32 s42,18874368\ns_mov_b32 s43,0x20000\ns_waitcnt lgkmcnt(0)\ns_mov_b64 s[40:41],s[44:45]\n" \
"s_mov_b64 s[44:45],s[46:47]\ns_mov_b32 s46,2359296\ns_mov_b32 s47,0x20000\ns_waitcnt vmcnt(19)\n" \
"v_lshlrev_b32_e64 v48,16,v224\nv_and_b32 v49,0xffff0000,v224\nv_readlane_b32 s70,v48,0\nv_readlane_b32 s71,v48,32\n" \
"v_and_b32 v51,0xffff0000,v225\nv_mov_b32 v72,s70\nv_mov_b32 v73,s71\nv_lshlrev_b32 v50,16,v225\n" \
"v_and_b32 v53,0xffff0000,v226\nv_permlane32_swap_b32 v72,v73\nv_pk_add_f32 v[48:49],v[48:49],v[72:73] op_sel_hi:[1,0] neg_lo:[0,1] neg_hi:[0,1]\nv_pk_add_f32 v[50:51],v[50:51],v[72:73] op_sel_hi:[1,0] neg_lo:[0,1] neg_hi:[0,1]\n" \
"v_pk_mul_f32 v[68:69],v[48:49],v[48:49]\nv_lshlrev_b32_e64 v52,16,v226\nv_and_b32 v55,0xffff0000,v227\nv_pk_add_f32 v[52:53],v[52:53],v[72:73] op_sel_hi:[1,0] neg_lo:[0,1] neg_hi:[0,1]\n" \
"v_pk_add_f32 v[66:67],v[48:49],v[50:51]\nv_pk_fma_f32 v[68:69],v[50:51],v[50:51],v[68:69]\nv_lshlrev_b32 v54,16,v227\ns_waitcnt vmcnt(18)\n" \
"v_and_b32 v57,0xffff0000,v228\nv_pk_add_f32 v[54:55],v[54:55],v[72:73] op_sel_hi:[1,0] neg_lo:[0,1] neg_hi:[0,1]\nv_pk_add_f32 v[66:67],v[66:67],v[52:53]\nv_pk_fma_f32 v[68:69],v[52:53],v[52:53],v[68:69]\n" \
"v_lshlrev_b32_e64 v56,16,v228\nv_and_b32 v59,0xffff0000,v229\nv_pk_add_f32 v[56:57],v[56:57],v[72:73] op_sel_hi:[1,0] neg_lo:[0,1] neg_hi:[0,1]\nv_pk_add_f32 v[66:67],v[66:67],v[54:55]\n" \
"v_pk_fma_f32 v[68:69],v[54:55],v[54:55],v[68:69]\nv_lshlrev_b32_e64 v58,16,v229\nv_and_b32 v61,0xffff0000,v230\nv_pk_add_f32 v[58:59],v[58:59],v[72:73] op_sel_hi:[1,0] neg_lo:[0,1] neg_hi:[0,1]\n" \
"v_pk_add_f32 v[66:67],v[66:67],v[56:57]\nv_pk_fma_f32 v[68:69],v[56:57],v[56:57],v[68:69]\nv_lshlrev_b32_e64 v60,16,v230\nv_and_b32 v63,0xffff0000,v231\n" \
"v_pk_add_f32 v[60:61],v[60:61],v[72:73] op_sel_hi:[1,0] neg_lo:[0,1] neg_hi:[0,1]\nv_pk_add_f32 v[66:67],v[66:67],v[58:59]\nv_pk_fma_f32 v[68:69],v[58:59],v[58:59],v[68:69]\nv_lshlrev_b32 v62,16,v231\n" \
"s_waitcnt vmcnt(17)\nv_and_b32 v65,0xffff0000,v232\nv_pk_add_f32 v[62:63],v[62:63],v[72:73] op_sel_hi:[1,0] neg_lo:[0,1] neg_hi:[0,1]\nv_pk_add_f32 v[66:67],v[66:67],v[60:61]\n" \
"v_pk_fma_f32 v[68:69],v[60:61],v[60:61],v[68:69]\nv_lshlrev_b32_e64 v64,16,v232\nv_pk_add_f32 v[64:65],v[64:65],v[72:73] op_sel_hi:[1,0] neg_lo:[0,1] neg_hi:[0,1]\nv_pk_add_f32 v[66:67],v[66:67],v[62:63]\n" \
"v_pk_fma_f32 v[68:69],v[62:63],v[62:63],v[68:69]\nv_pk_add_f32 v[66:67],v[66:67],v[64:65]\nv_pk_fma_f32 v[68:69],v[64:65],v[64:65],v[68:69]\nv_add_f32 v66,v66,v67\n" \
"v_add_f32 v68,v68,v69\ns_nop 1\nv_permlane16_swap_b32 v66,v68\nv_add_f32 v66,v66,v68\n" \
"s_nop 1\nv_add_f32_dpp v66,v66,v66 quad_perm:[1,0,3,2] row_mask:0xf bank_mask:0xf bound_ctrl:1\ns_nop 1\nv_add_f32_dpp v66,v66,v66 quad_perm:[2,3,0,1] row_mask:0xf bank_mask:0xf bound_ctrl:1\n" \
"s_nop 1\nv_add_f32_dpp v66,v66,v66 row_half_mirror row_mask:0xf bank_mask:0xf bound_ctrl:1\ns_nop 1\nv_add_f32_dpp v66,v66,v66 row_mirror row_mask:0xf bank_mask:0xf bound_ctrl:1\n" \
"v_mul_f32 v70,0x3ae38e39,v66\nv_mul_f32 v68,0x3ae38e39,v66\ns_nop 1\nv_permlane16_swap_b32 v70,v68\n" \
"v_fma_f32 v68,-v70,v70,v68\nv_max_f32 v68,0,v68\nv_add_f32 v68,0x3727c5ac,v68\nv_rsq_f32 v66,v68\n" \
"v_add_f32 v250,v70,v72\nv_mul_f32 v67,-v70,v66\nv_pk_fma_f32 v[48:49],v[48:49],v[66:67],v[66:67] op_sel:[0,0,1] op_sel_hi:[1,0,1]\nv_pk_fma_f32 v[50:51],v[50:51],v[66:67],v[66:67] op_sel:[0,0,1] op_sel_hi:[1,0,1]\n" \
"v_cvt_pk_f16_f32 v80,v48,v49\nv_cvt_pk_f16_f32 v81,v50,v51\nv_pk_fma_f32 v[52:53],v[52:53],v[66:67],v[66:67] op_sel:[0,0,1] op_sel_hi:[1,0,1]\nv_pk_fma_f32 v[54:55],v[54:55],v[66:67],v[66:67] op_sel:[0,0,1] op_sel_hi:[1,0,1]\n" \
"ds_write2_b32 v78,v80,v81 offset0:0 offset1:1\nv_cvt_pk_f16_f32 v82,v52,v53\nv_cvt_pk_f16_f32 v83,v54,v55\nv_pk_fma_f32 v[56:57],v[56:57],v[66:67],v[66:67] op_sel:[0,0,1] op_sel_hi:[1,0,1]\n" \
"v_pk_fma_f32 v[58:59],v[58:59],v[66:67],v[66:67] op_sel:[0,0,1] op_sel_hi:[1,0,1]\nds_write2_b32 v78,v82,v83 offset0:2 offset1:3\nv_cvt_pk_f16_f32 v84,v56,v57\nv_cvt_pk_f16_f32 v85,v58,v59\n" \
"v_pk_fma_f32 v[60:61],v[60:61],v[66:67],v[66:67] op_sel:[0,0,1] op_sel_hi:[1,0,1]\nv_pk_fma_f32 v[62:63],v[62:63],v[66:67],v[66:67] op_sel:[0,0,1] op_sel_hi:[1,0,1]\nds_write2_b32 v78,v84,v85 offset0:4 offset1:5\nv_cvt_pk_f16_f32 v86,v60,v61\n" \
"v_cvt_pk_f16_f32 v87,v62,v63\nv_pk_fma_f32 v[64:65],v[64:65],v[66:67],v[66:67] op_sel:[0,0,1] op_sel_hi:[1,0,1]\nds_write2_b32 v78,v86,v87 offset0:6 offset1:7\nv_cvt_pk_f16_f32 v88,v64,v65\n" \
"ds_write_b32 v78,v88 offset:32\ns_waitcnt vmcnt(11)\ns_barrier\nds_read_u16 v16,v246\n" \
"ds_read_u16 v17,v246 offset:5216\nds_read_u16 v18,v246 offset:578\nds_read_u16 v19,v246 offset:5794\nds_read_u16 v20,v246 offset:1156\n" \
"ds_read_u16 v21,v246 offset:6372\nds_read_u16 v22,v246 offset:1734\nds_read_u16 v23,v246 offset:6950\ns_waitcnt vmcnt(8)\n" \
"v_lshrrev_b32 v96,1,v234\nds_read_u16 v24,v247\nv_lshrrev_b32 v97,1,v235\nv_lshrrev_b32 v98,1,v236\n" \
"s_waitcnt lgkmcnt(7)\nv_lshl_or_b32 v16,v17,16,v16\nv_lshrrev_b32 v99,1,v237\nds_read_u16 v25,v247 offset:5216\n" \
"s_waitcnt vmcnt(7)\nv_lshrrev_b32 v100,1,v238\nv_lshrrev_b32 v101,1,v239\nv_lshrrev_b32 v102,1,v240\n" \
"v_lshrrev_b32 v103,1,v241\nds_read_u16 v26,v247 offset:578\nv_add_u32 v96,s72,v96\nv_add_u32 v97,s72,v97\n" \
"s_waitcnt lgkmcnt(7)\nv_lshl_or_b32 v17,v19,16,v18\nv_add_u32 v98,s72,v98\nds_read_u16 v27,v247 offset:5794\n" \
"v_add_u32 v99,s72,v99\nv_add_u32 v100,s72,v100\nv_add_u32 v101,s72,v101\nv_add_u32 v102,s72,v102\n" \
"ds_read_u16 v28,v247 offset:1156\nv_add_u32 v103,s72,v103\nv_pk_maximum3_f16 v124,v16,v17,v16\ns_waitcnt lgkmcnt(7)\n" \
"v_lshl_or_b32 v18,v21,16,v20\nv_pk_minimum3_f16 v125,v16,v17,v16\nds_read_u16 v29,v247 offset:6372\nds_read_u16 v30,v247 offset:1734\n" \
"s_waitcnt lgkmcnt(7)\nv_lshl_or_b32 v19,v23,16,v22\nbuffer_store_dwordx4 v[16:19],v2,s[36:39],s64 offen sc1\nds_read_u16 v31,v247 offset:6950\n" \
"ds_read_u16 v104,v248\nv_pk_maximum3_f16 v124,v18,v19,v124\nv_pk_minimum3_f16 v125,v18,v19,v125\ns_waitcnt lgkmcnt(7)\n" \
"v_lshl_or_b32 v24,v25,16,v24\nds_read_u16 v105,v248 offset:5216\nds_read_u16 v106,v248 offset:578\ns_waitcnt lgkmcnt(7)\n" \
"v_lshl_or_b32 v25,v27,16,v26\nv_pk_maximum3_f16 v124,v24,v25,v124\nds_read_u16 v107,v248 offset:5794\nv_pk_minimum3_f16 v125,v24,v25,v125\n" \
"ds_read_u16 v108,v248 offset:1156\ns_waitcnt lgkmcnt(7)\nv_lshl_or_b32 v26,v29,16,v28\nds_read_u16 v109,v248 offset:6372\n" \
"ds_read_u16 v110,v248 offset:1734\ns_waitcnt lgkmcnt(7)\nv_lshl_or_b32 v27,v31,16,v30\nbuffer_store_dwordx4 v[24:27],v2,s[36:39],s65 offen sc1\n" \
"ds_read_u16 v111,v248 offset:6950\nds_read_b128 v[116:119],v113 offset:20224\nv_pk_maximum3_f16 v124,v26,v27,v124\nv_pk_minimum3_f16 v125,v26,v27,v125\n" \
"ds_read_b128 v[120:123],v113 offset:24320\ns_waitcnt lgkmcnt(8)\nv_lshl_or_b32 v104,v105,16,v104\nds_read_b32 v16,v96\n" \
"ds_read_b32 v24,v96 offset:1160\nds_read_b32 v17,v97\nds_read_b32 v25,v97 offset:1160\ns_waitcnt lgkmcnt(10)\n" \
"v_lshl_or_b32 v105,v107,16,v106\nds_read_b32 v18,v98\nv_pk_maximum3_f16 v124,v104,v105,v124\nds_read_b32 v26,v98 offset:1160\n" \
"v_pk_minimum3_f16 v125,v104,v105,v125\nds_read_b32 v19,v99\nds_read_b32 v27,v99 offset:1160\nds_read_b32 v20,v100\n" \
"s_waitcnt lgkmcnt(13)\nv_lshl_or_b32 v106,v109,16,v108\nds_read_b32 v28,v100 offset:1160\nds_read_b32 v21,v101\n" \
"ds_read_b32 v29,v101 offset:1160\ns_waitcnt lgkmcnt(14)\nv_lshl_or_b32 v107,v111,16,v110\nbuffer_store_dwordx4 v[104:107],v114,s[36:39],s66 offen sc1\n" \
"s_waitcnt lgkmcnt(13)\nbuffer_store_dwordx4 v[116:119],v1,s[36:39],s67 offen sc1\ns_waitcnt lgkmcnt(12)\nbuffer_store_dwordx4 v[120:123],v1,s[36:39],s67 offen offset:1024 sc1\n" \
"ds_read_b32 v22,v102\nds_read_b32 v30,v102 offset:1160\nds_read_b32 v23,v103\nds_read_b32 v31,v103 offset:1160\n" \
"v_pk_maximum3_f16 v124,v106,v107,v124\nbuffer_load_dword v233,s[8:11],s95 offen lds\nv_and_b32 v124,0x7fff7fff,v124\nv_pk_minimum3_f16 v125,v106,v107,v125\n" \
"v_and_b32 v125,0x7fff7fff,v125\nv_pk_max_u16 v124,v124,v125\nv_lshlrev_b32 v125,16,v124\nv_max_u32 v124,v124,v125\n" \
"s_mov_b32 m0,162176\ns_nop 0\nv_max_u32_dpp v124,v124,v124 quad_perm:[1,0,3,2] row_mask:0xf bank_mask:0xf bound_ctrl:1\ns_nop 1\n" \
"v_max_u32_dpp v124,v124,v124 quad_perm:[2,3,0,1] row_mask:0xf bank_mask:0xf bound_ctrl:1\ns_nop 1\nv_max_u32_dpp v124,v124,v124 row_half_mirror row_mask:0xf bank_mask:0xf bound_ctrl:1\ns_nop 1\n" \
"v_max_u32_dpp v124,v124,v124 row_mirror row_mask:0xf bank_mask:0xf bound_ctrl:1\ns_nop 0\nv_readlane_b32 s68,v124,0\nv_readlane_b32 s31,v124,16\n" \
"v_readlane_b32 s96,v124,32\nv_readlane_b32 s97,v124,48\ns_max_u32 s31,s31,s96\ns_max_u32 s68,s68,s97\n" \
"s_max_u32 s68,s68,s31\ns_mul_i32 s31,s32,576\ns_add_u32 s31,s31,2304\ns_andn2_b32 s31,s31,127\n" \
"s_cmp_gt_u32 s3,1\ns_cselect_b32 s31,s31,2147483648\nbuffer_load_dword v79,s[24:27],s31 offen lds\nv_mov_b32 v124,s68\n" \
"ds_write_b32 v251,v124 offset:32\nv_mov_b32 v114,161664\nbuffer_load_dwordx4 v[48:51],v2,s[36:39],s73 offen sc1\nbuffer_load_dwordx4 v[60:63],v2,s[36:39],s75 offen sc1\n" \
"buffer_load_dwordx4 v[52:55],v2,s[36:39],s74 offen sc1\nbuffer_load_dwordx4 v[64:67],v2,s[36:39],s76 offen sc1\nbuffer_load_dwordx4 v[56:59],v1,s[36:39],s77 offen sc1\nbuffer_load_dwordx4 v[68:71],v1,s[36:39],s78 offen sc1\n" \
"s_mov_b32 s79,0xffffff\ns_lshl3_add_u32 s80,s2,4294615040\nv_add_u32 v112,28416,v1\ns_waitcnt vmcnt(8) lgkmcnt(0)\n" \
"s_barrier\nds_read_b128 v[116:119],v114 offset:32\nds_read_b128 v[192:195],v112\nds_read_b128 v[196:199],v112 offset:1024\n" \
"ds_read_b128 v[200:203],v112 offset:2048\nds_read_b128 v[204:207],v112 offset:3072\nds_read_b128 v[208:211],v112 offset:4096\nds_read_b128 v[212:215],v112 offset:5120\n" \
"ds_read_b128 v[32:35],v112 offset:6144\nds_read_b128 v[36:39],v112 offset:7168\ns_waitcnt lgkmcnt(8)\nv_max3_u32 v116,v116,v117,v118\n" \
"v_max_u32 v116,v116,v119\nv_and_b32 v119,0xf8,v0\nv_cmp_ne_u32 vcc,192,v119\nv_lshlrev_b32 v116,1,v116\n" \
"v_mov_b32 v117,s29\nv_lshlrev_b32 v118,11,v0\nv_bfi_b32 v117,s79,v117,v116\nv_add_u32 v118,s80,v118\n" \
"v_bfrev_b32 v119,1\nv_cndmask_b32 v118,v118,v119,vcc\nv_mov_b32 v116,s28\nbuffer_store_dwordx2 v[116:117],v118,s[36:39],0 offen sc1\n" \
"s_waitcnt vmcnt(6)\nv_cmp_ne_u64 vcc,s[28:29],v[48:49]\ns_waitcnt vmcnt(4)\nv_cmp_ne_u64 s[84:85],s[28:29],v[52:53]\n" \
"s_waitcnt vmcnt(2)\nv_cmp_ne_u64 s[82:83],s[28:29],v[56:57]\ns_or_b64 vcc,vcc,s[84:85]\nv_cmp_ne_u64 s[84:85],s[28:29],v[60:61]\n" \
"s_or_b64 vcc,vcc,s[82:83]\nv_cmp_ne_u64 s[82:83],s[28:29],v[64:65]\ns_or_b64 vcc,vcc,s[84:85]\ns_waitcnt vmcnt(1)\n" \
"v_cmp_ne_u64 s[84:85],s[28:29],v[68:69]\ns_or_b64 vcc,vcc,s[82:83]\ns_or_b64 vcc,vcc,s[84:85]\ns_cbranch_vccnz .LsmN_%=\n" \
".LbmN_%=:\nv_pk_add_f32 v[50:51],v[50:51],v[62:63]\nv_pk_add_f32 v[54:55],v[54:55],v[66:67]\nv_pk_mul_f32 v[62:63],v[50:51],2.0 op_sel_hi:[1,0]\n" \
"v_pk_add_f32 v[58:59],v[58:59],v[70:71]\nv_max3_f32 v112,|v62|,|v63|,0\nv_pk_fma_f32 v[66:67],v[54:55],2.0,v[74:75] op_sel_hi:[1,0,0]\nv_max3_f32 v112,|v66|,|v67|,v112\n" \
"v_pk_fma_f32 v[70:71],v[58:59],2.0,2.0 op_sel_hi:[1,0,0]\nv_max3_f32 v112,|v70|,|v71|,v112\nv_cmp_lt_f32 s[82:83],s81,v112\nv_cmp_ngt_f32 vcc,0x41000000,v112\n" \
"s_cmp_lg_u64 s[82:83],0\ns_cselect_b32 s82,2,0\nv_cvt_pk_f16_f32 v62,v62,v63\ns_cmp_lg_u64 vcc,0\n" \
"s_addc_u32 s82,s82,0\nds_write_b32 v252,v62 offset:62848\nv_cvt_pk_f16_f32 v66,v66,v67\nds_write_b32 v253,v66 offset:62848\n" \
"v_cvt_pk_f16_f32 v70,v70,v71\nv_mov_b32 v113,s82\nds_write_b32 v254,v70 offset:62848\nds_write_b32 v251,v113\n" \
"s_waitcnt lgkmcnt(0)\ns_barrier\nds_read_b128 v[124:127],v114\nds_read_b64 v[96:97],v234 offset:62848\n" \
"ds_read_b64 v[98:99],v235 offset:62848\nds_read_b64 v[100:101],v236 offset:62848\nds_read_b64 v[102:103],v237 offset:62848\nds_read_b64 v[104:105],v238 offset:62848\n" \
"ds_read_b64 v[106:107],v239 offset:62848\nds_read_b64 v[108:109],v240 offset:62848\nds_read_b64 v[110:111],v241 offset:62848\ns_waitcnt lgkmcnt(8)\n" \
"v_or3_b32 v124,v124,v125,v126\nv_or_b32 v124,v124,v127\nv_cmp_ne_u32 vcc,2,v124\ns_nop 0\n" \
"s_cbranch_vccnz .LsxN_%=\n.LbxN_%=:\ns_waitcnt lgkmcnt(7)\nv_pk_fma_f16 v80,v16,v96,v97\n" \
"s_waitcnt lgkmcnt(6)\nv_pk_fma_f16 v81,v17,v98,v99\ns_waitcnt lgkmcnt(5)\nv_pk_fma_f16 v82,v18,v100,v101\n" \
"s_waitcnt lgkmcnt(4)\nv_pk_fma_f16 v83,v19,v102,v103\nv_pk_fma_f16 v88,v24,v96,v97\ns_waitcnt lgkmcnt(3)\n" \
"v_pk_fma_f16 v84,v20,v104,v105\ns_waitcnt lgkmcnt(2)\nv_pk_fma_f16 v85,v21,v106,v107\nv_pk_fma_f16 v89,v25,v98,v99\n" \
"s_waitcnt lgkmcnt(1)\nv_pk_fma_f16 v86,v22,v108,v109\nv_pk_fma_f16 v90,v26,v100,v101\ns_waitcnt lgkmcnt(0)\n" \
"v_pk_fma_f16 v87,v23,v110,v111\nv_pk_fma_f16 v91,v27,v102,v103\nv_pk_fma_f16 v92,v28,v104,v105\nv_pk_fma_f16 v93,v29,v106,v107\n" \
"v_pk_fma_f16 v94,v30,v108,v109\nv_bfe_u32 v233,v0,3,3\nv_mul_u32_u24 v233,8192,v233\nv_lshl_add_u32 v233,v1,3,v233\n" \
"s_lshl2_add_u32 s96,s33,s3\ns_add_u32 s96,s96,138\ns_cmp_lt_u32 s33,13\ns_cselect_b32 s96,s96,256\n" \
"s_and_b32 s97,s96,31\ns_lshl_b32 s97,s97,3\ns_cmp_lt_u32 s96,256\ns_cselect_b32 s96,s96,0x40000\n" \
"s_lshr_b32 s96,s96,5\ns_add_u32 s96,s96,s97\ns_lshl_b32 s96,s96,6\ns_add_u32 s96,s96,s32\n" \
"s_mul_i32 s96,s96,1152\ns_mov_b32 m0,162176\nv_pk_fma_f16 v95,v31,v110,v111\nbuffer_load_dword v233,s[8:11],s96 offen lds\n" \
"v_mfma_f32_16x16x32_f16 v[48:51],v[80:83],v[192:195],0\ns_add_u32 s73,s35,s88\ns_add_u32 s74,s73,4096\nv_mfma_f32_16x16x32_f16 v[52:55],v[80:83],v[196:199],0\n" \
"v_and_b32 v112,15,v0\nv_bfrev_b32 v113,1\nv_mfma_f32_16x16x32_f16 v[56:59],v[84:87],v[32:35],0\nv_cmp_eq_u32 vcc,0,v112\n" \
"s_add_u32 s75,s73,8192\nv_mfma_f32_16x16x32_f16 v[60:63],v[84:87],v[36:39],0\nv_cndmask_b32 v113,v113,v3,vcc\nv_cmp_gt_u32 vcc,64,v0\n" \
"v_mfma_f32_16x16x32_f16 v[64:67],v[88:91],v[192:195],0\ns_cmp_lt_u32 s3,2\ns_cselect_b32 s75,s75,2147483648\nv_mfma_f32_16x16x32_f16 v[68:71],v[88:91],v[196:199],0\n" \
"v_pk_add_f32 v[56:57],v[48:49],v[56:57] neg_lo:[0,1] neg_hi:[0,1]\nv_sub_f32 v58,v50,v58\nv_mfma_f32_16x16x32_f16 v[72:75],v[92:95],v[32:35],0\nv_pk_add_f32 v[60:61],v[52:53],v[60:61] neg_lo:[0,1] neg_hi:[0,1]\n" \
"v_sub_f32_e64 v62,v54,v62\nv_mfma_f32_16x16x32_f16 v[76:79],v[92:95],v[36:39],0\nv_pk_fma_f32 v[48:49],v[48:49],2.0,v[56:57] op_sel_hi:[1,0,1] neg_lo:[0,0,1] neg_hi:[0,0,1]\nv_fma_f32 v50,v50,2.0,-v58\n" \
"v_pk_fma_f32 v[52:53],v[52:53],2.0,v[60:61] op_sel_hi:[1,0,1] neg_lo:[0,0,1] neg_hi:[0,0,1]\nv_fma_f32 v54,v54,2.0,-v62\nv_cvt_pk_f16_f32 v16,v48,v52\nv_pk_add_f32 v[72:73],v[64:65],v[72:73] neg_lo:[0,1] neg_hi:[0,1]\n" \
"v_sub_f32 v74,v66,v74\nv_pk_fma_f32 v[64:65],v[64:65],2.0,v[72:73] op_sel_hi:[1,0,1] neg_lo:[0,0,1] neg_hi:[0,0,1]\nv_pk_add_f32 v[76:77],v[68:69],v[76:77] neg_lo:[0,1] neg_hi:[0,1]\nv_sub_f32 v78,v70,v78\n" \
"v_fma_f32 v66,v66,2.0,-v74\nv_pk_fma_f32 v[68:69],v[68:69],2.0,v[76:77] op_sel_hi:[1,0,1] neg_lo:[0,0,1] neg_hi:[0,0,1]\nv_fma_f32 v70,v70,2.0,-v78\nv_cvt_pk_f16_f32 v17,v49,v53\n" \
"v_cvt_pk_f16_f32 v18,v50,v54\nv_cvt_pk_f16_f32 v24,v64,v68\nv_cvt_pk_f16_f32 v25,v65,v69\nv_cvt_pk_f16_f32 v26,v66,v70\n" \
"v_mfma_f32_16x16x32_f16 v[48:51],v[200:203],v[16:19],0\nv_cvt_pk_f16_f32 v20,v56,v60\nv_cvt_pk_f16_f32 v21,v57,v61\nv_mfma_f32_16x16x32_f16 v[52:55],v[204:207],v[16:19],0\n" \
"v_cvt_pk_f16_f32 v22,v58,v62\nv_cvt_pk_f16_f32 v28,v72,v76\nv_mfma_f32_16x16x32_f16 v[64:67],v[200:203],v[24:27],0\nv_cvt_pk_f16_f32 v29,v73,v77\n" \
"v_cvt_pk_f16_f32 v30,v74,v78\nv_mfma_f32_16x16x32_f16 v[68:71],v[204:207],v[24:27],0\nv_cndmask_b32_e64 v113,v113,v3,vcc\nv_mfma_f32_16x16x32_f16 v[56:59],v[200:203],v[20:23],0\n" \
"v_cvt_pk_f16_f32 v80,v48,v52\nv_cvt_pk_f16_f32 v82,v49,v53\nv_mfma_f32_16x16x32_f16 v[60:63],v[204:207],v[20:23],0\nv_cvt_pk_f16_f32 v84,v50,v54\n" \
"v_mfma_f32_16x16x32_f16 v[72:75],v[200:203],v[28:31],0\ns_nop 0\nv_cvt_pk_f16_f32 v81,v64,v68\nv_mfma_f32_16x16x32_f16 v[76:79],v[204:207],v[28:31],0\n" \
"ds_write_b64 v255,v[80:81]\nv_cvt_pk_f16_f32 v83,v65,v69\nds_write_b64 v255,v[82:83] offset:512\nv_cvt_pk_f16_f32 v85,v66,v70\n" \
"ds_write_b64 v255,v[84:85] offset:1024\nv_cvt_pk_f16_f32 v86,v56,v60\nv_cvt_pk_f16_f32 v88,v57,v61\nv_cvt_pk_f16_f32 v90,v58,v62\n" \
"v_cvt_pk_f16_f32 v87,v72,v76\nds_write_b64 v255,v[86:87] offset:256\nv_cvt_pk_f16_f32 v89,v73,v77\nds_write_b64 v255,v[88:89] offset:768\n" \
"v_cvt_pk_f16_f32 v91,v74,v78\nds_write_b64 v255,v[90:91] offset:1280\ns_waitcnt lgkmcnt(0)\ns_barrier\n" \
"ds_read_b128 v[16:19],v243\nds_read_b128 v[20:23],v244\nds_read_b128 v[24:27],v245\ns_waitcnt lgkmcnt(2)\n" \
"v_mfma_f32_16x16x32_f16 v[48:51],v[40:43],v[16:19],0\nv_mfma_f32_16x16x32_f16 v[52:55],v[44:47],v[16:19],0\ns_waitcnt lgkmcnt(1)\nv_mfma_f32_16x16x32_f16 v[56:59],v[40:43],v[20:23],0\n" \
"v_mfma_f32_16x16x32_f16 v[60:63],v[44:47],v[20:23],0\ns_waitcnt lgkmcnt(0)\nv_mfma_f32_16x16x32_f16 v[64:67],v[40:43],v[24:27],0\nv_mfma_f32_16x16x32_f16 v[68:71],v[44:47],v[24:27],0\n" \
"s_nop 1\nv_cvt_pk_f16_f32 v80,v48,v52\nv_and_b32 v80,-2,v80\nv_cvt_pk_f16_f32 v81,v49,v53\n" \
"v_cvt_pk_f16_f32 v82,v50,v54\nv_cvt_pk_f16_f32 v83,v51,v55\nbuffer_store_dwordx4 v[80:83],v3,s[36:39],s73 offen sc1\nv_cvt_pk_f16_f32 v84,v56,v60\n" \
"v_cvt_pk_f16_f32 v85,v57,v61\nv_cvt_pk_f16_f32 v86,v58,v62\nv_cvt_pk_f16_f32 v87,v59,v63\nv_and_b32_e64 v84,-2,v84\n" \
"buffer_store_dwordx4 v[84:87],v3,s[36:39],s74 offen sc1\nv_cvt_pk_f16_f32 v88,v64,v68\nv_cvt_pk_f16_f32 v89,v65,v69\nv_cvt_pk_f16_f32 v90,v66,v70\n" \
"v_cvt_pk_f16_f32 v91,v67,v71\nv_and_b32 v88,-2,v88\nbuffer_store_dwordx4 v[88:91],v113,s[36:39],s75 offen sc1\ns_load_dwordx4 s[20:23],s[0:1],56\n" \
"s_mov_b32 s14,s87\ns_mov_b32 s12,s88\ns_mov_b32 s17,s74\ns_cmp_lt_u32 s3,2\n" \
"s_cselect_b32 s18,s75,s73\ns_mov_b32 s16,s73\ns_mov_b32 s26,4096\ns_mov_b32 s27,0x20000\n" \
"v_and_b32 v112,3,v0\nv_and_b32 v113,12,v0\nv_lshlrev_b32 v112,2,v112\ns_mov_b32 s13,s89\n" \
"v_max_u32 v112,v112,v113\nv_mov_b32 v114,1048576\nv_cmp_gt_u32 vcc,12,v112\nv_and_b32 v63,0xffff0000,v224\n" \
"v_and_b32 v65,0xffff0000,v225\ns_lshl2_add_u32 s95,s33,s3\ns_add_u32 s95,s95,190\ns_cmp_lt_u32 s33,13\n" \
"s_cselect_b32 s95,s95,256\ns_and_b32 s100,s95,31\ns_lshl_b32 s100,s100,3\ns_cmp_lt_u32 s95,256\n" \
"s_cselect_b32 s95,s95,0x40000\ns_lshr_b32 s95,s95,5\ns_add_u32 s95,s95,s100\ns_lshl_b32 s95,s95,6\n" \
"s_add_u32 s95,s95,s32\ns_mul_i32 s95,s95,1152\ns_lshl2_add_u32 s101,s33,s3\ns_add_u32 s101,s101,206\n" \
"s_cmp_lt_u32 s33,13\ns_cselect_b32 s101,s101,256\ns_add_u32 s96,s92,0\ns_add_u32 s97,s92,9437184\n" \
"s_add_u32 s100,s92,9510912\nv_and_b32 v67,0xffff0000,v226\nv_and_b32 v69,0xffff0000,v227\nv_and_b32 v71,0xffff0000,v228\n" \
"v_and_b32 v73,0xffff0000,v229\nv_and_b32 v75,0xffff0000,v230\nv_and_b32 v77,0xffff0000,v231\nv_and_b32 v79,0xffff0000,v232\n" \
"v_lshlrev_b32 v126,3,v1\nv_lshlrev_b32 v62,16,v224\nv_lshlrev_b32 v64,16,v225\nv_lshlrev_b32 v66,16,v226\n" \
"v_lshlrev_b32 v68,16,v227\nv_lshlrev_b32 v70,16,v228\nv_lshlrev_b32 v72,16,v229\nv_lshlrev_b32 v74,16,v230\n" \
"v_lshlrev_b32 v76,16,v231\nv_lshlrev_b32 v78,16,v232\nv_min_u32 v126,0x480,v126\nv_pk_add_f32 v[62:63],v[62:63],v[250:251] op_sel_hi:[1,0] neg_lo:[0,1] neg_hi:[0,1]\n" \
"v_pk_add_f32 v[64:65],v[64:65],v[250:251] op_sel_hi:[1,0] neg_lo:[0,1] neg_hi:[0,1]\nv_pk_add_f32 v[66:67],v[66:67],v[250:251] op_sel_hi:[1,0] neg_lo:[0,1] neg_hi:[0,1]\nv_pk_add_f32 v[68:69],v[68:69],v[250:251] op_sel_hi:[1,0] neg_lo:[0,1] neg_hi:[0,1]\nv_pk_add_f32 v[70:71],v[70:71],v[250:251] op_sel_hi:[1,0] neg_lo:[0,1] neg_hi:[0,1]\n" \
"v_pk_add_f32 v[72:73],v[72:73],v[250:251] op_sel_hi:[1,0] neg_lo:[0,1] neg_hi:[0,1]\nv_pk_add_f32 v[74:75],v[74:75],v[250:251] op_sel_hi:[1,0] neg_lo:[0,1] neg_hi:[0,1]\nv_pk_add_f32 v[76:77],v[76:77],v[250:251] op_sel_hi:[1,0] neg_lo:[0,1] neg_hi:[0,1]\nv_pk_add_f32 v[78:79],v[78:79],v[250:251] op_sel_hi:[1,0] neg_lo:[0,1] neg_hi:[0,1]\n" \
"s_mul_i32 s31,s32,1152\nv_lshlrev_b32 v127,3,v1\ns_waitcnt lgkmcnt(0)\ns_mov_b64 s[24:25],s[22:23]\n" \
"s_mov_b32 s22,9216\ns_mov_b32 s23,0x20000\nv_mov_b64 v[16:17],v[192:193]\nv_mov_b64 v[18:19],v[194:195]\n" \
"v_mov_b64 v[20:21],v[196:197]\nv_mov_b64 v[22:23],v[198:199]\nv_mov_b64 v[24:25],v[208:209]\nv_mov_b64 v[26:27],v[210:211]\n" \
"v_mov_b64 v[28:29],v[212:213]\nv_mov_b64 v[30:31],v[214:215]\nv_mov_b64 v[32:33],v[216:217]\nv_mov_b64 v[34:35],v[218:219]\n" \
"v_mov_b64 v[36:37],v[220:221]\nv_mov_b64 v[38:39],v[222:223]\nv_mov_b64 v[40:41],v[200:201]\nv_mov_b64 v[42:43],v[202:203]\n" \
"v_mov_b64 v[44:45],v[204:205]\nv_mov_b64 v[46:47],v[206:207]\nv_mov_b64 v[56:57],v[242:243]\nv_mov_b64 v[58:59],v[244:245]\n" \
"v_mov_b32 v60,v249\nv_mov_b32 v61,v250\ns_mov_b32 s55,0xbdd2d3e7\ns_mul_i32 s56,s3,9568\n" \
"s_lshl_b32 s58,s3,13\ns_and_b32 s84,s33,1\ns_add_u32 s87,s12,65536\ns_mov_b32 s90,7405568\n" \
"s_sub_u32 s98,s57,s12\ns_mov_b32 s99,s12\ns_mov_b32 m0,162176\ns_nop 0\n" \
"buffer_load_dword v126,s[20:23],s31 offen lds\ns_and_b32 s22,s33,7\ns_lshr_b32 s20,s33,3\ns_lshr_b32 s21,s32,1\n" \
"s_mul_i32 s52,s20,4505600\ns_add_u32 s52,s52,12123648\ns_mul_i32 s48,s20,589824\ns_lshl4_add_u32 s48,s12,s48\n" \
"s_lshl_b32 s86,s21,11\ns_add_u32 s48,s48,9764864\ns_lshl_b32 s67,s21,7\ns_lshl_b32 s85,s21,17\n" \
"s_sub_u32 s86,s14,s86\ns_add_u32 s49,s48,4096\ns_add_u32 s50,s48,8192\ns_add_u32 s51,s48,12288\n" \
"s_add_u32 s53,s52,3276800\ns_and_b32 s31,s101,31\ns_lshl_b32 s31,s31,3\ns_cmp_lt_u32 s101,256\n" \
"s_cselect_b32 s101,s101,0x40000\ns_lshr_b32 s101,s101,5\ns_add_u32 s101,s101,s31\ns_lshl_b32 s101,s101,6\n" \
"s_add_u32 s101,s101,s32\ns_mul_i32 s101,s101,1152\ns_add_u32 s31,s92,9584640\ns_add_u32 s83,s52,2048000\n" \
"s_add_u32 s67,s67,163200\ns_add_u32 s85,s85,65536\ns_add_u32 s86,s86,0x10000\ns_mov_b32 m0,162176\n" \
"s_nop 0\nbuffer_load_dword v127,s[24:27],0 offen lds\ns_and_b32 s24,s32,1\ns_lshl3_add_u32 s22,s24,s22\n" \
"s_bfe_u32 s24,s33,0x10002\ns_lshl1_add_u32 s24,s32,s24\ns_mul_i32 s19,s24,18\ns_and_b32 s24,s33,3\n" \
"s_lshl2_add_u32 s23,s24,s19\ns_lshl_b32 s24,s3,5\ns_add_u32 s54,s14,s24\ns_lshl2_add_u32 s24,s20,s21\n" \
"s_add_u32 s23,s23,s3\ns_lshl2_add_u32 s24,s24,s3\ns_add_u32 s23,s23,1\ns_lshl_b32 s24,s24,9\n" \
"s_lshl_b32 s23,s23,9\ns_add_u32 s24,s24,262144\nv_bfe_u32 v127,v0,3,3\nv_mul_u32_u24 v127,8192,v127\n" \
"v_lshl_add_u32 v127,v1,3,v127\ns_mov_b32 m0,162176\ns_sub_u32 s59,s24,s23\nbuffer_load_dword v127,s[8:11],s95 offen lds\n" \
"s_lshr_b32 s25,s3,1\ns_mul_i32 s24,s21,1152\ns_mul_i32 s25,s25,576\ns_add_u32 s24,s24,s25\n" \
"s_lshl4_add_u32 s24,s24,s22\ns_and_b32 s25,s3,1\ns_lshl_b32 s24,s24,8\ns_lshl_b32 s65,s25,17\n" \
"s_add_u32 s65,s65,s24\ns_lshl_b32 s25,s25,20\ns_add_u32 s24,s24,s25\ns_add_u32 s60,s24,655488\n" \
"s_add_u32 s61,s24,786432\ns_add_u32 s62,s24,786560\ns_add_u32 s63,s24,917504\ns_add_u32 s64,s24,917632\n" \
"s_mov_b32 m0,162176\ns_add_u32 s95,s92,73728\nbuffer_load_dword v127,s[8:11],s101 offen lds\ns_lshl2_add_u32 s24,s21,s3\n" \
"s_mul_i32 s24,s24,9216\ns_add_u32 s24,s24,s52\ns_add_u32 s68,s24,819712\ns_lshl_b32 s24,s22,14\n" \
"s_add_u32 s24,s24,s52\ns_lshl2_add_u32 s25,s21,s3\ns_add_u32 s24,s24,967168\ns_lshl_b32 s25,s25,8\n" \
"s_add_u32 s79,s25,s24\ns_add_u32 s25,s21,4\ns_and_b32 s25,s25,3\ns_add_u32 s24,s24,s12\n" \
"s_lshl_b32 s25,s25,12\ns_add_u32 s25,s25,s24\ns_sub_u32 s74,s25,819200\ns_add_u32 m0,s57,0\n" \
"s_add_u32 s101,s92,147456\nbuffer_load_dwordx4 v4,s[8:11],s96 offen lds\ns_add_u32 s25,s21,3\ns_and_b32 s25,s25,3\n" \
"s_lshl_b32 s25,s25,12\ns_add_u32 s25,s25,s24\ns_sub_u32 s71,s25,2048000\ns_sub_u32 s76,s25,1638400\n" \
"s_add_u32 s25,s21,2\ns_and_b32 s25,s25,3\ns_lshl_b32 s25,s25,12\nbuffer_load_dwordx4 v5,s[8:11],s96 offen offset:1024 lds\n" \
"s_add_u32 s25,s25,s24\ns_sub_u32 s72,s25,1638400\ns_sub_u32 s77,s25,1638400\ns_add_u32 s25,s21,1\n" \
"s_and_b32 s25,s25,3\ns_lshl_b32 s25,s25,12\ns_add_u32 s25,s25,s24\ns_sub_u32 s73,s25,1638400\n" \
"s_sub_u32 s78,s25,1228800\nbuffer_load_dwordx4 v6,s[8:11],s96 offen offset:2048 lds\ns_lshl2_add_u32 s26,s33,s3\ns_and_b32 s24,s26,15\n" \
"s_and_b32 s25,s26,8\ns_lshl2_add_u32 s24,s24,192\ns_mul_i32 s25,s25,28\ns_add_u32 s25,s25,s24\n" \
"s_sub_u32 s25,s25,32\ns_bitcmp1_b32 s26,4\ns_cselect_b32 s24,s25,s24\nbuffer_load_dwordx4 v7,s[8:11],s96 offen offset:3072 lds\n" \
"s_and_b32 s25,s26,31\ns_lshl2_add_u32 s25,s25,448\ns_bitcmp1_b32 s26,5\ns_cselect_b32 s24,s25,s24\n" \
"s_lshr_b32 s25,s26,6\ns_lshl1_add_u32 s25,s21,s25\ns_mul_i32 s25,s25,576\ns_add_u32 s24,s24,s25\n" \
"s_and_b32 s25,s32,1\ns_add_u32 m0,s57,4096\ns_add_u32 s96,s92,221184\nbuffer_load_dwordx4 v4,s[8:11],s97 offen lds\n" \
"s_lshl1_add_u32 s24,s24,s25\ns_lshl_b32 s24,s24,11\ns_and_b32 s25,s33,3\ns_mul_i32 s27,s3,4896\n" \
"s_add_u32 s24,s24,s25\ns_add_u32 s27,s27,9216\ns_add_u32 s89,s24,1\nv_mov_b32 v113,s27\n" \
"s_lshl2_add_u32 s24,s2,s3\nbuffer_load_dwordx4 v5,s[8:11],s97 offen offset:1024 lds\nv_cndmask_b32 v112,v114,v113,vcc\ns_add_u32 s65,s65,2097152\n" \
"s_lshl_b32 s24,s24,8\nv_add_u32 v48,v234,v112\nv_add_u32 v49,v235,v112\nv_add_u32 v50,v236,v112\n" \
"v_add_u32 v51,v237,v112\nv_add_u32 v52,v238,v112\nbuffer_load_dwordx4 v6,s[8:11],s97 offen offset:2048 lds\nv_add_u32 v53,v239,v112\n" \
"v_add_u32 v54,v240,v112\nv_add_u32 v55,v241,v112\ns_add_u32 s75,s59,196608\ns_add_u32 s66,s65,128\n" \
"s_add_u32 s69,s68,3072\ns_add_u32 s70,s68,6144\ns_add_u32 s80,s79,4096\ns_add_u32 s81,s79,8192\n" \
"buffer_load_dwordx4 v7,s[8:11],s97 offen offset:3072 lds\ns_add_u32 s82,s79,12288\ns_add_u32 s88,s74,819200\ns_add_u32 s91,s24,69206016\n" \
"s_add_u32 m0,s57,8192\ns_nop 0\nbuffer_load_dwordx4 v4,s[8:11],s95 offen lds\nbuffer_load_dwordx4 v5,s[8:11],s95 offen offset:1024 lds\n" \
"buffer_load_dwordx4 v6,s[8:11],s95 offen offset:2048 lds\nbuffer_load_dwordx4 v7,s[8:11],s95 offen offset:3072 lds\ns_add_u32 m0,s57,12288\ns_nop 0\n" \
"buffer_load_dwordx4 v4,s[8:11],s100 offen lds\nbuffer_load_dwordx4 v5,s[8:11],s100 offen offset:1024 lds\nbuffer_load_dwordx4 v6,s[8:11],s100 offen offset:2048 lds\nbuffer_load_dwordx4 v7,s[8:11],s100 offen offset:3072 lds\n" \
"s_add_u32 m0,s57,16384\ns_nop 0\nbuffer_load_dwordx4 v4,s[8:11],s101 offen lds\nbuffer_load_dwordx4 v5,s[8:11],s101 offen offset:1024 lds\n" \
"buffer_load_dwordx4 v6,s[8:11],s101 offen offset:2048 lds\nbuffer_load_dwordx4 v7,s[8:11],s101 offen offset:3072 lds\ns_add_u32 m0,s57,20480\ns_nop 0\n" \
"buffer_load_dwordx4 v4,s[8:11],s31 offen lds\nbuffer_load_dwordx4 v5,s[8:11],s31 offen offset:1024 lds\nbuffer_load_dwordx4 v6,s[8:11],s31 offen offset:2048 lds\nbuffer_load_dwordx4 v7,s[8:11],s31 offen offset:3072 lds\n" \
"s_add_u32 m0,s57,24576\ns_nop 0\nbuffer_load_dwordx4 v4,s[8:11],s96 offen lds\nbuffer_load_dwordx4 v5,s[8:11],s96 offen offset:1024 lds\n" \
"buffer_load_dwordx4 v6,s[8:11],s96 offen offset:2048 lds\nbuffer_load_dwordx4 v7,s[8:11],s96 offen offset:3072 lds\nbuffer_load_dwordx4 v[104:107],v1,s[36:39],s7 offen sc1\nbuffer_load_dwordx4 v[108:111],v1,s[36:39],s7 offen offset:1024 sc1\n" \
"s_waitcnt vmcnt(1)\nv_xor_b32 v112,s29,v105\nv_xor_b32 v113,s29,v107\ns_waitcnt vmcnt(0)\n" \
"v_xor_b32 v114,s29,v109\nv_xor_b32 v115,s29,v111\nv_or3_b32 v112,v112,v113,v114\nv_xor_b32 v113,s28,v104\n" \
"v_xor_b32 v114,s28,v106\nv_or_b32 v112,v112,v115\nv_xor_b32 v115,s28,v108\nv_lshl_or_b32 v112,v112,8,v113\n" \
"v_xor_b32 v113,s28,v110\nv_or3_b32 v112,v112,v114,v115\nv_or_b32 v112,v112,v113\nv_cmp_ne_u32 vcc,0,v112\n" \
"s_nop 0\ns_cbranch_vccnz .LsrN_%=\n.LbrN_%=:\nv_min_u32 v120,2288,v2\n" \
"s_mov_b32 s5,26624\nv_max3_u32 v112,v105,v107,v109\nv_max_u32_e64 v112,v112,v111\ns_mov_b32 s6,31232\n" \
"ds_read_b64_tr_b16 a[0:1],v8\nv_max_u32_dpp v112,v112,v112 quad_perm:[1,0,3,2] row_mask:0xf bank_mask:0xf bound_ctrl:1\nds_read_b64_tr_b16 a[4:5],v9\nds_read_b64_tr_b16 a[8:9],v10\n" \
"v_max_u32_dpp v112,v112,v112 quad_perm:[2,3,0,1] row_mask:0xf bank_mask:0xf bound_ctrl:1\nds_read_b64_tr_b16 a[12:13],v11\nds_read_b64_tr_b16 a[16:17],v8 offset:2048\nv_max_u32_dpp v112,v112,v112 row_half_mirror row_mask:0xf bank_mask:0xf bound_ctrl:1\n" \
"ds_read_b64_tr_b16 a[20:21],v9 offset:2048\nds_read_b64_tr_b16 a[24:25],v10 offset:2048\nv_max_u32_dpp v112,v112,v112 row_mirror row_mask:0xf bank_mask:0xf bound_ctrl:1\nds_read_b64_tr_b16 a[28:29],v11 offset:2048\n" \
"v_readlane_b32 s94,v112,0\nv_readlane_b32 s97,v112,16\nv_readlane_b32 s95,v112,32\nv_readlane_b32 s100,v112,48\n" \
"s_max_u32 s97,s97,s95\ns_max_u32 s94,s94,s100\ns_max_u32 s94,s94,s97\ns_lshr_b32 s94,s94,24\n" \
"s_max_u32 s94,s94,64\ns_min_u32 s94,s94,250\ns_add_u32 s101,s92,9658368\ns_add_u32 m0,s57,0\n" \
"ds_read_b64_tr_b16 a[2:3],v8 offset:4096\ns_waitcnt lgkmcnt(1)\nbuffer_load_dwordx4 v4,s[8:11],s101 offen lds\nbuffer_load_dwordx4 v5,s[8:11],s101 offen offset:1024 lds\n" \
"buffer_load_dwordx4 v6,s[8:11],s101 offen offset:2048 lds\nbuffer_load_dwordx4 v7,s[8:11],s101 offen offset:3072 lds\nds_read_b64_tr_b16 a[6:7],v9 offset:4096\nds_read_b64_tr_b16 a[10:11],v10 offset:4096\n" \
"ds_read_b64_tr_b16 a[14:15],v11 offset:4096\nds_read_b64_tr_b16 a[18:19],v8 offset:6144\nds_read_b64_tr_b16 a[22:23],v9 offset:6144\nds_read_b64_tr_b16 a[26:27],v10 offset:6144\n" \
"ds_read_b64_tr_b16 a[30:31],v11 offset:6144\ns_add_u32 s31,s92,294912\ns_add_u32 m0,s57,4096\nds_read_b64_tr_b16 a[32:33],v8 offset:8192\n" \
"s_waitcnt lgkmcnt(1)\nbuffer_load_dwordx4 v4,s[8:11],s31 offen lds\nbuffer_load_dwordx4 v5,s[8:11],s31 offen offset:1024 lds\nbuffer_load_dwordx4 v6,s[8:11],s31 offen offset:2048 lds\n" \
"buffer_load_dwordx4 v7,s[8:11],s31 offen offset:3072 lds\nds_read_b64_tr_b16 a[36:37],v9 offset:8192\nds_read_b64_tr_b16 a[40:41],v10 offset:8192\nds_read_b64_tr_b16 a[44:45],v11 offset:8192\n" \
"ds_read_b64_tr_b16 a[48:49],v8 offset:10240\nds_read_b64_tr_b16 a[52:53],v9 offset:10240\nds_read_b64_tr_b16 a[56:57],v10 offset:10240\nds_read_b64_tr_b16 a[60:61],v11 offset:10240\n" \
"s_add_u32 s96,s92,9732096\ns_add_u32 m0,s57,8192\nds_read_b64_tr_b16 a[34:35],v8 offset:12288\ns_waitcnt lgkmcnt(1)\n" \
"buffer_load_dwordx4 v4,s[8:11],s96 offen lds\nbuffer_load_dwordx4 v5,s[8:11],s96 offen offset:1024 lds\nbuffer_load_dwordx4 v6,s[8:11],s96 offen offset:2048 lds\nbuffer_load_dwordx4 v7,s[8:11],s96 offen offset:3072 lds\n" \
"ds_read_b64_tr_b16 a[38:39],v9 offset:12288\nds_read_b64_tr_b16 a[42:43],v10 offset:12288\nds_read_b64_tr_b16 a[46:47],v11 offset:12288\nds_read_b64_tr_b16 a[50:51],v8 offset:14336\n" \
"ds_read_b64_tr_b16 a[54:55],v9 offset:14336\nds_read_b64_tr_b16 a[58:59],v10 offset:14336\nds_read_b64_tr_b16 a[62:63],v11 offset:14336\ns_add_u32 s97,s92,368640\n" \
"s_add_u32 m0,s57,12288\nds_read_b64_tr_b16 a[64:65],v8 offset:16384\ns_waitcnt lgkmcnt(1)\nbuffer_load_dwordx4 v4,s[8:11],s97 offen lds\n" \
"buffer_load_dwordx4 v5,s[8:11],s97 offen offset:1024 lds\nbuffer_load_dwordx4 v6,s[8:11],s97 offen offset:2048 lds\nbuffer_load_dwordx4 v7,s[8:11],s97 offen offset:3072 lds\nbuffer_load_dwordx4 v[80:83],v120,s[36:39],s5 offen sc1\n" \
"buffer_load_dwordx4 v[84:87],v120,s[36:39],s5 offen offset:2304 sc1\nbuffer_load_dwordx4 v[88:91],v120,s[36:39],s6 offen sc1\nbuffer_load_dwordx4 v[92:95],v120,s[36:39],s6 offen offset:2304 sc1\nds_read_b64_tr_b16 a[68:69],v9 offset:16384\n" \
"ds_read_b64_tr_b16 a[72:73],v10 offset:16384\nds_read_b64_tr_b16 a[76:77],v11 offset:16384\nds_read_b64_tr_b16 a[80:81],v8 offset:18432\nds_read_b64_tr_b16 a[84:85],v9 offset:18432\n" \
"ds_read_b64_tr_b16 a[88:89],v10 offset:18432\nds_read_b64_tr_b16 a[92:93],v11 offset:18432\ns_add_u32 s95,s92,9805824\ns_add_u32 m0,s57,16384\n" \
"ds_read_b64_tr_b16 a[66:67],v8 offset:20480\ns_waitcnt lgkmcnt(1)\nbuffer_load_dwordx4 v4,s[8:11],s95 offen lds\nbuffer_load_dwordx4 v5,s[8:11],s95 offen offset:1024 lds\n" \
"buffer_load_dwordx4 v6,s[8:11],s95 offen offset:2048 lds\nbuffer_load_dwordx4 v7,s[8:11],s95 offen offset:3072 lds\nds_read_b64_tr_b16 a[70:71],v9 offset:20480\nds_read_b64_tr_b16 a[74:75],v10 offset:20480\n" \
"ds_read_b64_tr_b16 a[78:79],v11 offset:20480\nds_read_b64_tr_b16 a[82:83],v8 offset:22528\nds_read_b64_tr_b16 a[86:87],v9 offset:22528\nds_read_b64_tr_b16 a[90:91],v10 offset:22528\n" \
"ds_read_b64_tr_b16 a[94:95],v11 offset:22528\ns_add_u32 s100,s92,442368\ns_add_u32 m0,s57,20480\nds_read_b64_tr_b16 a[96:97],v8 offset:24576\n" \
"s_waitcnt lgkmcnt(1)\nbuffer_load_dwordx4 v4,s[8:11],s100 offen lds\nbuffer_load_dwordx4 v5,s[8:11],s100 offen offset:1024 lds\nbuffer_load_dwordx4 v6,s[8:11],s100 offen offset:2048 lds\n" \
"buffer_load_dwordx4 v7,s[8:11],s100 offen offset:3072 lds\nds_read_b64_tr_b16 a[100:101],v9 offset:24576\nds_read_b64_tr_b16 a[104:105],v10 offset:24576\nds_read_b64_tr_b16 a[108:109],v11 offset:24576\n" \
"ds_read_b64_tr_b16 a[112:113],v8 offset:26624\nds_read_b64_tr_b16 a[116:117],v9 offset:26624\nds_read_b64_tr_b16 a[120:121],v10 offset:26624\nds_read_b64_tr_b16 a[124:125],v11 offset:26624\n" \
"s_add_u32 s101,s92,9879552\ns_add_u32 m0,s57,24576\ns_waitcnt vmcnt(24)\nds_read_b64_tr_b16 a[98:99],v8\n" \
"s_waitcnt lgkmcnt(1)\nbuffer_load_dwordx4 v4,s[8:11],s101 offen lds\nbuffer_load_dwordx4 v5,s[8:11],s101 offen offset:1024 lds\nbuffer_load_dwordx4 v6,s[8:11],s101 offen offset:2048 lds\n" \
"buffer_load_dwordx4 v7,s[8:11],s101 offen offset:3072 lds\nds_read_b64_tr_b16 a[102:103],v9\nds_read_b64_tr_b16 a[106:107],v10\nds_read_b64_tr_b16 a[110:111],v11\n" \
"ds_read_b64_tr_b16 a[114:115],v8 offset:2048\nds_read_b64_tr_b16 a[118:119],v9 offset:2048\nds_read_b64_tr_b16 a[122:123],v10 offset:2048\nds_read_b64_tr_b16 a[126:127],v11 offset:2048\n" \
"s_add_u32 s31,s92,516096\ns_add_u32 m0,s57,0\ns_waitcnt vmcnt(24)\nds_read_b64_tr_b16 a[128:129],v8 offset:4096\n" \
"s_waitcnt lgkmcnt(1)\nbuffer_load_dwordx4 v4,s[8:11],s31 offen lds\nbuffer_load_dwordx4 v5,s[8:11],s31 offen offset:1024 lds\nbuffer_load_dwordx4 v6,s[8:11],s31 offen offset:2048 lds\n" \
"buffer_load_dwordx4 v7,s[8:11],s31 offen offset:3072 lds\nds_read_b64_tr_b16 a[132:133],v9 offset:4096\nds_read_b64_tr_b16 a[136:137],v10 offset:4096\nds_read_b64_tr_b16 a[140:141],v11 offset:4096\n" \
"ds_read_b64_tr_b16 a[144:145],v8 offset:6144\nds_read_b64_tr_b16 a[148:149],v9 offset:6144\nds_read_b64_tr_b16 a[152:153],v10 offset:6144\nds_read_b64_tr_b16 a[156:157],v11 offset:6144\n" \
"s_add_u32 s96,s92,9953280\ns_add_u32 m0,s57,4096\ns_waitcnt vmcnt(24)\nds_read_b64_tr_b16 a[130:131],v8 offset:8192\n" \
"s_waitcnt lgkmcnt(1)\nbuffer_load_dwordx4 v4,s[8:11],s96 offen lds\nbuffer_load_dwordx4 v5,s[8:11],s96 offen offset:1024 lds\nbuffer_load_dwordx4 v6,s[8:11],s96 offen offset:2048 lds\n" \
"buffer_load_dwordx4 v7,s[8:11],s96 offen offset:3072 lds\nds_read_b64_tr_b16 a[134:135],v9 offset:8192\nds_read_b64_tr_b16 a[138:139],v10 offset:8192\nds_read_b64_tr_b16 a[142:143],v11 offset:8192\n" \
"ds_read_b64_tr_b16 a[146:147],v8 offset:10240\nds_read_b64_tr_b16 a[150:151],v9 offset:10240\nds_read_b64_tr_b16 a[154:155],v10 offset:10240\nds_read_b64_tr_b16 a[158:159],v11 offset:10240\n" \
"s_bfe_u32 s26,s33,0x20001\ns_lshl2_add_u32 s15,s32,s26\ns_lshl2_add_u32 s15,s15,s3\ns_lshl_b32 s15,s15,14\n" \
"s_add_u32 s15,s15,5308416\ns_add_u32 s97,s15,0\ns_add_u32 m0,s57,8192\ns_waitcnt vmcnt(24)\n" \
"ds_read_b64_tr_b16 a[160:161],v8 offset:12288\ns_waitcnt lgkmcnt(1)\nbuffer_load_dwordx4 v1,s[36:39],s97 offen sc1 lds\nbuffer_load_dwordx4 v1,s[36:39],s97 offen offset:1024 sc1 lds\n" \
"buffer_load_dwordx4 v1,s[36:39],s97 offen offset:2048 sc1 lds\nbuffer_load_dwordx4 v1,s[36:39],s97 offen offset:3072 sc1 lds\nds_read_b64_tr_b16 a[164:165],v9 offset:12288\nds_read_b64_tr_b16 a[168:169],v10 offset:12288\n" \
"ds_read_b64_tr_b16 a[172:173],v11 offset:12288\nds_read_b64_tr_b16 a[176:177],v8 offset:14336\nds_read_b64_tr_b16 a[180:181],v9 offset:14336\nds_read_b64_tr_b16 a[184:185],v10 offset:14336\n" \
"ds_read_b64_tr_b16 a[188:189],v11 offset:14336\ns_add_u32 s95,s15,4096\ns_add_u32 m0,s57,12288\ns_waitcnt vmcnt(20)\n" \
"ds_read_b64_tr_b16 a[162:163],v8 offset:16384\ns_waitcnt lgkmcnt(1)\nbuffer_load_dwordx4 v1,s[36:39],s95 offen sc1 lds\nbuffer_load_dwordx4 v1,s[36:39],s95 offen offset:1024 sc1 lds\n" \
"buffer_load_dwordx4 v1,s[36:39],s95 offen offset:2048 sc1 lds\nbuffer_load_dwordx4 v1,s[36:39],s95 offen offset:3072 sc1 lds\nds_read_b64_tr_b16 a[166:167],v9 offset:16384\nds_read_b64_tr_b16 a[170:171],v10 offset:16384\n" \
"ds_read_b64_tr_b16 a[174:175],v11 offset:16384\nds_read_b64_tr_b16 a[178:179],v8 offset:18432\nds_read_b64_tr_b16 a[182:183],v9 offset:18432\nds_read_b64_tr_b16 a[186:187],v10 offset:18432\n" \
"ds_read_b64_tr_b16 a[190:191],v11 offset:18432\ns_add_u32 s100,s15,8192\ns_add_u32 m0,s57,16384\ns_waitcnt vmcnt(20)\n" \
"ds_read_b64_tr_b16 a[192:193],v8 offset:20480\ns_waitcnt lgkmcnt(1)\nbuffer_load_dwordx4 v1,s[36:39],s100 offen sc1 lds\nbuffer_load_dwordx4 v1,s[36:39],s100 offen offset:1024 sc1 lds\n" \
"buffer_load_dwordx4 v1,s[36:39],s100 offen offset:2048 sc1 lds\nbuffer_load_dwordx4 v1,s[36:39],s100 offen offset:3072 sc1 lds\nds_read_b64_tr_b16 a[196:197],v9 offset:20480\nds_read_b64_tr_b16 a[200:201],v10 offset:20480\n" \
"ds_read_b64_tr_b16 a[204:205],v11 offset:20480\nds_read_b64_tr_b16 a[208:209],v8 offset:22528\nds_read_b64_tr_b16 a[212:213],v9 offset:22528\nds_read_b64_tr_b16 a[216:217],v10 offset:22528\n" \
"ds_read_b64_tr_b16 a[220:221],v11 offset:22528\ns_add_u32 s101,s15,12288\ns_add_u32 m0,s57,20480\ns_waitcnt lgkmcnt(0)\n" \
"buffer_load_dwordx4 v1,s[36:39],s101 offen sc1 lds\nbuffer_load_dwordx4 v1,s[36:39],s101 offen offset:1024 sc1 lds\nbuffer_load_dwordx4 v1,s[36:39],s101 offen offset:2048 sc1 lds\nbuffer_load_dwordx4 v1,s[36:39],s101 offen offset:3072 sc1 lds\n" \
"v_cmp_ne_u64 vcc,s[28:29],v[80:81]\nv_cmp_ne_u64 s[24:25],s[28:29],v[84:85]\nv_cmp_ne_u64 s[26:27],s[28:29],v[88:89]\ns_or_b64 vcc,vcc,s[24:25]\n" \
"v_cmp_ne_u64 s[24:25],s[28:29],v[92:93]\ns_or_b64 vcc,vcc,s[26:27]\ns_or_b64 vcc,vcc,s[24:25]\ns_cbranch_vccnz .LsnN_%=\n" \
".LbnN_%=:\nv_bfe_u32 v112,v0,1,5\nv_mul_u32_u24 v112,72,v112\nv_lshrrev_b32 v113,6,v0\n" \
"v_mul_u32_u24 v114,144,v0\nv_lshl_add_u32 v112,v113,5,v112\nv_subrev_u32 v114,18368,v114\nv_mov_b32 v115,1048576\n" \
"v_cmp_gt_u32 vcc,144,v0\nv_and_b32 v113,1,v0\nv_lshl_add_u32 v112,v113,4,v112\nv_cndmask_b32 v114,v115,v114,vcc\n" \
"v_and_b32 v97,0xffff0000,v82\nv_lshlrev_b32 v96,16,v82\ns_add_u32 s24,s93,s94\nv_cmp_gt_u32 vcc,128,v0\n" \
"v_add_u32 v113,8,v112\nv_mul_f32 v96,0x3ee38e39,v96\nv_mul_f32 v97,0x3ee38e39,v97\ns_sub_u32 s24,s24,253\n" \
"v_cndmask_b32 v112,v114,v112,vcc\nv_and_b32 v99,0xffff0000,v83\nv_ldexp_f32 v96,v96,s24\nv_ldexp_f32 v97,v97,s24\n" \
"v_lshlrev_b32_e64 v98,16,v83\nds_write_b64 v112,v[96:97] offset:28800\nv_add_u32 v115,72,v114\nv_mul_f32 v98,0x3ee38e39,v98\n" \
"v_mul_f32 v99,0x3ee38e39,v99\nv_cndmask_b32_e64 v113,v115,v113,vcc\nv_ldexp_f32 v98,v98,s24\nv_ldexp_f32 v99,v99,s24\n" \
"ds_write_b64 v113,v[98:99] offset:28800\nv_and_b32 v101,0xffff0000,v86\nv_lshlrev_b32 v100,16,v86\nds_write_b64 v112,v[100:101] offset:31104\n" \
"v_and_b32 v103,0xffff0000,v87\nv_lshlrev_b32 v102,16,v87\ns_lshr_b32 s25,s33,3\nds_write_b64 v113,v[102:103] offset:31104\n" \
"s_mul_i32 s25,s25,576\nv_and_b32 v105,0xffff0000,v90\nv_lshlrev_b32 v104,16,v90\ns_sub_u32 s25,163200,s25\n" \
"ds_write_b64 v112,v[104:105] offset:33408\nv_add_u32 v114,s25,v2\nv_subrev_u32 v115,163200,v114\nv_and_b32 v107,0xffff0000,v91\n" \
"v_lshlrev_b32 v106,16,v91\nv_mov_b32 v116,1048576\nv_cmp_gt_u32 vcc,576,v115\nds_write_b64 v113,v[106:107] offset:33408\n" \
"v_and_b32 v109,0xffff0000,v94\nv_cndmask_b32 v114,v116,v114,vcc\nv_and_b32 v111,0xffff0000,v95\nv_lshlrev_b32 v108,16,v94\n" \
"v_lshlrev_b32 v110,16,v95\nds_write_b128 v114,v[108:111]\nv_and_b32 v124,48,v0\nv_cmp_gt_u32 vcc,64,v0\n" \
"v_bfe_u32 v127,v0,5,1\nv_and_b32 v13,31,v0\nv_cndmask_b32_e64 v124,v124,v3,vcc\nv_mul_u32_u24 v127,2448,v127\n" \
"v_mul_u32_u24 v13,72,v13\ns_mul_i32 s27,s3,4896\nv_add3_u32 v13,v13,v127,s27\nv_mov_b32 v127,0\n" \
"s_waitcnt vmcnt(24)\nds_read_b64_tr_b16 a[194:195],v8 offset:24576\nds_read_b64_tr_b16 a[198:199],v9 offset:24576\nds_read_b64_tr_b16 a[202:203],v10 offset:24576\n" \
"ds_read_b64_tr_b16 a[206:207],v11 offset:24576\nds_read_b64_tr_b16 a[210:211],v8 offset:26624\nds_read_b64_tr_b16 a[214:215],v9 offset:26624\nds_read_b64_tr_b16 a[218:219],v10 offset:26624\n" \
"ds_read_b64_tr_b16 a[222:223],v11 offset:26624\ns_waitcnt vmcnt(20)\nds_read_b64_tr_b16 a[224:225],v8\nds_read_b64_tr_b16 a[228:229],v9\n" \
"ds_read_b64_tr_b16 a[232:233],v10\nds_read_b64_tr_b16 a[236:237],v11\nds_read_b64_tr_b16 a[240:241],v8 offset:2048\nds_read_b64_tr_b16 a[244:245],v9 offset:2048\n" \
"s_waitcnt lgkmcnt(14)\ns_barrier\nbuffer_load_dwordx4 v[112:115],v3,s[36:39],s16 offen sc1\nbuffer_load_dwordx4 v[116:119],v3,s[36:39],s17 offen sc1\n" \
"buffer_load_dwordx4 v[120:123],v124,s[36:39],s18 offen sc1\nds_read_b64_tr_b16 a[248:249],v10 offset:2048\nds_read_b64_tr_b16 a[252:253],v11 offset:2048\ns_waitcnt vmcnt(19)\n" \
"ds_read_b64_tr_b16 a[226:227],v8 offset:4096\nds_read_b64_tr_b16 a[230:231],v9 offset:4096\nds_read_b64_tr_b16 a[234:235],v10 offset:4096\nds_read_b64_tr_b16 a[238:239],v11 offset:4096\n" \
"ds_read_b64_tr_b16 a[242:243],v8 offset:6144\nds_read_b64_tr_b16 a[246:247],v9 offset:6144\nds_read_b64_tr_b16 a[250:251],v10 offset:6144\nds_read_b64_tr_b16 a[254:255],v11 offset:6144\n" \
"s_waitcnt vmcnt(15)\nds_read_b64_tr_b16 v[192:193],v15 offset:8192\nds_read_b64_tr_b16 v[196:197],v15 offset:8704\nds_read_b64_tr_b16 v[194:195],v15 offset:9216\n" \
"ds_read_b64_tr_b16 v[198:199],v15 offset:9728\nds_read_b64_tr_b16 v[200:201],v15 offset:10240\nds_read_b64_tr_b16 v[204:205],v15 offset:10752\nds_read_b64_tr_b16 v[202:203],v15 offset:11264\n" \
"ds_read_b64_tr_b16 v[206:207],v15 offset:11776\ns_waitcnt vmcnt(11)\nds_read_b64_tr_b16 v[208:209],v15 offset:12288\nds_read_b64_tr_b16 v[212:213],v15 offset:12800\n" \
"ds_read_b64_tr_b16 v[210:211],v15 offset:13312\nds_read_b64_tr_b16 v[214:215],v15 offset:13824\nds_read_b64_tr_b16 v[216:217],v15 offset:14336\nds_read_b64_tr_b16 v[220:221],v15 offset:14848\n" \
"ds_read_b64_tr_b16 v[218:219],v15 offset:15360\nds_read_b64_tr_b16 v[222:223],v15 offset:15872\ns_waitcnt vmcnt(7)\nds_read_b64_tr_b16 v[224:225],v15 offset:16384\n" \
"ds_read_b64_tr_b16 v[228:229],v15 offset:16896\nds_read_b64_tr_b16 v[226:227],v15 offset:17408\nds_read_b64_tr_b16 v[230:231],v15 offset:17920\nds_read_b64_tr_b16 v[232:233],v15 offset:18432\n" \
"ds_read_b64_tr_b16 v[236:237],v15 offset:18944\nds_read_b64_tr_b16 v[234:235],v15 offset:19456\nds_read_b64_tr_b16 v[238:239],v15 offset:19968\ns_waitcnt vmcnt(3)\n" \
"ds_read_b64_tr_b16 v[240:241],v15 offset:20480\nds_read_b64_tr_b16 v[244:245],v15 offset:20992\nds_read_b64_tr_b16 v[242:243],v15 offset:21504\nds_read_b64_tr_b16 v[246:247],v15 offset:22016\n" \
"ds_read_b64_tr_b16 v[248:249],v15 offset:22528\nds_read_b64_tr_b16 v[252:253],v15 offset:23040\nds_read_b64_tr_b16 v[250:251],v15 offset:23552\nds_read_b64_tr_b16 v[254:255],v15 offset:24064\n" \
"s_waitcnt vmcnt(1)\nv_and_b32 v125,v112,v116\ns_waitcnt vmcnt(0)\nv_bitop3_b32 v125,v125,v120,1 bitop3:0x80\n" \
"v_cmp_eq_u32 vcc,0,v125\ns_nop 0\ns_cbranch_vccnz .LsuN_%=\n.LbuN_%=:\n" \
"v_mfma_f32_16x16x32_f16 v[80:83],v[32:35],v[112:115],0\nv_and_b32 v3,31,v0\nv_mul_u32_u24 v3,72,v3\nv_mfma_f32_16x16x32_f16 v[84:87],v[36:39],v[112:115],0\n" \
"v_add_u32 v13,9216,v13\nv_mov_b32 v104,-1\nv_mfma_f32_16x16x32_f16 v[88:91],v[32:35],v[116:119],0\nv_mov_b32 v105,-1\n" \
"v_mov_b32 v106,-1\nv_mfma_f32_16x16x32_f16 v[92:95],v[36:39],v[116:119],0\nv_mov_b32 v107,-1\ns_add_u32 s27,s2,0\n" \
"v_mfma_f32_16x16x32_f16 v[96:99],v[32:35],v[120:123],0\ns_lshr_b32 s31,s27,4\ns_mul_i32 s96,s31,37\ns_lshr_b32 s96,s96,8\n" \
"s_lshl2_add_u32 s31,s96,s31\ns_mul_i32 s31,s31,409600\ns_and_b32 s96,s27,15\ns_lshl_b32 s96,s96,14\n" \
"s_add_u32 s31,s31,s96\ns_add_u32 s31,s31,13090816\ns_cmp_lt_u32 s27,158\ns_cselect_b32 s31,s31,2147483648\n" \
"buffer_store_dwordx4 v[104:107],v2,s[36:39],s31 offen sc1\nv_mfma_f32_16x16x32_f16 v[100:103],v[36:39],v[120:123],0\nv_cvt_pk_f16_f32 v112,v84,v80\nv_cvt_pk_f16_f32 v113,v85,v81\n" \
"v_cvt_pk_f16_f32 v114,v86,v82\nv_cvt_pk_f16_f32 v115,v87,v83\nds_write_b128 v57,v[112:115]\nv_cvt_pk_f16_f32 v116,v92,v88\n" \
"v_cvt_pk_f16_f32 v117,v93,v89\ns_add_u32 s31,s31,4096\nbuffer_store_dwordx4 v[104:107],v2,s[36:39],s31 offen sc1\nv_cvt_pk_f16_f32 v118,v94,v90\n" \
"v_cvt_pk_f16_f32 v119,v95,v91\nds_write_b128 v58,v[116:119]\nv_cvt_pk_f16_f32 v120,v100,v96\nv_cvt_pk_f16_f32 v121,v101,v97\n" \
"v_cvt_pk_f16_f32 v122,v102,v98\nv_cvt_pk_f16_f32 v123,v103,v99\nds_write_b128 v60,v[120:123]\ns_add_u32 s31,s31,4096\n" \
"buffer_store_dwordx4 v[104:107],v2,s[36:39],s31 offen sc1\nv_add_u32 v60,31104,v3\nv_add_u32 v3,28800,v3\nds_read2_b64 v[32:35],v3 offset0:0 offset1:1\n" \
"ds_read2_b64 v[36:39],v3 offset0:2 offset1:3\ns_add_u32 s31,s31,4096\ns_add_u32 s27,s2,111\ns_lshr_b32 s97,s27,4\n" \
"s_mul_i32 s95,s97,37\ns_lshr_b32 s95,s95,8\ns_lshl2_add_u32 s97,s95,s97\ns_mul_i32 s97,s97,409600\n" \
"s_and_b32 s95,s27,15\ns_lshl_b32 s95,s95,14\nbuffer_store_dwordx4 v[104:107],v2,s[36:39],s31 offen sc1\ns_add_u32 s97,s97,s95\n" \
"s_add_u32 s97,s97,13090816\ns_cmp_lt_u32 s27,158\ns_cselect_b32 s97,s97,2147483648\ns_waitcnt lgkmcnt(2)\n" \
"s_barrier\nds_read_b64 v[112:113],v56\nds_read_b64 v[116:117],v56 offset:512\nds_read_b64 v[120:121],v56 offset:256\n" \
"ds_read_b64 v[124:125],v56 offset:768\nds_read_b64 v[114:115],v56 offset:1024\nds_read_b64 v[122:123],v56 offset:1280\nds_read_b64 v[56:57],v3 offset:64\n" \
"buffer_store_dwordx4 v[104:107],v2,s[36:39],s97 offen sc1\ns_add_u32 s97,s97,4096\nv_bfe_u32 v110,v0,1,2\nv_bfe_u32 v111,v0,4,1\n" \
"v_xor_b32 v110,v110,v111\nv_and_b32 v111,1,v0\nv_lshl_add_u32 v110,v110,1,v111\nv_bfe_u32 v111,v0,3,3\n" \
"v_lshlrev_b32 v111,12,v111\nv_lshl_add_u32 v4,v110,4,v111\nv_xor_b32 v5,64,v4\nv_add_u32 v5,31744,v5\n" \
"v_add_u32 v6,63488,v4\nbuffer_store_dwordx4 v[104:107],v2,s[36:39],s97 offen sc1\ns_waitcnt lgkmcnt(5)\nv_swap_b32 v113,v116\n" \
"s_add_u32 s97,s97,4096\ns_waitcnt lgkmcnt(3)\nv_swap_b32 v121,v124\nv_add_u32 v7,63488,v5\n" \
"s_waitcnt lgkmcnt(2)\nv_mfma_f32_16x16x32_f16 v[80:83],v[112:115],v[40:43],0\nv_mov_b32 v118,v115\ns_waitcnt lgkmcnt(1)\n" \
"v_mov_b32 v126,v123\nv_mfma_f32_16x16x32_f16 v[84:87],v[112:115],v[44:47],0\ns_lshr_b32 s26,s32,1\ns_mul_i32 s92,s26,1152\n" \
"v_mfma_f32_16x16x32_f16 v[88:91],v[120:123],v[40:43],0\nbuffer_store_dwordx4 v[104:107],v2,s[36:39],s97 offen sc1\nv_mfma_f32_16x16x32_f16 v[92:95],v[120:123],v[44:47],0\ns_add_u32 s97,s97,4096\n" \
"s_lshr_b32 s26,s3,1\nv_mfma_f32_16x16x32_f16 v[96:99],v[116:119],v[40:43],0\ns_lshl_b32 s26,s26,6\ns_add_u32 s92,s92,s26\n" \
"v_mfma_f32_16x16x32_f16 v[100:103],v[116:119],v[44:47],0\nv_pk_add_f32 v[88:89],v[80:81],v[88:89] neg_lo:[0,1] neg_hi:[0,1]\nv_pk_add_f32 v[90:91],v[82:83],v[90:91] neg_lo:[0,1] neg_hi:[0,1]\nv_pk_add_f32 v[92:93],v[84:85],v[92:93] neg_lo:[0,1] neg_hi:[0,1]\n" \
"buffer_store_dwordx4 v[104:107],v2,s[36:39],s97 offen sc1\nv_pk_add_f32 v[94:95],v[86:87],v[94:95] neg_lo:[0,1] neg_hi:[0,1]\nv_pk_fma_f32 v[80:81],v[80:81],2.0,v[88:89] op_sel_hi:[1,0,1] neg_lo:[0,0,1] neg_hi:[0,0,1]\nv_pk_fma_f32 v[82:83],v[82:83],2.0,v[90:91] op_sel_hi:[1,0,1] neg_lo:[0,0,1] neg_hi:[0,0,1]\n" \
"v_pk_fma_f32 v[84:85],v[84:85],2.0,v[92:93] op_sel_hi:[1,0,1] neg_lo:[0,0,1] neg_hi:[0,0,1]\nv_pk_fma_f32 v[86:87],v[86:87],2.0,v[94:95] op_sel_hi:[1,0,1] neg_lo:[0,0,1] neg_hi:[0,0,1]\nv_cvt_pk_f16_f32 v112,v80,v84\nv_cvt_pk_f16_f32 v113,v81,v85\n" \
"v_cvt_pk_f16_f32 v114,v82,v86\nv_cvt_pk_f16_f32 v115,v83,v87\nv_mfma_f32_16x16x32_f16 v[104:107],v[124:127],v[40:43],0\nv_cvt_pk_f16_f32 v116,v88,v92\n" \
"s_lshr_b32 s100,s33,3\ns_add_u32 s100,s100,0\ns_sub_u32 s101,s100,4\ns_min_u32 s100,s100,s101\n" \
"s_lshl_b32 s101,s100,12\ns_add_u32 s101,s101,s57\ns_add_u32 m0,s101,0\ns_and_b32 s101,s100,1\n" \
"s_lshr_b32 s100,s100,1\ns_lshl_b32 s100,s100,10\ns_add_u32 s100,s100,s101\ns_lshl_b32 s100,s100,7\n" \
"s_lshr_b32 s31,s33,3\ns_add_u32 s31,s31,1\ns_sub_u32 s96,s31,4\ns_min_u32 s31,s31,s96\n" \
"s_lshl_b32 s96,s31,12\ns_add_u32 s96,s96,s57\ns_lshr_b32 s97,s33,3\ns_add_u32 s97,s97,2\n" \
"s_sub_u32 s95,s97,4\ns_min_u32 s97,s97,s95\ns_lshl_b32 s95,s97,12\ns_add_u32 s95,s95,s57\n" \
"v_mfma_f32_16x16x32_f16 v[80:83],v[16:19],v[112:115],0\nv_cvt_pk_f16_f32 v117,v89,v93\nv_cvt_pk_f16_f32 v118,v90,v94\nv_mfma_f32_16x16x32_f16 v[84:87],v[20:23],v[112:115],0\n" \
"v_cvt_pk_f16_f32 v119,v91,v95\nv_pk_add_f32 v[104:105],v[96:97],v[104:105] neg_lo:[0,1] neg_hi:[0,1]\nv_mfma_f32_16x16x32_f16 v[108:111],v[124:127],v[44:47],0\nv_pk_add_f32 v[106:107],v[98:99],v[106:107] neg_lo:[0,1] neg_hi:[0,1]\n" \
"v_pk_fma_f32 v[96:97],v[96:97],2.0,v[104:105] op_sel_hi:[1,0,1] neg_lo:[0,0,1] neg_hi:[0,0,1]\nv_mfma_f32_16x16x32_f16 v[88:91],v[24:27],v[116:119],0\nv_pk_fma_f32 v[98:99],v[98:99],2.0,v[106:107] op_sel_hi:[1,0,1] neg_lo:[0,0,1] neg_hi:[0,0,1]\nds_read2_b64 v[40:43],v3 offset0:4 offset1:5\n" \
"v_mfma_f32_16x16x32_f16 v[92:95],v[28:31],v[116:119],0\nds_write2_b32 v48,v84,v80 offset0:0 offset1:1\nds_write2_b32 v49,v85,v81 offset0:0 offset1:1\nds_write2_b32 v50,v86,v82 offset0:0 offset1:1\n" \
"v_pk_add_f32 v[108:109],v[100:101],v[108:109] neg_lo:[0,1] neg_hi:[0,1]\nv_pk_add_f32 v[110:111],v[102:103],v[110:111] neg_lo:[0,1] neg_hi:[0,1]\nv_pk_fma_f32 v[100:101],v[100:101],2.0,v[108:109] op_sel_hi:[1,0,1] neg_lo:[0,0,1] neg_hi:[0,0,1]\nv_pk_fma_f32 v[102:103],v[102:103],2.0,v[110:111] op_sel_hi:[1,0,1] neg_lo:[0,0,1] neg_hi:[0,0,1]\n" \
"ds_write2_b32 v51,v87,v83 offset0:0 offset1:1\nv_cvt_pk_f16_f32 v120,v96,v100\nv_cvt_pk_f16_f32 v121,v97,v101\nv_cvt_pk_f16_f32 v122,v98,v102\n" \
"v_cvt_pk_f16_f32 v123,v99,v103\nds_write2_b32 v52,v92,v88 offset0:0 offset1:1\nds_write2_b32 v53,v93,v89 offset0:0 offset1:1\nv_mfma_f32_16x16x32_f16 v[96:99],v[16:19],v[120:123],0\n" \
"ds_write2_b32 v54,v94,v90 offset0:0 offset1:1\nv_mfma_f32_16x16x32_f16 v[100:103],v[20:23],v[120:123],0\nds_write2_b32 v55,v95,v91 offset0:0 offset1:1\nv_cvt_pk_f16_f32 v124,v104,v108\n" \
"v_cvt_pk_f16_f32 v125,v105,v109\nv_cvt_pk_f16_f32 v126,v106,v110\nv_cvt_pk_f16_f32 v127,v107,v111\nv_add_u32 v48,2448,v48\n" \
"v_add_u32 v49,2448,v49\nv_mfma_f32_16x16x32_f16 v[104:107],v[24:27],v[124:127],0\nds_write2_b32 v48,v100,v96 offset0:0 offset1:1\nv_mfma_f32_16x16x32_f16 v[108:111],v[28:31],v[124:127],0\n" \
"ds_write2_b32 v49,v101,v97 offset0:0 offset1:1\nv_add_u32 v50,2448,v50\nds_write2_b32 v50,v102,v98 offset0:0 offset1:1\nv_add_u32 v51,2448,v51\n" \
"ds_write2_b32 v51,v103,v99 offset0:0 offset1:1\nv_add_u32 v52,2448,v52\nv_add_u32 v53,2448,v53\nv_add_u32 v54,2448,v54\n" \
"ds_write2_b32 v52,v108,v104 offset0:0 offset1:1\nds_write2_b32 v53,v109,v105 offset0:0 offset1:1\nds_write2_b32 v54,v110,v106 offset0:0 offset1:1\nv_add_u32 v55,2448,v55\n" \
"ds_write2_b32 v55,v111,v107 offset0:0 offset1:1\nds_read2_b64 v[80:83],v13 offset0:0 offset1:1\nds_read2_b64 v[84:87],v13 offset0:2 offset1:3\ns_lshl_b32 s26,s3,8\n" \
"s_add_u32 s92,s92,s26\ns_lshl_b32 s92,s92,4\ns_and_b32 s26,s32,1\ns_lshl3_add_u32 s92,s26,s92\n" \
"s_and_b32 s26,s33,7\ns_add_u32 s92,s92,s26\nds_read2_b64 v[88:91],v13 offset0:4 offset1:5\ns_lshl_b32 s92,s92,8\n" \
"s_add_u32 s100,s100,s92\nbuffer_load_dwordx4 v4,s[40:43],s100 offen lds\nds_read2_b64 v[44:47],v3 offset0:6 offset1:7\nv_and_b32 v48,31,v0\n" \
"ds_read2_b64 v[92:95],v13 offset0:6 offset1:7\nv_mad_u32_u24 v49,v48,56,0\nv_sub_u32 v48,v13,v49\nv_and_b32 v49,31,v0\n" \
"v_mul_u32_u24 v49,68,v49\nv_sub_u32 v49,v13,v49\nbuffer_load_dwordx4 v5,s[40:43],s100 offen offset:1024 lds\nds_read_b64 v[96:97],v13 offset:64\n" \
"s_waitcnt lgkmcnt(5)\nv_pk_fma_f32 v[62:63],v[80:81],v[32:33],v[62:63]\nv_pk_fma_f32 v[64:65],v[82:83],v[34:35],v[64:65]\nv_pk_mul_f32 v[102:103],v[62:63],v[62:63]\n" \
"s_waitcnt lgkmcnt(4)\nv_pk_fma_f32 v[66:67],v[84:85],v[36:37],v[66:67]\nv_pk_add_f32 v[100:101],v[62:63],v[64:65]\nv_pk_fma_f32 v[102:103],v[64:65],v[64:65],v[102:103]\n" \
"v_pk_fma_f32 v[68:69],v[86:87],v[38:39],v[68:69]\nbuffer_load_dwordx4 v6,s[40:43],s100 offen offset:2048 lds\nv_pk_add_f32 v[100:101],v[100:101],v[66:67]\nv_pk_fma_f32 v[102:103],v[66:67],v[66:67],v[102:103]\n" \
"ds_read2_b64 v[112:115],v60 offset0:0 offset1:1\nds_read2_b64 v[116:119],v60 offset0:2 offset1:3\ns_waitcnt lgkmcnt(5)\nv_pk_fma_f32 v[70:71],v[88:89],v[40:41],v[70:71]\n" \
"v_pk_add_f32 v[100:101],v[100:101],v[68:69]\nds_read2_b64 v[120:123],v60 offset0:4 offset1:5\nv_pk_fma_f32 v[102:103],v[68:69],v[68:69],v[102:103]\nbuffer_load_dwordx4 v7,s[40:43],s100 offen offset:3072 lds\n" \
"ds_read2_b64 v[124:127],v60 offset0:6 offset1:7\nv_pk_fma_f32 v[72:73],v[90:91],v[42:43],v[72:73]\nv_pk_add_f32 v[100:101],v[100:101],v[70:71]\nds_read_b64 v[98:99],v60 offset:64\n" \
"v_add_u32 v60,2304,v60\nv_pk_fma_f32 v[102:103],v[70:71],v[70:71],v[102:103]\nds_read2_b64 v[16:19],v60 offset0:0 offset1:1\ns_waitcnt lgkmcnt(7)\n" \
"v_pk_fma_f32 v[74:75],v[92:93],v[44:45],v[74:75]\ns_add_u32 m0,s96,0\ns_and_b32 s96,s31,1\ns_lshr_b32 s31,s31,1\n" \
"s_lshl_b32 s31,s31,10\ns_add_u32 s31,s31,s96\ns_lshl_b32 s31,s31,7\ns_add_u32 s31,s31,s92\n" \
"buffer_load_dwordx4 v4,s[40:43],s31 offen lds\nv_pk_add_f32 v[100:101],v[100:101],v[72:73]\nv_pk_fma_f32 v[102:103],v[72:73],v[72:73],v[102:103]\nv_pk_fma_f32 v[76:77],v[94:95],v[46:47],v[76:77]\n" \
"v_pk_add_f32 v[100:101],v[100:101],v[74:75]\nv_pk_fma_f32 v[102:103],v[74:75],v[74:75],v[102:103]\nds_read2_b64 v[20:23],v60 offset0:2 offset1:3\ns_waitcnt lgkmcnt(7)\n" \
"v_pk_fma_f32 v[78:79],v[96:97],v[56:57],v[78:79]\nv_pk_add_f32 v[100:101],v[100:101],v[76:77]\nbuffer_load_dwordx4 v5,s[40:43],s31 offen offset:1024 lds\nv_pk_fma_f32 v[102:103],v[76:77],v[76:77],v[102:103]\n" \
"v_pk_add_f32 v[100:101],v[100:101],v[78:79]\nv_pk_fma_f32 v[102:103],v[78:79],v[78:79],v[102:103]\nds_read2_b64 v[24:27],v60 offset0:4 offset1:5\nv_add_f32 v100,v100,v101\n" \
"v_add_f32 v102,v102,v103\nds_read2_b64 v[28:31],v60 offset0:6 offset1:7\nds_read_b64 v[58:59],v60 offset:64\nbuffer_load_dwordx4 v6,s[40:43],s31 offen offset:2048 lds\n" \
"v_permlane16_swap_b32 v100,v102\nv_add_f32 v100,v100,v102\ns_lshr_b32 s100,s33,3\ns_add_u32 s100,s100,3\n" \
"v_add_f32_dpp v100,v100,v100 quad_perm:[1,0,3,2] row_mask:0xf bank_mask:0xf bound_ctrl:1\ns_sub_u32 s101,s100,4\ns_min_u32 s100,s100,s101\nv_add_f32_dpp v100,v100,v100 quad_perm:[2,3,0,1] row_mask:0xf bank_mask:0xf bound_ctrl:1\n" \
"buffer_load_dwordx4 v7,s[40:43],s31 offen offset:3072 lds\ns_add_u32 m0,s95,0\nv_add_f32_dpp v100,v100,v100 row_half_mirror row_mask:0xf bank_mask:0xf bound_ctrl:1\ns_and_b32 s95,s97,1\n" \
"s_lshr_b32 s97,s97,1\nv_add_f32_dpp v100,v100,v100 row_mirror row_mask:0xf bank_mask:0xf bound_ctrl:1\nv_mul_f32 v104,0x3ae38e39,v100\nv_mul_f32 v102,0x3ae38e39,v100\n" \
"s_lshl_b32 s97,s97,10\ns_add_u32 s97,s97,s95\ns_lshl_b32 s97,s97,7\ns_add_u32 s97,s97,s92\n" \
"buffer_load_dwordx4 v4,s[40:43],s97 offen lds\nv_permlane16_swap_b32 v104,v102\nv_fma_f32 v102,-v104,v104,v102\nv_max_f32 v102,0,v102\n" \
"v_add_f32 v102,0x3727c5ac,v102\nv_rsq_f32 v100,v102\ns_lshl_b32 s101,s100,12\nbuffer_load_dwordx4 v5,s[40:43],s97 offen offset:1024 lds\n" \
"v_mul_f32 v101,-v104,v100\nv_pk_fma_f32 v[80:81],v[62:63],v[100:101],v[100:101] op_sel:[0,0,1] op_sel_hi:[1,0,1]\nv_pk_fma_f32 v[82:83],v[64:65],v[100:101],v[100:101] op_sel:[0,0,1] op_sel_hi:[1,0,1]\nv_pk_fma_f32 v[84:85],v[66:67],v[100:101],v[100:101] op_sel:[0,0,1] op_sel_hi:[1,0,1]\n" \
"v_pk_fma_f32 v[86:87],v[68:69],v[100:101],v[100:101] op_sel:[0,0,1] op_sel_hi:[1,0,1]\nv_pk_fma_f32 v[88:89],v[70:71],v[100:101],v[100:101] op_sel:[0,0,1] op_sel_hi:[1,0,1]\nv_pk_fma_f32 v[90:91],v[72:73],v[100:101],v[100:101] op_sel:[0,0,1] op_sel_hi:[1,0,1]\nv_pk_fma_f32 v[92:93],v[74:75],v[100:101],v[100:101] op_sel:[0,0,1] op_sel_hi:[1,0,1]\n" \
"buffer_load_dwordx4 v6,s[40:43],s97 offen offset:2048 lds\nv_pk_fma_f32 v[94:95],v[76:77],v[100:101],v[100:101] op_sel:[0,0,1] op_sel_hi:[1,0,1]\nv_pk_fma_f32 v[96:97],v[78:79],v[100:101],v[100:101] op_sel:[0,0,1] op_sel_hi:[1,0,1]\ns_add_u32 s101,s101,s57\n" \
"s_waitcnt lgkmcnt(9)\nv_readlane_b32 s24,v112,0\nv_mov_b32 v60,v61\ns_mov_b32 s25,s24\n" \
"v_pk_add_f32 v[112:113],v[112:113],s[24:25] neg_lo:[0,1] neg_hi:[0,1]\ns_waitcnt lgkmcnt(4)\nv_pk_fma_f32 v[80:81],v[80:81],v[16:17],v[112:113]\nbuffer_load_dwordx4 v7,s[40:43],s97 offen offset:3072 lds\n" \
"v_pk_mul_f32 v[102:103],v[80:81],v[80:81]\ns_add_u32 m0,s101,0\ns_and_b32 s101,s100,1\ns_lshr_b32 s100,s100,1\n" \
"s_lshl_b32 s100,s100,10\ns_add_u32 s100,s100,s101\ns_lshl_b32 s100,s100,7\ns_add_u32 s100,s100,s92\n" \
"buffer_load_dwordx4 v4,s[40:43],s100 offen lds\nv_pk_add_f32 v[114:115],v[114:115],s[24:25] neg_lo:[0,1] neg_hi:[0,1]\nv_pk_fma_f32 v[82:83],v[82:83],v[18:19],v[114:115]\nv_pk_add_f32 v[100:101],v[80:81],v[82:83]\n" \
"v_pk_fma_f32 v[102:103],v[82:83],v[82:83],v[102:103]\nv_pk_add_f32 v[116:117],v[116:117],s[24:25] neg_lo:[0,1] neg_hi:[0,1]\ns_waitcnt lgkmcnt(3)\nv_pk_fma_f32 v[84:85],v[84:85],v[20:21],v[116:117]\n" \
"v_pk_add_f32 v[100:101],v[100:101],v[84:85]\nbuffer_load_dwordx4 v5,s[40:43],s100 offen offset:1024 lds\nv_pk_fma_f32 v[102:103],v[84:85],v[84:85],v[102:103]\nv_pk_add_f32 v[118:119],v[118:119],s[24:25] neg_lo:[0,1] neg_hi:[0,1]\n" \
"v_pk_fma_f32 v[86:87],v[86:87],v[22:23],v[118:119]\nv_pk_add_f32 v[100:101],v[100:101],v[86:87]\nv_pk_fma_f32 v[102:103],v[86:87],v[86:87],v[102:103]\nv_pk_add_f32 v[120:121],v[120:121],s[24:25] neg_lo:[0,1] neg_hi:[0,1]\n" \
"s_waitcnt lgkmcnt(2)\nv_pk_fma_f32 v[88:89],v[88:89],v[24:25],v[120:121]\nbuffer_load_dwordx4 v6,s[40:43],s100 offen offset:2048 lds\nv_pk_add_f32 v[100:101],v[100:101],v[88:89]\n" \
"v_pk_fma_f32 v[102:103],v[88:89],v[88:89],v[102:103]\nv_pk_add_f32 v[122:123],v[122:123],s[24:25] neg_lo:[0,1] neg_hi:[0,1]\nv_pk_fma_f32 v[90:91],v[90:91],v[26:27],v[122:123]\nv_pk_add_f32 v[100:101],v[100:101],v[90:91]\n" \
"v_pk_fma_f32 v[102:103],v[90:91],v[90:91],v[102:103]\nv_pk_add_f32 v[124:125],v[124:125],s[24:25] neg_lo:[0,1] neg_hi:[0,1]\nbuffer_load_dwordx4 v7,s[40:43],s100 offen offset:3072 lds\ns_waitcnt lgkmcnt(1)\n" \
"v_pk_fma_f32 v[92:93],v[92:93],v[28:29],v[124:125]\nv_pk_add_f32 v[100:101],v[100:101],v[92:93]\nv_pk_fma_f32 v[102:103],v[92:93],v[92:93],v[102:103]\nv_pk_add_f32 v[126:127],v[126:127],s[24:25] neg_lo:[0,1] neg_hi:[0,1]\n" \
"v_pk_fma_f32 v[94:95],v[94:95],v[30:31],v[126:127]\nv_pk_add_f32 v[100:101],v[100:101],v[94:95]\nv_pk_fma_f32 v[102:103],v[94:95],v[94:95],v[102:103]\nv_pk_add_f32 v[98:99],v[98:99],s[24:25] neg_lo:[0,1] neg_hi:[0,1]\n" \
"s_waitcnt lgkmcnt(0)\nv_pk_fma_f32 v[96:97],v[96:97],v[58:59],v[98:99]\nv_pk_add_f32 v[100:101],v[100:101],v[96:97]\nv_pk_fma_f32 v[102:103],v[96:97],v[96:97],v[102:103]\n" \
"v_add_f32 v100,v100,v101\nv_add_f32 v102,v102,v103\nv_pk_add_f32 v[62:63],v[62:63],v[60:61] op_sel_hi:[1,0]\nv_cvt_pk_bf16_f32 v32,v62,v63\n" \
"v_permlane16_swap_b32 v100,v102\nv_add_f32 v100,v100,v102\nv_pk_add_f32 v[64:65],v[64:65],v[60:61] op_sel_hi:[1,0]\nv_cvt_pk_bf16_f32 v33,v64,v65\n" \
"v_add_f32_dpp v100,v100,v100 quad_perm:[1,0,3,2] row_mask:0xf bank_mask:0xf bound_ctrl:1\nv_pk_add_f32 v[66:67],v[66:67],v[60:61] op_sel_hi:[1,0]\nv_cvt_pk_bf16_f32 v34,v66,v67\nv_add_f32_dpp v100,v100,v100 quad_perm:[2,3,0,1] row_mask:0xf bank_mask:0xf bound_ctrl:1\n" \
"v_pk_add_f32 v[68:69],v[68:69],v[60:61] op_sel_hi:[1,0]\nv_cvt_pk_bf16_f32 v35,v68,v69\nv_add_f32_dpp v100,v100,v100 row_half_mirror row_mask:0xf bank_mask:0xf bound_ctrl:1\nv_pk_add_f32 v[70:71],v[70:71],v[60:61] op_sel_hi:[1,0]\n" \
"v_cvt_pk_bf16_f32 v36,v70,v71\nv_add_f32_dpp v100,v100,v100 row_mirror row_mask:0xf bank_mask:0xf bound_ctrl:1\nv_mul_f32 v104,0x3ae38e39,v100\nv_mul_f32 v102,0x3ae38e39,v100\n" \
"v_pk_add_f32 v[72:73],v[72:73],v[60:61] op_sel_hi:[1,0]\nv_cvt_pk_bf16_f32 v37,v72,v73\nv_permlane16_swap_b32 v104,v102\nv_fma_f32 v102,-v104,v104,v102\n" \
"v_max_f32 v102,0,v102\nv_add_f32 v102,0x3727c5ac,v102\nv_rsq_f32_e64 v100,v102\nv_pk_add_f32 v[74:75],v[74:75],v[60:61] op_sel_hi:[1,0]\n" \
"v_mul_f32 v101,-v104,v100\nv_pk_fma_f32 v[80:81],v[80:81],v[100:101],v[100:101] op_sel:[0,0,1] op_sel_hi:[1,0,1]\nv_pk_fma_f32 v[82:83],v[82:83],v[100:101],v[100:101] op_sel:[0,0,1] op_sel_hi:[1,0,1]\nv_pk_fma_f32 v[84:85],v[84:85],v[100:101],v[100:101] op_sel:[0,0,1] op_sel_hi:[1,0,1]\n" \
"v_pk_fma_f32 v[86:87],v[86:87],v[100:101],v[100:101] op_sel:[0,0,1] op_sel_hi:[1,0,1]\nv_cvt_pk_bf16_f32 v16,v80,v81\nv_cvt_pk_bf16_f32 v17,v82,v83\nv_cvt_pk_bf16_f32 v18,v84,v85\n" \
"v_cvt_pk_bf16_f32 v19,v86,v87\nds_write_b128 v48,v[16:19]\nv_pk_fma_f32 v[88:89],v[88:89],v[100:101],v[100:101] op_sel:[0,0,1] op_sel_hi:[1,0,1]\nv_pk_fma_f32 v[90:91],v[90:91],v[100:101],v[100:101] op_sel:[0,0,1] op_sel_hi:[1,0,1]\n" \
"v_pk_fma_f32 v[92:93],v[92:93],v[100:101],v[100:101] op_sel:[0,0,1] op_sel_hi:[1,0,1]\nv_pk_fma_f32 v[94:95],v[94:95],v[100:101],v[100:101] op_sel:[0,0,1] op_sel_hi:[1,0,1]\nv_cvt_pk_bf16_f32 v20,v88,v89\nv_cvt_pk_bf16_f32 v21,v90,v91\n" \
"v_cvt_pk_bf16_f32 v22,v92,v93\nv_cvt_pk_bf16_f32 v23,v94,v95\nds_write_b128 v48,v[20:23] offset:512\nv_pk_fma_f32 v[96:97],v[96:97],v[100:101],v[100:101] op_sel:[0,0,1] op_sel_hi:[1,0,1]\n" \
"v_cvt_pk_bf16_f32 v24,v96,v97\nds_write_b32 v49,v24 offset:1024\nv_cvt_pk_bf16_f32 v38,v74,v75\nv_pk_add_f32 v[76:77],v[76:77],v[60:61] op_sel_hi:[1,0]\n" \
"v_cvt_pk_bf16_f32 v39,v76,v77\nv_pk_add_f32 v[78:79],v[78:79],v[60:61] op_sel_hi:[1,0]\nv_cvt_pk_bf16_f32 v40,v78,v79\nv_bfe_u32 v50,v0,2,2\n" \
"v_mul_u32_u24 v50,4896,v50\nv_bfe_u32 v51,v0,4,2\nv_lshl_add_u32 v51,v51,2,s3\nv_lshl_add_u32 v50,v51,5,v50\n" \
"v_and_b32 v52,3,v0\nv_lshl_add_u32 v50,v52,3,v50\nv_and_b32 v52,0xf0,v1\nv_lshl_add_u32 v51,v51,16,v52\n" \
"v_bfrev_b32 v52,1\nv_cmp_gt_u32 vcc,256,v1\nv_and_b32 v53,31,v0\nv_mul_u32_u24 v53,12,v53\n" \
"v_cndmask_b32 v52,v52,v51,vcc\nv_sub_u32 v53,v12,v53\ns_lshl_b32 s24,s34,8\ns_add_u32 s24,s24,9764864\n" \
"s_waitcnt lgkmcnt(0)\ns_barrier\nds_read_b64_tr_b16 v[112:113],v50 offset:9216\nds_read_b64_tr_b16 v[114:115],v50 offset:11664\n" \
"ds_read_b64_tr_b16 v[116:117],v50 offset:9728\nds_read_b64_tr_b16 v[118:119],v50 offset:12176\nds_read_b64_tr_b16 v[120:121],v50 offset:10240\nds_read_b64_tr_b16 v[122:123],v50 offset:12688\n" \
"s_add_u32 s25,s24,1048576\ns_add_u32 s26,s24,2097152\ns_add_u32 s13,s13,7405568\ns_load_dwordx2 s[4:5],s[0:1],56\n" \
"s_load_dwordx2 s[16:17],s[0:1],64\ns_waitcnt lgkmcnt(0)\nbuffer_store_dwordx4 v[112:115],v51,s[36:39],s24 offen sc1\nbuffer_store_dwordx4 v[116:119],v51,s[36:39],s25 offen sc1\n" \
"buffer_store_dwordx4 v[120:123],v52,s[36:39],s26 offen sc1\nbuffer_store_dwordx4 v[32:35],v12,s[36:39],s13 offen sc1\nbuffer_store_dwordx4 v[36:39],v12,s[36:39],s13 offen offset:512 sc1\nbuffer_store_dword v40,v53,s[36:39],s13 offen offset:1024 sc1\n" \
"s_lshr_b32 s31,s33,3\ns_lshl1_add_u32 s31,s31,0\ns_sub_u32 s96,s31,7\ns_min_u32 s31,s31,s96\n" \
"s_lshl_b32 s96,s31,12\ns_add_u32 s96,s96,s57\ns_add_u32 m0,s96,0\ns_and_b32 s96,s31,1\n" \
"s_lshr_b32 s31,s31,1\ns_lshl_b32 s31,s31,10\ns_add_u32 s31,s31,s96\ns_lshl_b32 s31,s31,7\n" \
"s_add_u32 s31,s31,s92\ns_add_u32 s31,s31,262144\ns_lshr_b32 s97,s33,3\ns_lshl1_add_u32 s97,s97,1\n" \
"s_sub_u32 s95,s97,7\ns_min_u32 s97,s97,s95\ns_lshl_b32 s95,s97,12\ns_add_u32 s95,s95,s57\n" \
"s_lshr_b32 s100,s33,3\ns_lshl1_add_u32 s100,s100,2\ns_sub_u32 s101,s100,7\ns_min_u32 s100,s100,s101\n" \
"s_lshl_b32 s101,s100,12\ns_add_u32 s101,s101,s57\nv_and_b32 v64,63,v0\ns_and_b32 s25,s33,7\n" \
"v_lshrrev_b32 v66,5,v64\nv_and_b32 v67,1,v0\ns_lshl2_add_u32 s25,s25,s3\ns_and_b32 s26,s33,6\n" \
"s_lshr_b32 s27,s3,1\nv_lshl_add_u32 v66,v66,1,v67\ns_mul_i32 s24,s32,1152\ns_lshl_b32 s25,s25,5\n" \
"s_add_u32 s26,s26,s27\ns_mov_b32 s6,9216\ns_mov_b32 s7,0x20000\nv_lshlrev_b32 v68,3,v66\n" \
"s_add_u32 s25,s25,s24\nv_bfe_u32 v72,v0,1,1\nv_lshrrev_b32 v74,2,v64\ns_lshl4_add_u32 s26,s26,s24\n" \
"v_and_b32 v73,12,v0\nv_lshrrev_b32 v71,4,v64\nv_and_b32 v69,7,v0\nbuffer_load_dwordx2 v[80:81],v68,s[4:7],s25 offen\n" \
"v_lshl_add_u32 v72,v72,2,s21\nv_and_b32 v74,12,v74\nv_add_u32 v71,v71,v73\ns_lshl_b32 s24,s3,4\n" \
"v_lshlrev_b32 v70,1,v69\nv_lshl_add_u32 v72,v72,4,v74\ns_mov_b32 s15,s19\nv_add_u32 v71,s24,v71\n" \
"buffer_load_ushort v82,v70,s[4:7],s26 offen offset:1024\ns_mov_b32 s18,4096\ns_mov_b32 s19,0x20000\nv_lshlrev_b32 v74,1,v71\n" \
"s_lshl_b32 s24,s22,8\nbuffer_load_ushort v83,v74,s[16:19],s24 offen\nv_add_u32 v72,s3,v72\nbuffer_load_ushort v84,v74,s[16:19],s24 offen offset:128\n" \
"v_lshlrev_b32_e64 v75,1,v72\nbuffer_load_ushort v85,v75,s[16:19],s24 offen\nv_lshl_add_u32 v73,v67,1,v73\nv_mul_u32_u24 v91,1152,v72\n" \
"v_lshl_add_u32 v91,v73,1,v91\nv_lshlrev_b32_e64 v73,2,v73\nv_add_u32 v73,163712,v73\nds_read_b64 v[86:87],v73\n" \
"s_mul_i32 s24,s22,147456\ns_mul_i32 s25,s20,288\nv_bfe_u32 v58,v0,1,1\ns_add_u32 s24,s24,s25\n" \
"v_mul_u32_u24 v58,544,v58\nv_bfe_u32 v75,v0,2,1\ns_lshl_b32 s25,s21,6\nv_lshl_add_u32 v58,v75,8,v58\n" \
"s_add_u32 s25,s25,s24\nv_bfe_u32 v75,v0,3,2\ns_add_u32 s24,s24,256\nv_lshl_add_u32 v58,v75,6,v58\n" \
"v_add_u32 v91,s24,v91\nv_mul_u32_u24 v90,1152,v71\nv_and_b32 v74,3,v0\nv_lshl_add_u32 v58,v66,4,v58\n" \
"s_mul_i32 s24,s3,1056\nv_lshl_add_u32 v90,v74,3,v90\nv_add_u32 v58,s24,v58\nv_add_u32 v90,s25,v90\n" \
"s_and_b32 s24,s3,1\nv_mul_u32_u24 v60,288,v75\ns_lshl_b32 s24,s24,2\ns_lshr_b32 s25,s3,1\n" \
"v_lshrrev_b32 v75,5,v64\ns_add_u32 s24,s24,s25\nv_lshl_add_u32 v60,v75,6,v60\ns_lshl_b32 s24,s24,5\n" \
"v_lshl_add_u32 v60,v69,2,v60\ns_add_u32 s24,s24,4224\nv_add_u32 v60,s24,v60\ns_bfe_u32 s24,s33,0x10001\n" \
"s_add_u32 s24,s24,s15\ns_add_u32 s24,s24,17\ns_lshl2_add_u32 s24,s24,s3\ns_waitcnt lgkmcnt(0)\n" \
"v_lshrrev_b32 v86,16,v86\ns_lshl_b32 s24,s24,7\nv_mov_b32 v92,v4\nv_mov_b32 v93,v5\n" \
"v_add_u32 v74,s57,v1\nv_or_b32 v95,v86,v87\nv_lshl_add_u32 v62,v64,1,s24\nv_bfrev_b32 v76,1\n" \
"v_cmp_eq_u32 vcc,0,v64\ns_lshl2_add_u32 s24,s34,s3\ns_lshl2_add_u32 s24,s24,65536\nv_cndmask_b32 v76,v76,0,vcc\n" \
"v_mov_b32 v77,1\ns_barrier\nv_mov_b32 v57,0xc0135761\nv_add_u32 v59,0x80000,v1\n" \
"v_lshl_add_u32 v61,v64,3,s23\nv_mov_b32 v63,0\nv_bfe_u32 v100,v0,4,2\nv_bfe_u32 v101,v0,2,2\n" \
"v_lshl_add_u32 v100,v100,3,v101\nv_bfe_u32 v101,v0,3,2\nv_lshl_add_u32 v100,v100,2,v101\nv_and_b32 v101,3,v0\n" \
"v_lshl_add_u32 v100,v100,2,v101\nv_lshlrev_b32 v100,3,v100\nv_xor_b32 v8,0,v100\nv_add_u32 v8,s57,v8\n" \
"v_mov_b32 v88,v8\nv_xor_b32 v9,32,v100\nv_add_u32 v9,s57,v9\nv_xor_b32 v10,64,v100\n" \
"v_add_u32 v10,s57,v10\nv_xor_b32 v11,96,v100\nv_add_u32 v11,s57,v11\ns_waitcnt vmcnt(5)\n" \
"buffer_store_dword v77,v76,s[36:39],s24 offen sc1\nds_read_b64_tr_b16 v[128:129],v8\nds_read_b64_tr_b16 v[130:131],v8 offset:512\nds_read_b64_tr_b16 v[132:133],v9\n" \
"ds_read_b64_tr_b16 v[134:135],v9 offset:512\nds_read_b64_tr_b16 v[136:137],v10\nds_read_b64_tr_b16 v[138:139],v10 offset:512\nds_read_b64_tr_b16 v[140:141],v11\n" \
"ds_read_b64_tr_b16 v[142:143],v11 offset:512\nds_read_b64_tr_b16 v[144:145],v8 offset:4096\nds_read_b64_tr_b16 v[146:147],v8 offset:4608\nds_read_b64_tr_b16 v[148:149],v9 offset:4096\n" \
"ds_read_b64_tr_b16 v[150:151],v9 offset:4608\nds_read_b64_tr_b16 v[152:153],v10 offset:4096\nds_read_b64_tr_b16 v[154:155],v10 offset:4608\nds_read_b64_tr_b16 v[156:157],v11 offset:4096\n" \
"ds_read_b64_tr_b16 v[158:159],v11 offset:4608\nds_read_b64_tr_b16 v[160:161],v8 offset:8192\nds_read_b64_tr_b16 v[162:163],v8 offset:8704\ns_waitcnt vmcnt(5)\n" \
"v_and_b32 v53,0xffff0000,v80\nv_and_b32 v55,0xffff0000,v81\nv_lshlrev_b32 v52,16,v80\nv_lshlrev_b32 v54,16,v81\n" \
"ds_read_b64_tr_b16 v[164:165],v9 offset:8192\nds_read_b64_tr_b16 v[166:167],v9 offset:8704\nds_read_b64_tr_b16 v[168:169],v10 offset:8192\nds_read_b64_tr_b16 v[170:171],v10 offset:8704\n" \
"ds_read_b64_tr_b16 v[172:173],v11 offset:8192\ns_waitcnt vmcnt(4)\nv_lshlrev_b32 v56,16,v82\nds_read_b64_tr_b16 v[174:175],v11 offset:8704\n" \
"ds_read_b64_tr_b16 v[176:177],v8 offset:12288\nds_read_b64_tr_b16 v[178:179],v8 offset:12800\nds_read_b64_tr_b16 v[180:181],v9 offset:12288\nds_read_b64_tr_b16 v[182:183],v9 offset:12800\n" \
"s_waitcnt vmcnt(2)\nv_lshl_or_b32 v89,v84,16,v83\nds_write_b128 v74,v[88:91] offset:28672\ns_waitcnt vmcnt(1)\n" \
"v_lshlrev_b32_e64 v94,16,v85\nds_write_b128 v74,v[92:95] offset:29696\nds_read_b64_tr_b16 v[184:185],v10 offset:12288\nds_read_b64_tr_b16 v[186:187],v10 offset:12800\n" \
"ds_read_b64_tr_b16 v[188:189],v11 offset:12288\nds_read_b64_tr_b16 v[190:191],v11 offset:12800\ns_waitcnt lgkmcnt(0)\nbuffer_load_dwordx4 v4,s[40:43],s31 offen lds\n" \
"buffer_load_dwordx4 v5,s[40:43],s31 offen offset:1024 lds\nbuffer_load_dwordx4 v6,s[40:43],s31 offen offset:2048 lds\nbuffer_load_dwordx4 v7,s[40:43],s31 offen offset:3072 lds\ns_add_u32 m0,s95,0\n" \
"s_and_b32 s95,s97,1\ns_lshr_b32 s97,s97,1\ns_lshl_b32 s97,s97,10\ns_add_u32 s97,s97,s95\n" \
"s_lshl_b32 s97,s97,7\ns_add_u32 s97,s97,s92\ns_add_u32 s97,s97,262144\nbuffer_load_dwordx4 v4,s[40:43],s97 offen lds\n" \
"buffer_load_dwordx4 v5,s[40:43],s97 offen offset:1024 lds\nbuffer_load_dwordx4 v6,s[40:43],s97 offen offset:2048 lds\nbuffer_load_dwordx4 v7,s[40:43],s97 offen offset:3072 lds\ns_add_u32 m0,s101,0\n" \
"s_and_b32 s101,s100,1\ns_lshr_b32 s100,s100,1\ns_lshl_b32 s100,s100,10\ns_add_u32 s100,s100,s101\n" \
"s_lshl_b32 s100,s100,7\ns_add_u32 s100,s100,s92\ns_add_u32 s100,s100,262144\nbuffer_load_dwordx4 v4,s[40:43],s100 offen lds\n" \
"buffer_load_dwordx4 v5,s[40:43],s100 offen offset:1024 lds\nbuffer_load_dwordx4 v6,s[40:43],s100 offen offset:2048 lds\nbuffer_load_dwordx4 v7,s[40:43],s100 offen offset:3072 lds\ns_lshr_b32 s31,s33,3\n" \
"s_lshl1_add_u32 s31,s31,3\ns_sub_u32 s96,s31,7\ns_min_u32 s31,s31,s96\ns_lshl_b32 s96,s31,12\n" \
"s_add_u32 s96,s96,s57\ns_add_u32 m0,s96,0\ns_and_b32 s96,s31,1\ns_lshr_b32 s31,s31,1\n" \
"s_lshl_b32 s31,s31,10\ns_add_u32 s31,s31,s96\ns_lshl_b32 s31,s31,7\ns_add_u32 s31,s31,s92\n" \
"s_add_u32 s31,s31,262144\nbuffer_load_dwordx4 v4,s[40:43],s31 offen lds\nbuffer_load_dwordx4 v5,s[40:43],s31 offen offset:1024 lds\nbuffer_load_dwordx4 v6,s[40:43],s31 offen offset:2048 lds\n" \
"buffer_load_dwordx4 v7,s[40:43],s31 offen offset:3072 lds\ns_lshr_b32 s97,s33,3\ns_lshl1_add_u32 s97,s97,4\ns_sub_u32 s95,s97,7\n" \
"s_min_u32 s97,s97,s95\ns_lshl_b32 s95,s97,12\ns_add_u32 s95,s95,s57\ns_add_u32 m0,s95,0\n" \
"s_and_b32 s95,s97,1\ns_lshr_b32 s97,s97,1\ns_lshl_b32 s97,s97,10\ns_add_u32 s97,s97,s95\n" \
"s_lshl_b32 s97,s97,7\ns_add_u32 s97,s97,s92\ns_add_u32 s97,s97,262144\nbuffer_load_dwordx4 v4,s[40:43],s97 offen lds\n" \
"buffer_load_dwordx4 v5,s[40:43],s97 offen offset:1024 lds\nbuffer_load_dwordx4 v6,s[40:43],s97 offen offset:2048 lds\nbuffer_load_dwordx4 v7,s[40:43],s97 offen offset:3072 lds\ns_lshr_b32 s100,s33,3\n" \
"s_lshl1_add_u32 s100,s100,5\ns_sub_u32 s101,s100,7\ns_min_u32 s100,s100,s101\ns_lshl_b32 s101,s100,12\n" \
"s_add_u32 s101,s101,s57\ns_add_u32 m0,s101,0\ns_and_b32 s101,s100,1\ns_lshr_b32 s100,s100,1\n" \
"s_lshl_b32 s100,s100,10\ns_add_u32 s100,s100,s101\ns_lshl_b32 s100,s100,7\ns_add_u32 s100,s100,s92\n" \
"s_add_u32 s100,s100,262144\nbuffer_load_dwordx4 v4,s[40:43],s100 offen lds\nbuffer_load_dwordx4 v5,s[40:43],s100 offen offset:1024 lds\nbuffer_load_dwordx4 v6,s[40:43],s100 offen offset:2048 lds\n" \
"buffer_load_dwordx4 v7,s[40:43],s100 offen offset:3072 lds\ns_lshr_b32 s31,s33,3\ns_lshl1_add_u32 s31,s31,6\ns_sub_u32 s96,s31,7\n" \
"s_min_u32 s31,s31,s96\ns_lshl_b32 s96,s31,12\ns_add_u32 s96,s96,s57\ns_add_u32 m0,s96,0\n" \
"s_and_b32 s96,s31,1\ns_lshr_b32 s31,s31,1\ns_lshl_b32 s31,s31,10\ns_add_u32 s31,s31,s96\n" \
"s_lshl_b32 s31,s31,7\ns_add_u32 s31,s31,s92\ns_add_u32 s31,s31,262144\nbuffer_load_dwordx4 v4,s[40:43],s31 offen lds\n" \
"buffer_load_dwordx4 v5,s[40:43],s31 offen offset:1024 lds\nbuffer_load_dwordx4 v6,s[40:43],s31 offen offset:2048 lds\nbuffer_load_dwordx4 v7,s[40:43],s31 offen offset:3072 lds\ns_branch .Lgemm_%=\n" \
".LC_%=:\nv_bfe_u32 v112,v0,3,2\nv_bfe_u32 v113,v0,5,1\nv_lshl_add_u32 v112,v112,8,v113\n" \
"s_lshl_b32 s48,s33,2\nv_lshrrev_b32 v113,3,v0\ns_add_u32 s48,s48,s3\nv_and_b32 v113,6,v113\n" \
"v_and_b32 v114,7,v0\ns_mul_i32 s48,s48,18432\ns_mul_i32 s49,s32,1152\nv_mul_u32_u24 v112,9216,v112\n" \
"v_xor_b32 v113,v113,v114\ns_add_u32 s48,s48,s49\nv_lshl_add_u32 v4,v113,4,v112\ns_mov_b32 s10,18874368\n" \
"s_mov_b32 s11,0x20000\ns_add_u32 s48,s48,1024\ns_add_u32 m0,s88,20224\ns_add_u32 s50,s48,9437184\n" \
"buffer_load_dwordx4 v4,s[8:11],s48 offen lds\ns_add_u32 m0,s88,24320\ns_mov_b32 s51,4096\nbuffer_load_dwordx4 v4,s[8:11],s50 offen lds\n" \
"s_add_u32 m0,s88,28416\ns_mov_b32 s52,8192\nbuffer_load_dwordx4 v2,s[4:7],0 offen lds\ns_add_u32 m0,s88,32512\n" \
"s_mov_b32 s53,14336\nbuffer_load_dwordx4 v2,s[4:7],s51 offen lds\nbuffer_load_dwordx4 v[234:237],v1,s[4:7],s52 offen\nbuffer_load_dwordx4 v[238:241],v1,s[4:7],s52 offen offset:1024\n" \
"buffer_load_dwordx4 v[242:245],v2,s[4:7],s53 offen\ns_mov_b32 s52,18432\nbuffer_load_dwordx4 v[252:255],v2,s[4:7],s52 offen\ns_and_b32 s60,s33,7\n" \
"s_mul_i32 s92,s3,589824\ns_mul_i32 s57,s3,30720\ns_add_u32 s92,s92,s49\ns_lshl_b32 s62,s60,7\n" \
"v_add_u32 v5,17408,v4\nv_add_u32 v6,34816,v4\nv_add_u32 v7,52224,v4\ns_add_u32 s57,s57,38272\n" \
"s_add_u32 s92,s92,s62\ns_load_dwordx4 s[44:47],s[0:1],72\nv_and_b32 v120,0x3c0,v1\ns_lshr_b32 s59,s33,3\n" \
"v_bfe_u32 v121,v0,3,2\nv_lshlrev_b32 v120,1,v120\ns_lshl_b32 s56,s34,8\nv_bfe_u32 v73,v0,4,2\n" \
"s_lshl_b32 s59,s59,3\nv_bfe_u32 v113,v0,4,2\nv_lshl_add_u32 v120,v121,5,v120\nv_and_b32 v122,3,v0\n" \
"s_mul_i32 s54,s34,9344\ns_add_u32 s56,s56,10289152\ns_add_u32 s59,s59,s32\nv_and_b32 v72,15,v0\n" \
"v_cvt_f32_u32 v73,v73\nv_cvt_f32_u32 v74,s3\nv_mul_u32_u24 v113,589824,v113\nv_and_b32 v112,15,v0\n" \
"v_lshl_add_u32 v120,v122,3,v120\ns_add_u32 s35,s54,524288\ns_cmp_eq_u32 s3,2\ns_cselect_b32 s56,s56,2147483648\n" \
"s_lshl_b32 s59,s59,3\nv_bfe_u32 v233,v0,3,3\nv_mul_u32_u24 v233,8192,v233\nv_lshl_add_u32 v233,v1,3,v233\n" \
"s_lshl2_add_u32 s100,s33,s3\ns_add_u32 s100,s100,4294967260\ns_and_b32 s101,s100,31\ns_lshl_b32 s101,s101,3\n" \
"s_cmp_lt_u32 s100,256\ns_cselect_b32 s100,s100,0x40000\ns_lshr_b32 s100,s100,5\ns_add_u32 s100,s100,s101\n" \
"s_lshl_b32 s100,s100,6\ns_add_u32 s100,s100,s32\ns_mul_i32 s100,s100,1152\ns_mov_b32 m0,162176\n" \
"v_cvt_f32_u32_e64 v72,v72\nv_fmamk_f32 v73,v73,0x41800000,v74\nv_lshl_add_u32 v113,v112,4,v113\ns_add_u32 s58,s35,8192\n" \
"v_bfe_u32 v119,v0,4,2\nv_xor_b32 v8,0,v120\nv_xor_b32 v9,32,v120\nv_xor_b32 v10,64,v120\n" \
"v_xor_b32 v11,96,v120\ns_add_u32 s59,s59,s60\nv_mul_f32 v72,0xbb800000,v72\nv_and_b32 v118,15,v0\n" \
"v_mul_f32 v73,4.0,v73\nv_and_b32 v116,0x70,v2\nv_add_u32 v113,s56,v113\nv_add_u32 v114,s58,v2\n" \
"v_cmp_gt_u32 vcc,68,v0\nv_lshrrev_b32 v115,3,v0\nv_cvt_f32_u32 v120,s34\ns_lshl_b32 s59,s59,7\n" \
"s_lshl_b32 s61,s34,4\nv_lshrrev_b32 v48,1,v118\nv_and_b32 v49,1,v119\nv_mul_f32 v73,v73,v72\n" \
"v_cndmask_b32 v113,v113,v114,vcc\nv_lshl_add_u32 v115,v115,16,v116\nv_mul_f32 v50,0x3b800000,v120\ns_add_u32 s87,s59,262144\n" \
"s_add_u32 s61,s61,65536\nv_cvt_f32_u32 v48,v48\nv_cvt_f32_u32 v49,v49\nv_fmamk_f32 v79,v72,0x41800000,v73\n" \
"v_bfe_u32 v100,v0,1,2\nv_and_b32 v101,1,v0\nv_add_u32 v115,s87,v115\nv_mov_b32 v116,s61\n" \
"v_cmp_gt_u32 vcc,24,v0\nv_mul_f32 v48,0xbe000000,v48\nv_fma_f32 v49,v49,2.0,v50\nv_cos_f32 v78,v79\n" \
"v_lshl_add_u32 v100,v101,2,v100\nv_cndmask_b32 v115,v116,v115,vcc\nv_mul_f32 v49,v48,v49\nv_fmamk_f32 v81,v72,0x42000000,v73\n" \
"v_fmamk_f32 v83,v72,0x42400000,v73\nv_mul_f32 v101,0x3a000000,v120\nv_cvt_f32_u32 v100,v100\nv_bfrev_b32 v116,1\n" \
"v_cmp_gt_u32 vcc,25,v0\nv_fmamk_f32 v63,v48,0x40800000,v49\nv_cos_f32 v80,v81\nv_cos_f32 v82,v83\n" \
"v_fmamk_f32 v100,v100,0x3e000000,v101\nv_cndmask_b32 v115,v116,v115,vcc\nv_mov_b32 v16,-1\nv_mov_b32 v17,-1\n" \
"v_mov_b32 v18,-1\nv_mov_b32 v19,-1\nv_lshrrev_b32 v50,1,v119\nv_and_b32 v51,1,v0\n" \
"v_cos_f32 v62,v63\nv_cos_f32 v76,v73\nv_sin_f32 v77,v73\nv_sin_f32 v79,v79\n" \
"v_cos_f32 v84,v72\nv_sin_f32 v85,v72\nv_cvt_f32_u32 v101,v119\nbuffer_store_dwordx4 v[16:19],v2,s[36:39],s35 offen sc1\n" \
"v_cmp_eq_u32 vcc,v50,v51\nv_add_f32 v101,v101,v101\nv_mov_b32 v50,0x3e800000\nv_mul_f32 v101,v101,v100\n" \
"v_sin_f32 v81,v81\nv_sin_f32 v83,v83\nv_cndmask_b32 v52,0,v50,vcc\nv_cos_f32 v56,v49\n" \
"v_sin_f32 v57,v49\nv_sin_f32 v63,v63\nv_cos_f32 v106,v101\nv_sin_f32 v107,v101\n" \
"v_cmp_gt_u32 vcc,8,v118\nv_cvt_pk_f16_f32 v128,v76,-v77\nv_cvt_pk_f16_f32 v132,v77,v76\nv_cvt_pk_f16_f32 v129,v78,-v79\n" \
"v_cvt_pk_f16_f32 v133,v79,v78\nv_pk_mul_f32 v[92:93],v[76:77],v[84:85] op_sel:[1,1] op_sel_hi:[0,1] neg_lo:[1,0]\nv_pk_mul_f32 v[94:95],v[78:79],v[84:85] op_sel:[1,1] op_sel_hi:[0,1] neg_lo:[1,0]\ns_add_u32 s55,s35,4096\n" \
"v_cndmask_b32 v102,0,0.5,vcc\nv_lshlrev_b32 v51,31,v0\nbuffer_store_dwordx4 v[16:19],v2,s[36:39],s55 offen sc1\nv_pk_fma_f32 v[76:77],v[76:77],v[84:85],v[92:93] op_sel_hi:[1,0,1]\n" \
"v_pk_fma_f32 v[78:79],v[78:79],v[84:85],v[94:95] op_sel_hi:[1,0,1]\nv_xor_b32 v54,v51,v52\nv_sub_f32_e64 v104,0.5,v102\nv_cvt_pk_f16_f32 v130,v80,-v81\n" \
"v_cvt_pk_f16_f32 v134,v81,v80\nv_cvt_pk_f16_f32 v131,v82,-v83\nv_cvt_pk_f16_f32 v135,v83,v82\nv_pk_mul_f32 v[92:93],v[80:81],v[84:85] op_sel:[1,1] op_sel_hi:[0,1] neg_lo:[1,0]\n" \
"v_pk_mul_f32 v[94:95],v[82:83],v[84:85] op_sel:[1,1] op_sel_hi:[0,1] neg_lo:[1,0]\nv_pk_mul_f32 v[58:59],v[56:57],v[52:53] op_sel_hi:[1,0]\nv_pk_mul_f32 v[60:61],v[56:57],v[54:55] op_sel_hi:[1,0]\nv_pk_mul_f32 v[64:65],v[62:63],v[52:53] op_sel_hi:[1,0]\n" \
"v_pk_mul_f32 v[66:67],v[62:63],v[54:55] op_sel_hi:[1,0]\nv_pk_mul_f32 v[108:109],v[106:107],v[102:103] op_sel_hi:[1,0]\nv_pk_mul_f32 v[110:111],v[106:107],v[104:105] op_sel_hi:[1,0]\nv_pk_fma_f32 v[80:81],v[80:81],v[84:85],v[92:93] op_sel_hi:[1,0,1]\n" \
"v_pk_fma_f32 v[82:83],v[82:83],v[84:85],v[94:95] op_sel_hi:[1,0,1]\nv_fmamk_f32 v57,v48,0x3f800000,v49\nv_fmamk_f32 v63,v48,0x40a00000,v49\nv_fmamk_f32 v107,v100,0x3f800000,v101\n" \
"v_cvt_pk_f16_f32 v136,v76,-v77\nv_cvt_pk_f16_f32 v140,v77,v76\nv_cvt_pk_f16_f32 v137,v78,-v79\nv_cvt_pk_f16_f32 v141,v79,v78\n" \
"v_pk_mul_f32 v[92:93],v[76:77],v[84:85] op_sel:[1,1] op_sel_hi:[0,1] neg_lo:[1,0]\nv_pk_mul_f32 v[94:95],v[78:79],v[84:85] op_sel:[1,1] op_sel_hi:[0,1] neg_lo:[1,0]\nv_cos_f32 v56,v57\nv_cos_f32 v62,v63\n" \
"v_cos_f32_e64 v106,v107\nbuffer_store_dwordx4 v[16:19],v113,s[36:39],0 offen sc1\nv_pk_fma_f32 v[76:77],v[76:77],v[84:85],v[92:93] op_sel_hi:[1,0,1]\nv_pk_fma_f32 v[78:79],v[78:79],v[84:85],v[94:95] op_sel_hi:[1,0,1]\n" \
"v_cvt_pk_f16_f32 v138,v80,-v81\nv_cvt_pk_f16_f32 v142,v81,v80\nv_cvt_pk_f16_f32 v139,v82,-v83\nv_cvt_pk_f16_f32 v143,v83,v82\n" \
"v_pk_mul_f32 v[92:93],v[80:81],v[84:85] op_sel:[1,1] op_sel_hi:[0,1] neg_lo:[1,0]\nv_pk_mul_f32 v[94:95],v[82:83],v[84:85] op_sel:[1,1] op_sel_hi:[0,1] neg_lo:[1,0]\nv_pk_fma_f32 v[80:81],v[80:81],v[84:85],v[92:93] op_sel_hi:[1,0,1]\nv_pk_fma_f32 v[82:83],v[82:83],v[84:85],v[94:95] op_sel_hi:[1,0,1]\n" \
"v_sin_f32 v57,v57\nv_sin_f32 v63,v63\nv_sin_f32_e64 v107,v107\nv_cvt_pk_f16_f32 v144,v76,-v77\n" \
"v_cvt_pk_f16_f32 v148,v77,v76\nv_cvt_pk_f16_f32 v145,v78,-v79\nv_cvt_pk_f16_f32 v149,v79,v78\nv_pk_mul_f32 v[92:93],v[76:77],v[84:85] op_sel:[1,1] op_sel_hi:[0,1] neg_lo:[1,0]\n" \
"v_pk_mul_f32 v[94:95],v[78:79],v[84:85] op_sel:[1,1] op_sel_hi:[0,1] neg_lo:[1,0]\nv_mov_b32 v20,0\nv_mov_b32 v21,0\nv_mov_b32 v22,0\n" \
"v_mov_b32 v23,0\nbuffer_store_dwordx4 v[20:23],v115,s[36:39],0 offen sc1\nv_pk_fma_f32 v[76:77],v[76:77],v[84:85],v[92:93] op_sel_hi:[1,0,1]\nv_pk_fma_f32 v[78:79],v[78:79],v[84:85],v[94:95] op_sel_hi:[1,0,1]\n" \
"v_cvt_pk_f16_f32 v40,v58,-v61\nv_cvt_pk_f16_f32 v44,v59,v60\nv_cvt_pk_f16_f32 v41,v64,-v67\nv_cvt_pk_f16_f32 v45,v65,v66\n" \
"v_cvt_pk_f16_f32 v216,v108,-v109\nv_cvt_pk_f16_f32 v220,v109,v108\nv_cvt_pk_f16_f32 v217,v110,-v111\nv_cvt_pk_f16_f32 v221,-v111,-v110\n" \
"v_cvt_pk_f16_f32 v146,v80,-v81\nv_cvt_pk_f16_f32 v150,v81,v80\nv_cvt_pk_f16_f32 v147,v82,-v83\nv_cvt_pk_f16_f32 v151,v83,v82\n" \
"v_pk_mul_f32 v[92:93],v[80:81],v[84:85] op_sel:[1,1] op_sel_hi:[0,1] neg_lo:[1,0]\nv_pk_mul_f32 v[94:95],v[82:83],v[84:85] op_sel:[1,1] op_sel_hi:[0,1] neg_lo:[1,0]\nbuffer_load_dword v233,s[8:11],s100 offen lds\nv_pk_mul_f32 v[58:59],v[56:57],v[52:53] op_sel_hi:[1,0]\n" \
"v_pk_mul_f32 v[60:61],v[56:57],v[54:55] op_sel_hi:[1,0]\nv_pk_mul_f32 v[64:65],v[62:63],v[52:53] op_sel_hi:[1,0]\nv_pk_mul_f32 v[66:67],v[62:63],v[54:55] op_sel_hi:[1,0]\nv_pk_mul_f32 v[108:109],v[106:107],v[102:103] op_sel_hi:[1,0]\n" \
"v_pk_mul_f32 v[110:111],v[106:107],v[104:105] op_sel_hi:[1,0]\nv_pk_fma_f32 v[80:81],v[80:81],v[84:85],v[92:93] op_sel_hi:[1,0,1]\nv_pk_fma_f32 v[82:83],v[82:83],v[84:85],v[94:95] op_sel_hi:[1,0,1]\nv_lshlrev_b32 v3,4,v119\n" \
"v_lshrrev_b32 v15,1,v1\nv_lshl_add_u32 v3,v118,6,v3\nv_cvt_pk_f16_f32 v42,v58,-v61\nv_cvt_pk_f16_f32 v46,v59,v60\n" \
"v_cvt_pk_f16_f32 v43,v64,-v67\nv_cvt_pk_f16_f32 v47,v65,v66\nv_cvt_pk_f16_f32 v218,v108,-v109\nv_cvt_pk_f16_f32 v222,v109,v108\n" \
"v_cvt_pk_f16_f32 v219,v110,-v111\nv_cvt_pk_f16_f32 v223,-v111,-v110\nv_cvt_pk_f16_f32 v152,v76,-v77\nv_cvt_pk_f16_f32 v156,v77,v76\n" \
"v_cvt_pk_f16_f32 v153,v78,-v79\nv_cvt_pk_f16_f32 v157,v79,v78\nv_cvt_pk_f16_f32 v154,v80,-v81\nv_cvt_pk_f16_f32 v158,v81,v80\n" \
"v_cvt_pk_f16_f32 v155,v82,-v83\nv_cvt_pk_f16_f32 v159,v83,v82\nv_add_u32 v15,s57,v15\nv_add_u32 v8,s57,v8\n" \
"v_add_u32 v9,s57,v9\nv_add_u32 v10,s57,v10\nv_add_u32 v11,s57,v11\nv_lshrrev_b32 v77,5,v0\n" \
"v_bfe_u32 v113,v0,1,5\nv_and_b32 v115,1,v0\ns_lshl_b32 s63,s3,1\nv_mul_u32_u24 v77,1160,v77\n" \
"v_and_b32 v76,31,v0\nv_and_b32 v114,6,v113\nv_add_u32 v115,s63,v115\nv_xor_b32 v114,v114,v115\n" \
"v_lshlrev_b32 v113,7,v113\nv_lshl_add_u32 v113,v114,4,v113\ns_add_u32 s64,s54,2916352\ns_add_u32 s65,s64,4096\n" \
"v_bfrev_b32 v114,1\ns_lshl1_add_u32 s95,s33,s3\ns_add_u32 s95,s95,74\ns_cmp_lt_u32 s3,2\n" \
"s_cselect_b32 s95,s95,256\ns_and_b32 s100,s95,31\ns_lshl_b32 s100,s100,3\ns_cmp_lt_u32 s95,256\n" \
"s_cselect_b32 s95,s95,0x40000\ns_lshr_b32 s95,s95,5\ns_add_u32 s95,s95,s100\ns_lshl_b32 s95,s95,6\n" \
"s_add_u32 s95,s95,s32\ns_mul_i32 s95,s95,1152\ns_mov_b32 m0,162176\ns_lshl_b32 s101,s33,7\n" \
"s_sub_u32 s101,s101,1280\nv_add_u32 v79,s101,v0\nv_mul_u32_u24 v78,52429,v79\nv_lshrrev_b32 v78,18,v78\n" \
"v_lshlrev_b32 v79,7,v79\ns_mov_b32 s101,6272\nv_mad_u32_u24 v79,v78,s101,v79\ns_mov_b32 s26,3981312\n" \
"s_mov_b32 s27,0x20000\nv_mad_u32_u24 v78,v76,36,v77\nv_cmp_gt_u32 vcc,72,v0\ns_lshl_b32 s67,s32,2\n" \
"s_add_u32 s66,s64,8192\nv_cndmask_b32 v114,v114,v2,vcc\ns_add_u32 s67,s67,s3\ns_lshl_b32 s67,s67,5\n" \
"s_add_u32 s67,s67,s33\ns_lshl_b32 s67,s67,11\ns_add_u32 s67,s67,5308416\ns_lshl2_add_u32 s69,s3,161664\n" \
"s_waitcnt vmcnt(20)\nv_readfirstlane_b32 s28,v126\nv_readfirstlane_b32 s29,v127\ns_mul_i32 s72,s3,2320\n" \
"v_mov_b32 v251,s69\nv_cmp_gt_u32 vcc,32,v0\ns_add_u32 s28,s28,1\ns_addc_u32 s29,s29,0\n" \
"s_lshl_b32 s7,s32,11\nv_cndmask_b32 v74,2.0,0,vcc\ns_mov_b32 s93,128\ns_mov_b32 s73,8192\n" \
"s_mov_b32 s74,12288\ns_mov_b32 s75,17408\ns_mov_b32 s76,21504\ns_mov_b32 s77,16384\n" \
"s_mov_b32 s78,25600\ns_add_u32 s7,s7,40960\ns_mov_b32 s81,0x3d800000\ns_mov_b32 s42,18874368\n" \
"s_mov_b32 s43,0x20000\ns_waitcnt lgkmcnt(0)\ns_mov_b64 s[40:41],s[44:45]\ns_mov_b64 s[44:45],s[46:47]\n" \
"s_mov_b32 s46,2359296\ns_mov_b32 s47,0x20000\ns_waitcnt vmcnt(19)\nv_lshlrev_b32_e64 v48,16,v224\n" \
"v_and_b32 v49,0xffff0000,v224\nv_readlane_b32 s70,v48,0\nv_readlane_b32 s71,v48,32\nv_and_b32 v51,0xffff0000,v225\n" \
"v_mov_b32 v72,s70\nv_mov_b32 v73,s71\nv_lshlrev_b32 v50,16,v225\nv_and_b32 v53,0xffff0000,v226\n" \
"v_permlane32_swap_b32 v72,v73\nv_pk_add_f32 v[48:49],v[48:49],v[72:73] op_sel_hi:[1,0] neg_lo:[0,1] neg_hi:[0,1]\nv_pk_add_f32 v[50:51],v[50:51],v[72:73] op_sel_hi:[1,0] neg_lo:[0,1] neg_hi:[0,1]\nv_pk_mul_f32 v[68:69],v[48:49],v[48:49]\n" \
"v_lshlrev_b32_e64 v52,16,v226\nv_and_b32 v55,0xffff0000,v227\nv_pk_add_f32 v[52:53],v[52:53],v[72:73] op_sel_hi:[1,0] neg_lo:[0,1] neg_hi:[0,1]\nv_pk_add_f32 v[66:67],v[48:49],v[50:51]\n" \
"v_pk_fma_f32 v[68:69],v[50:51],v[50:51],v[68:69]\nv_lshlrev_b32 v54,16,v227\ns_waitcnt vmcnt(18)\nv_and_b32 v57,0xffff0000,v228\n" \
"v_pk_add_f32 v[54:55],v[54:55],v[72:73] op_sel_hi:[1,0] neg_lo:[0,1] neg_hi:[0,1]\nv_pk_add_f32 v[66:67],v[66:67],v[52:53]\nv_pk_fma_f32 v[68:69],v[52:53],v[52:53],v[68:69]\nv_lshlrev_b32_e64 v56,16,v228\n" \
"v_and_b32 v59,0xffff0000,v229\nv_pk_add_f32 v[56:57],v[56:57],v[72:73] op_sel_hi:[1,0] neg_lo:[0,1] neg_hi:[0,1]\nv_pk_add_f32 v[66:67],v[66:67],v[54:55]\nv_pk_fma_f32 v[68:69],v[54:55],v[54:55],v[68:69]\n" \
"v_lshlrev_b32_e64 v58,16,v229\nv_and_b32 v61,0xffff0000,v230\nv_pk_add_f32 v[58:59],v[58:59],v[72:73] op_sel_hi:[1,0] neg_lo:[0,1] neg_hi:[0,1]\nv_pk_add_f32 v[66:67],v[66:67],v[56:57]\n" \
"v_pk_fma_f32 v[68:69],v[56:57],v[56:57],v[68:69]\nv_lshlrev_b32_e64 v60,16,v230\nv_and_b32 v63,0xffff0000,v231\nv_pk_add_f32 v[60:61],v[60:61],v[72:73] op_sel_hi:[1,0] neg_lo:[0,1] neg_hi:[0,1]\n" \
"v_pk_add_f32 v[66:67],v[66:67],v[58:59]\nv_pk_fma_f32 v[68:69],v[58:59],v[58:59],v[68:69]\nv_lshlrev_b32 v62,16,v231\ns_waitcnt vmcnt(17)\n" \
"v_and_b32 v65,0xffff0000,v232\nv_pk_add_f32 v[62:63],v[62:63],v[72:73] op_sel_hi:[1,0] neg_lo:[0,1] neg_hi:[0,1]\nv_pk_add_f32 v[66:67],v[66:67],v[60:61]\nv_pk_fma_f32 v[68:69],v[60:61],v[60:61],v[68:69]\n" \
"v_lshlrev_b32_e64 v64,16,v232\nv_pk_add_f32 v[64:65],v[64:65],v[72:73] op_sel_hi:[1,0] neg_lo:[0,1] neg_hi:[0,1]\nv_pk_add_f32 v[66:67],v[66:67],v[62:63]\nv_pk_fma_f32 v[68:69],v[62:63],v[62:63],v[68:69]\n" \
"v_pk_add_f32 v[66:67],v[66:67],v[64:65]\nv_pk_fma_f32 v[68:69],v[64:65],v[64:65],v[68:69]\nv_add_f32 v66,v66,v67\nv_add_f32 v68,v68,v69\n" \
"s_nop 1\nv_permlane16_swap_b32 v66,v68\nv_add_f32 v66,v66,v68\ns_nop 1\n" \
"v_add_f32_dpp v66,v66,v66 quad_perm:[1,0,3,2] row_mask:0xf bank_mask:0xf bound_ctrl:1\ns_nop 1\nv_add_f32_dpp v66,v66,v66 quad_perm:[2,3,0,1] row_mask:0xf bank_mask:0xf bound_ctrl:1\ns_nop 1\n" \
"v_add_f32_dpp v66,v66,v66 row_half_mirror row_mask:0xf bank_mask:0xf bound_ctrl:1\ns_nop 1\nv_add_f32_dpp v66,v66,v66 row_mirror row_mask:0xf bank_mask:0xf bound_ctrl:1\nv_mul_f32 v70,0x3ae38e39,v66\n" \
"v_mul_f32 v68,0x3ae38e39,v66\ns_nop 1\nv_permlane16_swap_b32 v70,v68\nv_fma_f32 v68,-v70,v70,v68\n" \
"v_max_f32 v68,0,v68\nv_add_f32 v68,0x3727c5ac,v68\nv_rsq_f32 v66,v68\nv_add_f32 v250,v70,v72\n" \
"v_mul_f32 v67,-v70,v66\nv_pk_fma_f32 v[48:49],v[48:49],v[66:67],v[66:67] op_sel:[0,0,1] op_sel_hi:[1,0,1]\nv_pk_fma_f32 v[50:51],v[50:51],v[66:67],v[66:67] op_sel:[0,0,1] op_sel_hi:[1,0,1]\nv_cvt_pk_f16_f32 v80,v48,v49\n" \
"v_cvt_pk_f16_f32 v81,v50,v51\nv_pk_fma_f32 v[52:53],v[52:53],v[66:67],v[66:67] op_sel:[0,0,1] op_sel_hi:[1,0,1]\nv_pk_fma_f32 v[54:55],v[54:55],v[66:67],v[66:67] op_sel:[0,0,1] op_sel_hi:[1,0,1]\nds_write2_b32 v78,v80,v81 offset0:0 offset1:1\n" \
"v_cvt_pk_f16_f32 v82,v52,v53\nv_cvt_pk_f16_f32 v83,v54,v55\nv_pk_fma_f32 v[56:57],v[56:57],v[66:67],v[66:67] op_sel:[0,0,1] op_sel_hi:[1,0,1]\nv_pk_fma_f32 v[58:59],v[58:59],v[66:67],v[66:67] op_sel:[0,0,1] op_sel_hi:[1,0,1]\n" \
"ds_write2_b32 v78,v82,v83 offset0:2 offset1:3\nv_cvt_pk_f16_f32 v84,v56,v57\nv_cvt_pk_f16_f32 v85,v58,v59\nv_pk_fma_f32 v[60:61],v[60:61],v[66:67],v[66:67] op_sel:[0,0,1] op_sel_hi:[1,0,1]\n" \
"v_pk_fma_f32 v[62:63],v[62:63],v[66:67],v[66:67] op_sel:[0,0,1] op_sel_hi:[1,0,1]\nds_write2_b32 v78,v84,v85 offset0:4 offset1:5\nv_cvt_pk_f16_f32 v86,v60,v61\nv_cvt_pk_f16_f32 v87,v62,v63\n" \
"v_pk_fma_f32 v[64:65],v[64:65],v[66:67],v[66:67] op_sel:[0,0,1] op_sel_hi:[1,0,1]\nds_write2_b32 v78,v86,v87 offset0:6 offset1:7\nv_cvt_pk_f16_f32 v88,v64,v65\nds_write_b32 v78,v88 offset:32\n" \
"s_waitcnt vmcnt(11)\ns_barrier\nds_read_u16 v16,v246\nds_read_u16 v17,v246 offset:5216\n" \
"ds_read_u16 v18,v246 offset:578\nds_read_u16 v19,v246 offset:5794\nds_read_u16 v20,v246 offset:1156\nds_read_u16 v21,v246 offset:6372\n" \
"ds_read_u16 v22,v246 offset:1734\nds_read_u16 v23,v246 offset:6950\ns_waitcnt vmcnt(8)\nv_lshrrev_b32 v96,1,v234\n" \
"ds_read_u16 v24,v247\nv_lshrrev_b32 v97,1,v235\nv_lshrrev_b32 v98,1,v236\ns_waitcnt lgkmcnt(7)\n" \
"v_lshl_or_b32 v16,v17,16,v16\nv_lshrrev_b32 v99,1,v237\nds_read_u16 v25,v247 offset:5216\ns_waitcnt vmcnt(7)\n" \
"v_lshrrev_b32 v100,1,v238\nv_lshrrev_b32 v101,1,v239\nv_lshrrev_b32 v102,1,v240\nv_lshrrev_b32 v103,1,v241\n" \
"ds_read_u16 v26,v247 offset:578\nv_add_u32 v96,s72,v96\nv_add_u32 v97,s72,v97\ns_waitcnt lgkmcnt(7)\n" \
"v_lshl_or_b32 v17,v19,16,v18\nv_add_u32 v98,s72,v98\nds_read_u16 v27,v247 offset:5794\nv_add_u32 v99,s72,v99\n" \
"v_add_u32 v100,s72,v100\nv_add_u32 v101,s72,v101\nv_add_u32 v102,s72,v102\nds_read_u16 v28,v247 offset:1156\n" \
"v_add_u32 v103,s72,v103\nv_pk_maximum3_f16 v124,v16,v17,v16\ns_waitcnt lgkmcnt(7)\nv_lshl_or_b32 v18,v21,16,v20\n" \
"v_pk_minimum3_f16 v125,v16,v17,v16\nds_read_u16 v29,v247 offset:6372\nds_read_u16 v30,v247 offset:1734\ns_waitcnt lgkmcnt(7)\n" \
"v_lshl_or_b32 v19,v23,16,v22\nbuffer_store_dwordx4 v[16:19],v2,s[36:39],s64 offen sc1\nds_read_u16 v31,v247 offset:6950\nds_read_u16 v104,v248\n" \
"v_pk_maximum3_f16 v124,v18,v19,v124\nv_pk_minimum3_f16 v125,v18,v19,v125\ns_waitcnt lgkmcnt(7)\nv_lshl_or_b32 v24,v25,16,v24\n" \
"ds_read_u16 v105,v248 offset:5216\nds_read_u16 v106,v248 offset:578\ns_waitcnt lgkmcnt(7)\nv_lshl_or_b32 v25,v27,16,v26\n" \
"v_pk_maximum3_f16 v124,v24,v25,v124\nds_read_u16 v107,v248 offset:5794\nv_pk_minimum3_f16 v125,v24,v25,v125\nds_read_u16 v108,v248 offset:1156\n" \
"s_waitcnt lgkmcnt(7)\nv_lshl_or_b32 v26,v29,16,v28\nds_read_u16 v109,v248 offset:6372\nds_read_u16 v110,v248 offset:1734\n" \
"s_waitcnt lgkmcnt(7)\nv_lshl_or_b32 v27,v31,16,v30\nbuffer_store_dwordx4 v[24:27],v2,s[36:39],s65 offen sc1\nds_read_u16 v111,v248 offset:6950\n" \
"ds_read_b128 v[116:119],v113 offset:20224\nv_pk_maximum3_f16 v124,v26,v27,v124\nv_pk_minimum3_f16 v125,v26,v27,v125\nds_read_b128 v[120:123],v113 offset:24320\n" \
"s_waitcnt lgkmcnt(8)\nv_lshl_or_b32 v104,v105,16,v104\nds_read_b32 v16,v96\nds_read_b32 v24,v96 offset:1160\n" \
"ds_read_b32 v17,v97\nds_read_b32 v25,v97 offset:1160\ns_waitcnt lgkmcnt(10)\nv_lshl_or_b32 v105,v107,16,v106\n" \
"ds_read_b32 v18,v98\nv_pk_maximum3_f16 v124,v104,v105,v124\nds_read_b32 v26,v98 offset:1160\nv_pk_minimum3_f16 v125,v104,v105,v125\n" \
"ds_read_b32 v19,v99\nds_read_b32 v27,v99 offset:1160\nds_read_b32 v20,v100\ns_waitcnt lgkmcnt(13)\n" \
"v_lshl_or_b32 v106,v109,16,v108\nds_read_b32 v28,v100 offset:1160\nds_read_b32 v21,v101\nds_read_b32 v29,v101 offset:1160\n" \
"s_waitcnt lgkmcnt(14)\nv_lshl_or_b32 v107,v111,16,v110\nbuffer_store_dwordx4 v[104:107],v114,s[36:39],s66 offen sc1\ns_waitcnt lgkmcnt(13)\n" \
"buffer_store_dwordx4 v[116:119],v1,s[36:39],s67 offen sc1\ns_waitcnt lgkmcnt(12)\nbuffer_store_dwordx4 v[120:123],v1,s[36:39],s67 offen offset:1024 sc1\nds_read_b32 v22,v102\n" \
"ds_read_b32 v30,v102 offset:1160\nds_read_b32 v23,v103\nds_read_b32 v31,v103 offset:1160\nv_pk_maximum3_f16 v124,v106,v107,v124\n" \
"buffer_load_dword v233,s[8:11],s95 offen lds\nv_and_b32 v124,0x7fff7fff,v124\nv_pk_minimum3_f16 v125,v106,v107,v125\nv_and_b32 v125,0x7fff7fff,v125\n" \
"v_pk_max_u16 v124,v124,v125\nv_lshlrev_b32 v125,16,v124\nv_max_u32 v124,v124,v125\ns_mov_b32 m0,162176\n" \
"s_nop 0\nv_max_u32_dpp v124,v124,v124 quad_perm:[1,0,3,2] row_mask:0xf bank_mask:0xf bound_ctrl:1\ns_nop 1\nv_max_u32_dpp v124,v124,v124 quad_perm:[2,3,0,1] row_mask:0xf bank_mask:0xf bound_ctrl:1\n" \
"s_nop 1\nv_max_u32_dpp v124,v124,v124 row_half_mirror row_mask:0xf bank_mask:0xf bound_ctrl:1\ns_nop 1\nv_max_u32_dpp v124,v124,v124 row_mirror row_mask:0xf bank_mask:0xf bound_ctrl:1\n" \
"s_nop 0\nv_readlane_b32 s68,v124,0\nv_readlane_b32 s31,v124,16\nv_readlane_b32 s96,v124,32\n" \
"v_readlane_b32 s97,v124,48\ns_max_u32 s31,s31,s96\ns_max_u32 s68,s68,s97\ns_max_u32 s68,s68,s31\n" \
"s_mul_i32 s31,s32,576\ns_add_u32 s31,s31,2304\ns_andn2_b32 s31,s31,127\ns_cmp_gt_u32 s3,1\n" \
"s_cselect_b32 s31,s31,2147483648\nbuffer_load_dword v79,s[24:27],s31 offen lds\nv_mov_b32 v124,s68\nds_write_b32 v251,v124 offset:32\n" \
"v_mov_b32 v114,161664\nv_and_b32 v80,15,v0\nv_bfe_u32 v81,v0,4,2\nv_cvt_f32_u32 v80,v80\n" \
"v_cvt_f32_u32 v82,s3\nv_fma_f32 v80,v80,4.0,v82\nv_cvt_f32_u32 v81,v81\nbuffer_load_dwordx4 v[48:51],v2,s[36:39],s73 offen sc1\n" \
"v_mul_f32 v82,0xbc800000,v81\nv_cos_f32 v92,v82\nv_sin_f32 v93,v82\nv_mul_f32 v80,0xbc800000,v80\n" \
"v_mul_f32 v81,4.0,v81\nbuffer_load_dwordx4 v[60:63],v2,s[36:39],s75 offen sc1\ns_mov_b32 s70,0x3f7fb10f\ns_mov_b32 s71,0xbd48fb30\n" \
"v_mul_f32 v81,v81,v80\nv_pk_mul_f32 v[100:101],v[92:93],s[70:71] op_sel:[1,1] op_sel_hi:[0,1] neg_lo:[1,0]\nv_fmamk_f32 v87,v80,0x40000000,v81\nv_pk_fma_f32 v[94:95],v[92:93],s[70:71],v[100:101] op_sel_hi:[1,0,1]\n" \
"v_cos_f32_e64 v86,v87\ns_mov_b32 s70,0x3f7fec43\ns_mov_b32 s71,0xbcc90ab0\nv_fmamk_f32 v89,v80,0x3f800000,v81\n" \
"v_fmamk_f32 v91,v80,0x40400000,v81\nv_pk_mul_f32 v[100:101],v[92:93],s[70:71] op_sel:[1,1] op_sel_hi:[0,1] neg_lo:[1,0]\nbuffer_load_dwordx4 v[52:55],v2,s[36:39],s74 offen sc1\nv_cos_f32 v88,v89\n" \
"v_cos_f32 v90,v91\nv_pk_fma_f32 v[96:97],v[92:93],s[70:71],v[100:101] op_sel_hi:[1,0,1]\nv_cos_f32 v84,v81\nv_sin_f32 v85,v81\n" \
"v_sin_f32 v87,v87\ns_mov_b32 s70,0x3f7f4e6d\ns_mov_b32 s71,0xbd96a905\nv_pk_mul_f32 v[100:101],v[92:93],s[70:71] op_sel:[1,1] op_sel_hi:[0,1] neg_lo:[1,0]\n" \
"v_sin_f32 v89,v89\nv_sin_f32_e64 v91,v91\nv_pk_fma_f32 v[98:99],v[92:93],s[70:71],v[100:101] op_sel_hi:[1,0,1]\nbuffer_load_dwordx4 v[64:67],v2,s[36:39],s76 offen sc1\n" \
"v_cvt_pk_f16_f32 v160,v84,-v85\nv_cvt_pk_f16_f32 v164,v85,v84\nv_cvt_pk_f16_f32 v161,v86,-v87\nv_cvt_pk_f16_f32 v165,v87,v86\n" \
"v_pk_mul_f32 v[100:101],v[84:85],v[92:93] op_sel:[1,1] op_sel_hi:[0,1] neg_lo:[1,0]\nv_pk_mul_f32 v[102:103],v[86:87],v[94:95] op_sel:[1,1] op_sel_hi:[0,1] neg_lo:[1,0]\nv_pk_fma_f32 v[84:85],v[84:85],v[92:93],v[100:101] op_sel_hi:[1,0,1]\nv_pk_fma_f32 v[86:87],v[86:87],v[94:95],v[102:103] op_sel_hi:[1,0,1]\n" \
"v_cvt_pk_f16_f32 v162,v88,-v89\nv_cvt_pk_f16_f32 v166,v89,v88\nv_cvt_pk_f16_f32 v163,v90,-v91\nv_cvt_pk_f16_f32 v167,v91,v90\n" \
"v_pk_mul_f32 v[100:101],v[88:89],v[96:97] op_sel:[1,1] op_sel_hi:[0,1] neg_lo:[1,0]\nv_pk_mul_f32 v[102:103],v[90:91],v[98:99] op_sel:[1,1] op_sel_hi:[0,1] neg_lo:[1,0]\nv_pk_fma_f32 v[88:89],v[88:89],v[96:97],v[100:101] op_sel_hi:[1,0,1]\nv_pk_fma_f32 v[90:91],v[90:91],v[98:99],v[102:103] op_sel_hi:[1,0,1]\n" \
"buffer_load_dwordx4 v[56:59],v1,s[36:39],s77 offen sc1\nv_cvt_pk_f16_f32 v168,v84,-v85\nv_cvt_pk_f16_f32 v172,v85,v84\nv_cvt_pk_f16_f32 v169,v86,-v87\n" \
"v_cvt_pk_f16_f32 v173,v87,v86\nv_pk_mul_f32 v[100:101],v[84:85],v[92:93] op_sel:[1,1] op_sel_hi:[0,1] neg_lo:[1,0]\nv_pk_mul_f32 v[102:103],v[86:87],v[94:95] op_sel:[1,1] op_sel_hi:[0,1] neg_lo:[1,0]\nv_pk_fma_f32 v[84:85],v[84:85],v[92:93],v[100:101] op_sel_hi:[1,0,1]\n" \
"s_waitcnt vmcnt(7) lgkmcnt(0)\ns_barrier\nds_read_b128 v[116:119],v114 offset:32\nv_pk_fma_f32 v[86:87],v[86:87],v[94:95],v[102:103] op_sel_hi:[1,0,1]\n" \
"v_cvt_pk_f16_f32 v170,v88,-v89\nv_cvt_pk_f16_f32 v174,v89,v88\nv_cvt_pk_f16_f32 v171,v90,-v91\nv_cvt_pk_f16_f32 v175,v91,v90\n" \
"v_pk_mul_f32 v[100:101],v[88:89],v[96:97] op_sel:[1,1] op_sel_hi:[0,1] neg_lo:[1,0]\nv_pk_mul_f32 v[102:103],v[90:91],v[98:99] op_sel:[1,1] op_sel_hi:[0,1] neg_lo:[1,0]\nv_pk_fma_f32 v[88:89],v[88:89],v[96:97],v[100:101] op_sel_hi:[1,0,1]\nv_pk_fma_f32 v[90:91],v[90:91],v[98:99],v[102:103] op_sel_hi:[1,0,1]\n" \
"buffer_load_dwordx4 v[68:71],v1,s[36:39],s78 offen sc1\nv_cvt_pk_f16_f32 v176,v84,-v85\nv_cvt_pk_f16_f32 v180,v85,v84\nv_cvt_pk_f16_f32 v177,v86,-v87\n" \
"v_cvt_pk_f16_f32 v181,v87,v86\nv_pk_mul_f32 v[100:101],v[84:85],v[92:93] op_sel:[1,1] op_sel_hi:[0,1] neg_lo:[1,0]\nv_pk_mul_f32 v[102:103],v[86:87],v[94:95] op_sel:[1,1] op_sel_hi:[0,1] neg_lo:[1,0]\ns_mov_b32 s79,0xffffff\n" \
"s_lshl3_add_u32 s80,s2,4294615040\nv_pk_fma_f32 v[84:85],v[84:85],v[92:93],v[100:101] op_sel_hi:[1,0,1]\nv_pk_fma_f32 v[86:87],v[86:87],v[94:95],v[102:103] op_sel_hi:[1,0,1]\ns_waitcnt lgkmcnt(0)\n" \
"v_max3_u32 v116,v116,v117,v118\nv_max_u32 v116,v116,v119\nv_and_b32 v119,0xf8,v0\nv_cmp_ne_u32 vcc,192,v119\n" \
"v_lshlrev_b32 v116,1,v116\nv_mov_b32 v117,s29\nv_lshlrev_b32 v118,11,v0\nv_bfi_b32 v117,s79,v117,v116\n" \
"v_add_u32 v118,s80,v118\nv_bfrev_b32_e64 v119,1\nv_cvt_pk_f16_f32 v178,v88,-v89\nv_cvt_pk_f16_f32 v182,v89,v88\n" \
"v_cvt_pk_f16_f32 v179,v90,-v91\nv_cvt_pk_f16_f32 v183,v91,v90\nv_pk_mul_f32 v[100:101],v[88:89],v[96:97] op_sel:[1,1] op_sel_hi:[0,1] neg_lo:[1,0]\nv_pk_mul_f32 v[102:103],v[90:91],v[98:99] op_sel:[1,1] op_sel_hi:[0,1] neg_lo:[1,0]\n" \
"v_cndmask_b32 v118,v118,v119,vcc\nv_mov_b32 v116,s28\nv_pk_fma_f32 v[88:89],v[88:89],v[96:97],v[100:101] op_sel_hi:[1,0,1]\nv_pk_fma_f32 v[90:91],v[90:91],v[98:99],v[102:103] op_sel_hi:[1,0,1]\n" \
"buffer_store_dwordx2 v[116:117],v118,s[36:39],0 offen sc1\nv_add_u32 v112,28416,v1\nv_cvt_pk_f16_f32 v184,v84,-v85\nv_cvt_pk_f16_f32 v188,v85,v84\n" \
"v_cvt_pk_f16_f32 v185,v86,-v87\nv_cvt_pk_f16_f32 v189,v87,v86\nv_cvt_pk_f16_f32 v186,v88,-v89\nv_cvt_pk_f16_f32 v190,v89,v88\n" \
"v_cvt_pk_f16_f32 v187,v90,-v91\nv_cvt_pk_f16_f32 v191,v91,v90\nds_read_b128 v[192:195],v112\nds_read_b128 v[196:199],v112 offset:1024\n" \
"ds_read_b128 v[200:203],v112 offset:2048\nds_read_b128 v[204:207],v112 offset:3072\nds_read_b128 v[208:211],v112 offset:4096\nds_read_b128 v[212:215],v112 offset:5120\n" \
"ds_read_b128 v[32:35],v112 offset:6144\nds_read_b128 v[36:39],v112 offset:7168\ns_waitcnt vmcnt(6)\nv_cmp_ne_u64 vcc,s[28:29],v[48:49]\n" \
"s_waitcnt vmcnt(4)\nv_cmp_ne_u64 s[84:85],s[28:29],v[52:53]\ns_waitcnt vmcnt(2)\nv_cmp_ne_u64 s[82:83],s[28:29],v[56:57]\n" \
"s_or_b64 vcc,vcc,s[84:85]\nv_cmp_ne_u64 s[84:85],s[28:29],v[60:61]\ns_or_b64 vcc,vcc,s[82:83]\nv_cmp_ne_u64 s[82:83],s[28:29],v[64:65]\n" \
"s_or_b64 vcc,vcc,s[84:85]\ns_waitcnt vmcnt(1)\nv_cmp_ne_u64 s[84:85],s[28:29],v[68:69]\ns_or_b64 vcc,vcc,s[82:83]\n" \
"s_or_b64 vcc,vcc,s[84:85]\ns_cbranch_vccnz .LsmC_%=\n.LbmC_%=:\nv_pk_add_f32 v[50:51],v[50:51],v[62:63]\n" \
"v_pk_add_f32 v[54:55],v[54:55],v[66:67]\nv_pk_mul_f32 v[62:63],v[50:51],2.0 op_sel_hi:[1,0]\nv_pk_add_f32 v[58:59],v[58:59],v[70:71]\nv_max3_f32 v112,|v62|,|v63|,0\n" \
"v_pk_fma_f32 v[66:67],v[54:55],2.0,v[74:75] op_sel_hi:[1,0,0]\nv_max3_f32 v112,|v66|,|v67|,v112\nv_pk_fma_f32 v[70:71],v[58:59],2.0,2.0 op_sel_hi:[1,0,0]\nv_max3_f32 v112,|v70|,|v71|,v112\n" \
"v_cmp_lt_f32 s[82:83],s81,v112\nv_cmp_ngt_f32 vcc,0x41000000,v112\ns_cmp_lg_u64 s[82:83],0\ns_cselect_b32 s82,2,0\n" \
"v_cvt_pk_f16_f32 v62,v62,v63\ns_cmp_lg_u64 vcc,0\ns_addc_u32 s82,s82,0\nds_write_b32 v252,v62 offset:62848\n" \
"v_cvt_pk_f16_f32 v66,v66,v67\nds_write_b32 v253,v66 offset:62848\nv_cvt_pk_f16_f32 v70,v70,v71\nv_mov_b32 v113,s82\n" \
"ds_write_b32 v254,v70 offset:62848\nds_write_b32 v251,v113\ns_waitcnt lgkmcnt(0)\ns_barrier\n" \
"ds_read_b128 v[124:127],v114\nds_read_b64 v[96:97],v234 offset:62848\nds_read_b64 v[98:99],v235 offset:62848\nds_read_b64 v[100:101],v236 offset:62848\n" \
"ds_read_b64 v[102:103],v237 offset:62848\nds_read_b64 v[104:105],v238 offset:62848\nds_read_b64 v[106:107],v239 offset:62848\nds_read_b64 v[108:109],v240 offset:62848\n" \
"ds_read_b64 v[110:111],v241 offset:62848\ns_waitcnt lgkmcnt(8)\nv_or3_b32 v124,v124,v125,v126\nv_or_b32 v124,v124,v127\n" \
"v_cmp_ne_u32 vcc,2,v124\ns_nop 0\ns_cbranch_vccnz .LsxC_%=\n.LbxC_%=:\n" \
"s_waitcnt lgkmcnt(7)\nv_pk_fma_f16 v80,v16,v96,v97\ns_waitcnt lgkmcnt(6)\nv_pk_fma_f16 v81,v17,v98,v99\n" \
"s_waitcnt lgkmcnt(5)\nv_pk_fma_f16 v82,v18,v100,v101\ns_waitcnt lgkmcnt(4)\nv_pk_fma_f16 v83,v19,v102,v103\n" \
"v_pk_fma_f16 v88,v24,v96,v97\ns_waitcnt lgkmcnt(3)\nv_pk_fma_f16 v84,v20,v104,v105\ns_waitcnt lgkmcnt(2)\n" \
"v_pk_fma_f16 v85,v21,v106,v107\nv_pk_fma_f16 v89,v25,v98,v99\ns_waitcnt lgkmcnt(1)\nv_pk_fma_f16 v86,v22,v108,v109\n" \
"v_pk_fma_f16 v90,v26,v100,v101\ns_waitcnt lgkmcnt(0)\nv_pk_fma_f16 v87,v23,v110,v111\nv_pk_fma_f16 v91,v27,v102,v103\n" \
"v_pk_fma_f16 v92,v28,v104,v105\nv_pk_fma_f16 v93,v29,v106,v107\nv_pk_fma_f16 v94,v30,v108,v109\ns_add_u32 s96,s92,0\n" \
"s_add_u32 m0,s57,0\ns_add_u32 s97,s92,9437184\nbuffer_load_dwordx4 v4,s[8:11],s96 offen lds\nv_pk_fma_f16 v95,v31,v110,v111\n" \
"v_mfma_f32_16x16x32_f16 v[48:51],v[80:83],v[192:195],0\nv_and_b32 v112,15,v0\nv_bfrev_b32 v113,1\nv_mfma_f32_16x16x32_f16 v[52:55],v[80:83],v[196:199],0\n" \
"v_cmp_eq_u32 vcc,0,v112\ns_add_u32 s73,s35,s88\nbuffer_load_dwordx4 v5,s[8:11],s96 offen offset:1024 lds\nv_mfma_f32_16x16x32_f16 v[56:59],v[84:87],v[32:35],0\n" \
"v_cndmask_b32 v113,v113,v3,vcc\nv_cmp_gt_u32 vcc,64,v0\nv_mfma_f32_16x16x32_f16 v[60:63],v[84:87],v[36:39],0\ns_add_u32 s75,s73,8192\n" \
"v_cndmask_b32 v113,v113,v3,vcc\nv_mfma_f32_16x16x32_f16 v[64:67],v[88:91],v[192:195],0\nbuffer_load_dwordx4 v6,s[8:11],s96 offen offset:2048 lds\nv_mfma_f32_16x16x32_f16 v[68:71],v[88:91],v[196:199],0\n" \
"v_pk_add_f32 v[56:57],v[48:49],v[56:57] neg_lo:[0,1] neg_hi:[0,1]\nv_sub_f32 v58,v50,v58\nv_mfma_f32_16x16x32_f16 v[72:75],v[92:95],v[32:35],0\nv_pk_add_f32 v[60:61],v[52:53],v[60:61] neg_lo:[0,1] neg_hi:[0,1]\n" \
"v_sub_f32 v62,v54,v62\nv_mfma_f32_16x16x32_f16 v[76:79],v[92:95],v[36:39],0\nbuffer_load_dwordx4 v7,s[8:11],s96 offen offset:3072 lds\nv_pk_fma_f32 v[48:49],v[48:49],2.0,v[56:57] op_sel_hi:[1,0,1] neg_lo:[0,0,1] neg_hi:[0,0,1]\n" \
"v_fma_f32 v50,v50,2.0,-v58\nv_pk_fma_f32 v[52:53],v[52:53],2.0,v[60:61] op_sel_hi:[1,0,1] neg_lo:[0,0,1] neg_hi:[0,0,1]\nv_fma_f32 v54,v54,2.0,-v62\nv_pk_add_f32 v[72:73],v[64:65],v[72:73] neg_lo:[0,1] neg_hi:[0,1]\n" \
"v_sub_f32_e64 v74,v66,v74\nv_pk_fma_f32 v[64:65],v[64:65],2.0,v[72:73] op_sel_hi:[1,0,1] neg_lo:[0,0,1] neg_hi:[0,0,1]\nv_pk_add_f32 v[76:77],v[68:69],v[76:77] neg_lo:[0,1] neg_hi:[0,1]\ns_add_u32 m0,s57,4096\n" \
"s_add_u32 s95,s92,73728\nbuffer_load_dwordx4 v4,s[8:11],s97 offen lds\nv_sub_f32_e64 v78,v70,v78\nv_fma_f32 v66,v66,2.0,-v74\n" \
"v_pk_fma_f32 v[68:69],v[68:69],2.0,v[76:77] op_sel_hi:[1,0,1] neg_lo:[0,0,1] neg_hi:[0,0,1]\nv_fma_f32 v70,v70,2.0,-v78\nv_cvt_pk_f16_f32 v16,v48,v52\nv_cvt_pk_f16_f32 v17,v49,v53\n" \
"v_cvt_pk_f16_f32 v18,v50,v54\nv_cvt_pk_f16_f32 v24,v64,v68\nbuffer_load_dwordx4 v5,s[8:11],s97 offen offset:1024 lds\nv_cvt_pk_f16_f32 v25,v65,v69\n" \
"v_cvt_pk_f16_f32 v26,v66,v70\nv_mfma_f32_16x16x32_f16 v[48:51],v[200:203],v[16:19],0\nv_cvt_pk_f16_f32 v20,v56,v60\nv_cvt_pk_f16_f32 v21,v57,v61\n" \
"v_mfma_f32_16x16x32_f16 v[52:55],v[204:207],v[16:19],0\nbuffer_load_dwordx4 v6,s[8:11],s97 offen offset:2048 lds\nv_mfma_f32_16x16x32_f16 v[64:67],v[200:203],v[24:27],0\nv_cvt_pk_f16_f32 v22,v58,v62\n" \
"v_cvt_pk_f16_f32 v28,v72,v76\nv_mfma_f32_16x16x32_f16 v[68:71],v[204:207],v[24:27],0\nv_cvt_pk_f16_f32 v29,v73,v77\nv_cvt_pk_f16_f32 v30,v74,v78\n" \
"buffer_load_dwordx4 v7,s[8:11],s97 offen offset:3072 lds\nv_mfma_f32_16x16x32_f16 v[56:59],v[200:203],v[20:23],0\nv_cvt_pk_f16_f32 v80,v48,v52\nv_cvt_pk_f16_f32 v82,v49,v53\n" \
"v_mfma_f32_16x16x32_f16 v[60:63],v[204:207],v[20:23],0\nv_cvt_pk_f16_f32 v84,v50,v54\nv_cvt_pk_f16_f32 v81,v64,v68\ns_add_u32 m0,s57,8192\n" \
"s_add_u32 s100,s92,9510912\nbuffer_load_dwordx4 v4,s[8:11],s95 offen lds\nv_mfma_f32_16x16x32_f16 v[72:75],v[200:203],v[28:31],0\nds_write_b64 v255,v[80:81]\n" \
"v_mfma_f32_16x16x32_f16 v[76:79],v[204:207],v[28:31],0\nv_cvt_pk_f16_f32 v83,v65,v69\nds_write_b64 v255,v[82:83] offset:512\nbuffer_load_dwordx4 v5,s[8:11],s95 offen offset:1024 lds\n" \
"v_cvt_pk_f16_f32 v85,v66,v70\nds_write_b64 v255,v[84:85] offset:1024\nv_cvt_pk_f16_f32 v86,v56,v60\nv_cvt_pk_f16_f32 v88,v57,v61\n" \
"v_cvt_pk_f16_f32 v90,v58,v62\nv_cvt_pk_f16_f32 v87,v72,v76\nbuffer_load_dwordx4 v6,s[8:11],s95 offen offset:2048 lds\nds_write_b64 v255,v[86:87] offset:256\n" \
"v_cvt_pk_f16_f32 v89,v73,v77\nds_write_b64 v255,v[88:89] offset:768\nv_cvt_pk_f16_f32 v91,v74,v78\nds_write_b64 v255,v[90:91] offset:1280\n" \
"buffer_load_dwordx4 v7,s[8:11],s95 offen offset:3072 lds\ns_cmp_lt_u32 s3,2\ns_cselect_b32 s75,s75,2147483648\ns_add_u32 s74,s73,4096\n" \
"s_add_u32 m0,s57,12288\ns_add_u32 s101,s92,147456\ns_mul_i32 s76,s32,18\ns_add_u32 s76,s76,s33\n" \
"s_sub_u32 s76,s76,14\ns_cmp_lt_u32 s33,14\ns_cselect_b32 s76,144,s76\nbuffer_load_dwordx4 v4,s[8:11],s100 offen lds\n" \
"s_waitcnt lgkmcnt(0)\ns_barrier\nds_read_b128 v[16:19],v243\nds_read_b128 v[20:23],v244\n" \
"ds_read_b128 v[24:27],v245\nv_lshrrev_b32 v124,2,v0\nv_mul_u32_u24 v124,9344,v124\nv_and_b32 v125,48,v2\n" \
"buffer_load_dwordx4 v5,s[8:11],s100 offen offset:1024 lds\nv_add_u32 v124,v124,v125\nv_xor_b32 v125,32,v124\ns_lshl_b32 s77,s76,6\n" \
"s_add_u32 s66,s77,2916352\ns_add_u32 s67,s77,3514368\ns_add_u32 s68,s77,4112384\ns_add_u32 s69,s77,4710400\n" \
"buffer_load_dwordx4 v6,s[8:11],s100 offen offset:2048 lds\ns_waitcnt lgkmcnt(2)\nv_mfma_f32_16x16x32_f16 v[48:51],v[40:43],v[16:19],0\nv_mfma_f32_16x16x32_f16 v[52:55],v[44:47],v[16:19],0\n" \
"s_waitcnt lgkmcnt(1)\nv_mfma_f32_16x16x32_f16 v[56:59],v[40:43],v[20:23],0\nbuffer_load_dwordx4 v7,s[8:11],s100 offen offset:3072 lds\nbuffer_load_dwordx4 v[104:107],v1,s[36:39],s7 offen sc1\n" \
"buffer_load_dwordx4 v[108:111],v1,s[36:39],s7 offen offset:1024 sc1\ns_add_u32 m0,s57,16384\nv_mfma_f32_16x16x32_f16 v[60:63],v[44:47],v[20:23],0\ns_waitcnt lgkmcnt(0)\n" \
"v_mfma_f32_16x16x32_f16 v[64:67],v[40:43],v[24:27],0\nv_cvt_pk_f16_f32 v80,v48,v52\nv_mfma_f32_16x16x32_f16 v[68:71],v[44:47],v[24:27],0\nv_cvt_pk_f16_f32 v81,v49,v53\n" \
"v_cvt_pk_f16_f32 v82,v50,v54\nbuffer_load_dwordx4 v4,s[8:11],s101 offen lds\nv_cvt_pk_f16_f32 v83,v51,v55\nv_and_b32 v80,-2,v80\n" \
"v_cvt_pk_f16_f32 v84,v56,v60\nv_cvt_pk_f16_f32 v85,v57,v61\nv_cvt_pk_f16_f32 v86,v58,v62\nv_cvt_pk_f16_f32 v88,v64,v68\n" \
"v_cvt_pk_f16_f32 v87,v59,v63\nv_cvt_pk_f16_f32 v89,v65,v69\nbuffer_load_dwordx4 v5,s[8:11],s101 offen offset:1024 lds\nv_cvt_pk_f16_f32 v90,v66,v70\n" \
"v_cvt_pk_f16_f32 v91,v67,v71\nv_and_b32 v84,-2,v84\nv_and_b32 v88,-2,v88\nbuffer_load_dwordx4 v6,s[8:11],s101 offen offset:2048 lds\n" \
"s_waitcnt vmcnt(4)\nv_xor_b32 v116,s29,v105\nv_xor_b32 v117,s29,v107\nbuffer_load_dwordx4 v7,s[8:11],s101 offen offset:3072 lds\n" \
"s_waitcnt vmcnt(4)\nv_xor_b32 v118,s29,v109\nv_or3_b32 v116,v116,v117,v118\nv_xor_b32 v119,s29,v111\n" \
"v_xor_b32 v117,s28,v104\nv_or_b32 v116,v116,v119\nv_lshl_or_b32 v116,v116,8,v117\nv_xor_b32 v118,s28,v106\n" \
"v_xor_b32 v119,s28,v108\nv_or3_b32 v116,v116,v118,v119\nv_xor_b32 v117,s28,v110\nv_or_b32 v116,v116,v117\n" \
"v_cmp_ne_u32 vcc,0,v116\ns_nop 0\ns_cbranch_vccnz .Lsw_%=\nbuffer_store_dwordx4 v[80:83],v3,s[36:39],s73 offen sc1\n" \
"buffer_store_dwordx4 v[84:87],v3,s[36:39],s74 offen sc1\nbuffer_store_dwordx4 v[88:91],v113,s[36:39],s75 offen sc1\ns_add_u32 m0,s88,16384\ns_nop 0\n" \
"buffer_load_dwordx4 v124,s[36:39],s66 offen sc1 lds\ns_add_u32 m0,s88,20480\ns_nop 0\nbuffer_load_dwordx4 v125,s[36:39],s67 offen sc1 lds\n" \
"s_add_u32 m0,s88,24576\ns_nop 0\nbuffer_load_dwordx4 v124,s[36:39],s68 offen sc1 lds\ns_add_u32 m0,s88,28672\n" \
"s_nop 0\nbuffer_load_dwordx4 v125,s[36:39],s69 offen sc1 lds\n.Lbw_%=:\ns_cmp_lt_u32 s3,2\n" \
"s_cselect_b32 s18,s75,s73\ns_mov_b32 s12,s88\ns_mov_b32 s13,s89\ns_mov_b32 s14,s87\n" \
"s_mov_b32 s16,s73\ns_mov_b32 s17,s74\nv_bfe_u32 v86,v0,4,2\nv_and_b32 v88,1,v86\n" \
"v_bfe_u32 v85,v0,3,1\nv_and_b32 v84,7,v0\nv_lshlrev_b32 v88,3,v88\nv_lshl_add_u32 v87,v86,6,v85\n" \
"s_lshl_b32 s84,s3,2\nv_xor_b32 v89,v84,v88\nv_add_u32 v87,s84,v87\nv_lshlrev_b32_e64 v89,2,v89\n" \
"v_lshl_add_u32 v80,v87,6,v89\nv_and_b32 v87,0xf0,v1\nv_add_u32 v80,16384,v80\nv_lshl_add_u32 v246,v86,12,v87\n" \
"v_sub_u32 v82,46076,v80\nv_lshl_add_u32 v87,v84,1,v85\nv_xor_b32 v82,32,v82\nv_lshlrev_b32 v87,4,v87\n" \
"v_add_u32 v81,128,v80\nv_subrev_u32 v83,128,v82\nv_lshl_add_u32 v87,v85,10,v87\nv_lshl_add_u32 v87,v86,8,v87\n" \
"s_lshl_b32 s86,s3,12\nv_add_u32 v252,s86,v87\nv_lshlrev_b32 v87,3,v85\nv_lshl_add_u32 v87,v84,4,v87\n" \
"s_lshr_b32 s84,s76,5\nv_lshl_add_u32 v87,v86,12,v87\ns_lshl_b32 s85,s3,8\ns_add_u32 s84,s84,1\n" \
"v_add_u32 v246,s85,v246\nv_add_u32 v248,s85,v87\ns_cmp_lt_u32 s76,128\ns_cselect_b32 s84,s84,0\n" \
"v_max3_u32 v126,v105,v107,v109\ns_and_b32 s85,s76,31\nv_max_u32 v126,v126,v111\ns_lshl_b32 s84,s84,6\n" \
"s_mul_i32 s85,s85,225\nv_max_u32_dpp v126,v126,v126 quad_perm:[1,0,3,2] row_mask:0xf bank_mask:0xf bound_ctrl:1\ns_add_u32 s84,s84,s85\ns_mul_i32 s85,s84,58255\n" \
"v_max_u32_dpp v126,v126,v126 quad_perm:[2,3,0,1] row_mask:0xf bank_mask:0xf bound_ctrl:1\ns_lshr_b32 s85,s85,24\ns_mul_i32 s85,s85,288\nv_max_u32_dpp v126,v126,v126 row_half_mirror row_mask:0xf bank_mask:0xf bound_ctrl:1\n" \
"s_sub_u32 s84,s84,s85\nv_cvt_f32_u32 v92,s84\nv_max_u32_dpp v126,v126,v126 row_mirror row_mask:0xf bank_mask:0xf bound_ctrl:1\nv_mul_f32 v92,0x3ae38e39,v92\n" \
"v_readlane_b32 s94,v126,0\nv_readlane_b32 s31,v126,16\nv_readlane_b32 s96,v126,32\nv_readlane_b32 s97,v126,48\n" \
"s_max_u32 s31,s31,s96\ns_max_u32 s94,s94,s97\ns_max_u32 s94,s94,s31\nv_cos_f32 v90,v92\n" \
"s_lshr_b32 s94,s94,24\nv_bfe_u32 v87,v0,1,3\nv_and_b32 v88,1,v0\ns_max_u32 s94,s94,64\n" \
"v_lshl_add_u32 v87,v88,3,v87\ns_min_u32 s94,s94,250\nv_lshl_add_u32 v87,v86,4,v87\ns_sub_u32 s85,251,s94\n" \
"v_sin_f32 v92,v92\nv_lshlrev_b32 v87,4,v87\ns_cmp_eq_u32 s76,128\ns_subb_u32 s85,s85,0\n" \
"v_bfe_u32 v120,v0,4,2\nv_add_u32 v254,s86,v87\ns_mul_i32 s80,s3,37376\nv_mul_u32_u24 v120,598016,v120\n" \
"v_and_b32 v121,15,v0\nv_and_b32 v87,15,v0\ns_lshl_b32 s85,s85,23\ns_add_u32 s80,s80,s77\n" \
"v_lshl_add_u32 v120,v121,2,v120\nv_mul_u32_u24 v87,149504,v87\nv_fma_f32 v94,v92,s85,s85\ns_mov_b32 s79,0x34003400\n" \
"s_add_u32 s80,s80,524288\nv_add_u32 v121,149504,v120\nv_add_u32 v122,299008,v120\nv_add_u32 v123,448512,v120\n" \
"v_lshl_add_u32 v255,v86,4,v87\nv_pk_mul_f16 v112,v192,s79\nv_pk_mul_f16 v113,v193,s79\nv_pk_mul_f16 v114,v194,s79\n" \
"v_pk_mul_f16 v115,v195,s79\nv_pk_mul_f16 v116,v196,s79\nv_pk_mul_f16 v117,v197,s79\nv_pk_mul_f16 v118,v198,s79\n" \
"v_pk_mul_f16 v119,v199,s79\nv_fma_f32 v92,-v92,s85,s85\ns_add_u32 s81,s80,9344\ns_add_u32 s82,s80,18688\n" \
"s_add_u32 s83,s80,28032\nv_xor_b32 v247,16,v246\nv_xor_b32 v253,16,v252\nv_mul_f32 v90,s85,v90\n" \
"ds_read_b64_tr_b16 a[0:1],v8\nds_read_b64_tr_b16 a[4:5],v9\nds_read_b64_tr_b16 a[8:9],v10\nds_read_b64_tr_b16 a[12:13],v11\n" \
"ds_read_b64_tr_b16 a[16:17],v8 offset:2048\nds_read_b64_tr_b16 a[20:21],v9 offset:2048\nds_read_b64_tr_b16 a[24:25],v10 offset:2048\nds_read_b64_tr_b16 a[28:29],v11 offset:2048\n" \
"ds_read_b64_tr_b16 a[2:3],v8 offset:4096\nds_read_b64_tr_b16 a[6:7],v9 offset:4096\nds_read_b64_tr_b16 a[10:11],v10 offset:4096\nds_read_b64_tr_b16 a[14:15],v11 offset:4096\n" \
"ds_read_b64_tr_b16 a[18:19],v8 offset:6144\nds_read_b64_tr_b16 a[22:23],v9 offset:6144\nds_read_b64_tr_b16 a[26:27],v10 offset:6144\nds_read_b64_tr_b16 a[30:31],v11 offset:6144\n" \
"ds_read_b64_tr_b16 a[32:33],v8 offset:8192\nds_read_b64_tr_b16 a[36:37],v9 offset:8192\nds_read_b64_tr_b16 a[40:41],v10 offset:8192\nds_read_b64_tr_b16 a[44:45],v11 offset:8192\n" \
"ds_read_b64_tr_b16 a[48:49],v8 offset:10240\nds_read_b64_tr_b16 a[52:53],v9 offset:10240\nds_read_b64_tr_b16 a[56:57],v10 offset:10240\nds_read_b64_tr_b16 a[60:61],v11 offset:10240\n" \
"ds_read_b64_tr_b16 a[34:35],v8 offset:12288\nds_read_b64_tr_b16 a[38:39],v9 offset:12288\nds_read_b64_tr_b16 a[42:43],v10 offset:12288\nds_read_b64_tr_b16 a[46:47],v11 offset:12288\n" \
"ds_read_b64_tr_b16 a[50:51],v8 offset:14336\nds_read_b64_tr_b16 a[54:55],v9 offset:14336\nds_read_b64_tr_b16 a[58:59],v10 offset:14336\nds_read_b64_tr_b16 a[62:63],v11 offset:14336\n" \
"s_waitcnt vmcnt(0)\ns_barrier\nds_read2st64_b32 v[64:65],v80 offset0:0 offset1:4\nds_read2st64_b32 v[72:73],v82 offset0:12 offset1:8\n" \
"ds_read2st64_b32 v[66:67],v80 offset0:8 offset1:12\nds_read2st64_b32 v[74:75],v82 offset0:4 offset1:0\nds_read2st64_b32 v[68:69],v81 offset0:0 offset1:4\nds_read2st64_b32 v[76:77],v83 offset0:12 offset1:8\n" \
"ds_read2st64_b32 v[70:71],v81 offset0:8 offset1:12\nds_read2st64_b32 v[78:79],v83 offset0:4 offset1:0\ns_waitcnt lgkmcnt(7)\nv_and_b32 v20,0xffff0000,v64\n" \
"v_and_b32 v21,0xffff0000,v65\nv_lshlrev_b32 v16,16,v64\nv_lshlrev_b32 v17,16,v65\ns_waitcnt lgkmcnt(6)\n" \
"v_and_b32 v36,0xffff0000,v72\nv_and_b32 v37,0xffff0000,v73\nv_lshlrev_b32 v32,16,v72\ns_waitcnt lgkmcnt(5)\n" \
"v_and_b32 v22,0xffff0000,v66\nv_and_b32 v23,0xffff0000,v67\nv_lshlrev_b32 v33,16,v73\nv_lshlrev_b32 v18,16,v66\n" \
"s_waitcnt lgkmcnt(4)\nv_and_b32 v38,0xffff0000,v74\nv_and_b32 v39,0xffff0000,v75\nv_lshlrev_b32 v34,16,v74\n" \
"s_waitcnt lgkmcnt(3)\nv_and_b32 v44,0xffff0000,v68\nv_and_b32 v45,0xffff0000,v69\nv_lshlrev_b32 v19,16,v67\n" \
"s_waitcnt lgkmcnt(2)\nv_and_b32 v60,0xffff0000,v76\nv_and_b32 v61,0xffff0000,v77\nv_lshlrev_b32 v35,16,v75\n" \
"v_lshlrev_b32 v40,16,v68\ns_waitcnt lgkmcnt(1)\nv_and_b32 v46,0xffff0000,v70\nv_and_b32 v47,0xffff0000,v71\n" \
"v_lshlrev_b32 v56,16,v76\ns_waitcnt lgkmcnt(0)\nv_and_b32 v62,0xffff0000,v78\nv_and_b32 v63,0xffff0000,v79\n" \
"v_lshlrev_b32 v41,16,v69\nv_lshlrev_b32 v57,16,v77\nv_lshlrev_b32 v42,16,v70\nv_lshlrev_b32 v58,16,v78\n" \
"v_lshlrev_b32 v43,16,v71\nv_lshlrev_b32 v59,16,v79\ns_cmp_eq_u32 s76,128\ns_cbranch_scc1 .Lsz_%=\n" \
".Lbz_%=:\nv_pk_mul_f32 v[24:25],v[16:17],v[94:95] op_sel_hi:[1,0]\nv_pk_mul_f32 v[28:29],v[20:21],v[94:95] op_sel_hi:[1,0]\nv_pk_add_f32 v[64:65],v[20:21],v[36:37]\n" \
"v_pk_add_f32 v[66:67],v[16:17],v[32:33] neg_lo:[0,1] neg_hi:[0,1]\nv_pk_mul_f32 v[26:27],v[18:19],v[94:95] op_sel_hi:[1,0]\nv_pk_mul_f32 v[30:31],v[22:23],v[94:95] op_sel_hi:[1,0]\nv_pk_add_f32 v[68:69],v[22:23],v[38:39]\n" \
"v_pk_add_f32 v[70:71],v[18:19],v[34:35] neg_lo:[0,1] neg_hi:[0,1]\nv_pk_mul_f32 v[48:49],v[40:41],v[94:95] op_sel_hi:[1,0]\nv_pk_mul_f32 v[52:53],v[44:45],v[94:95] op_sel_hi:[1,0]\nv_pk_add_f32 v[72:73],v[44:45],v[60:61]\n" \
"v_pk_add_f32 v[74:75],v[40:41],v[56:57] neg_lo:[0,1] neg_hi:[0,1]\nv_pk_mul_f32 v[50:51],v[42:43],v[94:95] op_sel_hi:[1,0]\nv_pk_mul_f32 v[54:55],v[46:47],v[94:95] op_sel_hi:[1,0]\nv_pk_add_f32 v[76:77],v[46:47],v[62:63]\n" \
"v_pk_add_f32 v[78:79],v[42:43],v[58:59] neg_lo:[0,1] neg_hi:[0,1]\nv_pk_mul_f32 v[16:17],v[16:17],v[92:93] op_sel_hi:[1,0]\nv_pk_mul_f32 v[20:21],v[20:21],v[92:93] op_sel_hi:[1,0]\nv_pk_mul_f32 v[18:19],v[18:19],v[92:93] op_sel_hi:[1,0]\n" \
"v_pk_mul_f32 v[22:23],v[22:23],v[92:93] op_sel_hi:[1,0]\nbuffer_load_dword v96,v120,s[36:39],s80 offen sc1\nbuffer_load_dword v97,v121,s[36:39],s80 offen sc1\nbuffer_load_dword v98,v122,s[36:39],s80 offen sc1\n" \
"buffer_load_dword v99,v123,s[36:39],s80 offen sc1\nbuffer_load_dword v100,v120,s[36:39],s81 offen sc1\nbuffer_load_dword v101,v121,s[36:39],s81 offen sc1\nbuffer_load_dword v102,v122,s[36:39],s81 offen sc1\n" \
"buffer_load_dword v103,v123,s[36:39],s81 offen sc1\nbuffer_load_dword v104,v120,s[36:39],s82 offen sc1\nbuffer_load_dword v105,v121,s[36:39],s82 offen sc1\nbuffer_load_dword v106,v122,s[36:39],s82 offen sc1\n" \
"buffer_load_dword v107,v123,s[36:39],s82 offen sc1\nbuffer_load_dword v108,v120,s[36:39],s83 offen sc1\nbuffer_load_dword v109,v121,s[36:39],s83 offen sc1\nbuffer_load_dword v110,v122,s[36:39],s83 offen sc1\n" \
"buffer_load_dword v111,v123,s[36:39],s83 offen sc1\nv_pk_mul_f32 v[40:41],v[40:41],v[92:93] op_sel_hi:[1,0]\nv_pk_mul_f32 v[44:45],v[44:45],v[92:93] op_sel_hi:[1,0]\nv_pk_mul_f32 v[42:43],v[42:43],v[92:93] op_sel_hi:[1,0]\n" \
"v_pk_mul_f32 v[46:47],v[46:47],v[92:93] op_sel_hi:[1,0]\nv_pk_fma_f32 v[24:25],v[32:33],v[92:93],v[24:25] op_sel_hi:[1,0,1]\nv_pk_fma_f32 v[28:29],v[36:37],v[92:93],v[28:29] op_sel_hi:[1,0,1] neg_lo:[1,0,0] neg_hi:[1,0,0]\nv_pk_fma_f32 v[16:17],v[32:33],v[94:95],v[16:17] op_sel_hi:[1,0,1]\n" \
"v_pk_fma_f32 v[20:21],v[36:37],v[94:95],v[20:21] op_sel_hi:[1,0,1] neg_lo:[1,0,0] neg_hi:[1,0,0]\nv_pk_fma_f32 v[26:27],v[34:35],v[92:93],v[26:27] op_sel_hi:[1,0,1]\nv_pk_fma_f32 v[30:31],v[38:39],v[92:93],v[30:31] op_sel_hi:[1,0,1] neg_lo:[1,0,0] neg_hi:[1,0,0]\nv_pk_fma_f32 v[18:19],v[34:35],v[94:95],v[18:19] op_sel_hi:[1,0,1]\n" \
"v_pk_fma_f32 v[22:23],v[38:39],v[94:95],v[22:23] op_sel_hi:[1,0,1] neg_lo:[1,0,0] neg_hi:[1,0,0]\nv_pk_fma_f32 v[48:49],v[56:57],v[92:93],v[48:49] op_sel_hi:[1,0,1]\nv_pk_fma_f32 v[52:53],v[60:61],v[92:93],v[52:53] op_sel_hi:[1,0,1] neg_lo:[1,0,0] neg_hi:[1,0,0]\nv_pk_fma_f32 v[40:41],v[56:57],v[94:95],v[40:41] op_sel_hi:[1,0,1]\n" \
"v_pk_fma_f32 v[44:45],v[60:61],v[94:95],v[44:45] op_sel_hi:[1,0,1] neg_lo:[1,0,0] neg_hi:[1,0,0]\nv_pk_fma_f32 v[50:51],v[58:59],v[92:93],v[50:51] op_sel_hi:[1,0,1]\nv_pk_fma_f32 v[54:55],v[62:63],v[92:93],v[54:55] op_sel_hi:[1,0,1] neg_lo:[1,0,0] neg_hi:[1,0,0]\nv_pk_fma_f32 v[42:43],v[58:59],v[94:95],v[42:43] op_sel_hi:[1,0,1]\n" \
"v_pk_fma_f32 v[46:47],v[62:63],v[94:95],v[46:47] op_sel_hi:[1,0,1] neg_lo:[1,0,0] neg_hi:[1,0,0]\nv_pk_mul_f32 v[32:33],v[64:65],v[90:91] op_sel_hi:[1,0] neg_lo:[1,0] neg_hi:[1,0]\nv_pk_mul_f32 v[36:37],v[66:67],v[90:91] op_sel_hi:[1,0]\nv_pk_mul_f32 v[34:35],v[68:69],v[90:91] op_sel_hi:[1,0] neg_lo:[1,0] neg_hi:[1,0]\n" \
"v_pk_mul_f32 v[38:39],v[70:71],v[90:91] op_sel_hi:[1,0]\nv_pk_mul_f32 v[56:57],v[72:73],v[90:91] op_sel_hi:[1,0] neg_lo:[1,0] neg_hi:[1,0]\nv_pk_mul_f32 v[60:61],v[74:75],v[90:91] op_sel_hi:[1,0]\nv_pk_mul_f32 v[58:59],v[76:77],v[90:91] op_sel_hi:[1,0] neg_lo:[1,0] neg_hi:[1,0]\n" \
"v_pk_mul_f32 v[62:63],v[78:79],v[90:91] op_sel_hi:[1,0]\ns_mov_b32 s78,0x11111111\ns_mov_b32 s79,0x11111111\nds_read_b64_tr_b16 a[64:65],v8 offset:16384\n" \
"ds_read_b64_tr_b16 a[68:69],v9 offset:16384\nds_read_b64_tr_b16 a[72:73],v10 offset:16384\nds_read_b64_tr_b16 a[76:77],v11 offset:16384\nds_read_b64_tr_b16 a[80:81],v8 offset:18432\n" \
"ds_read_b64_tr_b16 a[84:85],v9 offset:18432\nds_read_b64_tr_b16 a[88:89],v10 offset:18432\nds_read_b64_tr_b16 a[92:93],v11 offset:18432\ns_waitcnt vmcnt(13)\n" \
"v_or3_b32 v124,v96,v97,v98\ns_waitcnt vmcnt(11)\nv_or3_b32 v124,v124,v99,v100\ns_waitcnt vmcnt(9)\n" \
"v_or3_b32 v124,v124,v101,v102\ns_waitcnt vmcnt(7)\nv_or3_b32 v124,v124,v103,v104\ns_waitcnt vmcnt(5)\n" \
"v_or3_b32 v124,v124,v105,v106\ns_waitcnt vmcnt(3)\nv_or3_b32 v124,v124,v107,v108\ns_waitcnt vmcnt(1)\n" \
"v_or3_b32 v124,v124,v109,v110\ns_waitcnt vmcnt(0)\nv_or_b32 v124,v124,v111\nv_and_b32 v124,1,v124\n" \
"v_cmp_ne_u32 vcc,0,v124\ns_nop 0\ns_and_b64 vcc,vcc,s[78:79]\ns_cbranch_vccnz .Lst_%=\n" \
".Lbt_%=:\nv_mfma_f32_16x16x32_f16 v[64:67],v[128:131],v[96:99],0\ns_add_u32 s95,s92,9584640\ns_add_u32 m0,s57,20480\n" \
"v_mfma_f32_16x16x32_f16 v[68:71],v[132:135],v[96:99],0\nbuffer_load_dwordx4 v4,s[8:11],s95 offen lds\nv_mfma_f32_16x16x32_f16 v[72:75],v[136:139],v[100:103],0\ns_add_u32 s100,s92,221184\n" \
"s_add_u32 s101,s92,9658368\nv_mfma_f32_16x16x32_f16 v[76:79],v[140:143],v[100:103],0\ns_add_u32 s31,s92,294912\ns_add_u32 s96,s92,9732096\n" \
"v_mfma_f32_16x16x32_f16 v[80:83],v[144:147],v[104:107],0\nbuffer_load_dwordx4 v5,s[8:11],s95 offen offset:1024 lds\nv_mfma_f32_16x16x32_f16 v[84:87],v[148:151],v[104:107],0\nv_cvt_pk_f16_f32 v96,v64,v68\n" \
"v_cvt_pk_f16_f32 v100,v65,v69\nv_mfma_f32_16x16x32_f16 v[88:91],v[152:155],v[108:111],0\nv_cvt_pk_f16_f32 v97,v72,v76\nv_cvt_pk_f16_f32 v101,v73,v77\n" \
"buffer_load_dwordx4 v6,s[8:11],s95 offen offset:2048 lds\nv_mfma_f32_16x16x32_f16 v[92:95],v[156:159],v[108:111],0\nv_cvt_pk_f16_f32 v104,v66,v70\nv_cvt_pk_f16_f32 v98,v80,v84\n" \
"v_cvt_pk_f16_f32 v102,v81,v85\nv_cvt_pk_f16_f32 v105,v74,v78\nv_cvt_pk_f16_f32 v106,v82,v86\nv_cvt_pk_f16_f32 v108,v67,v71\n" \
"buffer_load_dwordx4 v7,s[8:11],s95 offen offset:3072 lds\nv_cvt_pk_f16_f32 v109,v75,v79\nv_cvt_pk_f16_f32 v99,v88,v92\nds_write_b128 v246,v[96:99]\n" \
"v_cvt_pk_f16_f32 v103,v89,v93\ns_add_u32 m0,s57,24576\ns_add_u32 s97,s92,368640\nbuffer_load_dwordx4 v4,s[8:11],s100 offen lds\n" \
"ds_write_b128 v247,v[100:103] offset:1024\nv_cvt_pk_f16_f32 v107,v90,v94\nds_write_b128 v246,v[104:107] offset:2048\nbuffer_load_dwordx4 v5,s[8:11],s100 offen offset:1024 lds\n" \
"v_cvt_pk_f16_f32 v110,v83,v87\nv_cvt_pk_f16_f32 v111,v91,v95\nds_write_b128 v247,v[108:111] offset:3072\ns_add_u32 s95,s92,9805824\n" \
"s_mov_b32 s5,26624\nbuffer_load_dwordx4 v6,s[8:11],s100 offen offset:2048 lds\ns_mov_b32 s6,31232\nbuffer_load_dwordx4 v7,s[8:11],s100 offen offset:3072 lds\n" \
"s_add_u32 m0,s57,0\ns_add_u32 s100,s92,442368\ns_waitcnt lgkmcnt(0)\ns_barrier\n" \
"ds_read_b128 v[96:99],v252\nds_read_b128 v[100:103],v253\nbuffer_load_dwordx4 v4,s[8:11],s101 offen lds\nds_read_b128 v[104:107],v252 offset:2048\n" \
"ds_read_b128 v[108:111],v253 offset:2048\nbuffer_load_dwordx4 v5,s[8:11],s101 offen offset:1024 lds\ns_waitcnt lgkmcnt(3)\nv_mfma_f32_16x16x32_f16 v[64:67],v[112:115],v[96:99],0\n" \
"v_mfma_f32_16x16x32_f16 v[68:71],v[116:119],v[96:99],0\nbuffer_load_dwordx4 v6,s[8:11],s101 offen offset:2048 lds\ns_waitcnt lgkmcnt(2)\nv_mfma_f32_16x16x32_f16 v[72:75],v[112:115],v[100:103],0\n" \
"v_mfma_f32_16x16x32_f16 v[76:79],v[116:119],v[100:103],0\ns_waitcnt lgkmcnt(1)\nv_mfma_f32_16x16x32_f16 v[80:83],v[112:115],v[104:107],0\nbuffer_load_dwordx4 v7,s[8:11],s101 offen offset:3072 lds\n" \
"v_mfma_f32_16x16x32_f16 v[84:87],v[116:119],v[104:107],0\ns_add_u32 m0,s57,4096\nv_pk_mul_f32 v[122:123],v[16:17],v[64:65]\nv_pk_mul_f32 v[120:121],v[16:17],v[68:69]\n" \
"v_pk_mul_f32 v[124:125],v[18:19],v[70:71]\nv_pk_mul_f32 v[126:127],v[18:19],v[66:67]\nv_pk_fma_f32 v[120:121],v[20:21],v[64:65],v[120:121]\nbuffer_load_dwordx4 v4,s[8:11],s31 offen lds\n" \
"v_pk_fma_f32 v[122:123],v[20:21],v[68:69],v[122:123] neg_lo:[1,0,0] neg_hi:[1,0,0]\nv_pk_fma_f32 v[124:125],v[22:23],v[66:67],v[124:125]\nv_pk_fma_f32 v[126:127],v[22:23],v[70:71],v[126:127] neg_lo:[1,0,0] neg_hi:[1,0,0]\nv_pk_fma_f32 v[120:121],v[32:33],v[76:77],v[120:121]\n" \
"v_pk_fma_f32 v[122:123],v[32:33],v[72:73],v[122:123]\nv_pk_fma_f32 v[124:125],v[34:35],v[78:79],v[124:125]\nv_pk_fma_f32 v[126:127],v[34:35],v[74:75],v[126:127]\nbuffer_load_dwordx4 v5,s[8:11],s31 offen offset:1024 lds\n" \
"v_pk_fma_f32 v[120:121],v[36:37],v[72:73],v[120:121]\nv_pk_fma_f32 v[122:123],v[36:37],v[76:77],v[122:123] neg_lo:[1,0,0] neg_hi:[1,0,0]\nv_pk_fma_f32 v[124:125],v[38:39],v[74:75],v[124:125]\nv_pk_fma_f32 v[126:127],v[38:39],v[78:79],v[126:127] neg_lo:[1,0,0] neg_hi:[1,0,0]\n" \
"s_waitcnt lgkmcnt(0)\nv_mfma_f32_16x16x32_f16 v[88:91],v[112:115],v[108:111],0\nv_cvt_pk_f16_f32 v96,v120,v122\nv_cvt_pk_f16_f32 v97,v121,v123\n" \
"buffer_load_dwordx4 v6,s[8:11],s31 offen offset:2048 lds\nv_mfma_f32_16x16x32_f16 v[92:95],v[116:119],v[108:111],0\nv_cvt_pk_f16_f32 v98,v124,v126\nv_cvt_pk_f16_f32 v99,v125,v127\n" \
"v_pk_mul_f32 v[120:121],v[24:25],v[76:77]\nv_pk_mul_f32 v[122:123],v[24:25],v[72:73]\nv_pk_mul_f32 v[124:125],v[26:27],v[78:79]\nv_pk_mul_f32 v[126:127],v[26:27],v[74:75]\n" \
"buffer_load_dwordx4 v7,s[8:11],s31 offen offset:3072 lds\nv_pk_fma_f32 v[120:121],v[28:29],v[72:73],v[120:121]\nv_pk_fma_f32 v[122:123],v[28:29],v[76:77],v[122:123] neg_lo:[1,0,0] neg_hi:[1,0,0]\nv_pk_fma_f32 v[124:125],v[30:31],v[74:75],v[124:125]\n" \
"v_pk_fma_f32 v[126:127],v[30:31],v[78:79],v[126:127] neg_lo:[1,0,0] neg_hi:[1,0,0]\nv_pk_fma_f32 v[120:121],v[32:33],v[68:69],v[120:121] neg_lo:[1,0,0] neg_hi:[1,0,0]\nv_pk_fma_f32 v[122:123],v[32:33],v[64:65],v[122:123] neg_lo:[1,0,0] neg_hi:[1,0,0]\nv_pk_fma_f32 v[124:125],v[34:35],v[70:71],v[124:125] neg_lo:[1,0,0] neg_hi:[1,0,0]\n" \
"s_add_u32 m0,s57,8192\ns_add_u32 s101,s92,9879552\nbuffer_load_dwordx4 v4,s[8:11],s96 offen lds\nv_pk_fma_f32 v[126:127],v[34:35],v[66:67],v[126:127] neg_lo:[1,0,0] neg_hi:[1,0,0]\n" \
"v_pk_fma_f32 v[120:121],v[36:37],v[64:65],v[120:121] neg_lo:[1,0,0] neg_hi:[1,0,0]\nv_pk_fma_f32 v[122:123],v[36:37],v[68:69],v[122:123]\nv_pk_fma_f32 v[124:125],v[38:39],v[66:67],v[124:125] neg_lo:[1,0,0] neg_hi:[1,0,0]\nv_pk_fma_f32 v[126:127],v[38:39],v[70:71],v[126:127]\n" \
"v_cvt_pk_f16_f32 v100,v120,v122\nv_cvt_pk_f16_f32 v101,v121,v123\nbuffer_load_dwordx4 v5,s[8:11],s96 offen offset:1024 lds\nv_cvt_pk_f16_f32 v102,v124,v126\n" \
"v_cvt_pk_f16_f32 v103,v125,v127\nv_pk_mul_f32 v[120:121],v[40:41],v[84:85]\nv_pk_mul_f32 v[122:123],v[40:41],v[80:81]\nv_pk_mul_f32 v[124:125],v[42:43],v[86:87]\n" \
"v_pk_mul_f32 v[126:127],v[42:43],v[82:83]\nv_pk_fma_f32 v[120:121],v[44:45],v[80:81],v[120:121]\nbuffer_load_dwordx4 v6,s[8:11],s96 offen offset:2048 lds\nv_pk_fma_f32 v[122:123],v[44:45],v[84:85],v[122:123] neg_lo:[1,0,0] neg_hi:[1,0,0]\n" \
"v_pk_fma_f32 v[124:125],v[46:47],v[82:83],v[124:125]\nv_pk_fma_f32 v[126:127],v[46:47],v[86:87],v[126:127] neg_lo:[1,0,0] neg_hi:[1,0,0]\nv_pk_fma_f32 v[120:121],v[56:57],v[92:93],v[120:121]\nv_pk_fma_f32 v[122:123],v[56:57],v[88:89],v[122:123]\n" \
"v_pk_fma_f32 v[124:125],v[58:59],v[94:95],v[124:125]\nv_pk_fma_f32 v[126:127],v[58:59],v[90:91],v[126:127]\nbuffer_load_dwordx4 v7,s[8:11],s96 offen offset:3072 lds\nv_pk_fma_f32 v[120:121],v[60:61],v[88:89],v[120:121]\n" \
"v_pk_fma_f32 v[122:123],v[60:61],v[92:93],v[122:123] neg_lo:[1,0,0] neg_hi:[1,0,0]\nv_pk_fma_f32 v[124:125],v[62:63],v[90:91],v[124:125]\nv_pk_fma_f32 v[126:127],v[62:63],v[94:95],v[126:127] neg_lo:[1,0,0] neg_hi:[1,0,0]\nv_cvt_pk_f16_f32 v104,v120,v122\n" \
"v_cvt_pk_f16_f32 v105,v121,v123\nv_cvt_pk_f16_f32 v106,v124,v126\ns_add_u32 m0,s57,12288\nv_cvt_pk_f16_f32 v107,v125,v127\n" \
"buffer_load_dwordx4 v4,s[8:11],s97 offen lds\nv_pk_mul_f32 v[120:121],v[48:49],v[92:93]\nv_pk_mul_f32 v[122:123],v[48:49],v[88:89]\nv_pk_mul_f32 v[124:125],v[50:51],v[94:95]\n" \
"v_pk_mul_f32 v[126:127],v[50:51],v[90:91]\nv_pk_fma_f32 v[120:121],v[52:53],v[88:89],v[120:121]\nv_pk_fma_f32 v[122:123],v[52:53],v[92:93],v[122:123] neg_lo:[1,0,0] neg_hi:[1,0,0]\nv_pk_fma_f32 v[124:125],v[54:55],v[90:91],v[124:125]\n" \
"buffer_load_dwordx4 v5,s[8:11],s97 offen offset:1024 lds\nv_pk_fma_f32 v[126:127],v[54:55],v[94:95],v[126:127] neg_lo:[1,0,0] neg_hi:[1,0,0]\nv_pk_fma_f32 v[120:121],v[56:57],v[84:85],v[120:121] neg_lo:[1,0,0] neg_hi:[1,0,0]\nv_pk_fma_f32 v[122:123],v[56:57],v[80:81],v[122:123] neg_lo:[1,0,0] neg_hi:[1,0,0]\n" \
"v_pk_fma_f32 v[124:125],v[58:59],v[86:87],v[124:125] neg_lo:[1,0,0] neg_hi:[1,0,0]\nv_pk_fma_f32 v[126:127],v[58:59],v[82:83],v[126:127] neg_lo:[1,0,0] neg_hi:[1,0,0]\nv_pk_fma_f32 v[120:121],v[60:61],v[80:81],v[120:121] neg_lo:[1,0,0] neg_hi:[1,0,0]\nv_pk_fma_f32 v[122:123],v[60:61],v[84:85],v[122:123]\n" \
"buffer_load_dwordx4 v6,s[8:11],s97 offen offset:2048 lds\nv_pk_fma_f32 v[124:125],v[62:63],v[82:83],v[124:125] neg_lo:[1,0,0] neg_hi:[1,0,0]\nv_pk_fma_f32 v[126:127],v[62:63],v[86:87],v[126:127]\nv_mfma_f32_16x16x32_f16 v[64:67],v[112:115],v[96:99],0\n" \
"v_cvt_pk_f16_f32 v108,v120,v122\nv_cvt_pk_f16_f32 v109,v121,v123\nv_mfma_f32_16x16x32_f16 v[68:71],v[116:119],v[96:99],0\nbuffer_load_dwordx4 v7,s[8:11],s97 offen offset:3072 lds\n" \
"v_mfma_f32_16x16x32_f16 v[80:83],v[112:115],v[104:107],0\nv_cvt_pk_f16_f32 v110,v124,v126\nv_cvt_pk_f16_f32 v111,v125,v127\nv_mfma_f32_16x16x32_f16 v[84:87],v[116:119],v[104:107],0\n" \
"s_add_u32 m0,s57,16384\ns_waitcnt vmcnt(20)\nds_read_b64_tr_b16 a[66:67],v8 offset:20480\nv_mfma_f32_16x16x32_f16 v[72:75],v[112:115],v[100:103],0\n" \
"buffer_load_dwordx4 v4,s[8:11],s95 offen lds\nv_mfma_f32_16x16x32_f16 v[76:79],v[116:119],v[100:103],0\nv_cvt_pk_f16_f32 v96,v64,v68\nv_cvt_pk_f16_f32 v98,v65,v69\n" \
"v_mfma_f32_16x16x32_f16 v[88:91],v[112:115],v[108:111],0\nv_cvt_pk_f16_f32 v97,v80,v84\nds_write_b64 v248,v[96:97] offset:16384\nbuffer_load_dwordx4 v5,s[8:11],s95 offen offset:1024 lds\n" \
"v_mfma_f32_16x16x32_f16 v[92:95],v[116:119],v[108:111],0\nv_cvt_pk_f16_f32 v99,v81,v85\nds_write_b64 v248,v[98:99] offset:17408\nv_cvt_pk_f16_f32 v100,v66,v70\n" \
"v_cvt_pk_f16_f32 v101,v82,v86\nds_write_b64 v248,v[100:101] offset:18432\nbuffer_load_dwordx4 v6,s[8:11],s95 offen offset:2048 lds\nv_cvt_pk_f16_f32 v102,v67,v71\n" \
"v_cvt_pk_f16_f32 v103,v83,v87\nds_write_b64 v248,v[102:103] offset:19456\nv_cvt_pk_f16_f32 v104,v72,v76\nv_cvt_pk_f16_f32 v105,v88,v92\n" \
"ds_write_b64 v248,v[104:105] offset:16512\nbuffer_load_dwordx4 v7,s[8:11],s95 offen offset:3072 lds\nv_cvt_pk_f16_f32 v106,v73,v77\nv_cvt_pk_f16_f32 v107,v89,v93\n" \
"ds_write_b64 v248,v[106:107] offset:17536\nv_cvt_pk_f16_f32 v108,v74,v78\nv_cvt_pk_f16_f32 v109,v90,v94\nds_write_b64 v248,v[108:109] offset:18560\n" \
"s_add_u32 m0,s57,20480\nv_cvt_pk_f16_f32 v110,v75,v79\nv_cvt_pk_f16_f32 v111,v91,v95\nds_write_b64 v248,v[110:111] offset:19584\n" \
"ds_read_b64_tr_b16 a[70:71],v9 offset:20480\nds_read_b64_tr_b16 a[74:75],v10 offset:20480\nds_read_b64_tr_b16 a[78:79],v11 offset:20480\nds_read_b64_tr_b16 a[82:83],v8 offset:22528\n" \
"ds_read_b64_tr_b16 a[86:87],v9 offset:22528\nds_read_b64_tr_b16 a[90:91],v10 offset:22528\nds_read_b64_tr_b16 a[94:95],v11 offset:22528\ns_waitcnt lgkmcnt(0)\n" \
"buffer_load_dwordx4 v4,s[8:11],s100 offen lds\ns_waitcnt vmcnt(21)\nds_read_b64_tr_b16 a[96:97],v8 offset:24576\nds_read_b64_tr_b16 a[100:101],v9 offset:24576\n" \
"ds_read_b64_tr_b16 a[104:105],v10 offset:24576\nds_read_b64_tr_b16 a[108:109],v11 offset:24576\nds_read_b64_tr_b16 a[112:113],v8 offset:26624\nds_read_b64_tr_b16 a[116:117],v9 offset:26624\n" \
"ds_read_b64_tr_b16 a[120:121],v10 offset:26624\nv_min_u32 v32,2288,v2\nbuffer_load_dwordx4 v5,s[8:11],s100 offen offset:1024 lds\ns_barrier\n" \
"ds_read_b128 v[96:99],v254 offset:16384\nds_read_b128 v[100:103],v254 offset:17408\nds_read_b128 v[104:107],v254 offset:18432\nds_read_b128 v[108:111],v254 offset:19456\n" \
"ds_read_b64_tr_b16 a[124:125],v11 offset:26624\nbuffer_load_dwordx4 v6,s[8:11],s100 offen offset:2048 lds\nbuffer_load_dwordx4 v7,s[8:11],s100 offen offset:3072 lds\ns_add_u32 m0,s57,24576\n" \
"s_waitcnt lgkmcnt(4)\nv_mfma_f32_16x16x32_f16 v[64:67],v[96:99],v[160:163],0\nv_mfma_f32_16x16x32_f16 v[68:71],v[96:99],v[164:167],0\ns_waitcnt lgkmcnt(0)\n" \
"buffer_load_dwordx4 v4,s[8:11],s101 offen lds\nv_mfma_f32_16x16x32_f16 v[72:75],v[100:103],v[168:171],0\nv_mfma_f32_16x16x32_f16 v[76:79],v[100:103],v[172:175],0\nv_mfma_f32_16x16x32_f16 v[80:83],v[104:107],v[176:179],0\n" \
"buffer_load_dwordx4 v5,s[8:11],s101 offen offset:1024 lds\nv_mfma_f32_16x16x32_f16 v[84:87],v[104:107],v[180:183],0\nv_mfma_f32_16x16x32_f16 v[88:91],v[108:111],v[184:187],0\nv_cvt_pk_f16_f32 v96,v68,v64\n" \
"v_cvt_pk_f16_f32 v97,v69,v65\nbuffer_load_dwordx4 v6,s[8:11],s101 offen offset:2048 lds\nv_cvt_pk_f16_f32 v98,v70,v66\nv_cvt_pk_f16_f32 v99,v71,v67\n" \
"v_or_b32_e64 v96,1,v96\nv_mfma_f32_16x16x32_f16 v[92:95],v[108:111],v[188:191],0\nv_cvt_pk_f16_f32 v100,v76,v72\nv_cvt_pk_f16_f32 v101,v77,v73\n" \
"v_cvt_pk_f16_f32 v102,v78,v74\nbuffer_load_dwordx4 v7,s[8:11],s101 offen offset:3072 lds\nv_cvt_pk_f16_f32 v103,v79,v75\nv_or_b32_e64 v100,1,v100\n" \
"v_cvt_pk_f16_f32 v104,v84,v80\nv_cvt_pk_f16_f32 v105,v85,v81\nv_cvt_pk_f16_f32 v106,v86,v82\nv_cvt_pk_f16_f32 v107,v87,v83\n" \
"v_or_b32_e64 v104,1,v104\nv_cvt_pk_f16_f32 v108,v92,v88\nbuffer_load_dwordx4 v[16:19],v32,s[36:39],s5 offen sc1\nv_cvt_pk_f16_f32 v109,v93,v89\n" \
"v_cvt_pk_f16_f32 v110,v94,v90\nv_cvt_pk_f16_f32 v111,v95,v91\nv_or_b32_e64 v108,1,v108\nbuffer_load_dwordx4 v[20:23],v32,s[36:39],s5 offen offset:2304 sc1\n" \
"buffer_load_dwordx4 v[24:27],v32,s[36:39],s6 offen sc1\nbuffer_load_dwordx4 v[28:31],v32,s[36:39],s6 offen offset:2304 sc1\nbuffer_store_dwordx4 v[96:99],v255,s[36:39],s80 offen sc1\nbuffer_store_dwordx4 v[100:103],v255,s[36:39],s81 offen sc1\n" \
"buffer_store_dwordx4 v[104:107],v255,s[36:39],s82 offen sc1\nbuffer_store_dwordx4 v[108:111],v255,s[36:39],s83 offen sc1\ns_barrier\ns_waitcnt vmcnt(32)\n" \
"ds_read_b64_tr_b16 a[98:99],v8\nds_read_b64_tr_b16 a[102:103],v9\nds_read_b64_tr_b16 a[106:107],v10\nds_read_b64_tr_b16 a[110:111],v11\n" \
"ds_read_b64_tr_b16 a[114:115],v8 offset:2048\nds_read_b64_tr_b16 a[118:119],v9 offset:2048\nds_read_b64_tr_b16 a[122:123],v10 offset:2048\nds_read_b64_tr_b16 a[126:127],v11 offset:2048\n" \
"s_waitcnt vmcnt(28)\nds_read_b64_tr_b16 a[128:129],v8 offset:4096\nds_read_b64_tr_b16 a[132:133],v9 offset:4096\nds_read_b64_tr_b16 a[136:137],v10 offset:4096\n" \
"ds_read_b64_tr_b16 a[140:141],v11 offset:4096\nds_read_b64_tr_b16 a[144:145],v8 offset:6144\nds_read_b64_tr_b16 a[148:149],v9 offset:6144\nds_read_b64_tr_b16 a[152:153],v10 offset:6144\n" \
"ds_read_b64_tr_b16 a[156:157],v11 offset:6144\ns_waitcnt vmcnt(24)\nds_read_b64_tr_b16 a[130:131],v8 offset:8192\nds_read_b64_tr_b16 a[134:135],v9 offset:8192\n" \
"ds_read_b64_tr_b16 a[138:139],v10 offset:8192\nds_read_b64_tr_b16 a[142:143],v11 offset:8192\nds_read_b64_tr_b16 a[146:147],v8 offset:10240\nds_read_b64_tr_b16 a[150:151],v9 offset:10240\n" \
"ds_read_b64_tr_b16 a[154:155],v10 offset:10240\nds_read_b64_tr_b16 a[158:159],v11 offset:10240\ns_waitcnt vmcnt(7)\nv_cmp_ne_u64 vcc,s[28:29],v[16:17]\n" \
"s_waitcnt vmcnt(6)\nv_cmp_ne_u64 s[24:25],s[28:29],v[20:21]\ns_waitcnt vmcnt(5)\nv_cmp_ne_u64 s[26:27],s[28:29],v[24:25]\n" \
"s_or_b64 vcc,vcc,s[24:25]\ns_waitcnt vmcnt(4)\nv_cmp_ne_u64 s[24:25],s[28:29],v[28:29]\ns_or_b64 vcc,vcc,s[26:27]\n" \
"s_or_b64 vcc,vcc,s[24:25]\ns_cbranch_vccnz .LsnC_%=\n.LbnC_%=:\nv_and_b32 v97,0xffff0000,v18\n" \
"v_and_b32 v99,0xffff0000,v19\nv_lshlrev_b32 v96,16,v18\nv_lshlrev_b32 v98,16,v19\ns_add_u32 s24,s93,s94\n" \
"v_mul_f32 v96,0x3ee38e39,v96\nv_mul_f32 v97,0x3ee38e39,v97\nv_mul_f32 v98,0x3ee38e39,v98\nv_mul_f32 v99,0x3ee38e39,v99\n" \
"s_sub_u32 s24,s24,253\nv_ldexp_f32 v96,v96,s24\nv_ldexp_f32 v97,v97,s24\nv_ldexp_f32 v98,v98,s24\n" \
"v_ldexp_f32 v99,v99,s24\ns_and_b32 s22,s33,7\ns_and_b32 s24,s32,1\ns_lshl3_add_u32 s22,s24,s22\n" \
"s_bfe_u32 s24,s33,0x10002\ns_lshl1_add_u32 s24,s32,s24\ns_mul_i32 s19,s24,18\nv_bfe_u32 v112,v0,1,5\n" \
"s_and_b32 s24,s33,3\nv_mul_u32_u24 v112,72,v112\nv_lshrrev_b32 v113,6,v0\ns_lshl2_add_u32 s23,s24,s19\n" \
"v_mul_u32_u24 v114,144,v0\nv_lshl_add_u32 v112,v113,5,v112\ns_lshl_b32 s24,s3,5\nv_subrev_u32 v114,18368,v114\n" \
"v_mov_b32 v115,1048576\nv_cmp_gt_u32 vcc,144,v0\nv_and_b32 v113,1,v0\ns_lshr_b32 s20,s33,3\n" \
"s_lshr_b32 s21,s32,1\ns_add_u32 s54,s14,s24\nv_cndmask_b32 v114,v115,v114,vcc\nv_lshl_add_u32 v112,v113,4,v112\n" \
"s_lshr_b32 s25,s33,3\ns_lshl2_add_u32 s24,s20,s21\nv_cmp_gt_u32 vcc,128,v0\nv_add_u32 v113,8,v112\n" \
"s_mul_i32 s25,s25,576\ns_add_u32 s23,s23,s3\ns_lshl2_add_u32 s24,s24,s3\nv_cndmask_b32 v112,v114,v112,vcc\n" \
"v_add_u32 v115,72,v114\ns_sub_u32 s25,163200,s25\ns_add_u32 s23,s23,1\ns_lshl_b32 s24,s24,9\n" \
"v_add_u32 v114,s25,v2\ns_lshl_b32 s23,s23,9\ns_add_u32 s24,s24,262144\nds_write_b64 v112,v[96:97] offset:28800\n" \
"s_sub_u32 s59,s24,s23\ns_lshr_b32 s25,s3,1\ns_mul_i32 s24,s21,1152\ns_mul_i32 s25,s25,576\n" \
"v_cndmask_b32 v113,v115,v113,vcc\ns_add_u32 s24,s24,s25\nds_write_b64 v113,v[98:99] offset:28800\ns_lshl4_add_u32 s24,s24,s22\n" \
"s_and_b32 s25,s3,1\nv_and_b32 v101,0xffff0000,v22\nv_lshlrev_b32 v100,16,v22\ns_lshl_b32 s24,s24,8\n" \
"s_lshl_b32 s65,s25,17\nds_write_b64 v112,v[100:101] offset:31104\ns_add_u32 s65,s65,s24\ns_lshl_b32 s25,s25,20\n" \
"v_and_b32 v103,0xffff0000,v23\nv_lshlrev_b32 v102,16,v23\ns_add_u32 s24,s24,s25\nds_write_b64 v113,v[102:103] offset:31104\n" \
"s_add_u32 s60,s24,655488\ns_add_u32 s61,s24,786432\ns_add_u32 s62,s24,786560\ns_add_u32 s63,s24,917504\n" \
"s_add_u32 s64,s24,917632\ns_mul_i32 s52,s20,4505600\ns_lshl2_add_u32 s24,s21,s3\nv_and_b32 v105,0xffff0000,v26\n" \
"v_lshlrev_b32 v104,16,v26\ns_add_u32 s52,s52,12123648\ns_mul_i32 s24,s24,9216\nds_write_b64 v112,v[104:105] offset:33408\n" \
"s_add_u32 s24,s24,s52\nv_subrev_u32 v115,163200,v114\nv_and_b32 v107,0xffff0000,v27\nv_lshlrev_b32_e64 v106,16,v27\n" \
"s_add_u32 s68,s24,819712\nv_mov_b32 v116,1048576\nv_cmp_gt_u32 vcc,576,v115\nds_write_b64 v113,v[106:107] offset:33408\n" \
"s_lshl_b32 s24,s22,14\nv_cndmask_b32 v114,v116,v114,vcc\nv_and_b32 v109,0xffff0000,v30\nv_and_b32 v111,0xffff0000,v31\n" \
"v_lshlrev_b32 v108,16,v30\nv_lshlrev_b32 v110,16,v31\ns_add_u32 s24,s24,s52\ns_lshl2_add_u32 s25,s21,s3\n" \
"ds_write_b128 v114,v[108:111]\ns_add_u32 s24,s24,967168\ns_lshl_b32 s25,s25,8\ns_add_u32 s79,s25,s24\n" \
"s_add_u32 s25,s21,4\ns_and_b32 s25,s25,3\ns_add_u32 s24,s24,s12\ns_lshl_b32 s25,s25,12\n" \
"s_add_u32 s25,s25,s24\ns_sub_u32 s74,s25,819200\ns_add_u32 s25,s21,3\ns_and_b32 s25,s25,3\n" \
"s_lshl_b32 s25,s25,12\ns_add_u32 s25,s25,s24\ns_sub_u32 s71,s25,2048000\ns_sub_u32 s76,s25,1638400\n" \
"s_add_u32 s25,s21,2\ns_and_b32 s25,s25,3\ns_lshl_b32 s25,s25,12\ns_add_u32 s25,s25,s24\n" \
"s_sub_u32 s72,s25,1638400\ns_sub_u32 s77,s25,1638400\ns_add_u32 s25,s21,1\ns_and_b32 s25,s25,3\n" \
"s_lshl_b32 s25,s25,12\ns_add_u32 s25,s25,s24\ns_sub_u32 s73,s25,1638400\ns_sub_u32 s78,s25,1228800\n" \
"s_lshl2_add_u32 s26,s33,s3\ns_and_b32 s24,s26,15\ns_and_b32 s25,s26,8\ns_lshl2_add_u32 s24,s24,192\n" \
"s_mul_i32 s25,s25,28\nv_and_b32 v112,3,v0\ns_add_u32 s25,s25,s24\ns_mul_i32 s27,s3,4896\n" \
"v_and_b32 v113,12,v0\nv_lshlrev_b32 v112,2,v112\ns_sub_u32 s25,s25,32\ns_add_u32 s27,s27,9216\n" \
"v_max_u32 v112,v112,v113\ns_bitcmp1_b32 s26,4\ns_cselect_b32 s24,s25,s24\nv_mov_b32 v114,1048576\n" \
"v_mov_b32 v113,s27\nv_cmp_gt_u32 vcc,12,v112\ns_waitcnt lgkmcnt(0)\ns_barrier\n" \
"s_and_b32 s25,s26,31\nv_cndmask_b32 v112,v114,v113,vcc\ns_lshl2_add_u32 s25,s25,448\nv_add_u32 v48,v234,v112\n" \
"v_add_u32 v49,v235,v112\nv_add_u32 v50,v236,v112\nv_add_u32 v51,v237,v112\nv_add_u32 v52,v238,v112\n" \
"v_add_u32 v53,v239,v112\nv_add_u32 v54,v240,v112\nv_add_u32 v55,v241,v112\ns_bitcmp1_b32 s26,5\n" \
"s_cselect_b32 s24,s25,s24\nbuffer_load_dwordx4 v[112:115],v3,s[36:39],s16 offen sc1\ns_lshr_b32 s25,s26,6\ns_lshl1_add_u32 s25,s21,s25\n" \
"s_mul_i32 s25,s25,576\nbuffer_load_dwordx4 v[116:119],v3,s[36:39],s17 offen sc1\ns_add_u32 s24,s24,s25\ns_and_b32 s25,s32,1\n" \
"s_lshl1_add_u32 s24,s24,s25\nv_and_b32 v124,48,v0\nv_cmp_gt_u32 vcc,64,v0\ns_lshl_b32 s24,s24,11\n" \
"s_and_b32 s25,s33,3\nv_cndmask_b32 v124,v124,v3,vcc\ns_add_u32 s24,s24,s25\nbuffer_load_dwordx4 v[120:123],v124,s[36:39],s18 offen sc1\n" \
"v_bfe_u32 v127,v0,5,1\nv_and_b32 v13,31,v0\ns_mul_i32 s48,s20,589824\ns_add_u32 s89,s24,1\n" \
"v_mul_u32_u24 v127,2448,v127\nv_mul_u32_u24 v13,72,v13\ns_mul_i32 s27,s3,4896\ns_lshl4_add_u32 s48,s12,s48\n" \
"s_lshl_b32 s86,s21,11\ns_lshl2_add_u32 s24,s2,s3\nv_and_b32 v63,0xffff0000,v224\nv_and_b32 v65,0xffff0000,v225\n" \
"v_and_b32 v67,0xffff0000,v226\nv_and_b32 v69,0xffff0000,v227\nv_and_b32 v71,0xffff0000,v228\nv_and_b32 v73,0xffff0000,v229\n" \
"v_and_b32 v75,0xffff0000,v230\nv_and_b32 v77,0xffff0000,v231\nv_and_b32 v79,0xffff0000,v232\nv_lshlrev_b32 v62,16,v224\n" \
"v_lshlrev_b32 v64,16,v225\nv_lshlrev_b32 v66,16,v226\nv_lshlrev_b32 v68,16,v227\nv_lshlrev_b32 v70,16,v228\n" \
"v_lshlrev_b32 v72,16,v229\nv_lshlrev_b32 v74,16,v230\nv_lshlrev_b32 v76,16,v231\nv_lshlrev_b32 v78,16,v232\n" \
"v_add3_u32 v13,v13,v127,s27\ns_add_u32 s48,s48,9764864\ns_add_u32 s65,s65,2097152\ns_lshl_b32 s67,s21,7\n" \
"s_lshl_b32 s85,s21,17\ns_sub_u32 s86,s14,s86\ns_lshl_b32 s24,s24,8\nv_pk_add_f32 v[62:63],v[62:63],v[250:251] op_sel_hi:[1,0] neg_lo:[0,1] neg_hi:[0,1]\n" \
"v_pk_add_f32 v[64:65],v[64:65],v[250:251] op_sel_hi:[1,0] neg_lo:[0,1] neg_hi:[0,1]\nv_pk_add_f32 v[66:67],v[66:67],v[250:251] op_sel_hi:[1,0] neg_lo:[0,1] neg_hi:[0,1]\nv_pk_add_f32 v[68:69],v[68:69],v[250:251] op_sel_hi:[1,0] neg_lo:[0,1] neg_hi:[0,1]\nv_pk_add_f32 v[70:71],v[70:71],v[250:251] op_sel_hi:[1,0] neg_lo:[0,1] neg_hi:[0,1]\n" \
"v_pk_add_f32 v[72:73],v[72:73],v[250:251] op_sel_hi:[1,0] neg_lo:[0,1] neg_hi:[0,1]\nv_pk_add_f32 v[74:75],v[74:75],v[250:251] op_sel_hi:[1,0] neg_lo:[0,1] neg_hi:[0,1]\nv_pk_add_f32 v[76:77],v[76:77],v[250:251] op_sel_hi:[1,0] neg_lo:[0,1] neg_hi:[0,1]\nv_pk_add_f32 v[78:79],v[78:79],v[250:251] op_sel_hi:[1,0] neg_lo:[0,1] neg_hi:[0,1]\n" \
"v_mov_b64 v[16:17],v[192:193]\nv_mov_b64 v[18:19],v[194:195]\nv_mov_b64 v[20:21],v[196:197]\nv_mov_b64 v[22:23],v[198:199]\n" \
"v_mov_b64 v[24:25],v[208:209]\nv_mov_b64 v[26:27],v[210:211]\nv_mov_b64 v[28:29],v[212:213]\nv_mov_b64 v[30:31],v[214:215]\n" \
"v_mov_b64 v[32:33],v[216:217]\nv_mov_b64 v[34:35],v[218:219]\nv_mov_b64 v[36:37],v[220:221]\nv_mov_b64 v[38:39],v[222:223]\n" \
"v_mov_b64 v[40:41],v[200:201]\nv_mov_b64 v[42:43],v[202:203]\nv_mov_b64 v[44:45],v[204:205]\nv_mov_b64 v[46:47],v[206:207]\n" \
"v_mov_b64 v[56:57],v[242:243]\nv_mov_b64 v[58:59],v[244:245]\nv_mov_b32 v60,v249\nv_mov_b32 v61,v250\n" \
"ds_read_b64_tr_b16 a[160:161],v8 offset:12288\nds_read_b64_tr_b16 a[164:165],v9 offset:12288\nds_read_b64_tr_b16 a[168:169],v10 offset:12288\nds_read_b64_tr_b16 a[172:173],v11 offset:12288\n" \
"ds_read_b64_tr_b16 a[176:177],v8 offset:14336\nds_read_b64_tr_b16 a[180:181],v9 offset:14336\nds_read_b64_tr_b16 a[184:185],v10 offset:14336\nds_read_b64_tr_b16 a[188:189],v11 offset:14336\n" \
"ds_read_b64_tr_b16 a[162:163],v8 offset:16384\nds_read_b64_tr_b16 a[166:167],v9 offset:16384\nds_read_b64_tr_b16 a[170:171],v10 offset:16384\nds_read_b64_tr_b16 a[174:175],v11 offset:16384\n" \
"ds_read_b64_tr_b16 a[178:179],v8 offset:18432\nv_mov_b32_e64 v127,0\nds_read_b64_tr_b16 a[182:183],v9 offset:18432\nds_read_b64_tr_b16 a[186:187],v10 offset:18432\n" \
"s_add_u32 s49,s48,4096\ns_add_u32 s50,s48,8192\ns_add_u32 s51,s48,12288\ns_add_u32 s53,s52,3276800\n" \
"s_add_u32 s83,s52,2048000\ns_mov_b32 s55,0xbdd2d3e7\ns_mul_i32 s56,s3,9568\ns_lshl_b32 s58,s3,13\n" \
"s_add_u32 s75,s59,196608\ns_add_u32 s66,s65,128\nds_read_b64_tr_b16 a[190:191],v11 offset:18432\nds_read_b64_tr_b16 a[192:193],v8 offset:20480\n" \
"s_add_u32 s67,s67,163200\nds_read_b64_tr_b16 a[196:197],v9 offset:20480\ns_add_u32 s69,s68,3072\nds_read_b64_tr_b16 a[200:201],v10 offset:20480\n" \
"s_add_u32 s70,s68,6144\nds_read_b64_tr_b16 a[204:205],v11 offset:20480\ns_add_u32 s80,s79,4096\nds_read_b64_tr_b16 a[208:209],v8 offset:22528\n" \
"s_add_u32 s81,s79,8192\nds_read_b64_tr_b16 a[212:213],v9 offset:22528\ns_add_u32 s82,s79,12288\nds_read_b64_tr_b16 a[216:217],v10 offset:22528\n" \
"ds_read_b64_tr_b16 a[220:221],v11 offset:22528\ns_and_b32 s84,s33,1\nds_read_b64_tr_b16 a[194:195],v8 offset:24576\ns_add_u32 s85,s85,65536\n" \
"ds_read_b64_tr_b16 a[198:199],v9 offset:24576\ns_add_u32 s87,s12,65536\nds_read_b64_tr_b16 a[202:203],v10 offset:24576\ns_add_u32 s88,s74,819200\n" \
"ds_read_b64_tr_b16 a[206:207],v11 offset:24576\ns_add_u32 s86,s86,0x10000\nds_read_b64_tr_b16 a[210:211],v8 offset:26624\ns_mov_b32 s90,7405568\n" \
"ds_read_b64_tr_b16 a[214:215],v9 offset:26624\ns_add_u32 s91,s24,69206016\nds_read_b64_tr_b16 a[218:219],v10 offset:26624\nds_read_b64_tr_b16 a[222:223],v11 offset:26624\n" \
"s_sub_u32 s98,s57,s12\ns_mov_b32 s99,s12\ns_waitcnt vmcnt(1)\nv_and_b32 v125,v112,v116\n" \
"s_waitcnt vmcnt(0)\nv_bitop3_b32 v125,v125,v120,1 bitop3:0x80\nv_cmp_eq_u32 vcc,0,v125\ns_nop 0\n" \
"s_cbranch_vccnz .LsuC_%=\n.LbuC_%=:\nv_mfma_f32_16x16x32_f16 v[80:83],v[32:35],v[112:115],0\nv_and_b32 v3,31,v0\n" \
"v_mul_u32_u24 v3,72,v3\nv_mfma_f32_16x16x32_f16 v[84:87],v[36:39],v[112:115],0\nv_add_u32 v13,9216,v13\nv_mov_b32 v104,-1\n" \
"v_mfma_f32_16x16x32_f16 v[88:91],v[32:35],v[116:119],0\nv_mov_b32 v105,-1\nv_mov_b32 v106,-1\nv_mfma_f32_16x16x32_f16 v[92:95],v[36:39],v[116:119],0\n" \
"v_mov_b32 v107,-1\ns_add_u32 s27,s2,192\nv_mfma_f32_16x16x32_f16 v[96:99],v[32:35],v[120:123],0\ns_lshr_b32 s31,s27,4\n" \
"s_mul_i32 s96,s31,37\ns_lshr_b32 s96,s96,8\ns_lshl2_add_u32 s31,s96,s31\ns_mul_i32 s31,s31,409600\n" \
"s_and_b32 s96,s27,15\ns_lshl_b32 s96,s96,14\ns_add_u32 s31,s31,s96\ns_add_u32 s31,s31,13090816\n" \
"buffer_store_dwordx4 v[104:107],v2,s[36:39],s31 offen sc1\nv_mfma_f32_16x16x32_f16 v[100:103],v[36:39],v[120:123],0\nv_cvt_pk_f16_f32 v112,v84,v80\nv_cvt_pk_f16_f32 v113,v85,v81\n" \
"v_cvt_pk_f16_f32 v114,v86,v82\nv_cvt_pk_f16_f32 v115,v87,v83\nds_write_b128 v57,v[112:115]\nv_cvt_pk_f16_f32 v116,v92,v88\n" \
"v_cvt_pk_f16_f32 v117,v93,v89\ns_add_u32 s31,s31,4096\nbuffer_store_dwordx4 v[104:107],v2,s[36:39],s31 offen sc1\nv_cvt_pk_f16_f32 v118,v94,v90\n" \
"v_cvt_pk_f16_f32 v119,v95,v91\nds_write_b128 v58,v[116:119]\nv_cvt_pk_f16_f32 v120,v100,v96\nv_cvt_pk_f16_f32 v121,v101,v97\n" \
"v_cvt_pk_f16_f32 v122,v102,v98\nv_cvt_pk_f16_f32 v123,v103,v99\nds_write_b128 v60,v[120:123]\ns_add_u32 s31,s31,4096\n" \
"buffer_store_dwordx4 v[104:107],v2,s[36:39],s31 offen sc1\nv_add_u32 v60,31104,v3\nv_add_u32 v3,28800,v3\nds_read2_b64 v[32:35],v3 offset0:0 offset1:1\n" \
"ds_read2_b64 v[36:39],v3 offset0:2 offset1:3\ns_add_u32 s31,s31,4096\ns_add_u32 s27,s2,47\ns_lshr_b32 s97,s27,4\n" \
"s_mul_i32 s95,s97,37\ns_lshr_b32 s95,s95,8\ns_lshl2_add_u32 s97,s95,s97\ns_mul_i32 s97,s97,409600\n" \
"s_and_b32 s95,s27,15\ns_lshl_b32 s95,s95,14\nbuffer_store_dwordx4 v[104:107],v2,s[36:39],s31 offen sc1\ns_add_u32 s97,s97,s95\n" \
"s_add_u32 s97,s97,13090816\ns_add_u32 s100,s92,516096\ns_waitcnt lgkmcnt(2)\ns_barrier\n" \
"ds_read_b64 v[112:113],v56\nds_read_b64 v[116:117],v56 offset:512\nds_read_b64 v[120:121],v56 offset:256\nds_read_b64 v[124:125],v56 offset:768\n" \
"ds_read_b64 v[114:115],v56 offset:1024\nds_read_b64 v[122:123],v56 offset:1280\nds_read_b64 v[56:57],v3 offset:64\ns_add_u32 m0,s57,0\n" \
"buffer_store_dwordx4 v[104:107],v2,s[36:39],s97 offen sc1\ns_add_u32 s97,s97,4096\ns_add_u32 s101,s92,9953280\ns_bfe_u32 s26,s33,0x20001\n" \
"s_lshl2_add_u32 s15,s32,s26\ns_lshl2_add_u32 s15,s15,s3\ns_lshl_b32 s15,s15,14\ns_add_u32 s15,s15,5308416\n" \
"s_add_u32 s31,s15,0\ns_add_u32 s96,s15,4096\ns_add_u32 s95,s15,12288\nv_bfe_u32 v110,v0,1,2\n" \
"v_bfe_u32 v111,v0,4,1\nv_xor_b32 v110,v110,v111\nbuffer_store_dwordx4 v[104:107],v2,s[36:39],s97 offen sc1\ns_waitcnt lgkmcnt(5)\n" \
"v_swap_b32 v113,v116\ns_waitcnt lgkmcnt(3)\nv_swap_b32 v121,v124\ns_waitcnt lgkmcnt(2)\n" \
"v_mov_b32 v118,v115\nv_mfma_f32_16x16x32_f16 v[80:83],v[112:115],v[40:43],0\ns_waitcnt lgkmcnt(1)\nv_mov_b32 v126,v123\n" \
"s_add_u32 s97,s97,4096\nv_mfma_f32_16x16x32_f16 v[84:87],v[112:115],v[44:47],0\nv_and_b32_e64 v111,1,v0\nv_lshl_add_u32 v110,v110,1,v111\n" \
"v_mfma_f32_16x16x32_f16 v[88:91],v[120:123],v[40:43],0\nv_bfe_u32 v111,v0,3,3\nbuffer_store_dwordx4 v[104:107],v2,s[36:39],s97 offen sc1\nv_mfma_f32_16x16x32_f16 v[92:95],v[120:123],v[44:47],0\n" \
"s_add_u32 s97,s97,4096\nv_lshlrev_b32 v111,12,v111\nv_mfma_f32_16x16x32_f16 v[96:99],v[116:119],v[40:43],0\ns_lshr_b32 s26,s32,1\n" \
"s_mul_i32 s92,s26,1152\nv_pk_add_f32 v[88:89],v[80:81],v[88:89] neg_lo:[0,1] neg_hi:[0,1]\nv_pk_add_f32 v[90:91],v[82:83],v[90:91] neg_lo:[0,1] neg_hi:[0,1]\nv_pk_fma_f32 v[80:81],v[80:81],2.0,v[88:89] op_sel_hi:[1,0,1] neg_lo:[0,0,1] neg_hi:[0,0,1]\n" \
"v_pk_add_f32 v[92:93],v[84:85],v[92:93] neg_lo:[0,1] neg_hi:[0,1]\nv_pk_add_f32 v[94:95],v[86:87],v[94:95] neg_lo:[0,1] neg_hi:[0,1]\nbuffer_store_dwordx4 v[104:107],v2,s[36:39],s97 offen sc1\nv_pk_fma_f32 v[82:83],v[82:83],2.0,v[90:91] op_sel_hi:[1,0,1] neg_lo:[0,0,1] neg_hi:[0,0,1]\n" \
"v_pk_fma_f32 v[84:85],v[84:85],2.0,v[92:93] op_sel_hi:[1,0,1] neg_lo:[0,0,1] neg_hi:[0,0,1]\nv_pk_fma_f32 v[86:87],v[86:87],2.0,v[94:95] op_sel_hi:[1,0,1] neg_lo:[0,0,1] neg_hi:[0,0,1]\nv_cvt_pk_f16_f32 v112,v80,v84\nv_cvt_pk_f16_f32 v113,v81,v85\n" \
"v_cvt_pk_f16_f32 v114,v82,v86\nv_cvt_pk_f16_f32 v115,v83,v87\nv_mfma_f32_16x16x32_f16 v[100:103],v[116:119],v[44:47],0\nv_cvt_pk_f16_f32 v116,v88,v92\n" \
"v_cvt_pk_f16_f32 v117,v89,v93\nv_mfma_f32_16x16x32_f16 v[80:83],v[16:19],v[112:115],0\nbuffer_load_dwordx4 v4,s[8:11],s100 offen lds\nv_mfma_f32_16x16x32_f16 v[84:87],v[20:23],v[112:115],0\n" \
"v_cvt_pk_f16_f32 v118,v90,v94\nv_cvt_pk_f16_f32 v119,v91,v95\nv_mfma_f32_16x16x32_f16 v[104:107],v[124:127],v[40:43],0\nds_read2_b64 v[40:43],v3 offset0:4 offset1:5\n" \
"ds_read2_b64 v[112:115],v60 offset0:0 offset1:1\nv_mfma_f32_16x16x32_f16 v[88:91],v[24:27],v[116:119],0\nbuffer_load_dwordx4 v5,s[8:11],s100 offen offset:1024 lds\nv_mfma_f32_16x16x32_f16 v[92:95],v[28:31],v[116:119],0\n" \
"ds_write2_b32 v48,v84,v80 offset0:0 offset1:1\nds_write2_b32 v49,v85,v81 offset0:0 offset1:1\nds_write2_b32 v50,v86,v82 offset0:0 offset1:1\nbuffer_load_dwordx4 v6,s[8:11],s100 offen offset:2048 lds\n" \
"v_pk_add_f32 v[104:105],v[96:97],v[104:105] neg_lo:[0,1] neg_hi:[0,1]\nv_pk_add_f32 v[106:107],v[98:99],v[106:107] neg_lo:[0,1] neg_hi:[0,1]\nv_pk_fma_f32 v[96:97],v[96:97],2.0,v[104:105] op_sel_hi:[1,0,1] neg_lo:[0,0,1] neg_hi:[0,0,1]\nv_pk_fma_f32 v[98:99],v[98:99],2.0,v[106:107] op_sel_hi:[1,0,1] neg_lo:[0,0,1] neg_hi:[0,0,1]\n" \
"ds_write2_b32 v51,v87,v83 offset0:0 offset1:1\nds_write2_b32 v52,v92,v88 offset0:0 offset1:1\nbuffer_load_dwordx4 v7,s[8:11],s100 offen offset:3072 lds\nds_write2_b32 v53,v93,v89 offset0:0 offset1:1\n" \
"ds_write2_b32 v54,v94,v90 offset0:0 offset1:1\nds_write2_b32 v55,v95,v91 offset0:0 offset1:1\nv_add_u32 v48,2448,v48\ns_add_u32 m0,s57,4096\n" \
"s_add_u32 s97,s15,8192\nbuffer_load_dwordx4 v4,s[8:11],s101 offen lds\nv_add_u32 v49,2448,v49\nv_add_u32 v50,2448,v50\n" \
"v_add_u32 v51,2448,v51\nv_add_u32 v52,2448,v52\nv_add_u32 v53,2448,v53\nv_add_u32 v54,2448,v54\n" \
"v_add_u32 v55,2448,v55\nbuffer_load_dwordx4 v5,s[8:11],s101 offen offset:1024 lds\nds_read2_b64 v[116:119],v60 offset0:2 offset1:3\nv_lshl_add_u32 v4,v110,4,v111\n" \
"v_mfma_f32_16x16x32_f16 v[108:111],v[124:127],v[44:47],0\nds_read2_b64 v[44:47],v3 offset0:6 offset1:7\nv_xor_b32 v5,64,v4\nv_add_u32 v5,31744,v5\n" \
"s_lshr_b32 s26,s3,1\nbuffer_load_dwordx4 v6,s[8:11],s101 offen offset:2048 lds\nv_add_u32 v6,63488,v4\ns_lshl_b32 s26,s26,6\n" \
"s_add_u32 s92,s92,s26\nv_pk_add_f32 v[108:109],v[100:101],v[108:109] neg_lo:[0,1] neg_hi:[0,1]\nv_pk_add_f32 v[110:111],v[102:103],v[110:111] neg_lo:[0,1] neg_hi:[0,1]\nv_pk_fma_f32 v[100:101],v[100:101],2.0,v[108:109] op_sel_hi:[1,0,1] neg_lo:[0,0,1] neg_hi:[0,0,1]\n" \
"v_pk_fma_f32 v[102:103],v[102:103],2.0,v[110:111] op_sel_hi:[1,0,1] neg_lo:[0,0,1] neg_hi:[0,0,1]\nv_cvt_pk_f16_f32 v120,v96,v100\nbuffer_load_dwordx4 v7,s[8:11],s101 offen offset:3072 lds\nv_cvt_pk_f16_f32 v121,v97,v101\n" \
"v_cvt_pk_f16_f32 v122,v98,v102\nv_cvt_pk_f16_f32 v123,v99,v103\nv_cvt_pk_f16_f32 v124,v104,v108\nv_cvt_pk_f16_f32 v125,v105,v109\n" \
"v_mfma_f32_16x16x32_f16 v[96:99],v[16:19],v[120:123],0\nv_cvt_pk_f16_f32 v126,v106,v110\ns_add_u32 m0,s57,8192\ns_lshr_b32 s100,s33,3\n" \
"buffer_load_dwordx4 v1,s[36:39],s31 offen sc1 lds\nv_mfma_f32_16x16x32_f16 v[100:103],v[20:23],v[120:123],0\nv_cvt_pk_f16_f32 v127,v107,v111\nds_read2_b64 v[120:123],v60 offset0:4 offset1:5\n" \
"v_add_u32 v7,63488,v5\nv_mfma_f32_16x16x32_f16 v[104:107],v[24:27],v[124:127],0\ns_lshl_b32 s26,s3,8\ns_add_u32 s92,s92,s26\n" \
"buffer_load_dwordx4 v1,s[36:39],s31 offen offset:1024 sc1 lds\nv_mfma_f32_16x16x32_f16 v[108:111],v[28:31],v[124:127],0\nds_write2_b32 v48,v100,v96 offset0:0 offset1:1\nds_write2_b32 v49,v101,v97 offset0:0 offset1:1\n" \
"ds_write2_b32 v50,v102,v98 offset0:0 offset1:1\nbuffer_load_dwordx4 v1,s[36:39],s31 offen offset:2048 sc1 lds\nds_write2_b32 v51,v103,v99 offset0:0 offset1:1\nds_read2_b64 v[124:127],v60 offset0:6 offset1:7\n" \
"ds_read_b64 v[98:99],v60 offset:64\nv_add_u32 v60,2304,v60\nds_write2_b32 v52,v108,v104 offset0:0 offset1:1\nds_write2_b32 v53,v109,v105 offset0:0 offset1:1\n" \
"buffer_load_dwordx4 v1,s[36:39],s31 offen offset:3072 sc1 lds\nds_write2_b32 v54,v110,v106 offset0:0 offset1:1\nds_write2_b32 v55,v111,v107 offset0:0 offset1:1\nds_read2_b64 v[80:83],v13 offset0:0 offset1:1\n" \
"ds_read2_b64 v[84:87],v13 offset0:2 offset1:3\ns_add_u32 m0,s57,12288\ns_lshl_b32 s92,s92,4\nbuffer_load_dwordx4 v1,s[36:39],s96 offen sc1 lds\n" \
"ds_read2_b64 v[88:91],v13 offset0:4 offset1:5\ns_and_b32 s26,s32,1\nds_read2_b64 v[92:95],v13 offset0:6 offset1:7\ns_lshl3_add_u32 s92,s26,s92\n" \
"s_and_b32 s26,s33,7\ns_add_u32 s92,s92,s26\ns_lshl_b32 s92,s92,8\ns_add_u32 s100,s100,0\n" \
"s_sub_u32 s101,s100,4\nbuffer_load_dwordx4 v1,s[36:39],s96 offen offset:1024 sc1 lds\nds_read_b64 v[96:97],v13 offset:64\nds_read2_b64 v[16:19],v60 offset0:0 offset1:1\n" \
"ds_read2_b64 v[20:23],v60 offset0:2 offset1:3\ns_waitcnt lgkmcnt(6)\nv_pk_fma_f32 v[62:63],v[80:81],v[32:33],v[62:63]\nv_pk_fma_f32 v[64:65],v[82:83],v[34:35],v[64:65]\n" \
"v_pk_mul_f32 v[102:103],v[62:63],v[62:63]\nv_pk_add_f32 v[100:101],v[62:63],v[64:65]\nv_pk_fma_f32 v[102:103],v[64:65],v[64:65],v[102:103]\nbuffer_load_dwordx4 v1,s[36:39],s96 offen offset:2048 sc1 lds\n" \
"s_waitcnt lgkmcnt(5)\nv_pk_fma_f32 v[66:67],v[84:85],v[36:37],v[66:67]\nv_pk_fma_f32 v[68:69],v[86:87],v[38:39],v[68:69]\nv_pk_add_f32 v[100:101],v[100:101],v[66:67]\n" \
"v_pk_fma_f32 v[102:103],v[66:67],v[66:67],v[102:103]\ns_waitcnt lgkmcnt(4)\nv_pk_fma_f32 v[70:71],v[88:89],v[40:41],v[70:71]\nv_pk_add_f32 v[100:101],v[100:101],v[68:69]\n" \
"v_pk_fma_f32 v[102:103],v[68:69],v[68:69],v[102:103]\nbuffer_load_dwordx4 v1,s[36:39],s96 offen offset:3072 sc1 lds\nv_pk_fma_f32 v[72:73],v[90:91],v[42:43],v[72:73]\nv_pk_add_f32 v[100:101],v[100:101],v[70:71]\n" \
"v_pk_fma_f32 v[102:103],v[70:71],v[70:71],v[102:103]\ns_waitcnt lgkmcnt(3)\nv_pk_fma_f32 v[74:75],v[92:93],v[44:45],v[74:75]\nv_pk_add_f32 v[100:101],v[100:101],v[72:73]\n" \
"v_pk_fma_f32 v[102:103],v[72:73],v[72:73],v[102:103]\nv_pk_fma_f32 v[76:77],v[94:95],v[46:47],v[76:77]\ns_add_u32 m0,s57,16384\ns_min_u32 s100,s100,s101\n" \
"buffer_load_dwordx4 v1,s[36:39],s97 offen sc1 lds\nv_pk_add_f32 v[100:101],v[100:101],v[74:75]\nv_pk_fma_f32 v[102:103],v[74:75],v[74:75],v[102:103]\ns_waitcnt lgkmcnt(2)\n" \
"v_pk_fma_f32 v[78:79],v[96:97],v[56:57],v[78:79]\nv_pk_add_f32 v[100:101],v[100:101],v[76:77]\nv_pk_fma_f32 v[102:103],v[76:77],v[76:77],v[102:103]\nv_pk_add_f32 v[100:101],v[100:101],v[78:79]\n" \
"v_pk_fma_f32 v[102:103],v[78:79],v[78:79],v[102:103]\nbuffer_load_dwordx4 v1,s[36:39],s97 offen offset:1024 sc1 lds\nds_read2_b64 v[24:27],v60 offset0:4 offset1:5\nv_add_f32 v100,v100,v101\n" \
"v_add_f32 v102,v102,v103\nds_read2_b64 v[28:31],v60 offset0:6 offset1:7\nds_read_b64 v[58:59],v60 offset:64\nv_permlane16_swap_b32 v100,v102\n" \
"v_add_f32 v100,v100,v102\ns_waitcnt vmcnt(14)\nds_read_b64_tr_b16 a[224:225],v8\nbuffer_load_dwordx4 v1,s[36:39],s97 offen offset:2048 sc1 lds\n" \
"v_add_f32_dpp v100,v100,v100 quad_perm:[1,0,3,2] row_mask:0xf bank_mask:0xf bound_ctrl:1\nds_read_b64_tr_b16 a[228:229],v9\nds_read_b64_tr_b16 a[232:233],v10\nv_add_f32_dpp v100,v100,v100 quad_perm:[2,3,0,1] row_mask:0xf bank_mask:0xf bound_ctrl:1\n" \
"ds_read_b64_tr_b16 a[236:237],v11\nds_read_b64_tr_b16 a[240:241],v8 offset:2048\nv_add_f32_dpp v100,v100,v100 row_half_mirror row_mask:0xf bank_mask:0xf bound_ctrl:1\nds_read_b64_tr_b16 a[244:245],v9 offset:2048\n" \
"buffer_load_dwordx4 v1,s[36:39],s97 offen offset:3072 sc1 lds\nv_add_f32_dpp v100,v100,v100 row_mirror row_mask:0xf bank_mask:0xf bound_ctrl:1\nv_mul_f32 v104,0x3ae38e39,v100\nv_mul_f32 v102,0x3ae38e39,v100\n" \
"ds_read_b64_tr_b16 a[248:249],v10 offset:2048\nds_read_b64_tr_b16 a[252:253],v11 offset:2048\nv_permlane16_swap_b32 v104,v102\nv_fma_f32 v102,-v104,v104,v102\n" \
"s_add_u32 m0,s57,20480\ns_lshl_b32 s101,s100,12\nbuffer_load_dwordx4 v1,s[36:39],s95 offen sc1 lds\nv_max_f32 v102,0,v102\n" \
"v_add_f32 v102,0x3727c5ac,v102\nv_rsq_f32 v100,v102\ns_waitcnt vmcnt(13)\nds_read_b64_tr_b16 a[226:227],v8 offset:4096\n" \
"v_mul_f32 v101,-v104,v100\nv_pk_fma_f32 v[80:81],v[62:63],v[100:101],v[100:101] op_sel:[0,0,1] op_sel_hi:[1,0,1]\nv_pk_fma_f32 v[82:83],v[64:65],v[100:101],v[100:101] op_sel:[0,0,1] op_sel_hi:[1,0,1]\nbuffer_load_dwordx4 v1,s[36:39],s95 offen offset:1024 sc1 lds\n" \
"v_pk_fma_f32 v[84:85],v[66:67],v[100:101],v[100:101] op_sel:[0,0,1] op_sel_hi:[1,0,1]\nv_pk_fma_f32 v[86:87],v[68:69],v[100:101],v[100:101] op_sel:[0,0,1] op_sel_hi:[1,0,1]\nv_pk_fma_f32 v[88:89],v[70:71],v[100:101],v[100:101] op_sel:[0,0,1] op_sel_hi:[1,0,1]\nv_pk_fma_f32 v[90:91],v[72:73],v[100:101],v[100:101] op_sel:[0,0,1] op_sel_hi:[1,0,1]\n" \
"v_pk_fma_f32 v[92:93],v[74:75],v[100:101],v[100:101] op_sel:[0,0,1] op_sel_hi:[1,0,1]\nv_pk_fma_f32 v[94:95],v[76:77],v[100:101],v[100:101] op_sel:[0,0,1] op_sel_hi:[1,0,1]\nv_pk_fma_f32 v[96:97],v[78:79],v[100:101],v[100:101] op_sel:[0,0,1] op_sel_hi:[1,0,1]\nbuffer_load_dwordx4 v1,s[36:39],s95 offen offset:2048 sc1 lds\n" \
"ds_read_b64_tr_b16 a[230:231],v9 offset:4096\nds_read_b64_tr_b16 a[234:235],v10 offset:4096\nds_read_b64_tr_b16 a[238:239],v11 offset:4096\nds_read_b64_tr_b16 a[242:243],v8 offset:6144\n" \
"ds_read_b64_tr_b16 a[246:247],v9 offset:6144\nds_read_b64_tr_b16 a[250:251],v10 offset:6144\nds_read_b64_tr_b16 a[254:255],v11 offset:6144\ns_waitcnt vmcnt(11)\n" \
"ds_read_b64_tr_b16 v[192:193],v15 offset:8192\nds_read_b64_tr_b16 v[196:197],v15 offset:8704\nbuffer_load_dwordx4 v1,s[36:39],s95 offen offset:3072 sc1 lds\nds_read_b64_tr_b16 v[194:195],v15 offset:9216\n" \
"ds_read_b64_tr_b16 v[198:199],v15 offset:9728\nds_read_b64_tr_b16 v[200:201],v15 offset:10240\nds_read_b64_tr_b16 v[204:205],v15 offset:10752\nds_read_b64_tr_b16 v[202:203],v15 offset:11264\n" \
"ds_read_b64_tr_b16 v[206:207],v15 offset:11776\ns_add_u32 s101,s101,s57\ns_add_u32 m0,s101,0\ns_and_b32 s101,s100,1\n" \
"s_lshr_b32 s100,s100,1\ns_lshl_b32 s100,s100,10\ns_add_u32 s100,s100,s101\ns_lshl_b32 s100,s100,7\n" \
"s_add_u32 s100,s100,s92\ns_lshr_b32 s31,s33,3\ns_add_u32 s31,s31,1\ns_sub_u32 s96,s31,4\n" \
"s_min_u32 s31,s31,s96\ns_lshl_b32 s96,s31,12\ns_add_u32 s96,s96,s57\ns_lshr_b32 s97,s33,3\n" \
"s_add_u32 s97,s97,2\ns_sub_u32 s95,s97,4\ns_min_u32 s97,s97,s95\ns_lshl_b32 s95,s97,12\n" \
"s_add_u32 s95,s95,s57\nv_readlane_b32 s24,v112,0\nv_mov_b32 v60,v61\ns_mov_b32 s25,s24\n" \
"v_pk_add_f32 v[112:113],v[112:113],s[24:25] neg_lo:[0,1] neg_hi:[0,1]\ns_waitcnt lgkmcnt(15)\nv_pk_fma_f32 v[80:81],v[80:81],v[16:17],v[112:113]\nv_pk_mul_f32 v[102:103],v[80:81],v[80:81]\n" \
"v_pk_add_f32 v[114:115],v[114:115],s[24:25] neg_lo:[0,1] neg_hi:[0,1]\nv_pk_fma_f32 v[82:83],v[82:83],v[18:19],v[114:115]\nv_pk_add_f32 v[100:101],v[80:81],v[82:83]\nv_pk_fma_f32 v[102:103],v[82:83],v[82:83],v[102:103]\n" \
"v_pk_add_f32 v[116:117],v[116:117],s[24:25] neg_lo:[0,1] neg_hi:[0,1]\nv_pk_fma_f32 v[84:85],v[84:85],v[20:21],v[116:117]\nv_pk_add_f32 v[100:101],v[100:101],v[84:85]\nv_pk_fma_f32 v[102:103],v[84:85],v[84:85],v[102:103]\n" \
"v_pk_add_f32 v[118:119],v[118:119],s[24:25] neg_lo:[0,1] neg_hi:[0,1]\nv_pk_fma_f32 v[86:87],v[86:87],v[22:23],v[118:119]\nv_pk_add_f32 v[100:101],v[100:101],v[86:87]\nv_pk_fma_f32 v[102:103],v[86:87],v[86:87],v[102:103]\n" \
"v_pk_add_f32 v[120:121],v[120:121],s[24:25] neg_lo:[0,1] neg_hi:[0,1]\nv_pk_fma_f32 v[88:89],v[88:89],v[24:25],v[120:121]\nv_pk_add_f32 v[100:101],v[100:101],v[88:89]\nv_pk_fma_f32 v[102:103],v[88:89],v[88:89],v[102:103]\n" \
"v_pk_add_f32 v[122:123],v[122:123],s[24:25] neg_lo:[0,1] neg_hi:[0,1]\nv_pk_fma_f32 v[90:91],v[90:91],v[26:27],v[122:123]\nv_pk_add_f32 v[100:101],v[100:101],v[90:91]\nv_pk_fma_f32 v[102:103],v[90:91],v[90:91],v[102:103]\n" \
"s_waitcnt vmcnt(8)\nds_read_b64_tr_b16 v[208:209],v15 offset:12288\nds_read_b64_tr_b16 v[212:213],v15 offset:12800\nds_read_b64_tr_b16 v[210:211],v15 offset:13312\n" \
"ds_read_b64_tr_b16 v[214:215],v15 offset:13824\nds_read_b64_tr_b16 v[216:217],v15 offset:14336\nds_read_b64_tr_b16 v[220:221],v15 offset:14848\nds_read_b64_tr_b16 v[218:219],v15 offset:15360\n" \
"ds_read_b64_tr_b16 v[222:223],v15 offset:15872\ns_waitcnt lgkmcnt(0)\nbuffer_load_dwordx4 v4,s[40:43],s100 offen lds\nv_pk_add_f32 v[124:125],v[124:125],s[24:25] neg_lo:[0,1] neg_hi:[0,1]\n" \
"v_pk_fma_f32 v[92:93],v[92:93],v[28:29],v[124:125]\nv_pk_add_f32 v[100:101],v[100:101],v[92:93]\nv_pk_fma_f32 v[102:103],v[92:93],v[92:93],v[102:103]\nv_pk_add_f32 v[126:127],v[126:127],s[24:25] neg_lo:[0,1] neg_hi:[0,1]\n" \
"v_pk_fma_f32 v[94:95],v[94:95],v[30:31],v[126:127]\nv_pk_add_f32 v[100:101],v[100:101],v[94:95]\nbuffer_load_dwordx4 v5,s[40:43],s100 offen offset:1024 lds\nv_pk_fma_f32 v[102:103],v[94:95],v[94:95],v[102:103]\n" \
"v_pk_add_f32 v[98:99],v[98:99],s[24:25] neg_lo:[0,1] neg_hi:[0,1]\nv_pk_fma_f32 v[96:97],v[96:97],v[58:59],v[98:99]\nv_pk_add_f32 v[100:101],v[100:101],v[96:97]\nv_pk_fma_f32 v[102:103],v[96:97],v[96:97],v[102:103]\n" \
"v_add_f32 v100,v100,v101\nv_add_f32 v102,v102,v103\nv_pk_add_f32 v[62:63],v[62:63],v[60:61] op_sel_hi:[1,0]\nbuffer_load_dwordx4 v6,s[40:43],s100 offen offset:2048 lds\n" \
"v_permlane16_swap_b32 v100,v102\nv_add_f32_e64 v100,v100,v102\nv_cvt_pk_bf16_f32 v32,v62,v63\nv_pk_add_f32 v[64:65],v[64:65],v[60:61] op_sel_hi:[1,0]\n" \
"v_add_f32_dpp v100,v100,v100 quad_perm:[1,0,3,2] row_mask:0xf bank_mask:0xf bound_ctrl:1\nv_cvt_pk_bf16_f32 v33,v64,v65\nv_pk_add_f32 v[66:67],v[66:67],v[60:61] op_sel_hi:[1,0]\nbuffer_load_dwordx4 v7,s[40:43],s100 offen offset:3072 lds\n" \
"v_add_f32_dpp v100,v100,v100 quad_perm:[2,3,0,1] row_mask:0xf bank_mask:0xf bound_ctrl:1\ns_add_u32 m0,s96,0\ns_and_b32 s96,s31,1\nv_add_f32_dpp v100,v100,v100 row_half_mirror row_mask:0xf bank_mask:0xf bound_ctrl:1\n" \
"s_lshr_b32 s31,s31,1\ns_lshl_b32 s31,s31,10\nv_add_f32_dpp v100,v100,v100 row_mirror row_mask:0xf bank_mask:0xf bound_ctrl:1\nv_mul_f32 v104,0x3ae38e39,v100\n" \
"s_add_u32 s31,s31,s96\ns_lshl_b32 s31,s31,7\ns_add_u32 s31,s31,s92\nbuffer_load_dwordx4 v4,s[40:43],s31 offen lds\n" \
"v_mul_f32 v102,0x3ae38e39,v100\ns_lshr_b32 s100,s33,3\ns_add_u32 s100,s100,3\nv_permlane16_swap_b32 v104,v102\n" \
"v_fma_f32 v102,-v104,v104,v102\nv_max_f32 v102,0,v102\nv_add_f32 v102,0x3727c5ac,v102\nbuffer_load_dwordx4 v5,s[40:43],s31 offen offset:1024 lds\n" \
"v_rsq_f32 v100,v102\ns_sub_u32 s101,s100,4\nv_mul_f32 v101,-v104,v100\nv_pk_fma_f32 v[80:81],v[80:81],v[100:101],v[100:101] op_sel:[0,0,1] op_sel_hi:[1,0,1]\n" \
"v_pk_fma_f32 v[82:83],v[82:83],v[100:101],v[100:101] op_sel:[0,0,1] op_sel_hi:[1,0,1]\nv_pk_fma_f32 v[84:85],v[84:85],v[100:101],v[100:101] op_sel:[0,0,1] op_sel_hi:[1,0,1]\nv_pk_fma_f32 v[86:87],v[86:87],v[100:101],v[100:101] op_sel:[0,0,1] op_sel_hi:[1,0,1]\nbuffer_load_dwordx4 v6,s[40:43],s31 offen offset:2048 lds\n" \
"v_cvt_pk_bf16_f32 v16,v80,v81\nv_cvt_pk_bf16_f32 v17,v82,v83\nv_cvt_pk_bf16_f32 v18,v84,v85\nv_cvt_pk_bf16_f32 v19,v86,v87\n" \
"v_pk_fma_f32 v[88:89],v[88:89],v[100:101],v[100:101] op_sel:[0,0,1] op_sel_hi:[1,0,1]\nv_pk_fma_f32 v[90:91],v[90:91],v[100:101],v[100:101] op_sel:[0,0,1] op_sel_hi:[1,0,1]\nv_pk_fma_f32 v[92:93],v[92:93],v[100:101],v[100:101] op_sel:[0,0,1] op_sel_hi:[1,0,1]\nbuffer_load_dwordx4 v7,s[40:43],s31 offen offset:3072 lds\n" \
"v_pk_fma_f32 v[94:95],v[94:95],v[100:101],v[100:101] op_sel:[0,0,1] op_sel_hi:[1,0,1]\nv_cvt_pk_bf16_f32 v20,v88,v89\nv_cvt_pk_bf16_f32 v21,v90,v91\nv_cvt_pk_bf16_f32 v22,v92,v93\n" \
"v_cvt_pk_bf16_f32 v23,v94,v95\nv_pk_fma_f32 v[96:97],v[96:97],v[100:101],v[100:101] op_sel:[0,0,1] op_sel_hi:[1,0,1]\nv_cvt_pk_bf16_f32 v24,v96,v97\ns_add_u32 m0,s95,0\n" \
"s_and_b32 s95,s97,1\ns_lshr_b32 s97,s97,1\ns_lshl_b32 s97,s97,10\ns_add_u32 s97,s97,s95\n" \
"s_lshl_b32 s97,s97,7\ns_add_u32 s97,s97,s92\nbuffer_load_dwordx4 v4,s[40:43],s97 offen lds\ns_min_u32 s100,s100,s101\n" \
"s_lshl_b32 s101,s100,12\ns_add_u32 s101,s101,s57\nv_cvt_pk_bf16_f32 v34,v66,v67\nv_pk_add_f32 v[68:69],v[68:69],v[60:61] op_sel_hi:[1,0]\n" \
"v_cvt_pk_bf16_f32 v35,v68,v69\nv_pk_add_f32 v[70:71],v[70:71],v[60:61] op_sel_hi:[1,0]\nv_cvt_pk_bf16_f32 v36,v70,v71\nbuffer_load_dwordx4 v5,s[40:43],s97 offen offset:1024 lds\n" \
"v_pk_add_f32 v[72:73],v[72:73],v[60:61] op_sel_hi:[1,0]\nv_cvt_pk_bf16_f32 v37,v72,v73\nv_pk_add_f32 v[74:75],v[74:75],v[60:61] op_sel_hi:[1,0]\nv_cvt_pk_bf16_f32 v38,v74,v75\n" \
"v_pk_add_f32 v[76:77],v[76:77],v[60:61] op_sel_hi:[1,0]\nv_cvt_pk_bf16_f32 v39,v76,v77\nv_pk_add_f32 v[78:79],v[78:79],v[60:61] op_sel_hi:[1,0]\nbuffer_load_dwordx4 v6,s[40:43],s97 offen offset:2048 lds\n" \
"v_cvt_pk_bf16_f32 v40,v78,v79\nv_and_b32 v48,31,v0\nv_mad_u32_u24 v49,v48,56,0\nv_sub_u32 v48,v13,v49\n" \
"ds_write_b128 v48,v[16:19]\nbuffer_load_dwordx4 v7,s[40:43],s97 offen offset:3072 lds\nds_write_b128 v48,v[20:23] offset:512\ns_add_u32 m0,s101,0\n" \
"s_and_b32 s101,s100,1\ns_lshr_b32 s100,s100,1\ns_lshl_b32 s100,s100,10\ns_add_u32 s100,s100,s101\n" \
"s_lshl_b32 s100,s100,7\ns_add_u32 s100,s100,s92\nbuffer_load_dwordx4 v4,s[40:43],s100 offen lds\nv_and_b32 v49,31,v0\n" \
"v_mul_u32_u24 v49,68,v49\nv_sub_u32 v49,v13,v49\nds_write_b32 v49,v24 offset:1024\nv_bfe_u32 v50,v0,2,2\n" \
"v_mul_u32_u24 v50,4896,v50\nv_bfe_u32 v51,v0,4,2\nv_lshl_add_u32 v51,v51,2,s3\nbuffer_load_dwordx4 v5,s[40:43],s100 offen offset:1024 lds\n" \
"v_lshl_add_u32 v50,v51,5,v50\nv_and_b32 v52,3,v0\nv_lshl_add_u32 v50,v52,3,v50\nv_and_b32 v52,0xf0,v1\n" \
"v_lshl_add_u32 v51,v51,16,v52\nv_bfrev_b32 v52,1\nv_cmp_gt_u32 vcc,256,v1\nv_and_b32 v53,31,v0\n" \
"buffer_load_dwordx4 v6,s[40:43],s100 offen offset:2048 lds\ns_waitcnt lgkmcnt(0)\ns_barrier\nds_read_b64_tr_b16 v[112:113],v50 offset:9216\n" \
"ds_read_b64_tr_b16 v[114:115],v50 offset:11664\nds_read_b64_tr_b16 v[116:117],v50 offset:9728\nds_read_b64_tr_b16 v[118:119],v50 offset:12176\nds_read_b64_tr_b16 v[120:121],v50 offset:10240\n" \
"ds_read_b64_tr_b16 v[122:123],v50 offset:12688\nbuffer_load_dwordx4 v7,s[40:43],s100 offen offset:3072 lds\nv_cndmask_b32 v52,v52,v51,vcc\nv_mul_u32_u24 v53,12,v53\n" \
"v_sub_u32 v53,v12,v53\ns_lshl_b32 s24,s34,8\ns_add_u32 s24,s24,9764864\ns_add_u32 s25,s24,1048576\n" \
"s_add_u32 s26,s24,2097152\ns_add_u32 s13,s13,7405568\ns_load_dwordx2 s[4:5],s[0:1],56\ns_load_dwordx2 s[16:17],s[0:1],64\n" \
"s_waitcnt lgkmcnt(0)\nbuffer_store_dwordx4 v[112:115],v51,s[36:39],s24 offen sc1\nbuffer_store_dwordx4 v[116:119],v51,s[36:39],s25 offen sc1\nbuffer_store_dwordx4 v[120:123],v52,s[36:39],s26 offen sc1\n" \
"buffer_store_dwordx4 v[32:35],v12,s[36:39],s13 offen sc1\nbuffer_store_dwordx4 v[36:39],v12,s[36:39],s13 offen offset:512 sc1\nbuffer_store_dword v40,v53,s[36:39],s13 offen offset:1024 sc1\ns_lshr_b32 s31,s33,3\n" \
"s_lshl1_add_u32 s31,s31,0\ns_sub_u32 s96,s31,7\ns_min_u32 s31,s31,s96\ns_lshl_b32 s96,s31,12\n" \
"s_add_u32 s96,s96,s57\ns_add_u32 m0,s96,0\ns_and_b32 s96,s31,1\ns_lshr_b32 s31,s31,1\n" \
"s_lshl_b32 s31,s31,10\ns_add_u32 s31,s31,s96\ns_lshl_b32 s31,s31,7\ns_add_u32 s31,s31,s92\n" \
"s_add_u32 s31,s31,262144\ns_lshr_b32 s97,s33,3\ns_lshl1_add_u32 s97,s97,1\ns_sub_u32 s95,s97,7\n" \
"s_min_u32 s97,s97,s95\ns_lshl_b32 s95,s97,12\ns_add_u32 s95,s95,s57\ns_lshr_b32 s100,s33,3\n" \
"s_lshl1_add_u32 s100,s100,2\ns_sub_u32 s101,s100,7\ns_min_u32 s100,s100,s101\ns_lshl_b32 s101,s100,12\n" \
"s_add_u32 s101,s101,s57\nv_and_b32 v64,63,v0\ns_and_b32 s25,s33,7\nv_lshrrev_b32 v66,5,v64\n" \
"v_and_b32 v67,1,v0\ns_lshl2_add_u32 s25,s25,s3\ns_and_b32 s26,s33,6\ns_lshr_b32 s27,s3,1\n" \
"v_lshl_add_u32 v66,v66,1,v67\ns_mul_i32 s24,s32,1152\ns_lshl_b32 s25,s25,5\ns_add_u32 s26,s26,s27\n" \
"s_mov_b32 s6,9216\ns_mov_b32 s7,0x20000\nv_lshlrev_b32 v68,3,v66\ns_add_u32 s25,s25,s24\n" \
"v_bfe_u32 v72,v0,1,1\nv_lshrrev_b32 v74,2,v64\ns_lshl4_add_u32 s26,s26,s24\nv_and_b32 v73,12,v0\n" \
"v_lshrrev_b32 v71,4,v64\nv_and_b32 v69,7,v0\nbuffer_load_dwordx2 v[80:81],v68,s[4:7],s25 offen\nv_lshl_add_u32 v72,v72,2,s21\n" \
"v_and_b32 v74,12,v74\nv_add_u32 v71,v71,v73\ns_lshl_b32 s24,s3,4\nv_lshlrev_b32 v70,1,v69\n" \
"v_lshl_add_u32 v72,v72,4,v74\ns_mov_b32 s15,s19\nv_add_u32 v71,s24,v71\nbuffer_load_ushort v82,v70,s[4:7],s26 offen offset:1024\n" \
"s_mov_b32 s18,4096\ns_mov_b32 s19,0x20000\nv_lshlrev_b32 v74,1,v71\ns_lshl_b32 s24,s22,8\n" \
"buffer_load_ushort v83,v74,s[16:19],s24 offen\nv_add_u32 v72,s3,v72\nbuffer_load_ushort v84,v74,s[16:19],s24 offen offset:128\nv_lshlrev_b32_e64 v75,1,v72\n" \
"buffer_load_ushort v85,v75,s[16:19],s24 offen\nv_lshl_add_u32 v73,v67,1,v73\nv_mul_u32_u24 v91,1152,v72\nv_lshl_add_u32 v91,v73,1,v91\n" \
"v_lshlrev_b32_e64 v73,2,v73\nv_add_u32 v73,163712,v73\nds_read_b64 v[86:87],v73\ns_mul_i32 s24,s22,147456\n" \
"s_mul_i32 s25,s20,288\nv_bfe_u32 v58,v0,1,1\ns_add_u32 s24,s24,s25\nv_mul_u32_u24 v58,544,v58\n" \
"v_bfe_u32 v75,v0,2,1\ns_lshl_b32 s25,s21,6\nv_lshl_add_u32 v58,v75,8,v58\ns_add_u32 s25,s25,s24\n" \
"v_bfe_u32 v75,v0,3,2\ns_add_u32 s24,s24,256\nv_lshl_add_u32 v58,v75,6,v58\nv_add_u32 v91,s24,v91\n" \
"v_mul_u32_u24 v90,1152,v71\nv_and_b32 v74,3,v0\nv_lshl_add_u32 v58,v66,4,v58\ns_mul_i32 s24,s3,1056\n" \
"v_lshl_add_u32 v90,v74,3,v90\nv_add_u32 v58,s24,v58\nv_add_u32 v90,s25,v90\ns_and_b32 s24,s3,1\n" \
"v_mul_u32_u24 v60,288,v75\ns_lshl_b32 s24,s24,2\ns_lshr_b32 s25,s3,1\nv_lshrrev_b32 v75,5,v64\n" \
"s_add_u32 s24,s24,s25\nv_lshl_add_u32 v60,v75,6,v60\ns_lshl_b32 s24,s24,5\nv_lshl_add_u32 v60,v69,2,v60\n" \
"s_add_u32 s24,s24,4224\nv_add_u32 v60,s24,v60\ns_bfe_u32 s24,s33,0x10001\ns_add_u32 s24,s24,s15\n" \
"s_add_u32 s24,s24,17\ns_lshl2_add_u32 s24,s24,s3\ns_waitcnt lgkmcnt(0)\nv_lshrrev_b32 v86,16,v86\n" \
"s_lshl_b32 s24,s24,7\nv_mov_b32 v92,v4\nv_mov_b32 v93,v5\nv_add_u32 v74,s57,v1\n" \
"v_or_b32 v95,v86,v87\nv_lshl_add_u32 v62,v64,1,s24\nv_bfrev_b32 v76,1\nv_cmp_eq_u32 vcc,0,v64\n" \
"s_lshl2_add_u32 s24,s34,s3\ns_lshl2_add_u32 s24,s24,65536\nv_cndmask_b32 v76,v76,0,vcc\nv_mov_b32 v77,1\n" \
"s_barrier\nv_mov_b32 v57,0xc0135761\nv_add_u32 v59,0x80000,v1\nv_lshl_add_u32 v61,v64,3,s23\n" \
"v_mov_b32 v63,0\ns_waitcnt vmcnt(31)\nds_read_b64_tr_b16 v[224:225],v15 offset:16384\nds_read_b64_tr_b16 v[228:229],v15 offset:16896\n" \
"ds_read_b64_tr_b16 v[226:227],v15 offset:17408\nds_read_b64_tr_b16 v[230:231],v15 offset:17920\nds_read_b64_tr_b16 v[232:233],v15 offset:18432\nds_read_b64_tr_b16 v[236:237],v15 offset:18944\n" \
"ds_read_b64_tr_b16 v[234:235],v15 offset:19456\nds_read_b64_tr_b16 v[238:239],v15 offset:19968\ns_waitcnt vmcnt(27)\nds_read_b64_tr_b16 v[240:241],v15 offset:20480\n" \
"ds_read_b64_tr_b16 v[244:245],v15 offset:20992\nds_read_b64_tr_b16 v[242:243],v15 offset:21504\nds_read_b64_tr_b16 v[246:247],v15 offset:22016\nds_read_b64_tr_b16 v[248:249],v15 offset:22528\n" \
"v_bfe_u32 v100,v0,4,2\nds_read_b64_tr_b16 v[252:253],v15 offset:23040\nds_read_b64_tr_b16 v[250:251],v15 offset:23552\nv_bfe_u32 v101,v0,2,2\n" \
"v_lshl_add_u32 v100,v100,3,v101\nv_bfe_u32 v101,v0,3,2\nv_lshl_add_u32 v100,v100,2,v101\nv_and_b32 v101,3,v0\n" \
"v_lshl_add_u32 v100,v100,2,v101\nv_lshlrev_b32 v100,3,v100\nv_xor_b32 v8,0,v100\nv_add_u32 v8,s57,v8\n" \
"v_mov_b32 v88,v8\ns_waitcnt vmcnt(5)\nbuffer_store_dword v77,v76,s[36:39],s24 offen sc1\nds_read_b64_tr_b16 v[254:255],v15 offset:24064\n" \
"v_xor_b32 v9,32,v100\nv_add_u32 v9,s57,v9\nv_xor_b32 v10,64,v100\nv_add_u32 v10,s57,v10\n" \
"v_xor_b32 v11,96,v100\nv_add_u32 v11,s57,v11\ns_waitcnt vmcnt(5)\nv_and_b32 v53,0xffff0000,v80\n" \
"v_and_b32 v55,0xffff0000,v81\nv_lshlrev_b32 v52,16,v80\nv_lshlrev_b32 v54,16,v81\ns_waitcnt vmcnt(4)\n" \
"v_lshlrev_b32 v56,16,v82\ns_waitcnt vmcnt(2)\nv_lshl_or_b32 v89,v84,16,v83\nds_write_b128 v74,v[88:91] offset:28672\n" \
"s_waitcnt vmcnt(1)\nv_lshlrev_b32_e64 v94,16,v85\nds_write_b128 v74,v[92:95] offset:29696\nds_read_b64_tr_b16 v[128:129],v8\n" \
"ds_read_b64_tr_b16 v[130:131],v8 offset:512\nds_read_b64_tr_b16 v[132:133],v9\nds_read_b64_tr_b16 v[134:135],v9 offset:512\nds_read_b64_tr_b16 v[136:137],v10\n" \
"ds_read_b64_tr_b16 v[138:139],v10 offset:512\nds_read_b64_tr_b16 v[140:141],v11\nds_read_b64_tr_b16 v[142:143],v11 offset:512\nds_read_b64_tr_b16 v[144:145],v8 offset:4096\n" \
"ds_read_b64_tr_b16 v[146:147],v8 offset:4608\nds_read_b64_tr_b16 v[148:149],v9 offset:4096\nds_read_b64_tr_b16 v[150:151],v9 offset:4608\nds_read_b64_tr_b16 v[152:153],v10 offset:4096\n" \
"ds_read_b64_tr_b16 v[154:155],v10 offset:4608\nds_read_b64_tr_b16 v[156:157],v11 offset:4096\nds_read_b64_tr_b16 v[158:159],v11 offset:4608\nds_read_b64_tr_b16 v[160:161],v8 offset:8192\n" \
"ds_read_b64_tr_b16 v[162:163],v8 offset:8704\nds_read_b64_tr_b16 v[164:165],v9 offset:8192\nds_read_b64_tr_b16 v[166:167],v9 offset:8704\nds_read_b64_tr_b16 v[168:169],v10 offset:8192\n" \
"ds_read_b64_tr_b16 v[170:171],v10 offset:8704\nds_read_b64_tr_b16 v[172:173],v11 offset:8192\nds_read_b64_tr_b16 v[174:175],v11 offset:8704\nds_read_b64_tr_b16 v[176:177],v8 offset:12288\n" \
"ds_read_b64_tr_b16 v[178:179],v8 offset:12800\nds_read_b64_tr_b16 v[180:181],v9 offset:12288\nds_read_b64_tr_b16 v[182:183],v9 offset:12800\nds_read_b64_tr_b16 v[184:185],v10 offset:12288\n" \
"ds_read_b64_tr_b16 v[186:187],v10 offset:12800\nds_read_b64_tr_b16 v[188:189],v11 offset:12288\nds_read_b64_tr_b16 v[190:191],v11 offset:12800\ns_waitcnt lgkmcnt(0)\n" \
"buffer_load_dwordx4 v4,s[40:43],s31 offen lds\nbuffer_load_dwordx4 v5,s[40:43],s31 offen offset:1024 lds\nbuffer_load_dwordx4 v6,s[40:43],s31 offen offset:2048 lds\nbuffer_load_dwordx4 v7,s[40:43],s31 offen offset:3072 lds\n" \
"s_add_u32 m0,s95,0\ns_and_b32 s95,s97,1\ns_lshr_b32 s97,s97,1\ns_lshl_b32 s97,s97,10\n" \
"s_add_u32 s97,s97,s95\ns_lshl_b32 s97,s97,7\ns_add_u32 s97,s97,s92\ns_add_u32 s97,s97,262144\n" \
"buffer_load_dwordx4 v4,s[40:43],s97 offen lds\nbuffer_load_dwordx4 v5,s[40:43],s97 offen offset:1024 lds\nbuffer_load_dwordx4 v6,s[40:43],s97 offen offset:2048 lds\nbuffer_load_dwordx4 v7,s[40:43],s97 offen offset:3072 lds\n" \
"s_add_u32 m0,s101,0\ns_and_b32 s101,s100,1\ns_lshr_b32 s100,s100,1\ns_lshl_b32 s100,s100,10\n" \
"s_add_u32 s100,s100,s101\ns_lshl_b32 s100,s100,7\ns_add_u32 s100,s100,s92\ns_add_u32 s100,s100,262144\n" \
"buffer_load_dwordx4 v4,s[40:43],s100 offen lds\nbuffer_load_dwordx4 v5,s[40:43],s100 offen offset:1024 lds\nbuffer_load_dwordx4 v6,s[40:43],s100 offen offset:2048 lds\nbuffer_load_dwordx4 v7,s[40:43],s100 offen offset:3072 lds\n" \
"s_lshr_b32 s31,s33,3\ns_lshl1_add_u32 s31,s31,3\ns_sub_u32 s96,s31,7\ns_min_u32 s31,s31,s96\n" \
"s_lshl_b32 s96,s31,12\ns_add_u32 s96,s96,s57\ns_add_u32 m0,s96,0\ns_and_b32 s96,s31,1\n" \
"s_lshr_b32 s31,s31,1\ns_lshl_b32 s31,s31,10\ns_add_u32 s31,s31,s96\ns_lshl_b32 s31,s31,7\n" \
"s_add_u32 s31,s31,s92\ns_add_u32 s31,s31,262144\nbuffer_load_dwordx4 v4,s[40:43],s31 offen lds\nbuffer_load_dwordx4 v5,s[40:43],s31 offen offset:1024 lds\n" \
"buffer_load_dwordx4 v6,s[40:43],s31 offen offset:2048 lds\nbuffer_load_dwordx4 v7,s[40:43],s31 offen offset:3072 lds\ns_lshr_b32 s97,s33,3\ns_lshl1_add_u32 s97,s97,4\n" \
"s_sub_u32 s95,s97,7\ns_min_u32 s97,s97,s95\ns_lshl_b32 s95,s97,12\ns_add_u32 s95,s95,s57\n" \
"s_add_u32 m0,s95,0\ns_and_b32 s95,s97,1\ns_lshr_b32 s97,s97,1\ns_lshl_b32 s97,s97,10\n" \
"s_add_u32 s97,s97,s95\ns_lshl_b32 s97,s97,7\ns_add_u32 s97,s97,s92\ns_add_u32 s97,s97,262144\n" \
"buffer_load_dwordx4 v4,s[40:43],s97 offen lds\nbuffer_load_dwordx4 v5,s[40:43],s97 offen offset:1024 lds\nbuffer_load_dwordx4 v6,s[40:43],s97 offen offset:2048 lds\nbuffer_load_dwordx4 v7,s[40:43],s97 offen offset:3072 lds\n" \
"s_lshr_b32 s100,s33,3\ns_lshl1_add_u32 s100,s100,5\ns_sub_u32 s101,s100,7\ns_min_u32 s100,s100,s101\n" \
"s_lshl_b32 s101,s100,12\ns_add_u32 s101,s101,s57\ns_add_u32 m0,s101,0\ns_and_b32 s101,s100,1\n" \
"s_lshr_b32 s100,s100,1\ns_lshl_b32 s100,s100,10\ns_add_u32 s100,s100,s101\ns_lshl_b32 s100,s100,7\n" \
"s_add_u32 s100,s100,s92\ns_add_u32 s100,s100,262144\nbuffer_load_dwordx4 v4,s[40:43],s100 offen lds\nbuffer_load_dwordx4 v5,s[40:43],s100 offen offset:1024 lds\n" \
"buffer_load_dwordx4 v6,s[40:43],s100 offen offset:2048 lds\nbuffer_load_dwordx4 v7,s[40:43],s100 offen offset:3072 lds\ns_lshr_b32 s31,s33,3\ns_lshl1_add_u32 s31,s31,6\n" \
"s_sub_u32 s96,s31,7\ns_min_u32 s31,s31,s96\ns_lshl_b32 s96,s31,12\ns_add_u32 s96,s96,s57\n" \
"s_add_u32 m0,s96,0\ns_and_b32 s96,s31,1\ns_lshr_b32 s31,s31,1\ns_lshl_b32 s31,s31,10\n" \
"s_add_u32 s31,s31,s96\ns_lshl_b32 s31,s31,7\ns_add_u32 s31,s31,s92\ns_add_u32 s31,s31,262144\n" \
"buffer_load_dwordx4 v4,s[40:43],s31 offen lds\nbuffer_load_dwordx4 v5,s[40:43],s31 offen offset:1024 lds\nbuffer_load_dwordx4 v6,s[40:43],s31 offen offset:2048 lds\nbuffer_load_dwordx4 v7,s[40:43],s31 offen offset:3072 lds\n" \
".Lgemm_%=:\ns_add_u32 s4,s36,s48\ns_addc_u32 s5,s37,0\ns_add_u32 s6,s36,s49\n" \
"s_addc_u32 s7,s37,0\ns_add_u32 s8,s36,s50\ns_addc_u32 s9,s37,0\ns_add_u32 s10,s36,s51\n" \
"s_addc_u32 s11,s37,0\ns_add_u32 s12,s36,s68\ns_addc_u32 s13,s37,0\ns_add_u32 s14,s36,s69\n" \
"s_addc_u32 s15,s37,0\ns_add_u32 s16,s36,s70\ns_addc_u32 s17,s37,0\ns_memrealtime s[94:95]\n" \
"global_load_dwordx4 v[64:67],v59,s[4:5] sc1\nglobal_load_dwordx4 v[68:71],v59,s[4:5] offset:1024 sc1\nglobal_load_dwordx4 v[72:75],v59,s[4:5] offset:2048 sc1\nglobal_load_dwordx4 v[76:79],v59,s[4:5] offset:3072 sc1\n" \
"global_load_dwordx4 v[80:83],v59,s[6:7] sc1\nglobal_load_dwordx4 v[84:87],v59,s[6:7] offset:1024 sc1\nglobal_load_dwordx4 v[88:91],v59,s[6:7] offset:2048 sc1\nglobal_load_dwordx4 v[92:95],v59,s[6:7] offset:3072 sc1\n" \
"global_load_dwordx4 v[96:99],v59,s[8:9] sc1\nglobal_load_dwordx4 v[100:103],v59,s[8:9] offset:1024 sc1\nglobal_load_dwordx4 v[104:107],v59,s[8:9] offset:2048 sc1\nglobal_load_dwordx4 v[108:111],v59,s[8:9] offset:3072 sc1\n" \
"global_load_dwordx4 v[112:115],v59,s[10:11] sc1\nglobal_load_dwordx4 v[116:119],v59,s[10:11] offset:1024 sc1\nglobal_load_dwordx4 v[120:123],v59,s[10:11] offset:2048 sc1\nglobal_load_dwordx4 v[124:127],v59,s[10:11] offset:3072 sc1\n" \
"s_branch .Lz0_%=\n.Lzr_%=:\nv_and_b32 v0,0x3ff,v59\nv_lshlrev_b32 v0,0,v0\n" \
"v_bfrev_b32 v1,1\ns_waitcnt lgkmcnt(0)\n.Lh_%=:\nbuffer_load_dwordx4 v[4:7],v0,s[36:39],s87 offen sc1\n" \
"s_memrealtime s[96:97]\ns_waitcnt vmcnt(0) lgkmcnt(0)\nv_bitop3_b32 v4,v4,v5,v6 bitop3:0x80\nv_and_b32 v4,v4,v7\n" \
"v_cmp_ne_u32 vcc,0,v4\nv_max3_u32 v2,v64,v68,v72\nv_max3_u32 v2,v2,v76,v80\nv_max3_u32 v2,v2,v84,v88\n" \
"v_max3_u32 v2,v2,v92,v96\nv_max3_u32 v2,v2,v100,v104\nv_max3_u32 v2,v2,v108,v112\nv_max3_u32 v2,v2,v116,v120\n" \
"v_max_u32 v2,v2,v124\nv_cndmask_b32 v0,v0,v1,vcc\nv_cmp_eq_u32 vcc,-1,v2\nv_cmp_ne_u32 s[100:101],v0,v1\n" \
"s_sub_u32 s96,s96,s94\ns_cbranch_vccz .Lj_%=\nv_cmpx_eq_u32 vcc,-1,v64\nglobal_load_dwordx4 v[64:67],v59,s[4:5] sc1\n" \
"s_mov_b64 exec,-1\nv_cmpx_eq_u32 vcc,-1,v68\nglobal_load_dwordx4 v[68:71],v59,s[4:5] offset:1024 sc1\ns_mov_b64 exec,-1\n" \
"v_cmpx_eq_u32 vcc,-1,v72\nglobal_load_dwordx4 v[72:75],v59,s[4:5] offset:2048 sc1\ns_mov_b64 exec,-1\nv_cmpx_eq_u32 vcc,-1,v76\n" \
"global_load_dwordx4 v[76:79],v59,s[4:5] offset:3072 sc1\ns_mov_b64 exec,-1\nv_cmpx_eq_u32 vcc,-1,v80\nglobal_load_dwordx4 v[80:83],v59,s[6:7] sc1\n" \
"s_mov_b64 exec,-1\nv_cmpx_eq_u32 vcc,-1,v84\nglobal_load_dwordx4 v[84:87],v59,s[6:7] offset:1024 sc1\ns_mov_b64 exec,-1\n" \
"v_cmpx_eq_u32 vcc,-1,v88\nglobal_load_dwordx4 v[88:91],v59,s[6:7] offset:2048 sc1\ns_mov_b64 exec,-1\nv_cmpx_eq_u32 vcc,-1,v92\n" \
"global_load_dwordx4 v[92:95],v59,s[6:7] offset:3072 sc1\ns_mov_b64 exec,-1\nv_cmpx_eq_u32 vcc,-1,v96\nglobal_load_dwordx4 v[96:99],v59,s[8:9] sc1\n" \
"s_mov_b64 exec,-1\nv_cmpx_eq_u32 vcc,-1,v100\nglobal_load_dwordx4 v[100:103],v59,s[8:9] offset:1024 sc1\ns_mov_b64 exec,-1\n" \
"v_cmpx_eq_u32 vcc,-1,v104\nglobal_load_dwordx4 v[104:107],v59,s[8:9] offset:2048 sc1\ns_mov_b64 exec,-1\nv_cmpx_eq_u32 vcc,-1,v108\n" \
"global_load_dwordx4 v[108:111],v59,s[8:9] offset:3072 sc1\ns_mov_b64 exec,-1\nv_cmpx_eq_u32 vcc,-1,v112\nglobal_load_dwordx4 v[112:115],v59,s[10:11] sc1\n" \
"s_mov_b64 exec,-1\nv_cmpx_eq_u32 vcc,-1,v116\nglobal_load_dwordx4 v[116:119],v59,s[10:11] offset:1024 sc1\ns_mov_b64 exec,-1\n" \
"v_cmpx_eq_u32 vcc,-1,v120\nglobal_load_dwordx4 v[120:123],v59,s[10:11] offset:2048 sc1\ns_mov_b64 exec,-1\nv_cmpx_eq_u32 vcc,-1,v124\n" \
"global_load_dwordx4 v[124:127],v59,s[10:11] offset:3072 sc1\ns_mov_b64 exec,-1\n.Lj_%=:\ns_cmp_gt_u32 s96,0x4c4b40\n" \
"s_cbranch_scc1 .Lg_%=\ns_cmp_lg_u64 s[100:101],0\ns_cbranch_scc1 .Lh_%=\n.Lg_%=:\n" \
"s_waitcnt vmcnt(0)\ns_mov_b32 s33,s86\ns_nop 0\ns_mov_b32 m0,s56\n" \
"s_and_b32 s95,s89,7\ns_andn2_b32 s101,s89,7\ns_mul_i32 s95,s95,409600\ns_add_u32 s95,s95,s52\n" \
"s_mov_b32 s86,0x11111111\ns_mov_b32 s87,0x11111111\nv_add_u32 v59,s85,v59\ns_add_u32 s100,s90,73728\n" \
"s_mov_b32 s35,73728\ns_mov_b32 s34,s91\ns_waitcnt lgkmcnt(0)\ns_cmp_eq_u32 s84,0\n" \
"s_cbranch_scc0 .Ls_%=\n.Lf_%=:\ns_waitcnt vmcnt(15)\nv_mfma_f32_16x16x32_bf16 v[0:3],v[64:67],a[0:3],0\n" \
"ds_read_b32 v44,v60\ns_waitcnt vmcnt(14)\nv_mfma_f32_16x16x32_bf16 v[0:3],v[68:71],a[16:19],v[0:3]\nds_read_b32 v45,v60 offset:9568\n" \
"v_add_f32 v40,1.0,v40\nv_mfma_f32_16x16x32_bf16 v[4:7],v[64:67],a[4:7],0\nds_read_b32 v46,v60 offset:19136\nv_add_u32 v59,0x10000,v59\n" \
"v_mfma_f32_16x16x32_bf16 v[4:7],v[68:71],a[20:23],v[4:7]\nds_read_b32 v47,v60 offset:28704\nv_and_b32 v59,0x7ffff,v59\nv_mfma_f32_16x16x32_bf16 v[8:11],v[64:67],a[8:11],0\n" \
"v_add_f32 v41,1.0,v41\nv_add_f32 v42,1.0,v42\nv_mfma_f32_16x16x32_bf16 v[8:11],v[68:71],a[24:27],v[8:11]\nv_add_f32 v43,1.0,v43\n" \
"ds_write_addtid_b32 v20 offset:5344\nv_mfma_f32_16x16x32_bf16 v[12:15],v[64:67],a[12:15],0\nv_rcp_f32 v40,v40\nv_mfma_f32_16x16x32_bf16 v[12:15],v[68:71],a[28:31],v[12:15]\n" \
"v_rcp_f32 v41,v41\nv_mfma_f32_16x16x32_bf16 v[16:19],v[64:67],v[192:195],0\nglobal_load_dwordx4 v[64:67],v59,s[4:5] sc1\nds_write_addtid_b32 v21 offset:5888\n" \
"v_mfma_f32_16x16x32_bf16 v[16:19],v[68:71],v[196:199],v[16:19]\nds_write_addtid_b32 v22 offset:5600\ns_waitcnt vmcnt(14)\nv_mfma_f32_16x16x32_bf16 v[0:3],v[72:75],a[32:35],v[0:3]\n" \
"ds_write_addtid_b32 v23 offset:6144\ns_waitcnt vmcnt(13)\nv_mfma_f32_16x16x32_bf16 v[0:3],v[76:79],a[48:51],v[0:3]\nv_rcp_f32 v42,v42\n" \
"v_mfma_f32_16x16x32_bf16 v[4:7],v[72:75],a[36:39],v[4:7]\nv_rcp_f32 v43,v43\nv_mfma_f32_16x16x32_bf16 v[4:7],v[76:79],a[52:55],v[4:7]\nglobal_load_dwordx4 v[68:71],v59,s[4:5] offset:1024 sc1\n" \
"v_mul_f32 v36,v36,v40\nv_mfma_f32_16x16x32_bf16 v[8:11],v[72:75],a[40:43],v[8:11]\nv_mul_f32 v37,v37,v41\nv_mul_f32 v38,v38,v42\n" \
"v_mfma_f32_16x16x32_bf16 v[8:11],v[76:79],a[56:59],v[8:11]\nv_mul_f32 v39,v39,v43\nv_cvt_pk_bf16_f32 v40,v36,v37\nv_mfma_f32_16x16x32_bf16 v[12:15],v[72:75],a[44:47],v[12:15]\n" \
"ds_write_addtid_b32 v24 offset:6400\nv_cvt_pk_bf16_f32 v41,v38,v39\nv_mfma_f32_16x16x32_bf16 v[12:15],v[76:79],a[60:63],v[12:15]\nds_write_addtid_b32 v25 offset:6944\n" \
"buffer_store_dwordx2 v[40:41],v61,s[36:39],s52 offen sc1\nv_mfma_f32_16x16x32_bf16 v[16:19],v[72:75],v[200:203],v[16:19]\nglobal_load_dwordx4 v[72:75],v59,s[4:5] offset:2048 sc1\nds_write_addtid_b32 v26 offset:6656\n" \
"v_mfma_f32_16x16x32_bf16 v[16:19],v[76:79],v[204:207],v[16:19]\nds_write_addtid_b32 v27 offset:7200\ns_waitcnt vmcnt(15)\nv_mfma_f32_16x16x32_bf16 v[0:3],v[80:83],a[64:67],v[0:3]\n" \
"ds_write_addtid_b32 v28 offset:7456\ns_waitcnt vmcnt(14)\nv_mfma_f32_16x16x32_bf16 v[0:3],v[84:87],a[80:83],v[0:3]\nds_write_addtid_b32 v29 offset:8000\n" \
"s_waitcnt lgkmcnt(10)\nv_mfma_f32_16x16x32_bf16 v[4:7],v[80:83],a[68:71],v[4:7]\nds_write_addtid_b32 v30 offset:7712\nv_add_f32 v44,v44,v45\n" \
"v_mfma_f32_16x16x32_bf16 v[4:7],v[84:87],a[84:87],v[4:7]\nglobal_load_dwordx4 v[76:79],v59,s[4:5] offset:3072 sc1\nds_write_addtid_b32 v31 offset:8256\nv_mfma_f32_16x16x32_bf16 v[8:11],v[80:83],a[72:75],v[8:11]\n" \
"ds_write_addtid_b32 v32 offset:8512\nv_add_f32 v46,v46,v47\nv_mfma_f32_16x16x32_bf16 v[8:11],v[84:87],a[88:91],v[8:11]\nds_write_addtid_b32 v33 offset:9056\n" \
"v_add_f32 v44,v44,v56\nv_mfma_f32_16x16x32_bf16 v[12:15],v[80:83],a[76:79],v[12:15]\nds_write_addtid_b32 v34 offset:8768\nv_add_f32 v44,v44,v46\n" \
"v_mfma_f32_16x16x32_bf16 v[12:15],v[84:87],a[92:95],v[12:15]\nds_write_addtid_b32 v35 offset:9312\nv_mul_f32 v45,v44,v44\nv_mfma_f32_16x16x32_bf16 v[16:19],v[80:83],v[208:211],v[16:19]\n" \
"global_load_dwordx4 v[80:83],v59,s[6:7] sc1\nv_fma_f32 v45,v45,s55,v57\nv_mfma_f32_16x16x32_bf16 v[16:19],v[84:87],v[212:215],v[16:19]\nv_mul_f32 v45,v44,v45\n" \
"s_waitcnt vmcnt(15)\nv_mfma_f32_16x16x32_bf16 v[0:3],v[88:91],a[96:99],v[0:3]\ns_waitcnt vmcnt(14)\nv_mfma_f32_16x16x32_bf16 v[0:3],v[92:95],a[112:115],v[0:3]\n" \
"v_exp_f32 v45,v45\nv_mfma_f32_16x16x32_bf16 v[4:7],v[88:91],a[100:103],v[4:7]\nv_add_f32 v45,1.0,v45\nv_mfma_f32_16x16x32_bf16 v[4:7],v[92:95],a[116:119],v[4:7]\n" \
"global_load_dwordx4 v[84:87],v59,s[6:7] offset:1024 sc1\nv_mfma_f32_16x16x32_bf16 v[8:11],v[88:91],a[104:107],v[8:11]\ns_waitcnt lgkmcnt(0)\ns_barrier\n" \
"v_mfma_f32_16x16x32_bf16 v[8:11],v[92:95],a[120:123],v[8:11]\nv_rcp_f32 v45,v45\nv_mfma_f32_16x16x32_bf16 v[12:15],v[88:91],a[108:111],v[12:15]\nv_mul_f32 v44,v44,v45\n" \
"v_cvt_pk_bf16_f32 v45,v44,v44\nv_mfma_f32_16x16x32_bf16 v[12:15],v[92:95],a[124:127],v[12:15]\nbuffer_store_short v45,v62,s[36:39],s52 offen sc1\nds_read_b128 v[36:39],v58 offset:5344\n" \
"v_mfma_f32_16x16x32_bf16 v[16:19],v[88:91],v[216:219],v[16:19]\nglobal_load_dwordx4 v[88:91],v59,s[6:7] offset:2048 sc1\nds_read_b128 v[40:43],v58 offset:14912\nv_mfma_f32_16x16x32_bf16 v[16:19],v[92:95],v[220:223],v[16:19]\n" \
"ds_read_b128 v[44:47],v58 offset:24480\ns_waitcnt vmcnt(16)\nv_mfma_f32_16x16x32_bf16 v[0:3],v[96:99],a[128:131],v[0:3]\nds_read_b128 v[48:51],v58 offset:34048\n" \
"s_waitcnt vmcnt(15)\nv_mfma_f32_16x16x32_bf16 v[0:3],v[100:103],a[144:147],v[0:3]\nv_mfma_f32_16x16x32_bf16 v[4:7],v[96:99],a[132:135],v[4:7]\nv_mfma_f32_16x16x32_bf16 v[4:7],v[100:103],a[148:151],v[4:7]\n" \
"global_load_dwordx4 v[92:95],v59,s[6:7] offset:3072 sc1\nv_mfma_f32_16x16x32_bf16 v[8:11],v[96:99],a[136:139],v[8:11]\nv_mfma_f32_16x16x32_bf16 v[8:11],v[100:103],a[152:155],v[8:11]\ns_cmp_eq_u32 s52,s83\n" \
"s_cbranch_scc1 .Lxq5_%=\n.Lyq5_%=:\nv_mfma_f32_16x16x32_bf16 v[12:15],v[96:99],a[140:143],v[12:15]\ns_waitcnt lgkmcnt(2)\n" \
"v_add_f32 v36,v36,v40\nv_mfma_f32_16x16x32_bf16 v[12:15],v[100:103],a[156:159],v[12:15]\nv_add_f32 v37,v37,v41\nv_add_f32 v38,v38,v42\n" \
"v_mfma_f32_16x16x32_bf16 v[16:19],v[96:99],v[224:227],v[16:19]\nglobal_load_dwordx4 v[96:99],v59,s[8:9] sc1\nv_add_f32 v39,v39,v43\nv_mfma_f32_16x16x32_bf16 v[16:19],v[100:103],v[228:231],v[16:19]\n" \
"v_add_f32 v36,v36,v52\ns_waitcnt vmcnt(16)\nv_mfma_f32_16x16x32_bf16 v[0:3],v[104:107],a[160:163],v[0:3]\nv_add_f32 v37,v37,v53\n" \
"s_waitcnt vmcnt(15)\nv_mfma_f32_16x16x32_bf16 v[0:3],v[108:111],a[176:179],v[0:3]\nv_add_f32 v38,v38,v54\nv_add_f32 v39,v39,v55\n" \
"v_mfma_f32_16x16x32_bf16 v[4:7],v[104:107],a[164:167],v[4:7]\ns_waitcnt lgkmcnt(0)\nv_add_f32 v44,v44,v48\nv_mfma_f32_16x16x32_bf16 v[4:7],v[108:111],a[180:183],v[4:7]\n" \
"global_load_dwordx4 v[100:103],v59,s[8:9] offset:1024 sc1\nv_add_f32 v45,v45,v49\nv_mfma_f32_16x16x32_bf16 v[8:11],v[104:107],a[168:171],v[8:11]\nv_add_f32 v46,v46,v50\n" \
"v_add_f32 v47,v47,v51\nv_mfma_f32_16x16x32_bf16 v[8:11],v[108:111],a[184:187],v[8:11]\nv_add_f32 v36,v36,v44\nv_add_f32 v37,v37,v45\n" \
"v_mfma_f32_16x16x32_bf16 v[12:15],v[104:107],a[172:175],v[12:15]\nv_add_f32 v38,v38,v46\nv_add_f32 v39,v39,v47\nv_mfma_f32_16x16x32_bf16 v[12:15],v[108:111],a[188:191],v[12:15]\n" \
"v_mul_f32 v40,v36,v36\nv_mul_f32 v41,v37,v37\nv_mfma_f32_16x16x32_bf16 v[16:19],v[104:107],v[232:235],v[16:19]\nglobal_load_dwordx4 v[104:107],v59,s[8:9] offset:2048 sc1\n" \
"v_mul_f32 v42,v38,v38\nv_mfma_f32_16x16x32_bf16 v[16:19],v[108:111],v[236:239],v[16:19]\nv_mul_f32 v43,v39,v39\ns_waitcnt vmcnt(16)\n" \
"v_mfma_f32_16x16x32_bf16 v[0:3],v[112:115],a[192:195],v[0:3]\nv_fma_f32 v40,v40,s55,v57\ns_waitcnt vmcnt(15)\nv_mfma_f32_16x16x32_bf16 v[0:3],v[116:119],a[208:211],v[0:3]\n" \
"v_fma_f32 v41,v41,s55,v57\nv_fma_f32 v42,v42,s55,v57\nv_mfma_f32_16x16x32_bf16 v[4:7],v[112:115],a[196:199],v[4:7]\nv_fma_f32 v43,v43,s55,v57\n" \
"v_mul_f32 v40,v36,v40\nv_mfma_f32_16x16x32_bf16 v[4:7],v[116:119],a[212:215],v[4:7]\nglobal_load_dwordx4 v[108:111],v59,s[8:9] offset:3072 sc1\nv_mul_f32 v41,v37,v41\n" \
"v_mfma_f32_16x16x32_bf16 v[8:11],v[112:115],a[200:203],v[8:11]\nv_mul_f32 v42,v38,v42\nv_mul_f32 v43,v39,v43\nv_mfma_f32_16x16x32_bf16 v[8:11],v[116:119],a[216:219],v[8:11]\n" \
"v_exp_f32 v40,v40\nv_mfma_f32_16x16x32_bf16 v[12:15],v[112:115],a[204:207],v[12:15]\nv_exp_f32 v41,v41\nv_mfma_f32_16x16x32_bf16 v[12:15],v[116:119],a[220:223],v[12:15]\n" \
"v_exp_f32 v42,v42\nv_mfma_f32_16x16x32_bf16 v[16:19],v[112:115],v[240:243],v[16:19]\nglobal_load_dwordx4 v[112:115],v59,s[10:11] sc1\nv_mfma_f32_16x16x32_bf16 v[16:19],v[116:119],v[244:247],v[16:19]\n" \
"s_waitcnt vmcnt(16)\nv_mfma_f32_16x16x32_bf16 v[0:3],v[120:123],a[224:227],v[0:3]\ns_waitcnt vmcnt(15)\nv_mfma_f32_16x16x32_bf16 v[0:3],v[124:127],a[240:243],v[0:3]\n" \
"v_exp_f32 v43,v43\nv_mfma_f32_16x16x32_bf16 v[4:7],v[120:123],a[228:231],v[4:7]\nv_add_f32 v40,1.0,v40\nv_add_f32 v41,1.0,v41\n" \
"v_mfma_f32_16x16x32_bf16 v[4:7],v[124:127],a[244:247],v[4:7]\nglobal_load_dwordx4 v[116:119],v59,s[10:11] offset:1024 sc1\nv_add_f32 v42,1.0,v42\nv_mfma_f32_16x16x32_bf16 v[8:11],v[120:123],a[232:235],v[8:11]\n" \
"v_add_f32 v43,1.0,v43\nv_mfma_f32_16x16x32_bf16 v[8:11],v[124:127],a[248:251],v[8:11]\nv_rcp_f32 v40,v40\nv_mfma_f32_16x16x32_bf16 v[12:15],v[120:123],a[236:239],v[12:15]\n" \
"s_cmp_eq_u32 s52,s95\ns_cbranch_scc1 .Lxt5_%=\n.Lyt5_%=:\nv_mfma_f32_16x16x32_bf16 v[12:15],v[124:127],a[252:255],v[12:15]\n" \
"v_rcp_f32 v41,v41\nv_mfma_f32_16x16x32_bf16 v[16:19],v[120:123],v[248:251],v[16:19]\nglobal_load_dwordx4 v[120:123],v59,s[10:11] offset:2048 sc1\nv_rcp_f32 v42,v42\n" \
"v_mfma_f32_16x16x32_bf16 v[16:19],v[124:127],v[252:255],v[16:19]\nglobal_load_dwordx4 v[124:127],v59,s[10:11] offset:3072 sc1\nv_rcp_f32 v43,v43\ns_add_u32 s52,s52,409600\n" \
"s_cmp_eq_u32 s52,s53\ns_cbranch_scc1 .Lls_%=\n.Ls_%=:\ns_waitcnt vmcnt(17)\n" \
"v_mfma_f32_16x16x32_bf16 v[20:23],v[64:67],a[0:3],0\nv_mul_f32 v36,v36,v40\ns_waitcnt vmcnt(16)\nv_mfma_f32_16x16x32_bf16 v[20:23],v[68:71],a[16:19],v[20:23]\n" \
"v_add_u32 v59,0x10000,v59\nv_mul_f32 v37,v37,v41\nv_mfma_f32_16x16x32_bf16 v[24:27],v[64:67],a[4:7],0\nv_and_b32 v59,0x7ffff,v59\n" \
"v_mul_f32 v38,v38,v42\nv_mfma_f32_16x16x32_bf16 v[24:27],v[68:71],a[20:23],v[24:27]\nv_mul_f32 v39,v39,v43\nv_cvt_pk_bf16_f32 v40,v36,v37\n" \
"v_mfma_f32_16x16x32_bf16 v[28:31],v[64:67],a[8:11],0\nds_write_addtid_b32 v0\nv_cvt_pk_bf16_f32 v41,v38,v39\nv_mfma_f32_16x16x32_bf16 v[28:31],v[68:71],a[24:27],v[28:31]\n" \
"ds_write_addtid_b32 v1 offset:544\nbuffer_store_dwordx2 v[40:41],v61,s[36:39],s52 offen sc1\nv_mfma_f32_16x16x32_bf16 v[32:35],v[64:67],a[12:15],0\nglobal_load_dwordx4 v[64:67],v59,s[4:5] sc1\n" \
"ds_write_addtid_b32 v2 offset:256\nv_mfma_f32_16x16x32_bf16 v[32:35],v[68:71],a[28:31],v[32:35]\nds_write_addtid_b32 v3 offset:800\ns_waitcnt vmcnt(16)\n" \
"v_mfma_f32_16x16x32_bf16 v[20:23],v[72:75],a[32:35],v[20:23]\nds_write_addtid_b32 v4 offset:1056\ns_waitcnt vmcnt(15)\nv_mfma_f32_16x16x32_bf16 v[20:23],v[76:79],a[48:51],v[20:23]\n" \
"ds_write_addtid_b32 v5 offset:1600\nv_mfma_f32_16x16x32_bf16 v[24:27],v[72:75],a[36:39],v[24:27]\nglobal_load_dwordx4 v[68:71],v59,s[4:5] offset:1024 sc1\nds_write_addtid_b32 v6 offset:1312\n" \
"v_mfma_f32_16x16x32_bf16 v[24:27],v[76:79],a[52:55],v[24:27]\nds_write_addtid_b32 v7 offset:1856\nv_mfma_f32_16x16x32_bf16 v[28:31],v[72:75],a[40:43],v[28:31]\nds_write_addtid_b32 v8 offset:2112\n" \
"v_mfma_f32_16x16x32_bf16 v[28:31],v[76:79],a[56:59],v[28:31]\nds_write_addtid_b32 v9 offset:2656\nv_mfma_f32_16x16x32_bf16 v[32:35],v[72:75],a[44:47],v[32:35]\nglobal_load_dwordx4 v[72:75],v59,s[4:5] offset:2048 sc1\n" \
"ds_write_addtid_b32 v10 offset:2368\nv_mfma_f32_16x16x32_bf16 v[32:35],v[76:79],a[60:63],v[32:35]\nds_write_addtid_b32 v11 offset:2912\ns_waitcnt vmcnt(16)\n" \
"v_mfma_f32_16x16x32_bf16 v[20:23],v[80:83],a[64:67],v[20:23]\nds_write_addtid_b32 v12 offset:3168\ns_waitcnt vmcnt(15)\nv_mfma_f32_16x16x32_bf16 v[20:23],v[84:87],a[80:83],v[20:23]\n" \
"ds_write_addtid_b32 v13 offset:3712\nv_mfma_f32_16x16x32_bf16 v[24:27],v[80:83],a[68:71],v[24:27]\nglobal_load_dwordx4 v[76:79],v59,s[4:5] offset:3072 sc1\nds_write_addtid_b32 v14 offset:3424\n" \
"v_mfma_f32_16x16x32_bf16 v[24:27],v[84:87],a[84:87],v[24:27]\nds_write_addtid_b32 v15 offset:3968\nv_mfma_f32_16x16x32_bf16 v[28:31],v[80:83],a[72:75],v[28:31]\nds_write_addtid_b32 v16 offset:4224\n" \
"v_mfma_f32_16x16x32_bf16 v[28:31],v[84:87],a[88:91],v[28:31]\nds_write_addtid_b32 v17 offset:4512\nv_mfma_f32_16x16x32_bf16 v[32:35],v[80:83],a[76:79],v[32:35]\nglobal_load_dwordx4 v[80:83],v59,s[6:7] sc1\n" \
"ds_write_addtid_b32 v18 offset:4800\nv_mfma_f32_16x16x32_bf16 v[32:35],v[84:87],a[92:95],v[32:35]\nds_write_addtid_b32 v19 offset:5088\ns_waitcnt vmcnt(15)\n" \
"v_mfma_f32_16x16x32_bf16 v[20:23],v[88:91],a[96:99],v[20:23]\ns_waitcnt vmcnt(14)\nv_mfma_f32_16x16x32_bf16 v[20:23],v[92:95],a[112:115],v[20:23]\nv_mfma_f32_16x16x32_bf16 v[24:27],v[88:91],a[100:103],v[24:27]\n" \
"global_load_dwordx4 v[84:87],v59,s[6:7] offset:1024 sc1\nv_mfma_f32_16x16x32_bf16 v[24:27],v[92:95],a[116:119],v[24:27]\nv_mfma_f32_16x16x32_bf16 v[28:31],v[88:91],a[104:107],v[28:31]\nv_mfma_f32_16x16x32_bf16 v[28:31],v[92:95],a[120:123],v[28:31]\n" \
"s_waitcnt lgkmcnt(0)\ns_barrier\nv_mfma_f32_16x16x32_bf16 v[32:35],v[88:91],a[108:111],v[32:35]\nglobal_load_dwordx4 v[88:91],v59,s[6:7] offset:2048 sc1\n" \
"ds_read_b128 v[36:39],v58\nv_mfma_f32_16x16x32_bf16 v[32:35],v[92:95],a[124:127],v[32:35]\nds_read_b128 v[40:43],v58 offset:9568\ns_waitcnt vmcnt(15)\n" \
"v_mfma_f32_16x16x32_bf16 v[20:23],v[96:99],a[128:131],v[20:23]\nds_read_b128 v[44:47],v58 offset:19136\ns_waitcnt vmcnt(14)\nv_mfma_f32_16x16x32_bf16 v[20:23],v[100:103],a[144:147],v[20:23]\n" \
"ds_read_b128 v[48:51],v58 offset:28704\nv_mfma_f32_16x16x32_bf16 v[24:27],v[96:99],a[132:135],v[24:27]\nglobal_load_dwordx4 v[92:95],v59,s[6:7] offset:3072 sc1\nv_mfma_f32_16x16x32_bf16 v[24:27],v[100:103],a[148:151],v[24:27]\n" \
"v_mfma_f32_16x16x32_bf16 v[28:31],v[96:99],a[136:139],v[28:31]\ns_cmp_eq_u32 s52,s83\ns_cbranch_scc1 .Lxq4_%=\n.Lyq4_%=:\n" \
"v_mfma_f32_16x16x32_bf16 v[28:31],v[100:103],a[152:155],v[28:31]\nv_mfma_f32_16x16x32_bf16 v[32:35],v[96:99],a[140:143],v[32:35]\nglobal_load_dwordx4 v[96:99],v59,s[8:9] sc1\nv_mfma_f32_16x16x32_bf16 v[32:35],v[100:103],a[156:159],v[32:35]\n" \
"s_waitcnt lgkmcnt(2)\ns_waitcnt vmcnt(15)\nv_mfma_f32_16x16x32_bf16 v[20:23],v[104:107],a[160:163],v[20:23]\nv_add_f32 v36,v36,v40\n" \
"s_waitcnt vmcnt(14)\nv_mfma_f32_16x16x32_bf16 v[20:23],v[108:111],a[176:179],v[20:23]\nv_add_f32 v37,v37,v41\nv_add_f32 v38,v38,v42\n" \
"v_mfma_f32_16x16x32_bf16 v[24:27],v[104:107],a[164:167],v[24:27]\nglobal_load_dwordx4 v[100:103],v59,s[8:9] offset:1024 sc1\nv_add_f32 v39,v39,v43\nv_mfma_f32_16x16x32_bf16 v[24:27],v[108:111],a[180:183],v[24:27]\n" \
"v_add_f32 v36,v36,v52\nv_add_f32 v37,v37,v53\nv_mfma_f32_16x16x32_bf16 v[28:31],v[104:107],a[168:171],v[28:31]\nv_add_f32 v38,v38,v54\n" \
"v_add_f32 v39,v39,v55\nv_mfma_f32_16x16x32_bf16 v[28:31],v[108:111],a[184:187],v[28:31]\ns_waitcnt lgkmcnt(0)\nv_add_f32 v44,v44,v48\n" \
"v_mfma_f32_16x16x32_bf16 v[32:35],v[104:107],a[172:175],v[32:35]\nglobal_load_dwordx4 v[104:107],v59,s[8:9] offset:2048 sc1\nv_add_f32 v45,v45,v49\nv_mfma_f32_16x16x32_bf16 v[32:35],v[108:111],a[188:191],v[32:35]\n" \
"v_add_f32 v46,v46,v50\ns_waitcnt vmcnt(15)\nv_mfma_f32_16x16x32_bf16 v[20:23],v[112:115],a[192:195],v[20:23]\nv_add_f32 v47,v47,v51\n" \
"s_waitcnt vmcnt(14)\nv_mfma_f32_16x16x32_bf16 v[20:23],v[116:119],a[208:211],v[20:23]\nv_add_f32 v36,v36,v44\nv_add_f32 v37,v37,v45\n" \
"v_mfma_f32_16x16x32_bf16 v[24:27],v[112:115],a[196:199],v[24:27]\nglobal_load_dwordx4 v[108:111],v59,s[8:9] offset:3072 sc1\nv_add_f32 v38,v38,v46\nv_mfma_f32_16x16x32_bf16 v[24:27],v[116:119],a[212:215],v[24:27]\n" \
"v_add_f32 v39,v39,v47\nv_mul_f32 v40,v36,v36\nv_mfma_f32_16x16x32_bf16 v[28:31],v[112:115],a[200:203],v[28:31]\nv_mul_f32 v41,v37,v37\n" \
"v_mul_f32 v42,v38,v38\nv_mfma_f32_16x16x32_bf16 v[28:31],v[116:119],a[216:219],v[28:31]\nv_mul_f32 v43,v39,v39\nv_fma_f32 v40,v40,s55,v57\n" \
"v_mfma_f32_16x16x32_bf16 v[32:35],v[112:115],a[204:207],v[32:35]\nglobal_load_dwordx4 v[112:115],v59,s[10:11] sc1\nv_fma_f32 v41,v41,s55,v57\nv_mfma_f32_16x16x32_bf16 v[32:35],v[116:119],a[220:223],v[32:35]\n" \
"v_fma_f32 v42,v42,s55,v57\ns_waitcnt vmcnt(15)\nv_mfma_f32_16x16x32_bf16 v[20:23],v[120:123],a[224:227],v[20:23]\nv_fma_f32 v43,v43,s55,v57\n" \
"s_waitcnt vmcnt(14)\nv_mfma_f32_16x16x32_bf16 v[20:23],v[124:127],a[240:243],v[20:23]\nv_mul_f32 v40,v36,v40\nv_mul_f32 v41,v37,v41\n" \
"v_mfma_f32_16x16x32_bf16 v[24:27],v[120:123],a[228:231],v[24:27]\nglobal_load_dwordx4 v[116:119],v59,s[10:11] offset:1024 sc1\nv_mul_f32 v42,v38,v42\nv_mfma_f32_16x16x32_bf16 v[24:27],v[124:127],a[244:247],v[24:27]\n" \
"v_mul_f32 v43,v39,v43\nv_mfma_f32_16x16x32_bf16 v[28:31],v[120:123],a[232:235],v[28:31]\ns_cmp_eq_u32 s52,s95\ns_cbranch_scc1 .Lxt4_%=\n" \
".Lyt4_%=:\nv_mfma_f32_16x16x32_bf16 v[28:31],v[124:127],a[248:251],v[28:31]\nv_exp_f32 v40,v40\nv_mfma_f32_16x16x32_bf16 v[32:35],v[120:123],a[236:239],v[32:35]\n" \
"global_load_dwordx4 v[120:123],v59,s[10:11] offset:2048 sc1\nv_exp_f32 v41,v41\nv_mfma_f32_16x16x32_bf16 v[32:35],v[124:127],a[252:255],v[32:35]\nglobal_load_dwordx4 v[124:127],v59,s[10:11] offset:3072 sc1\n" \
"v_exp_f32 v42,v42\nv_exp_f32 v43,v43\ns_add_u32 s52,s52,409600\ns_cmp_eq_u32 s52,s53\n" \
"s_cbranch_scc0 .Lf_%=\nv_mfma_f32_16x16x32_bf16 v[0:3],v[64:67],a[0:3],0\nds_read_b32 v44,v60\ns_waitcnt vmcnt(16)\n" \
"v_mfma_f32_16x16x32_bf16 v[0:3],v[68:71],a[16:19],v[0:3]\nds_read_b32 v45,v60 offset:9568\nv_and_b32 v59,0x3ff,v59\nv_mfma_f32_16x16x32_bf16 v[4:7],v[64:67],a[4:7],0\n" \
"ds_read_b32 v46,v60 offset:19136\nds_write_addtid_b32 v20 offset:5344\nv_mfma_f32_16x16x32_bf16 v[4:7],v[68:71],a[20:23],v[4:7]\nds_read_b32 v47,v60 offset:28704\n" \
"ds_write_addtid_b32 v21 offset:5888\nv_mfma_f32_16x16x32_bf16 v[8:11],v[64:67],a[8:11],0\ns_add_u32 s4,s12,409600\ns_addc_u32 s5,s13,0\n" \
"v_mfma_f32_16x16x32_bf16 v[8:11],v[68:71],a[24:27],v[8:11]\ns_add_u32 s6,s14,409600\ns_addc_u32 s7,s15,0\nv_mfma_f32_16x16x32_bf16 v[12:15],v[64:67],a[12:15],0\n" \
"s_add_u32 s8,s16,409600\ns_addc_u32 s9,s17,0\nv_mfma_f32_16x16x32_bf16 v[12:15],v[68:71],a[28:31],v[12:15]\nds_write_addtid_b32 v22 offset:5600\n" \
"ds_write_addtid_b32 v23 offset:6144\nv_mfma_f32_16x16x32_bf16 v[16:19],v[64:67],v[192:195],0\nds_write_addtid_b32 v24 offset:6400\nds_write_addtid_b32 v25 offset:6944\n" \
"v_mfma_f32_16x16x32_bf16 v[16:19],v[68:71],v[196:199],v[16:19]\nds_write_addtid_b32 v26 offset:6656\ns_waitcnt vmcnt(15)\nv_mfma_f32_16x16x32_bf16 v[0:3],v[72:75],a[32:35],v[0:3]\n" \
"v_add_u32 v71,s57,v59\ns_waitcnt vmcnt(14)\nv_mfma_f32_16x16x32_bf16 v[0:3],v[76:79],a[48:51],v[0:3]\ns_waitcnt vmcnt(8)\n" \
"v_cmp_eq_u32 vcc,0,v63\nv_mfma_f32_16x16x32_bf16 v[4:7],v[72:75],a[36:39],v[4:7]\nds_read_b32 v64,v71 offset:28672\nds_read_b64 v[68:69],v71 offset:29696\n" \
"v_mfma_f32_16x16x32_bf16 v[4:7],v[76:79],a[52:55],v[4:7]\ns_and_b64 vcc,vcc,s[86:87]\ns_cbranch_scc1 .LsaF_%=\n.LbaF_%=:\n" \
"v_mfma_f32_16x16x32_bf16 v[8:11],v[72:75],a[40:43],v[8:11]\nds_write_addtid_b32 v27 offset:7200\nds_write_addtid_b32 v28 offset:7456\nv_mfma_f32_16x16x32_bf16 v[8:11],v[76:79],a[56:59],v[8:11]\n" \
"ds_write_addtid_b32 v29 offset:8000\nds_write_addtid_b32 v30 offset:7712\nv_mfma_f32_16x16x32_bf16 v[12:15],v[72:75],a[44:47],v[12:15]\ns_waitcnt lgkmcnt(4)\n" \
"v_xor_b32 v65,32,v64\nv_mfma_f32_16x16x32_bf16 v[12:15],v[76:79],a[60:63],v[12:15]\nv_xor_b32 v66,64,v64\nv_xor_b32 v67,96,v64\n" \
"v_mfma_f32_16x16x32_bf16 v[16:19],v[72:75],v[200:203],v[16:19]\nv_add_u32 v70,0xf800,v68\nv_add_u32 v71,0xf800,v69\nv_mfma_f32_16x16x32_bf16 v[16:19],v[76:79],v[204:207],v[16:19]\n" \
"ds_write_addtid_b32 v31 offset:8256\nv_mfma_f32_16x16x32_bf16 v[0:3],v[80:83],a[64:67],v[0:3]\nglobal_load_dwordx4 v[72:75],v59,s[12:13] sc1\nv_mfma_f32_16x16x32_bf16 v[0:3],v[84:87],a[80:83],v[0:3]\n" \
"ds_write_addtid_b32 v32 offset:8512\nds_write_addtid_b32 v33 offset:9056\nv_mfma_f32_16x16x32_bf16 v[4:7],v[80:83],a[68:71],v[4:7]\nds_write_addtid_b32 v34 offset:8768\n" \
"ds_write_addtid_b32 v35 offset:9312\nv_mfma_f32_16x16x32_bf16 v[4:7],v[84:87],a[84:87],v[4:7]\nglobal_load_dwordx4 v[76:79],v59,s[12:13] offset:1024 sc1\nds_read_b64_tr_b16 a[0:1],v64\n" \
"v_mfma_f32_16x16x32_bf16 v[8:11],v[80:83],a[72:75],v[8:11]\nds_read_b64_tr_b16 a[2:3],v64 offset:512\nds_read_b64_tr_b16 a[4:5],v65\nv_mfma_f32_16x16x32_bf16 v[8:11],v[84:87],a[88:91],v[8:11]\n" \
"ds_read_b64_tr_b16 a[6:7],v65 offset:512\nds_read_b64_tr_b16 a[8:9],v66\nv_mfma_f32_16x16x32_bf16 v[12:15],v[80:83],a[76:79],v[12:15]\nds_read_b64_tr_b16 a[10:11],v66 offset:512\n" \
"ds_read_b64_tr_b16 a[12:13],v67\nv_mfma_f32_16x16x32_bf16 v[12:15],v[84:87],a[92:95],v[12:15]\nds_read_b64_tr_b16 a[14:15],v67 offset:512\nds_read_b64_tr_b16 a[16:17],v64 offset:4096\n" \
"v_mfma_f32_16x16x32_bf16 v[16:19],v[80:83],v[208:211],v[16:19]\ns_waitcnt lgkmcnt(0)\ns_barrier\nv_mfma_f32_16x16x32_bf16 v[16:19],v[84:87],v[212:215],v[16:19]\n" \
"ds_read_b64_tr_b16 a[18:19],v64 offset:4608\nv_mfma_f32_16x16x32_bf16 v[0:3],v[88:91],a[96:99],v[0:3]\nglobal_load_dwordx4 v[80:83],v59,s[12:13] offset:2048 sc1\nv_mfma_f32_16x16x32_bf16 v[0:3],v[92:95],a[112:115],v[0:3]\n" \
"s_add_u32 m0,s57,0\nv_mfma_f32_16x16x32_bf16 v[4:7],v[88:91],a[100:103],v[4:7]\nds_read_b64_tr_b16 a[20:21],v65 offset:4096\nds_read_b64_tr_b16 a[22:23],v65 offset:4608\n" \
"v_mfma_f32_16x16x32_bf16 v[4:7],v[92:95],a[116:119],v[4:7]\nbuffer_load_dwordx4 v68,s[40:43],s60 offen lds\nds_read_b64_tr_b16 a[24:25],v66 offset:4096\nv_mfma_f32_16x16x32_bf16 v[8:11],v[88:91],a[104:107],v[8:11]\n" \
"ds_read_b64_tr_b16 a[26:27],v66 offset:4608\nds_read_b64_tr_b16 a[28:29],v67 offset:4096\nv_mfma_f32_16x16x32_bf16 v[8:11],v[92:95],a[120:123],v[8:11]\nds_read_b64_tr_b16 a[30:31],v67 offset:4608\n" \
"ds_read_b64_tr_b16 a[32:33],v64 offset:8192\nv_mfma_f32_16x16x32_bf16 v[12:15],v[88:91],a[108:111],v[12:15]\nbuffer_load_dwordx4 v69,s[40:43],s60 offen offset:1024 lds\nds_read_b64_tr_b16 a[34:35],v64 offset:8704\n" \
"v_mfma_f32_16x16x32_bf16 v[12:15],v[92:95],a[124:127],v[12:15]\nds_read_b64_tr_b16 a[36:37],v65 offset:8192\nds_read_b64_tr_b16 a[38:39],v65 offset:8704\nv_mfma_f32_16x16x32_bf16 v[16:19],v[88:91],v[216:219],v[16:19]\n" \
"ds_read_b64_tr_b16 a[40:41],v66 offset:8192\nds_read_b64_tr_b16 a[42:43],v66 offset:8704\nv_mfma_f32_16x16x32_bf16 v[16:19],v[92:95],v[220:223],v[16:19]\nbuffer_load_dwordx4 v70,s[40:43],s60 offen offset:2048 lds\n" \
"s_waitcnt vmcnt(13)\nv_mfma_f32_16x16x32_bf16 v[0:3],v[96:99],a[128:131],v[0:3]\nds_read_b64_tr_b16 a[44:45],v67 offset:8192\ns_waitcnt vmcnt(12)\n" \
"v_mfma_f32_16x16x32_bf16 v[0:3],v[100:103],a[144:147],v[0:3]\nds_read_b64_tr_b16 a[46:47],v67 offset:8704\nv_add_f32 v40,1.0,v40\nv_mfma_f32_16x16x32_bf16 v[4:7],v[96:99],a[132:135],v[4:7]\n" \
"buffer_load_dwordx4 v71,s[40:43],s60 offen offset:3072 lds\nv_add_f32 v41,1.0,v41\nv_mfma_f32_16x16x32_bf16 v[4:7],v[100:103],a[148:151],v[4:7]\nv_add_f32 v42,1.0,v42\n" \
"v_add_f32 v43,1.0,v43\nv_mfma_f32_16x16x32_bf16 v[8:11],v[96:99],a[136:139],v[8:11]\nds_read_b64_tr_b16 a[48:49],v64 offset:12288\nds_read_b64_tr_b16 a[50:51],v64 offset:12800\n" \
"v_mfma_f32_16x16x32_bf16 v[8:11],v[100:103],a[152:155],v[8:11]\nglobal_load_dwordx4 v[84:87],v59,s[14:15] sc1\nds_read_b64_tr_b16 a[52:53],v65 offset:12288\nv_mfma_f32_16x16x32_bf16 v[12:15],v[96:99],a[140:143],v[12:15]\n" \
"ds_read_b64_tr_b16 a[54:55],v65 offset:12800\nds_read_b64_tr_b16 a[56:57],v66 offset:12288\nv_mfma_f32_16x16x32_bf16 v[12:15],v[100:103],a[156:159],v[12:15]\nds_read_b64_tr_b16 a[58:59],v66 offset:12800\n" \
"ds_read_b64_tr_b16 a[60:61],v67 offset:12288\nv_mfma_f32_16x16x32_bf16 v[16:19],v[96:99],v[224:227],v[16:19]\nglobal_load_dwordx4 v[88:91],v59,s[14:15] offset:1024 sc1\nds_read_b64_tr_b16 a[62:63],v67 offset:12800\n" \
"v_mfma_f32_16x16x32_bf16 v[16:19],v[100:103],v[228:231],v[16:19]\ns_waitcnt lgkmcnt(15)\ns_waitcnt vmcnt(14)\nv_mfma_f32_16x16x32_bf16 v[0:3],v[104:107],a[160:163],v[0:3]\n" \
"v_add_f32 v44,v44,v45\ns_waitcnt vmcnt(13)\nv_mfma_f32_16x16x32_bf16 v[0:3],v[108:111],a[176:179],v[0:3]\nglobal_load_dwordx4 v[92:95],v59,s[14:15] offset:2048 sc1\n" \
"v_add_f32 v46,v46,v47\nv_mfma_f32_16x16x32_bf16 v[4:7],v[104:107],a[164:167],v[4:7]\ns_add_u32 m0,s57,4096\nv_mfma_f32_16x16x32_bf16 v[4:7],v[108:111],a[180:183],v[4:7]\n" \
"v_rcp_f32 v40,v40\nv_mfma_f32_16x16x32_bf16 v[8:11],v[104:107],a[168:171],v[8:11]\nbuffer_load_dwordx4 v68,s[40:43],s61 offen lds\nds_read_b64_tr_b16 a[64:65],v64 offset:16384\n" \
"v_mfma_f32_16x16x32_bf16 v[8:11],v[108:111],a[184:187],v[8:11]\nds_read_b64_tr_b16 a[66:67],v64 offset:16896\nds_read_b64_tr_b16 a[68:69],v65 offset:16384\nv_mfma_f32_16x16x32_bf16 v[12:15],v[104:107],a[172:175],v[12:15]\n" \
"ds_read_b64_tr_b16 a[70:71],v65 offset:16896\nds_read_b64_tr_b16 a[72:73],v66 offset:16384\nv_mfma_f32_16x16x32_bf16 v[12:15],v[108:111],a[188:191],v[12:15]\nbuffer_load_dwordx4 v69,s[40:43],s61 offen offset:1024 lds\n" \
"ds_read_b64_tr_b16 a[74:75],v66 offset:16896\nv_mfma_f32_16x16x32_bf16 v[16:19],v[104:107],v[232:235],v[16:19]\nds_read_b64_tr_b16 a[76:77],v67 offset:16384\nds_read_b64_tr_b16 a[78:79],v67 offset:16896\n" \
"v_mfma_f32_16x16x32_bf16 v[16:19],v[108:111],v[236:239],v[16:19]\nv_add_f32 v44,v44,v56\ns_waitcnt vmcnt(15)\nv_mfma_f32_16x16x32_bf16 v[0:3],v[112:115],a[192:195],v[0:3]\n" \
"buffer_load_dwordx4 v70,s[40:43],s61 offen offset:2048 lds\ns_waitcnt vmcnt(15)\nv_mfma_f32_16x16x32_bf16 v[0:3],v[116:119],a[208:211],v[0:3]\nv_rcp_f32 v41,v41\n" \
"v_mfma_f32_16x16x32_bf16 v[4:7],v[112:115],a[196:199],v[4:7]\nv_add_f32 v44,v44,v46\nv_mul_f32 v45,v44,v44\nv_mfma_f32_16x16x32_bf16 v[4:7],v[116:119],a[212:215],v[4:7]\n" \
"buffer_load_dwordx4 v71,s[40:43],s61 offen offset:3072 lds\nv_fma_f32 v45,v45,s55,v57\nv_mfma_f32_16x16x32_bf16 v[8:11],v[112:115],a[200:203],v[8:11]\nds_read_b64_tr_b16 a[80:81],v64 offset:20480\n" \
"ds_read_b64_tr_b16 a[82:83],v64 offset:20992\nv_mfma_f32_16x16x32_bf16 v[8:11],v[116:119],a[216:219],v[8:11]\nds_read_b64_tr_b16 a[84:85],v65 offset:20480\nds_read_b64_tr_b16 a[86:87],v65 offset:20992\n" \
"v_mfma_f32_16x16x32_bf16 v[12:15],v[112:115],a[204:207],v[12:15]\nglobal_load_dwordx4 v[108:111],v59,s[4:5] sc1\nds_read_b64_tr_b16 a[88:89],v66 offset:20480\nv_mfma_f32_16x16x32_bf16 v[12:15],v[116:119],a[220:223],v[12:15]\n" \
"s_waitcnt lgkmcnt(15)\ns_add_u32 m0,s57,8192\nv_mfma_f32_16x16x32_bf16 v[16:19],v[112:115],v[240:243],v[16:19]\nds_read_b64_tr_b16 a[90:91],v66 offset:20992\n" \
"ds_read_b64_tr_b16 a[92:93],v67 offset:20480\nv_mfma_f32_16x16x32_bf16 v[16:19],v[116:119],v[244:247],v[16:19]\nbuffer_load_dwordx4 v68,s[40:43],s62 offen lds\ns_waitcnt vmcnt(17)\n" \
"v_mfma_f32_16x16x32_bf16 v[0:3],v[120:123],a[224:227],v[0:3]\nds_read_b64_tr_b16 a[94:95],v67 offset:20992\ns_waitcnt vmcnt(16)\nv_mfma_f32_16x16x32_bf16 v[0:3],v[124:127],a[240:243],v[0:3]\n" \
"v_rcp_f32 v42,v42\nv_mfma_f32_16x16x32_bf16 v[4:7],v[120:123],a[228:231],v[4:7]\nbuffer_load_dwordx4 v69,s[40:43],s62 offen offset:1024 lds\nv_mul_f32 v45,v44,v45\n" \
"v_mfma_f32_16x16x32_bf16 v[4:7],v[124:127],a[244:247],v[4:7]\nv_rcp_f32 v43,v43\nv_mfma_f32_16x16x32_bf16 v[8:11],v[120:123],a[232:235],v[8:11]\nds_read_b64_tr_b16 a[96:97],v64 offset:24576\n" \
"ds_read_b64_tr_b16 a[98:99],v64 offset:25088\nv_mfma_f32_16x16x32_bf16 v[8:11],v[124:127],a[248:251],v[8:11]\nbuffer_load_dwordx4 v70,s[40:43],s62 offen offset:2048 lds\nds_read_b64_tr_b16 a[100:101],v65 offset:24576\n" \
"v_mfma_f32_16x16x32_bf16 v[12:15],v[120:123],a[236:239],v[12:15]\nds_read_b64_tr_b16 a[102:103],v65 offset:25088\nds_read_b64_tr_b16 a[104:105],v66 offset:24576\nv_mfma_f32_16x16x32_bf16 v[12:15],v[124:127],a[252:255],v[12:15]\n" \
"ds_read_b64_tr_b16 a[106:107],v66 offset:25088\nds_read_b64_tr_b16 a[108:109],v67 offset:24576\nv_mfma_f32_16x16x32_bf16 v[16:19],v[120:123],v[248:251],v[16:19]\nbuffer_load_dwordx4 v71,s[40:43],s62 offen offset:3072 lds\n" \
"ds_read_b64_tr_b16 a[110:111],v67 offset:25088\nv_mfma_f32_16x16x32_bf16 v[16:19],v[124:127],v[252:255],v[16:19]\ns_mov_b32 m0,s56\ns_waitcnt vmcnt(18)\n" \
"v_mfma_f32_16x16x32_bf16 v[208:211],v[128:131],v[72:75],0\nv_mul_f32 v36,v36,v40\ns_waitcnt vmcnt(17)\nv_mfma_f32_16x16x32_bf16 v[208:211],v[160:163],v[76:79],v[208:211]\n" \
"global_load_dwordx4 v[112:115],v59,s[4:5] offset:1024 sc1\nds_write_addtid_b32 v0\nv_mfma_f32_16x16x32_bf16 v[212:215],v[132:135],v[72:75],0\nds_write_addtid_b32 v1 offset:544\n" \
"ds_write_addtid_b32 v2 offset:256\nv_mfma_f32_16x16x32_bf16 v[212:215],v[164:167],v[76:79],v[212:215]\nds_write_addtid_b32 v3 offset:800\nds_write_addtid_b32 v4 offset:1056\n" \
"v_mfma_f32_16x16x32_bf16 v[216:219],v[136:139],v[72:75],0\nglobal_load_dwordx4 v[116:119],v59,s[4:5] offset:2048 sc1\nds_write_addtid_b32 v5 offset:1600\nv_mfma_f32_16x16x32_bf16 v[216:219],v[168:171],v[76:79],v[216:219]\n" \
"ds_write_addtid_b32 v6 offset:1312\nds_write_addtid_b32 v7 offset:1856\nv_mfma_f32_16x16x32_bf16 v[220:223],v[140:143],v[72:75],0\nds_write_addtid_b32 v8 offset:2112\n" \
"ds_write_addtid_b32 v9 offset:2656\nv_mfma_f32_16x16x32_bf16 v[220:223],v[172:175],v[76:79],v[220:223]\nglobal_load_dwordx4 v[120:123],v59,s[6:7] sc1\nds_write_addtid_b32 v10 offset:2368\n" \
"v_mfma_f32_16x16x32_bf16 v[224:227],v[144:147],v[72:75],0\nds_write_addtid_b32 v11 offset:2912\nds_write_addtid_b32 v12 offset:3168\nv_mfma_f32_16x16x32_bf16 v[224:227],v[176:179],v[76:79],v[224:227]\n" \
"ds_write_addtid_b32 v13 offset:3712\nds_write_addtid_b32 v14 offset:3424\nv_mfma_f32_16x16x32_bf16 v[228:231],v[148:151],v[72:75],0\nglobal_load_dwordx4 v[124:127],v59,s[6:7] offset:1024 sc1\n" \
"ds_write_addtid_b32 v15 offset:3968\nv_mfma_f32_16x16x32_bf16 v[228:231],v[180:183],v[76:79],v[228:231]\nds_write_addtid_b32 v16 offset:4224\nds_write_addtid_b32 v17 offset:4512\n" \
"v_mfma_f32_16x16x32_bf16 v[232:235],v[152:155],v[72:75],0\nds_write_addtid_b32 v18 offset:4800\nds_write_addtid_b32 v19 offset:5088\nv_mfma_f32_16x16x32_bf16 v[232:235],v[184:187],v[76:79],v[232:235]\n" \
"global_load_dwordx4 v[192:195],v59,s[6:7] offset:2048 sc1\ns_waitcnt vmcnt(17)\nv_mfma_f32_16x16x32_bf16 v[236:239],v[156:159],v[72:75],0\nds_read_b64_tr_b16 a[112:113],v64\n" \
"ds_read_b64_tr_b16 a[114:115],v64 offset:512\nv_mfma_f32_16x16x32_bf16 v[236:239],v[188:191],v[76:79],v[236:239]\ns_waitcnt lgkmcnt(15)\nv_mfma_f32_16x16x32_bf16 v[208:211],a[0:3],v[80:83],v[208:211]\n" \
"s_waitcnt vmcnt(16)\nv_mfma_f32_16x16x32_bf16 v[208:211],a[32:35],v[84:87],v[208:211]\nglobal_load_dwordx4 v[96:99],v59,s[16:17] sc1\nds_read_b64_tr_b16 a[116:117],v65\n" \
"v_mfma_f32_16x16x32_bf16 v[212:215],a[4:7],v[80:83],v[212:215]\ns_waitcnt lgkmcnt(15)\ns_add_u32 m0,s57,12288\nv_mfma_f32_16x16x32_bf16 v[212:215],a[36:39],v[84:87],v[212:215]\n" \
"s_waitcnt lgkmcnt(0)\ns_barrier\nv_mfma_f32_16x16x32_bf16 v[216:219],a[8:11],v[80:83],v[216:219]\nbuffer_load_dwordx4 v68,s[40:43],s63 offen lds\n" \
"ds_read_b64_tr_b16 a[118:119],v65 offset:512\nv_mfma_f32_16x16x32_bf16 v[216:219],a[40:43],v[84:87],v[216:219]\nds_read_b64_tr_b16 a[120:121],v66\nds_read_b64_tr_b16 a[122:123],v66 offset:512\n" \
"v_mfma_f32_16x16x32_bf16 v[220:223],a[12:15],v[80:83],v[220:223]\nds_read_b64_tr_b16 a[124:125],v67\nds_read_b64_tr_b16 a[126:127],v67 offset:512\nv_mfma_f32_16x16x32_bf16 v[220:223],a[44:47],v[84:87],v[220:223]\n" \
"buffer_load_dwordx4 v69,s[40:43],s63 offen offset:1024 lds\nv_mfma_f32_16x16x32_bf16 v[224:227],a[16:19],v[80:83],v[224:227]\nds_read_b32 v252,v60\nv_mfma_f32_16x16x32_bf16 v[224:227],a[48:51],v[84:87],v[224:227]\n" \
"ds_read_b32 v253,v60 offset:9568\nds_read_b32 v254,v60 offset:19136\nv_mfma_f32_16x16x32_bf16 v[228:231],a[20:23],v[80:83],v[228:231]\nbuffer_load_dwordx4 v70,s[40:43],s63 offen offset:2048 lds\n" \
"ds_read_b32 v255,v60 offset:28704\nv_mfma_f32_16x16x32_bf16 v[228:231],a[52:55],v[84:87],v[228:231]\nv_exp_f32 v45,v45\nv_mfma_f32_16x16x32_bf16 v[232:235],a[24:27],v[80:83],v[232:235]\n" \
"v_mul_f32 v37,v37,v41\nv_add_f32 v45,1.0,v45\nv_mfma_f32_16x16x32_bf16 v[232:235],a[56:59],v[84:87],v[232:235]\nbuffer_load_dwordx4 v71,s[40:43],s63 offen offset:3072 lds\n" \
"v_mul_f32 v38,v38,v42\nv_mfma_f32_16x16x32_bf16 v[236:239],a[28:31],v[80:83],v[236:239]\ns_add_u32 m0,s57,16384\nv_mfma_f32_16x16x32_bf16 v[236:239],a[60:63],v[84:87],v[236:239]\n" \
"s_waitcnt vmcnt(20)\nv_mfma_f32_16x16x32_bf16 v[208:211],a[64:67],v[88:91],v[208:211]\ns_waitcnt vmcnt(19)\nv_mfma_f32_16x16x32_bf16 v[208:211],a[96:99],v[92:95],v[208:211]\n" \
"buffer_load_dwordx4 v68,s[40:43],s64 offen lds\ns_waitcnt vmcnt(16)\nv_mfma_f32_16x16x32_bf16 v[212:215],a[68:71],v[88:91],v[212:215]\nds_read_b64_tr_b16 a[128:129],v64 offset:4096\n" \
"ds_read_b64_tr_b16 a[130:131],v64 offset:4608\nv_mfma_f32_16x16x32_bf16 v[212:215],a[100:103],v[92:95],v[212:215]\nds_read_b64_tr_b16 a[132:133],v65 offset:4096\nds_read_b64_tr_b16 a[134:135],v65 offset:4608\n" \
"v_mfma_f32_16x16x32_bf16 v[216:219],a[72:75],v[88:91],v[216:219]\nbuffer_load_dwordx4 v69,s[40:43],s64 offen offset:1024 lds\nds_read_b64_tr_b16 a[136:137],v66 offset:4096\nv_mfma_f32_16x16x32_bf16 v[216:219],a[104:107],v[92:95],v[216:219]\n" \
"ds_read_b64_tr_b16 a[138:139],v66 offset:4608\nds_read_b64_tr_b16 a[140:141],v67 offset:4096\nv_mfma_f32_16x16x32_bf16 v[220:223],a[76:79],v[88:91],v[220:223]\nds_read_b64_tr_b16 a[142:143],v67 offset:4608\n" \
"s_waitcnt lgkmcnt(8)\nv_mfma_f32_16x16x32_bf16 v[220:223],a[108:111],v[92:95],v[220:223]\nbuffer_load_dwordx4 v70,s[40:43],s64 offen offset:2048 lds\nv_mfma_f32_16x16x32_bf16 v[224:227],a[80:83],v[88:91],v[224:227]\n" \
"v_add_f32 v252,v252,v253\nv_add_f32 v254,v254,v255\nv_mfma_f32_16x16x32_bf16 v[228:231],a[84:87],v[88:91],v[228:231]\nv_add_f32 v252,v252,v56\n" \
"v_add_f32 v252,v252,v254\nv_mfma_f32_16x16x32_bf16 v[232:235],a[88:91],v[88:91],v[232:235]\nbuffer_load_dwordx4 v71,s[40:43],s64 offen offset:3072 lds\nv_mul_f32 v253,v252,v252\n" \
"v_mfma_f32_16x16x32_bf16 v[236:239],a[92:95],v[88:91],v[236:239]\nv_fma_f32 v253,v253,s55,v57\ns_waitcnt vmcnt(18)\nv_mfma_f32_16x16x32_bf16 v[0:3],v[128:131],v[108:111],0\n" \
"v_mul_f32 v253,v252,v253\ns_waitcnt vmcnt(13)\nv_mfma_f32_16x16x32_bf16 v[0:3],v[160:163],v[112:115],v[0:3]\nglobal_load_dwordx4 v[100:103],v59,s[16:17] offset:1024 sc1\n" \
"v_mul_f32 v39,v39,v43\nv_mfma_f32_16x16x32_bf16 v[4:7],v[132:135],v[108:111],0\ns_add_u32 m0,s57,20480\nv_mfma_f32_16x16x32_bf16 v[4:7],v[164:167],v[112:115],v[4:7]\n" \
"v_exp_f32 v253,v253\nv_mfma_f32_16x16x32_bf16 v[8:11],v[136:139],v[108:111],0\nbuffer_load_dwordx4 v68,s[40:43],s65 offen lds\nv_mfma_f32_16x16x32_bf16 v[8:11],v[168:171],v[112:115],v[8:11]\n" \
"ds_read_b64_tr_b16 a[144:145],v64 offset:8192\nds_read_b64_tr_b16 a[146:147],v64 offset:8704\nv_mfma_f32_16x16x32_bf16 v[12:15],v[140:143],v[108:111],0\nds_read_b64_tr_b16 a[148:149],v65 offset:8192\n" \
"ds_read_b64_tr_b16 a[150:151],v65 offset:8704\nv_mfma_f32_16x16x32_bf16 v[12:15],v[172:175],v[112:115],v[12:15]\nbuffer_load_dwordx4 v69,s[40:43],s65 offen offset:1024 lds\nds_read_b64_tr_b16 a[152:153],v66 offset:8192\n" \
"v_mfma_f32_16x16x32_bf16 v[16:19],v[144:147],v[108:111],0\nds_read_b64_tr_b16 a[154:155],v66 offset:8704\nds_read_b64_tr_b16 a[156:157],v67 offset:8192\nv_mfma_f32_16x16x32_bf16 v[16:19],v[176:179],v[112:115],v[16:19]\n" \
"ds_read_b64_tr_b16 a[158:159],v67 offset:8704\nv_add_f32 v253,1.0,v253\nv_mfma_f32_16x16x32_bf16 v[20:23],v[148:151],v[108:111],0\nbuffer_load_dwordx4 v70,s[40:43],s65 offen offset:2048 lds\n" \
"v_cvt_pk_bf16_f32 v40,v36,v37\nv_mfma_f32_16x16x32_bf16 v[20:23],v[180:183],v[112:115],v[20:23]\nv_rcp_f32 v45,v45\nv_mfma_f32_16x16x32_bf16 v[24:27],v[152:155],v[108:111],0\n" \
"v_rcp_f32 v253,v253\nv_mfma_f32_16x16x32_bf16 v[24:27],v[184:187],v[112:115],v[24:27]\nbuffer_load_dwordx4 v71,s[40:43],s65 offen offset:3072 lds\nv_mul_f32 v44,v44,v45\n" \
"v_mfma_f32_16x16x32_bf16 v[28:31],v[156:159],v[108:111],0\nv_mul_f32 v252,v252,v253\nv_cvt_pk_bf16_f32 v41,v38,v39\nv_mfma_f32_16x16x32_bf16 v[28:31],v[188:191],v[112:115],v[28:31]\n" \
"v_cvt_pk_bf16_f32 v45,v44,v44\ns_waitcnt vmcnt(17)\nv_mfma_f32_16x16x32_bf16 v[0:3],a[0:3],v[116:119],v[0:3]\nglobal_load_dwordx4 v[104:107],v59,s[16:17] offset:2048 sc1\n" \
"s_waitcnt vmcnt(17)\nv_mfma_f32_16x16x32_bf16 v[0:3],a[32:35],v[120:123],v[0:3]\ns_add_u32 m0,s57,24576\nv_mfma_f32_16x16x32_bf16 v[4:7],a[4:7],v[116:119],v[4:7]\n" \
"v_cvt_pk_bf16_f32 v253,v252,v252\nbuffer_store_dwordx2 v[40:41],v61,s[36:39],s52 offen sc1\nv_mfma_f32_16x16x32_bf16 v[4:7],a[36:39],v[120:123],v[4:7]\nbuffer_load_dwordx4 v68,s[40:43],s66 offen lds\n" \
"buffer_store_short v45,v62,s[36:39],s52 offen sc1\nds_read_b128 v[36:39],v58 offset:5344\nds_read_b128 v[40:43],v58 offset:14912\nds_read_b128 v[44:47],v58 offset:24480\n" \
"ds_read_b128 v[48:51],v58 offset:34048\ns_waitcnt lgkmcnt(2)\nv_add_f32 v36,v36,v40\nv_add_f32 v37,v37,v41\n" \
"v_add_f32 v38,v38,v42\nv_add_f32 v39,v39,v43\nv_add_f32 v36,v36,v52\nv_add_f32 v37,v37,v53\n" \
"v_add_f32 v38,v38,v54\nv_add_f32 v39,v39,v55\ns_waitcnt lgkmcnt(0)\nv_add_f32 v44,v44,v48\n" \
"v_add_f32 v45,v45,v49\nv_add_f32 v46,v46,v50\nv_add_f32 v47,v47,v51\nv_add_f32 v36,v36,v44\n" \
"v_add_f32 v37,v37,v45\nv_add_f32 v38,v38,v46\nv_add_f32 v39,v39,v47\nv_mul_f32 v40,v36,v36\n" \
"v_mul_f32 v41,v37,v37\nv_mul_f32 v42,v38,v38\nv_mul_f32 v43,v39,v39\nv_fma_f32 v40,v40,s55,v57\n" \
"v_fma_f32 v41,v41,s55,v57\nv_fma_f32 v42,v42,s55,v57\nv_fma_f32 v43,v43,s55,v57\nv_mul_f32 v40,v36,v40\n" \
"v_mul_f32 v41,v37,v41\nv_mul_f32 v42,v38,v42\nv_mul_f32 v43,v39,v43\nv_exp_f32 v40,v40\n" \
"v_exp_f32 v41,v41\nv_exp_f32 v42,v42\nv_exp_f32 v43,v43\nv_add_f32 v40,1.0,v40\n" \
"v_add_f32 v41,1.0,v41\nv_add_f32 v42,1.0,v42\nv_add_f32 v43,1.0,v43\nv_rcp_f32 v40,v40\n" \
"v_rcp_f32 v41,v41\nv_rcp_f32 v42,v42\nv_rcp_f32 v43,v43\nv_mul_f32 v36,v36,v40\n" \
"v_mul_f32 v37,v37,v41\nv_mul_f32 v38,v38,v42\nv_mul_f32 v39,v39,v43\nv_cvt_pk_bf16_f32 v40,v36,v37\n" \
"v_cvt_pk_bf16_f32 v41,v38,v39\nv_mfma_f32_16x16x32_bf16 v[8:11],a[8:11],v[116:119],v[8:11]\ns_add_u32 s52,s52,409600\nbuffer_store_dwordx2 v[40:41],v61,s[36:39],s52 offen sc1\n" \
"v_mfma_f32_16x16x32_bf16 v[8:11],a[40:43],v[120:123],v[8:11]\nds_read_b128 v[36:39],v58\nds_read_b128 v[40:43],v58 offset:9568\nv_mfma_f32_16x16x32_bf16 v[12:15],a[12:15],v[116:119],v[12:15]\n" \
"buffer_load_dwordx4 v69,s[40:43],s66 offen offset:1024 lds\nds_read_b128 v[44:47],v58 offset:19136\nv_mfma_f32_16x16x32_bf16 v[12:15],a[44:47],v[120:123],v[12:15]\nds_read_b128 v[48:51],v58 offset:28704\n" \
"s_add_u32 s52,s52,409600\nv_mfma_f32_16x16x32_bf16 v[16:19],a[16:19],v[116:119],v[16:19]\nv_mfma_f32_16x16x32_bf16 v[16:19],a[48:51],v[120:123],v[16:19]\nbuffer_load_dwordx4 v70,s[40:43],s66 offen offset:2048 lds\n" \
"v_mfma_f32_16x16x32_bf16 v[20:23],a[20:23],v[116:119],v[20:23]\nv_mfma_f32_16x16x32_bf16 v[20:23],a[52:55],v[120:123],v[20:23]\nv_mfma_f32_16x16x32_bf16 v[24:27],a[24:27],v[116:119],v[24:27]\nbuffer_load_dwordx4 v71,s[40:43],s66 offen offset:3072 lds\n" \
"v_mfma_f32_16x16x32_bf16 v[24:27],a[56:59],v[120:123],v[24:27]\ns_waitcnt lgkmcnt(2)\nv_add_f32 v36,v36,v40\nv_mfma_f32_16x16x32_bf16 v[28:31],a[28:31],v[116:119],v[28:31]\n" \
"v_add_f32 v37,v37,v41\nv_add_f32 v38,v38,v42\nv_mfma_f32_16x16x32_bf16 v[28:31],a[60:63],v[120:123],v[28:31]\nglobal_load_dwordx4 v[196:199],v59,s[8:9] sc1\n" \
"s_waitcnt vmcnt(24)\nv_mfma_f32_16x16x32_bf16 v[0:3],a[64:67],v[124:127],v[0:3]\nv_add_f32 v39,v39,v43\ns_waitcnt vmcnt(23)\n" \
"v_mfma_f32_16x16x32_bf16 v[0:3],a[96:99],v[192:195],v[0:3]\nv_add_f32 v36,v36,v52\nv_add_f32 v37,v37,v53\nv_mfma_f32_16x16x32_bf16 v[4:7],a[68:71],v[124:127],v[4:7]\n" \
"global_load_dwordx4 v[200:203],v59,s[8:9] offset:1024 sc1\nv_add_f32 v38,v38,v54\nv_mfma_f32_16x16x32_bf16 v[4:7],a[100:103],v[192:195],v[4:7]\ns_waitcnt vmcnt(19)\n" \
"ds_read_b64_tr_b16 a[160:161],v64 offset:12288\nv_mfma_f32_16x16x32_bf16 v[8:11],a[72:75],v[124:127],v[8:11]\nds_read_b64_tr_b16 a[162:163],v64 offset:12800\nds_read_b64_tr_b16 a[164:165],v65 offset:12288\n" \
"v_mfma_f32_16x16x32_bf16 v[8:11],a[104:107],v[192:195],v[8:11]\nglobal_load_dwordx4 v[204:207],v59,s[8:9] offset:2048 sc1\nds_read_b64_tr_b16 a[166:167],v65 offset:12800\nv_mfma_f32_16x16x32_bf16 v[12:15],a[76:79],v[124:127],v[12:15]\n" \
"s_add_u32 s59,s59,0x10000\nbuffer_load_dword v63,v61,s[36:39],s59 offen sc1\nv_mfma_f32_16x16x32_bf16 v[12:15],a[108:111],v[192:195],v[12:15]\nv_add_u32 v59,819200,v59\n" \
"ds_read_b64_tr_b16 a[168:169],v66 offset:12288\nv_add_f32 v39,v39,v55\nv_mfma_f32_16x16x32_bf16 v[16:19],a[80:83],v[124:127],v[16:19]\nds_read_b64_tr_b16 a[170:171],v66 offset:12800\n" \
"ds_read_b64_tr_b16 a[172:173],v67 offset:12288\ns_waitcnt lgkmcnt(7)\nv_mfma_f32_16x16x32_bf16 v[20:23],a[84:87],v[124:127],v[20:23]\nds_read_b64_tr_b16 a[174:175],v67 offset:12800\n" \
"v_add_f32 v44,v44,v48\nv_add_f32 v45,v45,v49\nv_mfma_f32_16x16x32_bf16 v[24:27],a[88:91],v[124:127],v[24:27]\nv_add_f32 v46,v46,v50\n" \
"v_add_f32 v47,v47,v51\nv_mfma_f32_16x16x32_bf16 v[28:31],a[92:95],v[124:127],v[28:31]\nv_add_f32 v36,v36,v44\nv_add_f32 v37,v37,v45\n" \
"v_mfma_f32_16x16x32_bf16 v[224:227],a[112:115],v[92:95],v[224:227]\nv_add_f32 v38,v38,v46\nv_mfma_f32_16x16x32_bf16 v[224:227],a[144:147],v[96:99],v[224:227]\nv_add_f32 v39,v39,v47\n" \
"v_mul_f32 v40,v36,v36\nv_mfma_f32_16x16x32_bf16 v[228:231],a[116:119],v[92:95],v[228:231]\nv_mul_f32 v41,v37,v37\nv_mul_f32 v42,v38,v38\n" \
"v_mfma_f32_16x16x32_bf16 v[228:231],a[148:151],v[96:99],v[228:231]\nv_mul_f32 v43,v39,v39\nv_fma_f32 v40,v40,s55,v57\nv_mfma_f32_16x16x32_bf16 v[232:235],a[120:123],v[92:95],v[232:235]\n" \
"s_waitcnt vmcnt(17)\nds_read_b64_tr_b16 a[176:177],v64 offset:16384\nds_read_b64_tr_b16 a[178:179],v64 offset:16896\nv_mfma_f32_16x16x32_bf16 v[232:235],a[152:155],v[96:99],v[232:235]\n" \
"ds_read_b64_tr_b16 a[180:181],v65 offset:16384\nds_read_b64_tr_b16 a[182:183],v65 offset:16896\nv_fma_f32 v41,v41,s55,v57\nv_mfma_f32_16x16x32_bf16 v[236:239],a[124:127],v[92:95],v[236:239]\n" \
"ds_read_b64_tr_b16 a[184:185],v66 offset:16384\nds_read_b64_tr_b16 a[186:187],v66 offset:16896\nv_fma_f32 v42,v42,s55,v57\nv_mfma_f32_16x16x32_bf16 v[236:239],a[156:159],v[96:99],v[236:239]\n" \
"ds_read_b64_tr_b16 a[188:189],v67 offset:16384\nds_read_b64_tr_b16 a[190:191],v67 offset:16896\ns_waitcnt lgkmcnt(15)\nv_mfma_f32_16x16x32_bf16 v[208:211],a[128:131],v[96:99],v[208:211]\n" \
"v_fma_f32 v43,v43,s55,v57\ns_waitcnt lgkmcnt(8)\ns_waitcnt vmcnt(16)\nv_mfma_f32_16x16x32_bf16 v[208:211],a[160:163],v[100:103],v[208:211]\n" \
"s_waitcnt vmcnt(12)\nds_read_b64_tr_b16 a[192:193],v64 offset:20480\nds_read_b64_tr_b16 a[194:195],v64 offset:20992\nds_read_b64_tr_b16 a[196:197],v65 offset:20480\n" \
"ds_read_b64_tr_b16 a[198:199],v65 offset:20992\nds_read_b64_tr_b16 a[200:201],v66 offset:20480\nds_read_b64_tr_b16 a[202:203],v66 offset:20992\nds_read_b64_tr_b16 a[204:205],v67 offset:20480\n" \
"ds_read_b64_tr_b16 a[206:207],v67 offset:20992\ns_waitcnt vmcnt(4)\nds_read_b64_tr_b16 a[208:209],v64 offset:24576\nds_read_b64_tr_b16 a[210:211],v64 offset:25088\n" \
"v_mfma_f32_16x16x32_bf16 v[212:215],a[132:135],v[96:99],v[212:215]\nds_read_b64_tr_b16 a[212:213],v65 offset:24576\nds_read_b64_tr_b16 a[214:215],v65 offset:25088\nv_mul_f32 v40,v36,v40\n" \
"v_mfma_f32_16x16x32_bf16 v[212:215],a[164:167],v[100:103],v[212:215]\nds_read_b64_tr_b16 a[216:217],v66 offset:24576\nds_read_b64_tr_b16 a[218:219],v66 offset:25088\nv_mul_f32 v41,v37,v41\n" \
"v_mfma_f32_16x16x32_bf16 v[216:219],a[136:139],v[96:99],v[216:219]\nds_read_b64_tr_b16 a[220:221],v67 offset:24576\nds_read_b64_tr_b16 a[222:223],v67 offset:25088\nv_mul_f32 v42,v38,v42\n" \
"v_mfma_f32_16x16x32_bf16 v[216:219],a[168:171],v[100:103],v[216:219]\nv_mul_f32 v43,v39,v43\nv_exp_f32 v40,v40\nv_mfma_f32_16x16x32_bf16 v[220:223],a[140:143],v[96:99],v[220:223]\n" \
"v_exp_f32 v41,v41\nv_mfma_f32_16x16x32_bf16 v[220:223],a[172:175],v[100:103],v[220:223]\nv_exp_f32 v42,v42\ns_waitcnt lgkmcnt(15)\n" \
"v_mfma_f32_16x16x32_bf16 v[224:227],a[176:179],v[100:103],v[224:227]\ns_waitcnt lgkmcnt(0)\nv_mfma_f32_16x16x32_bf16 v[224:227],a[208:211],v[104:107],v[224:227]\nv_exp_f32 v43,v43\n" \
"v_add_f32 v40,1.0,v40\nv_mfma_f32_16x16x32_bf16 v[228:231],a[180:183],v[100:103],v[228:231]\nv_add_f32 v41,1.0,v41\nv_add_f32 v42,1.0,v42\n" \
"v_add_f32 v43,1.0,v43\nv_mfma_f32_16x16x32_bf16 v[228:231],a[212:215],v[104:107],v[228:231]\nv_rcp_f32 v40,v40\nv_mfma_f32_16x16x32_bf16 v[232:235],a[184:187],v[100:103],v[232:235]\n" \
"v_rcp_f32 v41,v41\nv_mfma_f32_16x16x32_bf16 v[232:235],a[216:219],v[104:107],v[232:235]\nv_rcp_f32 v42,v42\nv_mfma_f32_16x16x32_bf16 v[236:239],a[188:191],v[100:103],v[236:239]\n" \
"v_rcp_f32 v43,v43\nv_mul_f32 v36,v36,v40\nv_mul_f32 v37,v37,v41\nv_mul_f32 v38,v38,v42\n" \
"v_mul_f32 v39,v39,v43\nv_cvt_pk_bf16_f32 v40,v36,v37\nv_cvt_pk_bf16_f32 v41,v38,v39\nv_mfma_f32_16x16x32_bf16 v[236:239],a[220:223],v[104:107],v[236:239]\n" \
"buffer_store_short v253,v62,s[36:39],s52 offen sc1\nv_mfma_f32_16x16x32_bf16 v[208:211],a[192:195],v[104:107],v[208:211]\nv_and_b32 v58,0x3ff,v59\nv_add_u32 v58,s99,v58\n" \
"v_mfma_f32_16x16x32_bf16 v[212:215],a[196:199],v[104:107],v[212:215]\nv_add_u32 v46,s98,v58\nds_read_b64 v[56:57],v46 offset:28680\nv_mfma_f32_16x16x32_bf16 v[216:219],a[200:203],v[104:107],v[216:219]\n" \
"buffer_store_dwordx2 v[40:41],v61,s[36:39],s52 offen sc1\nglobal_load_dwordx4 v[72:75],v59,s[12:13] sc1\nv_mfma_f32_16x16x32_bf16 v[220:223],a[204:207],v[104:107],v[220:223]\nv_mfma_f32_16x16x32_bf16 v[16:19],a[112:115],v[192:195],v[16:19]\n" \
"s_waitcnt vmcnt(6)\nv_mfma_f32_16x16x32_bf16 v[16:19],a[144:147],v[196:199],v[16:19]\nglobal_load_dwordx4 v[76:79],v59,s[12:13] offset:1024 sc1\nv_mfma_f32_16x16x32_bf16 v[20:23],a[116:119],v[192:195],v[20:23]\n" \
"s_waitcnt lgkmcnt(0)\ns_barrier\ns_mov_b32 m0,s58\nv_mfma_f32_16x16x32_bf16 v[20:23],a[148:151],v[196:199],v[20:23]\n" \
"ds_write_addtid_b32 v208\nds_write_addtid_b32 v209 offset:256\nv_mfma_f32_16x16x32_bf16 v[24:27],a[120:123],v[192:195],v[24:27]\nglobal_load_dwordx4 v[80:83],v59,s[12:13] offset:2048 sc1\n" \
"ds_write_addtid_b32 v210 offset:512\nv_mfma_f32_16x16x32_bf16 v[24:27],a[152:155],v[196:199],v[24:27]\nds_write_addtid_b32 v211 offset:768\nds_write_addtid_b32 v212 offset:1024\n" \
"v_mfma_f32_16x16x32_bf16 v[28:31],a[124:127],v[192:195],v[28:31]\nds_write_addtid_b32 v213 offset:1280\nds_write_addtid_b32 v214 offset:1536\nv_mfma_f32_16x16x32_bf16 v[28:31],a[156:159],v[196:199],v[28:31]\n" \
"global_load_dwordx4 v[84:87],v59,s[14:15] sc1\nds_write_addtid_b32 v215 offset:1792\nv_mfma_f32_16x16x32_bf16 v[0:3],a[128:131],v[196:199],v[0:3]\nds_write_addtid_b32 v216 offset:2048\n" \
"s_waitcnt vmcnt(8)\nv_mfma_f32_16x16x32_bf16 v[0:3],a[160:163],v[200:203],v[0:3]\nds_write_addtid_b32 v217 offset:2304\nds_write_addtid_b32 v218 offset:2560\n" \
"v_mfma_f32_16x16x32_bf16 v[4:7],a[132:135],v[196:199],v[4:7]\nglobal_load_dwordx4 v[88:91],v59,s[14:15] offset:1024 sc1\nds_write_addtid_b32 v219 offset:2816\nv_mfma_f32_16x16x32_bf16 v[4:7],a[164:167],v[200:203],v[4:7]\n" \
"ds_write_addtid_b32 v220 offset:3072\nds_write_addtid_b32 v221 offset:3328\nv_mfma_f32_16x16x32_bf16 v[8:11],a[136:139],v[196:199],v[8:11]\nds_write_addtid_b32 v222 offset:3584\n" \
"ds_write_addtid_b32 v223 offset:3840\nv_mfma_f32_16x16x32_bf16 v[8:11],a[168:171],v[200:203],v[8:11]\nglobal_load_dwordx4 v[92:95],v59,s[14:15] offset:2048 sc1\nds_write_addtid_b32 v224 offset:4096\n" \
"v_mfma_f32_16x16x32_bf16 v[12:15],a[140:143],v[196:199],v[12:15]\nds_write_addtid_b32 v225 offset:4352\nds_write_addtid_b32 v226 offset:4608\nv_mfma_f32_16x16x32_bf16 v[12:15],a[172:175],v[200:203],v[12:15]\n" \
"ds_write_addtid_b32 v227 offset:4864\nds_write_addtid_b32 v228 offset:5120\nv_mfma_f32_16x16x32_bf16 v[16:19],a[176:179],v[200:203],v[16:19]\nglobal_load_dwordx4 v[96:99],v59,s[16:17] sc1\n" \
"s_waitcnt vmcnt(10)\nv_mfma_f32_16x16x32_bf16 v[16:19],a[208:211],v[204:207],v[16:19]\nds_write_addtid_b32 v229 offset:5376\nds_write_addtid_b32 v230 offset:5632\n" \
"v_mfma_f32_16x16x32_bf16 v[20:23],a[180:183],v[200:203],v[20:23]\nds_write_addtid_b32 v231 offset:5888\nds_write_addtid_b32 v232 offset:6144\nv_mfma_f32_16x16x32_bf16 v[20:23],a[212:215],v[204:207],v[20:23]\n" \
"global_load_dwordx4 v[100:103],v59,s[16:17] offset:1024 sc1\nds_write_addtid_b32 v233 offset:6400\nv_mfma_f32_16x16x32_bf16 v[24:27],a[184:187],v[200:203],v[24:27]\nds_write_addtid_b32 v234 offset:6656\n" \
"ds_write_addtid_b32 v235 offset:6912\nv_mfma_f32_16x16x32_bf16 v[24:27],a[216:219],v[204:207],v[24:27]\nds_write_addtid_b32 v236 offset:7168\nds_write_addtid_b32 v237 offset:7424\n" \
"v_mfma_f32_16x16x32_bf16 v[28:31],a[188:191],v[200:203],v[28:31]\nglobal_load_dwordx4 v[104:107],v59,s[16:17] offset:2048 sc1\nds_write_addtid_b32 v238 offset:7680\nv_mfma_f32_16x16x32_bf16 v[28:31],a[220:223],v[204:207],v[28:31]\n" \
"ds_write_addtid_b32 v239 offset:7936\nv_mfma_f32_16x16x32_bf16 v[0:3],a[192:195],v[204:207],v[0:3]\nv_mfma_f32_16x16x32_bf16 v[4:7],a[196:199],v[204:207],v[4:7]\nv_mfma_f32_16x16x32_bf16 v[8:11],a[200:203],v[204:207],v[8:11]\n" \
"v_mfma_f32_16x16x32_bf16 v[12:15],a[204:207],v[204:207],v[12:15]\ns_waitcnt vmcnt(7)\nv_mfma_f32_16x16x32_bf16 v[208:211],v[128:131],v[72:75],0\ns_mov_b64 exec,1\n" \
"buffer_store_dword v61,off,s[36:39],s54 sc1\ns_mov_b64 exec,-1\nv_mfma_f32_16x16x32_bf16 v[208:211],v[160:163],v[76:79],v[208:211]\nv_cmp_eq_u32 vcc,0,v63\n" \
"v_mfma_f32_16x16x32_bf16 v[212:215],v[132:135],v[72:75],0\ns_waitcnt lgkmcnt(0)\ns_barrier\nv_mfma_f32_16x16x32_bf16 v[212:215],v[164:167],v[76:79],v[212:215]\n" \
"s_and_b64 vcc,vcc,s[86:87]\ns_cbranch_scc1 .LsbF_%=\n.LbbF_%=:\nv_mfma_f32_16x16x32_bf16 v[216:219],v[136:139],v[72:75],0\n" \
"global_load_dwordx4 v[108:111],v59,s[4:5] sc1\nds_write_addtid_b32 v0 offset:32768\nv_mfma_f32_16x16x32_bf16 v[216:219],v[168:171],v[76:79],v[216:219]\nds_write_addtid_b32 v1 offset:33024\n" \
"ds_write_addtid_b32 v2 offset:33280\nv_mfma_f32_16x16x32_bf16 v[220:223],v[140:143],v[72:75],0\nds_write_addtid_b32 v3 offset:33536\nds_write_addtid_b32 v4 offset:33792\n" \
"v_mfma_f32_16x16x32_bf16 v[220:223],v[172:175],v[76:79],v[220:223]\nglobal_load_dwordx4 v[112:115],v59,s[4:5] offset:1024 sc1\nds_write_addtid_b32 v5 offset:34048\nv_mfma_f32_16x16x32_bf16 v[224:227],v[144:147],v[72:75],0\n" \
"ds_write_addtid_b32 v6 offset:34304\nds_write_addtid_b32 v7 offset:34560\nv_mfma_f32_16x16x32_bf16 v[224:227],v[176:179],v[76:79],v[224:227]\nds_write_addtid_b32 v8 offset:34816\n" \
"ds_write_addtid_b32 v9 offset:35072\nv_mfma_f32_16x16x32_bf16 v[228:231],v[148:151],v[72:75],0\nglobal_load_dwordx4 v[116:119],v59,s[4:5] offset:2048 sc1\nds_write_addtid_b32 v10 offset:35328\n" \
"v_mfma_f32_16x16x32_bf16 v[228:231],v[180:183],v[76:79],v[228:231]\nds_write_addtid_b32 v11 offset:35584\nds_write_addtid_b32 v12 offset:35840\nv_mfma_f32_16x16x32_bf16 v[232:235],v[152:155],v[72:75],0\n" \
"ds_write_addtid_b32 v13 offset:36096\nds_write_addtid_b32 v14 offset:36352\nv_mfma_f32_16x16x32_bf16 v[232:235],v[184:187],v[76:79],v[232:235]\nglobal_load_dwordx4 v[120:123],v59,s[6:7] sc1\n" \
"ds_write_addtid_b32 v15 offset:36608\nv_mfma_f32_16x16x32_bf16 v[236:239],v[156:159],v[72:75],0\nds_write_addtid_b32 v16 offset:36864\nds_write_addtid_b32 v17 offset:37120\n" \
"v_mfma_f32_16x16x32_bf16 v[236:239],v[188:191],v[76:79],v[236:239]\nds_write_addtid_b32 v18 offset:37376\ns_waitcnt vmcnt(10)\nv_mfma_f32_16x16x32_bf16 v[208:211],a[0:3],v[80:83],v[208:211]\n" \
"global_load_dwordx4 v[124:127],v59,s[6:7] offset:1024 sc1\nds_write_addtid_b32 v19 offset:37632\nv_mfma_f32_16x16x32_bf16 v[208:211],a[32:35],v[84:87],v[208:211]\nds_write_addtid_b32 v20 offset:37888\n" \
"ds_write_addtid_b32 v21 offset:38144\nv_mfma_f32_16x16x32_bf16 v[212:215],a[4:7],v[80:83],v[212:215]\nds_write_addtid_b32 v22 offset:38400\nds_write_addtid_b32 v23 offset:38656\n" \
"v_mfma_f32_16x16x32_bf16 v[212:215],a[36:39],v[84:87],v[212:215]\nglobal_load_dwordx4 v[192:195],v59,s[6:7] offset:2048 sc1\nds_write_addtid_b32 v24 offset:38912\nv_mfma_f32_16x16x32_bf16 v[216:219],a[8:11],v[80:83],v[216:219]\n" \
"ds_write_addtid_b32 v25 offset:39168\nds_write_addtid_b32 v26 offset:39424\nv_mfma_f32_16x16x32_bf16 v[216:219],a[40:43],v[84:87],v[216:219]\nds_write_addtid_b32 v27 offset:39680\n" \
"ds_write_addtid_b32 v28 offset:39936\nv_mfma_f32_16x16x32_bf16 v[220:223],a[12:15],v[80:83],v[220:223]\nglobal_load_dwordx4 v[196:199],v59,s[8:9] sc1\nds_write_addtid_b32 v29 offset:40192\n" \
"v_mfma_f32_16x16x32_bf16 v[220:223],a[44:47],v[84:87],v[220:223]\nds_write_addtid_b32 v30 offset:40448\nds_write_addtid_b32 v31 offset:40704\nv_mfma_f32_16x16x32_bf16 v[224:227],a[16:19],v[80:83],v[224:227]\n" \
"ds_read_b128 v[32:35],v58\nds_read_b128 v[36:39],v58 offset:8192\nv_mfma_f32_16x16x32_bf16 v[224:227],a[48:51],v[84:87],v[224:227]\nglobal_load_dwordx4 v[200:203],v59,s[8:9] offset:1024 sc1\n" \
"ds_read_b128 v[40:43],v58 offset:16384\nv_mfma_f32_16x16x32_bf16 v[228:231],a[20:23],v[80:83],v[228:231]\nds_read_b128 v[44:47],v58 offset:24576\nds_read_b128 v[48:51],v58 offset:4096\n" \
"v_mfma_f32_16x16x32_bf16 v[228:231],a[52:55],v[84:87],v[228:231]\nds_read_b128 v[240:243],v58 offset:12288\nds_read_b128 v[244:247],v58 offset:20480\nv_mfma_f32_16x16x32_bf16 v[232:235],a[24:27],v[80:83],v[232:235]\n" \
"global_load_dwordx4 v[204:207],v59,s[8:9] offset:2048 sc1\nds_read_b128 v[248:251],v58 offset:28672\nv_mfma_f32_16x16x32_bf16 v[232:235],a[56:59],v[84:87],v[232:235]\ns_waitcnt lgkmcnt(0)\n" \
"s_barrier\nv_mfma_f32_16x16x32_bf16 v[236:239],a[28:31],v[80:83],v[236:239]\nv_mfma_f32_16x16x32_bf16 v[236:239],a[60:63],v[84:87],v[236:239]\ns_waitcnt vmcnt(13)\n" \
"v_mfma_f32_16x16x32_bf16 v[208:211],a[64:67],v[88:91],v[208:211]\nv_mfma_f32_16x16x32_bf16 v[208:211],a[96:99],v[92:95],v[208:211]\nv_mfma_f32_16x16x32_bf16 v[212:215],a[68:71],v[88:91],v[212:215]\nv_add_f32 v32,v32,v36\n" \
"v_mfma_f32_16x16x32_bf16 v[212:215],a[100:103],v[92:95],v[212:215]\nv_add_f32 v33,v33,v37\nv_add_f32 v34,v34,v38\nv_mfma_f32_16x16x32_bf16 v[216:219],a[72:75],v[88:91],v[216:219]\n" \
"v_add_f32 v35,v35,v39\nv_add_f32 v40,v40,v44\nv_mfma_f32_16x16x32_bf16 v[216:219],a[104:107],v[92:95],v[216:219]\nv_add_f32 v41,v41,v45\n" \
"v_add_f32 v42,v42,v46\nv_mfma_f32_16x16x32_bf16 v[220:223],a[76:79],v[88:91],v[220:223]\nv_add_f32 v43,v43,v47\nv_add_f32 v32,v32,v40\n" \
"v_mfma_f32_16x16x32_bf16 v[220:223],a[108:111],v[92:95],v[220:223]\nv_add_f32 v33,v33,v41\nv_add_f32 v34,v34,v42\nv_mfma_f32_16x16x32_bf16 v[224:227],a[80:83],v[88:91],v[224:227]\n" \
"v_add_f32 v35,v35,v43\nv_cvt_pk_bf16_f32 v36,v32,v33\nv_mfma_f32_16x16x32_bf16 v[224:227],a[112:115],v[92:95],v[224:227]\nv_cvt_pk_bf16_f32 v37,v34,v35\n" \
"v_mfma_f32_16x16x32_bf16 v[228:231],a[84:87],v[88:91],v[228:231]\nv_add_f32 v48,v48,v240\nv_add_f32 v49,v49,v241\nv_mfma_f32_16x16x32_bf16 v[228:231],a[116:119],v[92:95],v[228:231]\n" \
"v_add_f32 v50,v50,v242\nv_add_f32 v51,v51,v243\nv_mfma_f32_16x16x32_bf16 v[232:235],a[88:91],v[88:91],v[232:235]\nv_add_f32 v244,v244,v248\n" \
"v_add_f32 v245,v245,v249\nv_mfma_f32_16x16x32_bf16 v[232:235],a[120:123],v[92:95],v[232:235]\nv_add_f32 v246,v246,v250\nv_add_f32 v247,v247,v251\n" \
"v_mfma_f32_16x16x32_bf16 v[236:239],a[92:95],v[88:91],v[236:239]\nv_add_f32 v48,v48,v244\nv_add_f32 v49,v49,v245\nv_mfma_f32_16x16x32_bf16 v[236:239],a[124:127],v[92:95],v[236:239]\n" \
"v_add_f32 v50,v50,v246\ns_waitcnt vmcnt(11)\nv_mfma_f32_16x16x32_bf16 v[208:211],a[128:131],v[96:99],v[208:211]\nv_add_f32 v51,v51,v247\n" \
"v_cvt_pk_bf16_f32 v38,v48,v49\nv_mfma_f32_16x16x32_bf16 v[208:211],a[160:163],v[100:103],v[208:211]\nv_cvt_pk_bf16_f32 v39,v50,v51\nbuffer_store_dwordx4 v[36:39],v59,s[36:39],s74 offen sc1\n" \
"ds_read_b128 v[32:35],v58 offset:32768\nv_mfma_f32_16x16x32_bf16 v[212:215],a[132:135],v[96:99],v[212:215]\nds_read_b128 v[36:39],v58 offset:40960\nds_read_b128 v[40:43],v58 offset:49152\n" \
"v_mfma_f32_16x16x32_bf16 v[212:215],a[164:167],v[100:103],v[212:215]\nds_read_b128 v[44:47],v58 offset:57344\nds_read_b128 v[48:51],v58 offset:36864\nv_mfma_f32_16x16x32_bf16 v[216:219],a[136:139],v[96:99],v[216:219]\n" \
"ds_read_b128 v[240:243],v58 offset:45056\nds_read_b128 v[244:247],v58 offset:53248\nv_mfma_f32_16x16x32_bf16 v[216:219],a[168:171],v[100:103],v[216:219]\nds_read_b128 v[248:251],v58 offset:61440\n" \
"v_mfma_f32_16x16x32_bf16 v[220:223],a[140:143],v[96:99],v[220:223]\nv_mfma_f32_16x16x32_bf16 v[220:223],a[172:175],v[100:103],v[220:223]\nv_mfma_f32_16x16x32_bf16 v[224:227],a[144:147],v[96:99],v[224:227]\nv_mfma_f32_16x16x32_bf16 v[224:227],a[176:179],v[100:103],v[224:227]\n" \
"v_mfma_f32_16x16x32_bf16 v[228:231],a[148:151],v[96:99],v[228:231]\nv_mfma_f32_16x16x32_bf16 v[228:231],a[180:183],v[100:103],v[228:231]\ns_waitcnt lgkmcnt(4)\nv_add_f32 v32,v32,v36\n" \
"v_add_f32 v33,v33,v37\nv_mfma_f32_16x16x32_bf16 v[232:235],a[152:155],v[96:99],v[232:235]\nv_add_f32 v34,v34,v38\nv_add_f32 v35,v35,v39\n" \
"v_add_f32 v40,v40,v44\nv_mfma_f32_16x16x32_bf16 v[232:235],a[184:187],v[100:103],v[232:235]\nv_add_f32 v41,v41,v45\nv_add_f32 v42,v42,v46\n" \
"v_add_f32 v43,v43,v47\nv_mfma_f32_16x16x32_bf16 v[236:239],a[156:159],v[96:99],v[236:239]\nv_add_f32 v32,v32,v40\nv_add_f32 v33,v33,v41\n" \
"v_add_f32 v34,v34,v42\nv_mfma_f32_16x16x32_bf16 v[236:239],a[188:191],v[100:103],v[236:239]\nv_add_f32 v35,v35,v43\nv_cvt_pk_bf16_f32 v36,v32,v33\n" \
"s_waitcnt vmcnt(11)\nv_mfma_f32_16x16x32_bf16 v[208:211],a[192:195],v[104:107],v[208:211]\nv_cvt_pk_bf16_f32 v37,v34,v35\ns_waitcnt lgkmcnt(0)\n" \
"v_add_f32 v48,v48,v240\nv_mfma_f32_16x16x32_bf16 v[212:215],a[196:199],v[104:107],v[212:215]\nv_add_f32 v49,v49,v241\nv_add_f32 v50,v50,v242\n" \
"v_add_f32 v51,v51,v243\nv_mfma_f32_16x16x32_bf16 v[216:219],a[200:203],v[104:107],v[216:219]\nv_add_f32 v244,v244,v248\nv_add_f32 v245,v245,v249\n" \
"v_mfma_f32_16x16x32_bf16 v[220:223],a[204:207],v[104:107],v[220:223]\nv_add_f32 v246,v246,v250\nv_add_f32 v247,v247,v251\nv_mfma_f32_16x16x32_bf16 v[224:227],a[208:211],v[104:107],v[224:227]\n" \
"v_add_f32 v48,v48,v244\nv_add_f32 v49,v49,v245\nv_mfma_f32_16x16x32_bf16 v[228:231],a[212:215],v[104:107],v[228:231]\nv_add_f32 v50,v50,v246\n" \
"v_add_f32 v51,v51,v247\nv_mfma_f32_16x16x32_bf16 v[232:235],a[216:219],v[104:107],v[232:235]\nv_cvt_pk_bf16_f32 v38,v48,v49\nv_cvt_pk_bf16_f32 v39,v50,v51\n" \
"v_mfma_f32_16x16x32_bf16 v[236:239],a[220:223],v[104:107],v[236:239]\nv_add_u32 v59,409600,v59\nbuffer_store_dwordx4 v[36:39],v59,s[36:39],s74 offen sc1\ns_branch .Le_%=\n" \
".Lzl_%=:\ns_branch .Lzr_%=\n.Lls_%=:\nv_mfma_f32_16x16x32_bf16 v[20:23],v[64:67],a[0:3],0\n" \
"v_and_b32 v59,0x3ff,v59\nv_mfma_f32_16x16x32_bf16 v[20:23],v[68:71],a[16:19],v[20:23]\ns_add_u32 s4,s12,409600\ns_addc_u32 s5,s13,0\n" \
"v_mfma_f32_16x16x32_bf16 v[24:27],v[64:67],a[4:7],0\ns_add_u32 s6,s14,409600\ns_addc_u32 s7,s15,0\nv_mfma_f32_16x16x32_bf16 v[24:27],v[68:71],a[20:23],v[24:27]\n" \
"s_add_u32 s8,s16,409600\ns_addc_u32 s9,s17,0\nv_mfma_f32_16x16x32_bf16 v[28:31],v[64:67],a[8:11],0\nds_write_addtid_b32 v0\n" \
"ds_write_addtid_b32 v1 offset:544\nv_mfma_f32_16x16x32_bf16 v[28:31],v[68:71],a[24:27],v[28:31]\nds_write_addtid_b32 v2 offset:256\nds_write_addtid_b32 v3 offset:800\n" \
"v_mfma_f32_16x16x32_bf16 v[32:35],v[64:67],a[12:15],0\nds_write_addtid_b32 v4 offset:1056\nds_write_addtid_b32 v5 offset:1600\nv_mfma_f32_16x16x32_bf16 v[32:35],v[68:71],a[28:31],v[32:35]\n" \
"ds_write_addtid_b32 v6 offset:1312\ns_waitcnt vmcnt(16)\nv_mfma_f32_16x16x32_bf16 v[20:23],v[72:75],a[32:35],v[20:23]\nv_add_u32 v71,s57,v59\n" \
"s_waitcnt vmcnt(15)\nv_mfma_f32_16x16x32_bf16 v[20:23],v[76:79],a[48:51],v[20:23]\ns_waitcnt vmcnt(8)\nv_cmp_eq_u32 vcc,0,v63\n" \
"v_mfma_f32_16x16x32_bf16 v[24:27],v[72:75],a[36:39],v[24:27]\nds_read_b32 v64,v71 offset:28672\nds_read_b64 v[68:69],v71 offset:29696\nv_mfma_f32_16x16x32_bf16 v[24:27],v[76:79],a[52:55],v[24:27]\n" \
"s_and_b64 vcc,vcc,s[86:87]\ns_cbranch_scc1 .LsaS_%=\n.LbaS_%=:\nv_mfma_f32_16x16x32_bf16 v[28:31],v[72:75],a[40:43],v[28:31]\n" \
"ds_write_addtid_b32 v7 offset:1856\nds_write_addtid_b32 v8 offset:2112\nv_mfma_f32_16x16x32_bf16 v[28:31],v[76:79],a[56:59],v[28:31]\nds_write_addtid_b32 v9 offset:2656\n" \
"ds_write_addtid_b32 v10 offset:2368\nv_mfma_f32_16x16x32_bf16 v[32:35],v[72:75],a[44:47],v[32:35]\ns_waitcnt lgkmcnt(4)\nv_xor_b32 v65,32,v64\n" \
"v_mfma_f32_16x16x32_bf16 v[32:35],v[76:79],a[60:63],v[32:35]\nv_xor_b32 v66,64,v64\nv_mfma_f32_16x16x32_bf16 v[20:23],v[80:83],a[64:67],v[20:23]\nglobal_load_dwordx4 v[72:75],v59,s[12:13] sc1\n" \
"v_mfma_f32_16x16x32_bf16 v[20:23],v[84:87],a[80:83],v[20:23]\nv_xor_b32 v67,96,v64\nv_add_u32 v70,0xf800,v68\nv_mfma_f32_16x16x32_bf16 v[24:27],v[80:83],a[68:71],v[24:27]\n" \
"v_add_u32 v71,0xf800,v69\nds_write_addtid_b32 v11 offset:2912\nv_mfma_f32_16x16x32_bf16 v[24:27],v[84:87],a[84:87],v[24:27]\nglobal_load_dwordx4 v[76:79],v59,s[12:13] offset:1024 sc1\n" \
"ds_write_addtid_b32 v12 offset:3168\nv_mfma_f32_16x16x32_bf16 v[28:31],v[80:83],a[72:75],v[28:31]\nds_write_addtid_b32 v13 offset:3712\nds_write_addtid_b32 v14 offset:3424\n" \
"v_mfma_f32_16x16x32_bf16 v[28:31],v[84:87],a[88:91],v[28:31]\nds_write_addtid_b32 v15 offset:3968\nds_write_addtid_b32 v16 offset:4224\nv_mfma_f32_16x16x32_bf16 v[32:35],v[80:83],a[76:79],v[32:35]\n" \
"ds_write_addtid_b32 v17 offset:4512\nds_write_addtid_b32 v18 offset:4800\nv_mfma_f32_16x16x32_bf16 v[32:35],v[84:87],a[92:95],v[32:35]\nds_write_addtid_b32 v19 offset:5088\n" \
"v_mfma_f32_16x16x32_bf16 v[20:23],v[88:91],a[96:99],v[20:23]\nglobal_load_dwordx4 v[80:83],v59,s[12:13] offset:2048 sc1\nv_mfma_f32_16x16x32_bf16 v[20:23],v[92:95],a[112:115],v[20:23]\nds_read_b64_tr_b16 a[0:1],v64\n" \
"ds_read_b64_tr_b16 a[2:3],v64 offset:512\nv_mfma_f32_16x16x32_bf16 v[24:27],v[88:91],a[100:103],v[24:27]\nds_read_b64_tr_b16 a[4:5],v65\nds_read_b64_tr_b16 a[6:7],v65 offset:512\n" \
"v_mfma_f32_16x16x32_bf16 v[24:27],v[92:95],a[116:119],v[24:27]\nglobal_load_dwordx4 v[84:87],v59,s[14:15] sc1\nds_read_b64_tr_b16 a[8:9],v66\nv_mfma_f32_16x16x32_bf16 v[28:31],v[88:91],a[104:107],v[28:31]\n" \
"ds_read_b64_tr_b16 a[10:11],v66 offset:512\nds_read_b64_tr_b16 a[12:13],v67\nv_mfma_f32_16x16x32_bf16 v[28:31],v[92:95],a[120:123],v[28:31]\ns_waitcnt lgkmcnt(0)\n" \
"s_barrier\nv_mfma_f32_16x16x32_bf16 v[32:35],v[88:91],a[108:111],v[32:35]\nds_read_b64_tr_b16 a[14:15],v67 offset:512\nds_read_b64_tr_b16 a[16:17],v64 offset:4096\n" \
"v_mfma_f32_16x16x32_bf16 v[32:35],v[92:95],a[124:127],v[32:35]\nds_read_b64_tr_b16 a[18:19],v64 offset:4608\ns_waitcnt vmcnt(11)\nv_mfma_f32_16x16x32_bf16 v[20:23],v[96:99],a[128:131],v[20:23]\n" \
"ds_read_b64_tr_b16 a[20:21],v65 offset:4096\ns_waitcnt vmcnt(10)\nv_mfma_f32_16x16x32_bf16 v[20:23],v[100:103],a[144:147],v[20:23]\nds_read_b64_tr_b16 a[22:23],v65 offset:4608\n" \
"ds_read_b64_tr_b16 a[24:25],v66 offset:4096\nv_mfma_f32_16x16x32_bf16 v[24:27],v[96:99],a[132:135],v[24:27]\ns_waitcnt lgkmcnt(5)\ns_add_u32 m0,s57,0\n" \
"v_mfma_f32_16x16x32_bf16 v[24:27],v[100:103],a[148:151],v[24:27]\nbuffer_load_dwordx4 v68,s[40:43],s60 offen lds\nds_read_b64_tr_b16 a[26:27],v66 offset:4608\nv_mfma_f32_16x16x32_bf16 v[28:31],v[96:99],a[136:139],v[28:31]\n" \
"ds_read_b64_tr_b16 a[28:29],v67 offset:4096\nds_read_b64_tr_b16 a[30:31],v67 offset:4608\nv_mfma_f32_16x16x32_bf16 v[28:31],v[100:103],a[152:155],v[28:31]\nds_read_b64_tr_b16 a[32:33],v64 offset:8192\n" \
"ds_read_b64_tr_b16 a[34:35],v64 offset:8704\nv_mfma_f32_16x16x32_bf16 v[32:35],v[96:99],a[140:143],v[32:35]\nbuffer_load_dwordx4 v69,s[40:43],s60 offen offset:1024 lds\nds_read_b64_tr_b16 a[36:37],v65 offset:8192\n" \
"v_mfma_f32_16x16x32_bf16 v[32:35],v[100:103],a[156:159],v[32:35]\nds_read_b64_tr_b16 a[38:39],v65 offset:8704\ns_waitcnt vmcnt(11)\nv_mfma_f32_16x16x32_bf16 v[20:23],v[104:107],a[160:163],v[20:23]\n" \
"ds_read_b64_tr_b16 a[40:41],v66 offset:8192\ns_waitcnt vmcnt(10)\nv_mfma_f32_16x16x32_bf16 v[20:23],v[108:111],a[176:179],v[20:23]\nbuffer_load_dwordx4 v70,s[40:43],s60 offen offset:2048 lds\n" \
"ds_read_b64_tr_b16 a[42:43],v66 offset:8704\nv_mfma_f32_16x16x32_bf16 v[24:27],v[104:107],a[164:167],v[24:27]\nds_read_b64_tr_b16 a[44:45],v67 offset:8192\nds_read_b64_tr_b16 a[46:47],v67 offset:8704\n" \
"v_mfma_f32_16x16x32_bf16 v[24:27],v[108:111],a[180:183],v[24:27]\nv_mul_f32 v36,v36,v40\nv_mul_f32 v37,v37,v41\nv_mfma_f32_16x16x32_bf16 v[28:31],v[104:107],a[168:171],v[28:31]\n" \
"buffer_load_dwordx4 v71,s[40:43],s60 offen offset:3072 lds\nds_read_b64_tr_b16 a[48:49],v64 offset:12288\nv_mfma_f32_16x16x32_bf16 v[28:31],v[108:111],a[184:187],v[28:31]\nds_read_b64_tr_b16 a[50:51],v64 offset:12800\n" \
"ds_read_b64_tr_b16 a[52:53],v65 offset:12288\nv_mfma_f32_16x16x32_bf16 v[32:35],v[104:107],a[172:175],v[32:35]\nds_read_b64_tr_b16 a[54:55],v65 offset:12800\nds_read_b64_tr_b16 a[56:57],v66 offset:12288\n" \
"v_mfma_f32_16x16x32_bf16 v[32:35],v[108:111],a[188:191],v[32:35]\nglobal_load_dwordx4 v[88:91],v59,s[14:15] offset:1024 sc1\ns_waitcnt vmcnt(12)\nv_mfma_f32_16x16x32_bf16 v[20:23],v[112:115],a[192:195],v[20:23]\n" \
"ds_read_b64_tr_b16 a[58:59],v66 offset:12800\ns_waitcnt vmcnt(11)\nv_mfma_f32_16x16x32_bf16 v[20:23],v[116:119],a[208:211],v[20:23]\nds_read_b64_tr_b16 a[60:61],v67 offset:12288\n" \
"ds_read_b64_tr_b16 a[62:63],v67 offset:12800\nv_mfma_f32_16x16x32_bf16 v[24:27],v[112:115],a[196:199],v[24:27]\nglobal_load_dwordx4 v[92:95],v59,s[14:15] offset:2048 sc1\nv_mul_f32 v38,v38,v42\n" \
"v_mfma_f32_16x16x32_bf16 v[24:27],v[116:119],a[212:215],v[24:27]\ns_waitcnt lgkmcnt(15)\ns_add_u32 m0,s57,4096\nv_mfma_f32_16x16x32_bf16 v[28:31],v[112:115],a[200:203],v[28:31]\n" \
"v_mul_f32 v39,v39,v43\nv_cvt_pk_bf16_f32 v40,v36,v37\nv_mfma_f32_16x16x32_bf16 v[28:31],v[116:119],a[216:219],v[28:31]\nbuffer_load_dwordx4 v68,s[40:43],s61 offen lds\n" \
"v_cvt_pk_bf16_f32 v41,v38,v39\nv_mfma_f32_16x16x32_bf16 v[32:35],v[112:115],a[204:207],v[32:35]\nds_read_b64_tr_b16 a[64:65],v64 offset:16384\nds_read_b64_tr_b16 a[66:67],v64 offset:16896\n" \
"v_mfma_f32_16x16x32_bf16 v[32:35],v[116:119],a[220:223],v[32:35]\nds_read_b64_tr_b16 a[68:69],v65 offset:16384\ns_waitcnt vmcnt(12)\nv_mfma_f32_16x16x32_bf16 v[20:23],v[120:123],a[224:227],v[20:23]\n" \
"buffer_load_dwordx4 v69,s[40:43],s61 offen offset:1024 lds\ns_waitcnt vmcnt(12)\nv_mfma_f32_16x16x32_bf16 v[20:23],v[124:127],a[240:243],v[20:23]\nds_read_b64_tr_b16 a[70:71],v65 offset:16896\n" \
"ds_read_b64_tr_b16 a[72:73],v66 offset:16384\nv_mfma_f32_16x16x32_bf16 v[24:27],v[120:123],a[228:231],v[24:27]\nds_read_b64_tr_b16 a[74:75],v66 offset:16896\nds_read_b64_tr_b16 a[76:77],v67 offset:16384\n" \
"v_mfma_f32_16x16x32_bf16 v[24:27],v[124:127],a[244:247],v[24:27]\nbuffer_load_dwordx4 v70,s[40:43],s61 offen offset:2048 lds\nds_read_b64_tr_b16 a[78:79],v67 offset:16896\nv_mfma_f32_16x16x32_bf16 v[28:31],v[120:123],a[232:235],v[28:31]\n" \
"buffer_store_dwordx2 v[40:41],v61,s[36:39],s52 offen sc1\nds_read_b128 v[36:39],v58\nds_read_b128 v[40:43],v58 offset:9568\nv_mfma_f32_16x16x32_bf16 v[28:31],v[124:127],a[248:251],v[28:31]\n" \
"ds_read_b128 v[44:47],v58 offset:19136\nds_read_b128 v[48:51],v58 offset:28704\nv_mfma_f32_16x16x32_bf16 v[32:35],v[120:123],a[236:239],v[32:35]\nbuffer_load_dwordx4 v71,s[40:43],s61 offen offset:3072 lds\n" \
"s_add_u32 s52,s52,409600\nv_mfma_f32_16x16x32_bf16 v[32:35],v[124:127],a[252:255],v[32:35]\ns_mov_b32 m0,s56\ns_waitcnt vmcnt(14)\n" \
"v_mfma_f32_16x16x32_bf16 v[208:211],v[128:131],v[72:75],0\nds_read_b64_tr_b16 a[80:81],v64 offset:20480\ns_waitcnt vmcnt(13)\nv_mfma_f32_16x16x32_bf16 v[208:211],v[160:163],v[76:79],v[208:211]\n" \
"global_load_dwordx4 v[108:111],v59,s[4:5] sc1\nds_write_addtid_b32 v20 offset:5344\nv_mfma_f32_16x16x32_bf16 v[212:215],v[132:135],v[72:75],0\nds_write_addtid_b32 v21 offset:5888\n" \
"ds_write_addtid_b32 v22 offset:5600\nv_mfma_f32_16x16x32_bf16 v[212:215],v[164:167],v[76:79],v[212:215]\nds_write_addtid_b32 v23 offset:6144\nds_write_addtid_b32 v24 offset:6400\n" \
"v_mfma_f32_16x16x32_bf16 v[216:219],v[136:139],v[72:75],0\nglobal_load_dwordx4 v[112:115],v59,s[4:5] offset:1024 sc1\nds_write_addtid_b32 v25 offset:6944\ns_waitcnt lgkmcnt(9)\n" \
"v_mfma_f32_16x16x32_bf16 v[216:219],v[168:171],v[76:79],v[216:219]\nds_write_addtid_b32 v26 offset:6656\nds_write_addtid_b32 v27 offset:7200\nv_add_f32 v36,v36,v40\n" \
"v_mfma_f32_16x16x32_bf16 v[220:223],v[140:143],v[72:75],0\nds_write_addtid_b32 v28 offset:7456\nds_write_addtid_b32 v29 offset:8000\nv_add_f32 v37,v37,v41\n" \
"v_mfma_f32_16x16x32_bf16 v[220:223],v[172:175],v[76:79],v[220:223]\nglobal_load_dwordx4 v[116:119],v59,s[4:5] offset:2048 sc1\nds_write_addtid_b32 v30 offset:7712\nv_add_f32 v38,v38,v42\n" \
"v_mfma_f32_16x16x32_bf16 v[224:227],v[144:147],v[72:75],0\nds_write_addtid_b32 v31 offset:8256\nds_write_addtid_b32 v32 offset:8512\nv_add_f32 v39,v39,v43\n" \
"v_mfma_f32_16x16x32_bf16 v[224:227],v[176:179],v[76:79],v[224:227]\nds_write_addtid_b32 v33 offset:9056\nds_write_addtid_b32 v34 offset:8768\nv_add_f32 v36,v36,v52\n" \
"v_mfma_f32_16x16x32_bf16 v[228:231],v[148:151],v[72:75],0\nglobal_load_dwordx4 v[120:123],v59,s[6:7] sc1\nds_write_addtid_b32 v35 offset:9312\nv_add_f32 v37,v37,v53\n" \
"v_mfma_f32_16x16x32_bf16 v[228:231],v[180:183],v[76:79],v[228:231]\nds_read_b64_tr_b16 a[82:83],v64 offset:20992\nds_read_b64_tr_b16 a[84:85],v65 offset:20480\nv_add_f32 v38,v38,v54\n" \
"v_mfma_f32_16x16x32_bf16 v[232:235],v[152:155],v[72:75],0\nds_read_b64_tr_b16 a[86:87],v65 offset:20992\nds_read_b64_tr_b16 a[88:89],v66 offset:20480\nv_add_f32 v39,v39,v55\n" \
"v_mfma_f32_16x16x32_bf16 v[232:235],v[184:187],v[76:79],v[232:235]\nglobal_load_dwordx4 v[124:127],v59,s[6:7] offset:1024 sc1\nds_read_b64_tr_b16 a[90:91],v66 offset:20992\ns_waitcnt lgkmcnt(15)\n" \
"v_mfma_f32_16x16x32_bf16 v[236:239],v[156:159],v[72:75],0\nds_read_b64_tr_b16 a[92:93],v67 offset:20480\nds_read_b64_tr_b16 a[94:95],v67 offset:20992\nv_add_f32 v44,v44,v48\n" \
"v_mfma_f32_16x16x32_bf16 v[236:239],v[188:191],v[76:79],v[236:239]\nv_add_f32 v45,v45,v49\ns_waitcnt lgkmcnt(15)\ns_waitcnt vmcnt(17)\n" \
"v_mfma_f32_16x16x32_bf16 v[208:211],a[0:3],v[80:83],v[208:211]\nv_add_f32 v46,v46,v50\ns_waitcnt vmcnt(16)\nv_mfma_f32_16x16x32_bf16 v[208:211],a[32:35],v[84:87],v[208:211]\n" \
"global_load_dwordx4 v[192:195],v59,s[6:7] offset:2048 sc1\nds_read_b64_tr_b16 a[96:97],v64 offset:24576\nv_add_f32 v47,v47,v51\nv_mfma_f32_16x16x32_bf16 v[212:215],a[4:7],v[80:83],v[212:215]\n" \
"s_waitcnt lgkmcnt(0)\ns_barrier\nv_add_f32 v36,v36,v44\nv_mfma_f32_16x16x32_bf16 v[212:215],a[36:39],v[84:87],v[212:215]\n" \
"ds_read_b64_tr_b16 a[98:99],v64 offset:25088\nds_read_b64_tr_b16 a[100:101],v65 offset:24576\nv_add_f32 v37,v37,v45\nv_mfma_f32_16x16x32_bf16 v[216:219],a[8:11],v[80:83],v[216:219]\n" \
"global_load_dwordx4 v[96:99],v59,s[16:17] sc1\nds_read_b64_tr_b16 a[102:103],v65 offset:25088\nv_add_f32 v38,v38,v46\nv_mfma_f32_16x16x32_bf16 v[216:219],a[40:43],v[84:87],v[216:219]\n" \
"s_add_u32 m0,s57,8192\nv_add_f32 v39,v39,v47\nv_mfma_f32_16x16x32_bf16 v[220:223],a[12:15],v[80:83],v[220:223]\nds_read_b64_tr_b16 a[104:105],v66 offset:24576\n" \
"ds_read_b64_tr_b16 a[106:107],v66 offset:25088\nv_mul_f32 v40,v36,v36\nv_mfma_f32_16x16x32_bf16 v[220:223],a[44:47],v[84:87],v[220:223]\nbuffer_load_dwordx4 v68,s[40:43],s62 offen lds\n" \
"v_mul_f32 v41,v37,v37\nv_mfma_f32_16x16x32_bf16 v[224:227],a[16:19],v[80:83],v[224:227]\nds_read_b64_tr_b16 a[108:109],v67 offset:24576\nv_mul_f32 v42,v38,v38\n" \
"v_mfma_f32_16x16x32_bf16 v[224:227],a[48:51],v[84:87],v[224:227]\nds_read_b64_tr_b16 a[110:111],v67 offset:25088\nv_mul_f32 v43,v39,v39\nv_fma_f32 v40,v40,s55,v57\n" \
"v_mfma_f32_16x16x32_bf16 v[228:231],a[20:23],v[80:83],v[228:231]\nbuffer_load_dwordx4 v69,s[40:43],s62 offen offset:1024 lds\nv_fma_f32 v41,v41,s55,v57\nv_fma_f32 v42,v42,s55,v57\n" \
"v_mfma_f32_16x16x32_bf16 v[228:231],a[52:55],v[84:87],v[228:231]\nv_fma_f32 v43,v43,s55,v57\nv_mul_f32 v40,v36,v40\nv_mul_f32 v41,v37,v41\n" \
"v_mfma_f32_16x16x32_bf16 v[232:235],a[24:27],v[80:83],v[232:235]\nv_mul_f32 v42,v38,v42\nv_mul_f32 v43,v39,v43\nv_mfma_f32_16x16x32_bf16 v[232:235],a[56:59],v[84:87],v[232:235]\n" \
"buffer_load_dwordx4 v70,s[40:43],s62 offen offset:2048 lds\nv_exp_f32 v40,v40\nv_mfma_f32_16x16x32_bf16 v[236:239],a[28:31],v[80:83],v[236:239]\nv_exp_f32 v41,v41\n" \
"v_exp_f32 v42,v42\nv_exp_f32 v43,v43\nv_add_f32 v40,1.0,v40\nv_add_f32 v41,1.0,v41\n" \
"v_add_f32 v42,1.0,v42\nv_add_f32 v43,1.0,v43\nv_rcp_f32 v40,v40\nv_rcp_f32 v41,v41\n" \
"v_rcp_f32 v42,v42\nv_rcp_f32 v43,v43\nv_mul_f32 v36,v36,v40\nv_mul_f32 v37,v37,v41\n" \
"v_mul_f32 v38,v38,v42\nv_mul_f32 v39,v39,v43\nv_cvt_pk_bf16_f32 v40,v36,v37\nv_cvt_pk_bf16_f32 v41,v38,v39\n" \
"buffer_store_dwordx2 v[40:41],v61,s[36:39],s52 offen sc1\nv_mfma_f32_16x16x32_bf16 v[236:239],a[60:63],v[84:87],v[236:239]\ns_waitcnt vmcnt(17)\nv_mfma_f32_16x16x32_bf16 v[208:211],a[64:67],v[88:91],v[208:211]\n" \
"s_waitcnt lgkmcnt(0)\ns_waitcnt vmcnt(16)\nv_mfma_f32_16x16x32_bf16 v[208:211],a[96:99],v[92:95],v[208:211]\nbuffer_load_dwordx4 v71,s[40:43],s62 offen offset:3072 lds\n" \
"v_mfma_f32_16x16x32_bf16 v[212:215],a[68:71],v[88:91],v[212:215]\ns_add_u32 m0,s57,12288\nv_mfma_f32_16x16x32_bf16 v[212:215],a[100:103],v[92:95],v[212:215]\nds_read_b64_tr_b16 a[112:113],v64\n" \
"ds_read_b64_tr_b16 a[114:115],v64 offset:512\nv_mfma_f32_16x16x32_bf16 v[216:219],a[72:75],v[88:91],v[216:219]\nbuffer_load_dwordx4 v68,s[40:43],s63 offen lds\nds_read_b64_tr_b16 a[116:117],v65\n" \
"v_mfma_f32_16x16x32_bf16 v[216:219],a[104:107],v[92:95],v[216:219]\nds_read_b64_tr_b16 a[118:119],v65 offset:512\nds_read_b64_tr_b16 a[120:121],v66\nv_mfma_f32_16x16x32_bf16 v[220:223],a[76:79],v[88:91],v[220:223]\n" \
"ds_read_b64_tr_b16 a[122:123],v66 offset:512\nds_read_b64_tr_b16 a[124:125],v67\nv_mfma_f32_16x16x32_bf16 v[220:223],a[108:111],v[92:95],v[220:223]\nbuffer_load_dwordx4 v69,s[40:43],s63 offen offset:1024 lds\n" \
"v_mfma_f32_16x16x32_bf16 v[224:227],a[80:83],v[88:91],v[224:227]\nds_read_b64_tr_b16 a[126:127],v67 offset:512\nds_read_b32 v48,v60\nv_mfma_f32_16x16x32_bf16 v[228:231],a[84:87],v[88:91],v[228:231]\n" \
"ds_read_b32 v49,v60 offset:9568\nds_read_b32 v50,v60 offset:19136\nv_mfma_f32_16x16x32_bf16 v[232:235],a[88:91],v[88:91],v[232:235]\nbuffer_load_dwordx4 v70,s[40:43],s63 offen offset:2048 lds\n" \
"ds_read_b32 v51,v60 offset:28704\nv_mfma_f32_16x16x32_bf16 v[236:239],a[92:95],v[88:91],v[236:239]\ns_waitcnt vmcnt(14)\nv_mfma_f32_16x16x32_bf16 v[0:3],v[128:131],v[108:111],0\n" \
"s_waitcnt vmcnt(13)\nv_mfma_f32_16x16x32_bf16 v[0:3],v[160:163],v[112:115],v[0:3]\nbuffer_load_dwordx4 v71,s[40:43],s63 offen offset:3072 lds\nv_mfma_f32_16x16x32_bf16 v[4:7],v[132:135],v[108:111],0\n" \
"v_mfma_f32_16x16x32_bf16 v[4:7],v[164:167],v[112:115],v[4:7]\nv_mfma_f32_16x16x32_bf16 v[8:11],v[136:139],v[108:111],0\nglobal_load_dwordx4 v[100:103],v59,s[16:17] offset:1024 sc1\nv_mfma_f32_16x16x32_bf16 v[8:11],v[168:171],v[112:115],v[8:11]\n" \
"s_add_u32 m0,s57,16384\nv_mfma_f32_16x16x32_bf16 v[12:15],v[140:143],v[108:111],0\nds_read_b64_tr_b16 a[128:129],v64 offset:4096\nds_read_b64_tr_b16 a[130:131],v64 offset:4608\n" \
"v_mfma_f32_16x16x32_bf16 v[12:15],v[172:175],v[112:115],v[12:15]\nbuffer_load_dwordx4 v68,s[40:43],s64 offen lds\nds_read_b64_tr_b16 a[132:133],v65 offset:4096\nv_mfma_f32_16x16x32_bf16 v[16:19],v[144:147],v[108:111],0\n" \
"ds_read_b64_tr_b16 a[134:135],v65 offset:4608\nds_read_b64_tr_b16 a[136:137],v66 offset:4096\nv_mfma_f32_16x16x32_bf16 v[16:19],v[176:179],v[112:115],v[16:19]\nds_read_b64_tr_b16 a[138:139],v66 offset:4608\n" \
"ds_read_b64_tr_b16 a[140:141],v67 offset:4096\nv_mfma_f32_16x16x32_bf16 v[20:23],v[148:151],v[108:111],0\nbuffer_load_dwordx4 v69,s[40:43],s64 offen offset:1024 lds\nds_read_b64_tr_b16 a[142:143],v67 offset:4608\n" \
"v_mfma_f32_16x16x32_bf16 v[20:23],v[180:183],v[112:115],v[20:23]\ns_waitcnt lgkmcnt(8)\nv_add_f32 v48,v48,v49\nv_mfma_f32_16x16x32_bf16 v[24:27],v[152:155],v[108:111],0\n" \
"v_add_f32 v50,v50,v51\nv_add_f32 v48,v48,v56\nv_mfma_f32_16x16x32_bf16 v[24:27],v[184:187],v[112:115],v[24:27]\nbuffer_load_dwordx4 v70,s[40:43],s64 offen offset:2048 lds\n" \
"v_add_f32 v48,v48,v50\nv_mfma_f32_16x16x32_bf16 v[28:31],v[156:159],v[108:111],0\nv_mul_f32 v49,v48,v48\nv_fma_f32 v49,v49,s55,v57\n" \
"v_mfma_f32_16x16x32_bf16 v[28:31],v[188:191],v[112:115],v[28:31]\nv_mul_f32 v49,v48,v49\ns_waitcnt vmcnt(17)\nv_mfma_f32_16x16x32_bf16 v[0:3],a[0:3],v[116:119],v[0:3]\n" \
"buffer_load_dwordx4 v71,s[40:43],s64 offen offset:3072 lds\ns_waitcnt vmcnt(17)\nv_mfma_f32_16x16x32_bf16 v[0:3],a[32:35],v[120:123],v[0:3]\ns_add_u32 m0,s57,20480\n" \
"v_mfma_f32_16x16x32_bf16 v[4:7],a[4:7],v[116:119],v[4:7]\nv_exp_f32 v49,v49\nv_mfma_f32_16x16x32_bf16 v[4:7],a[36:39],v[120:123],v[4:7]\nbuffer_load_dwordx4 v68,s[40:43],s65 offen lds\n" \
"v_add_f32 v49,1.0,v49\nv_mfma_f32_16x16x32_bf16 v[8:11],a[8:11],v[116:119],v[8:11]\nv_rcp_f32 v49,v49\nv_mfma_f32_16x16x32_bf16 v[8:11],a[40:43],v[120:123],v[8:11]\n" \
"v_mul_f32 v48,v48,v49\nv_cvt_pk_bf16_f32 v49,v48,v48\nv_mfma_f32_16x16x32_bf16 v[12:15],a[12:15],v[116:119],v[12:15]\nbuffer_load_dwordx4 v69,s[40:43],s65 offen offset:1024 lds\n" \
"buffer_store_short v49,v62,s[36:39],s52 offen sc1\nv_mfma_f32_16x16x32_bf16 v[12:15],a[44:47],v[120:123],v[12:15]\nds_read_b128 v[36:39],v58 offset:5344\nds_read_b128 v[40:43],v58 offset:14912\n" \
"v_mfma_f32_16x16x32_bf16 v[16:19],a[16:19],v[116:119],v[16:19]\nds_read_b128 v[44:47],v58 offset:24480\nds_read_b128 v[48:51],v58 offset:34048\nv_mfma_f32_16x16x32_bf16 v[16:19],a[48:51],v[120:123],v[16:19]\n" \
"buffer_load_dwordx4 v70,s[40:43],s65 offen offset:2048 lds\ns_add_u32 s52,s52,409600\nv_mfma_f32_16x16x32_bf16 v[20:23],a[20:23],v[116:119],v[20:23]\nv_mfma_f32_16x16x32_bf16 v[20:23],a[52:55],v[120:123],v[20:23]\n" \
"v_mfma_f32_16x16x32_bf16 v[24:27],a[24:27],v[116:119],v[24:27]\nbuffer_load_dwordx4 v71,s[40:43],s65 offen offset:3072 lds\nv_mfma_f32_16x16x32_bf16 v[24:27],a[56:59],v[120:123],v[24:27]\nv_mfma_f32_16x16x32_bf16 v[28:31],a[28:31],v[116:119],v[28:31]\n" \
"v_mfma_f32_16x16x32_bf16 v[28:31],a[60:63],v[120:123],v[28:31]\nglobal_load_dwordx4 v[104:107],v59,s[16:17] offset:2048 sc1\ns_waitcnt vmcnt(22)\nv_mfma_f32_16x16x32_bf16 v[0:3],a[64:67],v[124:127],v[0:3]\n" \
"s_waitcnt lgkmcnt(2)\ns_waitcnt vmcnt(21)\nv_mfma_f32_16x16x32_bf16 v[0:3],a[96:99],v[192:195],v[0:3]\ns_add_u32 m0,s57,24576\n" \
"v_mfma_f32_16x16x32_bf16 v[4:7],a[68:71],v[124:127],v[4:7]\nbuffer_load_dwordx4 v68,s[40:43],s66 offen lds\nv_add_f32 v36,v36,v40\nv_mfma_f32_16x16x32_bf16 v[4:7],a[100:103],v[192:195],v[4:7]\n" \
"v_add_f32 v37,v37,v41\nv_add_f32 v38,v38,v42\nv_mfma_f32_16x16x32_bf16 v[8:11],a[72:75],v[124:127],v[8:11]\nv_add_f32 v39,v39,v43\n" \
"v_add_f32 v36,v36,v52\nv_mfma_f32_16x16x32_bf16 v[8:11],a[104:107],v[192:195],v[8:11]\ns_waitcnt vmcnt(16)\nds_read_b64_tr_b16 a[144:145],v64 offset:8192\n" \
"ds_read_b64_tr_b16 a[146:147],v64 offset:8704\nv_mfma_f32_16x16x32_bf16 v[12:15],a[76:79],v[124:127],v[12:15]\nds_read_b64_tr_b16 a[148:149],v65 offset:8192\nds_read_b64_tr_b16 a[150:151],v65 offset:8704\n" \
"v_add_f32 v37,v37,v53\nv_mfma_f32_16x16x32_bf16 v[12:15],a[108:111],v[192:195],v[12:15]\nds_read_b64_tr_b16 a[152:153],v66 offset:8192\nds_read_b64_tr_b16 a[154:155],v66 offset:8704\n" \
"v_add_f32 v38,v38,v54\nv_mfma_f32_16x16x32_bf16 v[16:19],a[80:83],v[124:127],v[16:19]\nds_read_b64_tr_b16 a[156:157],v67 offset:8192\nds_read_b64_tr_b16 a[158:159],v67 offset:8704\n" \
"v_add_f32 v39,v39,v55\nv_mfma_f32_16x16x32_bf16 v[20:23],a[84:87],v[124:127],v[20:23]\nbuffer_load_dwordx4 v69,s[40:43],s66 offen offset:1024 lds\ns_waitcnt lgkmcnt(8)\n" \
"v_add_f32 v44,v44,v48\nv_mfma_f32_16x16x32_bf16 v[24:27],a[88:91],v[124:127],v[24:27]\nv_add_f32 v45,v45,v49\nv_add_f32 v46,v46,v50\n" \
"v_add_f32 v47,v47,v51\nv_mfma_f32_16x16x32_bf16 v[28:31],a[92:95],v[124:127],v[28:31]\nv_add_f32 v36,v36,v44\nv_add_f32 v37,v37,v45\n" \
"v_mfma_f32_16x16x32_bf16 v[224:227],a[112:115],v[92:95],v[224:227]\nv_add_f32 v38,v38,v46\ns_waitcnt lgkmcnt(0)\nv_mfma_f32_16x16x32_bf16 v[224:227],a[144:147],v[96:99],v[224:227]\n" \
"s_waitcnt vmcnt(13)\nds_read_b64_tr_b16 a[160:161],v64 offset:12288\nds_read_b64_tr_b16 a[162:163],v64 offset:12800\nv_mfma_f32_16x16x32_bf16 v[228:231],a[116:119],v[92:95],v[228:231]\n" \
"ds_read_b64_tr_b16 a[164:165],v65 offset:12288\nds_read_b64_tr_b16 a[166:167],v65 offset:12800\nv_add_f32 v39,v39,v47\nv_mfma_f32_16x16x32_bf16 v[228:231],a[148:151],v[96:99],v[228:231]\n" \
"ds_read_b64_tr_b16 a[168:169],v66 offset:12288\nds_read_b64_tr_b16 a[170:171],v66 offset:12800\nv_mul_f32 v40,v36,v36\nv_mfma_f32_16x16x32_bf16 v[232:235],a[120:123],v[92:95],v[232:235]\n" \
"ds_read_b64_tr_b16 a[172:173],v67 offset:12288\nds_read_b64_tr_b16 a[174:175],v67 offset:12800\nv_mul_f32 v41,v37,v37\nv_mfma_f32_16x16x32_bf16 v[232:235],a[152:155],v[96:99],v[232:235]\n" \
"buffer_load_dwordx4 v70,s[40:43],s66 offen offset:2048 lds\nv_mul_f32 v42,v38,v38\nv_mul_f32 v43,v39,v39\nv_mfma_f32_16x16x32_bf16 v[236:239],a[124:127],v[92:95],v[236:239]\n" \
"v_fma_f32 v40,v40,s55,v57\nv_fma_f32 v41,v41,s55,v57\nv_fma_f32 v42,v42,s55,v57\nv_mfma_f32_16x16x32_bf16 v[236:239],a[156:159],v[96:99],v[236:239]\n" \
"v_fma_f32 v43,v43,s55,v57\nv_mul_f32 v40,v36,v40\nv_mfma_f32_16x16x32_bf16 v[208:211],a[128:131],v[96:99],v[208:211]\nbuffer_load_dwordx4 v71,s[40:43],s66 offen offset:3072 lds\n" \
"s_waitcnt vmcnt(10)\nds_read_b64_tr_b16 a[176:177],v64 offset:16384\nds_read_b64_tr_b16 a[178:179],v64 offset:16896\ns_waitcnt lgkmcnt(2)\n" \
"v_mfma_f32_16x16x32_bf16 v[208:211],a[160:163],v[100:103],v[208:211]\nds_read_b64_tr_b16 a[180:181],v65 offset:16384\nds_read_b64_tr_b16 a[182:183],v65 offset:16896\nds_read_b64_tr_b16 a[184:185],v66 offset:16384\n" \
"ds_read_b64_tr_b16 a[186:187],v66 offset:16896\nds_read_b64_tr_b16 a[188:189],v67 offset:16384\nds_read_b64_tr_b16 a[190:191],v67 offset:16896\ns_waitcnt vmcnt(5)\n" \
"ds_read_b64_tr_b16 a[192:193],v64 offset:20480\nds_read_b64_tr_b16 a[194:195],v64 offset:20992\nds_read_b64_tr_b16 a[196:197],v65 offset:20480\nds_read_b64_tr_b16 a[198:199],v65 offset:20992\n" \
"ds_read_b64_tr_b16 a[200:201],v66 offset:20480\nds_read_b64_tr_b16 a[202:203],v66 offset:20992\nds_read_b64_tr_b16 a[204:205],v67 offset:20480\nds_read_b64_tr_b16 a[206:207],v67 offset:20992\n" \
"s_waitcnt vmcnt(0)\nds_read_b64_tr_b16 a[208:209],v64 offset:24576\nds_read_b64_tr_b16 a[210:211],v64 offset:25088\nv_mfma_f32_16x16x32_bf16 v[212:215],a[132:135],v[96:99],v[212:215]\n" \
"ds_read_b64_tr_b16 a[212:213],v65 offset:24576\nds_read_b64_tr_b16 a[214:215],v65 offset:25088\nv_mul_f32 v41,v37,v41\nv_mfma_f32_16x16x32_bf16 v[212:215],a[164:167],v[100:103],v[212:215]\n" \
"ds_read_b64_tr_b16 a[216:217],v66 offset:24576\nds_read_b64_tr_b16 a[218:219],v66 offset:25088\nv_mul_f32 v42,v38,v42\nv_mfma_f32_16x16x32_bf16 v[216:219],a[136:139],v[96:99],v[216:219]\n" \
"ds_read_b64_tr_b16 a[220:221],v67 offset:24576\nds_read_b64_tr_b16 a[222:223],v67 offset:25088\nv_mul_f32 v43,v39,v43\nv_mfma_f32_16x16x32_bf16 v[216:219],a[168:171],v[100:103],v[216:219]\n" \
"global_load_dwordx4 v[196:199],v59,s[8:9] sc1\nv_exp_f32 v40,v40\nv_mfma_f32_16x16x32_bf16 v[220:223],a[140:143],v[96:99],v[220:223]\nv_exp_f32 v41,v41\n" \
"v_mfma_f32_16x16x32_bf16 v[220:223],a[172:175],v[100:103],v[220:223]\nv_exp_f32 v42,v42\ns_waitcnt lgkmcnt(15)\nv_mfma_f32_16x16x32_bf16 v[224:227],a[176:179],v[100:103],v[224:227]\n" \
"s_waitcnt lgkmcnt(0)\nv_mfma_f32_16x16x32_bf16 v[224:227],a[208:211],v[104:107],v[224:227]\nglobal_load_dwordx4 v[200:203],v59,s[8:9] offset:1024 sc1\nv_exp_f32 v43,v43\n" \
"v_mfma_f32_16x16x32_bf16 v[228:231],a[180:183],v[100:103],v[228:231]\nv_add_f32 v40,1.0,v40\nv_add_f32 v41,1.0,v41\nv_add_f32 v42,1.0,v42\n" \
"v_mfma_f32_16x16x32_bf16 v[228:231],a[212:215],v[104:107],v[228:231]\nv_add_f32 v43,1.0,v43\nv_rcp_f32 v40,v40\nv_mfma_f32_16x16x32_bf16 v[232:235],a[184:187],v[100:103],v[232:235]\n" \
"global_load_dwordx4 v[204:207],v59,s[8:9] offset:2048 sc1\nv_rcp_f32 v41,v41\nv_mfma_f32_16x16x32_bf16 v[232:235],a[216:219],v[104:107],v[232:235]\ns_add_u32 s59,s59,0x10000\n" \
"buffer_load_dword v63,v61,s[36:39],s59 offen sc1\nv_mfma_f32_16x16x32_bf16 v[236:239],a[188:191],v[100:103],v[236:239]\nv_rcp_f32 v42,v42\nv_rcp_f32 v43,v43\n" \
"v_mul_f32 v36,v36,v40\nv_mul_f32 v37,v37,v41\nv_mul_f32 v38,v38,v42\nv_mul_f32 v39,v39,v43\n" \
"v_cvt_pk_bf16_f32 v40,v36,v37\nv_cvt_pk_bf16_f32 v41,v38,v39\nv_mfma_f32_16x16x32_bf16 v[236:239],a[220:223],v[104:107],v[236:239]\nv_add_u32 v59,819200,v59\n" \
"v_mfma_f32_16x16x32_bf16 v[208:211],a[192:195],v[104:107],v[208:211]\nv_and_b32 v58,0x3ff,v59\nv_add_u32 v58,s99,v58\nv_mfma_f32_16x16x32_bf16 v[212:215],a[196:199],v[104:107],v[212:215]\n" \
"v_add_u32 v46,s98,v58\nds_read_b64 v[56:57],v46 offset:28680\nv_mfma_f32_16x16x32_bf16 v[216:219],a[200:203],v[104:107],v[216:219]\nbuffer_store_dwordx2 v[40:41],v61,s[36:39],s52 offen sc1\n" \
"global_load_dwordx4 v[72:75],v59,s[12:13] sc1\nv_mfma_f32_16x16x32_bf16 v[220:223],a[204:207],v[104:107],v[220:223]\nv_mfma_f32_16x16x32_bf16 v[16:19],a[112:115],v[192:195],v[16:19]\ns_waitcnt vmcnt(5)\n" \
"v_mfma_f32_16x16x32_bf16 v[16:19],a[144:147],v[196:199],v[16:19]\nglobal_load_dwordx4 v[76:79],v59,s[12:13] offset:1024 sc1\nv_mfma_f32_16x16x32_bf16 v[20:23],a[116:119],v[192:195],v[20:23]\ns_waitcnt lgkmcnt(0)\n" \
"s_barrier\ns_mov_b32 m0,s58\nv_mfma_f32_16x16x32_bf16 v[20:23],a[148:151],v[196:199],v[20:23]\nds_write_addtid_b32 v208\n" \
"ds_write_addtid_b32 v209 offset:256\nv_mfma_f32_16x16x32_bf16 v[24:27],a[120:123],v[192:195],v[24:27]\nglobal_load_dwordx4 v[80:83],v59,s[12:13] offset:2048 sc1\nds_write_addtid_b32 v210 offset:512\n" \
"v_mfma_f32_16x16x32_bf16 v[24:27],a[152:155],v[196:199],v[24:27]\nds_write_addtid_b32 v211 offset:768\nds_write_addtid_b32 v212 offset:1024\nv_mfma_f32_16x16x32_bf16 v[28:31],a[124:127],v[192:195],v[28:31]\n" \
"ds_write_addtid_b32 v213 offset:1280\nds_write_addtid_b32 v214 offset:1536\nv_mfma_f32_16x16x32_bf16 v[28:31],a[156:159],v[196:199],v[28:31]\nglobal_load_dwordx4 v[84:87],v59,s[14:15] sc1\n" \
"ds_write_addtid_b32 v215 offset:1792\nv_mfma_f32_16x16x32_bf16 v[0:3],a[128:131],v[196:199],v[0:3]\nds_write_addtid_b32 v216 offset:2048\ns_waitcnt vmcnt(7)\n" \
"v_mfma_f32_16x16x32_bf16 v[0:3],a[160:163],v[200:203],v[0:3]\nds_write_addtid_b32 v217 offset:2304\nds_write_addtid_b32 v218 offset:2560\nv_mfma_f32_16x16x32_bf16 v[4:7],a[132:135],v[196:199],v[4:7]\n" \
"global_load_dwordx4 v[88:91],v59,s[14:15] offset:1024 sc1\nds_write_addtid_b32 v219 offset:2816\nv_mfma_f32_16x16x32_bf16 v[4:7],a[164:167],v[200:203],v[4:7]\nds_write_addtid_b32 v220 offset:3072\n" \
"ds_write_addtid_b32 v221 offset:3328\nv_mfma_f32_16x16x32_bf16 v[8:11],a[136:139],v[196:199],v[8:11]\nds_write_addtid_b32 v222 offset:3584\nds_write_addtid_b32 v223 offset:3840\n" \
"v_mfma_f32_16x16x32_bf16 v[8:11],a[168:171],v[200:203],v[8:11]\nglobal_load_dwordx4 v[92:95],v59,s[14:15] offset:2048 sc1\nds_write_addtid_b32 v224 offset:4096\nv_mfma_f32_16x16x32_bf16 v[12:15],a[140:143],v[196:199],v[12:15]\n" \
"ds_write_addtid_b32 v225 offset:4352\nds_write_addtid_b32 v226 offset:4608\nv_mfma_f32_16x16x32_bf16 v[12:15],a[172:175],v[200:203],v[12:15]\nds_write_addtid_b32 v227 offset:4864\n" \
"ds_write_addtid_b32 v228 offset:5120\nv_mfma_f32_16x16x32_bf16 v[16:19],a[176:179],v[200:203],v[16:19]\nglobal_load_dwordx4 v[96:99],v59,s[16:17] sc1\ns_waitcnt vmcnt(9)\n" \
"v_mfma_f32_16x16x32_bf16 v[16:19],a[208:211],v[204:207],v[16:19]\nds_write_addtid_b32 v229 offset:5376\nds_write_addtid_b32 v230 offset:5632\nv_mfma_f32_16x16x32_bf16 v[20:23],a[180:183],v[200:203],v[20:23]\n" \
"ds_write_addtid_b32 v231 offset:5888\nds_write_addtid_b32 v232 offset:6144\nv_mfma_f32_16x16x32_bf16 v[20:23],a[212:215],v[204:207],v[20:23]\nglobal_load_dwordx4 v[100:103],v59,s[16:17] offset:1024 sc1\n" \
"ds_write_addtid_b32 v233 offset:6400\nv_mfma_f32_16x16x32_bf16 v[24:27],a[184:187],v[200:203],v[24:27]\nds_write_addtid_b32 v234 offset:6656\nds_write_addtid_b32 v235 offset:6912\n" \
"v_mfma_f32_16x16x32_bf16 v[24:27],a[216:219],v[204:207],v[24:27]\nds_write_addtid_b32 v236 offset:7168\nds_write_addtid_b32 v237 offset:7424\nv_mfma_f32_16x16x32_bf16 v[28:31],a[188:191],v[200:203],v[28:31]\n" \
"global_load_dwordx4 v[104:107],v59,s[16:17] offset:2048 sc1\nds_write_addtid_b32 v238 offset:7680\nv_mfma_f32_16x16x32_bf16 v[28:31],a[220:223],v[204:207],v[28:31]\nds_write_addtid_b32 v239 offset:7936\n" \
"v_mfma_f32_16x16x32_bf16 v[0:3],a[192:195],v[204:207],v[0:3]\nv_mfma_f32_16x16x32_bf16 v[4:7],a[196:199],v[204:207],v[4:7]\nv_mfma_f32_16x16x32_bf16 v[8:11],a[200:203],v[204:207],v[8:11]\nv_mfma_f32_16x16x32_bf16 v[12:15],a[204:207],v[204:207],v[12:15]\n" \
"s_waitcnt vmcnt(7)\nv_mfma_f32_16x16x32_bf16 v[208:211],v[128:131],v[72:75],0\ns_mov_b64 exec,1\nbuffer_store_dword v61,off,s[36:39],s54 sc1\n" \
"s_mov_b64 exec,-1\nv_mfma_f32_16x16x32_bf16 v[208:211],v[160:163],v[76:79],v[208:211]\nv_cmp_eq_u32 vcc,0,v63\nv_mfma_f32_16x16x32_bf16 v[212:215],v[132:135],v[72:75],0\n" \
"s_waitcnt lgkmcnt(0)\ns_barrier\nv_mfma_f32_16x16x32_bf16 v[212:215],v[164:167],v[76:79],v[212:215]\ns_and_b64 vcc,vcc,s[86:87]\n" \
"s_cbranch_scc1 .LsbS_%=\n.LbbS_%=:\nv_mfma_f32_16x16x32_bf16 v[216:219],v[136:139],v[72:75],0\nglobal_load_dwordx4 v[108:111],v59,s[4:5] sc1\n" \
"ds_write_addtid_b32 v0 offset:32768\nv_mfma_f32_16x16x32_bf16 v[216:219],v[168:171],v[76:79],v[216:219]\nds_write_addtid_b32 v1 offset:33024\nds_write_addtid_b32 v2 offset:33280\n" \
"v_mfma_f32_16x16x32_bf16 v[220:223],v[140:143],v[72:75],0\nds_write_addtid_b32 v3 offset:33536\nds_write_addtid_b32 v4 offset:33792\nv_mfma_f32_16x16x32_bf16 v[220:223],v[172:175],v[76:79],v[220:223]\n" \
"global_load_dwordx4 v[112:115],v59,s[4:5] offset:1024 sc1\nds_write_addtid_b32 v5 offset:34048\nv_mfma_f32_16x16x32_bf16 v[224:227],v[144:147],v[72:75],0\nds_write_addtid_b32 v6 offset:34304\n" \
"ds_write_addtid_b32 v7 offset:34560\nv_mfma_f32_16x16x32_bf16 v[224:227],v[176:179],v[76:79],v[224:227]\nds_write_addtid_b32 v8 offset:34816\nds_write_addtid_b32 v9 offset:35072\n" \
"v_mfma_f32_16x16x32_bf16 v[228:231],v[148:151],v[72:75],0\nglobal_load_dwordx4 v[116:119],v59,s[4:5] offset:2048 sc1\nds_write_addtid_b32 v10 offset:35328\nv_mfma_f32_16x16x32_bf16 v[228:231],v[180:183],v[76:79],v[228:231]\n" \
"ds_write_addtid_b32 v11 offset:35584\nds_write_addtid_b32 v12 offset:35840\nv_mfma_f32_16x16x32_bf16 v[232:235],v[152:155],v[72:75],0\nds_write_addtid_b32 v13 offset:36096\n" \
"ds_write_addtid_b32 v14 offset:36352\nv_mfma_f32_16x16x32_bf16 v[232:235],v[184:187],v[76:79],v[232:235]\nglobal_load_dwordx4 v[120:123],v59,s[6:7] sc1\nds_write_addtid_b32 v15 offset:36608\n" \
"v_mfma_f32_16x16x32_bf16 v[236:239],v[156:159],v[72:75],0\nds_write_addtid_b32 v16 offset:36864\nds_write_addtid_b32 v17 offset:37120\nv_mfma_f32_16x16x32_bf16 v[236:239],v[188:191],v[76:79],v[236:239]\n" \
"ds_write_addtid_b32 v18 offset:37376\ns_waitcnt vmcnt(10)\nv_mfma_f32_16x16x32_bf16 v[208:211],a[0:3],v[80:83],v[208:211]\nglobal_load_dwordx4 v[124:127],v59,s[6:7] offset:1024 sc1\n" \
"ds_write_addtid_b32 v19 offset:37632\nv_mfma_f32_16x16x32_bf16 v[208:211],a[32:35],v[84:87],v[208:211]\nds_write_addtid_b32 v20 offset:37888\nds_write_addtid_b32 v21 offset:38144\n" \
"v_mfma_f32_16x16x32_bf16 v[212:215],a[4:7],v[80:83],v[212:215]\nds_write_addtid_b32 v22 offset:38400\nds_write_addtid_b32 v23 offset:38656\nv_mfma_f32_16x16x32_bf16 v[212:215],a[36:39],v[84:87],v[212:215]\n" \
"global_load_dwordx4 v[192:195],v59,s[6:7] offset:2048 sc1\nds_write_addtid_b32 v24 offset:38912\nv_mfma_f32_16x16x32_bf16 v[216:219],a[8:11],v[80:83],v[216:219]\nds_write_addtid_b32 v25 offset:39168\n" \
"ds_write_addtid_b32 v26 offset:39424\nv_mfma_f32_16x16x32_bf16 v[216:219],a[40:43],v[84:87],v[216:219]\nds_write_addtid_b32 v27 offset:39680\nds_write_addtid_b32 v28 offset:39936\n" \
"v_mfma_f32_16x16x32_bf16 v[220:223],a[12:15],v[80:83],v[220:223]\nglobal_load_dwordx4 v[196:199],v59,s[8:9] sc1\nds_write_addtid_b32 v29 offset:40192\nv_mfma_f32_16x16x32_bf16 v[220:223],a[44:47],v[84:87],v[220:223]\n" \
"ds_write_addtid_b32 v30 offset:40448\nds_write_addtid_b32 v31 offset:40704\nv_mfma_f32_16x16x32_bf16 v[224:227],a[16:19],v[80:83],v[224:227]\nds_read_b128 v[32:35],v58\n" \
"ds_read_b128 v[36:39],v58 offset:8192\nv_mfma_f32_16x16x32_bf16 v[224:227],a[48:51],v[84:87],v[224:227]\nglobal_load_dwordx4 v[200:203],v59,s[8:9] offset:1024 sc1\nds_read_b128 v[40:43],v58 offset:16384\n" \
"v_mfma_f32_16x16x32_bf16 v[228:231],a[20:23],v[80:83],v[228:231]\nds_read_b128 v[44:47],v58 offset:24576\nds_read_b128 v[48:51],v58 offset:4096\nv_mfma_f32_16x16x32_bf16 v[228:231],a[52:55],v[84:87],v[228:231]\n" \
"ds_read_b128 v[240:243],v58 offset:12288\nds_read_b128 v[244:247],v58 offset:20480\nv_mfma_f32_16x16x32_bf16 v[232:235],a[24:27],v[80:83],v[232:235]\nglobal_load_dwordx4 v[204:207],v59,s[8:9] offset:2048 sc1\n" \
"ds_read_b128 v[248:251],v58 offset:28672\nv_mfma_f32_16x16x32_bf16 v[232:235],a[56:59],v[84:87],v[232:235]\ns_waitcnt lgkmcnt(0)\ns_barrier\n" \
"v_mfma_f32_16x16x32_bf16 v[236:239],a[28:31],v[80:83],v[236:239]\nv_mfma_f32_16x16x32_bf16 v[236:239],a[60:63],v[84:87],v[236:239]\ns_waitcnt vmcnt(13)\nv_mfma_f32_16x16x32_bf16 v[208:211],a[64:67],v[88:91],v[208:211]\n" \
"v_mfma_f32_16x16x32_bf16 v[208:211],a[96:99],v[92:95],v[208:211]\nv_mfma_f32_16x16x32_bf16 v[212:215],a[68:71],v[88:91],v[212:215]\nv_add_f32 v32,v32,v36\nv_mfma_f32_16x16x32_bf16 v[212:215],a[100:103],v[92:95],v[212:215]\n" \
"v_add_f32 v33,v33,v37\nv_add_f32 v34,v34,v38\nv_mfma_f32_16x16x32_bf16 v[216:219],a[72:75],v[88:91],v[216:219]\nv_add_f32 v35,v35,v39\n" \
"v_add_f32 v40,v40,v44\nv_mfma_f32_16x16x32_bf16 v[216:219],a[104:107],v[92:95],v[216:219]\nv_add_f32 v41,v41,v45\nv_add_f32 v42,v42,v46\n" \
"v_mfma_f32_16x16x32_bf16 v[220:223],a[76:79],v[88:91],v[220:223]\nv_add_f32 v43,v43,v47\nv_add_f32 v32,v32,v40\nv_mfma_f32_16x16x32_bf16 v[220:223],a[108:111],v[92:95],v[220:223]\n" \
"v_add_f32 v33,v33,v41\nv_add_f32 v34,v34,v42\nv_mfma_f32_16x16x32_bf16 v[224:227],a[80:83],v[88:91],v[224:227]\nv_add_f32 v35,v35,v43\n" \
"v_cvt_pk_bf16_f32 v36,v32,v33\nv_mfma_f32_16x16x32_bf16 v[224:227],a[112:115],v[92:95],v[224:227]\nv_cvt_pk_bf16_f32 v37,v34,v35\nv_mfma_f32_16x16x32_bf16 v[228:231],a[84:87],v[88:91],v[228:231]\n" \
"v_add_f32 v48,v48,v240\nv_add_f32 v49,v49,v241\nv_mfma_f32_16x16x32_bf16 v[228:231],a[116:119],v[92:95],v[228:231]\nv_add_f32 v50,v50,v242\n" \
"v_add_f32 v51,v51,v243\nv_mfma_f32_16x16x32_bf16 v[232:235],a[88:91],v[88:91],v[232:235]\nv_add_f32 v244,v244,v248\nv_add_f32 v245,v245,v249\n" \
"v_mfma_f32_16x16x32_bf16 v[232:235],a[120:123],v[92:95],v[232:235]\nv_add_f32 v246,v246,v250\nv_add_f32 v247,v247,v251\nv_mfma_f32_16x16x32_bf16 v[236:239],a[92:95],v[88:91],v[236:239]\n" \
"v_add_f32 v48,v48,v244\nv_add_f32 v49,v49,v245\nv_mfma_f32_16x16x32_bf16 v[236:239],a[124:127],v[92:95],v[236:239]\nv_add_f32 v50,v50,v246\n" \
"s_waitcnt vmcnt(11)\nv_mfma_f32_16x16x32_bf16 v[208:211],a[128:131],v[96:99],v[208:211]\nv_add_f32 v51,v51,v247\nv_cvt_pk_bf16_f32 v38,v48,v49\n" \
"v_mfma_f32_16x16x32_bf16 v[208:211],a[160:163],v[100:103],v[208:211]\nv_cvt_pk_bf16_f32 v39,v50,v51\nbuffer_store_dwordx4 v[36:39],v59,s[36:39],s74 offen sc1\nds_read_b128 v[32:35],v58 offset:32768\n" \
"v_mfma_f32_16x16x32_bf16 v[212:215],a[132:135],v[96:99],v[212:215]\nds_read_b128 v[36:39],v58 offset:40960\nds_read_b128 v[40:43],v58 offset:49152\nv_mfma_f32_16x16x32_bf16 v[212:215],a[164:167],v[100:103],v[212:215]\n" \
"ds_read_b128 v[44:47],v58 offset:57344\nds_read_b128 v[48:51],v58 offset:36864\nv_mfma_f32_16x16x32_bf16 v[216:219],a[136:139],v[96:99],v[216:219]\nds_read_b128 v[240:243],v58 offset:45056\n" \
"ds_read_b128 v[244:247],v58 offset:53248\nv_mfma_f32_16x16x32_bf16 v[216:219],a[168:171],v[100:103],v[216:219]\nds_read_b128 v[248:251],v58 offset:61440\nv_mfma_f32_16x16x32_bf16 v[220:223],a[140:143],v[96:99],v[220:223]\n" \
"v_mfma_f32_16x16x32_bf16 v[220:223],a[172:175],v[100:103],v[220:223]\nv_mfma_f32_16x16x32_bf16 v[224:227],a[144:147],v[96:99],v[224:227]\nv_mfma_f32_16x16x32_bf16 v[224:227],a[176:179],v[100:103],v[224:227]\nv_mfma_f32_16x16x32_bf16 v[228:231],a[148:151],v[96:99],v[228:231]\n" \
"v_mfma_f32_16x16x32_bf16 v[228:231],a[180:183],v[100:103],v[228:231]\ns_waitcnt lgkmcnt(4)\nv_add_f32 v32,v32,v36\nv_add_f32 v33,v33,v37\n" \
"v_mfma_f32_16x16x32_bf16 v[232:235],a[152:155],v[96:99],v[232:235]\nv_add_f32 v34,v34,v38\nv_add_f32 v35,v35,v39\nv_add_f32 v40,v40,v44\n" \
"v_mfma_f32_16x16x32_bf16 v[232:235],a[184:187],v[100:103],v[232:235]\nv_add_f32 v41,v41,v45\nv_add_f32 v42,v42,v46\nv_add_f32 v43,v43,v47\n" \
"v_mfma_f32_16x16x32_bf16 v[236:239],a[156:159],v[96:99],v[236:239]\nv_add_f32 v32,v32,v40\nv_add_f32 v33,v33,v41\nv_add_f32 v34,v34,v42\n" \
"v_mfma_f32_16x16x32_bf16 v[236:239],a[188:191],v[100:103],v[236:239]\nv_add_f32 v35,v35,v43\nv_cvt_pk_bf16_f32 v36,v32,v33\ns_waitcnt vmcnt(11)\n" \
"v_mfma_f32_16x16x32_bf16 v[208:211],a[192:195],v[104:107],v[208:211]\nv_cvt_pk_bf16_f32 v37,v34,v35\ns_waitcnt lgkmcnt(0)\nv_add_f32 v48,v48,v240\n" \
"v_mfma_f32_16x16x32_bf16 v[212:215],a[196:199],v[104:107],v[212:215]\nv_add_f32 v49,v49,v241\nv_add_f32 v50,v50,v242\nv_add_f32 v51,v51,v243\n" \
"v_mfma_f32_16x16x32_bf16 v[216:219],a[200:203],v[104:107],v[216:219]\nv_add_f32 v244,v244,v248\nv_add_f32 v245,v245,v249\nv_mfma_f32_16x16x32_bf16 v[220:223],a[204:207],v[104:107],v[220:223]\n" \
"v_add_f32 v246,v246,v250\nv_add_f32 v247,v247,v251\nv_mfma_f32_16x16x32_bf16 v[224:227],a[208:211],v[104:107],v[224:227]\nv_add_f32 v48,v48,v244\n" \
"v_add_f32 v49,v49,v245\nv_mfma_f32_16x16x32_bf16 v[228:231],a[212:215],v[104:107],v[228:231]\nv_add_f32 v50,v50,v246\nv_add_f32 v51,v51,v247\n" \
"v_mfma_f32_16x16x32_bf16 v[232:235],a[216:219],v[104:107],v[232:235]\nv_cvt_pk_bf16_f32 v38,v48,v49\nv_cvt_pk_bf16_f32 v39,v50,v51\nv_mfma_f32_16x16x32_bf16 v[236:239],a[220:223],v[104:107],v[236:239]\n" \
"v_add_u32 v59,409600,v59\nbuffer_store_dwordx4 v[36:39],v59,s[36:39],s74 offen sc1\n.Le_%=:\ns_waitcnt vmcnt(9)\n" \
"v_mfma_f32_16x16x32_bf16 v[0:3],v[128:131],v[108:111],0\nv_add_u32 v59,409600,v59\nv_mfma_f32_16x16x32_bf16 v[0:3],v[160:163],v[112:115],v[0:3]\nds_write_addtid_b32 v208\n" \
"v_mfma_f32_16x16x32_bf16 v[4:7],v[132:135],v[108:111],0\nglobal_load_dwordx4 v[72:75],v59,s[12:13] sc1\nds_write_addtid_b32 v209 offset:256\nv_mfma_f32_16x16x32_bf16 v[4:7],v[164:167],v[112:115],v[4:7]\n" \
"ds_write_addtid_b32 v210 offset:512\nv_mfma_f32_16x16x32_bf16 v[8:11],v[136:139],v[108:111],0\nds_write_addtid_b32 v211 offset:768\nv_mfma_f32_16x16x32_bf16 v[8:11],v[168:171],v[112:115],v[8:11]\n" \
"ds_write_addtid_b32 v212 offset:1024\nv_mfma_f32_16x16x32_bf16 v[12:15],v[140:143],v[108:111],0\nds_write_addtid_b32 v213 offset:1280\nv_mfma_f32_16x16x32_bf16 v[12:15],v[172:175],v[112:115],v[12:15]\n" \
"ds_write_addtid_b32 v214 offset:1536\nv_mfma_f32_16x16x32_bf16 v[16:19],v[144:147],v[108:111],0\nglobal_load_dwordx4 v[76:79],v59,s[12:13] offset:1024 sc1\nds_write_addtid_b32 v215 offset:1792\n" \
"v_mfma_f32_16x16x32_bf16 v[16:19],v[176:179],v[112:115],v[16:19]\nds_write_addtid_b32 v216 offset:2048\nv_mfma_f32_16x16x32_bf16 v[20:23],v[148:151],v[108:111],0\nds_write_addtid_b32 v217 offset:2304\n" \
"v_mfma_f32_16x16x32_bf16 v[20:23],v[180:183],v[112:115],v[20:23]\nds_write_addtid_b32 v218 offset:2560\nv_mfma_f32_16x16x32_bf16 v[24:27],v[152:155],v[108:111],0\nds_write_addtid_b32 v219 offset:2816\n" \
"v_mfma_f32_16x16x32_bf16 v[24:27],v[184:187],v[112:115],v[24:27]\nds_write_addtid_b32 v220 offset:3072\nv_mfma_f32_16x16x32_bf16 v[28:31],v[156:159],v[108:111],0\nglobal_load_dwordx4 v[80:83],v59,s[12:13] offset:2048 sc1\n" \
"ds_write_addtid_b32 v221 offset:3328\nv_mfma_f32_16x16x32_bf16 v[28:31],v[188:191],v[112:115],v[28:31]\nds_write_addtid_b32 v222 offset:3584\ns_waitcnt vmcnt(10)\n" \
"v_mfma_f32_16x16x32_bf16 v[0:3],a[0:3],v[116:119],v[0:3]\nds_write_addtid_b32 v223 offset:3840\nv_bfe_u32 v69,v59,2,8\nv_mfma_f32_16x16x32_bf16 v[0:3],a[32:35],v[120:123],v[0:3]\n" \
"ds_write_addtid_b32 v224 offset:4096\nbuffer_load_dword v64,v69,s[36:39],s79 offen sc1\nv_mfma_f32_16x16x32_bf16 v[4:7],a[4:7],v[116:119],v[4:7]\nds_write_addtid_b32 v225 offset:4352\n" \
"buffer_load_dword v65,v69,s[36:39],s80 offen sc1\nv_mfma_f32_16x16x32_bf16 v[4:7],a[36:39],v[120:123],v[4:7]\nds_write_addtid_b32 v226 offset:4608\nbuffer_load_dword v66,v69,s[36:39],s81 offen sc1\n" \
"v_mfma_f32_16x16x32_bf16 v[8:11],a[8:11],v[116:119],v[8:11]\nglobal_load_dwordx4 v[84:87],v59,s[14:15] sc1\nds_write_addtid_b32 v227 offset:4864\nv_mfma_f32_16x16x32_bf16 v[8:11],a[40:43],v[120:123],v[8:11]\n" \
"ds_write_addtid_b32 v228 offset:5120\nbuffer_load_dword v67,v69,s[36:39],s82 offen sc1\nv_mfma_f32_16x16x32_bf16 v[12:15],a[12:15],v[116:119],v[12:15]\nds_write_addtid_b32 v229 offset:5376\n" \
"buffer_load_dword v68,v57,s[36:39],s90 offen sc1\nv_mfma_f32_16x16x32_bf16 v[12:15],a[44:47],v[120:123],v[12:15]\nds_write_addtid_b32 v230 offset:5632\nv_add_u32 v70,s98,v58\n" \
"v_mfma_f32_16x16x32_bf16 v[16:19],a[16:19],v[116:119],v[16:19]\ns_add_u32 s59,s59,0x10000\nbuffer_load_dword v63,v61,s[36:39],s59 offen sc1\nv_mfma_f32_16x16x32_bf16 v[16:19],a[48:51],v[120:123],v[16:19]\n" \
"ds_write_addtid_b32 v231 offset:5888\nv_mfma_f32_16x16x32_bf16 v[20:23],a[20:23],v[116:119],v[20:23]\nglobal_load_dwordx4 v[88:91],v59,s[14:15] offset:1024 sc1\nds_write_addtid_b32 v232 offset:6144\n" \
"v_mfma_f32_16x16x32_bf16 v[20:23],a[52:55],v[120:123],v[20:23]\nds_write_addtid_b32 v233 offset:6400\nv_mfma_f32_16x16x32_bf16 v[24:27],a[24:27],v[116:119],v[24:27]\nds_write_addtid_b32 v234 offset:6656\n" \
"v_mfma_f32_16x16x32_bf16 v[24:27],a[56:59],v[120:123],v[24:27]\nds_write_addtid_b32 v235 offset:6912\nv_mfma_f32_16x16x32_bf16 v[28:31],a[28:31],v[116:119],v[28:31]\nds_write_addtid_b32 v236 offset:7168\n" \
"v_mfma_f32_16x16x32_bf16 v[28:31],a[60:63],v[120:123],v[28:31]\nds_write_addtid_b32 v237 offset:7424\ns_waitcnt vmcnt(16)\nv_mfma_f32_16x16x32_bf16 v[0:3],a[64:67],v[124:127],v[0:3]\n" \
"global_load_dwordx4 v[92:95],v59,s[14:15] offset:2048 sc1\nds_write_addtid_b32 v238 offset:7680\nv_mfma_f32_16x16x32_bf16 v[0:3],a[96:99],v[192:195],v[0:3]\nds_write_addtid_b32 v239 offset:7936\n" \
"v_mfma_f32_16x16x32_bf16 v[4:7],a[68:71],v[124:127],v[4:7]\nds_read_b64 v[70:71],v70 offset:29704\nv_mfma_f32_16x16x32_bf16 v[4:7],a[100:103],v[192:195],v[4:7]\nv_mfma_f32_16x16x32_bf16 v[8:11],a[72:75],v[124:127],v[8:11]\n" \
"v_mfma_f32_16x16x32_bf16 v[8:11],a[104:107],v[192:195],v[8:11]\nv_mfma_f32_16x16x32_bf16 v[12:15],a[76:79],v[124:127],v[12:15]\nglobal_load_dwordx4 v[96:99],v59,s[16:17] sc1\nv_mfma_f32_16x16x32_bf16 v[12:15],a[108:111],v[192:195],v[12:15]\n" \
"s_waitcnt lgkmcnt(0)\ns_barrier\nv_mfma_f32_16x16x32_bf16 v[16:19],a[80:83],v[124:127],v[16:19]\nds_read_b128 v[32:35],v58\n" \
"v_mfma_f32_16x16x32_bf16 v[16:19],a[112:115],v[192:195],v[16:19]\nds_read_b128 v[36:39],v58 offset:8192\nv_mfma_f32_16x16x32_bf16 v[20:23],a[84:87],v[124:127],v[20:23]\nds_read_b128 v[40:43],v58 offset:16384\n" \
"v_mfma_f32_16x16x32_bf16 v[20:23],a[116:119],v[192:195],v[20:23]\nds_read_b128 v[44:47],v58 offset:24576\nv_mfma_f32_16x16x32_bf16 v[24:27],a[88:91],v[124:127],v[24:27]\nglobal_load_dwordx4 v[100:103],v59,s[16:17] offset:1024 sc1\n" \
"ds_read_b128 v[48:51],v58 offset:4096\nv_mfma_f32_16x16x32_bf16 v[24:27],a[120:123],v[192:195],v[24:27]\nds_read_b128 v[240:243],v58 offset:12288\nv_mfma_f32_16x16x32_bf16 v[28:31],a[92:95],v[124:127],v[28:31]\n" \
"ds_read_b128 v[244:247],v58 offset:20480\nv_mfma_f32_16x16x32_bf16 v[28:31],a[124:127],v[192:195],v[28:31]\nds_read_b128 v[248:251],v58 offset:28672\ns_waitcnt vmcnt(17)\n" \
"v_mfma_f32_16x16x32_bf16 v[0:3],a[128:131],v[196:199],v[0:3]\nv_mfma_f32_16x16x32_bf16 v[0:3],a[160:163],v[200:203],v[0:3]\nv_mfma_f32_16x16x32_bf16 v[4:7],a[132:135],v[196:199],v[4:7]\nglobal_load_dwordx4 v[104:107],v59,s[16:17] offset:2048 sc1\n" \
"v_mfma_f32_16x16x32_bf16 v[4:7],a[164:167],v[200:203],v[4:7]\ns_waitcnt lgkmcnt(4)\nv_add_f32 v32,v32,v36\nv_mfma_f32_16x16x32_bf16 v[8:11],a[136:139],v[196:199],v[8:11]\n" \
"v_add_f32 v33,v33,v37\nv_add_f32 v34,v34,v38\nv_mfma_f32_16x16x32_bf16 v[8:11],a[168:171],v[200:203],v[8:11]\nv_add_f32 v35,v35,v39\n" \
"v_add_f32 v40,v40,v44\nv_mfma_f32_16x16x32_bf16 v[12:15],a[140:143],v[196:199],v[12:15]\nv_add_f32 v41,v41,v45\nv_add_f32 v42,v42,v46\n" \
"v_mfma_f32_16x16x32_bf16 v[12:15],a[172:175],v[200:203],v[12:15]\nv_add_f32 v43,v43,v47\nv_add_f32 v32,v32,v40\nv_mfma_f32_16x16x32_bf16 v[16:19],a[144:147],v[196:199],v[16:19]\n" \
"v_add_f32 v33,v33,v41\nv_add_f32 v34,v34,v42\nv_mfma_f32_16x16x32_bf16 v[16:19],a[176:179],v[200:203],v[16:19]\nv_add_f32 v35,v35,v43\n" \
"v_cvt_pk_bf16_f32 v36,v32,v33\nv_mfma_f32_16x16x32_bf16 v[20:23],a[148:151],v[196:199],v[20:23]\nv_cvt_pk_bf16_f32 v37,v34,v35\ns_waitcnt lgkmcnt(0)\n" \
"v_mfma_f32_16x16x32_bf16 v[20:23],a[180:183],v[200:203],v[20:23]\nv_add_f32 v48,v48,v240\nv_add_f32 v49,v49,v241\nv_mfma_f32_16x16x32_bf16 v[24:27],a[152:155],v[196:199],v[24:27]\n" \
"v_add_f32 v50,v50,v242\nv_add_f32 v51,v51,v243\nv_mfma_f32_16x16x32_bf16 v[24:27],a[184:187],v[200:203],v[24:27]\nv_add_f32 v244,v244,v248\n" \
"v_add_f32 v245,v245,v249\nv_mfma_f32_16x16x32_bf16 v[28:31],a[156:159],v[196:199],v[28:31]\nv_add_f32 v246,v246,v250\nv_add_f32 v247,v247,v251\n" \
"v_mfma_f32_16x16x32_bf16 v[28:31],a[188:191],v[200:203],v[28:31]\nv_add_f32 v48,v48,v244\ns_waitcnt vmcnt(17)\nv_mfma_f32_16x16x32_bf16 v[0:3],a[192:195],v[204:207],v[0:3]\n" \
"v_add_f32 v49,v49,v245\nv_add_f32 v50,v50,v246\nv_mfma_f32_16x16x32_bf16 v[4:7],a[196:199],v[204:207],v[4:7]\nv_add_f32 v51,v51,v247\n" \
"v_cvt_pk_bf16_f32 v38,v48,v49\nv_mfma_f32_16x16x32_bf16 v[8:11],a[200:203],v[204:207],v[8:11]\nv_cvt_pk_bf16_f32 v39,v50,v51\nbuffer_store_dwordx4 v[36:39],v59,s[36:39],s74 offen sc1\n" \
"v_mfma_f32_16x16x32_bf16 v[12:15],a[204:207],v[204:207],v[12:15]\nv_mfma_f32_16x16x32_bf16 v[16:19],a[208:211],v[204:207],v[16:19]\nv_mfma_f32_16x16x32_bf16 v[20:23],a[212:215],v[204:207],v[20:23]\nv_mfma_f32_16x16x32_bf16 v[24:27],a[216:219],v[204:207],v[24:27]\n" \
"v_mfma_f32_16x16x32_bf16 v[28:31],a[220:223],v[204:207],v[28:31]\ns_waitcnt vmcnt(14)\nv_mfma_f32_16x16x32_bf16 v[208:211],v[128:131],v[72:75],0\nv_add_u32 v59,409600,v59\n" \
"v_mfma_f32_16x16x32_bf16 v[208:211],v[160:163],v[76:79],v[208:211]\ns_waitcnt vmcnt(6)\nv_cmp_eq_u32 vcc,0,v63\nv_mfma_f32_16x16x32_bf16 v[212:215],v[132:135],v[72:75],0\n" \
"ds_write_addtid_b32 v0 offset:32768\nv_mfma_f32_16x16x32_bf16 v[212:215],v[164:167],v[76:79],v[212:215]\ns_and_b64 vcc,vcc,s[86:87]\ns_cbranch_scc1 .Lsg4_%=\n" \
".Lbg4_%=:\nv_mfma_f32_16x16x32_bf16 v[216:219],v[136:139],v[72:75],0\nglobal_load_dwordx4 v[108:111],v59,s[12:13] sc1\nds_write_addtid_b32 v1 offset:33024\n" \
"v_mfma_f32_16x16x32_bf16 v[216:219],v[168:171],v[76:79],v[216:219]\nds_write_addtid_b32 v2 offset:33280\nv_mfma_f32_16x16x32_bf16 v[220:223],v[140:143],v[72:75],0\nds_write_addtid_b32 v3 offset:33536\n" \
"v_max3_u32 v52,v64,v65,v66\nv_mfma_f32_16x16x32_bf16 v[220:223],v[172:175],v[76:79],v[220:223]\nds_write_addtid_b32 v4 offset:33792\nv_max_u32 v52,v52,v67\n" \
"v_mfma_f32_16x16x32_bf16 v[224:227],v[144:147],v[72:75],0\nglobal_load_dwordx4 v[112:115],v59,s[12:13] offset:1024 sc1\nds_write_addtid_b32 v5 offset:34048\nv_mfma_f32_16x16x32_bf16 v[224:227],v[176:179],v[76:79],v[224:227]\n" \
"ds_write_addtid_b32 v6 offset:34304\nv_cmp_eq_u32 vcc,-1,v52\nv_mfma_f32_16x16x32_bf16 v[228:231],v[148:151],v[72:75],0\nds_write_addtid_b32 v7 offset:34560\n" \
"v_mfma_f32_16x16x32_bf16 v[228:231],v[180:183],v[76:79],v[228:231]\nds_write_addtid_b32 v8 offset:34816\ns_cbranch_vccnz .Lse_%=\n.Lbe_%=:\n" \
"v_mfma_f32_16x16x32_bf16 v[232:235],v[152:155],v[72:75],0\nds_write_addtid_b32 v9 offset:35072\nv_lshlrev_b32 v52,16,v64\nv_mfma_f32_16x16x32_bf16 v[232:235],v[184:187],v[76:79],v[232:235]\n" \
"ds_write_addtid_b32 v10 offset:35328\nv_and_b32 v53,0xffff0000,v64\nv_mfma_f32_16x16x32_bf16 v[236:239],v[156:159],v[72:75],0\nglobal_load_dwordx4 v[116:119],v59,s[12:13] offset:2048 sc1\n" \
"ds_write_addtid_b32 v11 offset:35584\nv_mfma_f32_16x16x32_bf16 v[236:239],v[188:191],v[76:79],v[236:239]\nds_write_addtid_b32 v12 offset:35840\nv_mfma_f32_16x16x32_bf16 v[208:211],a[0:3],v[80:83],v[208:211]\n" \
"ds_write_addtid_b32 v13 offset:36096\nv_lshlrev_b32 v54,16,v65\nv_mfma_f32_16x16x32_bf16 v[208:211],a[32:35],v[84:87],v[208:211]\nds_write_addtid_b32 v14 offset:36352\n" \
"v_and_b32 v55,0xffff0000,v65\nv_mfma_f32_16x16x32_bf16 v[212:215],a[4:7],v[80:83],v[212:215]\nds_write_addtid_b32 v15 offset:36608\nv_lshlrev_b32 v252,16,v66\n" \
"v_mfma_f32_16x16x32_bf16 v[212:215],a[36:39],v[84:87],v[212:215]\nds_write_addtid_b32 v16 offset:36864\nv_and_b32 v253,0xffff0000,v66\nv_mfma_f32_16x16x32_bf16 v[216:219],a[8:11],v[80:83],v[216:219]\n" \
"global_load_dwordx4 v[120:123],v59,s[14:15] sc1\nds_write_addtid_b32 v17 offset:37120\nv_mfma_f32_16x16x32_bf16 v[216:219],a[40:43],v[84:87],v[216:219]\nds_write_addtid_b32 v18 offset:37376\n" \
"v_lshlrev_b32 v254,16,v67\nv_mfma_f32_16x16x32_bf16 v[220:223],a[12:15],v[80:83],v[220:223]\nds_write_addtid_b32 v19 offset:37632\nv_and_b32 v255,0xffff0000,v67\n" \
"v_mfma_f32_16x16x32_bf16 v[220:223],a[44:47],v[84:87],v[220:223]\nds_write_addtid_b32 v20 offset:37888\nv_add_f32 v52,v52,v54\nv_mfma_f32_16x16x32_bf16 v[224:227],a[16:19],v[80:83],v[224:227]\n" \
"ds_write_addtid_b32 v21 offset:38144\nv_add_f32 v53,v53,v55\nv_mfma_f32_16x16x32_bf16 v[224:227],a[48:51],v[84:87],v[224:227]\nds_write_addtid_b32 v22 offset:38400\n" \
"v_add_f32 v252,v252,v254\nv_mfma_f32_16x16x32_bf16 v[228:231],a[20:23],v[80:83],v[228:231]\nglobal_load_dwordx4 v[124:127],v59,s[14:15] offset:1024 sc1\nds_write_addtid_b32 v23 offset:38656\n" \
"v_mfma_f32_16x16x32_bf16 v[228:231],a[52:55],v[84:87],v[228:231]\nds_write_addtid_b32 v24 offset:38912\nv_add_f32 v253,v253,v255\nv_mfma_f32_16x16x32_bf16 v[232:235],a[24:27],v[80:83],v[232:235]\n" \
"ds_write_addtid_b32 v25 offset:39168\ns_waitcnt lgkmcnt(15)\nv_mfma_f32_16x16x32_bf16 v[232:235],a[56:59],v[84:87],v[232:235]\nds_write_addtid_b32 v26 offset:39424\n" \
"v_add_f32 v52,v52,v70\nv_mfma_f32_16x16x32_bf16 v[236:239],a[28:31],v[80:83],v[236:239]\nds_write_addtid_b32 v27 offset:39680\nv_add_f32 v53,v53,v70\n" \
"v_mfma_f32_16x16x32_bf16 v[236:239],a[60:63],v[84:87],v[236:239]\nds_write_addtid_b32 v28 offset:39936\ns_waitcnt vmcnt(9)\nv_mfma_f32_16x16x32_bf16 v[208:211],a[64:67],v[88:91],v[208:211]\n" \
"global_load_dwordx4 v[192:195],v59,s[14:15] offset:2048 sc1\nds_write_addtid_b32 v29 offset:40192\nv_mfma_f32_16x16x32_bf16 v[208:211],a[96:99],v[92:95],v[208:211]\nds_write_addtid_b32 v30 offset:40448\n" \
"v_lshlrev_b32 v54,16,v71\nv_mfma_f32_16x16x32_bf16 v[212:215],a[68:71],v[88:91],v[212:215]\nds_write_addtid_b32 v31 offset:40704\nv_and_b32 v55,0xffff0000,v71\n" \
"v_mfma_f32_16x16x32_bf16 v[212:215],a[100:103],v[92:95],v[212:215]\nv_lshlrev_b32 v254,16,v68\nv_and_b32 v255,0xffff0000,v68\nv_mfma_f32_16x16x32_bf16 v[216:219],a[72:75],v[88:91],v[216:219]\n" \
"v_add_f32 v52,v52,v252\nv_add_f32 v53,v53,v253\nv_mfma_f32_16x16x32_bf16 v[216:219],a[104:107],v[92:95],v[216:219]\nv_fma_f32 v52,v54,v52,v254\n" \
"v_fma_f32 v53,v55,v53,v255\nv_mfma_f32_16x16x32_bf16 v[220:223],a[76:79],v[88:91],v[220:223]\nglobal_load_dwordx4 v[196:199],v59,s[16:17] sc1\nv_cvt_pk_bf16_f32 v52,v52,v53\n" \
"v_mfma_f32_16x16x32_bf16 v[220:223],a[108:111],v[92:95],v[220:223]\nbuffer_store_dword v52,v57,s[44:47],0 offen sc1\nv_mfma_f32_16x16x32_bf16 v[224:227],a[80:83],v[88:91],v[224:227]\ns_waitcnt lgkmcnt(0)\n" \
"s_barrier\nv_mfma_f32_16x16x32_bf16 v[224:227],a[112:115],v[92:95],v[224:227]\nds_read_b128 v[32:35],v58 offset:32768\nv_mfma_f32_16x16x32_bf16 v[228:231],a[84:87],v[88:91],v[228:231]\n" \
"ds_read_b128 v[36:39],v58 offset:40960\nv_mfma_f32_16x16x32_bf16 v[228:231],a[116:119],v[92:95],v[228:231]\nds_read_b128 v[40:43],v58 offset:49152\nv_mfma_f32_16x16x32_bf16 v[232:235],a[88:91],v[88:91],v[232:235]\n" \
"global_load_dwordx4 v[200:203],v59,s[16:17] offset:1024 sc1\nds_read_b128 v[44:47],v58 offset:57344\nv_mfma_f32_16x16x32_bf16 v[232:235],a[120:123],v[92:95],v[232:235]\nds_read_b128 v[48:51],v58 offset:36864\n" \
"v_mfma_f32_16x16x32_bf16 v[236:239],a[92:95],v[88:91],v[236:239]\nds_read_b128 v[240:243],v58 offset:45056\nv_mfma_f32_16x16x32_bf16 v[236:239],a[124:127],v[92:95],v[236:239]\nds_read_b128 v[244:247],v58 offset:53248\n" \
"s_waitcnt vmcnt(11)\nv_mfma_f32_16x16x32_bf16 v[208:211],a[128:131],v[96:99],v[208:211]\nds_read_b128 v[248:251],v58 offset:61440\nv_mfma_f32_16x16x32_bf16 v[208:211],a[160:163],v[100:103],v[208:211]\n" \
"v_mfma_f32_16x16x32_bf16 v[212:215],a[132:135],v[96:99],v[212:215]\nglobal_load_dwordx4 v[204:207],v59,s[16:17] offset:2048 sc1\nv_mfma_f32_16x16x32_bf16 v[212:215],a[164:167],v[100:103],v[212:215]\nv_mfma_f32_16x16x32_bf16 v[216:219],a[136:139],v[96:99],v[216:219]\n" \
"s_waitcnt lgkmcnt(4)\nv_add_f32 v32,v32,v36\nv_mfma_f32_16x16x32_bf16 v[216:219],a[168:171],v[100:103],v[216:219]\nv_add_f32 v33,v33,v37\n" \
"v_add_f32 v34,v34,v38\nv_mfma_f32_16x16x32_bf16 v[220:223],a[140:143],v[96:99],v[220:223]\nv_add_f32 v35,v35,v39\nv_add_f32 v40,v40,v44\n" \
"v_mfma_f32_16x16x32_bf16 v[220:223],a[172:175],v[100:103],v[220:223]\nv_add_f32 v41,v41,v45\nv_add_f32 v42,v42,v46\nv_mfma_f32_16x16x32_bf16 v[224:227],a[144:147],v[96:99],v[224:227]\n" \
"v_add_f32 v43,v43,v47\nv_add_f32 v32,v32,v40\nv_mfma_f32_16x16x32_bf16 v[224:227],a[176:179],v[100:103],v[224:227]\nv_add_f32 v33,v33,v41\n" \
"v_add_f32 v34,v34,v42\nv_mfma_f32_16x16x32_bf16 v[228:231],a[148:151],v[96:99],v[228:231]\nv_add_f32 v35,v35,v43\nv_cvt_pk_bf16_f32 v36,v32,v33\n" \
"v_mfma_f32_16x16x32_bf16 v[228:231],a[180:183],v[100:103],v[228:231]\nv_cvt_pk_bf16_f32 v37,v34,v35\ns_waitcnt lgkmcnt(0)\nv_mfma_f32_16x16x32_bf16 v[232:235],a[152:155],v[96:99],v[232:235]\n" \
"v_add_f32 v48,v48,v240\nv_add_f32 v49,v49,v241\nv_mfma_f32_16x16x32_bf16 v[232:235],a[184:187],v[100:103],v[232:235]\nv_add_f32 v50,v50,v242\n" \
"v_add_f32 v51,v51,v243\nv_mfma_f32_16x16x32_bf16 v[236:239],a[156:159],v[96:99],v[236:239]\nv_add_f32 v244,v244,v248\nv_add_f32 v245,v245,v249\n" \
"v_mfma_f32_16x16x32_bf16 v[236:239],a[188:191],v[100:103],v[236:239]\nv_add_f32 v246,v246,v250\ns_waitcnt vmcnt(11)\nv_mfma_f32_16x16x32_bf16 v[208:211],a[192:195],v[104:107],v[208:211]\n" \
"v_add_f32 v247,v247,v251\nv_add_f32 v48,v48,v244\nv_mfma_f32_16x16x32_bf16 v[212:215],a[196:199],v[104:107],v[212:215]\nv_add_f32 v49,v49,v245\n" \
"v_add_f32 v50,v50,v246\nv_mfma_f32_16x16x32_bf16 v[216:219],a[200:203],v[104:107],v[216:219]\nv_add_f32 v51,v51,v247\nv_cvt_pk_bf16_f32 v38,v48,v49\n" \
"v_mfma_f32_16x16x32_bf16 v[220:223],a[204:207],v[104:107],v[220:223]\nv_cvt_pk_bf16_f32 v39,v50,v51\nbuffer_store_dwordx4 v[36:39],v59,s[36:39],s74 offen sc1\nv_mfma_f32_16x16x32_bf16 v[224:227],a[208:211],v[104:107],v[224:227]\n" \
"v_mfma_f32_16x16x32_bf16 v[228:231],a[212:215],v[104:107],v[228:231]\nv_mfma_f32_16x16x32_bf16 v[232:235],a[216:219],v[104:107],v[232:235]\nv_mfma_f32_16x16x32_bf16 v[236:239],a[220:223],v[104:107],v[236:239]\ns_waitcnt vmcnt(9)\n" \
"v_mfma_f32_16x16x32_bf16 v[0:3],v[128:131],v[108:111],0\nv_add_u32 v59,409600,v59\nv_add_u32 v60,s98,v58\nv_mfma_f32_16x16x32_bf16 v[0:3],v[160:163],v[112:115],v[0:3]\n" \
"ds_write_addtid_b32 v208\nv_mfma_f32_16x16x32_bf16 v[4:7],v[132:135],v[108:111],0\nglobal_load_dwordx4 v[72:75],v59,s[12:13] sc1\nds_write_addtid_b32 v209 offset:256\n" \
"v_mfma_f32_16x16x32_bf16 v[4:7],v[164:167],v[112:115],v[4:7]\nds_write_addtid_b32 v210 offset:512\nv_mfma_f32_16x16x32_bf16 v[8:11],v[136:139],v[108:111],0\nds_write_addtid_b32 v211 offset:768\n" \
"v_mfma_f32_16x16x32_bf16 v[8:11],v[168:171],v[112:115],v[8:11]\nbuffer_load_dwordx4 v[64:67],v59,s[36:39],s71 offen sc1\nds_write_addtid_b32 v212 offset:1024\nv_mfma_f32_16x16x32_bf16 v[12:15],v[140:143],v[108:111],0\n" \
"ds_write_addtid_b32 v213 offset:1280\nv_mfma_f32_16x16x32_bf16 v[12:15],v[172:175],v[112:115],v[12:15]\nds_write_addtid_b32 v214 offset:1536\nv_mfma_f32_16x16x32_bf16 v[16:19],v[144:147],v[108:111],0\n" \
"global_load_dwordx4 v[76:79],v59,s[12:13] offset:1024 sc1\nds_write_addtid_b32 v215 offset:1792\nv_mfma_f32_16x16x32_bf16 v[16:19],v[176:179],v[112:115],v[16:19]\nds_write_addtid_b32 v216 offset:2048\n" \
"v_mfma_f32_16x16x32_bf16 v[20:23],v[148:151],v[108:111],0\nds_write_addtid_b32 v217 offset:2304\nv_mfma_f32_16x16x32_bf16 v[20:23],v[180:183],v[112:115],v[20:23]\nds_write_addtid_b32 v218 offset:2560\n" \
"v_mfma_f32_16x16x32_bf16 v[24:27],v[152:155],v[108:111],0\nds_write_addtid_b32 v219 offset:2816\nv_mfma_f32_16x16x32_bf16 v[24:27],v[184:187],v[112:115],v[24:27]\nds_write_addtid_b32 v220 offset:3072\n" \
"v_mfma_f32_16x16x32_bf16 v[28:31],v[156:159],v[108:111],0\nglobal_load_dwordx4 v[80:83],v59,s[12:13] offset:2048 sc1\nds_write_addtid_b32 v221 offset:3328\nv_mfma_f32_16x16x32_bf16 v[28:31],v[188:191],v[112:115],v[28:31]\n" \
"ds_write_addtid_b32 v222 offset:3584\ns_waitcnt vmcnt(11)\nv_mfma_f32_16x16x32_bf16 v[0:3],a[0:3],v[116:119],v[0:3]\nds_write_addtid_b32 v223 offset:3840\n" \
"v_mfma_f32_16x16x32_bf16 v[0:3],a[32:35],v[120:123],v[0:3]\nbuffer_load_dwordx4 v[52:55],v59,s[36:39],s76 offen sc1\nds_write_addtid_b32 v224 offset:4096\nv_mfma_f32_16x16x32_bf16 v[4:7],a[4:7],v[116:119],v[4:7]\n" \
"ds_write_addtid_b32 v225 offset:4352\nv_mfma_f32_16x16x32_bf16 v[4:7],a[36:39],v[120:123],v[4:7]\nds_write_addtid_b32 v226 offset:4608\nv_mfma_f32_16x16x32_bf16 v[8:11],a[8:11],v[116:119],v[8:11]\n" \
"global_load_dwordx4 v[84:87],v59,s[14:15] sc1\nds_write_addtid_b32 v227 offset:4864\nv_mfma_f32_16x16x32_bf16 v[8:11],a[40:43],v[120:123],v[8:11]\nds_write_addtid_b32 v228 offset:5120\n" \
"v_mfma_f32_16x16x32_bf16 v[12:15],a[12:15],v[116:119],v[12:15]\nds_write_addtid_b32 v229 offset:5376\nv_mfma_f32_16x16x32_bf16 v[12:15],a[44:47],v[120:123],v[12:15]\nds_write_addtid_b32 v230 offset:5632\n" \
"v_mfma_f32_16x16x32_bf16 v[16:19],a[16:19],v[116:119],v[16:19]\nds_write_addtid_b32 v231 offset:5888\nv_mfma_f32_16x16x32_bf16 v[16:19],a[48:51],v[120:123],v[16:19]\nds_write_addtid_b32 v232 offset:6144\n" \
"v_mfma_f32_16x16x32_bf16 v[20:23],a[20:23],v[116:119],v[20:23]\nglobal_load_dwordx4 v[88:91],v59,s[14:15] offset:1024 sc1\nds_write_addtid_b32 v233 offset:6400\nv_mfma_f32_16x16x32_bf16 v[20:23],a[52:55],v[120:123],v[20:23]\n" \
"ds_write_addtid_b32 v234 offset:6656\nv_mfma_f32_16x16x32_bf16 v[24:27],a[24:27],v[116:119],v[24:27]\nds_write_addtid_b32 v235 offset:6912\nv_mfma_f32_16x16x32_bf16 v[24:27],a[56:59],v[120:123],v[24:27]\n" \
"ds_write_addtid_b32 v236 offset:7168\nv_mfma_f32_16x16x32_bf16 v[28:31],a[28:31],v[116:119],v[28:31]\nds_write_addtid_b32 v237 offset:7424\nv_mfma_f32_16x16x32_bf16 v[28:31],a[60:63],v[120:123],v[28:31]\n" \
"ds_write_addtid_b32 v238 offset:7680\ns_waitcnt vmcnt(12)\nv_mfma_f32_16x16x32_bf16 v[0:3],a[64:67],v[124:127],v[0:3]\nglobal_load_dwordx4 v[92:95],v59,s[14:15] offset:2048 sc1\n" \
"ds_write_addtid_b32 v239 offset:7936\nv_mfma_f32_16x16x32_bf16 v[0:3],a[96:99],v[192:195],v[0:3]\nds_read_b32 v61,v60 offset:28676\nv_mfma_f32_16x16x32_bf16 v[4:7],a[68:71],v[124:127],v[4:7]\n" \
"v_mfma_f32_16x16x32_bf16 v[4:7],a[100:103],v[192:195],v[4:7]\nv_mfma_f32_16x16x32_bf16 v[8:11],a[72:75],v[124:127],v[8:11]\nv_mfma_f32_16x16x32_bf16 v[8:11],a[104:107],v[192:195],v[8:11]\nv_mfma_f32_16x16x32_bf16 v[12:15],a[76:79],v[124:127],v[12:15]\n" \
"global_load_dwordx4 v[96:99],v59,s[16:17] sc1\nv_mfma_f32_16x16x32_bf16 v[12:15],a[108:111],v[192:195],v[12:15]\ns_waitcnt lgkmcnt(0)\ns_barrier\n" \
"v_mfma_f32_16x16x32_bf16 v[16:19],a[80:83],v[124:127],v[16:19]\nds_read_b128 v[32:35],v58\nv_mfma_f32_16x16x32_bf16 v[16:19],a[112:115],v[192:195],v[16:19]\nds_read_b128 v[36:39],v58 offset:8192\n" \
"v_mfma_f32_16x16x32_bf16 v[20:23],a[84:87],v[124:127],v[20:23]\nds_read_b128 v[40:43],v58 offset:16384\nv_lshlrev_b32 v60,16,v61\nv_mfma_f32_16x16x32_bf16 v[20:23],a[116:119],v[192:195],v[20:23]\n" \
"ds_read_b128 v[44:47],v58 offset:24576\nv_and_b32 v61,0xffff0000,v61\nv_mfma_f32_16x16x32_bf16 v[24:27],a[88:91],v[124:127],v[24:27]\nglobal_load_dwordx4 v[100:103],v59,s[16:17] offset:1024 sc1\n" \
"ds_read_b128 v[48:51],v58 offset:4096\nv_mfma_f32_16x16x32_bf16 v[24:27],a[120:123],v[192:195],v[24:27]\nds_read_b128 v[240:243],v58 offset:12288\nv_mfma_f32_16x16x32_bf16 v[28:31],a[92:95],v[124:127],v[28:31]\n" \
"ds_read_b128 v[244:247],v58 offset:20480\nv_mfma_f32_16x16x32_bf16 v[28:31],a[124:127],v[192:195],v[28:31]\nds_read_b128 v[248:251],v58 offset:28672\ns_waitcnt vmcnt(12)\n" \
"v_mfma_f32_16x16x32_bf16 v[0:3],a[128:131],v[196:199],v[0:3]\nv_mfma_f32_16x16x32_bf16 v[0:3],a[160:163],v[200:203],v[0:3]\nv_mfma_f32_16x16x32_bf16 v[4:7],a[132:135],v[196:199],v[4:7]\nglobal_load_dwordx4 v[104:107],v59,s[16:17] offset:2048 sc1\n" \
"v_mfma_f32_16x16x32_bf16 v[4:7],a[164:167],v[200:203],v[4:7]\ns_waitcnt lgkmcnt(4)\nv_add_f32 v32,v32,v36\nv_mfma_f32_16x16x32_bf16 v[8:11],a[136:139],v[196:199],v[8:11]\n" \
"v_add_f32 v33,v33,v37\nv_add_f32 v34,v34,v38\nv_mfma_f32_16x16x32_bf16 v[8:11],a[168:171],v[200:203],v[8:11]\nv_add_f32 v35,v35,v39\n" \
"v_add_f32 v40,v40,v44\nv_mfma_f32_16x16x32_bf16 v[12:15],a[140:143],v[196:199],v[12:15]\nv_add_f32 v41,v41,v45\nv_add_f32 v42,v42,v46\n" \
"v_mfma_f32_16x16x32_bf16 v[12:15],a[172:175],v[200:203],v[12:15]\nv_add_f32 v43,v43,v47\nv_add_f32 v32,v32,v40\nv_mfma_f32_16x16x32_bf16 v[16:19],a[144:147],v[196:199],v[16:19]\n" \
"v_add_f32 v33,v33,v41\nv_add_f32 v34,v34,v42\nv_mfma_f32_16x16x32_bf16 v[16:19],a[176:179],v[200:203],v[16:19]\nv_add_f32 v35,v35,v43\n" \
"v_cvt_pk_bf16_f32 v36,v32,v33\nv_mfma_f32_16x16x32_bf16 v[20:23],a[148:151],v[196:199],v[20:23]\nv_cvt_pk_bf16_f32 v37,v34,v35\ns_waitcnt lgkmcnt(0)\n" \
"v_mfma_f32_16x16x32_bf16 v[20:23],a[180:183],v[200:203],v[20:23]\nv_add_f32 v48,v48,v240\nv_add_f32 v49,v49,v241\nv_mfma_f32_16x16x32_bf16 v[24:27],a[152:155],v[196:199],v[24:27]\n" \
"v_add_f32 v50,v50,v242\nv_add_f32 v51,v51,v243\nv_mfma_f32_16x16x32_bf16 v[24:27],a[184:187],v[200:203],v[24:27]\nv_add_f32 v244,v244,v248\n" \
"v_add_f32 v245,v245,v249\nv_mfma_f32_16x16x32_bf16 v[28:31],a[156:159],v[196:199],v[28:31]\nv_add_f32 v246,v246,v250\nv_add_f32 v247,v247,v251\n" \
"v_mfma_f32_16x16x32_bf16 v[28:31],a[188:191],v[200:203],v[28:31]\nv_add_f32 v48,v48,v244\ns_waitcnt vmcnt(12)\nv_mfma_f32_16x16x32_bf16 v[0:3],a[192:195],v[204:207],v[0:3]\n" \
"v_add_f32 v49,v49,v245\nv_add_f32 v50,v50,v246\nv_mfma_f32_16x16x32_bf16 v[4:7],a[196:199],v[204:207],v[4:7]\nv_add_f32 v51,v51,v247\n" \
"v_cvt_pk_bf16_f32 v38,v48,v49\nv_mfma_f32_16x16x32_bf16 v[8:11],a[200:203],v[204:207],v[8:11]\nv_cvt_pk_bf16_f32 v39,v50,v51\nbuffer_store_dwordx4 v[36:39],v59,s[36:39],s74 offen sc1\n" \
"v_mfma_f32_16x16x32_bf16 v[12:15],a[204:207],v[204:207],v[12:15]\nv_mfma_f32_16x16x32_bf16 v[16:19],a[208:211],v[204:207],v[16:19]\nv_mfma_f32_16x16x32_bf16 v[20:23],a[212:215],v[204:207],v[20:23]\nv_mfma_f32_16x16x32_bf16 v[24:27],a[216:219],v[204:207],v[24:27]\n" \
"v_mfma_f32_16x16x32_bf16 v[28:31],a[220:223],v[204:207],v[28:31]\ns_waitcnt vmcnt(9)\nv_mfma_f32_16x16x32_bf16 v[208:211],v[128:131],v[72:75],0\nv_add_u32 v59,409600,v59\n" \
"s_waitcnt vmcnt(7)\nv_mfma_f32_16x16x32_bf16 v[208:211],v[160:163],v[76:79],v[208:211]\nds_write_addtid_b32 v0 offset:32768\nv_max_u32 v62,v64,v52\n" \
"v_mfma_f32_16x16x32_bf16 v[212:215],v[132:135],v[72:75],0\nglobal_load_dwordx4 v[108:111],v59,s[12:13] sc1\nds_write_addtid_b32 v1 offset:33024\nv_mfma_f32_16x16x32_bf16 v[212:215],v[164:167],v[76:79],v[212:215]\n" \
"ds_write_addtid_b32 v2 offset:33280\nv_cmp_eq_u32 vcc,-1,v62\nv_mfma_f32_16x16x32_bf16 v[216:219],v[136:139],v[72:75],0\nds_write_addtid_b32 v3 offset:33536\n" \
"v_mfma_f32_16x16x32_bf16 v[216:219],v[168:171],v[76:79],v[216:219]\nds_write_addtid_b32 v4 offset:33792\ns_cbranch_vccnz .Lsp1_%=\n.Lbp1_%=:\n" \
"v_mfma_f32_16x16x32_bf16 v[220:223],v[140:143],v[72:75],0\nds_write_addtid_b32 v5 offset:34048\nv_and_b32 v71,0xffff0000,v67\nv_mfma_f32_16x16x32_bf16 v[220:223],v[172:175],v[76:79],v[220:223]\n" \
"ds_write_addtid_b32 v6 offset:34304\nv_lshlrev_b32 v70,16,v67\nv_mfma_f32_16x16x32_bf16 v[224:227],v[144:147],v[72:75],0\nglobal_load_dwordx4 v[112:115],v59,s[12:13] offset:1024 sc1\n" \
"ds_write_addtid_b32 v7 offset:34560\nv_mfma_f32_16x16x32_bf16 v[224:227],v[176:179],v[76:79],v[224:227]\nds_write_addtid_b32 v8 offset:34816\nv_and_b32 v69,0xffff0000,v66\n" \
"v_mfma_f32_16x16x32_bf16 v[228:231],v[148:151],v[72:75],0\nds_write_addtid_b32 v9 offset:35072\nv_lshlrev_b32 v68,16,v66\nv_mfma_f32_16x16x32_bf16 v[228:231],v[180:183],v[76:79],v[228:231]\n" \
"ds_write_addtid_b32 v10 offset:35328\nv_and_b32 v67,0xffff0000,v65\nv_mfma_f32_16x16x32_bf16 v[232:235],v[152:155],v[72:75],0\nds_write_addtid_b32 v11 offset:35584\n" \
"v_lshlrev_b32 v66,16,v65\nv_mfma_f32_16x16x32_bf16 v[232:235],v[184:187],v[76:79],v[232:235]\nds_write_addtid_b32 v12 offset:35840\nv_and_b32 v65,0xffff0000,v64\n" \
"v_mfma_f32_16x16x32_bf16 v[236:239],v[156:159],v[72:75],0\nglobal_load_dwordx4 v[116:119],v59,s[12:13] offset:2048 sc1\nds_write_addtid_b32 v13 offset:36096\nv_mfma_f32_16x16x32_bf16 v[236:239],v[188:191],v[76:79],v[236:239]\n" \
"ds_write_addtid_b32 v14 offset:36352\ns_waitcnt vmcnt(9)\nv_mfma_f32_16x16x32_bf16 v[208:211],a[0:3],v[80:83],v[208:211]\nds_write_addtid_b32 v15 offset:36608\n" \
"v_lshlrev_b32 v64,16,v64\nv_mfma_f32_16x16x32_bf16 v[208:211],a[32:35],v[84:87],v[208:211]\nds_write_addtid_b32 v16 offset:36864\nv_add_f32 v64,v64,v60\n" \
"v_mfma_f32_16x16x32_bf16 v[212:215],a[4:7],v[80:83],v[212:215]\nds_write_addtid_b32 v17 offset:37120\nv_add_f32 v65,v65,v60\nv_mfma_f32_16x16x32_bf16 v[212:215],a[36:39],v[84:87],v[212:215]\n" \
"ds_write_addtid_b32 v18 offset:37376\nv_add_f32 v66,v66,v60\nv_mfma_f32_16x16x32_bf16 v[216:219],a[8:11],v[80:83],v[216:219]\nglobal_load_dwordx4 v[120:123],v59,s[14:15] sc1\n" \
"ds_write_addtid_b32 v19 offset:37632\nv_mfma_f32_16x16x32_bf16 v[216:219],a[40:43],v[84:87],v[216:219]\nds_write_addtid_b32 v20 offset:37888\nv_add_f32 v67,v67,v60\n" \
"v_mfma_f32_16x16x32_bf16 v[220:223],a[12:15],v[80:83],v[220:223]\nds_write_addtid_b32 v21 offset:38144\nv_add_f32 v68,v68,v61\nv_mfma_f32_16x16x32_bf16 v[220:223],a[44:47],v[84:87],v[220:223]\n" \
"ds_write_addtid_b32 v22 offset:38400\nv_add_f32 v69,v69,v61\nv_mfma_f32_16x16x32_bf16 v[224:227],a[16:19],v[80:83],v[224:227]\nds_write_addtid_b32 v23 offset:38656\n" \
"v_add_f32 v70,v70,v61\nv_mfma_f32_16x16x32_bf16 v[224:227],a[48:51],v[84:87],v[224:227]\nds_write_addtid_b32 v24 offset:38912\nv_add_f32 v71,v71,v61\n" \
"v_mfma_f32_16x16x32_bf16 v[228:231],a[20:23],v[80:83],v[228:231]\nglobal_load_dwordx4 v[124:127],v59,s[14:15] offset:1024 sc1\nds_write_addtid_b32 v25 offset:39168\nv_mfma_f32_16x16x32_bf16 v[228:231],a[52:55],v[84:87],v[228:231]\n" \
"ds_write_addtid_b32 v26 offset:39424\nv_and_b32 v255,0xffff0000,v55\nv_mfma_f32_16x16x32_bf16 v[232:235],a[24:27],v[80:83],v[232:235]\nds_write_addtid_b32 v27 offset:39680\n" \
"v_lshlrev_b32 v254,16,v55\nv_mfma_f32_16x16x32_bf16 v[232:235],a[56:59],v[84:87],v[232:235]\nds_write_addtid_b32 v28 offset:39936\nv_and_b32 v253,0xffff0000,v54\n" \
"v_mfma_f32_16x16x32_bf16 v[236:239],a[28:31],v[80:83],v[236:239]\nds_write_addtid_b32 v29 offset:40192\nv_lshlrev_b32 v252,16,v54\nv_mfma_f32_16x16x32_bf16 v[236:239],a[60:63],v[84:87],v[236:239]\n" \
"ds_write_addtid_b32 v30 offset:40448\ns_waitcnt vmcnt(9)\nv_mfma_f32_16x16x32_bf16 v[208:211],a[64:67],v[88:91],v[208:211]\nglobal_load_dwordx4 v[192:195],v59,s[14:15] offset:2048 sc1\n" \
"ds_write_addtid_b32 v31 offset:40704\nv_mfma_f32_16x16x32_bf16 v[208:211],a[96:99],v[92:95],v[208:211]\nv_and_b32 v55,0xffff0000,v53\nv_lshlrev_b32 v54,16,v53\n" \
"v_mfma_f32_16x16x32_bf16 v[212:215],a[68:71],v[88:91],v[212:215]\nv_and_b32 v53,0xffff0000,v52\nv_lshlrev_b32 v52,16,v52\nv_mfma_f32_16x16x32_bf16 v[212:215],a[100:103],v[92:95],v[212:215]\n" \
"v_add_f32 v52,v52,v60\nv_add_f32 v53,v53,v60\nv_mfma_f32_16x16x32_bf16 v[216:219],a[72:75],v[88:91],v[216:219]\nv_add_f32 v54,v54,v60\n" \
"v_add_f32 v55,v55,v60\nv_mfma_f32_16x16x32_bf16 v[216:219],a[104:107],v[92:95],v[216:219]\nv_add_f32 v252,v252,v61\nv_add_f32 v253,v253,v61\n" \
"v_mfma_f32_16x16x32_bf16 v[220:223],a[76:79],v[88:91],v[220:223]\nglobal_load_dwordx4 v[196:199],v59,s[16:17] sc1\nv_add_f32 v254,v254,v61\nv_mfma_f32_16x16x32_bf16 v[220:223],a[108:111],v[92:95],v[220:223]\n" \
"s_waitcnt lgkmcnt(0)\ns_barrier\nv_mfma_f32_16x16x32_bf16 v[224:227],a[80:83],v[88:91],v[224:227]\nds_read_b128 v[32:35],v58 offset:32768\n" \
"v_add_f32 v255,v255,v61\nv_mfma_f32_16x16x32_bf16 v[224:227],a[112:115],v[92:95],v[224:227]\nds_read_b128 v[36:39],v58 offset:40960\nv_mfma_f32_16x16x32_bf16 v[228:231],a[84:87],v[88:91],v[228:231]\n" \
"ds_read_b128 v[40:43],v58 offset:49152\nv_mfma_f32_16x16x32_bf16 v[228:231],a[116:119],v[92:95],v[228:231]\nds_read_b128 v[44:47],v58 offset:57344\nv_mfma_f32_16x16x32_bf16 v[232:235],a[88:91],v[88:91],v[232:235]\n" \
"global_load_dwordx4 v[200:203],v59,s[16:17] offset:1024 sc1\nds_read_b128 v[48:51],v58 offset:36864\nv_mfma_f32_16x16x32_bf16 v[232:235],a[120:123],v[92:95],v[232:235]\nds_read_b128 v[240:243],v58 offset:45056\n" \
"v_mfma_f32_16x16x32_bf16 v[236:239],a[92:95],v[88:91],v[236:239]\nds_read_b128 v[244:247],v58 offset:53248\nv_mfma_f32_16x16x32_bf16 v[236:239],a[124:127],v[92:95],v[236:239]\nbuffer_load_dwordx4 v[60:63],v59,s[36:39],s72 offen sc1\n" \
"s_waitcnt vmcnt(11)\nv_mfma_f32_16x16x32_bf16 v[208:211],a[128:131],v[96:99],v[208:211]\nds_read_b128 v[248:251],v58 offset:61440\nv_mfma_f32_16x16x32_bf16 v[208:211],a[160:163],v[100:103],v[208:211]\n" \
"v_mfma_f32_16x16x32_bf16 v[212:215],a[132:135],v[96:99],v[212:215]\nglobal_load_dwordx4 v[204:207],v59,s[16:17] offset:2048 sc1\nv_mfma_f32_16x16x32_bf16 v[212:215],a[164:167],v[100:103],v[212:215]\ns_waitcnt lgkmcnt(4)\n" \
"v_add_f32 v32,v32,v36\nv_mfma_f32_16x16x32_bf16 v[216:219],a[136:139],v[96:99],v[216:219]\nv_add_f32 v33,v33,v37\nv_add_f32 v34,v34,v38\n" \
"v_mfma_f32_16x16x32_bf16 v[216:219],a[168:171],v[100:103],v[216:219]\nv_add_f32 v35,v35,v39\nv_add_f32 v40,v40,v44\nv_mfma_f32_16x16x32_bf16 v[220:223],a[140:143],v[96:99],v[220:223]\n" \
"v_add_f32 v41,v41,v45\nv_add_f32 v42,v42,v46\nv_mfma_f32_16x16x32_bf16 v[220:223],a[172:175],v[100:103],v[220:223]\nv_add_f32 v43,v43,v47\n" \
"v_add_f32 v32,v32,v40\nv_mfma_f32_16x16x32_bf16 v[224:227],a[144:147],v[96:99],v[224:227]\nv_add_f32 v33,v33,v41\nv_add_f32 v34,v34,v42\n" \
"v_mfma_f32_16x16x32_bf16 v[224:227],a[176:179],v[100:103],v[224:227]\nv_add_f32 v35,v35,v43\nv_cvt_pk_bf16_f32 v36,v32,v33\nv_mfma_f32_16x16x32_bf16 v[228:231],a[148:151],v[96:99],v[228:231]\n" \
"v_cvt_pk_bf16_f32 v37,v34,v35\ns_waitcnt lgkmcnt(0)\nv_mfma_f32_16x16x32_bf16 v[228:231],a[180:183],v[100:103],v[228:231]\nv_add_f32 v48,v48,v240\n" \
"v_add_f32 v49,v49,v241\nv_mfma_f32_16x16x32_bf16 v[232:235],a[152:155],v[96:99],v[232:235]\nv_add_f32 v50,v50,v242\nv_add_f32 v51,v51,v243\n" \
"v_mfma_f32_16x16x32_bf16 v[232:235],a[184:187],v[100:103],v[232:235]\nv_add_f32 v244,v244,v248\nv_add_f32 v245,v245,v249\nv_mfma_f32_16x16x32_bf16 v[236:239],a[156:159],v[96:99],v[236:239]\n" \
"v_add_f32 v246,v246,v250\nv_add_f32 v247,v247,v251\nv_mfma_f32_16x16x32_bf16 v[236:239],a[188:191],v[100:103],v[236:239]\nv_add_f32 v48,v48,v244\n" \
"s_waitcnt vmcnt(11)\nv_mfma_f32_16x16x32_bf16 v[208:211],a[192:195],v[104:107],v[208:211]\nv_add_f32 v49,v49,v245\nv_add_f32 v50,v50,v246\n" \
"v_mfma_f32_16x16x32_bf16 v[212:215],a[196:199],v[104:107],v[212:215]\nv_add_f32 v51,v51,v247\nv_cvt_pk_bf16_f32 v38,v48,v49\nv_mfma_f32_16x16x32_bf16 v[216:219],a[200:203],v[104:107],v[216:219]\n" \
"v_cvt_pk_bf16_f32 v39,v50,v51\nbuffer_store_dwordx4 v[36:39],v59,s[36:39],s74 offen sc1\nv_mfma_f32_16x16x32_bf16 v[220:223],a[204:207],v[104:107],v[220:223]\nv_mfma_f32_16x16x32_bf16 v[224:227],a[208:211],v[104:107],v[224:227]\n" \
"v_mfma_f32_16x16x32_bf16 v[228:231],a[212:215],v[104:107],v[228:231]\nv_mfma_f32_16x16x32_bf16 v[232:235],a[216:219],v[104:107],v[232:235]\nv_mfma_f32_16x16x32_bf16 v[236:239],a[220:223],v[104:107],v[236:239]\ns_waitcnt vmcnt(9)\n" \
"v_mfma_f32_16x16x32_bf16 v[0:3],v[128:131],v[108:111],0\nv_add_u32 v59,409600,v59\nv_mfma_f32_16x16x32_bf16 v[0:3],v[160:163],v[112:115],v[0:3]\nds_write_addtid_b32 v208\n" \
"ds_write_addtid_b32 v209 offset:256\nv_mfma_f32_16x16x32_bf16 v[4:7],v[132:135],v[108:111],0\nglobal_load_dwordx4 v[72:75],v59,s[12:13] sc1\nds_write_addtid_b32 v210 offset:512\n" \
"v_mfma_f32_16x16x32_bf16 v[4:7],v[164:167],v[112:115],v[4:7]\nds_write_addtid_b32 v211 offset:768\nds_write_addtid_b32 v212 offset:1024\nv_mfma_f32_16x16x32_bf16 v[8:11],v[136:139],v[108:111],0\n" \
"ds_write_addtid_b32 v213 offset:1280\nds_write_addtid_b32 v214 offset:1536\nv_mfma_f32_16x16x32_bf16 v[8:11],v[168:171],v[112:115],v[8:11]\nds_write_addtid_b32 v215 offset:1792\n" \
"ds_write_addtid_b32 v216 offset:2048\nv_mfma_f32_16x16x32_bf16 v[12:15],v[140:143],v[108:111],0\nds_write_addtid_b32 v217 offset:2304\nds_write_addtid_b32 v218 offset:2560\n" \
"v_mfma_f32_16x16x32_bf16 v[12:15],v[172:175],v[112:115],v[12:15]\nds_write_addtid_b32 v219 offset:2816\nds_write_addtid_b32 v220 offset:3072\nv_mfma_f32_16x16x32_bf16 v[16:19],v[144:147],v[108:111],0\n" \
"global_load_dwordx4 v[76:79],v59,s[12:13] offset:1024 sc1\nds_write_addtid_b32 v221 offset:3328\nv_mfma_f32_16x16x32_bf16 v[16:19],v[176:179],v[112:115],v[16:19]\nds_write_addtid_b32 v222 offset:3584\n" \
"ds_write_addtid_b32 v223 offset:3840\nv_mfma_f32_16x16x32_bf16 v[20:23],v[148:151],v[108:111],0\nds_write_addtid_b32 v224 offset:4096\nds_write_addtid_b32 v225 offset:4352\n" \
"v_mfma_f32_16x16x32_bf16 v[20:23],v[180:183],v[112:115],v[20:23]\nds_write_addtid_b32 v226 offset:4608\nds_write_addtid_b32 v227 offset:4864\nv_mfma_f32_16x16x32_bf16 v[24:27],v[152:155],v[108:111],0\n" \
"ds_write_addtid_b32 v228 offset:5120\nds_write_addtid_b32 v229 offset:5376\nv_mfma_f32_16x16x32_bf16 v[24:27],v[184:187],v[112:115],v[24:27]\nds_write_addtid_b32 v230 offset:5632\n" \
"ds_write_addtid_b32 v231 offset:5888\nv_mfma_f32_16x16x32_bf16 v[28:31],v[156:159],v[108:111],0\nglobal_load_dwordx4 v[80:83],v59,s[12:13] offset:2048 sc1\nds_write_addtid_b32 v232 offset:6144\n" \
"v_mfma_f32_16x16x32_bf16 v[28:31],v[188:191],v[112:115],v[28:31]\nds_write_addtid_b32 v233 offset:6400\ns_waitcnt vmcnt(10)\nv_mfma_f32_16x16x32_bf16 v[0:3],a[0:3],v[116:119],v[0:3]\n" \
"ds_write_addtid_b32 v234 offset:6656\nds_write_addtid_b32 v235 offset:6912\nv_mfma_f32_16x16x32_bf16 v[0:3],a[32:35],v[120:123],v[0:3]\nbuffer_load_dwordx4 v[108:111],v59,s[36:39],s77 offen sc1\n" \
"ds_write_addtid_b32 v236 offset:7168\nv_mfma_f32_16x16x32_bf16 v[4:7],a[4:7],v[116:119],v[4:7]\nds_write_addtid_b32 v237 offset:7424\nds_write_addtid_b32 v238 offset:7680\n" \
"v_mfma_f32_16x16x32_bf16 v[4:7],a[36:39],v[120:123],v[4:7]\nds_write_addtid_b32 v239 offset:7936\nv_mfma_f32_16x16x32_bf16 v[8:11],a[8:11],v[116:119],v[8:11]\nglobal_load_dwordx4 v[84:87],v59,s[14:15] sc1\n" \
"v_mfma_f32_16x16x32_bf16 v[8:11],a[40:43],v[120:123],v[8:11]\nv_mfma_f32_16x16x32_bf16 v[12:15],a[12:15],v[116:119],v[12:15]\nv_mfma_f32_16x16x32_bf16 v[12:15],a[44:47],v[120:123],v[12:15]\nv_mfma_f32_16x16x32_bf16 v[16:19],a[16:19],v[116:119],v[16:19]\n" \
"v_mfma_f32_16x16x32_bf16 v[16:19],a[48:51],v[120:123],v[16:19]\ns_waitcnt lgkmcnt(0)\ns_barrier\nv_mfma_f32_16x16x32_bf16 v[20:23],a[20:23],v[116:119],v[20:23]\n" \
"global_load_dwordx4 v[88:91],v59,s[14:15] offset:1024 sc1\nds_read_b128 v[32:35],v58\nv_mfma_f32_16x16x32_bf16 v[20:23],a[52:55],v[120:123],v[20:23]\nds_read_b128 v[36:39],v58 offset:8192\n" \
"ds_read_b128 v[40:43],v58 offset:16384\nv_mfma_f32_16x16x32_bf16 v[24:27],a[24:27],v[116:119],v[24:27]\nds_read_b128 v[44:47],v58 offset:24576\nds_read_b128 v[48:51],v58 offset:4096\n" \
"v_mfma_f32_16x16x32_bf16 v[24:27],a[56:59],v[120:123],v[24:27]\nds_read_b128 v[240:243],v58 offset:12288\nds_read_b128 v[244:247],v58 offset:20480\nv_mfma_f32_16x16x32_bf16 v[28:31],a[28:31],v[116:119],v[28:31]\n" \
"ds_read_b128 v[248:251],v58 offset:28672\ns_waitcnt vmcnt(8)\nv_mfma_f32_16x16x32_bf16 v[28:31],a[60:63],v[120:123],v[28:31]\nv_cmp_eq_u32 vcc,-1,v60\n" \
"v_mfma_f32_16x16x32_bf16 v[0:3],a[64:67],v[124:127],v[0:3]\nglobal_load_dwordx4 v[92:95],v59,s[14:15] offset:2048 sc1\nv_mfma_f32_16x16x32_bf16 v[0:3],a[96:99],v[192:195],v[0:3]\nbuffer_load_dwordx2 v[116:117],v56,s[36:39],s90 offen sc1\n" \
"s_cbranch_vccnz .Lsp2A_%=\n.Lbp2A_%=:\nv_mfma_f32_16x16x32_bf16 v[4:7],a[68:71],v[124:127],v[4:7]\nbuffer_load_dwordx2 v[118:119],v56,s[36:39],s90 offen offset:32 sc1\n" \
"v_lshlrev_b32 v112,16,v60\nv_mfma_f32_16x16x32_bf16 v[4:7],a[100:103],v[192:195],v[4:7]\nv_and_b32 v113,0xffff0000,v60\nv_add_f32 v64,v64,v112\n" \
"v_mfma_f32_16x16x32_bf16 v[8:11],a[72:75],v[124:127],v[8:11]\ns_waitcnt lgkmcnt(4)\nv_add_f32 v32,v32,v36\nv_mfma_f32_16x16x32_bf16 v[8:11],a[104:107],v[192:195],v[8:11]\n" \
"v_add_f32 v33,v33,v37\nv_add_f32 v34,v34,v38\nv_mfma_f32_16x16x32_bf16 v[12:15],a[76:79],v[124:127],v[12:15]\nglobal_load_dwordx4 v[96:99],v59,s[16:17] sc1\n" \
"v_add_f32 v35,v35,v39\nv_mfma_f32_16x16x32_bf16 v[12:15],a[108:111],v[192:195],v[12:15]\nbuffer_load_dwordx2 v[120:121],v56,s[36:39],s100 offen sc1\nv_add_f32 v40,v40,v44\n" \
"v_mfma_f32_16x16x32_bf16 v[16:19],a[80:83],v[124:127],v[16:19]\nbuffer_load_dwordx2 v[122:123],v56,s[36:39],s100 offen offset:32 sc1\nv_add_f32 v41,v41,v45\nv_mfma_f32_16x16x32_bf16 v[16:19],a[112:115],v[192:195],v[16:19]\n" \
"v_add_f32 v42,v42,v46\nv_add_f32 v43,v43,v47\nv_mfma_f32_16x16x32_bf16 v[20:23],a[84:87],v[124:127],v[20:23]\nv_add_f32 v32,v32,v40\n" \
"v_add_f32 v33,v33,v41\nv_mfma_f32_16x16x32_bf16 v[20:23],a[116:119],v[192:195],v[20:23]\nv_add_f32 v34,v34,v42\nv_add_f32 v35,v35,v43\n" \
"v_mfma_f32_16x16x32_bf16 v[24:27],a[88:91],v[124:127],v[24:27]\nglobal_load_dwordx4 v[100:103],v59,s[16:17] offset:1024 sc1\nv_cvt_pk_bf16_f32 v36,v32,v33\nv_mfma_f32_16x16x32_bf16 v[24:27],a[120:123],v[192:195],v[24:27]\n" \
"v_cvt_pk_bf16_f32 v37,v34,v35\ns_waitcnt lgkmcnt(0)\nv_mfma_f32_16x16x32_bf16 v[28:31],a[92:95],v[124:127],v[28:31]\nv_add_f32 v48,v48,v240\n" \
"v_add_f32 v49,v49,v241\nv_mfma_f32_16x16x32_bf16 v[28:31],a[124:127],v[192:195],v[28:31]\nv_add_f32 v50,v50,v242\nv_mfma_f32_16x16x32_bf16 v[0:3],a[128:131],v[196:199],v[0:3]\n" \
"v_add_f32 v65,v65,v113\nv_add_f32 v51,v51,v243\nv_mfma_f32_16x16x32_bf16 v[0:3],a[160:163],v[200:203],v[0:3]\nv_lshlrev_b32 v112,16,v61\n" \
"v_add_f32 v244,v244,v248\nv_mfma_f32_16x16x32_bf16 v[4:7],a[132:135],v[196:199],v[4:7]\nglobal_load_dwordx4 v[104:107],v59,s[16:17] offset:2048 sc1\nv_and_b32 v113,0xffff0000,v61\n" \
"v_mfma_f32_16x16x32_bf16 v[4:7],a[164:167],v[200:203],v[4:7]\nv_add_f32 v245,v245,v249\nv_add_f32 v66,v66,v112\nv_mfma_f32_16x16x32_bf16 v[8:11],a[136:139],v[196:199],v[8:11]\n" \
"v_add_f32 v246,v246,v250\nv_add_f32 v67,v67,v113\nv_mfma_f32_16x16x32_bf16 v[8:11],a[168:171],v[200:203],v[8:11]\nv_add_f32 v247,v247,v251\n" \
"v_lshlrev_b32 v112,16,v62\nv_mfma_f32_16x16x32_bf16 v[12:15],a[140:143],v[196:199],v[12:15]\nv_add_f32 v48,v48,v244\nv_and_b32 v113,0xffff0000,v62\n" \
"v_mfma_f32_16x16x32_bf16 v[12:15],a[172:175],v[200:203],v[12:15]\nv_add_f32 v49,v49,v245\nv_add_f32 v68,v68,v112\nv_mfma_f32_16x16x32_bf16 v[16:19],a[144:147],v[196:199],v[16:19]\n" \
"v_add_f32 v50,v50,v246\nv_add_f32 v69,v69,v113\nv_mfma_f32_16x16x32_bf16 v[16:19],a[176:179],v[200:203],v[16:19]\nv_add_f32 v51,v51,v247\n" \
"v_lshlrev_b32 v112,16,v63\nv_mfma_f32_16x16x32_bf16 v[20:23],a[148:151],v[196:199],v[20:23]\nv_cvt_pk_bf16_f32 v38,v48,v49\nv_and_b32 v113,0xffff0000,v63\n" \
"v_mfma_f32_16x16x32_bf16 v[20:23],a[180:183],v[200:203],v[20:23]\nv_cvt_pk_bf16_f32 v39,v50,v51\nv_add_f32 v70,v70,v112\nv_mfma_f32_16x16x32_bf16 v[24:27],a[152:155],v[196:199],v[24:27]\n" \
"buffer_store_dwordx4 v[36:39],v59,s[36:39],s74 offen sc1\nv_add_f32 v71,v71,v113\nv_mfma_f32_16x16x32_bf16 v[24:27],a[184:187],v[200:203],v[24:27]\nv_mfma_f32_16x16x32_bf16 v[28:31],a[156:159],v[196:199],v[28:31]\n" \
"v_mfma_f32_16x16x32_bf16 v[28:31],a[188:191],v[200:203],v[28:31]\ns_waitcnt vmcnt(16)\nv_mfma_f32_16x16x32_bf16 v[0:3],a[192:195],v[204:207],v[0:3]\nv_mfma_f32_16x16x32_bf16 v[4:7],a[196:199],v[204:207],v[4:7]\n" \
"v_mfma_f32_16x16x32_bf16 v[8:11],a[200:203],v[204:207],v[8:11]\nv_mfma_f32_16x16x32_bf16 v[12:15],a[204:207],v[204:207],v[12:15]\nv_mfma_f32_16x16x32_bf16 v[16:19],a[208:211],v[204:207],v[16:19]\nv_mfma_f32_16x16x32_bf16 v[20:23],a[212:215],v[204:207],v[20:23]\n" \
"v_mfma_f32_16x16x32_bf16 v[24:27],a[216:219],v[204:207],v[24:27]\nv_mfma_f32_16x16x32_bf16 v[28:31],a[220:223],v[204:207],v[28:31]\ns_waitcnt vmcnt(13)\nv_mfma_f32_16x16x32_bf16 v[208:211],v[128:131],v[72:75],0\n" \
"v_add_u32 v59,409600,v59\ns_waitcnt vmcnt(11)\nv_mfma_f32_16x16x32_bf16 v[208:211],v[160:163],v[76:79],v[208:211]\nds_write_addtid_b32 v0 offset:32768\n" \
"ds_write_addtid_b32 v1 offset:33024\nv_mfma_f32_16x16x32_bf16 v[212:215],v[132:135],v[72:75],0\nds_write_addtid_b32 v2 offset:33280\nds_write_addtid_b32 v3 offset:33536\n" \
"v_mfma_f32_16x16x32_bf16 v[212:215],v[164:167],v[76:79],v[212:215]\nbuffer_load_dwordx4 v[112:115],v59,s[36:39],s73 offen sc1\nds_write_addtid_b32 v4 offset:33792\nv_mfma_f32_16x16x32_bf16 v[216:219],v[136:139],v[72:75],0\n" \
"ds_write_addtid_b32 v5 offset:34048\nds_write_addtid_b32 v6 offset:34304\nv_mfma_f32_16x16x32_bf16 v[216:219],v[168:171],v[76:79],v[216:219]\nds_write_addtid_b32 v7 offset:34560\n" \
"ds_write_addtid_b32 v8 offset:34816\nv_mfma_f32_16x16x32_bf16 v[220:223],v[140:143],v[72:75],0\nds_write_addtid_b32 v9 offset:35072\nds_write_addtid_b32 v10 offset:35328\n" \
"v_mfma_f32_16x16x32_bf16 v[220:223],v[172:175],v[76:79],v[220:223]\nds_write_addtid_b32 v11 offset:35584\ns_waitcnt vmcnt(11)\nv_mfma_f32_16x16x32_bf16 v[208:211],a[0:3],v[80:83],v[208:211]\n" \
"ds_write_addtid_b32 v12 offset:35840\nds_write_addtid_b32 v13 offset:36096\nv_mfma_f32_16x16x32_bf16 v[208:211],a[32:35],v[84:87],v[208:211]\nds_write_addtid_b32 v14 offset:36352\n" \
"ds_write_addtid_b32 v15 offset:36608\nv_mfma_f32_16x16x32_bf16 v[212:215],a[4:7],v[80:83],v[212:215]\nds_write_addtid_b32 v16 offset:36864\nds_write_addtid_b32 v17 offset:37120\n" \
"v_mfma_f32_16x16x32_bf16 v[212:215],a[36:39],v[84:87],v[212:215]\nds_write_addtid_b32 v18 offset:37376\nds_write_addtid_b32 v19 offset:37632\nv_mfma_f32_16x16x32_bf16 v[216:219],a[8:11],v[80:83],v[216:219]\n" \
"ds_write_addtid_b32 v20 offset:37888\nds_write_addtid_b32 v21 offset:38144\nv_mfma_f32_16x16x32_bf16 v[216:219],a[40:43],v[84:87],v[216:219]\nds_write_addtid_b32 v22 offset:38400\n" \
"ds_write_addtid_b32 v23 offset:38656\nv_mfma_f32_16x16x32_bf16 v[220:223],a[12:15],v[80:83],v[220:223]\nds_write_addtid_b32 v24 offset:38912\nds_write_addtid_b32 v25 offset:39168\n" \
"v_mfma_f32_16x16x32_bf16 v[220:223],a[44:47],v[84:87],v[220:223]\nds_write_addtid_b32 v26 offset:39424\ns_waitcnt vmcnt(9)\nv_mfma_f32_16x16x32_bf16 v[208:211],a[64:67],v[88:91],v[208:211]\n" \
"ds_write_addtid_b32 v27 offset:39680\nds_write_addtid_b32 v28 offset:39936\nv_mfma_f32_16x16x32_bf16 v[208:211],a[96:99],v[92:95],v[208:211]\nds_write_addtid_b32 v29 offset:40192\n" \
"ds_write_addtid_b32 v30 offset:40448\nv_mfma_f32_16x16x32_bf16 v[212:215],a[68:71],v[88:91],v[212:215]\nds_write_addtid_b32 v31 offset:40704\nv_cmp_eq_u32 vcc,-1,v108\n" \
"v_mfma_f32_16x16x32_bf16 v[212:215],a[100:103],v[92:95],v[212:215]\nv_and_b32 v62,0x30,v59\nv_add_u32 v62,s67,v62\nv_mfma_f32_16x16x32_bf16 v[216:219],a[72:75],v[88:91],v[216:219]\n" \
"s_cbranch_vccnz .Lsp2B_%=\n.Lbp2B_%=:\nv_lshlrev_b32 v60,16,v108\nv_mfma_f32_16x16x32_bf16 v[216:219],a[104:107],v[92:95],v[216:219]\n" \
"v_and_b32 v61,0xffff0000,v108\nv_add_f32 v52,v52,v60\nv_mfma_f32_16x16x32_bf16 v[220:223],a[76:79],v[88:91],v[220:223]\nv_add_f32 v53,v53,v61\n" \
"v_lshlrev_b32 v60,16,v109\nv_mfma_f32_16x16x32_bf16 v[220:223],a[108:111],v[92:95],v[220:223]\nv_and_b32 v61,0xffff0000,v109\ns_waitcnt vmcnt(3)\n" \
"v_mfma_f32_16x16x32_bf16 v[208:211],a[128:131],v[96:99],v[208:211]\ns_waitcnt lgkmcnt(0)\ns_barrier\nv_mfma_f32_16x16x32_bf16 v[208:211],a[160:163],v[100:103],v[208:211]\n" \
"ds_read_b128 v[32:35],v58 offset:32768\nds_read_b128 v[36:39],v58 offset:40960\nv_mfma_f32_16x16x32_bf16 v[212:215],a[132:135],v[96:99],v[212:215]\nds_read_b128 v[40:43],v58 offset:49152\n" \
"ds_read_b128 v[44:47],v58 offset:57344\nv_mfma_f32_16x16x32_bf16 v[212:215],a[164:167],v[100:103],v[212:215]\nds_read_b128 v[48:51],v58 offset:36864\nds_read_b128 v[240:243],v58 offset:45056\n" \
"v_mfma_f32_16x16x32_bf16 v[216:219],a[136:139],v[96:99],v[216:219]\nds_read_b128 v[244:247],v58 offset:53248\nds_read_b128 v[248:251],v58 offset:61440\nv_mfma_f32_16x16x32_bf16 v[216:219],a[168:171],v[100:103],v[216:219]\n" \
"v_add_f32 v54,v54,v60\nv_add_f32 v55,v55,v61\nv_mfma_f32_16x16x32_bf16 v[220:223],a[140:143],v[96:99],v[220:223]\nv_lshlrev_b32 v60,16,v110\n" \
"v_and_b32 v61,0xffff0000,v110\nv_mfma_f32_16x16x32_bf16 v[220:223],a[172:175],v[100:103],v[220:223]\nv_add_f32 v252,v252,v60\ns_waitcnt vmcnt(2)\n" \
"v_mfma_f32_16x16x32_bf16 v[208:211],a[192:195],v[104:107],v[208:211]\nv_add_f32 v253,v253,v61\nv_lshlrev_b32 v60,16,v111\nv_mfma_f32_16x16x32_bf16 v[212:215],a[196:199],v[104:107],v[212:215]\n" \
"v_and_b32 v61,0xffff0000,v111\nv_add_f32 v254,v254,v60\nv_mfma_f32_16x16x32_bf16 v[216:219],a[200:203],v[104:107],v[216:219]\ns_waitcnt lgkmcnt(4)\n" \
"v_add_f32 v32,v32,v36\nv_mfma_f32_16x16x32_bf16 v[220:223],a[204:207],v[104:107],v[220:223]\nv_add_f32 v33,v33,v37\nv_add_f32 v34,v34,v38\n" \
"v_mfma_f32_16x16x32_bf16 v[224:227],v[144:147],v[72:75],0\nv_add_f32 v35,v35,v39\nv_add_f32 v40,v40,v44\nv_mfma_f32_16x16x32_bf16 v[224:227],v[176:179],v[76:79],v[224:227]\n" \
"v_add_f32 v41,v41,v45\nv_add_f32 v42,v42,v46\nv_mfma_f32_16x16x32_bf16 v[228:231],v[148:151],v[72:75],0\nds_write_addtid_b32 v208\n" \
"ds_write_addtid_b32 v209 offset:256\nv_mfma_f32_16x16x32_bf16 v[228:231],v[180:183],v[76:79],v[228:231]\nds_write_addtid_b32 v210 offset:512\nds_write_addtid_b32 v211 offset:768\n" \
"v_mfma_f32_16x16x32_bf16 v[232:235],v[152:155],v[72:75],0\nds_write_addtid_b32 v212 offset:1024\nds_write_addtid_b32 v213 offset:1280\nv_mfma_f32_16x16x32_bf16 v[232:235],v[184:187],v[76:79],v[232:235]\n" \
"ds_write_addtid_b32 v214 offset:1536\nds_write_addtid_b32 v215 offset:1792\nv_mfma_f32_16x16x32_bf16 v[236:239],v[156:159],v[72:75],0\nds_write_addtid_b32 v216 offset:2048\n" \
"ds_write_addtid_b32 v217 offset:2304\nv_mfma_f32_16x16x32_bf16 v[236:239],v[188:191],v[76:79],v[236:239]\nds_write_addtid_b32 v218 offset:2560\nds_write_addtid_b32 v219 offset:2816\n" \
"v_mfma_f32_16x16x32_bf16 v[224:227],a[16:19],v[80:83],v[224:227]\nds_write_addtid_b32 v220 offset:3072\nds_write_addtid_b32 v221 offset:3328\nv_mfma_f32_16x16x32_bf16 v[224:227],a[48:51],v[84:87],v[224:227]\n" \
"ds_write_addtid_b32 v222 offset:3584\nds_write_addtid_b32 v223 offset:3840\nv_mfma_f32_16x16x32_bf16 v[228:231],a[20:23],v[80:83],v[228:231]\nv_add_f32 v43,v43,v47\n" \
"v_add_f32 v32,v32,v40\nv_mfma_f32_16x16x32_bf16 v[228:231],a[52:55],v[84:87],v[228:231]\nv_add_f32 v33,v33,v41\nv_add_f32 v34,v34,v42\n" \
"v_mfma_f32_16x16x32_bf16 v[232:235],a[24:27],v[80:83],v[232:235]\nv_add_f32 v255,v255,v61\nv_add_f32 v35,v35,v43\nv_mfma_f32_16x16x32_bf16 v[232:235],a[56:59],v[84:87],v[232:235]\n" \
"s_waitcnt vmcnt(0)\nv_mfma_f32_16x16x32_bf16 v[236:239],a[28:31],v[80:83],v[236:239]\nbuffer_load_dwordx4 v[124:127],v59,s[36:39],s78 offen sc1\nv_cmp_eq_u32 vcc,-1,v112\n" \
"v_mfma_f32_16x16x32_bf16 v[236:239],a[60:63],v[84:87],v[236:239]\ns_waitcnt lgkmcnt(0)\ns_barrier\nv_mfma_f32_16x16x32_bf16 v[224:227],a[80:83],v[88:91],v[224:227]\n" \
"ds_read_b128 v[16:19],v58\nds_read_b128 v[20:23],v58 offset:8192\nv_mfma_f32_16x16x32_bf16 v[224:227],a[112:115],v[92:95],v[224:227]\nds_read_b128 v[24:27],v58 offset:16384\n" \
"ds_read_b128 v[28:31],v58 offset:24576\nv_mfma_f32_16x16x32_bf16 v[228:231],a[84:87],v[88:91],v[228:231]\nv_add_f32 v48,v48,v240\ns_cbranch_vccnz .Lsp3A_%=\n" \
".Lbp3A_%=:\nv_mfma_f32_16x16x32_bf16 v[228:231],a[116:119],v[92:95],v[228:231]\nv_add_f32 v49,v49,v241\nv_lshlrev_b32 v60,16,v112\n" \
"v_mfma_f32_16x16x32_bf16 v[232:235],a[88:91],v[88:91],v[232:235]\nv_add_f32 v50,v50,v242\nv_and_b32 v61,0xffff0000,v112\nv_mfma_f32_16x16x32_bf16 v[232:235],a[120:123],v[92:95],v[232:235]\n" \
"v_add_f32 v51,v51,v243\nv_add_f32 v64,v64,v60\nv_mfma_f32_16x16x32_bf16 v[236:239],a[92:95],v[88:91],v[236:239]\nv_add_f32 v244,v244,v248\n" \
"v_add_f32 v65,v65,v61\nv_mfma_f32_16x16x32_bf16 v[236:239],a[124:127],v[92:95],v[236:239]\nv_add_f32 v245,v245,v249\nv_lshlrev_b32 v60,16,v113\n" \
"v_mfma_f32_16x16x32_bf16 v[224:227],a[144:147],v[96:99],v[224:227]\nv_add_f32 v246,v246,v250\nv_and_b32 v61,0xffff0000,v113\nv_mfma_f32_16x16x32_bf16 v[224:227],a[176:179],v[100:103],v[224:227]\n" \
"v_add_f32 v247,v247,v251\nv_add_f32 v66,v66,v60\nv_mfma_f32_16x16x32_bf16 v[228:231],a[148:151],v[96:99],v[228:231]\nv_add_f32 v48,v48,v244\n" \
"v_add_f32 v67,v67,v61\nv_mfma_f32_16x16x32_bf16 v[228:231],a[180:183],v[100:103],v[228:231]\nv_add_f32 v49,v49,v245\nv_lshlrev_b32 v60,16,v114\n" \
"v_add_f32 v50,v50,v246\nv_mfma_f32_16x16x32_bf16 v[232:235],a[152:155],v[96:99],v[232:235]\nv_and_b32 v61,0xffff0000,v114\nv_add_f32 v51,v51,v247\n" \
"v_add_f32 v68,v68,v60\nv_mfma_f32_16x16x32_bf16 v[232:235],a[184:187],v[100:103],v[232:235]\nv_add_f32 v69,v69,v61\nv_lshlrev_b32 v60,16,v115\n" \
"v_and_b32 v61,0xffff0000,v115\nv_mfma_f32_16x16x32_bf16 v[236:239],a[156:159],v[96:99],v[236:239]\nv_add_f32 v70,v70,v60\nds_read_b128 v[8:11],v62\n" \
"v_mfma_f32_16x16x32_bf16 v[236:239],a[188:191],v[100:103],v[236:239]\nv_add_f32 v71,v71,v61\nds_read_b128 v[12:15],v62 offset:64\nv_mfma_f32_16x16x32_bf16 v[224:227],a[208:211],v[104:107],v[224:227]\n" \
"v_lshlrev_b32 v0,16,v116\nv_and_b32 v1,0xffff0000,v116\nv_mfma_f32_16x16x32_bf16 v[228:231],a[212:215],v[104:107],v[228:231]\nv_lshlrev_b32 v2,16,v117\n" \
"v_and_b32 v3,0xffff0000,v117\nv_mfma_f32_16x16x32_bf16 v[232:235],a[216:219],v[104:107],v[232:235]\nv_lshlrev_b32 v4,16,v120\nv_and_b32 v5,0xffff0000,v120\n" \
"v_mfma_f32_16x16x32_bf16 v[236:239],a[220:223],v[104:107],v[236:239]\nv_lshlrev_b32 v6,16,v121\nv_and_b32 v7,0xffff0000,v121\ns_waitcnt lgkmcnt(0)\n" \
"v_fma_f32 v0,v8,v64,v0\nv_fma_f32 v1,v9,v65,v1\nv_fma_f32 v2,v10,v66,v2\nv_fma_f32 v3,v11,v67,v3\n" \
"v_fma_f32 v4,v8,v68,v4\nv_fma_f32 v5,v9,v69,v5\nv_fma_f32 v6,v10,v70,v6\nv_fma_f32 v7,v11,v71,v7\n" \
"v_fma_f32 v0,v8,v32,v0\nv_fma_f32 v1,v9,v33,v1\nv_fma_f32 v2,v10,v34,v2\nv_fma_f32 v3,v11,v35,v3\n" \
"v_cvt_pk_bf16_f32 v0,v0,v1\nv_cvt_pk_bf16_f32 v1,v2,v3\nbuffer_store_dwordx2 v[0:1],v56,s[44:47],0 offen sc1\nv_fma_f32 v4,v8,v48,v4\n" \
"v_fma_f32 v5,v9,v49,v5\nv_fma_f32 v6,v10,v50,v6\nv_fma_f32 v7,v11,v51,v7\nv_cvt_pk_bf16_f32 v4,v4,v5\n" \
"v_cvt_pk_bf16_f32 v5,v6,v7\nbuffer_store_dwordx2 v[4:5],v56,s[44:47],s35 offen sc1\nv_lshlrev_b32 v64,16,v118\nv_and_b32 v65,0xffff0000,v118\n" \
"v_lshlrev_b32 v66,16,v119\nv_and_b32 v67,0xffff0000,v119\nds_write_addtid_b32 v224 offset:4096\nds_write_addtid_b32 v225 offset:4352\n" \
"ds_write_addtid_b32 v226 offset:4608\nds_write_addtid_b32 v227 offset:4864\nds_write_addtid_b32 v228 offset:5120\nds_write_addtid_b32 v229 offset:5376\n" \
"ds_write_addtid_b32 v230 offset:5632\nds_write_addtid_b32 v231 offset:5888\nds_write_addtid_b32 v232 offset:6144\nds_write_addtid_b32 v233 offset:6400\n" \
"ds_write_addtid_b32 v234 offset:6656\nds_write_addtid_b32 v235 offset:6912\nds_write_addtid_b32 v236 offset:7168\nds_write_addtid_b32 v237 offset:7424\n" \
"ds_write_addtid_b32 v238 offset:7680\nds_write_addtid_b32 v239 offset:7936\nv_lshlrev_b32 v68,16,v122\nv_and_b32 v69,0xffff0000,v122\n" \
"v_lshlrev_b32 v70,16,v123\nv_and_b32 v71,0xffff0000,v123\ns_waitcnt lgkmcnt(15)\nv_pk_add_f32 v[16:17],v[16:17],v[20:21]\n" \
"v_pk_add_f32 v[18:19],v[18:19],v[22:23]\nv_pk_add_f32 v[24:25],v[24:25],v[28:29]\nv_pk_add_f32 v[26:27],v[26:27],v[30:31]\nv_pk_add_f32 v[16:17],v[16:17],v[24:25]\n" \
"v_pk_add_f32 v[18:19],v[18:19],v[26:27]\ns_waitcnt lgkmcnt(0)\ns_barrier\nds_read_b128 v[32:35],v58 offset:4096\n" \
"ds_read_b128 v[36:39],v58 offset:12288\nds_read_b128 v[40:43],v58 offset:20480\nds_read_b128 v[44:47],v58 offset:28672\ns_waitcnt vmcnt(2)\n" \
"v_cmp_eq_u32 vcc,-1,v124\ns_cbranch_vccnz .Lsp3B_%=\n.Lbp3B_%=:\nv_lshlrev_b32 v20,16,v124\n" \
"v_and_b32 v21,0xffff0000,v124\nv_lshlrev_b32 v22,16,v125\nv_and_b32 v23,0xffff0000,v125\nv_lshlrev_b32 v24,16,v126\n" \
"v_and_b32 v25,0xffff0000,v126\nv_lshlrev_b32 v26,16,v127\nv_and_b32 v27,0xffff0000,v127\nv_pk_add_f32 v[52:53],v[52:53],v[20:21]\n" \
"v_pk_add_f32 v[54:55],v[54:55],v[22:23]\nv_pk_add_f32 v[252:253],v[252:253],v[24:25]\nv_pk_add_f32 v[254:255],v[254:255],v[26:27]\nv_pk_fma_f32 v[64:65],v[12:13],v[52:53],v[64:65]\n" \
"v_pk_fma_f32 v[66:67],v[14:15],v[54:55],v[66:67]\nv_pk_fma_f32 v[68:69],v[12:13],v[252:253],v[68:69]\nv_pk_fma_f32 v[70:71],v[14:15],v[254:255],v[70:71]\nv_pk_fma_f32 v[64:65],v[12:13],v[16:17],v[64:65]\n" \
"v_pk_fma_f32 v[66:67],v[14:15],v[18:19],v[66:67]\nv_cvt_pk_bf16_f32 v64,v64,v65\nv_cvt_pk_bf16_f32 v65,v66,v67\nbuffer_store_dwordx2 v[64:65],v56,s[44:47],0 offen offset:32 sc1\n" \
"s_waitcnt lgkmcnt(0)\nv_pk_add_f32 v[32:33],v[32:33],v[36:37]\nv_pk_add_f32 v[34:35],v[34:35],v[38:39]\nv_pk_add_f32 v[40:41],v[40:41],v[44:45]\n" \
"v_pk_add_f32 v[42:43],v[42:43],v[46:47]\nv_pk_add_f32 v[32:33],v[32:33],v[40:41]\nv_pk_add_f32 v[34:35],v[34:35],v[42:43]\nv_pk_fma_f32 v[68:69],v[12:13],v[32:33],v[68:69]\n" \
"v_pk_fma_f32 v[70:71],v[14:15],v[34:35],v[70:71]\nv_cvt_pk_bf16_f32 v68,v68,v69\nv_cvt_pk_bf16_f32 v69,v70,v71\nbuffer_store_dwordx2 v[68:69],v56,s[44:47],s35 offen offset:32 sc1\n" \
"s_branch .Lend_%=\n.LsaF_%=:\ns_memrealtime s[94:95]\ns_waitcnt lgkmcnt(0)\n" \
".LpaF_%=:\ns_sleep 2\nbuffer_load_dword v63,v61,s[36:39],s59 offen sc1\ns_memrealtime s[96:97]\n" \
"s_waitcnt vmcnt(0) lgkmcnt(0)\ns_sub_u32 s96,s96,s94\ns_cmp_gt_u32 s96,0x4c4b40\ns_cbranch_scc1 .LbaF_%=\n" \
"v_cmp_eq_u32 vcc,0,v63\ns_and_b64 vcc,vcc,s[86:87]\ns_cbranch_scc1 .LpaF_%=\ns_branch .LbaF_%=\n" \
".LsbF_%=:\ns_memrealtime s[94:95]\ns_waitcnt lgkmcnt(0)\n.LpbF_%=:\n" \
"s_sleep 2\nbuffer_load_dword v63,v61,s[36:39],s59 offen sc1\ns_memrealtime s[96:97]\ns_waitcnt vmcnt(0) lgkmcnt(0)\n" \
"s_sub_u32 s96,s96,s94\ns_cmp_gt_u32 s96,0x4c4b40\ns_cbranch_scc1 .LbbF_%=\nv_cmp_eq_u32 vcc,0,v63\n" \
"s_and_b64 vcc,vcc,s[86:87]\ns_cbranch_scc1 .LpbF_%=\ns_branch .LbbF_%=\n.LsaS_%=:\n" \
"s_memrealtime s[94:95]\ns_waitcnt lgkmcnt(0)\n.LpaS_%=:\ns_sleep 2\n" \
"buffer_load_dword v63,v61,s[36:39],s59 offen sc1\ns_memrealtime s[96:97]\ns_waitcnt vmcnt(0) lgkmcnt(0)\ns_sub_u32 s96,s96,s94\n" \
"s_cmp_gt_u32 s96,0x4c4b40\ns_cbranch_scc1 .LbaS_%=\nv_cmp_eq_u32 vcc,0,v63\ns_and_b64 vcc,vcc,s[86:87]\n" \
"s_cbranch_scc1 .LpaS_%=\ns_branch .LbaS_%=\n.LsbS_%=:\ns_memrealtime s[94:95]\n" \
"s_waitcnt lgkmcnt(0)\n.LpbS_%=:\ns_sleep 2\nbuffer_load_dword v63,v61,s[36:39],s59 offen sc1\n" \
"s_memrealtime s[96:97]\ns_waitcnt vmcnt(0) lgkmcnt(0)\ns_sub_u32 s96,s96,s94\ns_cmp_gt_u32 s96,0x4c4b40\n" \
"s_cbranch_scc1 .LbbS_%=\nv_cmp_eq_u32 vcc,0,v63\ns_and_b64 vcc,vcc,s[86:87]\ns_cbranch_scc1 .LpbS_%=\n" \
"s_branch .LbbS_%=\n.Lsp3B_%=:\ns_memrealtime s[94:95]\ns_waitcnt lgkmcnt(0)\n" \
".Lpp3B_%=:\ns_sub_u32 s101,s78,0\nbuffer_load_dwordx4 v[124:127],v59,s[36:39],s101 offen sc1\ns_memrealtime s[96:97]\n" \
"s_waitcnt vmcnt(0) lgkmcnt(0)\ns_sub_u32 s96,s96,s94\ns_cmp_gt_u32 s96,0x4c4b40\ns_cbranch_scc1 .Lbp3B_%=\n" \
"v_cmp_eq_u32 vcc,-1,v124\ns_cbranch_vccnz .Lpp3B_%=\ns_branch .Lbp3B_%=\n.Lsg4_%=:\n" \
"s_memrealtime s[94:95]\ns_waitcnt lgkmcnt(0)\n.Lpg4_%=:\ns_sleep 2\n" \
"buffer_load_dword v63,v61,s[36:39],s59 offen sc1\ns_memrealtime s[96:97]\ns_waitcnt vmcnt(0) lgkmcnt(0)\ns_sub_u32 s96,s96,s94\n" \
"s_cmp_gt_u32 s96,0x4c4b40\ns_cbranch_scc1 .Lbg4_%=\nv_cmp_eq_u32 vcc,0,v63\ns_and_b64 vcc,vcc,s[86:87]\n" \
"s_cbranch_scc1 .Lpg4_%=\ns_branch .Lbg4_%=\n.Lse_%=:\ns_memrealtime s[94:95]\n" \
"s_waitcnt lgkmcnt(0)\n.Lpe_%=:\nbuffer_load_dword v64,v69,s[36:39],s79 offen sc1\nbuffer_load_dword v65,v69,s[36:39],s80 offen sc1\n" \
"buffer_load_dword v66,v69,s[36:39],s81 offen sc1\nbuffer_load_dword v67,v69,s[36:39],s82 offen sc1\ns_memrealtime s[96:97]\ns_waitcnt vmcnt(0) lgkmcnt(0)\n" \
"s_sub_u32 s96,s96,s94\ns_cmp_gt_u32 s96,0x4c4b40\ns_cbranch_scc1 .Lbe_%=\nv_max3_u32 v52,v64,v65,v66\n" \
"v_max_u32 v52,v52,v67\nv_cmp_eq_u32 vcc,-1,v52\ns_cbranch_vccnz .Lpe_%=\ns_branch .Lbe_%=\n" \
".Lsp1_%=:\ns_memrealtime s[94:95]\ns_waitcnt lgkmcnt(0)\n.Lpp1_%=:\n" \
"s_sub_u32 s101,s71,409600\nbuffer_load_dwordx4 v[64:67],v59,s[36:39],s101 offen sc1\ns_sub_u32 s101,s76,409600\nbuffer_load_dwordx4 v[52:55],v59,s[36:39],s101 offen sc1\n" \
"s_memrealtime s[96:97]\ns_waitcnt vmcnt(0) lgkmcnt(0)\ns_sub_u32 s96,s96,s94\ns_cmp_gt_u32 s96,0x4c4b40\n" \
"s_cbranch_scc1 .Lbp1_%=\nv_max_u32 v62,v64,v52\nv_cmp_eq_u32 vcc,-1,v62\ns_cbranch_vccnz .Lpp1_%=\n" \
"s_branch .Lbp1_%=\n.Lsp2A_%=:\ns_memrealtime s[94:95]\ns_waitcnt lgkmcnt(0)\n" \
".Lpp2A_%=:\ns_sub_u32 s101,s72,409600\nbuffer_load_dwordx4 v[60:63],v59,s[36:39],s101 offen sc1\ns_memrealtime s[96:97]\n" \
"s_waitcnt vmcnt(0) lgkmcnt(0)\ns_sub_u32 s96,s96,s94\ns_cmp_gt_u32 s96,0x4c4b40\ns_cbranch_scc1 .Lbp2A_%=\n" \
"v_cmp_eq_u32 vcc,-1,v60\ns_cbranch_vccnz .Lpp2A_%=\ns_branch .Lbp2A_%=\n.Lsp2B_%=:\n" \
"s_memrealtime s[94:95]\ns_waitcnt lgkmcnt(0)\n.Lpp2B_%=:\ns_sub_u32 s101,s77,409600\n" \
"buffer_load_dwordx4 v[108:111],v59,s[36:39],s101 offen sc1\ns_memrealtime s[96:97]\ns_waitcnt vmcnt(0) lgkmcnt(0)\ns_sub_u32 s96,s96,s94\n" \
"s_cmp_gt_u32 s96,0x4c4b40\ns_cbranch_scc1 .Lbp2B_%=\nv_cmp_eq_u32 vcc,-1,v108\ns_cbranch_vccnz .Lpp2B_%=\n" \
"s_branch .Lbp2B_%=\n.Lsp3A_%=:\ns_memrealtime s[94:95]\ns_waitcnt lgkmcnt(0)\n" \
".Lpp3A_%=:\ns_sub_u32 s101,s73,0\nbuffer_load_dwordx4 v[112:115],v59,s[36:39],s101 offen sc1\ns_memrealtime s[96:97]\n" \
"s_waitcnt vmcnt(0) lgkmcnt(0)\ns_sub_u32 s96,s96,s94\ns_cmp_gt_u32 s96,0x4c4b40\ns_cbranch_scc1 .Lbp3A_%=\n" \
"v_cmp_eq_u32 vcc,-1,v112\ns_cbranch_vccnz .Lpp3A_%=\ns_branch .Lbp3A_%=\n.Lz0_%=:\n" \
"s_branch .Lz1_%=\n.Lxq5_%=:\ns_mov_b64 exec,1\nbuffer_store_dword v61,off,s[36:39],s54 sc1\n" \
"s_mov_b64 exec,-1\nbuffer_load_dword v63,v61,s[36:39],s75 offen sc1\ns_mov_b32 s75,s59\ns_add_u32 s54,s54,0x10000\n" \
"s_add_u32 s83,s83,819200\ns_branch .Lyq5_%=\n.Lz1_%=:\ns_branch .Lz2_%=\n" \
".Lxt5_%=:\nv_and_b32 v20,0x300,v59\nv_and_b32 v21,0xf0,v59\nv_lshlrev_b32 v20,4,v20\n" \
"s_mov_b32 m0,162176\nv_lshl_or_b32 v20,v21,3,v20\nbuffer_load_dword v20,s[40:43],s101 offen lds\ns_mov_b32 m0,s56\n" \
"s_branch .Lyt5_%=\n.Lz2_%=:\ns_branch .Lz3_%=\n.Lxq4_%=:\n" \
"s_mov_b64 exec,1\nbuffer_store_dword v61,off,s[36:39],s54 sc1\ns_mov_b64 exec,-1\nbuffer_load_dword v63,v61,s[36:39],s75 offen sc1\n" \
"s_mov_b32 s75,s59\ns_add_u32 s54,s54,0x10000\ns_add_u32 s83,s83,819200\ns_branch .Lyq4_%=\n" \
".Lz3_%=:\ns_branch .Lz4_%=\n.Lxt4_%=:\nv_and_b32 v0,0x300,v59\n" \
"v_and_b32 v1,0xf0,v59\nv_lshlrev_b32 v0,4,v0\ns_mov_b32 m0,162176\nv_lshl_or_b32 v0,v1,3,v0\n" \
"buffer_load_dword v0,s[40:43],s101 offen lds\ns_mov_b32 m0,s56\ns_branch .Lyt4_%=\n.Lz4_%=:\n" \
"s_branch .Lzl_%=\n.LsmP_%=:\ns_sleep 2\nbuffer_load_dwordx4 v[48:51],v2,s[36:39],s73 offen sc1\n" \
"buffer_load_dwordx4 v[60:63],v2,s[36:39],s75 offen sc1\nbuffer_load_dwordx4 v[52:55],v2,s[36:39],s74 offen sc1\nbuffer_load_dwordx4 v[64:67],v2,s[36:39],s76 offen sc1\nbuffer_load_dwordx4 v[56:59],v1,s[36:39],s77 offen sc1\n" \
"buffer_load_dwordx4 v[68:71],v1,s[36:39],s78 offen sc1\ns_memrealtime s[96:97]\ns_waitcnt vmcnt(0) lgkmcnt(0)\ns_sub_u32 s96,s96,s30\n" \
"s_cmp_gt_u32 s96,0x4c4b40\ns_cbranch_scc1 .LbmP_%=\nv_cmp_ne_u64 vcc,s[28:29],v[48:49]\nv_cmp_ne_u64 s[84:85],s[28:29],v[52:53]\n" \
"v_cmp_ne_u64 s[82:83],s[28:29],v[56:57]\ns_or_b64 vcc,vcc,s[84:85]\nv_cmp_ne_u64 s[84:85],s[28:29],v[60:61]\ns_or_b64 vcc,vcc,s[82:83]\n" \
"v_cmp_ne_u64 s[82:83],s[28:29],v[64:65]\ns_or_b64 vcc,vcc,s[84:85]\nv_cmp_ne_u64 s[84:85],s[28:29],v[68:69]\ns_or_b64 vcc,vcc,s[82:83]\n" \
"s_or_b64 vcc,vcc,s[84:85]\ns_cbranch_vccnz .LsmP_%=\ns_branch .LbmP_%=\n.LsxP_%=:\n" \
"s_waitcnt lgkmcnt(0)\nv_mul_f32 v74,0.5,v74\nv_pk_add_f32 v[124:125],v[54:55],v[74:75] op_sel_hi:[1,0]\nv_pk_add_f32 v[126:127],v[58:59],1.0 op_sel_hi:[1,0]\n" \
"v_max3_f32 v112,|v50|,|v51|,|v124|\nv_max3_f32 v112,|v125|,|v126|,v112\nv_max_f32 v112,|v127|,v112\ns_nop 1\n" \
"v_max_u32_dpp v112,v112,v112 quad_perm:[1,0,3,2] row_mask:0xf bank_mask:0xf bound_ctrl:1\ns_nop 1\nv_max_u32_dpp v112,v112,v112 quad_perm:[2,3,0,1] row_mask:0xf bank_mask:0xf bound_ctrl:1\ns_nop 1\n" \
"v_max_u32_dpp v112,v112,v112 row_half_mirror row_mask:0xf bank_mask:0xf bound_ctrl:1\ns_nop 1\nv_max_u32_dpp v112,v112,v112 row_mirror row_mask:0xf bank_mask:0xf bound_ctrl:1\ns_nop 0\n" \
"v_readlane_b32 s82,v112,0\nv_readlane_b32 s100,v112,16\nv_readlane_b32 s101,v112,32\nv_readlane_b32 s31,v112,48\n" \
"s_max_u32 s100,s100,s101\ns_max_u32 s82,s82,s31\ns_max_u32 s82,s82,s100\nv_mov_b32 v113,s82\n" \
"ds_write_b32 v251,v113 offset:16\ns_waitcnt lgkmcnt(0)\ns_barrier\nds_read_b128 v[124:127],v114 offset:16\n" \
"s_waitcnt lgkmcnt(0)\nv_max3_u32 v124,v124,v125,v126\nv_max_u32 v124,v124,v127\ns_nop 0\n" \
"v_readfirstlane_b32 s93,v124\ns_nop 0\ns_lshr_b32 s93,s93,23\ns_max_u32 s93,s93,64\n" \
"s_min_u32 s93,s93,254\ns_sub_u32 s82,256,s93\ns_lshl_b32 s82,s82,23\nv_mov_b32 v124,s82\n" \
"v_mul_f32 v74,v74,v124\nv_pk_mul_f32 v[62:63],v[50:51],v[124:125] op_sel_hi:[1,0]\nv_max3_f32 v112,|v62|,|v63|,0\nv_cvt_pk_f16_f32 v62,v62,v63\n" \
"ds_write_b32 v252,v62 offset:62848\nv_pk_fma_f32 v[66:67],v[54:55],v[124:125],v[74:75] op_sel_hi:[1,0,0]\nv_max3_f32 v112,|v66|,|v67|,v112\nv_cvt_pk_f16_f32 v66,v66,v67\n" \
"ds_write_b32 v253,v66 offset:62848\nv_pk_fma_f32 v[70:71],v[58:59],v[124:125],v[124:125] op_sel_hi:[1,0,0]\nv_max3_f32 v112,|v70|,|v71|,v112\nv_cvt_pk_f16_f32 v70,v70,v71\n" \
"ds_write_b32 v254,v70 offset:62848\ns_waitcnt lgkmcnt(0)\ns_barrier\nds_read_b64 v[96:97],v234 offset:62848\n" \
"ds_read_b64 v[98:99],v235 offset:62848\nds_read_b64 v[100:101],v236 offset:62848\nds_read_b64 v[102:103],v237 offset:62848\nds_read_b64 v[104:105],v238 offset:62848\n" \
"ds_read_b64 v[106:107],v239 offset:62848\nds_read_b64 v[108:109],v240 offset:62848\nds_read_b64 v[110:111],v241 offset:62848\ns_waitcnt lgkmcnt(0)\n" \
"s_branch .LbxP_%=\n.LsrP_%=:\ns_sleep 2\nbuffer_load_dwordx4 v[104:107],v1,s[36:39],s7 offen sc1\n" \
"buffer_load_dwordx4 v[108:111],v1,s[36:39],s7 offen offset:1024 sc1\ns_memrealtime s[96:97]\ns_waitcnt vmcnt(0) lgkmcnt(0)\ns_sub_u32 s96,s96,s30\n" \
"s_cmp_gt_u32 s96,0x4c4b40\ns_cbranch_scc1 .LbrP_%=\nv_xor_b32 v112,s29,v105\nv_xor_b32 v113,s29,v107\n" \
"v_xor_b32 v114,s29,v109\nv_xor_b32 v115,s29,v111\nv_or3_b32 v112,v112,v113,v114\nv_xor_b32 v113,s28,v104\n" \
"v_xor_b32 v114,s28,v106\nv_or_b32 v112,v112,v115\nv_xor_b32 v115,s28,v108\nv_lshl_or_b32 v112,v112,8,v113\n" \
"v_xor_b32 v113,s28,v110\nv_or3_b32 v112,v112,v114,v115\nv_or_b32 v112,v112,v113\nv_cmp_ne_u32 vcc,0,v112\n" \
"s_nop 0\ns_cbranch_vccnz .LsrP_%=\ns_branch .LbrP_%=\n.LsnP_%=:\n" \
"s_sleep 2\nbuffer_load_dwordx4 v[80:83],v120,s[36:39],s5 offen sc1\nbuffer_load_dwordx4 v[84:87],v120,s[36:39],s5 offen offset:2304 sc1\nbuffer_load_dwordx4 v[88:91],v120,s[36:39],s6 offen sc1\n" \
"buffer_load_dwordx4 v[92:95],v120,s[36:39],s6 offen offset:2304 sc1\ns_memrealtime s[96:97]\ns_waitcnt vmcnt(0) lgkmcnt(0)\ns_sub_u32 s96,s96,s30\n" \
"s_cmp_gt_u32 s96,0x4c4b40\ns_cbranch_scc1 .LbnP_%=\nv_cmp_ne_u64 vcc,s[28:29],v[80:81]\nv_cmp_ne_u64 s[24:25],s[28:29],v[84:85]\n" \
"v_cmp_ne_u64 s[26:27],s[28:29],v[88:89]\ns_or_b64 vcc,vcc,s[24:25]\nv_cmp_ne_u64 s[24:25],s[28:29],v[92:93]\ns_or_b64 vcc,vcc,s[26:27]\n" \
"s_or_b64 vcc,vcc,s[24:25]\ns_cbranch_vccnz .LsnP_%=\ns_branch .LbnP_%=\n.LsuP_%=:\n" \
"v_and_b32 v125,1,v112\nv_cmpx_eq_u32 vcc,0,v125\nbuffer_load_dwordx4 v[112:115],v3,s[36:39],s16 offen sc1\ns_mov_b64 exec,-1\n" \
"v_and_b32 v125,1,v116\nv_cmpx_eq_u32 vcc,0,v125\nbuffer_load_dwordx4 v[116:119],v3,s[36:39],s17 offen sc1\ns_mov_b64 exec,-1\n" \
"v_and_b32 v125,1,v120\nv_cmpx_eq_u32 vcc,0,v125\nbuffer_load_dwordx4 v[120:123],v124,s[36:39],s18 offen sc1\ns_mov_b64 exec,-1\n" \
"s_memrealtime s[96:97]\ns_waitcnt vmcnt(0) lgkmcnt(0)\ns_sub_u32 s96,s96,s30\ns_cmp_gt_u32 s96,0x4c4b40\n" \
"s_cbranch_scc1 .LbuP_%=\nv_and_b32 v125,v112,v116\nv_bitop3_b32 v125,v125,v120,1 bitop3:0x80\nv_cmp_eq_u32 vcc,0,v125\n" \
"s_nop 0\ns_cbranch_vccnz .LsuP_%=\ns_branch .LbuP_%=\n.LsmN_%=:\n" \
"s_sleep 2\nbuffer_load_dwordx4 v[48:51],v2,s[36:39],s73 offen sc1\nbuffer_load_dwordx4 v[60:63],v2,s[36:39],s75 offen sc1\nbuffer_load_dwordx4 v[52:55],v2,s[36:39],s74 offen sc1\n" \
"buffer_load_dwordx4 v[64:67],v2,s[36:39],s76 offen sc1\nbuffer_load_dwordx4 v[56:59],v1,s[36:39],s77 offen sc1\nbuffer_load_dwordx4 v[68:71],v1,s[36:39],s78 offen sc1\ns_memrealtime s[96:97]\n" \
"s_waitcnt vmcnt(0) lgkmcnt(0)\ns_sub_u32 s96,s96,s30\ns_cmp_gt_u32 s96,0x4c4b40\ns_cbranch_scc1 .LbmN_%=\n" \
"v_cmp_ne_u64 vcc,s[28:29],v[48:49]\nv_cmp_ne_u64 s[84:85],s[28:29],v[52:53]\nv_cmp_ne_u64 s[82:83],s[28:29],v[56:57]\ns_or_b64 vcc,vcc,s[84:85]\n" \
"v_cmp_ne_u64 s[84:85],s[28:29],v[60:61]\ns_or_b64 vcc,vcc,s[82:83]\nv_cmp_ne_u64 s[82:83],s[28:29],v[64:65]\ns_or_b64 vcc,vcc,s[84:85]\n" \
"v_cmp_ne_u64 s[84:85],s[28:29],v[68:69]\ns_or_b64 vcc,vcc,s[82:83]\ns_or_b64 vcc,vcc,s[84:85]\ns_cbranch_vccnz .LsmN_%=\n" \
"s_branch .LbmN_%=\n.LsxN_%=:\ns_waitcnt lgkmcnt(0)\nv_mul_f32 v74,0.5,v74\n" \
"v_pk_add_f32 v[124:125],v[54:55],v[74:75] op_sel_hi:[1,0]\nv_pk_add_f32 v[126:127],v[58:59],1.0 op_sel_hi:[1,0]\nv_max3_f32 v112,|v50|,|v51|,|v124|\nv_max3_f32 v112,|v125|,|v126|,v112\n" \
"v_max_f32 v112,|v127|,v112\ns_nop 1\nv_max_u32_dpp v112,v112,v112 quad_perm:[1,0,3,2] row_mask:0xf bank_mask:0xf bound_ctrl:1\ns_nop 1\n" \
"v_max_u32_dpp v112,v112,v112 quad_perm:[2,3,0,1] row_mask:0xf bank_mask:0xf bound_ctrl:1\ns_nop 1\nv_max_u32_dpp v112,v112,v112 row_half_mirror row_mask:0xf bank_mask:0xf bound_ctrl:1\ns_nop 1\n" \
"v_max_u32_dpp v112,v112,v112 row_mirror row_mask:0xf bank_mask:0xf bound_ctrl:1\ns_nop 0\nv_readlane_b32 s82,v112,0\nv_readlane_b32 s100,v112,16\n" \
"v_readlane_b32 s101,v112,32\nv_readlane_b32 s31,v112,48\ns_max_u32 s100,s100,s101\ns_max_u32 s82,s82,s31\n" \
"s_max_u32 s82,s82,s100\nv_mov_b32 v113,s82\nds_write_b32 v251,v113 offset:16\ns_waitcnt lgkmcnt(0)\n" \
"s_barrier\nds_read_b128 v[124:127],v114 offset:16\ns_waitcnt lgkmcnt(0)\nv_max3_u32 v124,v124,v125,v126\n" \
"v_max_u32 v124,v124,v127\ns_nop 0\nv_readfirstlane_b32 s93,v124\ns_nop 0\n" \
"s_lshr_b32 s93,s93,23\ns_max_u32 s93,s93,64\ns_min_u32 s93,s93,254\ns_sub_u32 s82,256,s93\n" \
"s_lshl_b32 s82,s82,23\nv_mov_b32 v124,s82\nv_mul_f32 v74,v74,v124\nv_pk_mul_f32 v[62:63],v[50:51],v[124:125] op_sel_hi:[1,0]\n" \
"v_max3_f32 v112,|v62|,|v63|,0\nv_cvt_pk_f16_f32 v62,v62,v63\nds_write_b32 v252,v62 offset:62848\nv_pk_fma_f32 v[66:67],v[54:55],v[124:125],v[74:75] op_sel_hi:[1,0,0]\n" \
"v_max3_f32 v112,|v66|,|v67|,v112\nv_cvt_pk_f16_f32 v66,v66,v67\nds_write_b32 v253,v66 offset:62848\nv_pk_fma_f32 v[70:71],v[58:59],v[124:125],v[124:125] op_sel_hi:[1,0,0]\n" \
"v_max3_f32 v112,|v70|,|v71|,v112\nv_cvt_pk_f16_f32 v70,v70,v71\nds_write_b32 v254,v70 offset:62848\ns_waitcnt lgkmcnt(0)\n" \
"s_barrier\nds_read_b64 v[96:97],v234 offset:62848\nds_read_b64 v[98:99],v235 offset:62848\nds_read_b64 v[100:101],v236 offset:62848\n" \
"ds_read_b64 v[102:103],v237 offset:62848\nds_read_b64 v[104:105],v238 offset:62848\nds_read_b64 v[106:107],v239 offset:62848\nds_read_b64 v[108:109],v240 offset:62848\n" \
"ds_read_b64 v[110:111],v241 offset:62848\ns_waitcnt lgkmcnt(0)\ns_branch .LbxN_%=\n.LsrN_%=:\n" \
"s_sleep 2\nbuffer_load_dwordx4 v[104:107],v1,s[36:39],s7 offen sc1\nbuffer_load_dwordx4 v[108:111],v1,s[36:39],s7 offen offset:1024 sc1\ns_memrealtime s[96:97]\n" \
"s_waitcnt vmcnt(0) lgkmcnt(0)\ns_sub_u32 s96,s96,s30\ns_cmp_gt_u32 s96,0x4c4b40\ns_cbranch_scc1 .LbrN_%=\n" \
"v_xor_b32 v112,s29,v105\nv_xor_b32 v113,s29,v107\nv_xor_b32 v114,s29,v109\nv_xor_b32 v115,s29,v111\n" \
"v_or3_b32 v112,v112,v113,v114\nv_xor_b32 v113,s28,v104\nv_xor_b32 v114,s28,v106\nv_or_b32 v112,v112,v115\n" \
"v_xor_b32 v115,s28,v108\nv_lshl_or_b32 v112,v112,8,v113\nv_xor_b32 v113,s28,v110\nv_or3_b32 v112,v112,v114,v115\n" \
"v_or_b32 v112,v112,v113\nv_cmp_ne_u32 vcc,0,v112\ns_nop 0\ns_cbranch_vccnz .LsrN_%=\n" \
"s_branch .LbrN_%=\n.LsnN_%=:\ns_sleep 2\nbuffer_load_dwordx4 v[80:83],v120,s[36:39],s5 offen sc1\n" \
"buffer_load_dwordx4 v[84:87],v120,s[36:39],s5 offen offset:2304 sc1\nbuffer_load_dwordx4 v[88:91],v120,s[36:39],s6 offen sc1\nbuffer_load_dwordx4 v[92:95],v120,s[36:39],s6 offen offset:2304 sc1\ns_memrealtime s[96:97]\n" \
"s_waitcnt vmcnt(0) lgkmcnt(0)\ns_sub_u32 s96,s96,s30\ns_cmp_gt_u32 s96,0x4c4b40\ns_cbranch_scc1 .LbnN_%=\n" \
"v_cmp_ne_u64 vcc,s[28:29],v[80:81]\nv_cmp_ne_u64 s[24:25],s[28:29],v[84:85]\nv_cmp_ne_u64 s[26:27],s[28:29],v[88:89]\ns_or_b64 vcc,vcc,s[24:25]\n" \
"v_cmp_ne_u64 s[24:25],s[28:29],v[92:93]\ns_or_b64 vcc,vcc,s[26:27]\ns_or_b64 vcc,vcc,s[24:25]\ns_cbranch_vccnz .LsnN_%=\n" \
"s_branch .LbnN_%=\n.LsuN_%=:\nv_and_b32 v125,1,v112\nv_cmpx_eq_u32 vcc,0,v125\n" \
"buffer_load_dwordx4 v[112:115],v3,s[36:39],s16 offen sc1\ns_mov_b64 exec,-1\nv_and_b32 v125,1,v116\nv_cmpx_eq_u32 vcc,0,v125\n" \
"buffer_load_dwordx4 v[116:119],v3,s[36:39],s17 offen sc1\ns_mov_b64 exec,-1\nv_and_b32 v125,1,v120\nv_cmpx_eq_u32 vcc,0,v125\n" \
"buffer_load_dwordx4 v[120:123],v124,s[36:39],s18 offen sc1\ns_mov_b64 exec,-1\ns_memrealtime s[96:97]\ns_waitcnt vmcnt(0) lgkmcnt(0)\n" \
"s_sub_u32 s96,s96,s30\ns_cmp_gt_u32 s96,0x4c4b40\ns_cbranch_scc1 .LbuN_%=\nv_and_b32 v125,v112,v116\n" \
"v_bitop3_b32 v125,v125,v120,1 bitop3:0x80\nv_cmp_eq_u32 vcc,0,v125\ns_nop 0\ns_cbranch_vccnz .LsuN_%=\n" \
"s_branch .LbuN_%=\n.LsmC_%=:\ns_sleep 2\nbuffer_load_dwordx4 v[48:51],v2,s[36:39],s73 offen sc1\n" \
"buffer_load_dwordx4 v[60:63],v2,s[36:39],s75 offen sc1\nbuffer_load_dwordx4 v[52:55],v2,s[36:39],s74 offen sc1\nbuffer_load_dwordx4 v[64:67],v2,s[36:39],s76 offen sc1\nbuffer_load_dwordx4 v[56:59],v1,s[36:39],s77 offen sc1\n" \
"buffer_load_dwordx4 v[68:71],v1,s[36:39],s78 offen sc1\ns_memrealtime s[96:97]\ns_waitcnt vmcnt(0) lgkmcnt(0)\ns_sub_u32 s96,s96,s30\n" \
"s_cmp_gt_u32 s96,0x4c4b40\ns_cbranch_scc1 .LbmC_%=\nv_cmp_ne_u64 vcc,s[28:29],v[48:49]\nv_cmp_ne_u64 s[84:85],s[28:29],v[52:53]\n" \
"v_cmp_ne_u64 s[82:83],s[28:29],v[56:57]\ns_or_b64 vcc,vcc,s[84:85]\nv_cmp_ne_u64 s[84:85],s[28:29],v[60:61]\ns_or_b64 vcc,vcc,s[82:83]\n" \
"v_cmp_ne_u64 s[82:83],s[28:29],v[64:65]\ns_or_b64 vcc,vcc,s[84:85]\nv_cmp_ne_u64 s[84:85],s[28:29],v[68:69]\ns_or_b64 vcc,vcc,s[82:83]\n" \
"s_or_b64 vcc,vcc,s[84:85]\ns_cbranch_vccnz .LsmC_%=\ns_branch .LbmC_%=\n.LsxC_%=:\n" \
"s_waitcnt lgkmcnt(0)\nv_mul_f32 v74,0.5,v74\nv_pk_add_f32 v[124:125],v[54:55],v[74:75] op_sel_hi:[1,0]\nv_pk_add_f32 v[126:127],v[58:59],1.0 op_sel_hi:[1,0]\n" \
"v_max3_f32 v112,|v50|,|v51|,|v124|\nv_max3_f32 v112,|v125|,|v126|,v112\nv_max_f32 v112,|v127|,v112\ns_nop 1\n" \
"v_max_u32_dpp v112,v112,v112 quad_perm:[1,0,3,2] row_mask:0xf bank_mask:0xf bound_ctrl:1\ns_nop 1\nv_max_u32_dpp v112,v112,v112 quad_perm:[2,3,0,1] row_mask:0xf bank_mask:0xf bound_ctrl:1\ns_nop 1\n" \
"v_max_u32_dpp v112,v112,v112 row_half_mirror row_mask:0xf bank_mask:0xf bound_ctrl:1\ns_nop 1\nv_max_u32_dpp v112,v112,v112 row_mirror row_mask:0xf bank_mask:0xf bound_ctrl:1\ns_nop 0\n" \
"v_readlane_b32 s82,v112,0\nv_readlane_b32 s100,v112,16\nv_readlane_b32 s101,v112,32\nv_readlane_b32 s31,v112,48\n" \
"s_max_u32 s100,s100,s101\ns_max_u32 s82,s82,s31\ns_max_u32 s82,s82,s100\nv_mov_b32 v113,s82\n" \
"ds_write_b32 v251,v113 offset:16\ns_waitcnt lgkmcnt(0)\ns_barrier\nds_read_b128 v[124:127],v114 offset:16\n" \
"s_waitcnt lgkmcnt(0)\nv_max3_u32 v124,v124,v125,v126\nv_max_u32 v124,v124,v127\ns_nop 0\n" \
"v_readfirstlane_b32 s93,v124\ns_nop 0\ns_lshr_b32 s93,s93,23\ns_max_u32 s93,s93,64\n" \
"s_min_u32 s93,s93,254\ns_sub_u32 s82,256,s93\ns_lshl_b32 s82,s82,23\nv_mov_b32 v124,s82\n" \
"v_mul_f32 v74,v74,v124\nv_pk_mul_f32 v[62:63],v[50:51],v[124:125] op_sel_hi:[1,0]\nv_max3_f32 v112,|v62|,|v63|,0\nv_cvt_pk_f16_f32 v62,v62,v63\n" \
"ds_write_b32 v252,v62 offset:62848\nv_pk_fma_f32 v[66:67],v[54:55],v[124:125],v[74:75] op_sel_hi:[1,0,0]\nv_max3_f32 v112,|v66|,|v67|,v112\nv_cvt_pk_f16_f32 v66,v66,v67\n" \
"ds_write_b32 v253,v66 offset:62848\nv_pk_fma_f32 v[70:71],v[58:59],v[124:125],v[124:125] op_sel_hi:[1,0,0]\nv_max3_f32 v112,|v70|,|v71|,v112\nv_cvt_pk_f16_f32 v70,v70,v71\n" \
"ds_write_b32 v254,v70 offset:62848\ns_waitcnt lgkmcnt(0)\ns_barrier\nds_read_b64 v[96:97],v234 offset:62848\n" \
"ds_read_b64 v[98:99],v235 offset:62848\nds_read_b64 v[100:101],v236 offset:62848\nds_read_b64 v[102:103],v237 offset:62848\nds_read_b64 v[104:105],v238 offset:62848\n" \
"ds_read_b64 v[106:107],v239 offset:62848\nds_read_b64 v[108:109],v240 offset:62848\nds_read_b64 v[110:111],v241 offset:62848\ns_waitcnt lgkmcnt(0)\n" \
"s_branch .LbxC_%=\n.Lsw_%=:\nbuffer_store_dwordx4 v[80:83],v3,s[36:39],s73 offen sc1\nbuffer_store_dwordx4 v[84:87],v3,s[36:39],s74 offen sc1\n" \
"buffer_store_dwordx4 v[88:91],v113,s[36:39],s75 offen sc1\n.Lpw_%=:\nbuffer_load_dwordx4 v[104:107],v1,s[36:39],s7 offen sc1\nbuffer_load_dwordx4 v[108:111],v1,s[36:39],s7 offen offset:1024 sc1\n" \
"s_memrealtime s[96:97]\ns_waitcnt vmcnt(0) lgkmcnt(0)\ns_sub_u32 s96,s96,s30\ns_cmp_gt_u32 s96,0x4c4b40\n" \
"s_cbranch_scc1 .Lqw_%=\nv_xor_b32 v116,s29,v105\nv_xor_b32 v117,s29,v107\nv_xor_b32 v118,s29,v109\n" \
"v_xor_b32 v119,s29,v111\nv_or3_b32 v116,v116,v117,v118\nv_xor_b32 v117,s28,v104\nv_xor_b32 v118,s28,v106\n" \
"v_or_b32 v116,v116,v119\nv_xor_b32 v119,s28,v108\nv_lshl_or_b32 v116,v116,8,v117\nv_xor_b32 v117,s28,v110\n" \
"v_or3_b32 v116,v116,v118,v119\nv_or_b32 v116,v116,v117\nv_cmp_ne_u32 vcc,0,v116\ns_nop 0\n" \
"s_cbranch_vccnz .Lpw_%=\n.Lqw_%=:\ns_add_u32 m0,s88,16384\ns_nop 0\n" \
"buffer_load_dwordx4 v124,s[36:39],s66 offen sc1 lds\ns_add_u32 m0,s88,20480\ns_nop 0\nbuffer_load_dwordx4 v125,s[36:39],s67 offen sc1 lds\n" \
"s_add_u32 m0,s88,24576\ns_nop 0\nbuffer_load_dwordx4 v124,s[36:39],s68 offen sc1 lds\ns_add_u32 m0,s88,28672\n" \
"s_nop 0\nbuffer_load_dwordx4 v125,s[36:39],s69 offen sc1 lds\ns_waitcnt vmcnt(0)\ns_branch .Lbw_%=\n" \
".Lsz_%=:\nv_subrev_u32 v124,16384,v80\nv_lshrrev_b32 v124,6,v124\nv_mul_u32_u24 v125,9344,v124\n" \
"v_lshl_add_u32 v124,v84,2,v125\nv_sub_u32 v125,2382780,v124\ns_mov_b32 s79,2925632\nbuffer_load_dword v64,v124,s[36:39],s79 offen sc1\n" \
"v_subrev_u32 v126,0,v125\ns_mov_b32 s79,2925632\nbuffer_load_dword v72,v126,s[36:39],s79 offen sc1\ns_mov_b32 s79,3075136\n" \
"buffer_load_dword v65,v124,s[36:39],s79 offen sc1\nv_subrev_u32 v126,149504,v125\ns_mov_b32 s79,2925632\nbuffer_load_dword v73,v126,s[36:39],s79 offen sc1\n" \
"s_mov_b32 s79,3224640\nbuffer_load_dword v66,v124,s[36:39],s79 offen sc1\nv_subrev_u32 v126,299008,v125\ns_mov_b32 s79,2925632\n" \
"buffer_load_dword v74,v126,s[36:39],s79 offen sc1\ns_mov_b32 s79,3374144\nbuffer_load_dword v67,v124,s[36:39],s79 offen sc1\nv_subrev_u32 v126,448512,v125\n" \
"s_mov_b32 s79,2925632\nbuffer_load_dword v75,v126,s[36:39],s79 offen sc1\ns_mov_b32 s79,2944320\nbuffer_load_dword v68,v124,s[36:39],s79 offen sc1\n" \
"v_subrev_u32 v126,18688,v125\ns_mov_b32 s79,2925632\nbuffer_load_dword v76,v126,s[36:39],s79 offen sc1\ns_mov_b32 s79,3093824\n" \
"buffer_load_dword v69,v124,s[36:39],s79 offen sc1\nv_subrev_u32 v126,168192,v125\ns_mov_b32 s79,2925632\nbuffer_load_dword v77,v126,s[36:39],s79 offen sc1\n" \
"s_mov_b32 s79,3243328\nbuffer_load_dword v70,v124,s[36:39],s79 offen sc1\nv_subrev_u32 v126,317696,v125\ns_mov_b32 s79,2925632\n" \
"buffer_load_dword v78,v126,s[36:39],s79 offen sc1\ns_mov_b32 s79,3392832\nbuffer_load_dword v71,v124,s[36:39],s79 offen sc1\nv_subrev_u32 v126,467200,v125\n" \
"s_mov_b32 s79,2925632\nbuffer_load_dword v79,v126,s[36:39],s79 offen sc1\ns_waitcnt vmcnt(0)\nv_lshlrev_b32 v124,16,v72\n" \
"v_and_b32 v125,0xffff0000,v72\nv_add_f32 v16,v16,v124\nv_sub_f32 v20,v20,v125\nv_lshlrev_b32 v124,16,v64\n" \
"v_and_b32 v125,0xffff0000,v64\nv_add_f32 v32,v32,v124\nv_sub_f32 v36,v36,v125\nv_lshlrev_b32 v124,16,v73\n" \
"v_and_b32 v125,0xffff0000,v73\nv_add_f32 v17,v17,v124\nv_sub_f32 v21,v21,v125\nv_lshlrev_b32 v124,16,v65\n" \
"v_and_b32 v125,0xffff0000,v65\nv_add_f32 v33,v33,v124\nv_sub_f32 v37,v37,v125\nv_lshlrev_b32 v124,16,v74\n" \
"v_and_b32 v125,0xffff0000,v74\nv_add_f32 v18,v18,v124\nv_sub_f32 v22,v22,v125\nv_lshlrev_b32 v124,16,v66\n" \
"v_and_b32 v125,0xffff0000,v66\nv_add_f32 v34,v34,v124\nv_sub_f32 v38,v38,v125\nv_lshlrev_b32 v124,16,v75\n" \
"v_and_b32 v125,0xffff0000,v75\nv_add_f32 v19,v19,v124\nv_sub_f32 v23,v23,v125\nv_lshlrev_b32 v124,16,v67\n" \
"v_and_b32 v125,0xffff0000,v67\nv_add_f32 v35,v35,v124\nv_sub_f32 v39,v39,v125\nv_lshlrev_b32 v124,16,v76\n" \
"v_and_b32 v125,0xffff0000,v76\nv_add_f32 v40,v40,v124\nv_sub_f32 v44,v44,v125\nv_lshlrev_b32 v124,16,v68\n" \
"v_and_b32 v125,0xffff0000,v68\nv_add_f32 v56,v56,v124\nv_sub_f32 v60,v60,v125\nv_lshlrev_b32 v124,16,v77\n" \
"v_and_b32 v125,0xffff0000,v77\nv_add_f32 v41,v41,v124\nv_sub_f32 v45,v45,v125\nv_lshlrev_b32 v124,16,v69\n" \
"v_and_b32 v125,0xffff0000,v69\nv_add_f32 v57,v57,v124\nv_sub_f32 v61,v61,v125\nv_lshlrev_b32 v124,16,v78\n" \
"v_and_b32 v125,0xffff0000,v78\nv_add_f32 v42,v42,v124\nv_sub_f32 v46,v46,v125\nv_lshlrev_b32 v124,16,v70\n" \
"v_and_b32 v125,0xffff0000,v70\nv_add_f32 v58,v58,v124\nv_sub_f32 v62,v62,v125\nv_lshlrev_b32 v124,16,v79\n" \
"v_and_b32 v125,0xffff0000,v79\nv_add_f32 v43,v43,v124\nv_sub_f32 v47,v47,v125\nv_lshlrev_b32 v124,16,v71\n" \
"v_and_b32 v125,0xffff0000,v71\nv_add_f32 v59,v59,v124\nv_sub_f32 v63,v63,v125\ns_branch .Lbz_%=\n" \
".Lst_%=:\ns_sleep 1\nbuffer_load_dword v96,v120,s[36:39],s80 offen sc1\nbuffer_load_dword v97,v121,s[36:39],s80 offen sc1\n" \
"buffer_load_dword v98,v122,s[36:39],s80 offen sc1\nbuffer_load_dword v99,v123,s[36:39],s80 offen sc1\nbuffer_load_dword v100,v120,s[36:39],s81 offen sc1\nbuffer_load_dword v101,v121,s[36:39],s81 offen sc1\n" \
"buffer_load_dword v102,v122,s[36:39],s81 offen sc1\nbuffer_load_dword v103,v123,s[36:39],s81 offen sc1\nbuffer_load_dword v104,v120,s[36:39],s82 offen sc1\nbuffer_load_dword v105,v121,s[36:39],s82 offen sc1\n" \
"buffer_load_dword v106,v122,s[36:39],s82 offen sc1\nbuffer_load_dword v107,v123,s[36:39],s82 offen sc1\nbuffer_load_dword v108,v120,s[36:39],s83 offen sc1\nbuffer_load_dword v109,v121,s[36:39],s83 offen sc1\n" \
"buffer_load_dword v110,v122,s[36:39],s83 offen sc1\nbuffer_load_dword v111,v123,s[36:39],s83 offen sc1\ns_memrealtime s[96:97]\ns_waitcnt vmcnt(0) lgkmcnt(0)\n" \
"s_sub_u32 s96,s96,s30\ns_cmp_gt_u32 s96,0x4c4b40\ns_cbranch_scc1 .Lbt_%=\nv_or3_b32 v124,v96,v97,v98\n" \
"v_or3_b32 v124,v124,v99,v100\nv_or3_b32 v124,v124,v101,v102\nv_or3_b32 v124,v124,v103,v104\nv_or3_b32 v124,v124,v105,v106\n" \
"v_or3_b32 v124,v124,v107,v108\nv_or3_b32 v124,v124,v109,v110\nv_or_b32 v124,v124,v111\nv_and_b32 v124,1,v124\n" \
"v_cmp_ne_u32 vcc,0,v124\ns_nop 0\ns_and_b64 vcc,vcc,s[78:79]\ns_cbranch_vccnz .Lst_%=\n" \
"s_branch .Lbt_%=\n.LsnC_%=:\ns_sleep 2\nbuffer_load_dwordx4 v[16:19],v32,s[36:39],s5 offen sc1\n" \
"buffer_load_dwordx4 v[20:23],v32,s[36:39],s5 offen offset:2304 sc1\nbuffer_load_dwordx4 v[24:27],v32,s[36:39],s6 offen sc1\nbuffer_load_dwordx4 v[28:31],v32,s[36:39],s6 offen offset:2304 sc1\ns_memrealtime s[96:97]\n" \
"s_waitcnt vmcnt(0) lgkmcnt(0)\ns_sub_u32 s96,s96,s30\ns_cmp_gt_u32 s96,0x4c4b40\ns_cbranch_scc1 .LbnC_%=\n" \
"v_cmp_ne_u64 vcc,s[28:29],v[16:17]\nv_cmp_ne_u64 s[24:25],s[28:29],v[20:21]\nv_cmp_ne_u64 s[26:27],s[28:29],v[24:25]\ns_or_b64 vcc,vcc,s[24:25]\n" \
"v_cmp_ne_u64 s[24:25],s[28:29],v[28:29]\ns_or_b64 vcc,vcc,s[26:27]\ns_or_b64 vcc,vcc,s[24:25]\ns_cbranch_vccnz .LsnC_%=\n" \
"s_branch .LbnC_%=\n.LsuC_%=:\nv_and_b32 v125,1,v112\nv_cmpx_eq_u32 vcc,0,v125\n" \
"buffer_load_dwordx4 v[112:115],v3,s[36:39],s16 offen sc1\ns_mov_b64 exec,-1\nv_and_b32 v125,1,v116\nv_cmpx_eq_u32 vcc,0,v125\n" \
"buffer_load_dwordx4 v[116:119],v3,s[36:39],s17 offen sc1\ns_mov_b64 exec,-1\nv_and_b32 v125,1,v120\nv_cmpx_eq_u32 vcc,0,v125\n" \
"buffer_load_dwordx4 v[120:123],v124,s[36:39],s18 offen sc1\ns_mov_b64 exec,-1\ns_memrealtime s[96:97]\ns_waitcnt vmcnt(0) lgkmcnt(0)\n" \
"s_sub_u32 s96,s96,s30\ns_cmp_gt_u32 s96,0x4c4b40\ns_cbranch_scc1 .LbuC_%=\nv_and_b32 v125,v112,v116\n" \
"v_bitop3_b32 v125,v125,v120,1 bitop3:0x80\nv_cmp_eq_u32 vcc,0,v125\ns_nop 0\ns_cbranch_vccnz .LsuC_%=\n" \
"s_branch .LbuC_%=\n.Lend_%=:\n"
#define ASM_CLOB "v1", "v2", "v3", "v4", "v5", "v6", "v7", "v8", "v9", "v10", "v11", "v12", "v13", "v14", "v15", "v16", "v17", "v18", "v19", "v20", "v21", "v22", "v23", "v24", "v25", "v26", "v27", "v28", "v29", "v30", "v31", "v32", "v33", "v34", "v35", "v36", "v37", "v38", "v39", "v40", "v41", "v42", "v43", "v44", "v45", "v46", "v47", "v48", "v49", "v50", "v51", "v52", "v53", "v54", "v55", "v56", "v57", "v58", "v59", "v60", "v61", "v62", "v63", "v64", "v65", "v66", "v67", "v68", "v69", "v70", "v71", "v72", "v73", "v74", "v75", "v76", "v77", "v78", "v79", "v80", "v81", "v82", "v83", "v84", "v85", "v86", "v87", "v88", "v89", "v90", "v91", "v92", "v93", "v94", "v95", "v96", "v97", "v98", "v99", "v100", "v101", "v102", "v103", "v104", "v105", "v106", "v107", "v108", "v109", "v110", "v111", "v112", "v113", "v114", "v115", "v116", "v117", "v118", "v119", "v120", "v121", "v122", "v123", "v124", "v125", "v126", "v127", "v128", "v129", "v130", "v131", "v132", "v133", "v134", "v135", "v136", "v137", "v138", "v139", "v140", "v141", "v142", "v143", "v144", "v145", "v146", "v147", "v148", "v149", "v150", "v151", "v152", "v153", "v154", "v155", "v156", "v157", "v158", "v159", "v160", "v161", "v162", "v163", "v164", "v165", "v166", "v167", "v168", "v169", "v170", "v171", "v172", "v173", "v174", "v175", "v176", "v177", "v178", "v179", "v180", "v181", "v182", "v183", "v184", "v185", "v186", "v187", "v188", "v189", "v190", "v191", "v192", "v193", "v194", "v195", "v196", "v197", "v198", "v199", "v200", "v201", "v202", "v203", "v204", "v205", "v206", "v207", "v208", "v209", "v210", "v211", "v212", "v213", "v214", "v215", "v216", "v217", "v218", "v219", "v220", "v221", "v222", "v223", "v224", "v225", "v226", "v227", "v228", "v229", "v230", "v231", "v232", "v233", "v234", "v235", "v236", "v237", "v238", "v239", "v240", "v241", "v242", "v243", "v244", "v245", "v246", "v247", "v248", "v249", "v250", "v251", "v252", "v253", "v254", "v255", "a0", "a1", "a2", "a3", "a4", "a5", "a6", "a7", "a8", "a9", "a10", "a11", "a12", "a13", "a14", "a15", "a16", "a17", "a18", "a19", "a20", "a21", "a22", "a23", "a24", "a25", "a26", "a27", "a28", "a29", "a30", "a31", "a32", "a33", "a34", "a35", "a36", "a37", "a38", "a39", "a40", "a41", "a42", "a43", "a44", "a45", "a46", "a47", "a48", "a49", "a50", "a51", "a52", "a53", "a54", "a55", "a56", "a57", "a58", "a59", "a60", "a61", "a62", "a63", "a64", "a65", "a66", "a67", "a68", "a69", "a70", "a71", "a72", "a73", "a74", "a75", "a76", "a77", "a78", "a79", "a80", "a81", "a82", "a83", "a84", "a85", "a86", "a87", "a88", "a89", "a90", "a91", "a92", "a93", "a94", "a95", "a96", "a97", "a98", "a99", "a100", "a101", "a102", "a103", "a104", "a105", "a106", "a107", "a108", "a109", "a110", "a111", "a112", "a113", "a114", "a115", "a116", "a117", "a118", "a119", "a120", "a121", "a122", "a123", "a124", "a125", "a126", "a127", "a128", "a129", "a130", "a131", "a132", "a133", "a134", "a135", "a136", "a137", "a138", "a139", "a140", "a141", "a142", "a143", "a144", "a145", "a146", "a147", "a148", "a149", "a150", "a151", "a152", "a153", "a154", "a155", "a156", "a157", "a158", "a159", "a160", "a161", "a162", "a163", "a164", "a165", "a166", "a167", "a168", "a169", "a170", "a171", "a172", "a173", "a174", "a175", "a176", "a177", "a178", "a179", "a180", "a181", "a182", "a183", "a184", "a185", "a186", "a187", "a188", "a189", "a190", "a191", "a192", "a193", "a194", "a195", "a196", "a197", "a198", "a199", "a200", "a201", "a202", "a203", "a204", "a205", "a206", "a207", "a208", "a209", "a210", "a211", "a212", "a213", "a214", "a215", "a216", "a217", "a218", "a219", "a220", "a221", "a222", "a223", "a224", "a225", "a226", "a227", "a228", "a229", "a230", "a231", "a232", "a233", "a234", "a235", "a236", "a237", "a238", "a239", "a240", "a241", "a242", "a243", "a244", "a245", "a246", "a247", "a248", "a249", "a250", "a251", "a252", "a253", "a254", "a255", "s3", "s6", "s7", "s8", "s9", "s10", "s11", "s12", "s13", "s14", "s15", "s16", "s17", "s18", "s19", "s20", "s21", "s22", "s23", "s24", "s25", "s26", "s27", "s28", "s29", "s30", "s31", "s32", "s33", "s34", "s35", "s36", "s37", "s38", "s39", "s40", "s41", "s42", "s43", "s44", "s45", "s46", "s47", "s48", "s49", "s50", "s51", "s52", "s53", "s54", "s55", "s56", "s57", "s58", "s59", "s60", "s61", "s62", "s63", "s64", "s65", "s66", "s67", "s68", "s69", "s70", "s71", "s72", "s73", "s74", "s75", "s76", "s77", "s78", "s79", "s80", "s81", "s82", "s83", "s84", "s85", "s86", "s87", "s88", "s89", "s90", "s91", "s92", "s93", "s94", "s95", "s96", "s97", "s98", "s99", "s100", "s101"
extern "C" __global__ __launch_bounds__(256, 1) void adm(
    const u16* x, const u16* wr, const u16* wi, const u16* w1, const u16* cvec, const u16* wmod, char* wsb, const u16* b1, const u16* b2, const u16* w2, u16* outp) {
  unsigned long long ka = (unsigned long long)__builtin_amdgcn_kernarg_segment_ptr(), tab = (unsigned long long)TAB;
  unsigned bid = blockIdx.x, tid = threadIdx.x;
  asm volatile(ASM_ALL : "+{s[0:1]}"(ka), "+{s2}"(bid), "+{s[4:5]}"(tab), "+{v0}"(tid) : : "vcc", "scc", "memory", ASM_CLOB);
}

extern "C" void kernel_launch(void* const* d_in, const int* in_sizes, int n_in, void* d_out, int out_size, void* d_ws,
                              size_t ws_size, hipStream_t stream) {
  (void)hipFuncSetAttribute((const void*)adm, hipFuncAttributeMaxDynamicSharedMemorySize, 163840);
  adm<<<256, 256, 163840, stream>>>((const u16*)d_in[0], (const u16*)d_in[1], (const u16*)d_in[2], (const u16*)d_in[4], (const u16*)d_in[8], (const u16*)d_in[9],
                                    (char*)d_ws, (const u16*)d_in[5], (const u16*)d_in[7], (const u16*)d_in[6], (u16*)d_out);
}
```

```cpp
#include <hip/hip_runtime.h>
typedef unsigned short u16;
static __device__ const unsigned __attribute__((aligned(128))) TAB[5632] = {
13312,13312,13312,13312,13312,0x2e1f3364,0x31a831a8,0x33642e1f,13312,0x31a831a8,0x34000000,0x31a8b1a8,13312,0x33642e1f,0x31a8b1a8,0xae1fb364,
13312,0x34000000,46080,0xb4000000,13312,0x3364ae1f,0xb1a8b1a8,0xae1f3364,13312,0x31a8b1a8,0xb4000000,0x31a831a8,13312,0x2e1fb364,0xb1a831a8,0x3364ae1f,
13312,46080,13312,46080,13312,0xae1fb364,0x31a831a8,0xb364ae1f,13312,0xb1a8b1a8,0x34000000,0xb1a831a8,13312,0xb364ae1f,0x31a8b1a8,0x2e1f3364,
13312,0xb4000000,46080,0x34000000,13312,0xb3642e1f,0xb1a8b1a8,0x2e1fb364,13312,0xb1a831a8,0xb4000000,0xb1a8b1a8,13312,0xae1f3364,0xb1a831a8,0xb3642e1f,
13312,13312,13312,13312,0x34000000,0x3364ae1f,0x31a8b1a8,0x2e1fb364,46080,0xb1a8b1a8,0xb4000000,0xb1a831a8,0xb4000000,0xae1f3364,0x31a831a8,0x3364ae1f,
13312,0x34000000,46080,0xb4000000,0x34000000,0xae1fb364,0xb1a831a8,0x33642e1f,46080,0xb1a831a8,0x34000000,0xb1a8b1a8,0xb4000000,0x33642e1f,0xb1a8b1a8,0x2e1f3364,
13312,46080,13312,46080,0x34000000,0xb3642e1f,0x31a8b1a8,0xae1f3364,46080,0x31a831a8,0xb4000000,0x31a8b1a8,0xb4000000,0x2e1fb364,0x31a831a8,0xb3642e1f,
13312,0xb4000000,46080,0x34000000,0x34000000,0x2e1f3364,0xb1a831a8,0xb364ae1f,46080,0x31a8b1a8,0x34000000,0x31a831a8,0xb4000000,0xb364ae1f,0xb1a8b1a8,0xae1fb364,
13312,13312,13312,13312,46080,0xae1fb364,0xb1a8b1a8,0xb364ae1f,13312,0x31a831a8,0x34000000,0x31a8b1a8,46080,0xb364ae1f,0xb1a831a8,0x2e1f3364,
13312,0x34000000,46080,0xb4000000,46080,0xb3642e1f,0x31a831a8,0x2e1fb364,13312,0x31a8b1a8,0xb4000000,0x31a831a8,46080,0xae1f3364,0x31a8b1a8,0xb3642e1f,
13312,46080,13312,46080,46080,0x2e1f3364,0xb1a8b1a8,0x33642e1f,13312,0xb1a8b1a8,0x34000000,0xb1a831a8,46080,0x33642e1f,0xb1a831a8,0xae1fb364,
13312,0xb4000000,46080,0x34000000,46080,0x3364ae1f,0x31a831a8,0xae1f3364,13312,0xb1a831a8,0xb4000000,0xb1a8b1a8,46080,0x2e1fb364,0x31a8b1a8,0x3364ae1f,
13312,13312,13312,13312,0xb4000000,0xb3642e1f,0xb1a831a8,0xae1f3364,46080,0xb1a8b1a8,0xb4000000,0xb1a831a8,0x34000000,0x2e1fb364,0xb1a8b1a8,0xb3642e1f,
13312,0x34000000,46080,0xb4000000,0xb4000000,0x2e1f3364,0x31a8b1a8,0xb364ae1f,46080,0xb1a831a8,0x34000000,0xb1a8b1a8,0x34000000,0xb364ae1f,0x31a831a8,0xae1fb364,
13312,46080,13312,46080,0xb4000000,0x3364ae1f,0xb1a831a8,0x2e1fb364,46080,0x31a831a8,0xb4000000,0x31a8b1a8,0x34000000,0xae1f3364,0xb1a8b1a8,0x3364ae1f,
13312,0xb4000000,46080,0x34000000,0xb4000000,0xae1fb364,0x31a8b1a8,0x33642e1f,46080,0x31a8b1a8,0x34000000,0x31a831a8,0x34000000,0x33642e1f,0x31a831a8,0x2e1f3364,
0x34000000,0x34000000,0x34000000,0x34000000,0x34000000,0x3364ae1f,0x31a8b1a8,0x2e1fb364,0x34000000,0x31a8b1a8,46080,0xb1a8b1a8,0x34000000,0x2e1fb364,0xb1a8b1a8,0xb3642e1f,
0x34000000,46080,0xb4000000,13312,0x34000000,0xae1fb364,0xb1a831a8,0x33642e1f,0x34000000,0xb1a8b1a8,13312,0x31a8b1a8,0x34000000,0xb364ae1f,0x31a831a8,0xae1fb364,
0x34000000,0xb4000000,0x34000000,0xb4000000,0x34000000,0xb3642e1f,0x31a8b1a8,0xae1f3364,0x34000000,0xb1a831a8,46080,0x31a831a8,0x34000000,0xae1f3364,0xb1a8b1a8,0x3364ae1f,
0x34000000,13312,0xb4000000,46080,0x34000000,0x2e1f3364,0xb1a831a8,0xb364ae1f,0x34000000,0x31a831a8,13312,0xb1a831a8,0x34000000,0x33642e1f,0x31a831a8,0x2e1f3364,
0x34000000,0x34000000,0x34000000,0x34000000,46080,0xae1fb364,0xb1a8b1a8,0xb364ae1f,0xb4000000,0xb1a831a8,13312,0x31a831a8,13312,0x33642e1f,0x31a8b1a8,0xae1fb364,
0x34000000,46080,0xb4000000,13312,46080,0xb3642e1f,0x31a831a8,0x2e1fb364,0xb4000000,0x31a831a8,46080,0xb1a831a8,13312,0x2e1fb364,0xb1a831a8,0x3364ae1f,
0x34000000,0xb4000000,0x34000000,0xb4000000,46080,0x2e1f3364,0xb1a8b1a8,0x33642e1f,0xb4000000,0x31a8b1a8,13312,0xb1a8b1a8,13312,0xb364ae1f,0x31a8b1a8,0x2e1f3364,
0x34000000,13312,0xb4000000,46080,46080,0x3364ae1f,0x31a831a8,0xae1f3364,0xb4000000,0xb1a8b1a8,46080,0x31a8b1a8,13312,0xae1f3364,0xb1a831a8,0xb3642e1f,
0x34000000,0x34000000,0x34000000,0x34000000,0xb4000000,0xb3642e1f,0xb1a831a8,0xae1f3364,0x34000000,0x31a8b1a8,46080,0xb1a8b1a8,0xb4000000,0xae1f3364,0x31a831a8,0x3364ae1f,
0x34000000,46080,0xb4000000,13312,0xb4000000,0x2e1f3364,0x31a8b1a8,0xb364ae1f,0x34000000,0xb1a8b1a8,13312,0x31a8b1a8,0xb4000000,0x33642e1f,0xb1a8b1a8,0x2e1f3364,
0x34000000,0xb4000000,0x34000000,0xb4000000,0xb4000000,0x3364ae1f,0xb1a831a8,0x2e1fb364,0x34000000,0xb1a831a8,46080,0x31a831a8,0xb4000000,0x2e1fb364,0x31a831a8,0xb3642e1f,
0x34000000,13312,0xb4000000,46080,0xb4000000,0xae1fb364,0x31a8b1a8,0x33642e1f,0x34000000,0x31a831a8,13312,0xb1a831a8,0xb4000000,0xb364ae1f,0xb1a8b1a8,0xae1fb364,
0x34000000,0x34000000,0x34000000,0x34000000,13312,0x2e1f3364,0x31a831a8,0x33642e1f,0xb4000000,0xb1a831a8,13312,0x31a831a8,46080,0xb364ae1f,0xb1a831a8,0x2e1f3364,
0x34000000,46080,0xb4000000,13312,13312,0x3364ae1f,0xb1a8b1a8,0xae1f3364,0xb4000000,0x31a831a8,46080,0xb1a831a8,46080,0xae1f3364,0x31a8b1a8,0xb3642e1f,
0x34000000,0xb4000000,0x34000000,0xb4000000,13312,0xae1fb364,0x31a831a8,0xb364ae1f,0xb4000000,0x31a8b1a8,13312,0xb1a8b1a8,46080,0x33642e1f,0xb1a831a8,0xae1fb364,
0x34000000,13312,0xb4000000,46080,13312,0xb3642e1f,0xb1a8b1a8,0x2e1fb364,0xb4000000,0xb1a8b1a8,46080,0x31a8b1a8,46080,0x2e1fb364,0x31a8b1a8,0x3364ae1f,
13312,13312,13312,0,13312,0x31243221,0x33e1298f,0,13312,0x33e1298f,0x2d79b384,0,0,0,0,0,
13312,0x32eeb000,0xb2eeb000,0,13312,0x2d79b384,0xb1243221,0,13312,0xad79b384,0x31243221,0,0,0,0,0,
13312,0xb2eeb000,0x32eeb000,0,13312,0xb3e1298f,0xad79b384,0,13312,0xb1243221,0xb3e1298f,0,0,0,0,0,
0,0,0,0,0,0,0,0,0,0,0,0,0,0,0,0,
13312,13312,13312,0,0x32eeb000,0x2d79b384,0xad79b384,0,0xb2eeb000,0xb1243221,0x31243221,0,0,0,0,0,
13312,0x32eeb000,0xb2eeb000,0,0x32eeb000,0xb3e1298f,0x33e1298f,0,0xb2eeb000,0x33e1298f,0xb3e1298f,0,0,0,0,0,
13312,0xb2eeb000,0x32eeb000,0,0x32eeb000,0x31243221,0xb1243221,0,0xb2eeb000,0xad79b384,0x2d79b384,0,0,0,0,0,
0,0,0,0,0,0,0,0,0,0,0,0,0,0,0,0,
13312,13312,13312,0,0xb2eeb000,0xb3e1298f,0xb1243221,0,0x32eeb000,0xad79b384,0xb3e1298f,0,0,0,0,0,
13312,0x32eeb000,0xb2eeb000,0,0xb2eeb000,0x31243221,0xad79b384,0,0x32eeb000,0xb1243221,0x2d79b384,0,0,0,0,0,
13312,0xb2eeb000,0x32eeb000,0,0xb2eeb000,0x2d79b384,0x33e1298f,0,0x32eeb000,0x33e1298f,0x31243221,0,0,0,0,0,
0,0,0,0,0,0,0,0,0,0,0,0,0,0,0,0,
0,0,0,0,0,0,0,0,0,0,0,0,0,0,0,0,
0,0,0,0,0,0,0,0,0,0,0,0,0,0,0,0,
0,0,0,0,0,0,0,0,0,0,0,0,0,0,0,0,
0,0,0,0,0,0,0,0,0,0,0,0,0,0,0,0,
0x34000000,0x34000000,0x34000000,0,0x34000000,0x3221b124,0x298fb3e1,0,0x34000000,0x298fb3e1,0xb384ad79,0,0,0,0,0,
0x34000000,0xb000b2ee,0xb00032ee,0,0x34000000,0xb384ad79,0x32213124,0,0x34000000,0xb3842d79,0x3221b124,0,0,0,0,0,
0x34000000,0xb00032ee,0xb000b2ee,0,0x34000000,0x298f33e1,0xb3842d79,0,0x34000000,0x32213124,0x298f33e1,0,0,0,0,0,
0,0,0,0,0,0,0,0,0,0,0,0,0,0,0,0,
0x34000000,0x34000000,0x34000000,0,0xb000b2ee,0xb384ad79,0xb3842d79,0,0xb00032ee,0x32213124,0x3221b124,0,0,0,0,0,
0x34000000,0xb000b2ee,0xb00032ee,0,0xb000b2ee,0x298f33e1,0x298fb3e1,0,0xb00032ee,0x298fb3e1,0x298f33e1,0,0,0,0,0,
0x34000000,0xb00032ee,0xb000b2ee,0,0xb000b2ee,0x3221b124,0x32213124,0,0xb00032ee,0xb3842d79,0xb384ad79,0,0,0,0,0,
0,0,0,0,0,0,0,0,0,0,0,0,0,0,0,0,
0x34000000,0x34000000,0x34000000,0,0xb00032ee,0x298f33e1,0x32213124,0,0xb000b2ee,0xb3842d79,0x298f33e1,0,0,0,0,0,
0x34000000,0xb000b2ee,0xb00032ee,0,0xb00032ee,0x3221b124,0xb3842d79,0,0xb000b2ee,0x32213124,0xb384ad79,0,0,0,0,0,
0x34000000,0xb00032ee,0xb000b2ee,0,0xb00032ee,0xb384ad79,0x298fb3e1,0,0xb000b2ee,0x298fb3e1,0x3221b124,0,0,0,0,0,
0,0,0,0,0,0,0,0,0,0,0,0,0,0,0,0,
0,0,0,0,0,0,0,0,0,0,0,0,0,0,0,0,
0,0,0,0,0,0,0,0,0,0,0,0,0,0,0,0,
0,0,0,0,0,0,0,0,0,0,0,0,0,0,0,0,
0,0,0,0,0,0,0,0,0,0,0,0,0,0,0,0,
13312,0x2a3e33d9,0x2e1f3364,0x307232a7,13312,0x307232a7,0x33642e1f,0x33d9aa3e,13312,0x32a73072,0x3364ae1f,0x2a3eb3d9,13312,0x33d92a3e,0x2e1fb364,0xb2a7b072,
13312,0x33d9aa3e,0xae1fb364,0xb2a73072,13312,0x32a7b072,0xb364ae1f,0x2a3e33d9,13312,0x3072b2a7,0xb3642e1f,0x33d92a3e,13312,0x2a3eb3d9,0xae1f3364,0x3072b2a7,
13312,0xaa3eb3d9,0x2e1f3364,0xb072b2a7,13312,0xb072b2a7,0x33642e1f,0xb3d92a3e,13312,0xb2a7b072,0x3364ae1f,0xaa3e33d9,13312,0xb3d9aa3e,0x2e1fb364,0x32a73072,
13312,0xb3d92a3e,0xae1fb364,0x32a7b072,13312,0xb2a73072,0xb364ae1f,0xaa3eb3d9,13312,0xb07232a7,0xb3642e1f,0xb3d9aa3e,13312,0xaa3e33d9,0xae1f3364,0xb07232a7,
0x31a831a8,0x32a73072,0x33642e1f,0x33d92a3e,0x31a8b1a8,0x2a3eb3d9,0xae1fb364,0xb2a7b072,0xb1a8b1a8,0xb3d92a3e,0xae1f3364,0x307232a7,0xb1a831a8,0x307232a7,0x3364ae1f,0xaa3eb3d9,
0x31a831a8,0x3072b2a7,0xb364ae1f,0xaa3e33d9,0x31a8b1a8,0xb3d9aa3e,0x2e1f3364,0x3072b2a7,0xb1a8b1a8,0x2a3e33d9,0x2e1fb364,0xb2a73072,0xb1a831a8,0x32a7b072,0xb3642e1f,0x33d9aa3e,
0x31a831a8,0xb2a7b072,0x33642e1f,0xb3d9aa3e,0x31a8b1a8,0xaa3e33d9,0xae1fb364,0x32a73072,0xb1a8b1a8,0x33d9aa3e,0xae1f3364,0xb072b2a7,0xb1a831a8,0xb072b2a7,0x3364ae1f,0x2a3e33d9,
0x31a831a8,0xb07232a7,0xb364ae1f,0x2a3eb3d9,0x31a8b1a8,0x33d92a3e,0x2e1f3364,0xb07232a7,0xb1a8b1a8,0xaa3eb3d9,0x2e1fb364,0x32a7b072,0xb1a831a8,0xb2a73072,0xb3642e1f,0xb3d92a3e,
0x34000000,0x33d9aa3e,0x3364ae1f,0x32a7b072,0xb4000000,0xb2a73072,0xae1f3364,0x2a3e33d9,0x34000000,0x3072b2a7,0xae1fb364,0xb3d9aa3e,0xb4000000,0xaa3e33d9,0x33642e1f,0x3072b2a7,
0x34000000,0xaa3eb3d9,0xb3642e1f,0x307232a7,0xb4000000,0x307232a7,0x2e1fb364,0xb3d92a3e,0x34000000,0xb2a7b072,0x2e1f3364,0x2a3eb3d9,0xb4000000,0x33d92a3e,0xb364ae1f,0x32a73072,
0x34000000,0xb3d92a3e,0x3364ae1f,0xb2a73072,0xb4000000,0x32a7b072,0xae1f3364,0xaa3eb3d9,0x34000000,0xb07232a7,0xae1fb364,0x33d92a3e,0xb4000000,0x2a3eb3d9,0x33642e1f,0xb07232a7,
0x34000000,0x2a3e33d9,0xb3642e1f,0xb072b2a7,0xb4000000,0xb072b2a7,0x2e1fb364,0x33d9aa3e,0x34000000,0x32a73072,0x2e1f3364,0xaa3e33d9,0xb4000000,0xb3d9aa3e,0xb364ae1f,0xb2a7b072,
0x31a8b1a8,0x3072b2a7,0x2e1fb364,0x2a3eb3d9,0x31a831a8,0x33d92a3e,0x3364ae1f,0x3072b2a7,0xb1a831a8,0x2a3e33d9,0x33642e1f,0x32a7b072,0xb1a8b1a8,0xb2a73072,0x2e1f3364,0x33d9aa3e,
0x31a8b1a8,0xb2a7b072,0xae1f3364,0x33d92a3e,0x31a831a8,0x2a3eb3d9,0xb3642e1f,0x32a73072,0xb1a831a8,0x33d9aa3e,0xb364ae1f,0x307232a7,0xb1a8b1a8,0x307232a7,0xae1fb364,0x2a3e33d9,
0x31a8b1a8,0xb07232a7,0x2e1fb364,0xaa3e33d9,0x31a831a8,0xb3d9aa3e,0x3364ae1f,0xb07232a7,0xb1a831a8,0xaa3eb3d9,0x33642e1f,0xb2a73072,0xb1a8b1a8,0x32a7b072,0x2e1f3364,0xb3d92a3e,
0x31a8b1a8,0x32a73072,0xae1f3364,0xb3d9aa3e,0x31a831a8,0xaa3e33d9,0xb3642e1f,0xb2a7b072,0xb1a831a8,0xb3d92a3e,0xb364ae1f,0xb072b2a7,0xb1a8b1a8,0xb072b2a7,0xae1fb364,0xaa3eb3d9,
0x34000000,0x33d9aa3e,0x3364ae1f,0x32a7b072,0x34000000,0x32a7b072,0x2e1fb364,0xaa3eb3d9,0x34000000,0x3072b2a7,0xae1fb364,0xb3d9aa3e,0x34000000,0x2a3eb3d9,0xb364ae1f,0xb07232a7,
0x34000000,0xaa3eb3d9,0xb3642e1f,0x307232a7,0x34000000,0xb072b2a7,0xae1f3364,0x33d9aa3e,0x34000000,0xb2a7b072,0x2e1f3364,0x2a3eb3d9,0x34000000,0xb3d9aa3e,0x33642e1f,0xb2a7b072,
0x34000000,0xb3d92a3e,0x3364ae1f,0xb2a73072,0x34000000,0xb2a73072,0x2e1fb364,0x2a3e33d9,0x34000000,0xb07232a7,0xae1fb364,0x33d92a3e,0x34000000,0xaa3e33d9,0xb364ae1f,0x3072b2a7,
0x34000000,0x2a3e33d9,0xb3642e1f,0xb072b2a7,0x34000000,0x307232a7,0xae1f3364,0xb3d92a3e,0x34000000,0x32a73072,0x2e1f3364,0xaa3e33d9,0x34000000,0x33d92a3e,0x33642e1f,0x32a73072,
0x31a8b1a8,0x3072b2a7,0x2e1fb364,0x2a3eb3d9,0xb1a8b1a8,0xb3d9aa3e,0xb3642e1f,0xb07232a7,0xb1a831a8,0x2a3e33d9,0x33642e1f,0x32a7b072,0x31a831a8,0x32a7b072,0xae1fb364,0xb3d92a3e,
0x31a8b1a8,0xb2a7b072,0xae1f3364,0x33d92a3e,0xb1a8b1a8,0xaa3e33d9,0x3364ae1f,0xb2a7b072,0xb1a831a8,0x33d9aa3e,0xb364ae1f,0x307232a7,0x31a831a8,0xb072b2a7,0x2e1f3364,0xaa3eb3d9,
0x31a8b1a8,0xb07232a7,0x2e1fb364,0xaa3e33d9,0xb1a8b1a8,0x33d92a3e,0xb3642e1f,0x3072b2a7,0xb1a831a8,0xaa3eb3d9,0x33642e1f,0xb2a73072,0x31a831a8,0xb2a73072,0xae1fb364,0x33d9aa3e,
0x31a8b1a8,0x32a73072,0xae1f3364,0xb3d9aa3e,0xb1a8b1a8,0x2a3eb3d9,0x3364ae1f,0x32a73072,0xb1a831a8,0xb3d92a3e,0xb364ae1f,0xb072b2a7,0x31a831a8,0x307232a7,0x2e1f3364,0x2a3e33d9,
46080,0xaa3eb3d9,0xae1fb364,0xb072b2a7,13312,0x307232a7,0x33642e1f,0x33d9aa3e,46080,0xb2a7b072,0xb3642e1f,0xaa3e33d9,13312,0x33d92a3e,0x2e1fb364,0xb2a7b072,
46080,0xb3d92a3e,0x2e1f3364,0x32a7b072,13312,0x32a7b072,0xb364ae1f,0x2a3e33d9,46080,0xb07232a7,0x3364ae1f,0xb3d9aa3e,13312,0x2a3eb3d9,0xae1f3364,0x3072b2a7,
46080,0x2a3e33d9,0xae1fb364,0x307232a7,13312,0xb072b2a7,0x33642e1f,0xb3d92a3e,46080,0x32a73072,0xb3642e1f,0x2a3eb3d9,13312,0xb3d9aa3e,0x2e1fb364,0x32a73072,
46080,0x33d9aa3e,0x2e1f3364,0xb2a73072,13312,0xb2a73072,0xb364ae1f,0xaa3eb3d9,46080,0x3072b2a7,0x3364ae1f,0x33d92a3e,13312,0xaa3e33d9,0xae1f3364,0xb07232a7,
0xb1a8b1a8,0xb2a7b072,0xb364ae1f,0xb3d9aa3e,0x31a8b1a8,0x2a3eb3d9,0xae1fb364,0xb2a7b072,0x31a831a8,0x33d9aa3e,0x2e1fb364,0xb072b2a7,0xb1a831a8,0x307232a7,0x3364ae1f,0xaa3eb3d9,
0xb1a8b1a8,0xb07232a7,0x33642e1f,0x2a3eb3d9,0x31a8b1a8,0xb3d9aa3e,0x2e1f3364,0x3072b2a7,0x31a831a8,0xaa3eb3d9,0xae1f3364,0x32a7b072,0xb1a831a8,0x32a7b072,0xb3642e1f,0x33d9aa3e,
0xb1a8b1a8,0x32a73072,0xb364ae1f,0x33d92a3e,0x31a8b1a8,0xaa3e33d9,0xae1fb364,0x32a73072,0x31a831a8,0xb3d92a3e,0x2e1fb364,0x307232a7,0xb1a831a8,0xb072b2a7,0x3364ae1f,0x2a3e33d9,
0xb1a8b1a8,0x3072b2a7,0x33642e1f,0xaa3e33d9,0x31a8b1a8,0x33d92a3e,0x2e1f3364,0xb07232a7,0x31a831a8,0x2a3e33d9,0xae1f3364,0xb2a73072,0xb1a831a8,0xb2a73072,0xb3642e1f,0xb3d92a3e,
13312,13312,13312,13312,0x2a3e33d9,0x307232a7,0x32a73072,0x33d92a3e,0x2e1f3364,0x33642e1f,0x3364ae1f,0x2e1fb364,0x307232a7,0x33d9aa3e,0x2a3eb3d9,0xb2a7b072,
0x31a831a8,0x31a8b1a8,0xb1a8b1a8,0xb1a831a8,0x32a73072,0x2a3eb3d9,0xb3d92a3e,0x307232a7,0x33642e1f,0xae1fb364,0xae1f3364,0x3364ae1f,0x33d92a3e,0xb2a7b072,0x307232a7,0xaa3eb3d9,
0x34000000,0xb4000000,0x34000000,0xb4000000,0x33d9aa3e,0xb2a73072,0x3072b2a7,0xaa3e33d9,0x3364ae1f,0xae1f3364,0xae1fb364,0x33642e1f,0x32a7b072,0x2a3e33d9,0xb3d9aa3e,0x3072b2a7,
0x31a8b1a8,0x31a831a8,0xb1a831a8,0xb1a8b1a8,0x3072b2a7,0x33d92a3e,0x2a3e33d9,0xb2a73072,0x2e1fb364,0x3364ae1f,0x33642e1f,0x2e1f3364,0x2a3eb3d9,0x3072b2a7,0x32a7b072,0x33d9aa3e,
13312,13312,13312,13312,0x33d9aa3e,0x32a7b072,0x3072b2a7,0x2a3eb3d9,0xae1fb364,0xb364ae1f,0xb3642e1f,0xae1f3364,0xb2a73072,0x2a3e33d9,0x33d92a3e,0x3072b2a7,
0x31a831a8,0x31a8b1a8,0xb1a8b1a8,0xb1a831a8,0x3072b2a7,0xb3d9aa3e,0x2a3e33d9,0x32a7b072,0xb364ae1f,0x2e1f3364,0x2e1fb364,0xb3642e1f,0xaa3e33d9,0x3072b2a7,0xb2a73072,0x33d9aa3e,
0x34000000,0xb4000000,0x34000000,0xb4000000,0xaa3eb3d9,0x307232a7,0xb2a7b072,0x33d92a3e,0xb3642e1f,0x2e1fb364,0x2e1f3364,0xb364ae1f,0x307232a7,0xb3d92a3e,0x2a3eb3d9,0x32a73072,
0x31a8b1a8,0x31a831a8,0xb1a831a8,0xb1a8b1a8,0xb2a7b072,0x2a3eb3d9,0x33d9aa3e,0x307232a7,0xae1f3364,0xb3642e1f,0xb364ae1f,0xae1fb364,0x33d92a3e,0x32a73072,0x307232a7,0x2a3e33d9,
13312,13312,13312,13312,0xaa3eb3d9,0xb072b2a7,0xb2a7b072,0xb3d9aa3e,0x2e1f3364,0x33642e1f,0x3364ae1f,0x2e1fb364,0xb072b2a7,0xb3d92a3e,0xaa3e33d9,0x32a73072,
0x31a831a8,0x31a8b1a8,0xb1a8b1a8,0xb1a831a8,0xb2a7b072,0xaa3e33d9,0x33d9aa3e,0xb072b2a7,0x33642e1f,0xae1fb364,0xae1f3364,0x3364ae1f,0xb3d9aa3e,0x32a73072,0xb072b2a7,0x2a3e33d9,
0x34000000,0xb4000000,0x34000000,0xb4000000,0xb3d92a3e,0x32a7b072,0xb07232a7,0x2a3eb3d9,0x3364ae1f,0xae1f3364,0xae1fb364,0x33642e1f,0xb2a73072,0xaa3eb3d9,0x33d92a3e,0xb07232a7,
0x31a8b1a8,0x31a831a8,0xb1a831a8,0xb1a8b1a8,0xb07232a7,0xb3d9aa3e,0xaa3eb3d9,0x32a7b072,0x2e1fb364,0x3364ae1f,0x33642e1f,0x2e1f3364,0xaa3e33d9,0xb07232a7,0xb2a73072,0xb3d92a3e,
13312,13312,13312,13312,0xb3d92a3e,0xb2a73072,0xb07232a7,0xaa3e33d9,0xae1fb364,0xb364ae1f,0xb3642e1f,0xae1f3364,0x32a7b072,0xaa3eb3d9,0xb3d9aa3e,0xb07232a7,
0x31a831a8,0x31a8b1a8,0xb1a8b1a8,0xb1a831a8,0xb07232a7,0x33d92a3e,0xaa3eb3d9,0xb2a73072,0xb364ae1f,0x2e1f3364,0x2e1fb364,0xb3642e1f,0x2a3eb3d9,0xb07232a7,0x32a7b072,0xb3d92a3e,
0x34000000,0xb4000000,0x34000000,0xb4000000,0x2a3e33d9,0xb072b2a7,0x32a73072,0xb3d9aa3e,0xb3642e1f,0x2e1fb364,0x2e1f3364,0xb364ae1f,0xb072b2a7,0x33d9aa3e,0xaa3e33d9,0xb2a7b072,
0x31a8b1a8,0x31a831a8,0xb1a831a8,0xb1a8b1a8,0x32a73072,0xaa3e33d9,0xb3d92a3e,0xb072b2a7,0xae1f3364,0xb3642e1f,0xb364ae1f,0xae1fb364,0xb3d9aa3e,0xb2a7b072,0xb072b2a7,0xaa3eb3d9,
0x34000000,0x34000000,0x34000000,0x34000000,0x33d9aa3e,0x32a7b072,0x3072b2a7,0x2a3eb3d9,0x3364ae1f,0x2e1fb364,0xae1fb364,0xb364ae1f,0x32a7b072,0xaa3eb3d9,0xb3d9aa3e,0xb07232a7,
0x31a8b1a8,0xb1a8b1a8,0xb1a831a8,0x31a831a8,0x3072b2a7,0xb3d9aa3e,0x2a3e33d9,0x32a7b072,0x2e1fb364,0xb3642e1f,0x33642e1f,0xae1fb364,0x2a3eb3d9,0xb07232a7,0x32a7b072,0xb3d92a3e,
46080,13312,46080,13312,0xaa3eb3d9,0x307232a7,0xb2a7b072,0x33d92a3e,0xae1fb364,0x33642e1f,0xb3642e1f,0x2e1fb364,0xb072b2a7,0x33d9aa3e,0xaa3e33d9,0xb2a7b072,
0xb1a8b1a8,0x31a8b1a8,0x31a831a8,0xb1a831a8,0xb2a7b072,0x2a3eb3d9,0x33d9aa3e,0x307232a7,0xb364ae1f,0xae1fb364,0x2e1fb364,0x3364ae1f,0xb3d9aa3e,0xb2a7b072,0xb072b2a7,0xaa3eb3d9,
0x34000000,0x34000000,0x34000000,0x34000000,0xaa3eb3d9,0xb072b2a7,0xb2a7b072,0xb3d9aa3e,0xb3642e1f,0xae1f3364,0x2e1f3364,0x33642e1f,0x307232a7,0x33d9aa3e,0x2a3eb3d9,0xb2a7b072,
0x31a8b1a8,0xb1a8b1a8,0xb1a831a8,0x31a831a8,0xb2a7b072,0xaa3e33d9,0x33d9aa3e,0xb072b2a7,0xae1f3364,0x3364ae1f,0xb364ae1f,0x2e1f3364,0x33d92a3e,0xb2a7b072,0x307232a7,0xaa3eb3d9,
46080,13312,46080,13312,0xb3d92a3e,0x32a7b072,0xb07232a7,0x2a3eb3d9,0x2e1f3364,0xb364ae1f,0x3364ae1f,0xae1f3364,0x32a7b072,0x2a3e33d9,0xb3d9aa3e,0x3072b2a7,
0xb1a8b1a8,0x31a8b1a8,0x31a831a8,0xb1a831a8,0xb07232a7,0xb3d9aa3e,0xaa3eb3d9,0x32a7b072,0x33642e1f,0x2e1f3364,0xae1f3364,0xb3642e1f,0x2a3eb3d9,0x3072b2a7,0x32a7b072,0x33d9aa3e,
0x34000000,0x34000000,0x34000000,0x34000000,0xb3d92a3e,0xb2a73072,0xb07232a7,0xaa3e33d9,0x3364ae1f,0x2e1fb364,0xae1fb364,0xb364ae1f,0xb2a73072,0x2a3e33d9,0x33d92a3e,0x3072b2a7,
0x31a8b1a8,0xb1a8b1a8,0xb1a831a8,0x31a831a8,0xb07232a7,0x33d92a3e,0xaa3eb3d9,0xb2a73072,0x2e1fb364,0xb3642e1f,0x33642e1f,0xae1fb364,0xaa3e33d9,0x3072b2a7,0xb2a73072,0x33d9aa3e,
46080,13312,46080,13312,0x2a3e33d9,0xb072b2a7,0x32a73072,0xb3d9aa3e,0xae1fb364,0x33642e1f,0xb3642e1f,0x2e1fb364,0x307232a7,0xb3d92a3e,0x2a3eb3d9,0x32a73072,
0xb1a8b1a8,0x31a8b1a8,0x31a831a8,0xb1a831a8,0x32a73072,0xaa3e33d9,0xb3d92a3e,0xb072b2a7,0xb364ae1f,0xae1fb364,0x2e1fb364,0x3364ae1f,0x33d92a3e,0x32a73072,0x307232a7,0x2a3e33d9,
0x34000000,0x34000000,0x34000000,0x34000000,0x2a3e33d9,0x307232a7,0x32a73072,0x33d92a3e,0xb3642e1f,0xae1f3364,0x2e1f3364,0x33642e1f,0xb072b2a7,0xb3d92a3e,0xaa3e33d9,0x32a73072,
0x31a8b1a8,0xb1a8b1a8,0xb1a831a8,0x31a831a8,0x32a73072,0x2a3eb3d9,0xb3d92a3e,0x307232a7,0xae1f3364,0x3364ae1f,0xb364ae1f,0x2e1f3364,0xb3d9aa3e,0x32a73072,0xb072b2a7,0x2a3e33d9,
46080,13312,46080,13312,0x33d9aa3e,0xb2a73072,0x3072b2a7,0xaa3e33d9,0x2e1f3364,0xb364ae1f,0x3364ae1f,0xae1f3364,0xb2a73072,0xaa3eb3d9,0x33d92a3e,0xb07232a7,
0xb1a8b1a8,0x31a8b1a8,0x31a831a8,0xb1a831a8,0x3072b2a7,0x33d92a3e,0x2a3e33d9,0xb2a73072,0x33642e1f,0x2e1f3364,0xae1f3364,0xb3642e1f,0xaa3e33d9,0xb07232a7,0xb2a73072,0xb3d92a3e,
0,304,648,952,576,880,1224,1528,1152,1456,1800,2104,1728,2032,104,408,
1728,2032,104,408,32,336,680,984,608,912,1256,1560,1184,1488,1832,2136,
1184,1488,1832,2136,1760,2064,352,1648,64,1360,72,376,64,1360,72,376,
64,1360,72,376,64,1360,72,376,64,1360,72,376,64,1360,72,376,
1296,1600,1944,2248,1872,2176,248,552,176,480,824,1128,752,1056,1400,1704,
752,1056,1400,1704,1328,1632,1976,2280,1904,2208,928,2224,640,1936,216,520,
640,1936,216,520,144,448,792,1096,720,1024,1368,1672,720,1024,1368,1672,
720,1024,1368,1672,720,1024,1368,1672,720,1024,1368,1672,720,1024,1368,1672,
320,624,968,1272,896,1200,1544,1848,1472,1776,2120,496,2048,208,1504,88,
2048,208,1504,88,1216,16,360,664,288,592,936,1240,864,1168,1512,1816,
864,1168,1512,1816,1440,1744,2088,120,2016,48,392,696,2016,48,392,696,
2016,48,392,696,2016,48,392,696,2016,48,392,696,2016,48,392,696,
1616,1920,2264,1072,2192,784,2080,232,1792,160,504,808,432,736,1080,1384,
432,736,1080,1384,1008,1312,1656,1960,1584,1888,2232,264,2160,192,536,840,
2160,192,536,840,464,768,1112,1416,1040,1344,1688,1992,1040,1344,1688,1992,
1040,1344,1688,1992,1040,1344,1688,1992,1040,1344,1688,1992,1040,1344,1688,1992,
152,456,800,1104,728,1032,1376,1680,1304,1608,1952,2256,1880,2184,256,560,
1880,2184,256,560,184,488,832,1136,760,1064,1408,1712,1336,1640,1984,2288,
1336,1640,1984,2288,1912,2216,1000,2296,712,2008,224,528,712,2008,224,528,
712,2008,224,528,712,2008,224,528,712,2008,224,528,712,2008,224,528,
1448,1752,2096,128,2024,56,400,704,328,632,976,1280,904,1208,1552,1856,
904,1208,1552,1856,1480,1784,2128,568,2056,280,1576,96,1288,24,368,672,
1288,24,368,672,296,600,944,1248,872,1176,1520,1824,872,1176,1520,1824,
872,1176,1520,1824,872,1176,1520,1824,872,1176,1520,1824,872,1176,1520,1824,
472,776,1120,1424,1048,1352,1696,2000,1624,1928,2272,1144,2200,856,2152,240,
2200,856,2152,240,1864,168,512,816,440,744,1088,1392,1016,1320,1664,1968,
1016,1320,1664,1968,1592,1896,2240,272,2168,200,544,848,2168,200,544,848,
2168,200,544,848,2168,200,544,848,2168,200,544,848,2168,200,544,848,
1768,2072,424,1720,136,1432,80,384,8,312,656,960,584,888,1232,1536,
584,888,1232,1536,1160,1464,1808,2112,1736,2040,112,416,40,344,688,992,
40,344,688,992,616,920,1264,1568,1192,1496,1840,2144,1192,1496,1840,2144,
1192,1496,1840,2144,1192,1496,1840,2144,1192,1496,1840,2144,1192,1496,1840,2144,
9920,10176,9792,0,12232,12488,12104,16,10818,10562,10946,32,13130,12874,13258,48,
9794,10050,10242,64,12106,12362,12554,80,10944,10688,10496,96,13256,13000,12808,112,
10244,9924,10116,128,12556,12236,12428,144,10494,10814,10622,160,12806,13126,12934,176,
10118,9798,9990,192,12430,12110,12302,208,10620,10940,10748,224,12932,13252,13060,240,
9992,10248,9864,4608,12304,12560,12176,4624,10746,10490,10874,4640,13058,12802,13186,4656,
9866,10122,10314,4672,12178,12434,12626,4688,10872,10616,10424,4704,13184,12928,12736,4720,
10316,9996,10188,4736,12628,12308,12500,4752,10422,10742,10550,4768,12734,13054,12862,4784,
10190,9870,10062,4800,12502,12182,12374,4816,10548,10868,10676,4832,12860,13180,12988,4848,
10064,10320,9936,1048576,12376,12632,12248,496,10674,10418,10802,480,12986,12730,13114,464,
9938,10194,9810,448,12250,12506,12122,432,10800,10544,10928,416,13112,12856,13240,400,
9812,10068,10260,384,12124,12380,12572,368,10926,10670,10478,352,13238,12982,12790,336,
10262,9942,10134,320,12574,12254,12446,304,10476,10796,10604,288,12788,13108,12916,272,
10136,9816,10008,1048576,12448,12128,12320,5104,10602,10922,10730,5088,12914,13234,13042,5072,
10010,10266,9882,5056,12322,12578,12194,5040,10728,10472,10856,5024,13040,12784,13168,5008,
9884,10140,10332,4992,12196,12452,12644,4976,10854,10598,10406,4960,13166,12910,12718,4944,
10334,10014,10206,4928,12646,12326,12518,4912,10404,10724,10532,4896,12716,13036,12844,4880,
10208,9888,10080,256,12520,12200,12392,1048576,10530,10850,10658,1048576,12842,13162,12970,1048576,
10082,10338,10368,1048576,12394,12650,12680,1048576,10656,10400,10370,1048576,12968,12712,12682,1048576,
9956,10212,10368,1048576,12268,12524,12680,1048576,10782,10526,10370,1048576,13094,12838,12682,1048576,
9830,10086,10368,1048576,12142,12398,12680,1048576,10908,10652,10370,1048576,13220,12964,12682,1048576,
10280,9960,10368,4864,12592,12272,12680,1048576,10458,10778,10370,1048576,12770,13090,12682,1048576,
10154,9834,10368,1048576,12466,12146,12680,1048576,10584,10904,10370,1048576,12896,13216,12682,1048576,
10028,10284,10368,1048576,12340,12596,12680,1048576,10710,10454,10370,1048576,13022,12766,12682,1048576,
9902,10158,10368,1048576,12214,12470,12680,1048576,10836,10580,10370,1048576,13148,12892,12682,1048576,
10352,10032,10368,1048576,12664,12344,12680,1048576,10386,10706,10370,1048576,12698,13018,12682,1048576,
10226,9906,10368,1048576,12538,12218,12680,1048576,10512,10832,10370,1048576,12824,13144,12682,1048576,
10100,10356,10368,1048576,12412,12668,12680,1048576,10638,10382,10370,1048576,12950,12694,12682,1048576,
9974,10230,10368,1048576,12286,12542,12680,1048576,10764,10508,10370,1048576,13076,12820,12682,1048576,
9848,10104,10368,1048576,12160,12416,12680,1048576,10890,10634,10370,1048576,13202,12946,12682,1048576,
10298,9978,10368,1048576,12610,12290,12680,1048576,10440,10760,10370,1048576,12752,13072,12682,1048576,
10172,9852,10368,1048576,12484,12164,12680,1048576,10566,10886,10370,1048576,12878,13198,12682,1048576,
10046,10302,10368,1048576,12358,12614,12680,1048576,10692,10436,10370,1048576,13004,12748,12682,1048576,
10048,10304,10368,1048576,12360,12616,12680,1048576,10690,10434,10370,1048576,13002,12746,12682,1048576,
9922,10178,10368,1048576,12234,12490,12680,1048576,10816,10560,10370,1048576,13128,12872,12682,1048576,
9796,10052,10368,1048576,12108,12364,12680,1048576,10942,10686,10370,1048576,13254,12998,12682,1048576,
10246,9926,10368,1048576,12558,12238,12680,1048576,10492,10812,10370,1048576,12804,13124,12682,1048576,
10120,9800,10368,1048576,12432,12112,12680,1048576,10618,10938,10370,1048576,12930,13250,12682,1048576,
9994,10250,10368,1048576,12306,12562,12680,1048576,10744,10488,10370,1048576,13056,12800,12682,1048576,
9868,10124,10368,1048576,12180,12436,12680,1048576,10870,10614,10370,1048576,13182,12926,12682,1048576,
10318,9998,10368,1048576,12630,12310,12680,1048576,10420,10740,10370,1048576,12732,13052,12682,1048576,
10192,9872,10368,1048576,12504,12184,12680,1048576,10546,10866,10370,1048576,12858,13178,12682,1048576,
10066,10322,10368,1048576,12378,12634,12680,1048576,10672,10416,10370,1048576,12984,12728,12682,1048576,
9940,10196,10368,1048576,12252,12508,12680,1048576,10798,10542,10370,1048576,13110,12854,12682,1048576,
9814,10070,10368,1048576,12126,12382,12680,1048576,10924,10668,10370,1048576,13236,12980,12682,1048576,
10264,9944,10368,1048576,12576,12256,12680,1048576,10474,10794,10370,1048576,12786,13106,12682,1048576,
10138,9818,10368,1048576,12450,12130,12680,1048576,10600,10920,10370,1048576,12912,13232,12682,1048576,
10012,10268,10368,1048576,12324,12580,12680,1048576,10726,10470,10370,1048576,13038,12782,12682,1048576,
9886,10142,10368,1048576,12198,12454,12680,1048576,10852,10596,10370,1048576,13164,12908,12682,1048576,
10336,10016,10368,1048576,12648,12328,12680,1048576,10402,10722,10370,1048576,12714,13034,12682,1048576,
10210,9890,10368,1048576,12522,12202,12680,1048576,10528,10848,10370,1048576,12840,13160,12682,1048576,
10084,10340,10368,1048576,12396,12652,12680,1048576,10654,10398,10370,1048576,12966,12710,12682,1048576,
9958,10214,10368,1048576,12270,12526,12680,1048576,10780,10524,10370,1048576,13092,12836,12682,1048576,
9832,10088,10368,1048576,12144,12400,12680,1048576,10906,10650,10370,1048576,13218,12962,12682,1048576,
10282,9962,10368,1048576,12594,12274,12680,1048576,10456,10776,10370,1048576,12768,13088,12682,1048576,
10156,9836,10368,1048576,12468,12148,12680,1048576,10582,10902,10370,1048576,12894,13214,12682,1048576,
10030,10286,10368,1048576,12342,12598,12680,1048576,10708,10452,10370,1048576,13020,12764,12682,1048576,
9904,10160,10368,1048576,12216,12472,12680,1048576,10834,10578,10370,1048576,13146,12890,12682,1048576,
10354,10034,10368,1048576,12666,12346,12680,1048576,10384,10704,10370,1048576,12696,13016,12682,1048576,
10228,9908,10368,1048576,12540,12220,12680,1048576,10510,10830,10370,1048576,12822,13142,12682,1048576,
10102,10358,10368,1048576,12414,12670,12680,1048576,10636,10380,10370,1048576,12948,12692,12682,1048576,
9976,10232,10368,1048576,12288,12544,12680,1048576,10762,10506,10370,1048576,13074,12818,12682,1048576,
9850,10106,10368,1048576,12162,12418,12680,1048576,10888,10632,10370,1048576,13200,12944,12682,1048576,
10300,9980,10368,1048576,12612,12292,12680,1048576,10438,10758,10370,1048576,12750,13070,12682,1048576,
10174,9854,10368,1048576,12486,12166,12680,1048576,10564,10884,10370,1048576,12876,13196,12682,1048576,
0,512,1536,0,16,528,1552,16,32,544,1568,32,48,560,1584,48,
64,576,1600,64,80,592,1616,80,96,608,1632,96,112,624,1648,112,
128,640,1664,128,144,656,1680,144,160,672,1696,160,176,688,1712,176,
192,704,1728,192,208,720,1744,208,224,736,1760,224,240,752,1776,240,
1536,5120,6144,4608,1552,5136,6160,4624,1568,5152,6176,4640,1584,5168,6192,4656,
1600,5184,6208,4672,1616,5200,6224,4688,1632,5216,6240,4704,1648,5232,6256,4720,
1664,5248,6272,4736,1680,5264,6288,4752,1696,5280,6304,4768,1712,5296,6320,4784,
1728,5312,6336,4800,1744,5328,6352,4816,1760,5344,6368,4832,1776,5360,6384,4848,
3072,4096,3072,0,3088,4592,3568,496,3104,4576,3552,480,3120,4560,3536,464,
3136,4544,3520,448,3152,4528,3504,432,3168,4512,3488,416,3184,4496,3472,400,
3200,4480,3456,384,3216,4464,3440,368,3232,4448,3424,352,3248,4432,3408,336,
3264,4416,3392,320,3280,4400,3376,304,3296,4384,3360,288,3312,4368,3344,272,
3072,8704,7680,4608,3088,9200,8176,5104,3104,9184,8160,5088,3120,9168,8144,5072,
3136,9152,8128,5056,3152,9136,8112,5040,3168,9120,8096,5024,3184,9104,8080,5008,
3200,9088,8064,4992,3216,9072,8048,4976,3232,9056,8032,4960,3248,9040,8016,4944,
3264,9024,8000,4928,3280,9008,7984,4912,3296,8992,7968,4896,3312,8976,7952,4880,
8,768,1792,256,24,784,1808,272,40,800,1824,288,56,816,1840,304,
72,832,1856,320,88,848,1872,336,104,864,1888,352,120,880,1904,368,
136,896,1920,384,152,912,1936,400,168,928,1952,416,184,944,1968,432,
200,960,1984,448,216,976,2000,464,232,992,2016,480,248,1008,2032,496,
1544,5376,6400,4864,1560,5392,6416,4880,1576,5408,6432,4896,1592,5424,6448,4912,
1608,5440,6464,4928,1624,5456,6480,4944,1640,5472,6496,4960,1656,5488,6512,4976,
1672,5504,6528,4992,1688,5520,6544,5008,1704,5536,6560,5024,1720,5552,6576,5040,
1736,5568,6592,5056,1752,5584,6608,5072,1768,5600,6624,5088,1784,5616,6640,5104,
3080,4352,3328,256,3096,4336,3312,240,3112,4320,3296,224,3128,4304,3280,208,
3144,4288,3264,192,3160,4272,3248,176,3176,4256,3232,160,3192,4240,3216,144,
3208,4224,3200,128,3224,4208,3184,112,3240,4192,3168,96,3256,4176,3152,80,
3272,4160,3136,64,3288,4144,3120,48,3304,4128,3104,32,3320,4112,3088,16,
3080,8960,7936,4864,3096,8944,7920,4848,3112,8928,7904,4832,3128,8912,7888,4816,
3144,8896,7872,4800,3160,8880,7856,4784,3176,8864,7840,4768,3192,8848,7824,4752,
3208,8832,7808,4736,3224,8816,7792,4720,3240,8800,7776,4704,3256,8784,7760,4688,
3272,8768,7744,4672,3288,8752,7728,4656,3304,8736,7712,4640,3320,8720,7696,4624,
4608,1024,2048,0,4624,1040,2064,16,4640,1056,2080,32,4656,1072,2096,48,
4672,1088,2112,64,4688,1104,2128,80,4704,1120,2144,96,4720,1136,2160,112,
4736,1152,2176,128,4752,1168,2192,144,4768,1184,2208,160,4784,1200,2224,176,
4800,1216,2240,192,4816,1232,2256,208,4832,1248,2272,224,4848,1264,2288,240,
6144,5632,6656,4608,6160,5648,6672,4624,6176,5664,6688,4640,6192,5680,6704,4656,
6208,5696,6720,4672,6224,5712,6736,4688,6240,5728,6752,4704,6256,5744,6768,4720,
6272,5760,6784,4736,6288,5776,6800,4752,6304,5792,6816,4768,6320,5808,6832,4784,
6336,5824,6848,4800,6352,5840,6864,4816,6368,5856,6880,4832,6384,5872,6896,4848,
7680,3584,2560,0,7696,4080,3056,496,7712,4064,3040,480,7728,4048,3024,464,
7744,4032,3008,448,7760,4016,2992,432,7776,4000,2976,416,7792,3984,2960,400,
7808,3968,2944,384,7824,3952,2928,368,7840,3936,2912,352,7856,3920,2896,336,
7872,3904,2880,320,7888,3888,2864,304,7904,3872,2848,288,7920,3856,2832,272,
7680,8192,7168,4608,7696,8688,7664,5104,7712,8672,7648,5088,7728,8656,7632,5072,
7744,8640,7616,5056,7760,8624,7600,5040,7776,8608,7584,5024,7792,8592,7568,5008,
7808,8576,7552,4992,7824,8560,7536,4976,7840,8544,7520,4960,7856,8528,7504,4944,
7872,8512,7488,4928,7888,8496,7472,4912,7904,8480,7456,4896,7920,8464,7440,4880,
4616,1280,2304,256,4632,1296,2320,272,4648,1312,2336,288,4664,1328,2352,304,
4680,1344,2368,320,4696,1360,2384,336,4712,1376,2400,352,4728,1392,2416,368,
4744,1408,2432,384,4760,1424,2448,400,4776,1440,2464,416,4792,1456,2480,432,
4808,1472,2496,448,4824,1488,2512,464,4840,1504,2528,480,4856,1520,2544,496,
6152,5888,6912,4864,6168,5904,6928,4880,6184,5920,6944,4896,6200,5936,6960,4912,
6216,5952,6976,4928,6232,5968,6992,4944,6248,5984,7008,4960,6264,6000,7024,4976,
6280,6016,7040,4992,6296,6032,7056,5008,6312,6048,7072,5024,6328,6064,7088,5040,
6344,6080,7104,5056,6360,6096,7120,5072,6376,6112,7136,5088,6392,6128,7152,5104,
7688,3840,2816,256,7704,3824,2800,240,7720,3808,2784,224,7736,3792,2768,208,
7752,3776,2752,192,7768,3760,2736,176,7784,3744,2720,160,7800,3728,2704,144,
7816,3712,2688,128,7832,3696,2672,112,7848,3680,2656,96,7864,3664,2640,80,
7880,3648,2624,64,7896,3632,2608,48,7912,3616,2592,32,7928,3600,2576,16,
7688,8448,7424,4864,7704,8432,7408,4848,7720,8416,7392,4832,7736,8400,7376,4816,
7752,8384,7360,4800,7768,8368,7344,4784,7784,8352,7328,4768,7800,8336,7312,4752,
7816,8320,7296,4736,7832,8304,7280,4720,7848,8288,7264,4704,7864,8272,7248,4688,
7880,8256,7232,4672,7896,8240,7216,4656,7912,8224,7200,4640,7928,8208,7184,4624,
4,68,1760,0,12,140,1768,16,20,212,1776,32,28,284,1784,48,
76,356,1832,64,84,428,1840,80,92,500,1848,96,100,572,1856,112,
148,644,1904,128,156,716,1912,144,164,788,1920,160,172,860,1928,176,
220,932,1976,192,228,1004,1984,208,236,1076,1992,224,244,1148,2000,240,
292,1220,2048,1536,300,1292,2056,1552,308,1364,2064,1568,316,1436,2072,1584,
364,1508,2120,1600,372,1580,2128,1616,380,1652,2136,1632,388,1724,2144,1648,
436,1796,2192,1664,444,1868,2200,1680,452,1940,2208,1696,460,2012,2216,1712,
508,2084,2264,1728,516,2156,2272,1744,524,2228,2280,1760,532,2300,2288,1776,
580,0,64,3072,588,8,136,3088,596,16,208,3104,604,24,280,3120,
652,72,352,3136,660,80,424,3152,668,88,496,3168,676,96,568,3184,
724,144,640,3200,732,152,712,3216,740,160,784,3232,748,168,856,3248,
796,216,928,3264,804,224,1000,3280,812,232,1072,3296,820,240,1144,3312,
868,288,1216,1048576,876,296,1288,1048576,884,304,1360,1048576,892,312,1432,1048576,
940,360,1504,1048576,948,368,1576,1048576,956,376,1648,1048576,964,384,1720,1048576,
1012,432,1792,1048576,1020,440,1864,1048576,1028,448,1936,1048576,1036,456,2008,1048576,
1084,504,2080,1048576,1092,512,2152,1048576,1100,520,2224,1048576,1108,528,2296,1048576,
1156,576,1760,8,1164,584,1768,24,1172,592,1776,40,1180,600,1784,56,
1228,648,1832,72,1236,656,1840,88,1244,664,1848,104,1252,672,1856,120,
1300,720,1904,136,1308,728,1912,152,1316,736,1920,168,1324,744,1928,184,
1372,792,1976,200,1380,800,1984,216,1388,808,1992,232,1396,816,2000,248,
1444,864,2048,1544,1452,872,2056,1560,1460,880,2064,1576,1468,888,2072,1592,
1516,936,2120,1608,1524,944,2128,1624,1532,952,2136,1640,1540,960,2144,1656,
1588,1008,2192,1672,1596,1016,2200,1688,1604,1024,2208,1704,1612,1032,2216,1720,
1660,1080,2264,1736,1668,1088,2272,1752,1676,1096,2280,1768,1684,1104,2288,1784,
1732,1152,64,3080,1740,1160,136,3096,1748,1168,208,3112,1756,1176,280,3128,
1804,1224,352,3144,1812,1232,424,3160,1820,1240,496,3176,1828,1248,568,3192,
1876,1296,640,3208,1884,1304,712,3224,1892,1312,784,3240,1900,1320,856,3256,
1948,1368,928,3272,1956,1376,1000,3288,1964,1384,1072,3304,1972,1392,1144,3320,
2020,1440,1216,1048576,2028,1448,1288,1048576,2036,1456,1360,1048576,2044,1464,1432,1048576,
2092,1512,1504,1048576,2100,1520,1576,1048576,2108,1528,1648,1048576,2116,1536,1720,1048576,
2164,1584,1792,1048576,2172,1592,1864,1048576,2180,1600,1936,1048576,2188,1608,2008,1048576,
2236,1656,2080,1048576,2244,1664,2152,1048576,2252,1672,2224,1048576,2260,1680,2296,1048576,
36,1728,1760,4608,44,1736,1768,4624,52,1744,1776,4640,60,1752,1784,4656,
108,1800,1832,4672,116,1808,1840,4688,124,1816,1848,4704,132,1824,1856,4720,
180,1872,1904,4736,188,1880,1912,4752,196,1888,1920,4768,204,1896,1928,4784,
252,1944,1976,4800,260,1952,1984,4816,268,1960,1992,4832,276,1968,2000,4848,
324,2016,2048,6144,332,2024,2056,6160,340,2032,2064,6176,348,2040,2072,6192,
396,2088,2120,6208,404,2096,2128,6224,412,2104,2136,6240,420,2112,2144,6256,
468,2160,2192,6272,476,2168,2200,6288,484,2176,2208,6304,492,2184,2216,6320,
540,2232,2264,6336,548,2240,2272,6352,556,2248,2280,6368,564,2256,2288,6384,
612,32,64,7680,620,40,136,7696,628,48,208,7712,636,56,280,7728,
684,104,352,7744,692,112,424,7760,700,120,496,7776,708,128,568,7792,
756,176,640,7808,764,184,712,7824,772,192,784,7840,780,200,856,7856,
828,248,928,7872,836,256,1000,7888,844,264,1072,7904,852,272,1144,7920,
900,320,1216,1048576,908,328,1288,1048576,916,336,1360,1048576,924,344,1432,1048576,
972,392,1504,1048576,980,400,1576,1048576,988,408,1648,1048576,996,416,1720,1048576,
1044,464,1792,1048576,1052,472,1864,1048576,1060,480,1936,1048576,1068,488,2008,1048576,
1116,536,2080,1048576,1124,544,2152,1048576,1132,552,2224,1048576,1140,560,2296,1048576,
1188,608,1760,4616,1196,616,1768,4632,1204,624,1776,4648,1212,632,1784,4664,
1260,680,1832,4680,1268,688,1840,4696,1276,696,1848,4712,1284,704,1856,4728,
1332,752,1904,4744,1340,760,1912,4760,1348,768,1920,4776,1356,776,1928,4792,
1404,824,1976,4808,1412,832,1984,4824,1420,840,1992,4840,1428,848,2000,4856,
1476,896,2048,6152,1484,904,2056,6168,1492,912,2064,6184,1500,920,2072,6200,
1548,968,2120,6216,1556,976,2128,6232,1564,984,2136,6248,1572,992,2144,6264,
1620,1040,2192,6280,1628,1048,2200,6296,1636,1056,2208,6312,1644,1064,2216,6328,
1692,1112,2264,6344,1700,1120,2272,6360,1708,1128,2280,6376,1716,1136,2288,6392,
1764,1184,64,7688,1772,1192,136,7704,1780,1200,208,7720,1788,1208,280,7736,
1836,1256,352,7752,1844,1264,424,7768,1852,1272,496,7784,1860,1280,568,7800,
1908,1328,640,7816,1916,1336,712,7832,1924,1344,784,7848,1932,1352,856,7864,
1980,1400,928,7880,1988,1408,1000,7896,1996,1416,1072,7912,2004,1424,1144,7928,
2052,1472,1216,1048576,2060,1480,1288,1048576,2068,1488,1360,1048576,2076,1496,1432,1048576,
2124,1544,1504,1048576,2132,1552,1576,1048576,2140,1560,1648,1048576,2148,1568,1720,1048576,
2196,1616,1792,1048576,2204,1624,1864,1048576,2212,1632,1936,1048576,2220,1640,2008,1048576,
2268,1688,2080,1048576,2276,1696,2152,1048576,2284,1704,2224,1048576,2292,1712,2296,1048576,
};
#define ASM_ALL \
"s_load_dwordx2 s[12:13],s[0:1],0\ns_load_dwordx4 s[16:19],s[0:1],8\ns_load_dwordx2 s[36:37],s[0:1],48\ns_load_dwordx2 s[8:9],s[0:1],24\n" \
"s_load_dwordx2 s[20:21],s[0:1],32\ns_load_dwordx2 s[24:25],s[0:1],40\ns_getpc_b64 s[96:97]\n.Lc0_%=:\n" \
"v_and_b32 v1,0xe0,v0\nv_lshlrev_b32 v2,7,v0\ns_lshr_b32 s33,s2,3\ns_mov_b32 s98,.Lend_%=-.Lc0_%=\n" \
"s_mov_b32 s99,0x20000\ns_sub_u32 s95,s33,9\nv_lshl_or_b32 v1,v1,24,v2\ns_lshl_b32 s95,s95,12\n" \
"s_mov_b32 m0,162176\nv_readfirstlane_b32 s3,v0\nbuffer_load_dword v1,s[96:99],s95 offen lds\ns_and_b32 s32,s2,7\n" \
"s_mov_b32 s15,0x20000\ns_mov_b32 s14,2359296\ns_lshr_b32 s3,s3,6\ns_lshl_b32 s34,s32,5\n" \
"v_and_b32 v13,31,v0\nv_bfe_u32 v12,v0,5,1\ns_or_b32 s34,s34,s33\nv_mul_u32_u24 v12,1179648,v12\n" \
"s_lshl_b32 s100,s3,8\nv_lshlrev_b32 v2,4,v0\ns_add_u32 s100,s100,s34\nv_lshl_add_u32 v15,v13,4,v12\n" \
"s_mul_i32 s89,s100,1152\nv_lshl_add_u32 v13,v13,2,v12\nv_mov_b32 v12,v15\nv_and_b32 v1,0x3f0,v2\n" \
"s_mov_b32 s38,134217728\ns_mov_b32 s39,0x20000\ns_cmp_gt_u32 s2,71\ns_cbranch_scc1 .Lnp_%=\n" \
"s_and_b32 s95,s32,3\ns_lshr_b32 s100,s32,2\ns_mul_i32 s95,s95,9\ns_mul_i32 s101,s100,576\n" \
"s_add_u32 s95,s95,s33\ns_mul_i32 s100,s100,1990656\ns_lshl_b32 s95,s95,6\ns_mov_b32 s22,576\n" \
"s_mov_b32 s23,0x20000\ns_mov_b32 s26,1990656\ns_mov_b32 s27,0x20000\ns_waitcnt lgkmcnt(0)\n" \
"s_add_u32 s20,s20,s101\ns_addc_u32 s21,s21,0\ns_add_u32 s24,s24,s100\ns_addc_u32 s25,s25,0\n" \
"buffer_load_dwordx2 v[126:127],off,s[36:39],0 sc1\nv_lshrrev_b32 v120,2,v0\nv_and_b32 v121,3,v0\nv_mul_u32_u24 v122,6912,v120\n" \
"v_lshlrev_b32 v123,3,v120\nv_lshl_add_u32 v121,v121,4,s95\nbuffer_load_dwordx2 v[24:25],v123,s[20:23],0 offen\nv_lshlrev_b32 v120,1,v120\n" \
"v_lshl_add_u32 v123,v122,2,v121\nbuffer_load_ushort v28,v120,s[20:23],0 offen offset:512\nv_add_u32 v121,v121,v122\nbuffer_load_dwordx4 v[80:83],v123,s[24:27],0 offen\n" \
"s_mov_b32 s95,6912\nbuffer_load_dwordx4 v[84:87],v123,s[24:27],s95 offen\ns_mov_b32 s95,13824\nbuffer_load_dwordx4 v[88:91],v123,s[24:27],s95 offen\n" \
"s_mov_b32 s95,20736\nbuffer_load_dwordx4 v[92:95],v123,s[24:27],s95 offen\ns_mov_b32 s95,1769472\nbuffer_load_dwordx4 v[96:99],v121,s[24:27],s95 offen\n" \
"s_branch .Lnq_%=\n.Lnp_%=:\ns_waitcnt lgkmcnt(0)\nbuffer_load_dwordx2 v[126:127],off,s[36:39],0 sc1\n" \
".Lnq_%=:\ns_memrealtime s[30:31]\nbuffer_load_dwordx4 v[224:227],v12,s[12:15],s89 offen\nbuffer_load_dwordx4 v[228:231],v12,s[12:15],s89 offen offset:512\n" \
"buffer_load_dword v232,v13,s[12:15],s89 offen offset:1024\ns_cmp_lt_u32 s3,2\ns_cselect_b64 s[16:17],s[16:17],s[18:19]\ns_mov_b32 s18,1183744\n" \
"s_mov_b32 s19,0x20000\ns_mul_i32 s100,s34,4624\nv_and_b32 v16,0x7f0,v2\ns_lshr_b32 s95,s3,1\n" \
"s_mul_i32 s95,s95,3168\ns_lshl_b32 s88,s3,10\ns_add_u32 s95,s95,s88\ns_add_u32 m0,s95,9792\n" \
"v_add_u32 v17,s100,v16\nbuffer_load_dwordx4 v16,s[16:19],s100 offen lds\nbuffer_load_dwordx4 v16,s[16:19],s100 offen offset:2048 lds\nv_add_u32 v17,4096,v17\n" \
"v_subrev_u32 v18,1183744,v17\ns_add_u32 m0,s95,13888\nv_min_u32 v17,v17,v18\ns_bitcmp1_b32 s3,0\n" \
"s_cselect_b64 exec,63,-1\nbuffer_load_dwordx4 v17,s[16:19],0 offen lds\ns_mov_b64 exec,-1\ns_mov_b32 s6,22528\n" \
"s_mov_b32 s7,0x20000\ns_mov_b32 s95,10240\nbuffer_load_dwordx4 v[246:249],v2,s[4:7],s95 offen\ns_cmp_gt_u32 s2,110\n" \
"s_cbranch_scc1 .LC_%=\ns_cmp_gt_u32 s2,71\ns_cbranch_scc1 .LN_%=\n.LP_%=:\n" \
"v_bfe_u32 v112,v0,3,2\nv_bfe_u32 v113,v0,5,1\nv_lshl_add_u32 v112,v112,8,v113\ns_lshl_b32 s48,s33,2\n" \
"v_lshrrev_b32 v113,3,v0\ns_add_u32 s48,s48,s3\nv_and_b32 v113,6,v113\nv_and_b32 v114,7,v0\n" \
"s_mul_i32 s48,s48,18432\ns_mul_i32 s49,s32,1152\nv_mul_u32_u24 v112,9216,v112\nv_xor_b32 v113,v113,v114\n" \
"s_add_u32 s48,s48,s49\nv_lshl_add_u32 v4,v113,4,v112\ns_mov_b32 s10,18874368\ns_mov_b32 s11,0x20000\n" \
"s_add_u32 s48,s48,1024\ns_add_u32 m0,s88,20224\ns_add_u32 s50,s48,9437184\nbuffer_load_dwordx4 v4,s[8:11],s48 offen lds\n" \
"s_add_u32 m0,s88,24320\nv_add_u32 v5,17408,v4\nbuffer_load_dwordx4 v4,s[8:11],s50 offen lds\nv_add_u32 v6,34816,v4\n" \
"v_add_u32 v7,52224,v4\nv_lshlrev_b32 v158,3,v0\nv_add_u32 v158,161152,v158\ns_mul_i32 s63,s32,9\n" \
"s_add_u32 s63,s63,s33\nv_bfrev_b32 v159,1\nv_cmp_gt_u32 s[64:65],16,v0\ns_lshl_b32 s86,s63,8\n" \
"s_add_u32 s63,s86,8192\nv_cndmask_b32 v159,v159,v2,s[64:65]\ns_mov_b32 s51,4096\ns_add_u32 m0,s88,28416\n" \
"s_waitcnt vmcnt(15)\nv_lshlrev_b32 v128,16,v24\nv_mul_f32 v129,0xbfb8aa3b,v128\nv_exp_f32 v129,v129\n" \
"v_and_b32 v130,0xffff0000,v24\nv_add_f32 v129,1.0,v129\nv_mul_f32 v131,0xbfb8aa3b,v130\nv_lshlrev_b32 v132,16,v25\n" \
"v_rcp_f32 v129,v129\nv_exp_f32 v131,v131\nv_mul_f32 v133,0xbfb8aa3b,v132\nv_exp_f32 v133,v133\n" \
"v_mul_f32 v128,v128,v129\nv_add_f32 v131,1.0,v131\nv_and_b32 v134,0xffff0000,v25\nv_cvt_pk_bf16_f32 v128,v128,v128\n" \
"v_rcp_f32 v131,v131\nv_add_f32 v133,1.0,v133\nv_mul_f32 v135,0xbfb8aa3b,v134\nv_lshlrev_b32 v128,16,v128\n" \
"s_waitcnt vmcnt(14)\nv_lshlrev_b32 v136,16,v28\nv_rcp_f32 v133,v133\nv_exp_f32 v135,v135\n" \
"v_mul_f32 v137,0xbfb8aa3b,v136\nv_mul_f32 v130,v130,v131\nv_exp_f32 v137,v137\nv_cvt_pk_bf16_f32 v130,v130,v130\n" \
"v_mul_f32 v132,v132,v133\nv_add_f32 v135,1.0,v135\nv_lshlrev_b32 v130,16,v130\nv_cvt_pk_bf16_f32 v132,v132,v132\n" \
"v_rcp_f32 v135,v135\nv_add_f32 v137,1.0,v137\nv_lshlrev_b32 v132,16,v132\nv_rcp_f32 v137,v137\n" \
"v_mul_f32 v134,v134,v135\nv_cvt_pk_bf16_f32 v134,v134,v134\nv_mul_f32 v136,v136,v137\nv_lshlrev_b32 v134,16,v134\n" \
"v_cvt_pk_bf16_f32 v136,v136,v136\nv_lshlrev_b32 v136,16,v136\ns_mov_b32 s52,8192\ns_mov_b32 s53,14336\n" \
"s_mul_i32 s57,s3,30720\ns_add_u32 s57,s57,38272\ns_and_b32 s60,s33,7\ns_mul_i32 s92,s3,589824\n" \
"s_add_u32 s92,s92,s49\ns_lshl_b32 s62,s60,7\ns_add_u32 s92,s92,s62\ns_load_dwordx4 s[44:47],s[0:1],72\n" \
"v_mov_b32 v16,-1\nv_mov_b32 v20,0\nv_mov_b32 v17,-1\nv_mov_b32 v21,0\n" \
"v_mov_b32 v18,-1\nv_mov_b32 v22,0\nv_mov_b32 v19,-1\nv_mov_b32 v23,0\n" \
"s_mul_i32 s54,s34,9344\ns_add_u32 s35,s54,524288\ns_add_u32 s55,s35,4096\nv_and_b32 v112,15,v0\n" \
"v_bfe_u32 v113,v0,4,2\nv_mul_u32_u24 v113,589824,v113\nv_lshl_add_u32 v113,v112,4,v113\ns_lshl_b32 s56,s34,8\n" \
"s_add_u32 s56,s56,10289152\ns_cmp_eq_u32 s3,2\ns_cselect_b32 s56,s56,2147483648\nv_add_u32 v113,s56,v113\n" \
"s_add_u32 s58,s35,8192\nv_add_u32 v114,s58,v2\nv_cmp_gt_u32 vcc,68,v0\ns_lshr_b32 s59,s33,3\n" \
"s_lshl_b32 s59,s59,3\nv_cndmask_b32 v113,v113,v114,vcc\ns_add_u32 s59,s59,s32\ns_lshl_b32 s59,s59,3\n" \
"s_add_u32 s59,s59,s60\ns_lshl_b32 s59,s59,7\ns_add_u32 s87,s59,262144\nv_lshrrev_b32 v115,3,v0\n" \
"v_and_b32 v116,0x70,v2\nv_lshl_add_u32 v115,v115,16,v116\nv_add_u32 v115,s87,v115\ns_lshl_b32 s61,s34,4\n" \
"s_add_u32 s61,s61,65536\nv_mov_b32 v116,s61\nv_cmp_gt_u32 vcc,24,v0\nv_and_b32 v118,15,v0\n" \
"v_bfe_u32 v119,v0,4,2\nv_cndmask_b32 v115,v116,v115,vcc\nv_bfrev_b32 v116,1\nv_cmp_gt_u32 vcc,25,v0\n" \
"v_lshlrev_b32 v3,4,v119\nv_lshl_add_u32 v3,v118,6,v3\nv_cndmask_b32 v115,v116,v115,vcc\nv_lshrrev_b32 v15,1,v1\n" \
"v_add_u32 v15,s57,v15\nv_and_b32 v120,0x3c0,v1\nv_bfe_u32 v121,v0,3,2\nv_and_b32 v122,3,v0\n" \
"v_lshlrev_b32 v120,1,v120\nv_lshl_add_u32 v120,v121,5,v120\nv_lshl_add_u32 v120,v122,3,v120\nv_xor_b32 v8,0,v120\n" \
"v_add_u32 v8,s57,v8\nv_xor_b32 v9,32,v120\nv_add_u32 v9,s57,v9\nv_xor_b32 v10,64,v120\n" \
"v_add_u32 v10,s57,v10\nv_xor_b32 v11,96,v120\nv_add_u32 v11,s57,v11\nv_cvt_f32_u32 v120,s34\n" \
"v_lshrrev_b32 v48,1,v118\nv_cvt_f32_u32 v48,v48\nv_and_b32 v49,1,v119\nv_cvt_f32_u32 v49,v49\n" \
"v_mul_f32 v48,0xbe000000,v48\nv_mul_f32 v50,0x3b800000,v120\nv_fma_f32 v49,v49,2.0,v50\nv_mul_f32 v49,v48,v49\n" \
"v_lshrrev_b32 v50,1,v119\nv_and_b32 v51,1,v0\nv_cmp_eq_u32 vcc,v50,v51\nv_mov_b32 v50,0x3e800000\n" \
"v_lshlrev_b32 v51,31,v0\nv_cndmask_b32 v52,0,v50,vcc\nv_xor_b32 v54,v51,v52\nv_cos_f32 v56,v49\n" \
"v_sin_f32 v57,v49\nv_fmamk_f32 v63,v48,0x40800000,v49\nv_pk_mul_f32 v[58:59],v[56:57],v[52:53] op_sel_hi:[1,0]\nv_pk_mul_f32 v[60:61],v[56:57],v[54:55] op_sel_hi:[1,0]\n" \
"v_cvt_pk_f16_f32 v40,v58,-v61\nv_cvt_pk_f16_f32 v44,v59,v60\nv_cos_f32 v62,v63\nv_sin_f32 v63,v63\n" \
"v_fmamk_f32 v57,v48,0x3f800000,v49\nv_pk_mul_f32 v[64:65],v[62:63],v[52:53] op_sel_hi:[1,0]\nv_pk_mul_f32 v[66:67],v[62:63],v[54:55] op_sel_hi:[1,0]\nv_cvt_pk_f16_f32 v41,v64,-v67\n" \
"v_cvt_pk_f16_f32 v45,v65,v66\nv_cos_f32 v56,v57\nv_sin_f32 v57,v57\nv_fmamk_f32 v63,v48,0x40a00000,v49\n" \
"v_pk_mul_f32 v[58:59],v[56:57],v[52:53] op_sel_hi:[1,0]\nv_pk_mul_f32 v[60:61],v[56:57],v[54:55] op_sel_hi:[1,0]\nv_cvt_pk_f16_f32 v42,v58,-v61\nv_cvt_pk_f16_f32 v46,v59,v60\n" \
"v_cos_f32 v62,v63\nv_sin_f32 v63,v63\nv_bfe_u32 v100,v0,1,2\nv_pk_mul_f32 v[64:65],v[62:63],v[52:53] op_sel_hi:[1,0]\n" \
"v_pk_mul_f32 v[66:67],v[62:63],v[54:55] op_sel_hi:[1,0]\nv_cvt_pk_f16_f32 v43,v64,-v67\nv_cvt_pk_f16_f32 v47,v65,v66\nv_and_b32 v101,1,v0\n" \
"v_lshl_add_u32 v100,v101,2,v100\nv_cvt_f32_u32 v100,v100\nv_mul_f32 v101,0x3a000000,v120\nv_fmamk_f32 v100,v100,0x3e000000,v101\n" \
"v_cvt_f32_u32 v101,v119\nv_add_f32 v101,v101,v101\nv_mul_f32 v101,v101,v100\nv_cmp_gt_u32 vcc,8,v118\n" \
"v_cos_f32 v106,v101\nv_sin_f32 v107,v101\nv_cndmask_b32 v102,0,0.5,vcc\nv_sub_f32_e64 v104,0.5,v102\n" \
"v_pk_mul_f32 v[108:109],v[106:107],v[102:103] op_sel_hi:[1,0]\nv_pk_mul_f32 v[110:111],v[106:107],v[104:105] op_sel_hi:[1,0]\nv_cvt_pk_f16_f32 v216,v108,-v109\nv_cvt_pk_f16_f32 v220,v109,v108\n" \
"v_cvt_pk_f16_f32 v217,v110,-v111\nv_cvt_pk_f16_f32 v221,-v111,-v110\nv_fmamk_f32 v107,v100,0x3f800000,v101\nv_cos_f32 v106,v107\n" \
"v_sin_f32 v107,v107\ns_nop 0\nv_pk_mul_f32 v[108:109],v[106:107],v[102:103] op_sel_hi:[1,0]\nv_pk_mul_f32 v[110:111],v[106:107],v[104:105] op_sel_hi:[1,0]\n" \
"v_cvt_pk_f16_f32 v218,v108,-v109\nv_cvt_pk_f16_f32 v222,v109,v108\nv_cvt_pk_f16_f32 v219,v110,-v111\nv_cvt_pk_f16_f32 v223,-v111,-v110\n" \
"v_readfirstlane_b32 s28,v126\nv_readfirstlane_b32 s29,v127\ns_add_u32 s28,s28,1\ns_addc_u32 s29,s29,0\n" \
"v_mov_b32 v168,s28\nv_mov_b32 v169,s29\ns_waitcnt vmcnt(13)\nv_and_b32 v149,0xffff0000,v80\n" \
"v_and_b32 v151,0xffff0000,v81\nv_lshlrev_b32 v148,16,v80\nv_lshlrev_b32 v150,16,v81\nv_pk_mul_f32 v[140:141],v[148:149],v[128:129] op_sel_hi:[1,0]\n" \
"v_pk_mul_f32 v[142:143],v[150:151],v[128:129] op_sel_hi:[1,0]\nv_and_b32 v149,0xffff0000,v82\nv_and_b32 v151,0xffff0000,v83\nv_lshlrev_b32 v148,16,v82\n" \
"v_lshlrev_b32 v150,16,v83\nv_pk_mul_f32 v[144:145],v[148:149],v[128:129] op_sel_hi:[1,0]\nv_pk_mul_f32 v[146:147],v[150:151],v[128:129] op_sel_hi:[1,0]\ns_waitcnt vmcnt(12)\n" \
"v_and_b32 v153,0xffff0000,v84\nv_and_b32 v155,0xffff0000,v85\nv_lshlrev_b32 v152,16,v84\nv_lshlrev_b32 v154,16,v85\n" \
"v_pk_fma_f32 v[140:141],v[152:153],v[130:131],v[140:141] op_sel_hi:[1,0,1]\nv_pk_fma_f32 v[142:143],v[154:155],v[130:131],v[142:143] op_sel_hi:[1,0,1]\ns_waitcnt vmcnt(11)\nv_and_b32 v149,0xffff0000,v88\n" \
"v_and_b32 v151,0xffff0000,v89\nv_lshlrev_b32 v148,16,v88\nv_lshlrev_b32 v150,16,v89\nv_and_b32 v153,0xffff0000,v86\n" \
"v_and_b32 v155,0xffff0000,v87\nv_pk_fma_f32 v[140:141],v[148:149],v[132:133],v[140:141] op_sel_hi:[1,0,1]\nv_pk_fma_f32 v[142:143],v[150:151],v[132:133],v[142:143] op_sel_hi:[1,0,1]\nv_lshlrev_b32 v152,16,v86\n" \
"v_lshlrev_b32 v154,16,v87\nv_pk_fma_f32 v[144:145],v[152:153],v[130:131],v[144:145] op_sel_hi:[1,0,1]\nv_pk_fma_f32 v[146:147],v[154:155],v[130:131],v[146:147] op_sel_hi:[1,0,1]\nv_and_b32 v149,0xffff0000,v90\n" \
"v_and_b32 v151,0xffff0000,v91\nv_lshlrev_b32 v148,16,v90\nv_lshlrev_b32 v150,16,v91\nv_pk_fma_f32 v[144:145],v[148:149],v[132:133],v[144:145] op_sel_hi:[1,0,1]\n" \
"v_pk_fma_f32 v[146:147],v[150:151],v[132:133],v[146:147] op_sel_hi:[1,0,1]\ns_waitcnt vmcnt(10)\nv_and_b32 v153,0xffff0000,v92\nv_and_b32 v155,0xffff0000,v93\n" \
"v_lshlrev_b32 v152,16,v92\nv_lshlrev_b32 v154,16,v93\nv_pk_fma_f32 v[140:141],v[152:153],v[134:135],v[140:141] op_sel_hi:[1,0,1]\nv_pk_fma_f32 v[142:143],v[154:155],v[134:135],v[142:143] op_sel_hi:[1,0,1]\n" \
"s_waitcnt vmcnt(9)\nv_and_b32 v149,0xffff0000,v96\nv_and_b32 v151,0xffff0000,v97\nv_lshlrev_b32 v148,16,v96\n" \
"v_lshlrev_b32 v150,16,v97\nv_and_b32 v153,0xffff0000,v94\nv_and_b32 v155,0xffff0000,v95\nv_pk_fma_f32 v[140:141],v[148:149],v[136:137],v[140:141] op_sel_hi:[1,0,1]\n" \
"v_pk_fma_f32 v[142:143],v[150:151],v[136:137],v[142:143] op_sel_hi:[1,0,1]\nv_lshlrev_b32 v152,16,v94\nv_lshlrev_b32 v154,16,v95\nv_pk_fma_f32 v[144:145],v[152:153],v[134:135],v[144:145] op_sel_hi:[1,0,1]\n" \
"v_pk_fma_f32 v[146:147],v[154:155],v[134:135],v[146:147] op_sel_hi:[1,0,1]\nv_and_b32 v149,0xffff0000,v98\nv_and_b32 v151,0xffff0000,v99\nv_lshlrev_b32 v148,16,v98\n" \
"v_lshlrev_b32 v150,16,v99\nv_pk_fma_f32 v[144:145],v[148:149],v[136:137],v[144:145] op_sel_hi:[1,0,1]\nv_pk_fma_f32 v[146:147],v[150:151],v[136:137],v[146:147] op_sel_hi:[1,0,1]\nv_and_b32 v148,3,v0\n" \
"v_permlane32_swap_b32 v140,v144\nv_permlane32_swap_b32 v141,v145\nv_permlane32_swap_b32 v142,v146\nv_permlane32_swap_b32 v143,v147\n" \
"v_pk_add_f32 v[140:141],v[140:141],v[144:145]\nv_pk_add_f32 v[142:143],v[142:143],v[146:147]\nv_lshrrev_b32 v149,4,v0\nv_and_b32 v150,12,v149\n" \
"v_permlane16_swap_b32 v140,v142\nv_permlane16_swap_b32 v141,v143\nv_lshlrev_b32 v148,5,v148\nv_pk_add_f32 v[140:141],v[140:141],v[142:143]\n" \
"v_mad_u32_u24 v148,v150,24,v148\nv_lshl_add_u32 v148,v149,3,v148\nv_add_f32_dpp v140,v140,v140 row_ror:8 row_mask:0xf bank_mask:0xf bound_ctrl:1\nv_add_f32_dpp v141,v141,v141 row_ror:8 row_mask:0xf bank_mask:0xf bound_ctrl:1\n" \
"v_add_u32 v148,161152,v148\nv_add_f32_dpp v140,v140,v140 row_ror:4 row_mask:0xf bank_mask:0xf bound_ctrl:1\nv_add_f32_dpp v141,v141,v141 row_ror:4 row_mask:0xf bank_mask:0xf bound_ctrl:1\nds_write_b64 v148,v[140:141]\n" \
"s_waitcnt lgkmcnt(0)\ns_barrier\nds_read_b64 v[160:161],v158\nds_read_b64 v[162:163],v158 offset:128\n" \
"ds_read_b64 v[164:165],v158 offset:256\nds_read_b64 v[166:167],v158 offset:384\ns_waitcnt lgkmcnt(2)\nv_pk_add_f32 v[160:161],v[160:161],v[162:163]\n" \
"s_waitcnt lgkmcnt(0)\nv_pk_add_f32 v[164:165],v[164:165],v[166:167]\nv_pk_add_f32 v[170:171],v[160:161],v[164:165]\nbuffer_store_dwordx4 v[168:171],v159,s[36:39],s63 offen sc1\n" \
"buffer_load_dwordx4 v2,s[4:7],0 offen lds\ns_add_u32 m0,s88,32512\ns_nop 0\nbuffer_load_dwordx4 v2,s[4:7],s51 offen lds\n" \
"buffer_load_dwordx4 v[234:237],v1,s[4:7],s52 offen\nbuffer_load_dwordx4 v[238:241],v1,s[4:7],s52 offen offset:1024\nbuffer_load_dwordx4 v[242:245],v2,s[4:7],s53 offen\ns_mov_b32 s52,18432\n" \
"buffer_load_dwordx4 v[252:255],v2,s[4:7],s52 offen\nbuffer_store_dwordx4 v[16:19],v2,s[36:39],s35 offen sc1\nbuffer_store_dwordx4 v[16:19],v2,s[36:39],s55 offen sc1\nbuffer_store_dwordx4 v[16:19],v113,s[36:39],0 offen sc1\n" \
"buffer_store_dwordx4 v[20:23],v115,s[36:39],0 offen sc1\nv_lshrrev_b32 v77,5,v0\nv_bfe_u32 v113,v0,1,5\nv_and_b32 v115,1,v0\n" \
"s_lshl_b32 s63,s3,1\nv_mul_u32_u24 v77,1160,v77\nv_and_b32 v76,31,v0\nv_and_b32 v114,6,v113\n" \
"v_add_u32 v115,s63,v115\nv_mad_u32_u24 v78,v76,36,v77\nv_xor_b32 v114,v114,v115\nv_lshlrev_b32 v113,7,v113\n" \
"v_lshl_add_u32 v113,v114,4,v113\ns_add_u32 s64,s54,2916352\ns_add_u32 s65,s64,4096\nv_bfrev_b32 v114,1\n" \
"v_cmp_gt_u32 vcc,72,v0\ns_lshl_b32 s67,s32,2\ns_add_u32 s66,s64,8192\nv_cndmask_b32 v114,v114,v2,vcc\n" \
"s_add_u32 s67,s67,s3\ns_lshl_b32 s67,s67,5\ns_add_u32 s67,s67,s33\ns_lshl_b32 s67,s67,11\n" \
"s_add_u32 s67,s67,5308416\ns_lshl2_add_u32 s69,s3,161664\ns_mul_i32 s72,s3,2320\nv_mov_b32 v251,s69\n" \
"v_cmp_gt_u32 vcc,32,v0\ns_lshl_b32 s7,s32,11\ns_mov_b32 s93,128\nv_cndmask_b32 v74,2.0,0,vcc\n" \
"s_mov_b32 s73,8192\ns_mov_b32 s74,12288\ns_mov_b32 s75,17408\ns_mov_b32 s76,21504\n" \
"s_mov_b32 s77,16384\ns_mov_b32 s78,25600\ns_add_u32 s7,s7,40960\ns_mov_b32 s81,0x3d800000\n" \
"s_mov_b32 s42,18874368\ns_mov_b32 s43,0x20000\ns_waitcnt vmcnt(7)\nv_lshrrev_b32 v96,1,v234\n" \
"v_lshrrev_b32 v97,1,v235\nv_lshrrev_b32 v98,1,v236\nv_lshrrev_b32 v99,1,v237\nv_add_u32 v96,s72,v96\n" \
"s_waitcnt vmcnt(6)\nv_lshrrev_b32 v100,1,v238\nv_lshrrev_b32 v101,1,v239\nv_lshrrev_b32 v102,1,v240\n" \
"v_lshrrev_b32 v103,1,v241\nv_add_u32 v97,s72,v97\nv_add_u32 v98,s72,v98\nv_add_u32 v99,s72,v99\n" \
"v_add_u32 v100,s72,v100\nv_add_u32 v101,s72,v101\nv_add_u32 v102,s72,v102\nv_add_u32 v103,s72,v103\n" \
"v_lshlrev_b32 v48,16,v224\nv_and_b32 v49,0xffff0000,v224\nv_readlane_b32 s70,v48,0\nv_readlane_b32 s71,v48,32\n" \
"v_and_b32 v51,0xffff0000,v225\nv_mov_b32 v72,s70\nv_mov_b32 v73,s71\nv_lshlrev_b32 v50,16,v225\n" \
"v_and_b32 v53,0xffff0000,v226\nv_permlane32_swap_b32 v72,v73\nv_pk_add_f32 v[48:49],v[48:49],v[72:73] op_sel_hi:[1,0] neg_lo:[0,1] neg_hi:[0,1]\nv_pk_add_f32 v[50:51],v[50:51],v[72:73] op_sel_hi:[1,0] neg_lo:[0,1] neg_hi:[0,1]\n" \
"v_pk_mul_f32 v[68:69],v[48:49],v[48:49]\nv_lshlrev_b32_e64 v52,16,v226\nv_and_b32 v55,0xffff0000,v227\nv_pk_add_f32 v[52:53],v[52:53],v[72:73] op_sel_hi:[1,0] neg_lo:[0,1] neg_hi:[0,1]\n" \
"v_pk_add_f32 v[66:67],v[48:49],v[50:51]\nv_pk_fma_f32 v[68:69],v[50:51],v[50:51],v[68:69]\nv_lshlrev_b32_e64 v54,16,v227\nv_and_b32 v57,0xffff0000,v228\n" \
"v_pk_add_f32 v[54:55],v[54:55],v[72:73] op_sel_hi:[1,0] neg_lo:[0,1] neg_hi:[0,1]\nv_pk_add_f32 v[66:67],v[66:67],v[52:53]\nv_pk_fma_f32 v[68:69],v[52:53],v[52:53],v[68:69]\nv_lshlrev_b32_e64 v56,16,v228\n" \
"v_and_b32 v59,0xffff0000,v229\nv_pk_add_f32 v[56:57],v[56:57],v[72:73] op_sel_hi:[1,0] neg_lo:[0,1] neg_hi:[0,1]\nv_pk_add_f32 v[66:67],v[66:67],v[54:55]\nv_pk_fma_f32 v[68:69],v[54:55],v[54:55],v[68:69]\n" \
"v_lshlrev_b32_e64 v58,16,v229\nv_and_b32 v61,0xffff0000,v230\nv_pk_add_f32 v[58:59],v[58:59],v[72:73] op_sel_hi:[1,0] neg_lo:[0,1] neg_hi:[0,1]\nv_pk_add_f32 v[66:67],v[66:67],v[56:57]\n" \
"v_pk_fma_f32 v[68:69],v[56:57],v[56:57],v[68:69]\nv_lshlrev_b32_e64 v60,16,v230\nv_and_b32 v63,0xffff0000,v231\nv_pk_add_f32 v[60:61],v[60:61],v[72:73] op_sel_hi:[1,0] neg_lo:[0,1] neg_hi:[0,1]\n" \
"v_pk_add_f32 v[66:67],v[66:67],v[58:59]\nv_pk_fma_f32 v[68:69],v[58:59],v[58:59],v[68:69]\nv_lshlrev_b32_e64 v62,16,v231\nv_and_b32 v65,0xffff0000,v232\n" \
"v_pk_add_f32 v[62:63],v[62:63],v[72:73] op_sel_hi:[1,0] neg_lo:[0,1] neg_hi:[0,1]\nv_pk_add_f32 v[66:67],v[66:67],v[60:61]\nv_pk_fma_f32 v[68:69],v[60:61],v[60:61],v[68:69]\nv_lshlrev_b32_e64 v64,16,v232\n" \
"v_pk_add_f32 v[64:65],v[64:65],v[72:73] op_sel_hi:[1,0] neg_lo:[0,1] neg_hi:[0,1]\nv_pk_add_f32 v[66:67],v[66:67],v[62:63]\nv_pk_fma_f32 v[68:69],v[62:63],v[62:63],v[68:69]\nv_pk_add_f32 v[66:67],v[66:67],v[64:65]\n" \
"v_pk_fma_f32 v[68:69],v[64:65],v[64:65],v[68:69]\nv_add_f32 v66,v66,v67\nv_add_f32 v68,v68,v69\ns_mov_b64 s[40:41],s[44:45]\n" \
"s_mov_b64 s[44:45],s[46:47]\nv_permlane16_swap_b32 v66,v68\nv_add_f32 v66,v66,v68\ns_mov_b32 s46,2359296\n" \
"s_mov_b32 s47,0x20000\nv_add_f32_dpp v66,v66,v66 quad_perm:[1,0,3,2] row_mask:0xf bank_mask:0xf bound_ctrl:1\ns_nop 1\nv_add_f32_dpp v66,v66,v66 quad_perm:[2,3,0,1] row_mask:0xf bank_mask:0xf bound_ctrl:1\n" \
"s_nop 1\nv_add_f32_dpp v66,v66,v66 row_half_mirror row_mask:0xf bank_mask:0xf bound_ctrl:1\ns_nop 1\nv_add_f32_dpp v66,v66,v66 row_mirror row_mask:0xf bank_mask:0xf bound_ctrl:1\n" \
"v_mul_f32 v70,0x3ae38e39,v66\nv_mul_f32 v68,0x3ae38e39,v66\ns_nop 1\nv_permlane16_swap_b32 v70,v68\n" \
"v_fma_f32 v68,-v70,v70,v68\nv_max_f32 v68,0,v68\nv_add_f32 v68,0x3727c5ac,v68\nv_rsq_f32 v66,v68\n" \
"v_add_f32 v250,v70,v72\nv_mul_f32 v67,-v70,v66\nv_pk_fma_f32 v[48:49],v[48:49],v[66:67],v[66:67] op_sel:[0,0,1] op_sel_hi:[1,0,1]\nv_pk_fma_f32 v[50:51],v[50:51],v[66:67],v[66:67] op_sel:[0,0,1] op_sel_hi:[1,0,1]\n" \
"v_cvt_pk_f16_f32 v80,v48,v49\nv_cvt_pk_f16_f32 v81,v50,v51\nv_pk_fma_f32 v[52:53],v[52:53],v[66:67],v[66:67] op_sel:[0,0,1] op_sel_hi:[1,0,1]\nv_pk_fma_f32 v[54:55],v[54:55],v[66:67],v[66:67] op_sel:[0,0,1] op_sel_hi:[1,0,1]\n" \
"ds_write2_b32 v78,v80,v81 offset0:0 offset1:1\nv_cvt_pk_f16_f32 v82,v52,v53\nv_cvt_pk_f16_f32 v83,v54,v55\nv_pk_fma_f32 v[56:57],v[56:57],v[66:67],v[66:67] op_sel:[0,0,1] op_sel_hi:[1,0,1]\n" \
"v_pk_fma_f32 v[58:59],v[58:59],v[66:67],v[66:67] op_sel:[0,0,1] op_sel_hi:[1,0,1]\nds_write2_b32 v78,v82,v83 offset0:2 offset1:3\nv_cvt_pk_f16_f32 v84,v56,v57\nv_cvt_pk_f16_f32 v85,v58,v59\n" \
"v_pk_fma_f32 v[60:61],v[60:61],v[66:67],v[66:67] op_sel:[0,0,1] op_sel_hi:[1,0,1]\nv_pk_fma_f32 v[62:63],v[62:63],v[66:67],v[66:67] op_sel:[0,0,1] op_sel_hi:[1,0,1]\nds_write2_b32 v78,v84,v85 offset0:4 offset1:5\nv_cvt_pk_f16_f32 v86,v60,v61\n" \
"v_cvt_pk_f16_f32 v87,v62,v63\nv_pk_fma_f32 v[64:65],v[64:65],v[66:67],v[66:67] op_sel:[0,0,1] op_sel_hi:[1,0,1]\nds_write2_b32 v78,v86,v87 offset0:6 offset1:7\nv_cvt_pk_f16_f32 v88,v64,v65\n" \
"ds_write_b32 v78,v88 offset:32\ns_barrier\nds_read_u16 v16,v246\nds_read_u16 v17,v246 offset:5216\n" \
"ds_read_u16 v18,v246 offset:578\nds_read_u16 v19,v246 offset:5794\nds_read_u16 v20,v246 offset:1156\nds_read_u16 v21,v246 offset:6372\n" \
"ds_read_u16 v22,v246 offset:1734\nds_read_u16 v23,v246 offset:6950\nds_read_u16 v24,v247\ns_waitcnt lgkmcnt(7)\n" \
"v_lshl_or_b32 v16,v17,16,v16\nds_read_u16 v25,v247 offset:5216\nds_read_u16 v26,v247 offset:578\ns_waitcnt lgkmcnt(7)\n" \
"v_lshl_or_b32 v17,v19,16,v18\nv_pk_maximum3_f16 v124,v16,v17,v16\nds_read_u16 v27,v247 offset:5794\nv_pk_minimum3_f16 v125,v16,v17,v16\n" \
"ds_read_u16 v28,v247 offset:1156\ns_waitcnt lgkmcnt(7)\nv_lshl_or_b32 v18,v21,16,v20\nds_read_u16 v29,v247 offset:6372\n" \
"ds_read_u16 v30,v247 offset:1734\ns_waitcnt lgkmcnt(7)\nv_lshl_or_b32 v19,v23,16,v22\nbuffer_store_dwordx4 v[16:19],v2,s[36:39],s64 offen sc1\n" \
"ds_read_u16 v31,v247 offset:6950\nds_read_u16 v104,v248\nv_pk_maximum3_f16 v124,v18,v19,v124\nv_pk_minimum3_f16 v125,v18,v19,v125\n" \
"s_waitcnt lgkmcnt(7)\nv_lshl_or_b32 v24,v25,16,v24\nds_read_u16 v105,v248 offset:5216\nds_read_u16 v106,v248 offset:578\n" \
"s_waitcnt lgkmcnt(7)\nv_lshl_or_b32 v25,v27,16,v26\nv_pk_maximum3_f16 v124,v24,v25,v124\nds_read_u16 v107,v248 offset:5794\n" \
"v_pk_minimum3_f16 v125,v24,v25,v125\nds_read_u16 v108,v248 offset:1156\ns_waitcnt lgkmcnt(7)\nv_lshl_or_b32 v26,v29,16,v28\n" \
"ds_read_u16 v109,v248 offset:6372\nds_read_u16 v110,v248 offset:1734\ns_waitcnt lgkmcnt(7)\nv_lshl_or_b32 v27,v31,16,v30\n" \
"buffer_store_dwordx4 v[24:27],v2,s[36:39],s65 offen sc1\nds_read_u16 v111,v248 offset:6950\nds_read_b128 v[116:119],v113 offset:20224\nv_pk_maximum3_f16 v124,v26,v27,v124\n" \
"v_pk_minimum3_f16 v125,v26,v27,v125\nds_read_b128 v[120:123],v113 offset:24320\ns_waitcnt lgkmcnt(8)\nv_lshl_or_b32 v104,v105,16,v104\n" \
"ds_read_b32 v16,v96\nds_read_b32 v24,v96 offset:1160\nds_read_b32 v17,v97\nds_read_b32 v25,v97 offset:1160\n" \
"s_waitcnt lgkmcnt(10)\nv_lshl_or_b32 v105,v107,16,v106\nds_read_b32 v18,v98\nv_pk_maximum3_f16 v124,v104,v105,v124\n" \
"ds_read_b32 v26,v98 offset:1160\nv_pk_minimum3_f16 v125,v104,v105,v125\nds_read_b32 v19,v99\nds_read_b32 v27,v99 offset:1160\n" \
"ds_read_b32 v20,v100\ns_waitcnt lgkmcnt(13)\nv_lshl_or_b32 v106,v109,16,v108\nds_read_b32 v28,v100 offset:1160\n" \
"ds_read_b32 v21,v101\nds_read_b32 v29,v101 offset:1160\ns_waitcnt lgkmcnt(14)\nv_lshl_or_b32 v107,v111,16,v110\n" \
"buffer_store_dwordx4 v[104:107],v114,s[36:39],s66 offen sc1\ns_waitcnt lgkmcnt(13)\nbuffer_store_dwordx4 v[116:119],v1,s[36:39],s67 offen sc1\ns_waitcnt lgkmcnt(12)\n" \
"buffer_store_dwordx4 v[120:123],v1,s[36:39],s67 offen offset:1024 sc1\nds_read_b32 v22,v102\nds_read_b32 v30,v102 offset:1160\nds_read_b32 v23,v103\n" \
"ds_read_b32 v31,v103 offset:1160\nv_pk_maximum3_f16 v124,v106,v107,v124\nv_and_b32 v124,0x7fff7fff,v124\nv_pk_minimum3_f16 v125,v106,v107,v125\n" \
"v_and_b32 v125,0x7fff7fff,v125\nv_pk_max_u16 v124,v124,v125\nv_lshlrev_b32 v125,16,v124\nv_max_u32 v124,v124,v125\n" \
"s_nop 1\nv_max_u32_dpp v124,v124,v124 quad_perm:[1,0,3,2] row_mask:0xf bank_mask:0xf bound_ctrl:1\ns_nop 1\nv_max_u32_dpp v124,v124,v124 quad_perm:[2,3,0,1] row_mask:0xf bank_mask:0xf bound_ctrl:1\n" \
"s_nop 1\nv_max_u32_dpp v124,v124,v124 row_half_mirror row_mask:0xf bank_mask:0xf bound_ctrl:1\ns_nop 1\nv_max_u32_dpp v124,v124,v124 row_mirror row_mask:0xf bank_mask:0xf bound_ctrl:1\n" \
"s_nop 0\nv_readlane_b32 s68,v124,0\nv_readlane_b32 s100,v124,16\nv_readlane_b32 s101,v124,32\n" \
"v_readlane_b32 s31,v124,48\ns_max_u32 s100,s100,s101\ns_max_u32 s68,s68,s31\ns_max_u32 s68,s68,s100\n" \
"v_mov_b32 v124,s68\nds_write_b32 v251,v124 offset:32\nv_mov_b32 v114,161664\nbuffer_load_dwordx4 v[48:51],v2,s[36:39],s73 offen sc1\n" \
"buffer_load_dwordx4 v[60:63],v2,s[36:39],s75 offen sc1\nbuffer_load_dwordx4 v[52:55],v2,s[36:39],s74 offen sc1\nbuffer_load_dwordx4 v[64:67],v2,s[36:39],s76 offen sc1\nbuffer_load_dwordx4 v[56:59],v1,s[36:39],s77 offen sc1\n" \
"buffer_load_dwordx4 v[68:71],v1,s[36:39],s78 offen sc1\ns_mov_b32 s79,0xffffff\ns_lshl3_add_u32 s80,s2,4294615040\nv_add_u32 v112,28416,v1\n" \
"s_waitcnt vmcnt(6) lgkmcnt(0)\ns_barrier\nds_read_b128 v[116:119],v114 offset:32\nds_read_b128 v[192:195],v112\n" \
"ds_read_b128 v[196:199],v112 offset:1024\nds_read_b128 v[200:203],v112 offset:2048\nds_read_b128 v[204:207],v112 offset:3072\nds_read_b128 v[208:211],v112 offset:4096\n" \
"ds_read_b128 v[212:215],v112 offset:5120\nds_read_b128 v[32:35],v112 offset:6144\nds_read_b128 v[36:39],v112 offset:7168\ns_waitcnt lgkmcnt(8)\n" \
"v_max3_u32 v116,v116,v117,v118\nv_max_u32 v116,v116,v119\nv_and_b32 v119,0xf8,v0\nv_cmp_ne_u32 vcc,192,v119\n" \
"v_lshlrev_b32 v116,1,v116\nv_mov_b32 v117,s29\nv_lshlrev_b32 v118,11,v0\nv_bfi_b32 v117,s79,v117,v116\n" \
"v_add_u32 v118,s80,v118\nv_bfrev_b32 v119,1\nv_cndmask_b32 v118,v118,v119,vcc\nv_mov_b32 v116,s28\n" \
"buffer_store_dwordx2 v[116:117],v118,s[36:39],0 offen sc1\ns_waitcnt vmcnt(6)\nv_cmp_ne_u64 vcc,s[28:29],v[48:49]\ns_waitcnt vmcnt(4)\n" \
"v_cmp_ne_u64 s[84:85],s[28:29],v[52:53]\ns_waitcnt vmcnt(2)\nv_cmp_ne_u64 s[82:83],s[28:29],v[56:57]\ns_or_b64 vcc,vcc,s[84:85]\n" \
"v_cmp_ne_u64 s[84:85],s[28:29],v[60:61]\ns_or_b64 vcc,vcc,s[82:83]\nv_cmp_ne_u64 s[82:83],s[28:29],v[64:65]\ns_or_b64 vcc,vcc,s[84:85]\n" \
"s_waitcnt vmcnt(1)\nv_cmp_ne_u64 s[84:85],s[28:29],v[68:69]\ns_or_b64 vcc,vcc,s[82:83]\ns_or_b64 vcc,vcc,s[84:85]\n" \
"s_cbranch_vccnz .LsmP_%=\n.LbmP_%=:\nv_pk_add_f32 v[50:51],v[50:51],v[62:63]\nv_pk_add_f32 v[54:55],v[54:55],v[66:67]\n" \
"v_pk_mul_f32 v[62:63],v[50:51],2.0 op_sel_hi:[1,0]\nv_pk_add_f32 v[58:59],v[58:59],v[70:71]\nv_max3_f32 v112,|v62|,|v63|,0\nv_pk_fma_f32 v[66:67],v[54:55],2.0,v[74:75] op_sel_hi:[1,0,0]\n" \
"v_max3_f32 v112,|v66|,|v67|,v112\nv_pk_fma_f32 v[70:71],v[58:59],2.0,2.0 op_sel_hi:[1,0,0]\nv_max3_f32 v112,|v70|,|v71|,v112\nv_cmp_lt_f32 s[82:83],s81,v112\n" \
"v_cmp_ngt_f32 vcc,0x41000000,v112\ns_cmp_lg_u64 s[82:83],0\ns_cselect_b32 s82,2,0\nv_cvt_pk_f16_f32 v62,v62,v63\n" \
"s_cmp_lg_u64 vcc,0\ns_addc_u32 s82,s82,0\nds_write_b32 v252,v62 offset:62848\nv_cvt_pk_f16_f32 v66,v66,v67\n" \
"ds_write_b32 v253,v66 offset:62848\nv_cvt_pk_f16_f32 v70,v70,v71\nv_mov_b32 v113,s82\nds_write_b32 v254,v70 offset:62848\n" \
"ds_write_b32 v251,v113\ns_waitcnt lgkmcnt(0)\ns_barrier\nds_read_b128 v[124:127],v114\n" \
"ds_read_b64 v[96:97],v234 offset:62848\nds_read_b64 v[98:99],v235 offset:62848\nds_read_b64 v[100:101],v236 offset:62848\nds_read_b64 v[102:103],v237 offset:62848\n" \
"ds_read_b64 v[104:105],v238 offset:62848\nds_read_b64 v[106:107],v239 offset:62848\nds_read_b64 v[108:109],v240 offset:62848\nds_read_b64 v[110:111],v241 offset:62848\n" \
"s_waitcnt lgkmcnt(8)\nv_or3_b32 v124,v124,v125,v126\nv_or_b32 v124,v124,v127\nv_cmp_ne_u32 vcc,2,v124\n" \
"s_nop 0\ns_cbranch_vccnz .LsxP_%=\n.LbxP_%=:\ns_waitcnt lgkmcnt(7)\n" \
"v_pk_fma_f16 v80,v16,v96,v97\ns_waitcnt lgkmcnt(6)\nv_pk_fma_f16 v81,v17,v98,v99\ns_waitcnt lgkmcnt(5)\n" \
"v_pk_fma_f16 v82,v18,v100,v101\ns_waitcnt lgkmcnt(4)\nv_pk_fma_f16 v83,v19,v102,v103\nv_pk_fma_f16 v88,v24,v96,v97\n" \
"s_waitcnt lgkmcnt(3)\nv_pk_fma_f16 v84,v20,v104,v105\ns_waitcnt lgkmcnt(2)\nv_pk_fma_f16 v85,v21,v106,v107\n" \
"v_pk_fma_f16 v89,v25,v98,v99\ns_waitcnt lgkmcnt(1)\nv_pk_fma_f16 v86,v22,v108,v109\nv_pk_fma_f16 v90,v26,v100,v101\n" \
"s_waitcnt lgkmcnt(0)\nv_pk_fma_f16 v87,v23,v110,v111\nv_pk_fma_f16 v91,v27,v102,v103\nv_pk_fma_f16 v92,v28,v104,v105\n" \
"v_pk_fma_f16 v93,v29,v106,v107\nv_pk_fma_f16 v94,v30,v108,v109\nv_bfe_u32 v233,v0,3,3\nv_mul_u32_u24 v233,8192,v233\n" \
"v_lshl_add_u32 v233,v1,3,v233\ns_lshl2_add_u32 s96,s33,s3\ns_add_u32 s96,s96,138\ns_and_b32 s97,s96,31\n" \
"s_lshl_b32 s97,s97,3\ns_cmp_lt_u32 s96,256\ns_cselect_b32 s96,s96,0x40000\ns_lshr_b32 s96,s96,5\n" \
"s_add_u32 s96,s96,s97\ns_lshl_b32 s96,s96,6\ns_add_u32 s96,s96,s32\ns_mul_i32 s96,s96,1152\n" \
"s_mov_b32 m0,162176\nv_pk_fma_f16 v95,v31,v110,v111\nbuffer_load_dword v233,s[8:11],s96 offen lds\nv_mfma_f32_16x16x32_f16 v[48:51],v[80:83],v[192:195],0\n" \
"s_add_u32 s73,s35,s88\ns_add_u32 s74,s73,4096\nv_mfma_f32_16x16x32_f16 v[52:55],v[80:83],v[196:199],0\nv_and_b32 v112,15,v0\n" \
"v_bfrev_b32 v113,1\nv_mfma_f32_16x16x32_f16 v[56:59],v[84:87],v[32:35],0\nv_cmp_eq_u32 vcc,0,v112\ns_add_u32 s75,s73,8192\n" \
"v_mfma_f32_16x16x32_f16 v[60:63],v[84:87],v[36:39],0\nv_cndmask_b32 v113,v113,v3,vcc\nv_cmp_gt_u32 vcc,64,v0\nv_mfma_f32_16x16x32_f16 v[64:67],v[88:91],v[192:195],0\n" \
"s_cmp_lt_u32 s3,2\ns_cselect_b32 s75,s75,2147483648\nv_mfma_f32_16x16x32_f16 v[68:71],v[88:91],v[196:199],0\nv_pk_add_f32 v[56:57],v[48:49],v[56:57] neg_lo:[0,1] neg_hi:[0,1]\n" \
"v_sub_f32 v58,v50,v58\nv_mfma_f32_16x16x32_f16 v[72:75],v[92:95],v[32:35],0\nv_pk_add_f32 v[60:61],v[52:53],v[60:61] neg_lo:[0,1] neg_hi:[0,1]\nv_sub_f32 v62,v54,v62\n" \
"v_mfma_f32_16x16x32_f16 v[76:79],v[92:95],v[36:39],0\nv_pk_fma_f32 v[48:49],v[48:49],2.0,v[56:57] op_sel_hi:[1,0,1] neg_lo:[0,0,1] neg_hi:[0,0,1]\nv_fma_f32 v50,v50,2.0,-v58\nv_pk_fma_f32 v[52:53],v[52:53],2.0,v[60:61] op_sel_hi:[1,0,1] neg_lo:[0,0,1] neg_hi:[0,0,1]\n" \
"v_fma_f32 v54,v54,2.0,-v62\nv_cvt_pk_f16_f32 v16,v48,v52\nv_pk_add_f32 v[72:73],v[64:65],v[72:73] neg_lo:[0,1] neg_hi:[0,1]\nv_sub_f32 v74,v66,v74\n" \
"v_pk_fma_f32 v[64:65],v[64:65],2.0,v[72:73] op_sel_hi:[1,0,1] neg_lo:[0,0,1] neg_hi:[0,0,1]\nv_pk_add_f32 v[76:77],v[68:69],v[76:77] neg_lo:[0,1] neg_hi:[0,1]\nv_sub_f32 v78,v70,v78\nv_fma_f32 v66,v66,2.0,-v74\n" \
"v_pk_fma_f32 v[68:69],v[68:69],2.0,v[76:77] op_sel_hi:[1,0,1] neg_lo:[0,0,1] neg_hi:[0,0,1]\nv_fma_f32 v70,v70,2.0,-v78\nv_cvt_pk_f16_f32 v17,v49,v53\nv_cvt_pk_f16_f32 v18,v50,v54\n" \
"v_cvt_pk_f16_f32 v24,v64,v68\nv_cvt_pk_f16_f32 v25,v65,v69\nv_cvt_pk_f16_f32 v26,v66,v70\nv_mfma_f32_16x16x32_f16 v[48:51],v[200:203],v[16:19],0\n" \
"v_cvt_pk_f16_f32 v20,v56,v60\nv_cvt_pk_f16_f32 v21,v57,v61\nv_mfma_f32_16x16x32_f16 v[52:55],v[204:207],v[16:19],0\nv_cvt_pk_f16_f32 v22,v58,v62\n" \
"v_cvt_pk_f16_f32 v28,v72,v76\nv_mfma_f32_16x16x32_f16 v[64:67],v[200:203],v[24:27],0\nv_cvt_pk_f16_f32 v29,v73,v77\nv_cvt_pk_f16_f32 v30,v74,v78\n" \
"v_mfma_f32_16x16x32_f16 v[68:71],v[204:207],v[24:27],0\nv_cndmask_b32_e64 v113,v113,v3,vcc\nv_mfma_f32_16x16x32_f16 v[56:59],v[200:203],v[20:23],0\nv_cvt_pk_f16_f32 v80,v48,v52\n" \
"v_cvt_pk_f16_f32 v82,v49,v53\nv_mfma_f32_16x16x32_f16 v[60:63],v[204:207],v[20:23],0\nv_cvt_pk_f16_f32 v84,v50,v54\nv_mfma_f32_16x16x32_f16 v[72:75],v[200:203],v[28:31],0\n" \
"s_nop 0\nv_cvt_pk_f16_f32 v81,v64,v68\nv_mfma_f32_16x16x32_f16 v[76:79],v[204:207],v[28:31],0\nds_write_b64 v255,v[80:81]\n" \
"v_cvt_pk_f16_f32 v83,v65,v69\nds_write_b64 v255,v[82:83] offset:512\nv_cvt_pk_f16_f32 v85,v66,v70\nds_write_b64 v255,v[84:85] offset:1024\n" \
"v_cvt_pk_f16_f32 v86,v56,v60\nv_cvt_pk_f16_f32 v88,v57,v61\nv_cvt_pk_f16_f32 v90,v58,v62\nv_cvt_pk_f16_f32 v87,v72,v76\n" \
"ds_write_b64 v255,v[86:87] offset:256\nv_cvt_pk_f16_f32 v89,v73,v77\nds_write_b64 v255,v[88:89] offset:768\nv_cvt_pk_f16_f32 v91,v74,v78\n" \
"ds_write_b64 v255,v[90:91] offset:1280\ns_waitcnt lgkmcnt(0)\ns_barrier\nds_read_b128 v[16:19],v243\n" \
"ds_read_b128 v[20:23],v244\nds_read_b128 v[24:27],v245\ns_waitcnt lgkmcnt(2)\nv_mfma_f32_16x16x32_f16 v[48:51],v[40:43],v[16:19],0\n" \
"v_mfma_f32_16x16x32_f16 v[52:55],v[44:47],v[16:19],0\ns_waitcnt lgkmcnt(1)\nv_mfma_f32_16x16x32_f16 v[56:59],v[40:43],v[20:23],0\nv_mfma_f32_16x16x32_f16 v[60:63],v[44:47],v[20:23],0\n" \
"s_waitcnt lgkmcnt(0)\nv_mfma_f32_16x16x32_f16 v[64:67],v[40:43],v[24:27],0\nv_mfma_f32_16x16x32_f16 v[68:71],v[44:47],v[24:27],0\ns_nop 1\n" \
"v_cvt_pk_f16_f32 v80,v48,v52\nv_and_b32 v80,-2,v80\nv_cvt_pk_f16_f32 v81,v49,v53\nv_cvt_pk_f16_f32 v82,v50,v54\n" \
"v_cvt_pk_f16_f32 v83,v51,v55\nbuffer_store_dwordx4 v[80:83],v3,s[36:39],s73 offen sc1\nv_cvt_pk_f16_f32 v84,v56,v60\nv_cvt_pk_f16_f32 v85,v57,v61\n" \
"v_cvt_pk_f16_f32 v86,v58,v62\nv_cvt_pk_f16_f32 v87,v59,v63\nv_and_b32_e64 v84,-2,v84\nbuffer_store_dwordx4 v[84:87],v3,s[36:39],s74 offen sc1\n" \
"v_cvt_pk_f16_f32 v88,v64,v68\nv_cvt_pk_f16_f32 v89,v65,v69\nv_cvt_pk_f16_f32 v90,v66,v70\nv_cvt_pk_f16_f32 v91,v67,v71\n" \
"v_and_b32 v88,-2,v88\nbuffer_store_dwordx4 v[88:91],v113,s[36:39],s75 offen sc1\ns_cmp_lt_u32 s3,2\ns_cselect_b32 s18,s75,s73\n" \
"s_mov_b32 s12,s88\ns_mov_b32 s13,s89\ns_mov_b32 s14,s87\ns_mov_b32 s16,s73\n" \
"s_mov_b32 s17,s74\ns_load_dwordx2 s[20:21],s[0:1],32\ns_load_dwordx2 s[24:25],s[0:1],40\ns_mov_b32 s22,1152\n" \
"s_mov_b32 s26,3981312\ns_lshr_b32 s95,s86,2\ns_add_u32 s95,s95,2304\ns_waitcnt lgkmcnt(0)\n" \
"v_lshrrev_b32 v120,2,v0\nv_and_b32 v121,3,v0\nv_mul_u32_u24 v122,6912,v120\nv_lshlrev_b32 v123,4,v120\n" \
"v_lshl_add_u32 v121,v121,4,s95\nbuffer_load_dwordx4 v[112:115],v123,s[20:23],0 offen\nv_lshlrev_b32 v120,1,v120\nv_lshl_add_u32 v123,v122,3,v121\n" \
"buffer_load_ushort v116,v120,s[20:23],0 offen offset:1024\nv_add_u32_e64 v121,v121,v122\nbuffer_load_dwordx4 v[16:19],v123,s[24:27],0 offen\ns_mov_b32 s95,6912\n" \
"buffer_load_dwordx4 v[20:23],v123,s[24:27],s95 offen\ns_mov_b32 s95,13824\nbuffer_load_dwordx4 v[24:27],v123,s[24:27],s95 offen\ns_mov_b32 s95,20736\n" \
"buffer_load_dwordx4 v[28:31],v123,s[24:27],s95 offen\ns_mov_b32 s95,27648\nbuffer_load_dwordx4 v[32:35],v123,s[24:27],s95 offen\ns_mov_b32 s95,34560\n" \
"buffer_load_dwordx4 v[36:39],v123,s[24:27],s95 offen\ns_mov_b32 s95,41472\nbuffer_load_dwordx4 v[40:43],v123,s[24:27],s95 offen\ns_mov_b32 s95,48384\n" \
"buffer_load_dwordx4 v[44:47],v123,s[24:27],s95 offen\ns_mov_b32 s95,3538944\nbuffer_load_dwordx4 v[48:51],v121,s[24:27],s95 offen\ns_lshr_b32 s26,s32,1\n" \
"s_cmp_eq_u32 s26,2\ns_cselect_b32 s26,1.0,0\nv_cmp_gt_u32 vcc,8,v0\nv_mov_b32 v100,s28\n" \
"v_mov_b32 v101,s29\ns_waitcnt vmcnt(10)\nv_lshlrev_b32 v52,16,v112\nv_mul_f32 v53,0xbfb8aa3b,v52\n" \
"v_exp_f32 v53,v53\nv_and_b32 v54,0xffff0000,v112\nv_add_f32 v53,1.0,v53\nv_mul_f32 v55,0xbfb8aa3b,v54\n" \
"v_lshlrev_b32 v56,16,v113\nv_rcp_f32 v53,v53\nv_exp_f32 v55,v55\nv_mul_f32 v57,0xbfb8aa3b,v56\n" \
"v_exp_f32 v57,v57\nv_mul_f32 v52,v52,v53\nv_add_f32 v55,1.0,v55\nv_and_b32 v58,0xffff0000,v113\n" \
"v_cvt_pk_bf16_f32 v52,v52,v52\nv_rcp_f32 v55,v55\nv_add_f32 v57,1.0,v57\nv_mul_f32 v59,0xbfb8aa3b,v58\n" \
"v_lshlrev_b32 v52,16,v52\nv_lshlrev_b32 v60,16,v114\nv_rcp_f32 v57,v57\nv_exp_f32 v59,v59\n" \
"v_mul_f32 v61,0xbfb8aa3b,v60\nv_mul_f32 v54,v54,v55\nv_exp_f32 v61,v61\nv_cvt_pk_bf16_f32 v54,v54,v54\n" \
"v_mul_f32 v56,v56,v57\nv_add_f32 v59,1.0,v59\nv_and_b32 v62,0xffff0000,v114\nv_lshlrev_b32 v54,16,v54\n" \
"v_cvt_pk_bf16_f32 v56,v56,v56\nv_rcp_f32 v59,v59\nv_add_f32 v61,1.0,v61\nv_mul_f32 v63,0xbfb8aa3b,v62\n" \
"v_lshlrev_b32 v56,16,v56\nv_lshlrev_b32 v64,16,v115\nv_rcp_f32 v61,v61\nv_exp_f32 v63,v63\n" \
"v_mul_f32 v65,0xbfb8aa3b,v64\nv_mul_f32 v58,v58,v59\nv_exp_f32 v65,v65\nv_cvt_pk_bf16_f32 v58,v58,v58\n" \
"v_mul_f32 v60,v60,v61\nv_add_f32 v63,1.0,v63\nv_and_b32 v66,0xffff0000,v115\nv_lshlrev_b32 v58,16,v58\n" \
"v_cvt_pk_bf16_f32 v60,v60,v60\nv_rcp_f32 v63,v63\nv_add_f32 v65,1.0,v65\nv_mul_f32 v67,0xbfb8aa3b,v66\n" \
"v_lshlrev_b32 v60,16,v60\ns_waitcnt vmcnt(9)\nv_lshlrev_b32 v68,16,v116\nv_rcp_f32 v65,v65\n" \
"v_exp_f32 v67,v67\nv_mul_f32 v69,0xbfb8aa3b,v68\nv_mul_f32 v62,v62,v63\nv_exp_f32 v69,v69\n" \
"v_cvt_pk_bf16_f32 v62,v62,v62\nv_mul_f32 v64,v64,v65\nv_add_f32 v67,1.0,v67\nv_lshlrev_b32 v62,16,v62\n" \
"v_cvt_pk_bf16_f32 v64,v64,v64\nv_rcp_f32 v67,v67\nv_add_f32 v69,1.0,v69\nv_lshlrev_b32 v64,16,v64\n" \
"v_rcp_f32 v69,v69\nv_mul_f32 v66,v66,v67\nv_cvt_pk_bf16_f32 v66,v66,v66\nv_mul_f32 v68,v68,v69\n" \
"v_lshlrev_b32 v66,16,v66\nv_cvt_pk_bf16_f32 v68,v68,v68\nv_lshlrev_b32 v68,16,v68\ns_waitcnt vmcnt(8)\n" \
"v_and_b32 v81,0xffff0000,v16\nv_and_b32 v83,0xffff0000,v17\nv_lshlrev_b32 v80,16,v16\nv_lshlrev_b32_e64 v82,16,v17\n" \
"v_pk_mul_f32 v[70:71],v[80:81],v[52:53] op_sel_hi:[1,0]\nv_pk_mul_f32 v[72:73],v[82:83],v[52:53] op_sel_hi:[1,0]\nv_and_b32 v81,0xffff0000,v18\nv_and_b32 v83,0xffff0000,v19\n" \
"v_lshlrev_b32 v80,16,v18\nv_lshlrev_b32 v82,16,v19\nv_pk_mul_f32 v[74:75],v[80:81],v[52:53] op_sel_hi:[1,0]\nv_pk_mul_f32 v[76:77],v[82:83],v[52:53] op_sel_hi:[1,0]\n" \
"s_waitcnt vmcnt(7)\nv_and_b32 v85,0xffff0000,v20\nv_and_b32 v87,0xffff0000,v21\nv_lshlrev_b32 v84,16,v20\n" \
"v_lshlrev_b32 v86,16,v21\nv_pk_fma_f32 v[70:71],v[84:85],v[54:55],v[70:71] op_sel_hi:[1,0,1]\nv_pk_fma_f32 v[72:73],v[86:87],v[54:55],v[72:73] op_sel_hi:[1,0,1]\ns_waitcnt vmcnt(6)\n" \
"v_and_b32 v81,0xffff0000,v24\nv_and_b32 v83,0xffff0000,v25\nv_lshlrev_b32 v80,16,v24\nv_lshlrev_b32 v82,16,v25\n" \
"v_and_b32 v85,0xffff0000,v22\nv_and_b32 v87,0xffff0000,v23\nv_pk_fma_f32 v[70:71],v[80:81],v[56:57],v[70:71] op_sel_hi:[1,0,1]\nv_pk_fma_f32 v[72:73],v[82:83],v[56:57],v[72:73] op_sel_hi:[1,0,1]\n" \
"v_lshlrev_b32 v84,16,v22\nv_lshlrev_b32 v86,16,v23\nv_pk_fma_f32 v[74:75],v[84:85],v[54:55],v[74:75] op_sel_hi:[1,0,1]\nv_pk_fma_f32 v[76:77],v[86:87],v[54:55],v[76:77] op_sel_hi:[1,0,1]\n" \
"v_and_b32 v81,0xffff0000,v26\nv_and_b32 v83,0xffff0000,v27\nv_lshlrev_b32 v80,16,v26\nv_lshlrev_b32 v82,16,v27\n" \
"v_pk_fma_f32 v[74:75],v[80:81],v[56:57],v[74:75] op_sel_hi:[1,0,1]\nv_pk_fma_f32 v[76:77],v[82:83],v[56:57],v[76:77] op_sel_hi:[1,0,1]\ns_waitcnt vmcnt(5)\nv_and_b32 v85,0xffff0000,v28\n" \
"v_and_b32 v87,0xffff0000,v29\nv_lshlrev_b32 v84,16,v28\nv_lshlrev_b32 v86,16,v29\nv_pk_fma_f32 v[70:71],v[84:85],v[58:59],v[70:71] op_sel_hi:[1,0,1]\n" \
"v_pk_fma_f32 v[72:73],v[86:87],v[58:59],v[72:73] op_sel_hi:[1,0,1]\ns_waitcnt vmcnt(4)\nv_and_b32 v81,0xffff0000,v32\nv_and_b32 v83,0xffff0000,v33\n" \
"v_lshlrev_b32 v80,16,v32\nv_lshlrev_b32 v82,16,v33\nv_and_b32 v85,0xffff0000,v30\nv_and_b32 v87,0xffff0000,v31\n" \
"v_pk_fma_f32 v[70:71],v[80:81],v[60:61],v[70:71] op_sel_hi:[1,0,1]\nv_pk_fma_f32 v[72:73],v[82:83],v[60:61],v[72:73] op_sel_hi:[1,0,1]\nv_lshlrev_b32 v84,16,v30\nv_lshlrev_b32 v86,16,v31\n" \
"v_pk_fma_f32 v[74:75],v[84:85],v[58:59],v[74:75] op_sel_hi:[1,0,1]\nv_pk_fma_f32 v[76:77],v[86:87],v[58:59],v[76:77] op_sel_hi:[1,0,1]\nv_and_b32 v81,0xffff0000,v34\nv_and_b32 v83,0xffff0000,v35\n" \
"v_lshlrev_b32 v80,16,v34\nv_lshlrev_b32 v82,16,v35\nv_pk_fma_f32 v[74:75],v[80:81],v[60:61],v[74:75] op_sel_hi:[1,0,1]\nv_pk_fma_f32 v[76:77],v[82:83],v[60:61],v[76:77] op_sel_hi:[1,0,1]\n" \
"s_waitcnt vmcnt(3)\nv_and_b32 v85,0xffff0000,v36\nv_and_b32 v87,0xffff0000,v37\nv_lshlrev_b32 v84,16,v36\n" \
"v_lshlrev_b32 v86,16,v37\nv_pk_fma_f32 v[70:71],v[84:85],v[62:63],v[70:71] op_sel_hi:[1,0,1]\nv_pk_fma_f32 v[72:73],v[86:87],v[62:63],v[72:73] op_sel_hi:[1,0,1]\ns_waitcnt vmcnt(2)\n" \
"v_and_b32 v81,0xffff0000,v40\nv_and_b32 v83,0xffff0000,v41\nv_lshlrev_b32 v80,16,v40\nv_lshlrev_b32 v82,16,v41\n" \
"v_and_b32 v85,0xffff0000,v38\nv_and_b32 v87,0xffff0000,v39\nv_pk_fma_f32 v[70:71],v[80:81],v[64:65],v[70:71] op_sel_hi:[1,0,1]\nv_pk_fma_f32 v[72:73],v[82:83],v[64:65],v[72:73] op_sel_hi:[1,0,1]\n" \
"v_lshlrev_b32 v84,16,v38\nv_lshlrev_b32 v86,16,v39\nv_pk_fma_f32 v[74:75],v[84:85],v[62:63],v[74:75] op_sel_hi:[1,0,1]\nv_pk_fma_f32 v[76:77],v[86:87],v[62:63],v[76:77] op_sel_hi:[1,0,1]\n" \
"v_and_b32 v81,0xffff0000,v42\nv_and_b32 v83,0xffff0000,v43\nv_lshlrev_b32 v80,16,v42\nv_lshlrev_b32 v82,16,v43\n" \
"v_pk_fma_f32 v[74:75],v[80:81],v[64:65],v[74:75] op_sel_hi:[1,0,1]\nv_pk_fma_f32 v[76:77],v[82:83],v[64:65],v[76:77] op_sel_hi:[1,0,1]\ns_waitcnt vmcnt(1)\nv_and_b32 v85,0xffff0000,v44\n" \
"v_and_b32 v87,0xffff0000,v45\nv_lshlrev_b32 v84,16,v44\nv_lshlrev_b32 v86,16,v45\nv_pk_fma_f32 v[70:71],v[84:85],v[66:67],v[70:71] op_sel_hi:[1,0,1]\n" \
"v_pk_fma_f32 v[72:73],v[86:87],v[66:67],v[72:73] op_sel_hi:[1,0,1]\ns_waitcnt vmcnt(0)\nv_and_b32 v81,0xffff0000,v48\nv_and_b32 v83,0xffff0000,v49\n" \
"v_lshlrev_b32 v80,16,v48\nv_lshlrev_b32 v82,16,v49\nv_and_b32 v85,0xffff0000,v46\nv_and_b32 v87,0xffff0000,v47\n" \
"v_pk_fma_f32 v[70:71],v[80:81],v[68:69],v[70:71] op_sel_hi:[1,0,1]\nv_pk_fma_f32 v[72:73],v[82:83],v[68:69],v[72:73] op_sel_hi:[1,0,1]\nv_lshlrev_b32 v84,16,v46\nv_lshlrev_b32 v86,16,v47\n" \
"v_pk_fma_f32 v[74:75],v[84:85],v[66:67],v[74:75] op_sel_hi:[1,0,1]\nv_pk_fma_f32 v[76:77],v[86:87],v[66:67],v[76:77] op_sel_hi:[1,0,1]\nv_and_b32 v81,0xffff0000,v50\nv_and_b32 v83,0xffff0000,v51\n" \
"v_lshlrev_b32 v80,16,v50\nv_lshlrev_b32 v82,16,v51\nv_pk_fma_f32 v[74:75],v[80:81],v[68:69],v[74:75] op_sel_hi:[1,0,1]\nv_pk_fma_f32 v[76:77],v[82:83],v[68:69],v[76:77] op_sel_hi:[1,0,1]\n" \
"v_and_b32 v80,3,v0\nv_permlane32_swap_b32 v70,v74\nv_permlane32_swap_b32 v71,v75\nv_permlane32_swap_b32 v72,v76\n" \
"v_permlane32_swap_b32 v73,v77\nv_pk_add_f32 v[70:71],v[70:71],v[74:75]\nv_pk_add_f32 v[72:73],v[72:73],v[76:77]\nv_lshrrev_b32 v81,4,v0\n" \
"v_and_b32 v82,12,v81\nv_permlane16_swap_b32 v70,v72\nv_permlane16_swap_b32 v71,v73\nv_lshlrev_b32 v80,5,v80\n" \
"v_pk_add_f32 v[70:71],v[70:71],v[72:73]\nv_mad_u32_u24 v80,v82,24,v80\nv_lshl_add_u32 v80,v81,3,v80\nv_add_f32_dpp v70,v70,v70 row_ror:8 row_mask:0xf bank_mask:0xf bound_ctrl:1\n" \
"v_add_f32_dpp v71,v71,v71 row_ror:8 row_mask:0xf bank_mask:0xf bound_ctrl:1\nv_add_u32 v80,161152,v80\nv_add_f32_dpp v70,v70,v70 row_ror:4 row_mask:0xf bank_mask:0xf bound_ctrl:1\nv_add_f32_dpp v71,v71,v71 row_ror:4 row_mask:0xf bank_mask:0xf bound_ctrl:1\n" \
"ds_write_b64 v80,v[70:71]\nv_and_b32 v80,7,v0\nv_lshlrev_b32 v80,4,v80\nv_add_u32 v80,161152,v80\n" \
"v_bfrev_b32 v81,1\nv_cndmask_b32 v81,v81,v2,vcc\ns_waitcnt lgkmcnt(0)\ns_barrier\n" \
"ds_read_b128 v[84:87],v80\nds_read_b128 v[88:91],v80 offset:128\nds_read_b128 v[92:95],v80 offset:256\nds_read_b128 v[96:99],v80 offset:384\n" \
"s_waitcnt lgkmcnt(2)\nv_pk_add_f32 v[84:85],v[84:85],v[88:89]\nv_pk_add_f32 v[86:87],v[86:87],v[90:91]\ns_waitcnt lgkmcnt(0)\n" \
"v_pk_add_f32 v[92:93],v[92:93],v[96:97]\nv_pk_add_f32 v[94:95],v[94:95],v[98:99]\nv_pk_add_f32 v[84:85],v[84:85],v[92:93]\nv_pk_add_f32 v[86:87],v[86:87],v[94:95]\n" \
"v_cvt_pk_bf16_f32 v88,v84,v85\nv_cvt_pk_bf16_f32 v90,v86,v87\nv_and_b32 v85,0xffff0000,v88\nv_and_b32 v87,0xffff0000,v90\n" \
"v_lshlrev_b32 v84,16,v88\nv_lshlrev_b32 v86,16,v90\nv_add_f32 v84,s26,v84\nv_add_f32 v85,s26,v85\n" \
"v_add_f32 v86,s26,v86\nv_add_f32 v87,s26,v87\ns_lshr_b32 s26,s86,1\nv_cvt_pk_bf16_f32 v102,v84,v85\n" \
"v_cvt_pk_bf16_f32 v103,v86,v87\ns_add_u32 s26,s26,26624\nbuffer_store_dwordx4 v[100:103],v81,s[36:39],s26 offen sc1\ns_and_b32 s22,s33,7\n" \
"s_and_b32 s24,s32,1\ns_lshl3_add_u32 s22,s24,s22\ns_bfe_u32 s24,s33,0x10002\ns_lshl1_add_u32 s24,s32,s24\n" \
"s_mul_i32 s19,s24,18\ns_and_b32 s24,s33,3\ns_lshl2_add_u32 s23,s24,s19\ns_lshl_b32 s24,s3,5\n" \
"s_lshr_b32 s20,s33,3\ns_lshr_b32 s21,s32,1\ns_add_u32 s54,s14,s24\ns_lshl2_add_u32 s24,s20,s21\n" \
"s_add_u32 s23,s23,s3\ns_lshl2_add_u32 s24,s24,s3\ns_add_u32 s23,s23,1\ns_lshl_b32 s24,s24,9\n" \
"s_lshl_b32 s23,s23,9\nv_bfe_u32 v127,v0,3,3\nv_mul_u32_u24 v127,8192,v127\nv_lshl_add_u32 v127,v1,3,v127\n" \
"s_lshl2_add_u32 s95,s33,s3\ns_add_u32 s95,s95,190\ns_and_b32 s100,s95,31\ns_lshl_b32 s100,s100,3\n" \
"s_cmp_lt_u32 s95,256\ns_cselect_b32 s95,s95,0x40000\ns_lshr_b32 s95,s95,5\ns_add_u32 s95,s95,s100\n" \
"s_lshl_b32 s95,s95,6\ns_add_u32 s95,s95,s32\ns_mul_i32 s95,s95,1152\ns_mov_b32 m0,162176\n" \
"s_add_u32 s101,s92,0\nbuffer_load_dword v127,s[8:11],s95 offen lds\ns_add_u32 s24,s24,262144\ns_sub_u32 s59,s24,s23\n" \
"s_lshr_b32 s25,s3,1\ns_mul_i32 s24,s21,1152\ns_mul_i32 s25,s25,576\ns_add_u32 s24,s24,s25\n" \
"s_lshl4_add_u32 s24,s24,s22\ns_and_b32 s25,s3,1\ns_lshl_b32 s24,s24,8\ns_lshl_b32 s65,s25,17\n" \
"s_add_u32 s65,s65,s24\ns_lshl_b32 s25,s25,20\ns_add_u32 s24,s24,s25\ns_add_u32 s60,s24,655488\n" \
"s_add_u32 s61,s24,786432\ns_add_u32 s62,s24,786560\ns_add_u32 m0,s57,0\ns_add_u32 s31,s92,9437184\n" \
"buffer_load_dwordx4 v4,s[8:11],s101 offen lds\ns_add_u32 s63,s24,917504\ns_add_u32 s64,s24,917632\ns_mul_i32 s52,s20,4505600\n" \
"s_lshl2_add_u32 s24,s21,s3\ns_add_u32 s52,s52,12123648\ns_mul_i32 s24,s24,9216\ns_add_u32 s24,s24,s52\n" \
"s_add_u32 s68,s24,819712\ns_lshl_b32 s24,s22,14\nbuffer_load_dwordx4 v5,s[8:11],s101 offen offset:1024 lds\ns_add_u32 s24,s24,s52\n" \
"s_lshl2_add_u32 s25,s21,s3\ns_add_u32 s24,s24,967168\ns_lshl_b32 s25,s25,8\ns_add_u32 s79,s25,s24\n" \
"s_add_u32 s25,s21,4\ns_and_b32 s25,s25,3\ns_add_u32 s24,s24,s12\ns_lshl_b32 s25,s25,12\n" \
"buffer_load_dwordx4 v6,s[8:11],s101 offen offset:2048 lds\ns_add_u32 s25,s25,s24\ns_sub_u32 s74,s25,819200\ns_add_u32 s25,s21,3\n" \
"s_and_b32 s25,s25,3\ns_lshl_b32 s25,s25,12\ns_add_u32 s25,s25,s24\ns_sub_u32 s71,s25,2048000\n" \
"s_sub_u32 s76,s25,1638400\ns_add_u32 s25,s21,2\nbuffer_load_dwordx4 v7,s[8:11],s101 offen offset:3072 lds\ns_and_b32 s25,s25,3\n" \
"s_lshl_b32 s25,s25,12\ns_add_u32 s25,s25,s24\ns_sub_u32 s72,s25,1638400\ns_sub_u32 s77,s25,1638400\n" \
"s_add_u32 s25,s21,1\ns_and_b32 s25,s25,3\ns_lshl_b32 s25,s25,12\ns_add_u32 s25,s25,s24\n" \
"s_add_u32 m0,s57,4096\ns_add_u32 s96,s92,73728\nbuffer_load_dwordx4 v4,s[8:11],s31 offen lds\ns_sub_u32 s73,s25,1638400\n" \
"s_sub_u32 s78,s25,1228800\ns_lshl2_add_u32 s26,s33,s3\ns_and_b32 s24,s26,15\ns_and_b32 s25,s26,8\n" \
"s_lshl2_add_u32 s24,s24,192\ns_mul_i32 s25,s25,28\ns_add_u32 s25,s25,s24\ns_sub_u32 s25,s25,32\n" \
"buffer_load_dwordx4 v5,s[8:11],s31 offen offset:1024 lds\ns_bitcmp1_b32 s26,4\ns_cselect_b32 s24,s25,s24\ns_and_b32 s25,s26,31\n" \
"s_lshl2_add_u32 s25,s25,448\ns_bitcmp1_b32 s26,5\ns_cselect_b32 s24,s25,s24\ns_lshr_b32 s25,s26,6\n" \
"s_lshl1_add_u32 s25,s21,s25\ns_mul_i32 s25,s25,576\nbuffer_load_dwordx4 v6,s[8:11],s31 offen offset:2048 lds\ns_add_u32 s24,s24,s25\n" \
"s_and_b32 s25,s32,1\ns_lshl1_add_u32 s24,s24,s25\nv_and_b32 v112,3,v0\ns_lshl_b32 s24,s24,11\n" \
"s_bfe_u32 s25,s33,131072\ns_mul_i32 s27,s3,4896\nv_and_b32 v113,12,v0\nv_lshlrev_b32 v112,2,v112\n" \
"buffer_load_dwordx4 v7,s[8:11],s31 offen offset:3072 lds\ns_add_u32 s24,s24,s25\ns_add_u32 s27,s27,9216\nv_max_u32 v112,v112,v113\n" \
"s_mul_i32 s48,s20,589824\ns_add_u32 s89,s24,1\nv_mov_b32 v114,1048576\nv_mov_b32 v113,s27\n" \
"v_cmp_gt_u32 vcc,12,v112\ns_add_u32 m0,s57,8192\ns_add_u32 s97,s92,9510912\nbuffer_load_dwordx4 v4,s[8:11],s96 offen lds\n" \
"s_lshl4_add_u32 s48,s12,s48\ns_lshl_b32 s86,s21,11\ns_lshl2_add_u32 s24,s2,s3\nv_and_b32 v63,0xffff0000,v224\n" \
"v_and_b32 v65,0xffff0000,v225\nv_and_b32 v67,0xffff0000,v226\nv_and_b32 v69,0xffff0000,v227\nv_and_b32 v71,0xffff0000,v228\n" \
"buffer_load_dwordx4 v5,s[8:11],s96 offen offset:1024 lds\nv_and_b32 v73,0xffff0000,v229\nv_and_b32 v75,0xffff0000,v230\nv_and_b32 v77,0xffff0000,v231\n" \
"v_and_b32 v79,0xffff0000,v232\nv_cndmask_b32 v112,v114,v113,vcc\nv_lshlrev_b32 v62,16,v224\nv_lshlrev_b32 v64,16,v225\n" \
"v_lshlrev_b32 v66,16,v226\nbuffer_load_dwordx4 v6,s[8:11],s96 offen offset:2048 lds\nv_lshlrev_b32 v68,16,v227\nv_lshlrev_b32 v70,16,v228\n" \
"v_lshlrev_b32 v72,16,v229\nv_lshlrev_b32 v74,16,v230\nv_lshlrev_b32 v76,16,v231\nv_lshlrev_b32 v78,16,v232\n" \
"s_add_u32 s48,s48,9764864\ns_add_u32 s65,s65,2097152\ns_lshl_b32 s67,s21,7\nbuffer_load_dwordx4 v7,s[8:11],s96 offen offset:3072 lds\n" \
"s_lshl_b32 s85,s21,17\ns_sub_u32 s86,s14,s86\ns_lshl_b32 s24,s24,8\nv_pk_add_f32 v[62:63],v[62:63],v[250:251] op_sel_hi:[1,0] neg_lo:[0,1] neg_hi:[0,1]\n" \
"v_pk_add_f32 v[64:65],v[64:65],v[250:251] op_sel_hi:[1,0] neg_lo:[0,1] neg_hi:[0,1]\nv_pk_add_f32 v[66:67],v[66:67],v[250:251] op_sel_hi:[1,0] neg_lo:[0,1] neg_hi:[0,1]\nv_pk_add_f32 v[68:69],v[68:69],v[250:251] op_sel_hi:[1,0] neg_lo:[0,1] neg_hi:[0,1]\nv_pk_add_f32 v[70:71],v[70:71],v[250:251] op_sel_hi:[1,0] neg_lo:[0,1] neg_hi:[0,1]\n" \
"s_add_u32 m0,s57,12288\ns_add_u32 s100,s92,9584640\nbuffer_load_dwordx4 v4,s[8:11],s97 offen lds\nv_pk_add_f32 v[72:73],v[72:73],v[250:251] op_sel_hi:[1,0] neg_lo:[0,1] neg_hi:[0,1]\n" \
"v_pk_add_f32 v[74:75],v[74:75],v[250:251] op_sel_hi:[1,0] neg_lo:[0,1] neg_hi:[0,1]\nv_pk_add_f32 v[76:77],v[76:77],v[250:251] op_sel_hi:[1,0] neg_lo:[0,1] neg_hi:[0,1]\nv_pk_add_f32 v[78:79],v[78:79],v[250:251] op_sel_hi:[1,0] neg_lo:[0,1] neg_hi:[0,1]\nv_mov_b64 v[16:17],v[192:193]\n" \
"v_mov_b64 v[18:19],v[194:195]\nv_mov_b64 v[20:21],v[196:197]\nv_mov_b64 v[22:23],v[198:199]\nbuffer_load_dwordx4 v5,s[8:11],s97 offen offset:1024 lds\n" \
"v_mov_b64 v[24:25],v[208:209]\nv_mov_b64 v[26:27],v[210:211]\nv_mov_b64 v[28:29],v[212:213]\nv_mov_b64 v[30:31],v[214:215]\n" \
"v_mov_b64 v[32:33],v[216:217]\nv_mov_b64 v[34:35],v[218:219]\nv_mov_b64 v[36:37],v[220:221]\nv_mov_b64 v[38:39],v[222:223]\n" \
"v_mov_b64 v[40:41],v[200:201]\nbuffer_load_dwordx4 v6,s[8:11],s97 offen offset:2048 lds\nv_mov_b64 v[42:43],v[202:203]\nv_mov_b64 v[44:45],v[204:205]\n" \
"v_mov_b64 v[46:47],v[206:207]\nv_mov_b64 v[56:57],v[242:243]\nv_mov_b64 v[58:59],v[244:245]\nv_mov_b32 v60,v249\n" \
"v_mov_b32 v61,v250\nv_add_u32 v48,v234,v112\nv_add_u32 v49,v235,v112\nbuffer_load_dwordx4 v7,s[8:11],s97 offen offset:3072 lds\n" \
"v_add_u32 v50,v236,v112\nv_add_u32 v51,v237,v112\nv_add_u32 v52,v238,v112\nv_add_u32 v53,v239,v112\n" \
"v_add_u32 v54,v240,v112\nv_add_u32_e64 v55,v241,v112\ns_add_u32 s49,s48,4096\ns_add_u32 s50,s48,8192\n" \
"s_add_u32 s51,s48,12288\ns_add_u32 m0,s57,16384\ns_add_u32 s53,s52,3276800\ns_add_u32 s83,s52,2048000\n" \
"s_mov_b32 s55,0xbdd2d3e7\ns_mul_i32 s56,s3,9568\ns_lshl_b32 s58,s3,13\ns_add_u32 s75,s59,196608\n" \
"s_add_u32 s66,s65,128\ns_add_u32 s67,s67,163200\ns_add_u32 s69,s68,3072\ns_add_u32 s70,s68,6144\n" \
"s_add_u32 s80,s79,4096\ns_add_u32 s81,s79,8192\ns_add_u32 s82,s79,12288\ns_and_b32 s84,s33,1\n" \
"s_add_u32 s85,s85,65536\ns_add_u32 s87,s12,65536\ns_add_u32 s88,s74,819200\ns_add_u32 s86,s86,0x10000\n" \
"s_mov_b32 s90,7405568\ns_add_u32 s91,s24,69206016\ns_sub_u32 s98,s57,s12\ns_mov_b32 s99,s12\n" \
"s_add_u32 s95,s92,147456\nbuffer_load_dwordx4 v4,s[8:11],s95 offen lds\ns_add_u32 s101,s92,221184\nbuffer_load_dwordx4 v5,s[8:11],s95 offen offset:1024 lds\n" \
"buffer_load_dwordx4 v6,s[8:11],s95 offen offset:2048 lds\nbuffer_load_dwordx4 v7,s[8:11],s95 offen offset:3072 lds\ns_add_u32 m0,s57,20480\ns_nop 0\n" \
"buffer_load_dwordx4 v4,s[8:11],s100 offen lds\nbuffer_load_dwordx4 v5,s[8:11],s100 offen offset:1024 lds\nbuffer_load_dwordx4 v6,s[8:11],s100 offen offset:2048 lds\nbuffer_load_dwordx4 v7,s[8:11],s100 offen offset:3072 lds\n" \
"s_add_u32 m0,s57,24576\ns_nop 0\nbuffer_load_dwordx4 v4,s[8:11],s101 offen lds\nbuffer_load_dwordx4 v5,s[8:11],s101 offen offset:1024 lds\n" \
"buffer_load_dwordx4 v6,s[8:11],s101 offen offset:2048 lds\nbuffer_load_dwordx4 v7,s[8:11],s101 offen offset:3072 lds\nbuffer_load_dwordx4 v[104:107],v1,s[36:39],s7 offen sc1\nbuffer_load_dwordx4 v[108:111],v1,s[36:39],s7 offen offset:1024 sc1\n" \
"s_waitcnt vmcnt(1)\nv_xor_b32 v112,s29,v105\nv_xor_b32 v113,s29,v107\ns_waitcnt vmcnt(0)\n" \
"v_xor_b32 v114,s29,v109\nv_xor_b32 v115,s29,v111\nv_or3_b32 v112,v112,v113,v114\nv_xor_b32 v113,s28,v104\n" \
"v_xor_b32 v114,s28,v106\nv_or_b32 v112,v112,v115\nv_xor_b32 v115,s28,v108\nv_lshl_or_b32 v112,v112,8,v113\n" \
"v_xor_b32 v113,s28,v110\nv_or3_b32 v112,v112,v114,v115\nv_or_b32 v112,v112,v113\nv_cmp_ne_u32 vcc,0,v112\n" \
"s_nop 0\ns_cbranch_vccnz .LsrP_%=\n.LbrP_%=:\nv_min_u32 v120,2288,v2\n" \
"s_mov_b32 s5,26624\nv_max3_u32 v112,v105,v107,v109\nv_max_u32 v112,v112,v111\ns_mov_b32 s6,31232\n" \
"v_or_b32 v102,s2,v0\nv_max_u32_dpp v112,v112,v112 quad_perm:[1,0,3,2] row_mask:0xf bank_mask:0xf bound_ctrl:1\nv_bfrev_b32 v103,1\nv_cmp_eq_u32 vcc,0,v102\n" \
"v_max_u32_dpp v112,v112,v112 quad_perm:[2,3,0,1] row_mask:0xf bank_mask:0xf bound_ctrl:1\nv_mov_b32 v100,s28\nv_cndmask_b32 v102,v103,v102,vcc\nv_max_u32_dpp v112,v112,v112 row_half_mirror row_mask:0xf bank_mask:0xf bound_ctrl:1\n" \
"v_mov_b32 v101,s29\nbuffer_store_dwordx2 v[100:101],v102,s[36:39],0 offen sc1\nv_max_u32_dpp v112,v112,v112 row_mirror row_mask:0xf bank_mask:0xf bound_ctrl:1\nds_read_b64_tr_b16 a[0:1],v8\n" \
"v_readlane_b32 s94,v112,0\nv_readlane_b32 s31,v112,16\nv_readlane_b32 s96,v112,32\nv_readlane_b32 s97,v112,48\n" \
"s_max_u32 s31,s31,s96\ns_max_u32 s94,s94,s97\ns_max_u32 s94,s94,s31\ns_lshr_b32 s94,s94,24\n" \
"s_max_u32 s94,s94,64\ns_min_u32 s94,s94,250\nds_read_b64_tr_b16 a[4:5],v9\nds_read_b64_tr_b16 a[8:9],v10\n" \
"ds_read_b64_tr_b16 a[12:13],v11\nds_read_b64_tr_b16 a[16:17],v8 offset:2048\nds_read_b64_tr_b16 a[20:21],v9 offset:2048\nds_read_b64_tr_b16 a[24:25],v10 offset:2048\n" \
"ds_read_b64_tr_b16 a[28:29],v11 offset:2048\ns_add_u32 s95,s92,9658368\ns_add_u32 m0,s57,0\nds_read_b64_tr_b16 a[2:3],v8 offset:4096\n" \
"s_waitcnt lgkmcnt(1)\nbuffer_load_dwordx4 v4,s[8:11],s95 offen lds\nbuffer_load_dwordx4 v5,s[8:11],s95 offen offset:1024 lds\nbuffer_load_dwordx4 v6,s[8:11],s95 offen offset:2048 lds\n" \
"buffer_load_dwordx4 v7,s[8:11],s95 offen offset:3072 lds\nds_read_b64_tr_b16 a[6:7],v9 offset:4096\nds_read_b64_tr_b16 a[10:11],v10 offset:4096\nds_read_b64_tr_b16 a[14:15],v11 offset:4096\n" \
"ds_read_b64_tr_b16 a[18:19],v8 offset:6144\nds_read_b64_tr_b16 a[22:23],v9 offset:6144\nds_read_b64_tr_b16 a[26:27],v10 offset:6144\nds_read_b64_tr_b16 a[30:31],v11 offset:6144\n" \
"s_add_u32 s100,s92,294912\ns_add_u32 m0,s57,4096\nds_read_b64_tr_b16 a[32:33],v8 offset:8192\ns_waitcnt lgkmcnt(1)\n" \
"buffer_load_dwordx4 v4,s[8:11],s100 offen lds\nbuffer_load_dwordx4 v5,s[8:11],s100 offen offset:1024 lds\nbuffer_load_dwordx4 v6,s[8:11],s100 offen offset:2048 lds\nbuffer_load_dwordx4 v7,s[8:11],s100 offen offset:3072 lds\n" \
"ds_read_b64_tr_b16 a[36:37],v9 offset:8192\nds_read_b64_tr_b16 a[40:41],v10 offset:8192\nds_read_b64_tr_b16 a[44:45],v11 offset:8192\nds_read_b64_tr_b16 a[48:49],v8 offset:10240\n" \
"ds_read_b64_tr_b16 a[52:53],v9 offset:10240\nds_read_b64_tr_b16 a[56:57],v10 offset:10240\nds_read_b64_tr_b16 a[60:61],v11 offset:10240\ns_add_u32 s101,s92,9732096\n" \
"s_add_u32 m0,s57,8192\nds_read_b64_tr_b16 a[34:35],v8 offset:12288\ns_waitcnt lgkmcnt(1)\nbuffer_load_dwordx4 v4,s[8:11],s101 offen lds\n" \
"buffer_load_dwordx4 v5,s[8:11],s101 offen offset:1024 lds\nbuffer_load_dwordx4 v6,s[8:11],s101 offen offset:2048 lds\nbuffer_load_dwordx4 v7,s[8:11],s101 offen offset:3072 lds\nds_read_b64_tr_b16 a[38:39],v9 offset:12288\n" \
"ds_read_b64_tr_b16 a[42:43],v10 offset:12288\nds_read_b64_tr_b16 a[46:47],v11 offset:12288\nds_read_b64_tr_b16 a[50:51],v8 offset:14336\nds_read_b64_tr_b16 a[54:55],v9 offset:14336\n" \
"ds_read_b64_tr_b16 a[58:59],v10 offset:14336\nds_read_b64_tr_b16 a[62:63],v11 offset:14336\ns_add_u32 s31,s92,368640\ns_add_u32 m0,s57,12288\n" \
"ds_read_b64_tr_b16 a[64:65],v8 offset:16384\ns_waitcnt lgkmcnt(1)\nbuffer_load_dwordx4 v4,s[8:11],s31 offen lds\nbuffer_load_dwordx4 v5,s[8:11],s31 offen offset:1024 lds\n" \
"buffer_load_dwordx4 v6,s[8:11],s31 offen offset:2048 lds\nbuffer_load_dwordx4 v7,s[8:11],s31 offen offset:3072 lds\nbuffer_load_dwordx4 v[80:83],v120,s[36:39],s5 offen sc1\nbuffer_load_dwordx4 v[84:87],v120,s[36:39],s5 offen offset:2304 sc1\n" \
"buffer_load_dwordx4 v[88:91],v120,s[36:39],s6 offen sc1\nbuffer_load_dwordx4 v[92:95],v120,s[36:39],s6 offen offset:2304 sc1\nds_read_b64_tr_b16 a[68:69],v9 offset:16384\nds_read_b64_tr_b16 a[72:73],v10 offset:16384\n" \
"ds_read_b64_tr_b16 a[76:77],v11 offset:16384\nds_read_b64_tr_b16 a[80:81],v8 offset:18432\nds_read_b64_tr_b16 a[84:85],v9 offset:18432\nds_read_b64_tr_b16 a[88:89],v10 offset:18432\n" \
"ds_read_b64_tr_b16 a[92:93],v11 offset:18432\ns_add_u32 s96,s92,9805824\ns_add_u32 m0,s57,16384\nds_read_b64_tr_b16 a[66:67],v8 offset:20480\n" \
"s_waitcnt lgkmcnt(1)\nbuffer_load_dwordx4 v4,s[8:11],s96 offen lds\nbuffer_load_dwordx4 v5,s[8:11],s96 offen offset:1024 lds\nbuffer_load_dwordx4 v6,s[8:11],s96 offen offset:2048 lds\n" \
"buffer_load_dwordx4 v7,s[8:11],s96 offen offset:3072 lds\nds_read_b64_tr_b16 a[70:71],v9 offset:20480\nds_read_b64_tr_b16 a[74:75],v10 offset:20480\nds_read_b64_tr_b16 a[78:79],v11 offset:20480\n" \
"ds_read_b64_tr_b16 a[82:83],v8 offset:22528\nds_read_b64_tr_b16 a[86:87],v9 offset:22528\nds_read_b64_tr_b16 a[90:91],v10 offset:22528\nds_read_b64_tr_b16 a[94:95],v11 offset:22528\n" \
"s_add_u32 s97,s92,442368\ns_add_u32 m0,s57,20480\nds_read_b64_tr_b16 a[96:97],v8 offset:24576\ns_waitcnt lgkmcnt(1)\n" \
"buffer_load_dwordx4 v4,s[8:11],s97 offen lds\nbuffer_load_dwordx4 v5,s[8:11],s97 offen offset:1024 lds\nbuffer_load_dwordx4 v6,s[8:11],s97 offen offset:2048 lds\nbuffer_load_dwordx4 v7,s[8:11],s97 offen offset:3072 lds\n" \
"ds_read_b64_tr_b16 a[100:101],v9 offset:24576\nds_read_b64_tr_b16 a[104:105],v10 offset:24576\nds_read_b64_tr_b16 a[108:109],v11 offset:24576\nds_read_b64_tr_b16 a[112:113],v8 offset:26624\n" \
"ds_read_b64_tr_b16 a[116:117],v9 offset:26624\nds_read_b64_tr_b16 a[120:121],v10 offset:26624\nds_read_b64_tr_b16 a[124:125],v11 offset:26624\ns_add_u32 s95,s92,9879552\n" \
"s_add_u32 m0,s57,24576\ns_waitcnt vmcnt(24)\nds_read_b64_tr_b16 a[98:99],v8\ns_waitcnt lgkmcnt(1)\n" \
"buffer_load_dwordx4 v4,s[8:11],s95 offen lds\nbuffer_load_dwordx4 v5,s[8:11],s95 offen offset:1024 lds\nbuffer_load_dwordx4 v6,s[8:11],s95 offen offset:2048 lds\nbuffer_load_dwordx4 v7,s[8:11],s95 offen offset:3072 lds\n" \
"ds_read_b64_tr_b16 a[102:103],v9\nds_read_b64_tr_b16 a[106:107],v10\nds_read_b64_tr_b16 a[110:111],v11\nds_read_b64_tr_b16 a[114:115],v8 offset:2048\n" \
"ds_read_b64_tr_b16 a[118:119],v9 offset:2048\nds_read_b64_tr_b16 a[122:123],v10 offset:2048\nds_read_b64_tr_b16 a[126:127],v11 offset:2048\ns_add_u32 s100,s92,516096\n" \
"s_add_u32 m0,s57,0\ns_waitcnt vmcnt(24)\nds_read_b64_tr_b16 a[128:129],v8 offset:4096\ns_waitcnt lgkmcnt(1)\n" \
"buffer_load_dwordx4 v4,s[8:11],s100 offen lds\nbuffer_load_dwordx4 v5,s[8:11],s100 offen offset:1024 lds\nbuffer_load_dwordx4 v6,s[8:11],s100 offen offset:2048 lds\nbuffer_load_dwordx4 v7,s[8:11],s100 offen offset:3072 lds\n" \
"ds_read_b64_tr_b16 a[132:133],v9 offset:4096\nds_read_b64_tr_b16 a[136:137],v10 offset:4096\nds_read_b64_tr_b16 a[140:141],v11 offset:4096\nds_read_b64_tr_b16 a[144:145],v8 offset:6144\n" \
"ds_read_b64_tr_b16 a[148:149],v9 offset:6144\nds_read_b64_tr_b16 a[152:153],v10 offset:6144\nds_read_b64_tr_b16 a[156:157],v11 offset:6144\ns_add_u32 s101,s92,9953280\n" \
"s_add_u32 m0,s57,4096\ns_waitcnt vmcnt(24)\nds_read_b64_tr_b16 a[130:131],v8 offset:8192\ns_waitcnt lgkmcnt(1)\n" \
"buffer_load_dwordx4 v4,s[8:11],s101 offen lds\nbuffer_load_dwordx4 v5,s[8:11],s101 offen offset:1024 lds\nbuffer_load_dwordx4 v6,s[8:11],s101 offen offset:2048 lds\nbuffer_load_dwordx4 v7,s[8:11],s101 offen offset:3072 lds\n" \
"ds_read_b64_tr_b16 a[134:135],v9 offset:8192\nds_read_b64_tr_b16 a[138:139],v10 offset:8192\nds_read_b64_tr_b16 a[142:143],v11 offset:8192\nds_read_b64_tr_b16 a[146:147],v8 offset:10240\n" \
"ds_read_b64_tr_b16 a[150:151],v9 offset:10240\nds_read_b64_tr_b16 a[154:155],v10 offset:10240\nds_read_b64_tr_b16 a[158:159],v11 offset:10240\ns_bfe_u32 s26,s33,0x20001\n" \
"s_lshl2_add_u32 s15,s32,s26\ns_lshl2_add_u32 s15,s15,s3\ns_lshl_b32 s15,s15,14\ns_add_u32 s15,s15,5308416\n" \
"s_add_u32 s31,s15,0\ns_add_u32 m0,s57,8192\ns_waitcnt vmcnt(24)\nds_read_b64_tr_b16 a[160:161],v8 offset:12288\n" \
"s_waitcnt lgkmcnt(1)\nbuffer_load_dwordx4 v1,s[36:39],s31 offen sc1 lds\nbuffer_load_dwordx4 v1,s[36:39],s31 offen offset:1024 sc1 lds\nbuffer_load_dwordx4 v1,s[36:39],s31 offen offset:2048 sc1 lds\n" \
"buffer_load_dwordx4 v1,s[36:39],s31 offen offset:3072 sc1 lds\nds_read_b64_tr_b16 a[164:165],v9 offset:12288\nds_read_b64_tr_b16 a[168:169],v10 offset:12288\nds_read_b64_tr_b16 a[172:173],v11 offset:12288\n" \
"ds_read_b64_tr_b16 a[176:177],v8 offset:14336\nds_read_b64_tr_b16 a[180:181],v9 offset:14336\nds_read_b64_tr_b16 a[184:185],v10 offset:14336\nds_read_b64_tr_b16 a[188:189],v11 offset:14336\n" \
"s_add_u32 s96,s15,4096\ns_add_u32 m0,s57,12288\ns_waitcnt vmcnt(20)\nds_read_b64_tr_b16 a[162:163],v8 offset:16384\n" \
"s_waitcnt lgkmcnt(1)\nbuffer_load_dwordx4 v1,s[36:39],s96 offen sc1 lds\nbuffer_load_dwordx4 v1,s[36:39],s96 offen offset:1024 sc1 lds\nbuffer_load_dwordx4 v1,s[36:39],s96 offen offset:2048 sc1 lds\n" \
"buffer_load_dwordx4 v1,s[36:39],s96 offen offset:3072 sc1 lds\nds_read_b64_tr_b16 a[166:167],v9 offset:16384\nds_read_b64_tr_b16 a[170:171],v10 offset:16384\nds_read_b64_tr_b16 a[174:175],v11 offset:16384\n" \
"ds_read_b64_tr_b16 a[178:179],v8 offset:18432\nds_read_b64_tr_b16 a[182:183],v9 offset:18432\nds_read_b64_tr_b16 a[186:187],v10 offset:18432\nds_read_b64_tr_b16 a[190:191],v11 offset:18432\n" \
"s_add_u32 s97,s15,8192\ns_add_u32 m0,s57,16384\ns_waitcnt vmcnt(20)\nds_read_b64_tr_b16 a[192:193],v8 offset:20480\n" \
"s_waitcnt lgkmcnt(1)\nbuffer_load_dwordx4 v1,s[36:39],s97 offen sc1 lds\nbuffer_load_dwordx4 v1,s[36:39],s97 offen offset:1024 sc1 lds\nbuffer_load_dwordx4 v1,s[36:39],s97 offen offset:2048 sc1 lds\n" \
"buffer_load_dwordx4 v1,s[36:39],s97 offen offset:3072 sc1 lds\nds_read_b64_tr_b16 a[196:197],v9 offset:20480\nds_read_b64_tr_b16 a[200:201],v10 offset:20480\nds_read_b64_tr_b16 a[204:205],v11 offset:20480\n" \
"ds_read_b64_tr_b16 a[208:209],v8 offset:22528\nds_read_b64_tr_b16 a[212:213],v9 offset:22528\nds_read_b64_tr_b16 a[216:217],v10 offset:22528\nds_read_b64_tr_b16 a[220:221],v11 offset:22528\n" \
"s_add_u32 s95,s15,12288\ns_add_u32 m0,s57,20480\ns_waitcnt lgkmcnt(0)\nbuffer_load_dwordx4 v1,s[36:39],s95 offen sc1 lds\n" \
"buffer_load_dwordx4 v1,s[36:39],s95 offen offset:1024 sc1 lds\nbuffer_load_dwordx4 v1,s[36:39],s95 offen offset:2048 sc1 lds\nbuffer_load_dwordx4 v1,s[36:39],s95 offen offset:3072 sc1 lds\nv_cmp_ne_u64 vcc,s[28:29],v[80:81]\n" \
"v_cmp_ne_u64 s[24:25],s[28:29],v[84:85]\nv_cmp_ne_u64 s[26:27],s[28:29],v[88:89]\ns_or_b64 vcc,vcc,s[24:25]\nv_cmp_ne_u64 s[24:25],s[28:29],v[92:93]\n" \
"s_or_b64 vcc,vcc,s[26:27]\ns_or_b64 vcc,vcc,s[24:25]\ns_cbranch_vccnz .LsnP_%=\n.LbnP_%=:\n" \
"v_bfe_u32 v112,v0,1,5\nv_mul_u32_u24 v112,72,v112\nv_lshrrev_b32 v113,6,v0\nv_mul_u32_u24 v114,144,v0\n" \
"v_lshl_add_u32 v112,v113,5,v112\nv_subrev_u32 v114,18368,v114\nv_mov_b32 v115,1048576\nv_cmp_gt_u32 vcc,144,v0\n" \
"v_and_b32 v113,1,v0\nv_lshl_add_u32 v112,v113,4,v112\nv_cndmask_b32 v114,v115,v114,vcc\nv_and_b32 v97,0xffff0000,v82\n" \
"v_lshlrev_b32 v96,16,v82\ns_add_u32 s24,s93,s94\nv_cmp_gt_u32 vcc,128,v0\nv_add_u32 v113,8,v112\n" \
"v_mul_f32 v96,0x3ee38e39,v96\nv_mul_f32 v97,0x3ee38e39,v97\ns_sub_u32 s24,s24,253\nv_cndmask_b32 v112,v114,v112,vcc\n" \
"v_and_b32 v99,0xffff0000,v83\nv_ldexp_f32 v96,v96,s24\nv_ldexp_f32 v97,v97,s24\nv_lshlrev_b32_e64 v98,16,v83\n" \
"ds_write_b64 v112,v[96:97] offset:28800\nv_add_u32 v115,72,v114\nv_mul_f32 v98,0x3ee38e39,v98\nv_mul_f32 v99,0x3ee38e39,v99\n" \
"v_cndmask_b32_e64 v113,v115,v113,vcc\nv_ldexp_f32 v98,v98,s24\nv_ldexp_f32 v99,v99,s24\nds_write_b64 v113,v[98:99] offset:28800\n" \
"v_and_b32 v101,0xffff0000,v86\nv_lshlrev_b32 v100,16,v86\nds_write_b64 v112,v[100:101] offset:31104\nv_and_b32 v103,0xffff0000,v87\n" \
"v_lshlrev_b32 v102,16,v87\ns_lshr_b32 s25,s33,3\nds_write_b64 v113,v[102:103] offset:31104\ns_mul_i32 s25,s25,576\n" \
"v_and_b32 v105,0xffff0000,v90\nv_lshlrev_b32 v104,16,v90\ns_sub_u32 s25,163200,s25\nds_write_b64 v112,v[104:105] offset:33408\n" \
"v_add_u32 v114,s25,v2\nv_subrev_u32 v115,163200,v114\nv_and_b32 v107,0xffff0000,v91\nv_lshlrev_b32 v106,16,v91\n" \
"v_mov_b32 v116,1048576\nv_cmp_gt_u32 vcc,576,v115\nds_write_b64 v113,v[106:107] offset:33408\nv_and_b32 v109,0xffff0000,v94\n" \
"v_cndmask_b32 v114,v116,v114,vcc\nv_and_b32 v111,0xffff0000,v95\nv_lshlrev_b32 v108,16,v94\nv_lshlrev_b32 v110,16,v95\n" \
"ds_write_b128 v114,v[108:111]\nbuffer_load_dwordx4 v[112:115],v3,s[36:39],s16 offen sc1\nbuffer_load_dwordx4 v[116:119],v3,s[36:39],s17 offen sc1\nv_and_b32 v124,48,v0\n" \
"v_cmp_gt_u32 vcc,64,v0\nv_bfe_u32 v127,v0,5,1\nv_and_b32 v13,31,v0\nv_cndmask_b32_e64 v124,v124,v3,vcc\n" \
"buffer_load_dwordx4 v[120:123],v124,s[36:39],s18 offen sc1\nv_mul_u32_u24 v127,2448,v127\nv_mul_u32_u24 v13,72,v13\ns_mul_i32 s27,s3,4896\n" \
"v_add3_u32 v13,v13,v127,s27\nv_mov_b32 v127,0\ns_waitcnt vmcnt(27)\nds_read_b64_tr_b16 a[194:195],v8 offset:24576\n" \
"ds_read_b64_tr_b16 a[198:199],v9 offset:24576\ns_waitcnt lgkmcnt(2)\ns_barrier\nds_read_b64_tr_b16 a[202:203],v10 offset:24576\n" \
"ds_read_b64_tr_b16 a[206:207],v11 offset:24576\nds_read_b64_tr_b16 a[210:211],v8 offset:26624\nds_read_b64_tr_b16 a[214:215],v9 offset:26624\nds_read_b64_tr_b16 a[218:219],v10 offset:26624\n" \
"ds_read_b64_tr_b16 a[222:223],v11 offset:26624\ns_waitcnt vmcnt(23)\nds_read_b64_tr_b16 a[224:225],v8\nds_read_b64_tr_b16 a[228:229],v9\n" \
"ds_read_b64_tr_b16 a[232:233],v10\nds_read_b64_tr_b16 a[236:237],v11\nds_read_b64_tr_b16 a[240:241],v8 offset:2048\nds_read_b64_tr_b16 a[244:245],v9 offset:2048\n" \
"ds_read_b64_tr_b16 a[248:249],v10 offset:2048\nds_read_b64_tr_b16 a[252:253],v11 offset:2048\ns_waitcnt vmcnt(19)\nds_read_b64_tr_b16 a[226:227],v8 offset:4096\n" \
"ds_read_b64_tr_b16 a[230:231],v9 offset:4096\nds_read_b64_tr_b16 a[234:235],v10 offset:4096\nds_read_b64_tr_b16 a[238:239],v11 offset:4096\nds_read_b64_tr_b16 a[242:243],v8 offset:6144\n" \
"ds_read_b64_tr_b16 a[246:247],v9 offset:6144\nds_read_b64_tr_b16 a[250:251],v10 offset:6144\nds_read_b64_tr_b16 a[254:255],v11 offset:6144\ns_waitcnt vmcnt(15)\n" \
"ds_read_b64_tr_b16 v[192:193],v15 offset:8192\nds_read_b64_tr_b16 v[196:197],v15 offset:8704\nds_read_b64_tr_b16 v[194:195],v15 offset:9216\nds_read_b64_tr_b16 v[198:199],v15 offset:9728\n" \
"ds_read_b64_tr_b16 v[200:201],v15 offset:10240\nds_read_b64_tr_b16 v[204:205],v15 offset:10752\nds_read_b64_tr_b16 v[202:203],v15 offset:11264\nds_read_b64_tr_b16 v[206:207],v15 offset:11776\n" \
"s_waitcnt vmcnt(11)\nds_read_b64_tr_b16 v[208:209],v15 offset:12288\nds_read_b64_tr_b16 v[212:213],v15 offset:12800\nds_read_b64_tr_b16 v[210:211],v15 offset:13312\n" \
"ds_read_b64_tr_b16 v[214:215],v15 offset:13824\nds_read_b64_tr_b16 v[216:217],v15 offset:14336\nds_read_b64_tr_b16 v[220:221],v15 offset:14848\nds_read_b64_tr_b16 v[218:219],v15 offset:15360\n" \
"ds_read_b64_tr_b16 v[222:223],v15 offset:15872\ns_waitcnt vmcnt(7)\nds_read_b64_tr_b16 v[224:225],v15 offset:16384\nds_read_b64_tr_b16 v[228:229],v15 offset:16896\n" \
"ds_read_b64_tr_b16 v[226:227],v15 offset:17408\nds_read_b64_tr_b16 v[230:231],v15 offset:17920\nds_read_b64_tr_b16 v[232:233],v15 offset:18432\nds_read_b64_tr_b16 v[236:237],v15 offset:18944\n" \
"ds_read_b64_tr_b16 v[234:235],v15 offset:19456\nds_read_b64_tr_b16 v[238:239],v15 offset:19968\ns_waitcnt vmcnt(3)\nds_read_b64_tr_b16 v[240:241],v15 offset:20480\n" \
"ds_read_b64_tr_b16 v[244:245],v15 offset:20992\nds_read_b64_tr_b16 v[242:243],v15 offset:21504\nds_read_b64_tr_b16 v[246:247],v15 offset:22016\nds_read_b64_tr_b16 v[248:249],v15 offset:22528\n" \
"ds_read_b64_tr_b16 v[252:253],v15 offset:23040\nds_read_b64_tr_b16 v[250:251],v15 offset:23552\nds_read_b64_tr_b16 v[254:255],v15 offset:24064\ns_waitcnt vmcnt(1)\n" \
"v_and_b32 v125,v112,v116\ns_waitcnt vmcnt(0)\nv_bitop3_b32 v125,v125,v120,1 bitop3:0x80\nv_cmp_eq_u32 vcc,0,v125\n" \
"s_nop 0\ns_cbranch_vccnz .LsuP_%=\n.LbuP_%=:\nv_mfma_f32_16x16x32_f16 v[80:83],v[32:35],v[112:115],0\n" \
"v_and_b32 v3,31,v0\nv_mul_u32_u24 v3,72,v3\nv_mfma_f32_16x16x32_f16 v[84:87],v[36:39],v[112:115],0\nv_add_u32 v13,9216,v13\n" \
"v_mov_b32 v104,-1\nv_mfma_f32_16x16x32_f16 v[88:91],v[32:35],v[116:119],0\nv_mov_b32 v105,-1\nv_mov_b32 v106,-1\n" \
"v_mfma_f32_16x16x32_f16 v[92:95],v[36:39],v[116:119],0\nv_mov_b32 v107,-1\ns_add_u32 s27,s2,0\nv_mfma_f32_16x16x32_f16 v[96:99],v[32:35],v[120:123],0\n" \
"s_lshr_b32 s100,s27,4\ns_mul_i32 s101,s100,37\ns_lshr_b32 s101,s101,8\ns_lshl2_add_u32 s100,s101,s100\n" \
"s_mul_i32 s100,s100,409600\ns_and_b32 s101,s27,15\ns_lshl_b32 s101,s101,14\ns_add_u32 s100,s100,s101\n" \
"s_add_u32 s100,s100,13090816\ns_cmp_lt_u32 s27,158\ns_cselect_b32 s100,s100,2147483648\nbuffer_store_dwordx4 v[104:107],v2,s[36:39],s100 offen sc1\n" \
"v_mfma_f32_16x16x32_f16 v[100:103],v[36:39],v[120:123],0\nv_cvt_pk_f16_f32 v112,v84,v80\nv_cvt_pk_f16_f32 v113,v85,v81\nv_cvt_pk_f16_f32 v114,v86,v82\n" \
"v_cvt_pk_f16_f32 v115,v87,v83\nds_write_b128 v57,v[112:115]\nv_cvt_pk_f16_f32 v116,v92,v88\nv_cvt_pk_f16_f32 v117,v93,v89\n" \
"s_add_u32 s100,s100,4096\nbuffer_store_dwordx4 v[104:107],v2,s[36:39],s100 offen sc1\nv_cvt_pk_f16_f32 v118,v94,v90\nv_cvt_pk_f16_f32 v119,v95,v91\n" \
"ds_write_b128 v58,v[116:119]\nv_cvt_pk_f16_f32 v120,v100,v96\nv_cvt_pk_f16_f32 v121,v101,v97\nv_cvt_pk_f16_f32 v122,v102,v98\n" \
"v_cvt_pk_f16_f32 v123,v103,v99\nds_write_b128 v60,v[120:123]\ns_add_u32 s100,s100,4096\nbuffer_store_dwordx4 v[104:107],v2,s[36:39],s100 offen sc1\n" \
"v_add_u32 v60,31104,v3\nv_add_u32 v3,28800,v3\nds_read2_b64 v[32:35],v3 offset0:0 offset1:1\nds_read2_b64 v[36:39],v3 offset0:2 offset1:3\n" \
"s_add_u32 s100,s100,4096\ns_add_u32 s27,s2,111\ns_lshr_b32 s31,s27,4\ns_mul_i32 s96,s31,37\n" \
"s_lshr_b32 s96,s96,8\ns_lshl2_add_u32 s31,s96,s31\ns_mul_i32 s31,s31,409600\ns_and_b32 s96,s27,15\n" \
"s_lshl_b32 s96,s96,14\nbuffer_store_dwordx4 v[104:107],v2,s[36:39],s100 offen sc1\ns_add_u32 s31,s31,s96\ns_add_u32 s31,s31,13090816\n" \
"s_cmp_lt_u32 s27,158\ns_cselect_b32 s31,s31,2147483648\ns_waitcnt lgkmcnt(2)\ns_barrier\n" \
"ds_read_b64 v[112:113],v56\nds_read_b64 v[116:117],v56 offset:512\nds_read_b64 v[120:121],v56 offset:256\nds_read_b64 v[124:125],v56 offset:768\n" \
"ds_read_b64 v[114:115],v56 offset:1024\nds_read_b64 v[122:123],v56 offset:1280\nds_read_b64 v[56:57],v3 offset:64\nbuffer_store_dwordx4 v[104:107],v2,s[36:39],s31 offen sc1\n" \
"s_add_u32 s31,s31,4096\nv_bfe_u32 v110,v0,1,2\nv_bfe_u32 v111,v0,4,1\nv_xor_b32 v110,v110,v111\n" \
"v_and_b32 v111,1,v0\nv_lshl_add_u32 v110,v110,1,v111\nv_bfe_u32 v111,v0,3,3\nv_lshlrev_b32 v111,12,v111\n" \
"v_lshl_add_u32 v4,v110,4,v111\nv_xor_b32 v5,64,v4\nv_add_u32 v5,31744,v5\nv_add_u32 v6,63488,v4\n" \
"buffer_store_dwordx4 v[104:107],v2,s[36:39],s31 offen sc1\ns_waitcnt lgkmcnt(5)\nv_swap_b32 v113,v116\ns_add_u32 s31,s31,4096\n" \
"s_waitcnt lgkmcnt(3)\nv_swap_b32 v121,v124\nv_add_u32 v7,63488,v5\ns_waitcnt lgkmcnt(2)\n" \
"v_mfma_f32_16x16x32_f16 v[80:83],v[112:115],v[40:43],0\nv_mov_b32 v118,v115\ns_waitcnt lgkmcnt(1)\nv_mov_b32 v126,v123\n" \
"v_mfma_f32_16x16x32_f16 v[84:87],v[112:115],v[44:47],0\ns_lshr_b32 s26,s32,1\ns_mul_i32 s92,s26,1152\nv_mfma_f32_16x16x32_f16 v[88:91],v[120:123],v[40:43],0\n" \
"buffer_store_dwordx4 v[104:107],v2,s[36:39],s31 offen sc1\nv_mfma_f32_16x16x32_f16 v[92:95],v[120:123],v[44:47],0\ns_add_u32 s31,s31,4096\ns_lshr_b32 s26,s3,1\n" \
"v_mfma_f32_16x16x32_f16 v[96:99],v[116:119],v[40:43],0\ns_lshl_b32 s26,s26,6\ns_add_u32 s92,s92,s26\nv_mfma_f32_16x16x32_f16 v[100:103],v[116:119],v[44:47],0\n" \
"v_pk_add_f32 v[88:89],v[80:81],v[88:89] neg_lo:[0,1] neg_hi:[0,1]\nv_pk_add_f32 v[90:91],v[82:83],v[90:91] neg_lo:[0,1] neg_hi:[0,1]\nv_pk_add_f32 v[92:93],v[84:85],v[92:93] neg_lo:[0,1] neg_hi:[0,1]\nbuffer_store_dwordx4 v[104:107],v2,s[36:39],s31 offen sc1\n" \
"v_pk_add_f32 v[94:95],v[86:87],v[94:95] neg_lo:[0,1] neg_hi:[0,1]\nv_pk_fma_f32 v[80:81],v[80:81],2.0,v[88:89] op_sel_hi:[1,0,1] neg_lo:[0,0,1] neg_hi:[0,0,1]\nv_pk_fma_f32 v[82:83],v[82:83],2.0,v[90:91] op_sel_hi:[1,0,1] neg_lo:[0,0,1] neg_hi:[0,0,1]\nv_pk_fma_f32 v[84:85],v[84:85],2.0,v[92:93] op_sel_hi:[1,0,1] neg_lo:[0,0,1] neg_hi:[0,0,1]\n" \
"v_pk_fma_f32 v[86:87],v[86:87],2.0,v[94:95] op_sel_hi:[1,0,1] neg_lo:[0,0,1] neg_hi:[0,0,1]\nv_cvt_pk_f16_f32 v112,v80,v84\nv_cvt_pk_f16_f32 v113,v81,v85\nv_cvt_pk_f16_f32 v114,v82,v86\n" \
"v_cvt_pk_f16_f32 v115,v83,v87\nv_mfma_f32_16x16x32_f16 v[104:107],v[124:127],v[40:43],0\nv_cvt_pk_f16_f32 v116,v88,v92\ns_lshr_b32 s97,s33,3\n" \
"s_add_u32 s97,s97,0\ns_sub_u32 s95,s97,4\ns_min_u32 s97,s97,s95\ns_lshl_b32 s95,s97,12\n" \
"s_add_u32 s95,s95,s57\ns_add_u32 m0,s95,0\ns_and_b32 s95,s97,1\ns_lshr_b32 s97,s97,1\n" \
"s_lshl_b32 s97,s97,10\ns_add_u32 s97,s97,s95\ns_lshl_b32 s97,s97,7\ns_lshr_b32 s100,s33,3\n" \
"s_add_u32 s100,s100,1\ns_sub_u32 s101,s100,4\ns_min_u32 s100,s100,s101\ns_lshl_b32 s101,s100,12\n" \
"s_add_u32 s101,s101,s57\ns_lshr_b32 s31,s33,3\ns_add_u32 s31,s31,2\ns_sub_u32 s96,s31,4\n" \
"s_min_u32 s31,s31,s96\ns_lshl_b32 s96,s31,12\ns_add_u32 s96,s96,s57\nv_mfma_f32_16x16x32_f16 v[80:83],v[16:19],v[112:115],0\n" \
"v_cvt_pk_f16_f32 v117,v89,v93\nv_cvt_pk_f16_f32 v118,v90,v94\nv_mfma_f32_16x16x32_f16 v[84:87],v[20:23],v[112:115],0\nv_cvt_pk_f16_f32 v119,v91,v95\n" \
"v_pk_add_f32 v[104:105],v[96:97],v[104:105] neg_lo:[0,1] neg_hi:[0,1]\nv_mfma_f32_16x16x32_f16 v[108:111],v[124:127],v[44:47],0\nv_pk_add_f32 v[106:107],v[98:99],v[106:107] neg_lo:[0,1] neg_hi:[0,1]\nv_pk_fma_f32 v[96:97],v[96:97],2.0,v[104:105] op_sel_hi:[1,0,1] neg_lo:[0,0,1] neg_hi:[0,0,1]\n" \
"v_mfma_f32_16x16x32_f16 v[88:91],v[24:27],v[116:119],0\nv_pk_fma_f32 v[98:99],v[98:99],2.0,v[106:107] op_sel_hi:[1,0,1] neg_lo:[0,0,1] neg_hi:[0,0,1]\nds_read2_b64 v[40:43],v3 offset0:4 offset1:5\nv_mfma_f32_16x16x32_f16 v[92:95],v[28:31],v[116:119],0\n" \
"ds_write2_b32 v48,v84,v80 offset0:0 offset1:1\nds_write2_b32 v49,v85,v81 offset0:0 offset1:1\nds_write2_b32 v50,v86,v82 offset0:0 offset1:1\nv_pk_add_f32 v[108:109],v[100:101],v[108:109] neg_lo:[0,1] neg_hi:[0,1]\n" \
"v_pk_add_f32 v[110:111],v[102:103],v[110:111] neg_lo:[0,1] neg_hi:[0,1]\nv_pk_fma_f32 v[100:101],v[100:101],2.0,v[108:109] op_sel_hi:[1,0,1] neg_lo:[0,0,1] neg_hi:[0,0,1]\nv_pk_fma_f32 v[102:103],v[102:103],2.0,v[110:111] op_sel_hi:[1,0,1] neg_lo:[0,0,1] neg_hi:[0,0,1]\nds_write2_b32 v51,v87,v83 offset0:0 offset1:1\n" \
"v_cvt_pk_f16_f32 v120,v96,v100\nv_cvt_pk_f16_f32 v121,v97,v101\nv_cvt_pk_f16_f32 v122,v98,v102\nv_cvt_pk_f16_f32 v123,v99,v103\n" \
"ds_write2_b32 v52,v92,v88 offset0:0 offset1:1\nds_write2_b32 v53,v93,v89 offset0:0 offset1:1\nv_mfma_f32_16x16x32_f16 v[96:99],v[16:19],v[120:123],0\nds_write2_b32 v54,v94,v90 offset0:0 offset1:1\n" \
"v_mfma_f32_16x16x32_f16 v[100:103],v[20:23],v[120:123],0\nds_write2_b32 v55,v95,v91 offset0:0 offset1:1\nv_cvt_pk_f16_f32 v124,v104,v108\nv_cvt_pk_f16_f32 v125,v105,v109\n" \
"v_cvt_pk_f16_f32 v126,v106,v110\nv_cvt_pk_f16_f32 v127,v107,v111\nv_add_u32 v48,2448,v48\nv_add_u32 v49,2448,v49\n" \
"v_mfma_f32_16x16x32_f16 v[104:107],v[24:27],v[124:127],0\nds_write2_b32 v48,v100,v96 offset0:0 offset1:1\nv_mfma_f32_16x16x32_f16 v[108:111],v[28:31],v[124:127],0\nds_write2_b32 v49,v101,v97 offset0:0 offset1:1\n" \
"v_add_u32 v50,2448,v50\nds_write2_b32 v50,v102,v98 offset0:0 offset1:1\nv_add_u32 v51,2448,v51\nds_write2_b32 v51,v103,v99 offset0:0 offset1:1\n" \
"v_add_u32 v52,2448,v52\nv_add_u32 v53,2448,v53\nv_add_u32 v54,2448,v54\nds_write2_b32 v52,v108,v104 offset0:0 offset1:1\n" \
"ds_write2_b32 v53,v109,v105 offset0:0 offset1:1\nds_write2_b32 v54,v110,v106 offset0:0 offset1:1\nv_add_u32 v55,2448,v55\nds_write2_b32 v55,v111,v107 offset0:0 offset1:1\n" \
"ds_read2_b64 v[80:83],v13 offset0:0 offset1:1\nds_read2_b64 v[84:87],v13 offset0:2 offset1:3\ns_lshl_b32 s26,s3,8\ns_add_u32 s92,s92,s26\n" \
"s_lshl_b32 s92,s92,4\ns_and_b32 s26,s32,1\ns_lshl3_add_u32 s92,s26,s92\ns_and_b32 s26,s33,7\n" \
"s_add_u32 s92,s92,s26\nds_read2_b64 v[88:91],v13 offset0:4 offset1:5\ns_lshl_b32 s92,s92,8\ns_add_u32 s97,s97,s92\n" \
"buffer_load_dwordx4 v4,s[40:43],s97 offen lds\nds_read2_b64 v[44:47],v3 offset0:6 offset1:7\nv_and_b32 v48,31,v0\nds_read2_b64 v[92:95],v13 offset0:6 offset1:7\n" \
"v_mad_u32_u24 v49,v48,56,0\nv_sub_u32 v48,v13,v49\nv_and_b32 v49,31,v0\nv_mul_u32_u24 v49,68,v49\n" \
"v_sub_u32 v49,v13,v49\nbuffer_load_dwordx4 v5,s[40:43],s97 offen offset:1024 lds\nds_read_b64 v[96:97],v13 offset:64\ns_waitcnt lgkmcnt(5)\n" \
"v_pk_fma_f32 v[62:63],v[80:81],v[32:33],v[62:63]\nv_pk_fma_f32 v[64:65],v[82:83],v[34:35],v[64:65]\nv_pk_mul_f32 v[102:103],v[62:63],v[62:63]\ns_waitcnt lgkmcnt(4)\n" \
"v_pk_fma_f32 v[66:67],v[84:85],v[36:37],v[66:67]\nv_pk_add_f32 v[100:101],v[62:63],v[64:65]\nv_pk_fma_f32 v[102:103],v[64:65],v[64:65],v[102:103]\nv_pk_fma_f32 v[68:69],v[86:87],v[38:39],v[68:69]\n" \
"buffer_load_dwordx4 v6,s[40:43],s97 offen offset:2048 lds\nv_pk_add_f32 v[100:101],v[100:101],v[66:67]\nv_pk_fma_f32 v[102:103],v[66:67],v[66:67],v[102:103]\nds_read2_b64 v[112:115],v60 offset0:0 offset1:1\n" \
"ds_read2_b64 v[116:119],v60 offset0:2 offset1:3\ns_waitcnt lgkmcnt(5)\nv_pk_fma_f32 v[70:71],v[88:89],v[40:41],v[70:71]\nv_pk_add_f32 v[100:101],v[100:101],v[68:69]\n" \
"ds_read2_b64 v[120:123],v60 offset0:4 offset1:5\nv_pk_fma_f32 v[102:103],v[68:69],v[68:69],v[102:103]\nbuffer_load_dwordx4 v7,s[40:43],s97 offen offset:3072 lds\nds_read2_b64 v[124:127],v60 offset0:6 offset1:7\n" \
"v_pk_fma_f32 v[72:73],v[90:91],v[42:43],v[72:73]\nv_pk_add_f32 v[100:101],v[100:101],v[70:71]\nds_read_b64 v[98:99],v60 offset:64\nv_add_u32 v60,2304,v60\n" \
"v_pk_fma_f32 v[102:103],v[70:71],v[70:71],v[102:103]\nds_read2_b64 v[16:19],v60 offset0:0 offset1:1\ns_waitcnt lgkmcnt(7)\nv_pk_fma_f32 v[74:75],v[92:93],v[44:45],v[74:75]\n" \
"s_add_u32 m0,s101,0\ns_and_b32 s101,s100,1\ns_lshr_b32 s100,s100,1\ns_lshl_b32 s100,s100,10\n" \
"s_add_u32 s100,s100,s101\ns_lshl_b32 s100,s100,7\ns_add_u32 s100,s100,s92\nbuffer_load_dwordx4 v4,s[40:43],s100 offen lds\n" \
"v_pk_add_f32 v[100:101],v[100:101],v[72:73]\nv_pk_fma_f32 v[102:103],v[72:73],v[72:73],v[102:103]\nv_pk_fma_f32 v[76:77],v[94:95],v[46:47],v[76:77]\nv_pk_add_f32 v[100:101],v[100:101],v[74:75]\n" \
"v_pk_fma_f32 v[102:103],v[74:75],v[74:75],v[102:103]\nds_read2_b64 v[20:23],v60 offset0:2 offset1:3\ns_waitcnt lgkmcnt(7)\nv_pk_fma_f32 v[78:79],v[96:97],v[56:57],v[78:79]\n" \
"v_pk_add_f32 v[100:101],v[100:101],v[76:77]\nbuffer_load_dwordx4 v5,s[40:43],s100 offen offset:1024 lds\nv_pk_fma_f32 v[102:103],v[76:77],v[76:77],v[102:103]\nv_pk_add_f32 v[100:101],v[100:101],v[78:79]\n" \
"v_pk_fma_f32 v[102:103],v[78:79],v[78:79],v[102:103]\nds_read2_b64 v[24:27],v60 offset0:4 offset1:5\nv_add_f32 v100,v100,v101\nv_add_f32 v102,v102,v103\n" \
"ds_read2_b64 v[28:31],v60 offset0:6 offset1:7\nds_read_b64 v[58:59],v60 offset:64\nbuffer_load_dwordx4 v6,s[40:43],s100 offen offset:2048 lds\nv_permlane16_swap_b32 v100,v102\n" \
"v_add_f32 v100,v100,v102\ns_lshr_b32 s97,s33,3\ns_add_u32 s97,s97,3\nv_add_f32_dpp v100,v100,v100 quad_perm:[1,0,3,2] row_mask:0xf bank_mask:0xf bound_ctrl:1\n" \
"s_sub_u32 s95,s97,4\ns_min_u32 s97,s97,s95\nv_add_f32_dpp v100,v100,v100 quad_perm:[2,3,0,1] row_mask:0xf bank_mask:0xf bound_ctrl:1\nbuffer_load_dwordx4 v7,s[40:43],s100 offen offset:3072 lds\n" \
"s_add_u32 m0,s96,0\nv_add_f32_dpp v100,v100,v100 row_half_mirror row_mask:0xf bank_mask:0xf bound_ctrl:1\ns_and_b32 s96,s31,1\ns_lshr_b32 s31,s31,1\n" \
"v_add_f32_dpp v100,v100,v100 row_mirror row_mask:0xf bank_mask:0xf bound_ctrl:1\nv_mul_f32 v104,0x3ae38e39,v100\nv_mul_f32 v102,0x3ae38e39,v100\ns_lshl_b32 s31,s31,10\n" \
"s_add_u32 s31,s31,s96\ns_lshl_b32 s31,s31,7\ns_add_u32 s31,s31,s92\nbuffer_load_dwordx4 v4,s[40:43],s31 offen lds\n" \
"v_permlane16_swap_b32 v104,v102\nv_fma_f32 v102,-v104,v104,v102\nv_max_f32 v102,0,v102\nv_add_f32 v102,0x3727c5ac,v102\n" \
"v_rsq_f32 v100,v102\ns_lshl_b32 s95,s97,12\nbuffer_load_dwordx4 v5,s[40:43],s31 offen offset:1024 lds\nv_mul_f32 v101,-v104,v100\n" \
"v_pk_fma_f32 v[80:81],v[62:63],v[100:101],v[100:101] op_sel:[0,0,1] op_sel_hi:[1,0,1]\nv_pk_fma_f32 v[82:83],v[64:65],v[100:101],v[100:101] op_sel:[0,0,1] op_sel_hi:[1,0,1]\nv_pk_fma_f32 v[84:85],v[66:67],v[100:101],v[100:101] op_sel:[0,0,1] op_sel_hi:[1,0,1]\nv_pk_fma_f32 v[86:87],v[68:69],v[100:101],v[100:101] op_sel:[0,0,1] op_sel_hi:[1,0,1]\n" \
"v_pk_fma_f32 v[88:89],v[70:71],v[100:101],v[100:101] op_sel:[0,0,1] op_sel_hi:[1,0,1]\nv_pk_fma_f32 v[90:91],v[72:73],v[100:101],v[100:101] op_sel:[0,0,1] op_sel_hi:[1,0,1]\nv_pk_fma_f32 v[92:93],v[74:75],v[100:101],v[100:101] op_sel:[0,0,1] op_sel_hi:[1,0,1]\nbuffer_load_dwordx4 v6,s[40:43],s31 offen offset:2048 lds\n" \
"v_pk_fma_f32 v[94:95],v[76:77],v[100:101],v[100:101] op_sel:[0,0,1] op_sel_hi:[1,0,1]\nv_pk_fma_f32 v[96:97],v[78:79],v[100:101],v[100:101] op_sel:[0,0,1] op_sel_hi:[1,0,1]\ns_add_u32 s95,s95,s57\ns_waitcnt lgkmcnt(9)\n" \
"v_readlane_b32 s24,v112,0\nv_mov_b32 v60,v61\ns_mov_b32 s25,s24\nv_pk_add_f32 v[112:113],v[112:113],s[24:25] neg_lo:[0,1] neg_hi:[0,1]\n" \
"s_waitcnt lgkmcnt(4)\nv_pk_fma_f32 v[80:81],v[80:81],v[16:17],v[112:113]\nbuffer_load_dwordx4 v7,s[40:43],s31 offen offset:3072 lds\nv_pk_mul_f32 v[102:103],v[80:81],v[80:81]\n" \
"s_add_u32 m0,s95,0\ns_and_b32 s95,s97,1\ns_lshr_b32 s97,s97,1\ns_lshl_b32 s97,s97,10\n" \
"s_add_u32 s97,s97,s95\ns_lshl_b32 s97,s97,7\ns_add_u32 s97,s97,s92\nbuffer_load_dwordx4 v4,s[40:43],s97 offen lds\n" \
"v_pk_add_f32 v[114:115],v[114:115],s[24:25] neg_lo:[0,1] neg_hi:[0,1]\nv_pk_fma_f32 v[82:83],v[82:83],v[18:19],v[114:115]\nv_pk_add_f32 v[100:101],v[80:81],v[82:83]\nv_pk_fma_f32 v[102:103],v[82:83],v[82:83],v[102:103]\n" \
"v_pk_add_f32 v[116:117],v[116:117],s[24:25] neg_lo:[0,1] neg_hi:[0,1]\ns_waitcnt lgkmcnt(3)\nv_pk_fma_f32 v[84:85],v[84:85],v[20:21],v[116:117]\nv_pk_add_f32 v[100:101],v[100:101],v[84:85]\n" \
"buffer_load_dwordx4 v5,s[40:43],s97 offen offset:1024 lds\nv_pk_fma_f32 v[102:103],v[84:85],v[84:85],v[102:103]\nv_pk_add_f32 v[118:119],v[118:119],s[24:25] neg_lo:[0,1] neg_hi:[0,1]\nv_pk_fma_f32 v[86:87],v[86:87],v[22:23],v[118:119]\n" \
"v_pk_add_f32 v[100:101],v[100:101],v[86:87]\nv_pk_fma_f32 v[102:103],v[86:87],v[86:87],v[102:103]\nv_pk_add_f32 v[120:121],v[120:121],s[24:25] neg_lo:[0,1] neg_hi:[0,1]\ns_waitcnt lgkmcnt(2)\n" \
"v_pk_fma_f32 v[88:89],v[88:89],v[24:25],v[120:121]\nbuffer_load_dwordx4 v6,s[40:43],s97 offen offset:2048 lds\nv_pk_add_f32 v[100:101],v[100:101],v[88:89]\nv_pk_fma_f32 v[102:103],v[88:89],v[88:89],v[102:103]\n" \
"v_pk_add_f32 v[122:123],v[122:123],s[24:25] neg_lo:[0,1] neg_hi:[0,1]\nv_pk_fma_f32 v[90:91],v[90:91],v[26:27],v[122:123]\nv_pk_add_f32 v[100:101],v[100:101],v[90:91]\nv_pk_fma_f32 v[102:103],v[90:91],v[90:91],v[102:103]\n" \
"v_pk_add_f32 v[124:125],v[124:125],s[24:25] neg_lo:[0,1] neg_hi:[0,1]\nbuffer_load_dwordx4 v7,s[40:43],s97 offen offset:3072 lds\ns_waitcnt lgkmcnt(1)\nv_pk_fma_f32 v[92:93],v[92:93],v[28:29],v[124:125]\n" \
"v_pk_add_f32 v[100:101],v[100:101],v[92:93]\nv_pk_fma_f32 v[102:103],v[92:93],v[92:93],v[102:103]\nv_pk_add_f32 v[126:127],v[126:127],s[24:25] neg_lo:[0,1] neg_hi:[0,1]\nv_pk_fma_f32 v[94:95],v[94:95],v[30:31],v[126:127]\n" \
"v_pk_add_f32 v[100:101],v[100:101],v[94:95]\nv_pk_fma_f32 v[102:103],v[94:95],v[94:95],v[102:103]\nv_pk_add_f32 v[98:99],v[98:99],s[24:25] neg_lo:[0,1] neg_hi:[0,1]\ns_waitcnt lgkmcnt(0)\n" \
"v_pk_fma_f32 v[96:97],v[96:97],v[58:59],v[98:99]\nv_pk_add_f32 v[100:101],v[100:101],v[96:97]\nv_pk_fma_f32 v[102:103],v[96:97],v[96:97],v[102:103]\nv_add_f32 v100,v100,v101\n" \
"v_add_f32 v102,v102,v103\nv_pk_add_f32 v[62:63],v[62:63],v[60:61] op_sel_hi:[1,0]\nv_cvt_pk_bf16_f32 v32,v62,v63\nv_permlane16_swap_b32 v100,v102\n" \
"v_add_f32 v100,v100,v102\nv_pk_add_f32 v[64:65],v[64:65],v[60:61] op_sel_hi:[1,0]\nv_cvt_pk_bf16_f32 v33,v64,v65\nv_add_f32_dpp v100,v100,v100 quad_perm:[1,0,3,2] row_mask:0xf bank_mask:0xf bound_ctrl:1\n" \
"v_pk_add_f32 v[66:67],v[66:67],v[60:61] op_sel_hi:[1,0]\nv_cvt_pk_bf16_f32 v34,v66,v67\nv_add_f32_dpp v100,v100,v100 quad_perm:[2,3,0,1] row_mask:0xf bank_mask:0xf bound_ctrl:1\nv_pk_add_f32 v[68:69],v[68:69],v[60:61] op_sel_hi:[1,0]\n" \
"v_cvt_pk_bf16_f32 v35,v68,v69\nv_add_f32_dpp v100,v100,v100 row_half_mirror row_mask:0xf bank_mask:0xf bound_ctrl:1\nv_pk_add_f32 v[70:71],v[70:71],v[60:61] op_sel_hi:[1,0]\nv_cvt_pk_bf16_f32 v36,v70,v71\n" \
"v_add_f32_dpp v100,v100,v100 row_mirror row_mask:0xf bank_mask:0xf bound_ctrl:1\nv_mul_f32 v104,0x3ae38e39,v100\nv_mul_f32 v102,0x3ae38e39,v100\nv_pk_add_f32 v[72:73],v[72:73],v[60:61] op_sel_hi:[1,0]\n" \
"v_cvt_pk_bf16_f32 v37,v72,v73\nv_permlane16_swap_b32 v104,v102\nv_fma_f32 v102,-v104,v104,v102\nv_max_f32 v102,0,v102\n" \
"v_add_f32 v102,0x3727c5ac,v102\nv_rsq_f32_e64 v100,v102\nv_pk_add_f32 v[74:75],v[74:75],v[60:61] op_sel_hi:[1,0]\nv_mul_f32 v101,-v104,v100\n" \
"v_pk_fma_f32 v[80:81],v[80:81],v[100:101],v[100:101] op_sel:[0,0,1] op_sel_hi:[1,0,1]\nv_pk_fma_f32 v[82:83],v[82:83],v[100:101],v[100:101] op_sel:[0,0,1] op_sel_hi:[1,0,1]\nv_pk_fma_f32 v[84:85],v[84:85],v[100:101],v[100:101] op_sel:[0,0,1] op_sel_hi:[1,0,1]\nv_pk_fma_f32 v[86:87],v[86:87],v[100:101],v[100:101] op_sel:[0,0,1] op_sel_hi:[1,0,1]\n" \
"v_cvt_pk_bf16_f32 v16,v80,v81\nv_cvt_pk_bf16_f32 v17,v82,v83\nv_cvt_pk_bf16_f32 v18,v84,v85\nv_cvt_pk_bf16_f32 v19,v86,v87\n" \
"ds_write_b128 v48,v[16:19]\nv_pk_fma_f32 v[88:89],v[88:89],v[100:101],v[100:101] op_sel:[0,0,1] op_sel_hi:[1,0,1]\nv_pk_fma_f32 v[90:91],v[90:91],v[100:101],v[100:101] op_sel:[0,0,1] op_sel_hi:[1,0,1]\nv_pk_fma_f32 v[92:93],v[92:93],v[100:101],v[100:101] op_sel:[0,0,1] op_sel_hi:[1,0,1]\n" \
"v_pk_fma_f32 v[94:95],v[94:95],v[100:101],v[100:101] op_sel:[0,0,1] op_sel_hi:[1,0,1]\nv_cvt_pk_bf16_f32 v20,v88,v89\nv_cvt_pk_bf16_f32 v21,v90,v91\nv_cvt_pk_bf16_f32 v22,v92,v93\n" \
"v_cvt_pk_bf16_f32 v23,v94,v95\nds_write_b128 v48,v[20:23] offset:512\nv_pk_fma_f32 v[96:97],v[96:97],v[100:101],v[100:101] op_sel:[0,0,1] op_sel_hi:[1,0,1]\nv_cvt_pk_bf16_f32 v24,v96,v97\n" \
"ds_write_b32 v49,v24 offset:1024\nv_cvt_pk_bf16_f32 v38,v74,v75\nv_pk_add_f32 v[76:77],v[76:77],v[60:61] op_sel_hi:[1,0]\nv_cvt_pk_bf16_f32 v39,v76,v77\n" \
"v_pk_add_f32 v[78:79],v[78:79],v[60:61] op_sel_hi:[1,0]\nv_cvt_pk_bf16_f32 v40,v78,v79\nv_bfe_u32 v50,v0,2,2\nv_mul_u32_u24 v50,4896,v50\n" \
"v_bfe_u32 v51,v0,4,2\nv_lshl_add_u32 v51,v51,2,s3\nv_lshl_add_u32 v50,v51,5,v50\nv_and_b32 v52,3,v0\n" \
"v_lshl_add_u32 v50,v52,3,v50\nv_and_b32 v52,0xf0,v1\nv_lshl_add_u32 v51,v51,16,v52\nv_bfrev_b32 v52,1\n" \
"v_cmp_gt_u32 vcc,256,v1\nv_and_b32 v53,31,v0\nv_mul_u32_u24 v53,12,v53\nv_cndmask_b32 v52,v52,v51,vcc\n" \
"v_sub_u32 v53,v12,v53\ns_lshl_b32 s24,s34,8\ns_add_u32 s24,s24,9764864\ns_waitcnt lgkmcnt(0)\n" \
"s_barrier\nds_read_b64_tr_b16 v[112:113],v50 offset:9216\nds_read_b64_tr_b16 v[114:115],v50 offset:11664\nds_read_b64_tr_b16 v[116:117],v50 offset:9728\n" \
"ds_read_b64_tr_b16 v[118:119],v50 offset:12176\nds_read_b64_tr_b16 v[120:121],v50 offset:10240\nds_read_b64_tr_b16 v[122:123],v50 offset:12688\ns_add_u32 s25,s24,1048576\n" \
"s_add_u32 s26,s24,2097152\ns_add_u32 s13,s13,7405568\ns_load_dwordx2 s[4:5],s[0:1],56\ns_load_dwordx2 s[16:17],s[0:1],64\n" \
"s_waitcnt lgkmcnt(0)\nbuffer_store_dwordx4 v[112:115],v51,s[36:39],s24 offen sc1\nbuffer_store_dwordx4 v[116:119],v51,s[36:39],s25 offen sc1\nbuffer_store_dwordx4 v[120:123],v52,s[36:39],s26 offen sc1\n" \
"buffer_store_dwordx4 v[32:35],v12,s[36:39],s13 offen sc1\nbuffer_store_dwordx4 v[36:39],v12,s[36:39],s13 offen offset:512 sc1\nbuffer_store_dword v40,v53,s[36:39],s13 offen offset:1024 sc1\ns_lshr_b32 s100,s33,3\n" \
"s_lshl1_add_u32 s100,s100,0\ns_sub_u32 s101,s100,7\ns_min_u32 s100,s100,s101\ns_lshl_b32 s101,s100,12\n" \
"s_add_u32 s101,s101,s57\ns_add_u32 m0,s101,0\ns_and_b32 s101,s100,1\ns_lshr_b32 s100,s100,1\n" \
"s_lshl_b32 s100,s100,10\ns_add_u32 s100,s100,s101\ns_lshl_b32 s100,s100,7\ns_add_u32 s100,s100,s92\n" \
"s_add_u32 s100,s100,262144\ns_lshr_b32 s31,s33,3\ns_lshl1_add_u32 s31,s31,1\ns_sub_u32 s96,s31,7\n" \
"s_min_u32 s31,s31,s96\ns_lshl_b32 s96,s31,12\ns_add_u32 s96,s96,s57\ns_lshr_b32 s97,s33,3\n" \
"s_lshl1_add_u32 s97,s97,2\ns_sub_u32 s95,s97,7\ns_min_u32 s97,s97,s95\ns_lshl_b32 s95,s97,12\n" \
"s_add_u32 s95,s95,s57\nv_and_b32 v64,63,v0\ns_and_b32 s25,s33,7\nv_lshrrev_b32 v66,5,v64\n" \
"v_and_b32 v67,1,v0\ns_lshl2_add_u32 s25,s25,s3\ns_and_b32 s26,s33,6\ns_lshr_b32 s27,s3,1\n" \
"v_lshl_add_u32 v66,v66,1,v67\ns_mul_i32 s24,s32,1152\ns_lshl_b32 s25,s25,5\ns_add_u32 s26,s26,s27\n" \
"s_mov_b32 s6,9216\ns_mov_b32 s7,0x20000\nv_lshlrev_b32 v68,3,v66\ns_add_u32 s25,s25,s24\n" \
"v_bfe_u32 v72,v0,1,1\nv_lshrrev_b32 v74,2,v64\ns_lshl4_add_u32 s26,s26,s24\nv_and_b32 v73,12,v0\n" \
"v_lshrrev_b32 v71,4,v64\nv_and_b32 v69,7,v0\nbuffer_load_dwordx2 v[80:81],v68,s[4:7],s25 offen\nv_lshl_add_u32 v72,v72,2,s21\n" \
"v_and_b32 v74,12,v74\nv_add_u32 v71,v71,v73\ns_lshl_b32 s24,s3,4\nv_lshlrev_b32 v70,1,v69\n" \
"v_lshl_add_u32 v72,v72,4,v74\ns_mov_b32 s15,s19\nv_add_u32 v71,s24,v71\nbuffer_load_ushort v82,v70,s[4:7],s26 offen offset:1024\n" \
"s_mov_b32 s18,4096\ns_mov_b32 s19,0x20000\nv_lshlrev_b32 v74,1,v71\ns_lshl_b32 s24,s22,8\n" \
"buffer_load_ushort v83,v74,s[16:19],s24 offen\nv_add_u32 v72,s3,v72\nbuffer_load_ushort v84,v74,s[16:19],s24 offen offset:128\nv_lshlrev_b32_e64 v75,1,v72\n" \
"buffer_load_ushort v85,v75,s[16:19],s24 offen\nv_lshl_add_u32 v73,v67,1,v73\nv_mul_u32_u24 v91,1152,v72\nv_lshl_add_u32 v91,v73,1,v91\n" \
"v_lshlrev_b32_e64 v73,2,v73\nv_add_u32 v73,163712,v73\nds_read_b64 v[86:87],v73\ns_mul_i32 s24,s22,147456\n" \
"s_mul_i32 s25,s20,288\nv_bfe_u32 v58,v0,1,1\ns_add_u32 s24,s24,s25\nv_mul_u32_u24 v58,544,v58\n" \
"v_bfe_u32 v75,v0,2,1\ns_lshl_b32 s25,s21,6\nv_lshl_add_u32 v58,v75,8,v58\ns_add_u32 s25,s25,s24\n" \
"v_bfe_u32 v75,v0,3,2\ns_add_u32 s24,s24,256\nv_lshl_add_u32 v58,v75,6,v58\nv_add_u32 v91,s24,v91\n" \
"v_mul_u32_u24 v90,1152,v71\nv_and_b32 v74,3,v0\nv_lshl_add_u32 v58,v66,4,v58\ns_mul_i32 s24,s3,1056\n" \
"v_lshl_add_u32 v90,v74,3,v90\nv_add_u32 v58,s24,v58\nv_add_u32 v90,s25,v90\ns_and_b32 s24,s3,1\n" \
"v_mul_u32_u24 v60,288,v75\ns_lshl_b32 s24,s24,2\ns_lshr_b32 s25,s3,1\nv_lshrrev_b32 v75,5,v64\n" \
"s_add_u32 s24,s24,s25\nv_lshl_add_u32 v60,v75,6,v60\ns_lshl_b32 s24,s24,5\nv_lshl_add_u32 v60,v69,2,v60\n" \
"s_add_u32 s24,s24,4224\nv_add_u32 v60,s24,v60\ns_bfe_u32 s24,s33,0x10001\ns_add_u32 s24,s24,s15\n" \
"s_add_u32 s24,s24,17\ns_lshl2_add_u32 s24,s24,s3\ns_waitcnt lgkmcnt(0)\nv_lshrrev_b32 v86,16,v86\n" \
"s_lshl_b32 s24,s24,7\nv_mov_b32 v92,v4\nv_mov_b32 v93,v5\nv_add_u32 v74,s57,v1\n" \
"v_or_b32 v95,v86,v87\nv_lshl_add_u32 v62,v64,1,s24\nv_bfrev_b32 v76,1\nv_cmp_eq_u32 vcc,0,v64\n" \
"s_lshl2_add_u32 s24,s34,s3\ns_lshl2_add_u32 s24,s24,65536\nv_cndmask_b32 v76,v76,0,vcc\nv_mov_b32 v77,1\n" \
"s_barrier\nv_mov_b32 v57,0xc0135761\nv_add_u32 v59,0x80000,v1\nv_lshl_add_u32 v61,v64,3,s23\n" \
"v_mov_b32 v63,0\nv_bfe_u32 v100,v0,4,2\nv_bfe_u32 v101,v0,2,2\nv_lshl_add_u32 v100,v100,3,v101\n" \
"v_bfe_u32 v101,v0,3,2\nv_lshl_add_u32 v100,v100,2,v101\nv_and_b32 v101,3,v0\nv_lshl_add_u32 v100,v100,2,v101\n" \
"v_lshlrev_b32 v100,3,v100\nv_xor_b32 v8,0,v100\nv_add_u32 v8,s57,v8\nv_mov_b32 v88,v8\n" \
"v_xor_b32 v9,32,v100\nv_add_u32 v9,s57,v9\nv_xor_b32 v10,64,v100\nv_add_u32 v10,s57,v10\n" \
"v_xor_b32 v11,96,v100\nv_add_u32 v11,s57,v11\ns_waitcnt vmcnt(5)\nbuffer_store_dword v77,v76,s[36:39],s24 offen sc1\n" \
"ds_read_b64_tr_b16 v[128:129],v8\nds_read_b64_tr_b16 v[130:131],v8 offset:512\nds_read_b64_tr_b16 v[132:133],v9\nds_read_b64_tr_b16 v[134:135],v9 offset:512\n" \
"ds_read_b64_tr_b16 v[136:137],v10\nds_read_b64_tr_b16 v[138:139],v10 offset:512\nds_read_b64_tr_b16 v[140:141],v11\nds_read_b64_tr_b16 v[142:143],v11 offset:512\n" \
"ds_read_b64_tr_b16 v[144:145],v8 offset:4096\nds_read_b64_tr_b16 v[146:147],v8 offset:4608\nds_read_b64_tr_b16 v[148:149],v9 offset:4096\nds_read_b64_tr_b16 v[150:151],v9 offset:4608\n" \
"ds_read_b64_tr_b16 v[152:153],v10 offset:4096\nds_read_b64_tr_b16 v[154:155],v10 offset:4608\nds_read_b64_tr_b16 v[156:157],v11 offset:4096\nds_read_b64_tr_b16 v[158:159],v11 offset:4608\n" \
"ds_read_b64_tr_b16 v[160:161],v8 offset:8192\nds_read_b64_tr_b16 v[162:163],v8 offset:8704\ns_waitcnt vmcnt(5)\nv_and_b32 v53,0xffff0000,v80\n" \
"v_and_b32 v55,0xffff0000,v81\nv_lshlrev_b32 v52,16,v80\nv_lshlrev_b32 v54,16,v81\nds_read_b64_tr_b16 v[164:165],v9 offset:8192\n" \
"ds_read_b64_tr_b16 v[166:167],v9 offset:8704\nds_read_b64_tr_b16 v[168:169],v10 offset:8192\nds_read_b64_tr_b16 v[170:171],v10 offset:8704\nds_read_b64_tr_b16 v[172:173],v11 offset:8192\n" \
"s_waitcnt vmcnt(4)\nv_lshlrev_b32 v56,16,v82\nds_read_b64_tr_b16 v[174:175],v11 offset:8704\nds_read_b64_tr_b16 v[176:177],v8 offset:12288\n" \
"ds_read_b64_tr_b16 v[178:179],v8 offset:12800\nds_read_b64_tr_b16 v[180:181],v9 offset:12288\nds_read_b64_tr_b16 v[182:183],v9 offset:12800\ns_waitcnt vmcnt(2)\n" \
"v_lshl_or_b32 v89,v84,16,v83\nds_write_b128 v74,v[88:91] offset:28672\ns_waitcnt vmcnt(1)\nv_lshlrev_b32_e64 v94,16,v85\n" \
"ds_write_b128 v74,v[92:95] offset:29696\nds_read_b64_tr_b16 v[184:185],v10 offset:12288\nds_read_b64_tr_b16 v[186:187],v10 offset:12800\nds_read_b64_tr_b16 v[188:189],v11 offset:12288\n" \
"ds_read_b64_tr_b16 v[190:191],v11 offset:12800\ns_waitcnt lgkmcnt(0)\nbuffer_load_dwordx4 v4,s[40:43],s100 offen lds\nbuffer_load_dwordx4 v5,s[40:43],s100 offen offset:1024 lds\n" \
"buffer_load_dwordx4 v6,s[40:43],s100 offen offset:2048 lds\nbuffer_load_dwordx4 v7,s[40:43],s100 offen offset:3072 lds\ns_add_u32 m0,s96,0\ns_and_b32 s96,s31,1\n" \
"s_lshr_b32 s31,s31,1\ns_lshl_b32 s31,s31,10\ns_add_u32 s31,s31,s96\ns_lshl_b32 s31,s31,7\n" \
"s_add_u32 s31,s31,s92\ns_add_u32 s31,s31,262144\nbuffer_load_dwordx4 v4,s[40:43],s31 offen lds\nbuffer_load_dwordx4 v5,s[40:43],s31 offen offset:1024 lds\n" \
"buffer_load_dwordx4 v6,s[40:43],s31 offen offset:2048 lds\nbuffer_load_dwordx4 v7,s[40:43],s31 offen offset:3072 lds\ns_add_u32 m0,s95,0\ns_and_b32 s95,s97,1\n" \
"s_lshr_b32 s97,s97,1\ns_lshl_b32 s97,s97,10\ns_add_u32 s97,s97,s95\ns_lshl_b32 s97,s97,7\n" \
"s_add_u32 s97,s97,s92\ns_add_u32 s97,s97,262144\nbuffer_load_dwordx4 v4,s[40:43],s97 offen lds\nbuffer_load_dwordx4 v5,s[40:43],s97 offen offset:1024 lds\n" \
"buffer_load_dwordx4 v6,s[40:43],s97 offen offset:2048 lds\nbuffer_load_dwordx4 v7,s[40:43],s97 offen offset:3072 lds\ns_lshr_b32 s100,s33,3\ns_lshl1_add_u32 s100,s100,3\n" \
"s_sub_u32 s101,s100,7\ns_min_u32 s100,s100,s101\ns_lshl_b32 s101,s100,12\ns_add_u32 s101,s101,s57\n" \
"s_add_u32 m0,s101,0\ns_and_b32 s101,s100,1\ns_lshr_b32 s100,s100,1\ns_lshl_b32 s100,s100,10\n" \
"s_add_u32 s100,s100,s101\ns_lshl_b32 s100,s100,7\ns_add_u32 s100,s100,s92\ns_add_u32 s100,s100,262144\n" \
"buffer_load_dwordx4 v4,s[40:43],s100 offen lds\nbuffer_load_dwordx4 v5,s[40:43],s100 offen offset:1024 lds\nbuffer_load_dwordx4 v6,s[40:43],s100 offen offset:2048 lds\nbuffer_load_dwordx4 v7,s[40:43],s100 offen offset:3072 lds\n" \
"s_lshr_b32 s31,s33,3\ns_lshl1_add_u32 s31,s31,4\ns_sub_u32 s96,s31,7\ns_min_u32 s31,s31,s96\n" \
"s_lshl_b32 s96,s31,12\ns_add_u32 s96,s96,s57\ns_add_u32 m0,s96,0\ns_and_b32 s96,s31,1\n" \
"s_lshr_b32 s31,s31,1\ns_lshl_b32 s31,s31,10\ns_add_u32 s31,s31,s96\ns_lshl_b32 s31,s31,7\n" \
"s_add_u32 s31,s31,s92\ns_add_u32 s31,s31,262144\nbuffer_load_dwordx4 v4,s[40:43],s31 offen lds\nbuffer_load_dwordx4 v5,s[40:43],s31 offen offset:1024 lds\n" \
"buffer_load_dwordx4 v6,s[40:43],s31 offen offset:2048 lds\nbuffer_load_dwordx4 v7,s[40:43],s31 offen offset:3072 lds\ns_lshr_b32 s97,s33,3\ns_lshl1_add_u32 s97,s97,5\n" \
"s_sub_u32 s95,s97,7\ns_min_u32 s97,s97,s95\ns_lshl_b32 s95,s97,12\ns_add_u32 s95,s95,s57\n" \
"s_add_u32 m0,s95,0\ns_and_b32 s95,s97,1\ns_lshr_b32 s97,s97,1\ns_lshl_b32 s97,s97,10\n" \
"s_add_u32 s97,s97,s95\ns_lshl_b32 s97,s97,7\ns_add_u32 s97,s97,s92\ns_add_u32 s97,s97,262144\n" \
"buffer_load_dwordx4 v4,s[40:43],s97 offen lds\nbuffer_load_dwordx4 v5,s[40:43],s97 offen offset:1024 lds\nbuffer_load_dwordx4 v6,s[40:43],s97 offen offset:2048 lds\nbuffer_load_dwordx4 v7,s[40:43],s97 offen offset:3072 lds\n" \
"s_lshr_b32 s100,s33,3\ns_lshl1_add_u32 s100,s100,6\ns_sub_u32 s101,s100,7\ns_min_u32 s100,s100,s101\n" \
"s_lshl_b32 s101,s100,12\ns_add_u32 s101,s101,s57\ns_add_u32 m0,s101,0\ns_and_b32 s101,s100,1\n" \
"s_lshr_b32 s100,s100,1\ns_lshl_b32 s100,s100,10\ns_add_u32 s100,s100,s101\ns_lshl_b32 s100,s100,7\n" \
"s_add_u32 s100,s100,s92\ns_add_u32 s100,s100,262144\nbuffer_load_dwordx4 v4,s[40:43],s100 offen lds\nbuffer_load_dwordx4 v5,s[40:43],s100 offen offset:1024 lds\n" \
"buffer_load_dwordx4 v6,s[40:43],s100 offen offset:2048 lds\nbuffer_load_dwordx4 v7,s[40:43],s100 offen offset:3072 lds\ns_branch .Lgemm_%=\n.LN_%=:\n" \
"v_bfe_u32 v112,v0,3,2\nv_bfe_u32 v113,v0,5,1\nv_lshl_add_u32 v112,v112,8,v113\ns_lshl_b32 s48,s33,2\n" \
"v_lshrrev_b32 v113,3,v0\ns_add_u32 s48,s48,s3\nv_and_b32 v113,6,v113\nv_and_b32 v114,7,v0\n" \
"s_mul_i32 s48,s48,18432\ns_mul_i32 s49,s32,1152\nv_mul_u32_u24 v112,9216,v112\nv_xor_b32 v113,v113,v114\n" \
"s_add_u32 s48,s48,s49\nv_lshl_add_u32 v4,v113,4,v112\ns_mov_b32 s10,18874368\ns_mov_b32 s11,0x20000\n" \
"s_add_u32 s48,s48,1024\ns_add_u32 m0,s88,20224\ns_add_u32 s50,s48,9437184\nbuffer_load_dwordx4 v4,s[8:11],s48 offen lds\n" \
"s_add_u32 m0,s88,24320\ns_mov_b32 s51,4096\nbuffer_load_dwordx4 v4,s[8:11],s50 offen lds\ns_add_u32 m0,s88,28416\n" \
"s_mov_b32 s52,8192\nbuffer_load_dwordx4 v2,s[4:7],0 offen lds\ns_add_u32 m0,s88,32512\ns_mov_b32 s53,14336\n" \
"buffer_load_dwordx4 v2,s[4:7],s51 offen lds\nbuffer_load_dwordx4 v[234:237],v1,s[4:7],s52 offen\nbuffer_load_dwordx4 v[238:241],v1,s[4:7],s52 offen offset:1024\nbuffer_load_dwordx4 v[242:245],v2,s[4:7],s53 offen\n" \
"s_mov_b32 s52,18432\nbuffer_load_dwordx4 v[252:255],v2,s[4:7],s52 offen\ns_and_b32 s60,s33,7\ns_mul_i32 s92,s3,589824\n" \
"s_mul_i32 s57,s3,30720\ns_add_u32 s92,s92,s49\ns_lshl_b32 s62,s60,7\nv_add_u32 v5,17408,v4\n" \
"v_add_u32 v6,34816,v4\nv_add_u32 v7,52224,v4\ns_add_u32 s57,s57,38272\ns_add_u32 s92,s92,s62\n" \
"s_load_dwordx4 s[44:47],s[0:1],72\nv_and_b32 v120,0x3c0,v1\ns_lshr_b32 s59,s33,3\nv_bfe_u32 v121,v0,3,2\n" \
"v_lshlrev_b32 v120,1,v120\ns_lshl_b32 s56,s34,8\ns_lshl_b32 s59,s59,3\nv_bfe_u32 v113,v0,4,2\n" \
"v_lshl_add_u32 v120,v121,5,v120\nv_and_b32 v122,3,v0\ns_mul_i32 s54,s34,9344\ns_add_u32 s56,s56,10289152\n" \
"s_add_u32 s59,s59,s32\nv_mul_u32_u24 v113,589824,v113\nv_and_b32 v112,15,v0\nv_lshl_add_u32 v120,v122,3,v120\n" \
"s_add_u32 s35,s54,524288\ns_cmp_eq_u32 s3,2\ns_cselect_b32 s56,s56,2147483648\ns_lshl_b32 s59,s59,3\n" \
"v_lshl_add_u32 v113,v112,4,v113\ns_add_u32 s58,s35,8192\nv_bfe_u32 v119,v0,4,2\nv_bfe_u32 v233,v0,3,3\n" \
"v_mul_u32_u24 v233,8192,v233\nv_lshl_add_u32 v233,v1,3,v233\ns_lshl2_add_u32 s100,s33,s3\ns_add_u32 s100,s100,4294967260\n" \
"s_and_b32 s101,s100,31\ns_lshl_b32 s101,s101,3\ns_cmp_lt_u32 s100,256\ns_cselect_b32 s100,s100,0x40000\n" \
"s_lshr_b32 s100,s100,5\ns_add_u32 s100,s100,s101\ns_lshl_b32 s100,s100,6\ns_add_u32 s100,s100,s32\n" \
"s_mul_i32 s100,s100,1152\ns_mov_b32 m0,162176\nv_xor_b32 v8,0,v120\nv_xor_b32 v9,32,v120\n" \
"v_xor_b32 v10,64,v120\nv_xor_b32 v11,96,v120\ns_add_u32 s59,s59,s60\nv_and_b32 v118,15,v0\n" \
"v_and_b32 v116,0x70,v2\nv_add_u32 v113,s56,v113\nv_add_u32 v114,s58,v2\nv_cmp_gt_u32 vcc,68,v0\n" \
"v_lshrrev_b32 v115,3,v0\nv_cvt_f32_u32 v120,s34\ns_lshl_b32 s59,s59,7\ns_lshl_b32 s61,s34,4\n" \
"v_lshrrev_b32 v48,1,v118\nv_and_b32 v49,1,v119\nv_cndmask_b32_e64 v113,v113,v114,vcc\nv_lshl_add_u32 v115,v115,16,v116\n" \
"v_mul_f32 v50,0x3b800000,v120\ns_add_u32 s87,s59,262144\ns_add_u32 s61,s61,65536\nv_cvt_f32_u32 v48,v48\n" \
"v_cvt_f32_u32 v49,v49\nv_bfe_u32 v100,v0,1,2\nv_and_b32 v101,1,v0\nv_add_u32 v115,s87,v115\n" \
"v_mov_b32 v116,s61\nv_cmp_gt_u32 vcc,24,v0\nv_mul_f32 v48,0xbe000000,v48\nv_fma_f32 v49,v49,2.0,v50\n" \
"v_lshl_add_u32 v100,v101,2,v100\nv_cndmask_b32 v115,v116,v115,vcc\nv_mul_f32 v49,v48,v49\nv_mul_f32 v101,0x3a000000,v120\n" \
"v_cvt_f32_u32 v100,v100\nv_bfrev_b32 v116,1\nv_cmp_gt_u32 vcc,25,v0\nv_fmamk_f32 v63,v48,0x40800000,v49\n" \
"v_fmamk_f32 v100,v100,0x3e000000,v101\nv_cndmask_b32 v115,v116,v115,vcc\nv_mov_b32 v16,-1\nv_mov_b32 v17,-1\n" \
"v_mov_b32 v18,-1\nv_mov_b32 v19,-1\nv_lshrrev_b32 v50,1,v119\nv_and_b32 v51,1,v0\n" \
"v_cos_f32 v62,v63\nv_cvt_f32_u32 v101,v119\nbuffer_store_dwordx4 v[16:19],v2,s[36:39],s35 offen sc1\nv_cmp_eq_u32 vcc,v50,v51\n" \
"v_add_f32 v101,v101,v101\nv_mov_b32 v50,0x3e800000\nv_mul_f32 v101,v101,v100\nv_cndmask_b32 v52,0,v50,vcc\n" \
"v_cos_f32 v56,v49\nv_sin_f32 v57,v49\nv_sin_f32 v63,v63\nv_cos_f32 v106,v101\n" \
"v_sin_f32 v107,v101\nv_cmp_gt_u32 vcc,8,v118\ns_add_u32 s55,s35,4096\nv_lshlrev_b32 v51,31,v0\n" \
"v_cndmask_b32 v102,0,0.5,vcc\nbuffer_store_dwordx4 v[16:19],v2,s[36:39],s55 offen sc1\nv_xor_b32 v54,v51,v52\nv_sub_f32_e64 v104,0.5,v102\n" \
"v_pk_mul_f32 v[58:59],v[56:57],v[52:53] op_sel_hi:[1,0]\nv_pk_mul_f32 v[60:61],v[56:57],v[54:55] op_sel_hi:[1,0]\nv_pk_mul_f32 v[64:65],v[62:63],v[52:53] op_sel_hi:[1,0]\nv_pk_mul_f32 v[66:67],v[62:63],v[54:55] op_sel_hi:[1,0]\n" \
"v_pk_mul_f32 v[108:109],v[106:107],v[102:103] op_sel_hi:[1,0]\nv_pk_mul_f32 v[110:111],v[106:107],v[104:105] op_sel_hi:[1,0]\nv_fmamk_f32 v57,v48,0x3f800000,v49\nv_fmamk_f32 v63,v48,0x40a00000,v49\n" \
"v_fmamk_f32 v107,v100,0x3f800000,v101\nv_cos_f32 v56,v57\nv_cos_f32 v62,v63\nv_cos_f32 v106,v107\n" \
"buffer_store_dwordx4 v[16:19],v113,s[36:39],0 offen sc1\nv_sin_f32 v57,v57\nv_sin_f32 v63,v63\nv_sin_f32 v107,v107\n" \
"v_mov_b32 v20,0\nv_mov_b32 v21,0\nv_mov_b32 v22,0\nv_mov_b32 v23,0\n" \
"buffer_store_dwordx4 v[20:23],v115,s[36:39],0 offen sc1\nv_cvt_pk_f16_f32 v40,v58,-v61\nv_cvt_pk_f16_f32 v44,v59,v60\nv_cvt_pk_f16_f32 v41,v64,-v67\n" \
"v_cvt_pk_f16_f32 v45,v65,v66\nv_cvt_pk_f16_f32 v216,v108,-v109\nv_cvt_pk_f16_f32 v220,v109,v108\nv_cvt_pk_f16_f32 v217,v110,-v111\n" \
"v_cvt_pk_f16_f32 v221,-v111,-v110\nv_pk_mul_f32 v[58:59],v[56:57],v[52:53] op_sel_hi:[1,0]\nv_pk_mul_f32 v[60:61],v[56:57],v[54:55] op_sel_hi:[1,0]\nv_pk_mul_f32 v[64:65],v[62:63],v[52:53] op_sel_hi:[1,0]\n" \
"v_pk_mul_f32 v[66:67],v[62:63],v[54:55] op_sel_hi:[1,0]\nv_pk_mul_f32 v[108:109],v[106:107],v[102:103] op_sel_hi:[1,0]\nv_pk_mul_f32 v[110:111],v[106:107],v[104:105] op_sel_hi:[1,0]\nv_lshlrev_b32 v3,4,v119\n" \
"v_lshrrev_b32 v15,1,v1\nv_lshl_add_u32 v3,v118,6,v3\nbuffer_load_dword v233,s[8:11],s100 offen lds\nv_cvt_pk_f16_f32 v42,v58,-v61\n" \
"v_cvt_pk_f16_f32 v46,v59,v60\nv_cvt_pk_f16_f32 v43,v64,-v67\nv_cvt_pk_f16_f32 v47,v65,v66\nv_cvt_pk_f16_f32 v218,v108,-v109\n" \
"v_cvt_pk_f16_f32 v222,v109,v108\nv_cvt_pk_f16_f32 v219,v110,-v111\nv_cvt_pk_f16_f32 v223,-v111,-v110\nv_add_u32 v15,s57,v15\n" \
"v_add_u32 v8,s57,v8\nv_add_u32 v9,s57,v9\nv_add_u32 v10,s57,v10\nv_add_u32 v11,s57,v11\n" \
"v_lshrrev_b32 v77,5,v0\nv_bfe_u32 v113,v0,1,5\nv_and_b32 v115,1,v0\ns_lshl_b32 s63,s3,1\n" \
"v_mul_u32_u24 v77,1160,v77\nv_and_b32 v76,31,v0\nv_and_b32 v114,6,v113\nv_add_u32 v115,s63,v115\n" \
"v_xor_b32 v114,v114,v115\nv_lshlrev_b32 v113,7,v113\nv_lshl_add_u32 v113,v114,4,v113\ns_add_u32 s64,s54,2916352\n" \
"s_add_u32 s65,s64,4096\nv_bfrev_b32 v114,1\ns_lshl1_add_u32 s95,s33,s3\ns_add_u32 s95,s95,74\n" \
"s_cmp_lt_u32 s3,2\ns_cselect_b32 s95,s95,256\ns_and_b32 s100,s95,31\ns_lshl_b32 s100,s100,3\n" \
"s_cmp_lt_u32 s95,256\ns_cselect_b32 s95,s95,0x40000\ns_lshr_b32 s95,s95,5\ns_add_u32 s95,s95,s100\n" \
"s_lshl_b32 s95,s95,6\ns_add_u32 s95,s95,s32\ns_mul_i32 s95,s95,1152\ns_mov_b32 m0,162176\n" \
"s_lshl_b32 s101,s33,7\ns_sub_u32 s101,s101,1280\nv_add_u32 v79,s101,v0\nv_mul_u32_u24 v78,52429,v79\n" \
"v_lshrrev_b32 v78,18,v78\nv_lshlrev_b32 v79,7,v79\ns_mov_b32 s101,6272\nv_mad_u32_u24 v79,v78,s101,v79\n" \
"s_mov_b32 s26,3981312\ns_mov_b32 s27,0x20000\nv_mad_u32_u24 v78,v76,36,v77\nv_cmp_gt_u32 vcc,72,v0\n" \
"s_lshl_b32 s67,s32,2\ns_add_u32 s66,s64,8192\nv_cndmask_b32 v114,v114,v2,vcc\ns_add_u32 s67,s67,s3\n" \
"s_lshl_b32 s67,s67,5\ns_add_u32 s67,s67,s33\ns_lshl_b32 s67,s67,11\ns_add_u32 s67,s67,5308416\n" \
"s_lshl2_add_u32 s69,s3,161664\ns_waitcnt vmcnt(20)\nv_readfirstlane_b32 s28,v126\nv_readfirstlane_b32 s29,v127\n" \
"s_mul_i32 s72,s3,2320\nv_mov_b32 v251,s69\nv_cmp_gt_u32 vcc,32,v0\ns_add_u32 s28,s28,1\n" \
"s_addc_u32 s29,s29,0\ns_lshl_b32 s7,s32,11\nv_cndmask_b32 v74,2.0,0,vcc\ns_mov_b32 s93,128\n" \
"s_mov_b32 s73,8192\ns_mov_b32 s74,12288\ns_mov_b32 s75,17408\ns_mov_b32 s76,21504\n" \
"s_mov_b32 s77,16384\ns_mov_b32 s78,25600\ns_add_u32 s7,s7,40960\ns_mov_b32 s81,0x3d800000\n" \
"s_mov_b32 s42,18874368\ns_mov_b32 s43,0x20000\ns_waitcnt lgkmcnt(0)\ns_mov_b64 s[40:41],s[44:45]\n" \
"s_mov_b64 s[44:45],s[46:47]\ns_mov_b32 s46,2359296\ns_mov_b32 s47,0x20000\ns_waitcnt vmcnt(19)\n" \
"v_lshlrev_b32_e64 v48,16,v224\nv_and_b32 v49,0xffff0000,v224\nv_readlane_b32 s70,v48,0\nv_readlane_b32 s71,v48,32\n" \
"v_and_b32 v51,0xffff0000,v225\nv_mov_b32 v72,s70\nv_mov_b32 v73,s71\nv_lshlrev_b32 v50,16,v225\n" \
"v_and_b32 v53,0xffff0000,v226\nv_permlane32_swap_b32 v72,v73\nv_pk_add_f32 v[48:49],v[48:49],v[72:73] op_sel_hi:[1,0] neg_lo:[0,1] neg_hi:[0,1]\nv_pk_add_f32 v[50:51],v[50:51],v[72:73] op_sel_hi:[1,0] neg_lo:[0,1] neg_hi:[0,1]\n" \
"v_pk_mul_f32 v[68:69],v[48:49],v[48:49]\nv_lshlrev_b32_e64 v52,16,v226\nv_and_b32 v55,0xffff0000,v227\nv_pk_add_f32 v[52:53],v[52:53],v[72:73] op_sel_hi:[1,0] neg_lo:[0,1] neg_hi:[0,1]\n" \
"v_pk_add_f32 v[66:67],v[48:49],v[50:51]\nv_pk_fma_f32 v[68:69],v[50:51],v[50:51],v[68:69]\nv_lshlrev_b32 v54,16,v227\ns_waitcnt vmcnt(18)\n" \
"v_and_b32 v57,0xffff0000,v228\nv_pk_add_f32 v[54:55],v[54:55],v[72:73] op_sel_hi:[1,0] neg_lo:[0,1] neg_hi:[0,1]\nv_pk_add_f32 v[66:67],v[66:67],v[52:53]\nv_pk_fma_f32 v[68:69],v[52:53],v[52:53],v[68:69]\n" \
"v_lshlrev_b32_e64 v56,16,v228\nv_and_b32 v59,0xffff0000,v229\nv_pk_add_f32 v[56:57],v[56:57],v[72:73] op_sel_hi:[1,0] neg_lo:[0,1] neg_hi:[0,1]\nv_pk_add_f32 v[66:67],v[66:67],v[54:55]\n" \
"v_pk_fma_f32 v[68:69],v[54:55],v[54:55],v[68:69]\nv_lshlrev_b32_e64 v58,16,v229\nv_and_b32 v61,0xffff0000,v230\nv_pk_add_f32 v[58:59],v[58:59],v[72:73] op_sel_hi:[1,0] neg_lo:[0,1] neg_hi:[0,1]\n" \
"v_pk_add_f32 v[66:67],v[66:67],v[56:57]\nv_pk_fma_f32 v[68:69],v[56:57],v[56:57],v[68:69]\nv_lshlrev_b32_e64 v60,16,v230\nv_and_b32 v63,0xffff0000,v231\n" \
"v_pk_add_f32 v[60:61],v[60:61],v[72:73] op_sel_hi:[1,0] neg_lo:[0,1] neg_hi:[0,1]\nv_pk_add_f32 v[66:67],v[66:67],v[58:59]\nv_pk_fma_f32 v[68:69],v[58:59],v[58:59],v[68:69]\nv_lshlrev_b32 v62,16,v231\n" \
"s_waitcnt vmcnt(17)\nv_and_b32 v65,0xffff0000,v232\nv_pk_add_f32 v[62:63],v[62:63],v[72:73] op_sel_hi:[1,0] neg_lo:[0,1] neg_hi:[0,1]\nv_pk_add_f32 v[66:67],v[66:67],v[60:61]\n" \
"v_pk_fma_f32 v[68:69],v[60:61],v[60:61],v[68:69]\nv_lshlrev_b32_e64 v64,16,v232\nv_pk_add_f32 v[64:65],v[64:65],v[72:73] op_sel_hi:[1,0] neg_lo:[0,1] neg_hi:[0,1]\nv_pk_add_f32 v[66:67],v[66:67],v[62:63]\n" \
"v_pk_fma_f32 v[68:69],v[62:63],v[62:63],v[68:69]\nv_pk_add_f32 v[66:67],v[66:67],v[64:65]\nv_pk_fma_f32 v[68:69],v[64:65],v[64:65],v[68:69]\nv_add_f32 v66,v66,v67\n" \
"v_add_f32 v68,v68,v69\ns_nop 1\nv_permlane16_swap_b32 v66,v68\nv_add_f32 v66,v66,v68\n" \
"s_nop 1\nv_add_f32_dpp v66,v66,v66 quad_perm:[1,0,3,2] row_mask:0xf bank_mask:0xf bound_ctrl:1\ns_nop 1\nv_add_f32_dpp v66,v66,v66 quad_perm:[2,3,0,1] row_mask:0xf bank_mask:0xf bound_ctrl:1\n" \
"s_nop 1\nv_add_f32_dpp v66,v66,v66 row_half_mirror row_mask:0xf bank_mask:0xf bound_ctrl:1\ns_nop 1\nv_add_f32_dpp v66,v66,v66 row_mirror row_mask:0xf bank_mask:0xf bound_ctrl:1\n" \
"v_mul_f32 v70,0x3ae38e39,v66\nv_mul_f32 v68,0x3ae38e39,v66\ns_nop 1\nv_permlane16_swap_b32 v70,v68\n" \
"v_fma_f32 v68,-v70,v70,v68\nv_max_f32 v68,0,v68\nv_add_f32 v68,0x3727c5ac,v68\nv_rsq_f32 v66,v68\n" \
"v_add_f32 v250,v70,v72\nv_mul_f32 v67,-v70,v66\nv_pk_fma_f32 v[48:49],v[48:49],v[66:67],v[66:67] op_sel:[0,0,1] op_sel_hi:[1,0,1]\nv_pk_fma_f32 v[50:51],v[50:51],v[66:67],v[66:67] op_sel:[0,0,1] op_sel_hi:[1,0,1]\n" \
"v_cvt_pk_f16_f32 v80,v48,v49\nv_cvt_pk_f16_f32 v81,v50,v51\nv_pk_fma_f32 v[52:53],v[52:53],v[66:67],v[66:67] op_sel:[0,0,1] op_sel_hi:[1,0,1]\nv_pk_fma_f32 v[54:55],v[54:55],v[66:67],v[66:67] op_sel:[0,0,1] op_sel_hi:[1,0,1]\n" \
"ds_write2_b32 v78,v80,v81 offset0:0 offset1:1\nv_cvt_pk_f16_f32 v82,v52,v53\nv_cvt_pk_f16_f32 v83,v54,v55\nv_pk_fma_f32 v[56:57],v[56:57],v[66:67],v[66:67] op_sel:[0,0,1] op_sel_hi:[1,0,1]\n" \
"v_pk_fma_f32 v[58:59],v[58:59],v[66:67],v[66:67] op_sel:[0,0,1] op_sel_hi:[1,0,1]\nds_write2_b32 v78,v82,v83 offset0:2 offset1:3\nv_cvt_pk_f16_f32 v84,v56,v57\nv_cvt_pk_f16_f32 v85,v58,v59\n" \
"v_pk_fma_f32 v[60:61],v[60:61],v[66:67],v[66:67] op_sel:[0,0,1] op_sel_hi:[1,0,1]\nv_pk_fma_f32 v[62:63],v[62:63],v[66:67],v[66:67] op_sel:[0,0,1] op_sel_hi:[1,0,1]\nds_write2_b32 v78,v84,v85 offset0:4 offset1:5\nv_cvt_pk_f16_f32 v86,v60,v61\n" \
"v_cvt_pk_f16_f32 v87,v62,v63\nv_pk_fma_f32 v[64:65],v[64:65],v[66:67],v[66:67] op_sel:[0,0,1] op_sel_hi:[1,0,1]\nds_write2_b32 v78,v86,v87 offset0:6 offset1:7\nv_cvt_pk_f16_f32 v88,v64,v65\n" \
"ds_write_b32 v78,v88 offset:32\ns_waitcnt vmcnt(11)\ns_barrier\nds_read_u16 v16,v246\n" \
"ds_read_u16 v17,v246 offset:5216\nds_read_u16 v18,v246 offset:578\nds_read_u16 v19,v246 offset:5794\nds_read_u16 v20,v246 offset:1156\n" \
"ds_read_u16 v21,v246 offset:6372\nds_read_u16 v22,v246 offset:1734\nds_read_u16 v23,v246 offset:6950\ns_waitcnt vmcnt(8)\n" \
"v_lshrrev_b32 v96,1,v234\nds_read_u16 v24,v247\nv_lshrrev_b32 v97,1,v235\nv_lshrrev_b32 v98,1,v236\n" \
"s_waitcnt lgkmcnt(7)\nv_lshl_or_b32 v16,v17,16,v16\nv_lshrrev_b32 v99,1,v237\nds_read_u16 v25,v247 offset:5216\n" \
"s_waitcnt vmcnt(7)\nv_lshrrev_b32 v100,1,v238\nv_lshrrev_b32 v101,1,v239\nv_lshrrev_b32 v102,1,v240\n" \
"v_lshrrev_b32 v103,1,v241\nds_read_u16 v26,v247 offset:578\nv_add_u32 v96,s72,v96\nv_add_u32 v97,s72,v97\n" \
"s_waitcnt lgkmcnt(7)\nv_lshl_or_b32 v17,v19,16,v18\nv_add_u32 v98,s72,v98\nds_read_u16 v27,v247 offset:5794\n" \
"v_add_u32 v99,s72,v99\nv_add_u32 v100,s72,v100\nv_add_u32 v101,s72,v101\nv_add_u32 v102,s72,v102\n" \
"ds_read_u16 v28,v247 offset:1156\nv_add_u32 v103,s72,v103\nv_pk_maximum3_f16 v124,v16,v17,v16\ns_waitcnt lgkmcnt(7)\n" \
"v_lshl_or_b32 v18,v21,16,v20\nv_pk_minimum3_f16 v125,v16,v17,v16\nds_read_u16 v29,v247 offset:6372\nds_read_u16 v30,v247 offset:1734\n" \
"s_waitcnt lgkmcnt(7)\nv_lshl_or_b32 v19,v23,16,v22\nbuffer_store_dwordx4 v[16:19],v2,s[36:39],s64 offen sc1\nds_read_u16 v31,v247 offset:6950\n" \
"ds_read_u16 v104,v248\nv_pk_maximum3_f16 v124,v18,v19,v124\nv_pk_minimum3_f16 v125,v18,v19,v125\ns_waitcnt lgkmcnt(7)\n" \
"v_lshl_or_b32 v24,v25,16,v24\nds_read_u16 v105,v248 offset:5216\nds_read_u16 v106,v248 offset:578\ns_waitcnt lgkmcnt(7)\n" \
"v_lshl_or_b32 v25,v27,16,v26\nv_pk_maximum3_f16 v124,v24,v25,v124\nds_read_u16 v107,v248 offset:5794\nv_pk_minimum3_f16 v125,v24,v25,v125\n" \
"ds_read_u16 v108,v248 offset:1156\ns_waitcnt lgkmcnt(7)\nv_lshl_or_b32 v26,v29,16,v28\nds_read_u16 v109,v248 offset:6372\n" \
"ds_read_u16 v110,v248 offset:1734\ns_waitcnt lgkmcnt(7)\nv_lshl_or_b32 v27,v31,16,v30\nbuffer_store_dwordx4 v[24:27],v2,s[36:39],s65 offen sc1\n" \
"ds_read_u16 v111,v248 offset:6950\nds_read_b128 v[116:119],v113 offset:20224\nv_pk_maximum3_f16 v124,v26,v27,v124\nv_pk_minimum3_f16 v125,v26,v27,v125\n" \
"ds_read_b128 v[120:123],v113 offset:24320\ns_waitcnt lgkmcnt(8)\nv_lshl_or_b32 v104,v105,16,v104\nds_read_b32 v16,v96\n" \
"ds_read_b32 v24,v96 offset:1160\nds_read_b32 v17,v97\nds_read_b32 v25,v97 offset:1160\ns_waitcnt lgkmcnt(10)\n" \
"v_lshl_or_b32 v105,v107,16,v106\nds_read_b32 v18,v98\nv_pk_maximum3_f16 v124,v104,v105,v124\nds_read_b32 v26,v98 offset:1160\n" \
"v_pk_minimum3_f16 v125,v104,v105,v125\nds_read_b32 v19,v99\nds_read_b32 v27,v99 offset:1160\nds_read_b32 v20,v100\n" \
"s_waitcnt lgkmcnt(13)\nv_lshl_or_b32 v106,v109,16,v108\nds_read_b32 v28,v100 offset:1160\nds_read_b32 v21,v101\n" \
"ds_read_b32 v29,v101 offset:1160\ns_waitcnt lgkmcnt(14)\nv_lshl_or_b32 v107,v111,16,v110\nbuffer_store_dwordx4 v[104:107],v114,s[36:39],s66 offen sc1\n" \
"s_waitcnt lgkmcnt(13)\nbuffer_store_dwordx4 v[116:119],v1,s[36:39],s67 offen sc1\ns_waitcnt lgkmcnt(12)\nbuffer_store_dwordx4 v[120:123],v1,s[36:39],s67 offen offset:1024 sc1\n" \
"ds_read_b32 v22,v102\nds_read_b32 v30,v102 offset:1160\nds_read_b32 v23,v103\nds_read_b32 v31,v103 offset:1160\n" \
"v_pk_maximum3_f16 v124,v106,v107,v124\nbuffer_load_dword v233,s[8:11],s95 offen lds\nv_and_b32 v124,0x7fff7fff,v124\nv_pk_minimum3_f16 v125,v106,v107,v125\n" \
"v_and_b32 v125,0x7fff7fff,v125\nv_pk_max_u16 v124,v124,v125\nv_lshlrev_b32 v125,16,v124\nv_max_u32 v124,v124,v125\n" \
"s_mov_b32 m0,162176\ns_nop 0\nv_max_u32_dpp v124,v124,v124 quad_perm:[1,0,3,2] row_mask:0xf bank_mask:0xf bound_ctrl:1\ns_nop 1\n" \
"v_max_u32_dpp v124,v124,v124 quad_perm:[2,3,0,1] row_mask:0xf bank_mask:0xf bound_ctrl:1\ns_nop 1\nv_max_u32_dpp v124,v124,v124 row_half_mirror row_mask:0xf bank_mask:0xf bound_ctrl:1\ns_nop 1\n" \
"v_max_u32_dpp v124,v124,v124 row_mirror row_mask:0xf bank_mask:0xf bound_ctrl:1\ns_nop 0\nv_readlane_b32 s68,v124,0\nv_readlane_b32 s31,v124,16\n" \
"v_readlane_b32 s96,v124,32\nv_readlane_b32 s97,v124,48\ns_max_u32 s31,s31,s96\ns_max_u32 s68,s68,s97\n" \
"s_max_u32 s68,s68,s31\ns_mul_i32 s31,s32,576\ns_add_u32 s31,s31,2304\ns_andn2_b32 s31,s31,127\n" \
"s_cmp_gt_u32 s3,1\ns_cselect_b32 s31,s31,2147483648\nbuffer_load_dword v79,s[24:27],s31 offen lds\nv_mov_b32 v124,s68\n" \
"ds_write_b32 v251,v124 offset:32\nv_mov_b32 v114,161664\nbuffer_load_dwordx4 v[48:51],v2,s[36:39],s73 offen sc1\nbuffer_load_dwordx4 v[60:63],v2,s[36:39],s75 offen sc1\n" \
"buffer_load_dwordx4 v[52:55],v2,s[36:39],s74 offen sc1\nbuffer_load_dwordx4 v[64:67],v2,s[36:39],s76 offen sc1\nbuffer_load_dwordx4 v[56:59],v1,s[36:39],s77 offen sc1\nbuffer_load_dwordx4 v[68:71],v1,s[36:39],s78 offen sc1\n" \
"s_mov_b32 s79,0xffffff\ns_lshl3_add_u32 s80,s2,4294615040\nv_add_u32 v112,28416,v1\ns_waitcnt vmcnt(8) lgkmcnt(0)\n" \
"s_barrier\nds_read_b128 v[116:119],v114 offset:32\nds_read_b128 v[192:195],v112\nds_read_b128 v[196:199],v112 offset:1024\n" \
"ds_read_b128 v[200:203],v112 offset:2048\nds_read_b128 v[204:207],v112 offset:3072\nds_read_b128 v[208:211],v112 offset:4096\nds_read_b128 v[212:215],v112 offset:5120\n" \
"ds_read_b128 v[32:35],v112 offset:6144\nds_read_b128 v[36:39],v112 offset:7168\ns_waitcnt lgkmcnt(8)\nv_max3_u32 v116,v116,v117,v118\n" \
"v_max_u32 v116,v116,v119\nv_and_b32 v119,0xf8,v0\nv_cmp_ne_u32 vcc,192,v119\nv_lshlrev_b32 v116,1,v116\n" \
"v_mov_b32 v117,s29\nv_lshlrev_b32 v118,11,v0\nv_bfi_b32 v117,s79,v117,v116\nv_add_u32 v118,s80,v118\n" \
"v_bfrev_b32 v119,1\nv_cndmask_b32 v118,v118,v119,vcc\nv_mov_b32 v116,s28\nbuffer_store_dwordx2 v[116:117],v118,s[36:39],0 offen sc1\n" \
"s_waitcnt vmcnt(6)\nv_cmp_ne_u64 vcc,s[28:29],v[48:49]\ns_waitcnt vmcnt(4)\nv_cmp_ne_u64 s[84:85],s[28:29],v[52:53]\n" \
"s_waitcnt vmcnt(2)\nv_cmp_ne_u64 s[82:83],s[28:29],v[56:57]\ns_or_b64 vcc,vcc,s[84:85]\nv_cmp_ne_u64 s[84:85],s[28:29],v[60:61]\n" \
"s_or_b64 vcc,vcc,s[82:83]\nv_cmp_ne_u64 s[82:83],s[28:29],v[64:65]\ns_or_b64 vcc,vcc,s[84:85]\ns_waitcnt vmcnt(1)\n" \
"v_cmp_ne_u64 s[84:85],s[28:29],v[68:69]\ns_or_b64 vcc,vcc,s[82:83]\ns_or_b64 vcc,vcc,s[84:85]\ns_cbranch_vccnz .LsmN_%=\n" \
".LbmN_%=:\nv_pk_add_f32 v[50:51],v[50:51],v[62:63]\nv_pk_add_f32 v[54:55],v[54:55],v[66:67]\nv_pk_mul_f32 v[62:63],v[50:51],2.0 op_sel_hi:[1,0]\n" \
"v_pk_add_f32 v[58:59],v[58:59],v[70:71]\nv_max3_f32 v112,|v62|,|v63|,0\nv_pk_fma_f32 v[66:67],v[54:55],2.0,v[74:75] op_sel_hi:[1,0,0]\nv_max3_f32 v112,|v66|,|v67|,v112\n" \
"v_pk_fma_f32 v[70:71],v[58:59],2.0,2.0 op_sel_hi:[1,0,0]\nv_max3_f32 v112,|v70|,|v71|,v112\nv_cmp_lt_f32 s[82:83],s81,v112\nv_cmp_ngt_f32 vcc,0x41000000,v112\n" \
"s_cmp_lg_u64 s[82:83],0\ns_cselect_b32 s82,2,0\nv_cvt_pk_f16_f32 v62,v62,v63\ns_cmp_lg_u64 vcc,0\n" \
"s_addc_u32 s82,s82,0\nds_write_b32 v252,v62 offset:62848\nv_cvt_pk_f16_f32 v66,v66,v67\nds_write_b32 v253,v66 offset:62848\n" \
"v_cvt_pk_f16_f32 v70,v70,v71\nv_mov_b32 v113,s82\nds_write_b32 v254,v70 offset:62848\nds_write_b32 v251,v113\n" \
"s_waitcnt lgkmcnt(0)\ns_barrier\nds_read_b128 v[124:127],v114\nds_read_b64 v[96:97],v234 offset:62848\n" \
"ds_read_b64 v[98:99],v235 offset:62848\nds_read_b64 v[100:101],v236 offset:62848\nds_read_b64 v[102:103],v237 offset:62848\nds_read_b64 v[104:105],v238 offset:62848\n" \
"ds_read_b64 v[106:107],v239 offset:62848\nds_read_b64 v[108:109],v240 offset:62848\nds_read_b64 v[110:111],v241 offset:62848\ns_waitcnt lgkmcnt(8)\n" \
"v_or3_b32 v124,v124,v125,v126\nv_or_b32 v124,v124,v127\nv_cmp_ne_u32 vcc,2,v124\ns_nop 0\n" \
"s_cbranch_vccnz .LsxN_%=\n.LbxN_%=:\ns_waitcnt lgkmcnt(7)\nv_pk_fma_f16 v80,v16,v96,v97\n" \
"s_waitcnt lgkmcnt(6)\nv_pk_fma_f16 v81,v17,v98,v99\ns_waitcnt lgkmcnt(5)\nv_pk_fma_f16 v82,v18,v100,v101\n" \
"s_waitcnt lgkmcnt(4)\nv_pk_fma_f16 v83,v19,v102,v103\nv_pk_fma_f16 v88,v24,v96,v97\ns_waitcnt lgkmcnt(3)\n" \
"v_pk_fma_f16 v84,v20,v104,v105\ns_waitcnt lgkmcnt(2)\nv_pk_fma_f16 v85,v21,v106,v107\nv_pk_fma_f16 v89,v25,v98,v99\n" \
"s_waitcnt lgkmcnt(1)\nv_pk_fma_f16 v86,v22,v108,v109\nv_pk_fma_f16 v90,v26,v100,v101\ns_waitcnt lgkmcnt(0)\n" \
"v_pk_fma_f16 v87,v23,v110,v111\nv_pk_fma_f16 v91,v27,v102,v103\nv_pk_fma_f16 v92,v28,v104,v105\nv_pk_fma_f16 v93,v29,v106,v107\n" \
"v_pk_fma_f16 v94,v30,v108,v109\nv_bfe_u32 v233,v0,3,3\nv_mul_u32_u24 v233,8192,v233\nv_lshl_add_u32 v233,v1,3,v233\n" \
"s_lshl2_add_u32 s96,s33,s3\ns_add_u32 s96,s96,138\ns_cmp_lt_u32 s33,13\ns_cselect_b32 s96,s96,256\n" \
"s_and_b32 s97,s96,31\ns_lshl_b32 s97,s97,3\ns_cmp_lt_u32 s96,256\ns_cselect_b32 s96,s96,0x40000\n" \
"s_lshr_b32 s96,s96,5\ns_add_u32 s96,s96,s97\ns_lshl_b32 s96,s96,6\ns_add_u32 s96,s96,s32\n" \
"s_mul_i32 s96,s96,1152\ns_mov_b32 m0,162176\nv_pk_fma_f16 v95,v31,v110,v111\nbuffer_load_dword v233,s[8:11],s96 offen lds\n" \
"v_mfma_f32_16x16x32_f16 v[48:51],v[80:83],v[192:195],0\ns_add_u32 s73,s35,s88\ns_add_u32 s74,s73,4096\nv_mfma_f32_16x16x32_f16 v[52:55],v[80:83],v[196:199],0\n" \
"v_and_b32 v112,15,v0\nv_bfrev_b32 v113,1\nv_mfma_f32_16x16x32_f16 v[56:59],v[84:87],v[32:35],0\nv_cmp_eq_u32 vcc,0,v112\n" \
"s_add_u32 s75,s73,8192\nv_mfma_f32_16x16x32_f16 v[60:63],v[84:87],v[36:39],0\nv_cndmask_b32 v113,v113,v3,vcc\nv_cmp_gt_u32 vcc,64,v0\n" \
"v_mfma_f32_16x16x32_f16 v[64:67],v[88:91],v[192:195],0\ns_cmp_lt_u32 s3,2\ns_cselect_b32 s75,s75,2147483648\nv_mfma_f32_16x16x32_f16 v[68:71],v[88:91],v[196:199],0\n" \
"v_pk_add_f32 v[56:57],v[48:49],v[56:57] neg_lo:[0,1] neg_hi:[0,1]\nv_sub_f32 v58,v50,v58\nv_mfma_f32_16x16x32_f16 v[72:75],v[92:95],v[32:35],0\nv_pk_add_f32 v[60:61],v[52:53],v[60:61] neg_lo:[0,1] neg_hi:[0,1]\n" \
"v_sub_f32_e64 v62,v54,v62\nv_mfma_f32_16x16x32_f16 v[76:79],v[92:95],v[36:39],0\nv_pk_fma_f32 v[48:49],v[48:49],2.0,v[56:57] op_sel_hi:[1,0,1] neg_lo:[0,0,1] neg_hi:[0,0,1]\nv_fma_f32 v50,v50,2.0,-v58\n" \
"v_pk_fma_f32 v[52:53],v[52:53],2.0,v[60:61] op_sel_hi:[1,0,1] neg_lo:[0,0,1] neg_hi:[0,0,1]\nv_fma_f32 v54,v54,2.0,-v62\nv_cvt_pk_f16_f32 v16,v48,v52\nv_pk_add_f32 v[72:73],v[64:65],v[72:73] neg_lo:[0,1] neg_hi:[0,1]\n" \
"v_sub_f32 v74,v66,v74\nv_pk_fma_f32 v[64:65],v[64:65],2.0,v[72:73] op_sel_hi:[1,0,1] neg_lo:[0,0,1] neg_hi:[0,0,1]\nv_pk_add_f32 v[76:77],v[68:69],v[76:77] neg_lo:[0,1] neg_hi:[0,1]\nv_sub_f32 v78,v70,v78\n" \
"v_fma_f32 v66,v66,2.0,-v74\nv_pk_fma_f32 v[68:69],v[68:69],2.0,v[76:77] op_sel_hi:[1,0,1] neg_lo:[0,0,1] neg_hi:[0,0,1]\nv_fma_f32 v70,v70,2.0,-v78\nv_cvt_pk_f16_f32 v17,v49,v53\n" \
"v_cvt_pk_f16_f32 v18,v50,v54\nv_cvt_pk_f16_f32 v24,v64,v68\nv_cvt_pk_f16_f32 v25,v65,v69\nv_cvt_pk_f16_f32 v26,v66,v70\n" \
"v_mfma_f32_16x16x32_f16 v[48:51],v[200:203],v[16:19],0\nv_cvt_pk_f16_f32 v20,v56,v60\nv_cvt_pk_f16_f32 v21,v57,v61\nv_mfma_f32_16x16x32_f16 v[52:55],v[204:207],v[16:19],0\n" \
"v_cvt_pk_f16_f32 v22,v58,v62\nv_cvt_pk_f16_f32 v28,v72,v76\nv_mfma_f32_16x16x32_f16 v[64:67],v[200:203],v[24:27],0\nv_cvt_pk_f16_f32 v29,v73,v77\n" \
"v_cvt_pk_f16_f32 v30,v74,v78\nv_mfma_f32_16x16x32_f16 v[68:71],v[204:207],v[24:27],0\nv_cndmask_b32_e64 v113,v113,v3,vcc\nv_mfma_f32_16x16x32_f16 v[56:59],v[200:203],v[20:23],0\n" \
"v_cvt_pk_f16_f32 v80,v48,v52\nv_cvt_pk_f16_f32 v82,v49,v53\nv_mfma_f32_16x16x32_f16 v[60:63],v[204:207],v[20:23],0\nv_cvt_pk_f16_f32 v84,v50,v54\n" \
"v_mfma_f32_16x16x32_f16 v[72:75],v[200:203],v[28:31],0\ns_nop 0\nv_cvt_pk_f16_f32 v81,v64,v68\nv_mfma_f32_16x16x32_f16 v[76:79],v[204:207],v[28:31],0\n" \
"ds_write_b64 v255,v[80:81]\nv_cvt_pk_f16_f32 v83,v65,v69\nds_write_b64 v255,v[82:83] offset:512\nv_cvt_pk_f16_f32 v85,v66,v70\n" \
"ds_write_b64 v255,v[84:85] offset:1024\nv_cvt_pk_f16_f32 v86,v56,v60\nv_cvt_pk_f16_f32 v88,v57,v61\nv_cvt_pk_f16_f32 v90,v58,v62\n" \
"v_cvt_pk_f16_f32 v87,v72,v76\nds_write_b64 v255,v[86:87] offset:256\nv_cvt_pk_f16_f32 v89,v73,v77\nds_write_b64 v255,v[88:89] offset:768\n" \
"v_cvt_pk_f16_f32 v91,v74,v78\nds_write_b64 v255,v[90:91] offset:1280\ns_waitcnt lgkmcnt(0)\ns_barrier\n" \
"ds_read_b128 v[16:19],v243\nds_read_b128 v[20:23],v244\nds_read_b128 v[24:27],v245\ns_waitcnt lgkmcnt(2)\n" \
"v_mfma_f32_16x16x32_f16 v[48:51],v[40:43],v[16:19],0\nv_mfma_f32_16x16x32_f16 v[52:55],v[44:47],v[16:19],0\ns_waitcnt lgkmcnt(1)\nv_mfma_f32_16x16x32_f16 v[56:59],v[40:43],v[20:23],0\n" \
"v_mfma_f32_16x16x32_f16 v[60:63],v[44:47],v[20:23],0\ns_waitcnt lgkmcnt(0)\nv_mfma_f32_16x16x32_f16 v[64:67],v[40:43],v[24:27],0\nv_mfma_f32_16x16x32_f16 v[68:71],v[44:47],v[24:27],0\n" \
"s_nop 1\nv_cvt_pk_f16_f32 v80,v48,v52\nv_and_b32 v80,-2,v80\nv_cvt_pk_f16_f32 v81,v49,v53\n" \
"v_cvt_pk_f16_f32 v82,v50,v54\nv_cvt_pk_f16_f32 v83,v51,v55\nbuffer_store_dwordx4 v[80:83],v3,s[36:39],s73 offen sc1\nv_cvt_pk_f16_f32 v84,v56,v60\n" \
"v_cvt_pk_f16_f32 v85,v57,v61\nv_cvt_pk_f16_f32 v86,v58,v62\nv_cvt_pk_f16_f32 v87,v59,v63\nv_and_b32_e64 v84,-2,v84\n" \
"buffer_store_dwordx4 v[84:87],v3,s[36:39],s74 offen sc1\nv_cvt_pk_f16_f32 v88,v64,v68\nv_cvt_pk_f16_f32 v89,v65,v69\nv_cvt_pk_f16_f32 v90,v66,v70\n" \
"v_cvt_pk_f16_f32 v91,v67,v71\nv_and_b32 v88,-2,v88\nbuffer_store_dwordx4 v[88:91],v113,s[36:39],s75 offen sc1\ns_load_dwordx4 s[20:23],s[0:1],56\n" \
"s_mov_b32 s14,s87\ns_mov_b32 s12,s88\ns_mov_b32 s17,s74\ns_cmp_lt_u32 s3,2\n" \
"s_cselect_b32 s18,s75,s73\ns_mov_b32 s16,s73\ns_mov_b32 s26,4096\ns_mov_b32 s27,0x20000\n" \
"v_and_b32 v112,3,v0\nv_and_b32 v113,12,v0\nv_lshlrev_b32 v112,2,v112\ns_mov_b32 s13,s89\n" \
"v_max_u32 v112,v112,v113\nv_mov_b32 v114,1048576\nv_cmp_gt_u32 vcc,12,v112\nv_and_b32 v63,0xffff0000,v224\n" \
"v_and_b32 v65,0xffff0000,v225\ns_lshl2_add_u32 s95,s33,s3\ns_add_u32 s95,s95,190\ns_cmp_lt_u32 s33,13\n" \
"s_cselect_b32 s95,s95,256\ns_and_b32 s100,s95,31\ns_lshl_b32 s100,s100,3\ns_cmp_lt_u32 s95,256\n" \
"s_cselect_b32 s95,s95,0x40000\ns_lshr_b32 s95,s95,5\ns_add_u32 s95,s95,s100\ns_lshl_b32 s95,s95,6\n" \
"s_add_u32 s95,s95,s32\ns_mul_i32 s95,s95,1152\ns_lshl2_add_u32 s101,s33,s3\ns_add_u32 s101,s101,206\n" \
"s_cmp_lt_u32 s33,13\ns_cselect_b32 s101,s101,256\ns_add_u32 s96,s92,0\ns_add_u32 s97,s92,9437184\n" \
"s_add_u32 s100,s92,9510912\nv_and_b32 v67,0xffff0000,v226\nv_and_b32 v69,0xffff0000,v227\nv_and_b32 v71,0xffff0000,v228\n" \
"v_and_b32 v73,0xffff0000,v229\nv_and_b32 v75,0xffff0000,v230\nv_and_b32 v77,0xffff0000,v231\nv_and_b32 v79,0xffff0000,v232\n" \
"v_lshlrev_b32 v126,3,v1\nv_lshlrev_b32 v62,16,v224\nv_lshlrev_b32 v64,16,v225\nv_lshlrev_b32 v66,16,v226\n" \
"v_lshlrev_b32 v68,16,v227\nv_lshlrev_b32 v70,16,v228\nv_lshlrev_b32 v72,16,v229\nv_lshlrev_b32 v74,16,v230\n" \
"v_lshlrev_b32 v76,16,v231\nv_lshlrev_b32 v78,16,v232\nv_min_u32 v126,0x480,v126\nv_pk_add_f32 v[62:63],v[62:63],v[250:251] op_sel_hi:[1,0] neg_lo:[0,1] neg_hi:[0,1]\n" \
"v_pk_add_f32 v[64:65],v[64:65],v[250:251] op_sel_hi:[1,0] neg_lo:[0,1] neg_hi:[0,1]\nv_pk_add_f32 v[66:67],v[66:67],v[250:251] op_sel_hi:[1,0] neg_lo:[0,1] neg_hi:[0,1]\nv_pk_add_f32 v[68:69],v[68:69],v[250:251] op_sel_hi:[1,0] neg_lo:[0,1] neg_hi:[0,1]\nv_pk_add_f32 v[70:71],v[70:71],v[250:251] op_sel_hi:[1,0] neg_lo:[0,1] neg_hi:[0,1]\n" \
"v_pk_add_f32 v[72:73],v[72:73],v[250:251] op_sel_hi:[1,0] neg_lo:[0,1] neg_hi:[0,1]\nv_pk_add_f32 v[74:75],v[74:75],v[250:251] op_sel_hi:[1,0] neg_lo:[0,1] neg_hi:[0,1]\nv_pk_add_f32 v[76:77],v[76:77],v[250:251] op_sel_hi:[1,0] neg_lo:[0,1] neg_hi:[0,1]\nv_pk_add_f32 v[78:79],v[78:79],v[250:251] op_sel_hi:[1,0] neg_lo:[0,1] neg_hi:[0,1]\n" \
"s_mul_i32 s31,s32,1152\nv_lshlrev_b32 v127,3,v1\ns_waitcnt lgkmcnt(0)\ns_mov_b64 s[24:25],s[22:23]\n" \
"s_mov_b32 s22,9216\ns_mov_b32 s23,0x20000\nv_mov_b64 v[16:17],v[192:193]\nv_mov_b64 v[18:19],v[194:195]\n" \
"v_mov_b64 v[20:21],v[196:197]\nv_mov_b64 v[22:23],v[198:199]\nv_mov_b64 v[24:25],v[208:209]\nv_mov_b64 v[26:27],v[210:211]\n" \
"v_mov_b64 v[28:29],v[212:213]\nv_mov_b64 v[30:31],v[214:215]\nv_mov_b64 v[32:33],v[216:217]\nv_mov_b64 v[34:35],v[218:219]\n" \
"v_mov_b64 v[36:37],v[220:221]\nv_mov_b64 v[38:39],v[222:223]\nv_mov_b64 v[40:41],v[200:201]\nv_mov_b64 v[42:43],v[202:203]\n" \
"v_mov_b64 v[44:45],v[204:205]\nv_mov_b64 v[46:47],v[206:207]\nv_mov_b64 v[56:57],v[242:243]\nv_mov_b64 v[58:59],v[244:245]\n" \
"v_mov_b32 v60,v249\nv_mov_b32 v61,v250\ns_mov_b32 s55,0xbdd2d3e7\ns_mul_i32 s56,s3,9568\n" \
"s_lshl_b32 s58,s3,13\ns_and_b32 s84,s33,1\ns_add_u32 s87,s12,65536\ns_mov_b32 s90,7405568\n" \
"s_sub_u32 s98,s57,s12\ns_mov_b32 s99,s12\ns_mov_b32 m0,162176\ns_nop 0\n" \
"buffer_load_dword v126,s[20:23],s31 offen lds\ns_and_b32 s22,s33,7\ns_lshr_b32 s20,s33,3\ns_lshr_b32 s21,s32,1\n" \
"s_mul_i32 s52,s20,4505600\ns_add_u32 s52,s52,12123648\ns_mul_i32 s48,s20,589824\ns_lshl4_add_u32 s48,s12,s48\n" \
"s_lshl_b32 s86,s21,11\ns_add_u32 s48,s48,9764864\ns_lshl_b32 s67,s21,7\ns_lshl_b32 s85,s21,17\n" \
"s_sub_u32 s86,s14,s86\ns_add_u32 s49,s48,4096\ns_add_u32 s50,s48,8192\ns_add_u32 s51,s48,12288\n" \
"s_add_u32 s53,s52,3276800\ns_and_b32 s31,s101,31\ns_lshl_b32 s31,s31,3\ns_cmp_lt_u32 s101,256\n" \
"s_cselect_b32 s101,s101,0x40000\ns_lshr_b32 s101,s101,5\ns_add_u32 s101,s101,s31\ns_lshl_b32 s101,s101,6\n" \
"s_add_u32 s101,s101,s32\ns_mul_i32 s101,s101,1152\ns_add_u32 s31,s92,9584640\ns_add_u32 s83,s52,2048000\n" \
"s_add_u32 s67,s67,163200\ns_add_u32 s85,s85,65536\ns_add_u32 s86,s86,0x10000\ns_mov_b32 m0,162176\n" \
"s_nop 0\nbuffer_load_dword v127,s[24:27],0 offen lds\ns_and_b32 s24,s32,1\ns_lshl3_add_u32 s22,s24,s22\n" \
"s_bfe_u32 s24,s33,0x10002\ns_lshl1_add_u32 s24,s32,s24\ns_mul_i32 s19,s24,18\ns_and_b32 s24,s33,3\n" \
"s_lshl2_add_u32 s23,s24,s19\ns_lshl_b32 s24,s3,5\ns_add_u32 s54,s14,s24\ns_lshl2_add_u32 s24,s20,s21\n" \
"s_add_u32 s23,s23,s3\ns_lshl2_add_u32 s24,s24,s3\ns_add_u32 s23,s23,1\ns_lshl_b32 s24,s24,9\n" \
"s_lshl_b32 s23,s23,9\ns_add_u32 s24,s24,262144\nv_bfe_u32 v127,v0,3,3\nv_mul_u32_u24 v127,8192,v127\n" \
"v_lshl_add_u32 v127,v1,3,v127\ns_mov_b32 m0,162176\ns_sub_u32 s59,s24,s23\nbuffer_load_dword v127,s[8:11],s95 offen lds\n" \
"s_lshr_b32 s25,s3,1\ns_mul_i32 s24,s21,1152\ns_mul_i32 s25,s25,576\ns_add_u32 s24,s24,s25\n" \
"s_lshl4_add_u32 s24,s24,s22\ns_and_b32 s25,s3,1\ns_lshl_b32 s24,s24,8\ns_lshl_b32 s65,s25,17\n" \
"s_add_u32 s65,s65,s24\ns_lshl_b32 s25,s25,20\ns_add_u32 s24,s24,s25\ns_add_u32 s60,s24,655488\n" \
"s_add_u32 s61,s24,786432\ns_add_u32 s62,s24,786560\ns_add_u32 s63,s24,917504\ns_add_u32 s64,s24,917632\n" \
"s_mov_b32 m0,162176\ns_add_u32 s95,s92,73728\nbuffer_load_dword v127,s[8:11],s101 offen lds\ns_lshl2_add_u32 s24,s21,s3\n" \
"s_mul_i32 s24,s24,9216\ns_add_u32 s24,s24,s52\ns_add_u32 s68,s24,819712\ns_lshl_b32 s24,s22,14\n" \
"s_add_u32 s24,s24,s52\ns_lshl2_add_u32 s25,s21,s3\ns_add_u32 s24,s24,967168\ns_lshl_b32 s25,s25,8\n" \
"s_add_u32 s79,s25,s24\ns_add_u32 s25,s21,4\ns_and_b32 s25,s25,3\ns_add_u32 s24,s24,s12\n" \
"s_lshl_b32 s25,s25,12\ns_add_u32 s25,s25,s24\ns_sub_u32 s74,s25,819200\ns_add_u32 m0,s57,0\n" \
"s_add_u32 s101,s92,147456\nbuffer_load_dwordx4 v4,s[8:11],s96 offen lds\ns_add_u32 s25,s21,3\ns_and_b32 s25,s25,3\n" \
"s_lshl_b32 s25,s25,12\ns_add_u32 s25,s25,s24\ns_sub_u32 s71,s25,2048000\ns_sub_u32 s76,s25,1638400\n" \
"s_add_u32 s25,s21,2\ns_and_b32 s25,s25,3\ns_lshl_b32 s25,s25,12\nbuffer_load_dwordx4 v5,s[8:11],s96 offen offset:1024 lds\n" \
"s_add_u32 s25,s25,s24\ns_sub_u32 s72,s25,1638400\ns_sub_u32 s77,s25,1638400\ns_add_u32 s25,s21,1\n" \
"s_and_b32 s25,s25,3\ns_lshl_b32 s25,s25,12\ns_add_u32 s25,s25,s24\ns_sub_u32 s73,s25,1638400\n" \
"s_sub_u32 s78,s25,1228800\nbuffer_load_dwordx4 v6,s[8:11],s96 offen offset:2048 lds\ns_lshl2_add_u32 s26,s33,s3\ns_and_b32 s24,s26,15\n" \
"s_and_b32 s25,s26,8\ns_lshl2_add_u32 s24,s24,192\ns_mul_i32 s25,s25,28\ns_add_u32 s25,s25,s24\n" \
"s_sub_u32 s25,s25,32\ns_bitcmp1_b32 s26,4\ns_cselect_b32 s24,s25,s24\nbuffer_load_dwordx4 v7,s[8:11],s96 offen offset:3072 lds\n" \
"s_and_b32 s25,s26,31\ns_lshl2_add_u32 s25,s25,448\ns_bitcmp1_b32 s26,5\ns_cselect_b32 s24,s25,s24\n" \
"s_lshr_b32 s25,s26,6\ns_lshl1_add_u32 s25,s21,s25\ns_mul_i32 s25,s25,576\ns_add_u32 s24,s24,s25\n" \
"s_and_b32 s25,s32,1\ns_add_u32 m0,s57,4096\ns_add_u32 s96,s92,221184\nbuffer_load_dwordx4 v4,s[8:11],s97 offen lds\n" \
"s_lshl1_add_u32 s24,s24,s25\ns_lshl_b32 s24,s24,11\ns_bfe_u32 s25,s33,131072\ns_mul_i32 s27,s3,4896\n" \
"s_add_u32 s24,s24,s25\ns_add_u32 s27,s27,9216\ns_add_u32 s89,s24,1\nv_mov_b32 v113,s27\n" \
"s_lshl2_add_u32 s24,s2,s3\nbuffer_load_dwordx4 v5,s[8:11],s97 offen offset:1024 lds\nv_cndmask_b32 v112,v114,v113,vcc\ns_add_u32 s65,s65,2097152\n" \
"s_lshl_b32 s24,s24,8\nv_add_u32 v48,v234,v112\nv_add_u32 v49,v235,v112\nv_add_u32 v50,v236,v112\n" \
"v_add_u32 v51,v237,v112\nv_add_u32 v52,v238,v112\nbuffer_load_dwordx4 v6,s[8:11],s97 offen offset:2048 lds\nv_add_u32 v53,v239,v112\n" \
"v_add_u32 v54,v240,v112\nv_add_u32_e64 v55,v241,v112\ns_add_u32 s75,s59,196608\ns_add_u32 s66,s65,128\n" \
"s_add_u32 s69,s68,3072\ns_add_u32 s70,s68,6144\ns_add_u32 s80,s79,4096\ns_add_u32 s81,s79,8192\n" \
"buffer_load_dwordx4 v7,s[8:11],s97 offen offset:3072 lds\ns_add_u32 s82,s79,12288\ns_add_u32 s88,s74,819200\ns_add_u32 s91,s24,69206016\n" \
"s_add_u32 m0,s57,8192\ns_nop 0\nbuffer_load_dwordx4 v4,s[8:11],s95 offen lds\nbuffer_load_dwordx4 v5,s[8:11],s95 offen offset:1024 lds\n" \
"buffer_load_dwordx4 v6,s[8:11],s95 offen offset:2048 lds\nbuffer_load_dwordx4 v7,s[8:11],s95 offen offset:3072 lds\ns_add_u32 m0,s57,12288\ns_nop 0\n" \
"buffer_load_dwordx4 v4,s[8:11],s100 offen lds\nbuffer_load_dwordx4 v5,s[8:11],s100 offen offset:1024 lds\nbuffer_load_dwordx4 v6,s[8:11],s100 offen offset:2048 lds\nbuffer_load_dwordx4 v7,s[8:11],s100 offen offset:3072 lds\n" \
"s_add_u32 m0,s57,16384\ns_nop 0\nbuffer_load_dwordx4 v4,s[8:11],s101 offen lds\nbuffer_load_dwordx4 v5,s[8:11],s101 offen offset:1024 lds\n" \
"buffer_load_dwordx4 v6,s[8:11],s101 offen offset:2048 lds\nbuffer_load_dwordx4 v7,s[8:11],s101 offen offset:3072 lds\ns_add_u32 m0,s57,20480\ns_nop 0\n" \
"buffer_load_dwordx4 v4,s[8:11],s31 offen lds\nbuffer_load_dwordx4 v5,s[8:11],s31 offen offset:1024 lds\nbuffer_load_dwordx4 v6,s[8:11],s31 offen offset:2048 lds\nbuffer_load_dwordx4 v7,s[8:11],s31 offen offset:3072 lds\n" \
"s_add_u32 m0,s57,24576\ns_nop 0\nbuffer_load_dwordx4 v4,s[8:11],s96 offen lds\nbuffer_load_dwordx4 v5,s[8:11],s96 offen offset:1024 lds\n" \
"buffer_load_dwordx4 v6,s[8:11],s96 offen offset:2048 lds\nbuffer_load_dwordx4 v7,s[8:11],s96 offen offset:3072 lds\nbuffer_load_dwordx4 v[104:107],v1,s[36:39],s7 offen sc1\nbuffer_load_dwordx4 v[108:111],v1,s[36:39],s7 offen offset:1024 sc1\n" \
"s_waitcnt vmcnt(1)\nv_xor_b32 v112,s29,v105\nv_xor_b32 v113,s29,v107\ns_waitcnt vmcnt(0)\n" \
"v_xor_b32 v114,s29,v109\nv_xor_b32 v115,s29,v111\nv_or3_b32 v112,v112,v113,v114\nv_xor_b32 v113,s28,v104\n" \
"v_xor_b32 v114,s28,v106\nv_or_b32 v112,v112,v115\nv_xor_b32 v115,s28,v108\nv_lshl_or_b32 v112,v112,8,v113\n" \
"v_xor_b32 v113,s28,v110\nv_or3_b32 v112,v112,v114,v115\nv_or_b32 v112,v112,v113\nv_cmp_ne_u32 vcc,0,v112\n" \
"s_nop 0\ns_cbranch_vccnz .LsrN_%=\n.LbrN_%=:\nv_min_u32 v120,2288,v2\n" \
"s_mov_b32 s5,26624\nv_max3_u32 v112,v105,v107,v109\nv_max_u32_e64 v112,v112,v111\ns_mov_b32 s6,31232\n" \
"ds_read_b64_tr_b16 a[0:1],v8\nv_max_u32_dpp v112,v112,v112 quad_perm:[1,0,3,2] row_mask:0xf bank_mask:0xf bound_ctrl:1\nds_read_b64_tr_b16 a[4:5],v9\nds_read_b64_tr_b16 a[8:9],v10\n" \
"v_max_u32_dpp v112,v112,v112 quad_perm:[2,3,0,1] row_mask:0xf bank_mask:0xf bound_ctrl:1\nds_read_b64_tr_b16 a[12:13],v11\nds_read_b64_tr_b16 a[16:17],v8 offset:2048\nv_max_u32_dpp v112,v112,v112 row_half_mirror row_mask:0xf bank_mask:0xf bound_ctrl:1\n" \
"ds_read_b64_tr_b16 a[20:21],v9 offset:2048\nds_read_b64_tr_b16 a[24:25],v10 offset:2048\nv_max_u32_dpp v112,v112,v112 row_mirror row_mask:0xf bank_mask:0xf bound_ctrl:1\nds_read_b64_tr_b16 a[28:29],v11 offset:2048\n" \
"v_readlane_b32 s94,v112,0\nv_readlane_b32 s97,v112,16\nv_readlane_b32 s95,v112,32\nv_readlane_b32 s100,v112,48\n" \
"s_max_u32 s97,s97,s95\ns_max_u32 s94,s94,s100\ns_max_u32 s94,s94,s97\ns_lshr_b32 s94,s94,24\n" \
"s_max_u32 s94,s94,64\ns_min_u32 s94,s94,250\ns_add_u32 s101,s92,9658368\ns_add_u32 m0,s57,0\n" \
"ds_read_b64_tr_b16 a[2:3],v8 offset:4096\ns_waitcnt lgkmcnt(1)\nbuffer_load_dwordx4 v4,s[8:11],s101 offen lds\nbuffer_load_dwordx4 v5,s[8:11],s101 offen offset:1024 lds\n" \
"buffer_load_dwordx4 v6,s[8:11],s101 offen offset:2048 lds\nbuffer_load_dwordx4 v7,s[8:11],s101 offen offset:3072 lds\nds_read_b64_tr_b16 a[6:7],v9 offset:4096\nds_read_b64_tr_b16 a[10:11],v10 offset:4096\n" \
"ds_read_b64_tr_b16 a[14:15],v11 offset:4096\nds_read_b64_tr_b16 a[18:19],v8 offset:6144\nds_read_b64_tr_b16 a[22:23],v9 offset:6144\nds_read_b64_tr_b16 a[26:27],v10 offset:6144\n" \
"ds_read_b64_tr_b16 a[30:31],v11 offset:6144\ns_add_u32 s31,s92,294912\ns_add_u32 m0,s57,4096\nds_read_b64_tr_b16 a[32:33],v8 offset:8192\n" \
"s_waitcnt lgkmcnt(1)\nbuffer_load_dwordx4 v4,s[8:11],s31 offen lds\nbuffer_load_dwordx4 v5,s[8:11],s31 offen offset:1024 lds\nbuffer_load_dwordx4 v6,s[8:11],s31 offen offset:2048 lds\n" \
"buffer_load_dwordx4 v7,s[8:11],s31 offen offset:3072 lds\nds_read_b64_tr_b16 a[36:37],v9 offset:8192\nds_read_b64_tr_b16 a[40:41],v10 offset:8192\nds_read_b64_tr_b16 a[44:45],v11 offset:8192\n" \
"ds_read_b64_tr_b16 a[48:49],v8 offset:10240\nds_read_b64_tr_b16 a[52:53],v9 offset:10240\nds_read_b64_tr_b16 a[56:57],v10 offset:10240\nds_read_b64_tr_b16 a[60:61],v11 offset:10240\n" \
"s_add_u32 s96,s92,9732096\ns_add_u32 m0,s57,8192\nds_read_b64_tr_b16 a[34:35],v8 offset:12288\ns_waitcnt lgkmcnt(1)\n" \
"buffer_load_dwordx4 v4,s[8:11],s96 offen lds\nbuffer_load_dwordx4 v5,s[8:11],s96 offen offset:1024 lds\nbuffer_load_dwordx4 v6,s[8:11],s96 offen offset:2048 lds\nbuffer_load_dwordx4 v7,s[8:11],s96 offen offset:3072 lds\n" \
"ds_read_b64_tr_b16 a[38:39],v9 offset:12288\nds_read_b64_tr_b16 a[42:43],v10 offset:12288\nds_read_b64_tr_b16 a[46:47],v11 offset:12288\nds_read_b64_tr_b16 a[50:51],v8 offset:14336\n" \
"ds_read_b64_tr_b16 a[54:55],v9 offset:14336\nds_read_b64_tr_b16 a[58:59],v10 offset:14336\nds_read_b64_tr_b16 a[62:63],v11 offset:14336\ns_add_u32 s97,s92,368640\n" \
"s_add_u32 m0,s57,12288\nds_read_b64_tr_b16 a[64:65],v8 offset:16384\ns_waitcnt lgkmcnt(1)\nbuffer_load_dwordx4 v4,s[8:11],s97 offen lds\n" \
"buffer_load_dwordx4 v5,s[8:11],s97 offen offset:1024 lds\nbuffer_load_dwordx4 v6,s[8:11],s97 offen offset:2048 lds\nbuffer_load_dwordx4 v7,s[8:11],s97 offen offset:3072 lds\nbuffer_load_dwordx4 v[80:83],v120,s[36:39],s5 offen sc1\n" \
"buffer_load_dwordx4 v[84:87],v120,s[36:39],s5 offen offset:2304 sc1\nbuffer_load_dwordx4 v[88:91],v120,s[36:39],s6 offen sc1\nbuffer_load_dwordx4 v[92:95],v120,s[36:39],s6 offen offset:2304 sc1\nds_read_b64_tr_b16 a[68:69],v9 offset:16384\n" \
"ds_read_b64_tr_b16 a[72:73],v10 offset:16384\nds_read_b64_tr_b16 a[76:77],v11 offset:16384\nds_read_b64_tr_b16 a[80:81],v8 offset:18432\nds_read_b64_tr_b16 a[84:85],v9 offset:18432\n" \
"ds_read_b64_tr_b16 a[88:89],v10 offset:18432\nds_read_b64_tr_b16 a[92:93],v11 offset:18432\ns_add_u32 s95,s92,9805824\ns_add_u32 m0,s57,16384\n" \
"ds_read_b64_tr_b16 a[66:67],v8 offset:20480\ns_waitcnt lgkmcnt(1)\nbuffer_load_dwordx4 v4,s[8:11],s95 offen lds\nbuffer_load_dwordx4 v5,s[8:11],s95 offen offset:1024 lds\n" \
"buffer_load_dwordx4 v6,s[8:11],s95 offen offset:2048 lds\nbuffer_load_dwordx4 v7,s[8:11],s95 offen offset:3072 lds\nds_read_b64_tr_b16 a[70:71],v9 offset:20480\nds_read_b64_tr_b16 a[74:75],v10 offset:20480\n" \
"ds_read_b64_tr_b16 a[78:79],v11 offset:20480\nds_read_b64_tr_b16 a[82:83],v8 offset:22528\nds_read_b64_tr_b16 a[86:87],v9 offset:22528\nds_read_b64_tr_b16 a[90:91],v10 offset:22528\n" \
"ds_read_b64_tr_b16 a[94:95],v11 offset:22528\ns_add_u32 s100,s92,442368\ns_add_u32 m0,s57,20480\nds_read_b64_tr_b16 a[96:97],v8 offset:24576\n" \
"s_waitcnt lgkmcnt(1)\nbuffer_load_dwordx4 v4,s[8:11],s100 offen lds\nbuffer_load_dwordx4 v5,s[8:11],s100 offen offset:1024 lds\nbuffer_load_dwordx4 v6,s[8:11],s100 offen offset:2048 lds\n" \
"buffer_load_dwordx4 v7,s[8:11],s100 offen offset:3072 lds\nds_read_b64_tr_b16 a[100:101],v9 offset:24576\nds_read_b64_tr_b16 a[104:105],v10 offset:24576\nds_read_b64_tr_b16 a[108:109],v11 offset:24576\n" \
"ds_read_b64_tr_b16 a[112:113],v8 offset:26624\nds_read_b64_tr_b16 a[116:117],v9 offset:26624\nds_read_b64_tr_b16 a[120:121],v10 offset:26624\nds_read_b64_tr_b16 a[124:125],v11 offset:26624\n" \
"s_add_u32 s101,s92,9879552\ns_add_u32 m0,s57,24576\ns_waitcnt vmcnt(24)\nds_read_b64_tr_b16 a[98:99],v8\n" \
"s_waitcnt lgkmcnt(1)\nbuffer_load_dwordx4 v4,s[8:11],s101 offen lds\nbuffer_load_dwordx4 v5,s[8:11],s101 offen offset:1024 lds\nbuffer_load_dwordx4 v6,s[8:11],s101 offen offset:2048 lds\n" \
"buffer_load_dwordx4 v7,s[8:11],s101 offen offset:3072 lds\nds_read_b64_tr_b16 a[102:103],v9\nds_read_b64_tr_b16 a[106:107],v10\nds_read_b64_tr_b16 a[110:111],v11\n" \
"ds_read_b64_tr_b16 a[114:115],v8 offset:2048\nds_read_b64_tr_b16 a[118:119],v9 offset:2048\nds_read_b64_tr_b16 a[122:123],v10 offset:2048\nds_read_b64_tr_b16 a[126:127],v11 offset:2048\n" \
"s_add_u32 s31,s92,516096\ns_add_u32 m0,s57,0\ns_waitcnt vmcnt(24)\nds_read_b64_tr_b16 a[128:129],v8 offset:4096\n" \
"s_waitcnt lgkmcnt(1)\nbuffer_load_dwordx4 v4,s[8:11],s31 offen lds\nbuffer_load_dwordx4 v5,s[8:11],s31 offen offset:1024 lds\nbuffer_load_dwordx4 v6,s[8:11],s31 offen offset:2048 lds\n" \
"buffer_load_dwordx4 v7,s[8:11],s31 offen offset:3072 lds\nds_read_b64_tr_b16 a[132:133],v9 offset:4096\nds_read_b64_tr_b16 a[136:137],v10 offset:4096\nds_read_b64_tr_b16 a[140:141],v11 offset:4096\n" \
"ds_read_b64_tr_b16 a[144:145],v8 offset:6144\nds_read_b64_tr_b16 a[148:149],v9 offset:6144\nds_read_b64_tr_b16 a[152:153],v10 offset:6144\nds_read_b64_tr_b16 a[156:157],v11 offset:6144\n" \
"s_add_u32 s96,s92,9953280\ns_add_u32 m0,s57,4096\ns_waitcnt vmcnt(24)\nds_read_b64_tr_b16 a[130:131],v8 offset:8192\n" \
"s_waitcnt lgkmcnt(1)\nbuffer_load_dwordx4 v4,s[8:11],s96 offen lds\nbuffer_load_dwordx4 v5,s[8:11],s96 offen offset:1024 lds\nbuffer_load_dwordx4 v6,s[8:11],s96 offen offset:2048 lds\n" \
"buffer_load_dwordx4 v7,s[8:11],s96 offen offset:3072 lds\nds_read_b64_tr_b16 a[134:135],v9 offset:8192\nds_read_b64_tr_b16 a[138:139],v10 offset:8192\nds_read_b64_tr_b16 a[142:143],v11 offset:8192\n" \
"ds_read_b64_tr_b16 a[146:147],v8 offset:10240\nds_read_b64_tr_b16 a[150:151],v9 offset:10240\nds_read_b64_tr_b16 a[154:155],v10 offset:10240\nds_read_b64_tr_b16 a[158:159],v11 offset:10240\n" \
"s_bfe_u32 s26,s33,0x20001\ns_lshl2_add_u32 s15,s32,s26\ns_lshl2_add_u32 s15,s15,s3\ns_lshl_b32 s15,s15,14\n" \
"s_add_u32 s15,s15,5308416\ns_add_u32 s97,s15,0\ns_add_u32 m0,s57,8192\ns_waitcnt vmcnt(24)\n" \
"ds_read_b64_tr_b16 a[160:161],v8 offset:12288\ns_waitcnt lgkmcnt(1)\nbuffer_load_dwordx4 v1,s[36:39],s97 offen sc1 lds\nbuffer_load_dwordx4 v1,s[36:39],s97 offen offset:1024 sc1 lds\n" \
"buffer_load_dwordx4 v1,s[36:39],s97 offen offset:2048 sc1 lds\nbuffer_load_dwordx4 v1,s[36:39],s97 offen offset:3072 sc1 lds\nds_read_b64_tr_b16 a[164:165],v9 offset:12288\nds_read_b64_tr_b16 a[168:169],v10 offset:12288\n" \
"ds_read_b64_tr_b16 a[172:173],v11 offset:12288\nds_read_b64_tr_b16 a[176:177],v8 offset:14336\nds_read_b64_tr_b16 a[180:181],v9 offset:14336\nds_read_b64_tr_b16 a[184:185],v10 offset:14336\n" \
"ds_read_b64_tr_b16 a[188:189],v11 offset:14336\ns_add_u32 s95,s15,4096\ns_add_u32 m0,s57,12288\ns_waitcnt vmcnt(20)\n" \
"ds_read_b64_tr_b16 a[162:163],v8 offset:16384\ns_waitcnt lgkmcnt(1)\nbuffer_load_dwordx4 v1,s[36:39],s95 offen sc1 lds\nbuffer_load_dwordx4 v1,s[36:39],s95 offen offset:1024 sc1 lds\n" \
"buffer_load_dwordx4 v1,s[36:39],s95 offen offset:2048 sc1 lds\nbuffer_load_dwordx4 v1,s[36:39],s95 offen offset:3072 sc1 lds\nds_read_b64_tr_b16 a[166:167],v9 offset:16384\nds_read_b64_tr_b16 a[170:171],v10 offset:16384\n" \
"ds_read_b64_tr_b16 a[174:175],v11 offset:16384\nds_read_b64_tr_b16 a[178:179],v8 offset:18432\nds_read_b64_tr_b16 a[182:183],v9 offset:18432\nds_read_b64_tr_b16 a[186:187],v10 offset:18432\n" \
"ds_read_b64_tr_b16 a[190:191],v11 offset:18432\ns_add_u32 s100,s15,8192\ns_add_u32 m0,s57,16384\ns_waitcnt vmcnt(20)\n" \
"ds_read_b64_tr_b16 a[192:193],v8 offset:20480\ns_waitcnt lgkmcnt(1)\nbuffer_load_dwordx4 v1,s[36:39],s100 offen sc1 lds\nbuffer_load_dwordx4 v1,s[36:39],s100 offen offset:1024 sc1 lds\n" \
"buffer_load_dwordx4 v1,s[36:39],s100 offen offset:2048 sc1 lds\nbuffer_load_dwordx4 v1,s[36:39],s100 offen offset:3072 sc1 lds\nds_read_b64_tr_b16 a[196:197],v9 offset:20480\nds_read_b64_tr_b16 a[200:201],v10 offset:20480\n" \
"ds_read_b64_tr_b16 a[204:205],v11 offset:20480\nds_read_b64_tr_b16 a[208:209],v8 offset:22528\nds_read_b64_tr_b16 a[212:213],v9 offset:22528\nds_read_b64_tr_b16 a[216:217],v10 offset:22528\n" \
"ds_read_b64_tr_b16 a[220:221],v11 offset:22528\ns_add_u32 s101,s15,12288\ns_add_u32 m0,s57,20480\ns_waitcnt lgkmcnt(0)\n" \
"buffer_load_dwordx4 v1,s[36:39],s101 offen sc1 lds\nbuffer_load_dwordx4 v1,s[36:39],s101 offen offset:1024 sc1 lds\nbuffer_load_dwordx4 v1,s[36:39],s101 offen offset:2048 sc1 lds\nbuffer_load_dwordx4 v1,s[36:39],s101 offen offset:3072 sc1 lds\n" \
"v_cmp_ne_u64 vcc,s[28:29],v[80:81]\nv_cmp_ne_u64 s[24:25],s[28:29],v[84:85]\nv_cmp_ne_u64 s[26:27],s[28:29],v[88:89]\ns_or_b64 vcc,vcc,s[24:25]\n" \
"v_cmp_ne_u64 s[24:25],s[28:29],v[92:93]\ns_or_b64 vcc,vcc,s[26:27]\ns_or_b64 vcc,vcc,s[24:25]\ns_cbranch_vccnz .LsnN_%=\n" \
".LbnN_%=:\nv_bfe_u32 v112,v0,1,5\nv_mul_u32_u24 v112,72,v112\nv_lshrrev_b32 v113,6,v0\n" \
"v_mul_u32_u24 v114,144,v0\nv_lshl_add_u32 v112,v113,5,v112\nv_subrev_u32 v114,18368,v114\nv_mov_b32 v115,1048576\n" \
"v_cmp_gt_u32 vcc,144,v0\nv_and_b32 v113,1,v0\nv_lshl_add_u32 v112,v113,4,v112\nv_cndmask_b32 v114,v115,v114,vcc\n" \
"v_and_b32 v97,0xffff0000,v82\nv_lshlrev_b32 v96,16,v82\ns_add_u32 s24,s93,s94\nv_cmp_gt_u32 vcc,128,v0\n" \
"v_add_u32 v113,8,v112\nv_mul_f32 v96,0x3ee38e39,v96\nv_mul_f32 v97,0x3ee38e39,v97\ns_sub_u32 s24,s24,253\n" \
"v_cndmask_b32 v112,v114,v112,vcc\nv_and_b32 v99,0xffff0000,v83\nv_ldexp_f32 v96,v96,s24\nv_ldexp_f32 v97,v97,s24\n" \
"v_lshlrev_b32_e64 v98,16,v83\nds_write_b64 v112,v[96:97] offset:28800\nv_add_u32 v115,72,v114\nv_mul_f32 v98,0x3ee38e39,v98\n" \
"v_mul_f32 v99,0x3ee38e39,v99\nv_cndmask_b32_e64 v113,v115,v113,vcc\nv_ldexp_f32 v98,v98,s24\nv_ldexp_f32 v99,v99,s24\n" \
"ds_write_b64 v113,v[98:99] offset:28800\nv_and_b32 v101,0xffff0000,v86\nv_lshlrev_b32 v100,16,v86\nds_write_b64 v112,v[100:101] offset:31104\n" \
"v_and_b32 v103,0xffff0000,v87\nv_lshlrev_b32 v102,16,v87\ns_lshr_b32 s25,s33,3\nds_write_b64 v113,v[102:103] offset:31104\n" \
"s_mul_i32 s25,s25,576\nv_and_b32 v105,0xffff0000,v90\nv_lshlrev_b32 v104,16,v90\ns_sub_u32 s25,163200,s25\n" \
"ds_write_b64 v112,v[104:105] offset:33408\nv_add_u32 v114,s25,v2\nv_subrev_u32 v115,163200,v114\nv_and_b32 v107,0xffff0000,v91\n" \
"v_lshlrev_b32 v106,16,v91\nv_mov_b32 v116,1048576\nv_cmp_gt_u32 vcc,576,v115\nds_write_b64 v113,v[106:107] offset:33408\n" \
"v_and_b32 v109,0xffff0000,v94\nv_cndmask_b32 v114,v116,v114,vcc\nv_and_b32 v111,0xffff0000,v95\nv_lshlrev_b32 v108,16,v94\n" \
"v_lshlrev_b32 v110,16,v95\nds_write_b128 v114,v[108:111]\nbuffer_load_dwordx4 v[112:115],v3,s[36:39],s16 offen sc1\nbuffer_load_dwordx4 v[116:119],v3,s[36:39],s17 offen sc1\n" \
"v_and_b32 v124,48,v0\nv_cmp_gt_u32 vcc,64,v0\nv_bfe_u32 v127,v0,5,1\nv_and_b32 v13,31,v0\n" \
"v_cndmask_b32_e64 v124,v124,v3,vcc\nbuffer_load_dwordx4 v[120:123],v124,s[36:39],s18 offen sc1\nv_mul_u32_u24 v127,2448,v127\nv_mul_u32_u24 v13,72,v13\n" \
"s_mul_i32 s27,s3,4896\nv_add3_u32 v13,v13,v127,s27\nv_mov_b32 v127,0\ns_waitcnt vmcnt(27)\n" \
"ds_read_b64_tr_b16 a[194:195],v8 offset:24576\nds_read_b64_tr_b16 a[198:199],v9 offset:24576\ns_waitcnt lgkmcnt(2)\ns_barrier\n" \
"ds_read_b64_tr_b16 a[202:203],v10 offset:24576\nds_read_b64_tr_b16 a[206:207],v11 offset:24576\nds_read_b64_tr_b16 a[210:211],v8 offset:26624\nds_read_b64_tr_b16 a[214:215],v9 offset:26624\n" \
"ds_read_b64_tr_b16 a[218:219],v10 offset:26624\nds_read_b64_tr_b16 a[222:223],v11 offset:26624\ns_waitcnt vmcnt(23)\nds_read_b64_tr_b16 a[224:225],v8\n" \
"ds_read_b64_tr_b16 a[228:229],v9\nds_read_b64_tr_b16 a[232:233],v10\nds_read_b64_tr_b16 a[236:237],v11\nds_read_b64_tr_b16 a[240:241],v8 offset:2048\n" \
"ds_read_b64_tr_b16 a[244:245],v9 offset:2048\nds_read_b64_tr_b16 a[248:249],v10 offset:2048\nds_read_b64_tr_b16 a[252:253],v11 offset:2048\ns_waitcnt vmcnt(19)\n" \
"ds_read_b64_tr_b16 a[226:227],v8 offset:4096\nds_read_b64_tr_b16 a[230:231],v9 offset:4096\nds_read_b64_tr_b16 a[234:235],v10 offset:4096\nds_read_b64_tr_b16 a[238:239],v11 offset:4096\n" \
"ds_read_b64_tr_b16 a[242:243],v8 offset:6144\nds_read_b64_tr_b16 a[246:247],v9 offset:6144\nds_read_b64_tr_b16 a[250:251],v10 offset:6144\nds_read_b64_tr_b16 a[254:255],v11 offset:6144\n" \
"s_waitcnt vmcnt(15)\nds_read_b64_tr_b16 v[192:193],v15 offset:8192\nds_read_b64_tr_b16 v[196:197],v15 offset:8704\nds_read_b64_tr_b16 v[194:195],v15 offset:9216\n" \
"ds_read_b64_tr_b16 v[198:199],v15 offset:9728\nds_read_b64_tr_b16 v[200:201],v15 offset:10240\nds_read_b64_tr_b16 v[204:205],v15 offset:10752\nds_read_b64_tr_b16 v[202:203],v15 offset:11264\n" \
"ds_read_b64_tr_b16 v[206:207],v15 offset:11776\ns_waitcnt vmcnt(11)\nds_read_b64_tr_b16 v[208:209],v15 offset:12288\nds_read_b64_tr_b16 v[212:213],v15 offset:12800\n" \
"ds_read_b64_tr_b16 v[210:211],v15 offset:13312\nds_read_b64_tr_b16 v[214:215],v15 offset:13824\nds_read_b64_tr_b16 v[216:217],v15 offset:14336\nds_read_b64_tr_b16 v[220:221],v15 offset:14848\n" \
"ds_read_b64_tr_b16 v[218:219],v15 offset:15360\nds_read_b64_tr_b16 v[222:223],v15 offset:15872\ns_waitcnt vmcnt(7)\nds_read_b64_tr_b16 v[224:225],v15 offset:16384\n" \
"ds_read_b64_tr_b16 v[228:229],v15 offset:16896\nds_read_b64_tr_b16 v[226:227],v15 offset:17408\nds_read_b64_tr_b16 v[230:231],v15 offset:17920\nds_read_b64_tr_b16 v[232:233],v15 offset:18432\n" \
"ds_read_b64_tr_b16 v[236:237],v15 offset:18944\nds_read_b64_tr_b16 v[234:235],v15 offset:19456\nds_read_b64_tr_b16 v[238:239],v15 offset:19968\ns_waitcnt vmcnt(3)\n" \
"ds_read_b64_tr_b16 v[240:241],v15 offset:20480\nds_read_b64_tr_b16 v[244:245],v15 offset:20992\nds_read_b64_tr_b16 v[242:243],v15 offset:21504\nds_read_b64_tr_b16 v[246:247],v15 offset:22016\n" \
"ds_read_b64_tr_b16 v[248:249],v15 offset:22528\nds_read_b64_tr_b16 v[252:253],v15 offset:23040\nds_read_b64_tr_b16 v[250:251],v15 offset:23552\nds_read_b64_tr_b16 v[254:255],v15 offset:24064\n" \
"s_waitcnt vmcnt(1)\nv_and_b32 v125,v112,v116\ns_waitcnt vmcnt(0)\nv_bitop3_b32 v125,v125,v120,1 bitop3:0x80\n" \
"v_cmp_eq_u32 vcc,0,v125\ns_nop 0\ns_cbranch_vccnz .LsuN_%=\n.LbuN_%=:\n" \
"v_mfma_f32_16x16x32_f16 v[80:83],v[32:35],v[112:115],0\nv_and_b32 v3,31,v0\nv_mul_u32_u24 v3,72,v3\nv_mfma_f32_16x16x32_f16 v[84:87],v[36:39],v[112:115],0\n" \
"v_add_u32 v13,9216,v13\nv_mov_b32 v104,-1\nv_mfma_f32_16x16x32_f16 v[88:91],v[32:35],v[116:119],0\nv_mov_b32 v105,-1\n" \
"v_mov_b32 v106,-1\nv_mfma_f32_16x16x32_f16 v[92:95],v[36:39],v[116:119],0\nv_mov_b32 v107,-1\ns_add_u32 s27,s2,0\n" \
"v_mfma_f32_16x16x32_f16 v[96:99],v[32:35],v[120:123],0\ns_lshr_b32 s31,s27,4\ns_mul_i32 s96,s31,37\ns_lshr_b32 s96,s96,8\n" \
"s_lshl2_add_u32 s31,s96,s31\ns_mul_i32 s31,s31,409600\ns_and_b32 s96,s27,15\ns_lshl_b32 s96,s96,14\n" \
"s_add_u32 s31,s31,s96\ns_add_u32 s31,s31,13090816\ns_cmp_lt_u32 s27,158\ns_cselect_b32 s31,s31,2147483648\n" \
"buffer_store_dwordx4 v[104:107],v2,s[36:39],s31 offen sc1\nv_mfma_f32_16x16x32_f16 v[100:103],v[36:39],v[120:123],0\nv_cvt_pk_f16_f32 v112,v84,v80\nv_cvt_pk_f16_f32 v113,v85,v81\n" \
"v_cvt_pk_f16_f32 v114,v86,v82\nv_cvt_pk_f16_f32 v115,v87,v83\nds_write_b128 v57,v[112:115]\nv_cvt_pk_f16_f32 v116,v92,v88\n" \
"v_cvt_pk_f16_f32 v117,v93,v89\ns_add_u32 s31,s31,4096\nbuffer_store_dwordx4 v[104:107],v2,s[36:39],s31 offen sc1\nv_cvt_pk_f16_f32 v118,v94,v90\n" \
"v_cvt_pk_f16_f32 v119,v95,v91\nds_write_b128 v58,v[116:119]\nv_cvt_pk_f16_f32 v120,v100,v96\nv_cvt_pk_f16_f32 v121,v101,v97\n" \
"v_cvt_pk_f16_f32 v122,v102,v98\nv_cvt_pk_f16_f32 v123,v103,v99\nds_write_b128 v60,v[120:123]\ns_add_u32 s31,s31,4096\n" \
"buffer_store_dwordx4 v[104:107],v2,s[36:39],s31 offen sc1\nv_add_u32 v60,31104,v3\nv_add_u32 v3,28800,v3\nds_read2_b64 v[32:35],v3 offset0:0 offset1:1\n" \
"ds_read2_b64 v[36:39],v3 offset0:2 offset1:3\ns_add_u32 s31,s31,4096\ns_add_u32 s27,s2,111\ns_lshr_b32 s97,s27,4\n" \
"s_mul_i32 s95,s97,37\ns_lshr_b32 s95,s95,8\ns_lshl2_add_u32 s97,s95,s97\ns_mul_i32 s97,s97,409600\n" \
"s_and_b32 s95,s27,15\ns_lshl_b32 s95,s95,14\nbuffer_store_dwordx4 v[104:107],v2,s[36:39],s31 offen sc1\ns_add_u32 s97,s97,s95\n" \
"s_add_u32 s97,s97,13090816\ns_cmp_lt_u32 s27,158\ns_cselect_b32 s97,s97,2147483648\ns_waitcnt lgkmcnt(2)\n" \
"s_barrier\nds_read_b64 v[112:113],v56\nds_read_b64 v[116:117],v56 offset:512\nds_read_b64 v[120:121],v56 offset:256\n" \
"ds_read_b64 v[124:125],v56 offset:768\nds_read_b64 v[114:115],v56 offset:1024\nds_read_b64 v[122:123],v56 offset:1280\nds_read_b64 v[56:57],v3 offset:64\n" \
"buffer_store_dwordx4 v[104:107],v2,s[36:39],s97 offen sc1\ns_add_u32 s97,s97,4096\nv_bfe_u32 v110,v0,1,2\nv_bfe_u32 v111,v0,4,1\n" \
"v_xor_b32 v110,v110,v111\nv_and_b32 v111,1,v0\nv_lshl_add_u32 v110,v110,1,v111\nv_bfe_u32 v111,v0,3,3\n" \
"v_lshlrev_b32 v111,12,v111\nv_lshl_add_u32 v4,v110,4,v111\nv_xor_b32 v5,64,v4\nv_add_u32 v5,31744,v5\n" \
"v_add_u32 v6,63488,v4\nbuffer_store_dwordx4 v[104:107],v2,s[36:39],s97 offen sc1\ns_waitcnt lgkmcnt(5)\nv_swap_b32 v113,v116\n" \
"s_add_u32 s97,s97,4096\ns_waitcnt lgkmcnt(3)\nv_swap_b32 v121,v124\nv_add_u32 v7,63488,v5\n" \
"s_waitcnt lgkmcnt(2)\nv_mfma_f32_16x16x32_f16 v[80:83],v[112:115],v[40:43],0\nv_mov_b32 v118,v115\ns_waitcnt lgkmcnt(1)\n" \
"v_mov_b32 v126,v123\nv_mfma_f32_16x16x32_f16 v[84:87],v[112:115],v[44:47],0\ns_lshr_b32 s26,s32,1\ns_mul_i32 s92,s26,1152\n" \
"v_mfma_f32_16x16x32_f16 v[88:91],v[120:123],v[40:43],0\nbuffer_store_dwordx4 v[104:107],v2,s[36:39],s97 offen sc1\nv_mfma_f32_16x16x32_f16 v[92:95],v[120:123],v[44:47],0\ns_add_u32 s97,s97,4096\n" \
"s_lshr_b32 s26,s3,1\nv_mfma_f32_16x16x32_f16 v[96:99],v[116:119],v[40:43],0\ns_lshl_b32 s26,s26,6\ns_add_u32 s92,s92,s26\n" \
"v_mfma_f32_16x16x32_f16 v[100:103],v[116:119],v[44:47],0\nv_pk_add_f32 v[88:89],v[80:81],v[88:89] neg_lo:[0,1] neg_hi:[0,1]\nv_pk_add_f32 v[90:91],v[82:83],v[90:91] neg_lo:[0,1] neg_hi:[0,1]\nv_pk_add_f32 v[92:93],v[84:85],v[92:93] neg_lo:[0,1] neg_hi:[0,1]\n" \
"buffer_store_dwordx4 v[104:107],v2,s[36:39],s97 offen sc1\nv_pk_add_f32 v[94:95],v[86:87],v[94:95] neg_lo:[0,1] neg_hi:[0,1]\nv_pk_fma_f32 v[80:81],v[80:81],2.0,v[88:89] op_sel_hi:[1,0,1] neg_lo:[0,0,1] neg_hi:[0,0,1]\nv_pk_fma_f32 v[82:83],v[82:83],2.0,v[90:91] op_sel_hi:[1,0,1] neg_lo:[0,0,1] neg_hi:[0,0,1]\n" \
"v_pk_fma_f32 v[84:85],v[84:85],2.0,v[92:93] op_sel_hi:[1,0,1] neg_lo:[0,0,1] neg_hi:[0,0,1]\nv_pk_fma_f32 v[86:87],v[86:87],2.0,v[94:95] op_sel_hi:[1,0,1] neg_lo:[0,0,1] neg_hi:[0,0,1]\nv_cvt_pk_f16_f32 v112,v80,v84\nv_cvt_pk_f16_f32 v113,v81,v85\n" \
"v_cvt_pk_f16_f32 v114,v82,v86\nv_cvt_pk_f16_f32 v115,v83,v87\nv_mfma_f32_16x16x32_f16 v[104:107],v[124:127],v[40:43],0\nv_cvt_pk_f16_f32 v116,v88,v92\n" \
"s_lshr_b32 s100,s33,3\ns_add_u32 s100,s100,0\ns_sub_u32 s101,s100,4\ns_min_u32 s100,s100,s101\n" \
"s_lshl_b32 s101,s100,12\ns_add_u32 s101,s101,s57\ns_add_u32 m0,s101,0\ns_and_b32 s101,s100,1\n" \
"s_lshr_b32 s100,s100,1\ns_lshl_b32 s100,s100,10\ns_add_u32 s100,s100,s101\ns_lshl_b32 s100,s100,7\n" \
"s_lshr_b32 s31,s33,3\ns_add_u32 s31,s31,1\ns_sub_u32 s96,s31,4\ns_min_u32 s31,s31,s96\n" \
"s_lshl_b32 s96,s31,12\ns_add_u32 s96,s96,s57\ns_lshr_b32 s97,s33,3\ns_add_u32 s97,s97,2\n" \
"s_sub_u32 s95,s97,4\ns_min_u32 s97,s97,s95\ns_lshl_b32 s95,s97,12\ns_add_u32 s95,s95,s57\n" \
"v_mfma_f32_16x16x32_f16 v[80:83],v[16:19],v[112:115],0\nv_cvt_pk_f16_f32 v117,v89,v93\nv_cvt_pk_f16_f32 v118,v90,v94\nv_mfma_f32_16x16x32_f16 v[84:87],v[20:23],v[112:115],0\n" \
"v_cvt_pk_f16_f32 v119,v91,v95\nv_pk_add_f32 v[104:105],v[96:97],v[104:105] neg_lo:[0,1] neg_hi:[0,1]\nv_mfma_f32_16x16x32_f16 v[108:111],v[124:127],v[44:47],0\nv_pk_add_f32 v[106:107],v[98:99],v[106:107] neg_lo:[0,1] neg_hi:[0,1]\n" \
"v_pk_fma_f32 v[96:97],v[96:97],2.0,v[104:105] op_sel_hi:[1,0,1] neg_lo:[0,0,1] neg_hi:[0,0,1]\nv_mfma_f32_16x16x32_f16 v[88:91],v[24:27],v[116:119],0\nv_pk_fma_f32 v[98:99],v[98:99],2.0,v[106:107] op_sel_hi:[1,0,1] neg_lo:[0,0,1] neg_hi:[0,0,1]\nds_read2_b64 v[40:43],v3 offset0:4 offset1:5\n" \
"v_mfma_f32_16x16x32_f16 v[92:95],v[28:31],v[116:119],0\nds_write2_b32 v48,v84,v80 offset0:0 offset1:1\nds_write2_b32 v49,v85,v81 offset0:0 offset1:1\nds_write2_b32 v50,v86,v82 offset0:0 offset1:1\n" \
"v_pk_add_f32 v[108:109],v[100:101],v[108:109] neg_lo:[0,1] neg_hi:[0,1]\nv_pk_add_f32 v[110:111],v[102:103],v[110:111] neg_lo:[0,1] neg_hi:[0,1]\nv_pk_fma_f32 v[100:101],v[100:101],2.0,v[108:109] op_sel_hi:[1,0,1] neg_lo:[0,0,1] neg_hi:[0,0,1]\nv_pk_fma_f32 v[102:103],v[102:103],2.0,v[110:111] op_sel_hi:[1,0,1] neg_lo:[0,0,1] neg_hi:[0,0,1]\n" \
"ds_write2_b32 v51,v87,v83 offset0:0 offset1:1\nv_cvt_pk_f16_f32 v120,v96,v100\nv_cvt_pk_f16_f32 v121,v97,v101\nv_cvt_pk_f16_f32 v122,v98,v102\n" \
"v_cvt_pk_f16_f32 v123,v99,v103\nds_write2_b32 v52,v92,v88 offset0:0 offset1:1\nds_write2_b32 v53,v93,v89 offset0:0 offset1:1\nv_mfma_f32_16x16x32_f16 v[96:99],v[16:19],v[120:123],0\n" \
"ds_write2_b32 v54,v94,v90 offset0:0 offset1:1\nv_mfma_f32_16x16x32_f16 v[100:103],v[20:23],v[120:123],0\nds_write2_b32 v55,v95,v91 offset0:0 offset1:1\nv_cvt_pk_f16_f32 v124,v104,v108\n" \
"v_cvt_pk_f16_f32 v125,v105,v109\nv_cvt_pk_f16_f32 v126,v106,v110\nv_cvt_pk_f16_f32 v127,v107,v111\nv_add_u32 v48,2448,v48\n" \
"v_add_u32 v49,2448,v49\nv_mfma_f32_16x16x32_f16 v[104:107],v[24:27],v[124:127],0\nds_write2_b32 v48,v100,v96 offset0:0 offset1:1\nv_mfma_f32_16x16x32_f16 v[108:111],v[28:31],v[124:127],0\n" \
"ds_write2_b32 v49,v101,v97 offset0:0 offset1:1\nv_add_u32 v50,2448,v50\nds_write2_b32 v50,v102,v98 offset0:0 offset1:1\nv_add_u32 v51,2448,v51\n" \
"ds_write2_b32 v51,v103,v99 offset0:0 offset1:1\nv_add_u32 v52,2448,v52\nv_add_u32 v53,2448,v53\nv_add_u32 v54,2448,v54\n" \
"ds_write2_b32 v52,v108,v104 offset0:0 offset1:1\nds_write2_b32 v53,v109,v105 offset0:0 offset1:1\nds_write2_b32 v54,v110,v106 offset0:0 offset1:1\nv_add_u32 v55,2448,v55\n" \
"ds_write2_b32 v55,v111,v107 offset0:0 offset1:1\nds_read2_b64 v[80:83],v13 offset0:0 offset1:1\nds_read2_b64 v[84:87],v13 offset0:2 offset1:3\ns_lshl_b32 s26,s3,8\n" \
"s_add_u32 s92,s92,s26\ns_lshl_b32 s92,s92,4\ns_and_b32 s26,s32,1\ns_lshl3_add_u32 s92,s26,s92\n" \
"s_and_b32 s26,s33,7\ns_add_u32 s92,s92,s26\nds_read2_b64 v[88:91],v13 offset0:4 offset1:5\ns_lshl_b32 s92,s92,8\n" \
"s_add_u32 s100,s100,s92\nbuffer_load_dwordx4 v4,s[40:43],s100 offen lds\nds_read2_b64 v[44:47],v3 offset0:6 offset1:7\nv_and_b32 v48,31,v0\n" \
"ds_read2_b64 v[92:95],v13 offset0:6 offset1:7\nv_mad_u32_u24 v49,v48,56,0\nv_sub_u32 v48,v13,v49\nv_and_b32 v49,31,v0\n" \
"v_mul_u32_u24 v49,68,v49\nv_sub_u32 v49,v13,v49\nbuffer_load_dwordx4 v5,s[40:43],s100 offen offset:1024 lds\nds_read_b64 v[96:97],v13 offset:64\n" \
"s_waitcnt lgkmcnt(5)\nv_pk_fma_f32 v[62:63],v[80:81],v[32:33],v[62:63]\nv_pk_fma_f32 v[64:65],v[82:83],v[34:35],v[64:65]\nv_pk_mul_f32 v[102:103],v[62:63],v[62:63]\n" \
"s_waitcnt lgkmcnt(4)\nv_pk_fma_f32 v[66:67],v[84:85],v[36:37],v[66:67]\nv_pk_add_f32 v[100:101],v[62:63],v[64:65]\nv_pk_fma_f32 v[102:103],v[64:65],v[64:65],v[102:103]\n" \
"v_pk_fma_f32 v[68:69],v[86:87],v[38:39],v[68:69]\nbuffer_load_dwordx4 v6,s[40:43],s100 offen offset:2048 lds\nv_pk_add_f32 v[100:101],v[100:101],v[66:67]\nv_pk_fma_f32 v[102:103],v[66:67],v[66:67],v[102:103]\n" \
"ds_read2_b64 v[112:115],v60 offset0:0 offset1:1\nds_read2_b64 v[116:119],v60 offset0:2 offset1:3\ns_waitcnt lgkmcnt(5)\nv_pk_fma_f32 v[70:71],v[88:89],v[40:41],v[70:71]\n" \
"v_pk_add_f32 v[100:101],v[100:101],v[68:69]\nds_read2_b64 v[120:123],v60 offset0:4 offset1:5\nv_pk_fma_f32 v[102:103],v[68:69],v[68:69],v[102:103]\nbuffer_load_dwordx4 v7,s[40:43],s100 offen offset:3072 lds\n" \
"ds_read2_b64 v[124:127],v60 offset0:6 offset1:7\nv_pk_fma_f32 v[72:73],v[90:91],v[42:43],v[72:73]\nv_pk_add_f32 v[100:101],v[100:101],v[70:71]\nds_read_b64 v[98:99],v60 offset:64\n" \
"v_add_u32 v60,2304,v60\nv_pk_fma_f32 v[102:103],v[70:71],v[70:71],v[102:103]\nds_read2_b64 v[16:19],v60 offset0:0 offset1:1\ns_waitcnt lgkmcnt(7)\n" \
"v_pk_fma_f32 v[74:75],v[92:93],v[44:45],v[74:75]\ns_add_u32 m0,s96,0\ns_and_b32 s96,s31,1\ns_lshr_b32 s31,s31,1\n" \
"s_lshl_b32 s31,s31,10\ns_add_u32 s31,s31,s96\ns_lshl_b32 s31,s31,7\ns_add_u32 s31,s31,s92\n" \
"buffer_load_dwordx4 v4,s[40:43],s31 offen lds\nv_pk_add_f32 v[100:101],v[100:101],v[72:73]\nv_pk_fma_f32 v[102:103],v[72:73],v[72:73],v[102:103]\nv_pk_fma_f32 v[76:77],v[94:95],v[46:47],v[76:77]\n" \
"v_pk_add_f32 v[100:101],v[100:101],v[74:75]\nv_pk_fma_f32 v[102:103],v[74:75],v[74:75],v[102:103]\nds_read2_b64 v[20:23],v60 offset0:2 offset1:3\ns_waitcnt lgkmcnt(7)\n" \
"v_pk_fma_f32 v[78:79],v[96:97],v[56:57],v[78:79]\nv_pk_add_f32 v[100:101],v[100:101],v[76:77]\nbuffer_load_dwordx4 v5,s[40:43],s31 offen offset:1024 lds\nv_pk_fma_f32 v[102:103],v[76:77],v[76:77],v[102:103]\n" \
"v_pk_add_f32 v[100:101],v[100:101],v[78:79]\nv_pk_fma_f32 v[102:103],v[78:79],v[78:79],v[102:103]\nds_read2_b64 v[24:27],v60 offset0:4 offset1:5\nv_add_f32 v100,v100,v101\n" \
"v_add_f32 v102,v102,v103\nds_read2_b64 v[28:31],v60 offset0:6 offset1:7\nds_read_b64 v[58:59],v60 offset:64\nbuffer_load_dwordx4 v6,s[40:43],s31 offen offset:2048 lds\n" \
"v_permlane16_swap_b32 v100,v102\nv_add_f32 v100,v100,v102\ns_lshr_b32 s100,s33,3\ns_add_u32 s100,s100,3\n" \
"v_add_f32_dpp v100,v100,v100 quad_perm:[1,0,3,2] row_mask:0xf bank_mask:0xf bound_ctrl:1\ns_sub_u32 s101,s100,4\ns_min_u32 s100,s100,s101\nv_add_f32_dpp v100,v100,v100 quad_perm:[2,3,0,1] row_mask:0xf bank_mask:0xf bound_ctrl:1\n" \
"buffer_load_dwordx4 v7,s[40:43],s31 offen offset:3072 lds\ns_add_u32 m0,s95,0\nv_add_f32_dpp v100,v100,v100 row_half_mirror row_mask:0xf bank_mask:0xf bound_ctrl:1\ns_and_b32 s95,s97,1\n" \
"s_lshr_b32 s97,s97,1\nv_add_f32_dpp v100,v100,v100 row_mirror row_mask:0xf bank_mask:0xf bound_ctrl:1\nv_mul_f32 v104,0x3ae38e39,v100\nv_mul_f32 v102,0x3ae38e39,v100\n" \
"s_lshl_b32 s97,s97,10\ns_add_u32 s97,s97,s95\ns_lshl_b32 s97,s97,7\ns_add_u32 s97,s97,s92\n" \
"buffer_load_dwordx4 v4,s[40:43],s97 offen lds\nv_permlane16_swap_b32 v104,v102\nv_fma_f32 v102,-v104,v104,v102\nv_max_f32 v102,0,v102\n" \
"v_add_f32 v102,0x3727c5ac,v102\nv_rsq_f32 v100,v102\ns_lshl_b32 s101,s100,12\nbuffer_load_dwordx4 v5,s[40:43],s97 offen offset:1024 lds\n" \
"v_mul_f32 v101,-v104,v100\nv_pk_fma_f32 v[80:81],v[62:63],v[100:101],v[100:101] op_sel:[0,0,1] op_sel_hi:[1,0,1]\nv_pk_fma_f32 v[82:83],v[64:65],v[100:101],v[100:101] op_sel:[0,0,1] op_sel_hi:[1,0,1]\nv_pk_fma_f32 v[84:85],v[66:67],v[100:101],v[100:101] op_sel:[0,0,1] op_sel_hi:[1,0,1]\n" \
"v_pk_fma_f32 v[86:87],v[68:69],v[100:101],v[100:101] op_sel:[0,0,1] op_sel_hi:[1,0,1]\nv_pk_fma_f32 v[88:89],v[70:71],v[100:101],v[100:101] op_sel:[0,0,1] op_sel_hi:[1,0,1]\nv_pk_fma_f32 v[90:91],v[72:73],v[100:101],v[100:101] op_sel:[0,0,1] op_sel_hi:[1,0,1]\nv_pk_fma_f32 v[92:93],v[74:75],v[100:101],v[100:101] op_sel:[0,0,1] op_sel_hi:[1,0,1]\n" \
"buffer_load_dwordx4 v6,s[40:43],s97 offen offset:2048 lds\nv_pk_fma_f32 v[94:95],v[76:77],v[100:101],v[100:101] op_sel:[0,0,1] op_sel_hi:[1,0,1]\nv_pk_fma_f32 v[96:97],v[78:79],v[100:101],v[100:101] op_sel:[0,0,1] op_sel_hi:[1,0,1]\ns_add_u32 s101,s101,s57\n" \
"s_waitcnt lgkmcnt(9)\nv_readlane_b32 s24,v112,0\nv_mov_b32 v60,v61\ns_mov_b32 s25,s24\n" \
"v_pk_add_f32 v[112:113],v[112:113],s[24:25] neg_lo:[0,1] neg_hi:[0,1]\ns_waitcnt lgkmcnt(4)\nv_pk_fma_f32 v[80:81],v[80:81],v[16:17],v[112:113]\nbuffer_load_dwordx4 v7,s[40:43],s97 offen offset:3072 lds\n" \
"v_pk_mul_f32 v[102:103],v[80:81],v[80:81]\ns_add_u32 m0,s101,0\ns_and_b32 s101,s100,1\ns_lshr_b32 s100,s100,1\n" \
"s_lshl_b32 s100,s100,10\ns_add_u32 s100,s100,s101\ns_lshl_b32 s100,s100,7\ns_add_u32 s100,s100,s92\n" \
"buffer_load_dwordx4 v4,s[40:43],s100 offen lds\nv_pk_add_f32 v[114:115],v[114:115],s[24:25] neg_lo:[0,1] neg_hi:[0,1]\nv_pk_fma_f32 v[82:83],v[82:83],v[18:19],v[114:115]\nv_pk_add_f32 v[100:101],v[80:81],v[82:83]\n" \
"v_pk_fma_f32 v[102:103],v[82:83],v[82:83],v[102:103]\nv_pk_add_f32 v[116:117],v[116:117],s[24:25] neg_lo:[0,1] neg_hi:[0,1]\ns_waitcnt lgkmcnt(3)\nv_pk_fma_f32 v[84:85],v[84:85],v[20:21],v[116:117]\n" \
"v_pk_add_f32 v[100:101],v[100:101],v[84:85]\nbuffer_load_dwordx4 v5,s[40:43],s100 offen offset:1024 lds\nv_pk_fma_f32 v[102:103],v[84:85],v[84:85],v[102:103]\nv_pk_add_f32 v[118:119],v[118:119],s[24:25] neg_lo:[0,1] neg_hi:[0,1]\n" \
"v_pk_fma_f32 v[86:87],v[86:87],v[22:23],v[118:119]\nv_pk_add_f32 v[100:101],v[100:101],v[86:87]\nv_pk_fma_f32 v[102:103],v[86:87],v[86:87],v[102:103]\nv_pk_add_f32 v[120:121],v[120:121],s[24:25] neg_lo:[0,1] neg_hi:[0,1]\n" \
"s_waitcnt lgkmcnt(2)\nv_pk_fma_f32 v[88:89],v[88:89],v[24:25],v[120:121]\nbuffer_load_dwordx4 v6,s[40:43],s100 offen offset:2048 lds\nv_pk_add_f32 v[100:101],v[100:101],v[88:89]\n" \
"v_pk_fma_f32 v[102:103],v[88:89],v[88:89],v[102:103]\nv_pk_add_f32 v[122:123],v[122:123],s[24:25] neg_lo:[0,1] neg_hi:[0,1]\nv_pk_fma_f32 v[90:91],v[90:91],v[26:27],v[122:123]\nv_pk_add_f32 v[100:101],v[100:101],v[90:91]\n" \
"v_pk_fma_f32 v[102:103],v[90:91],v[90:91],v[102:103]\nv_pk_add_f32 v[124:125],v[124:125],s[24:25] neg_lo:[0,1] neg_hi:[0,1]\nbuffer_load_dwordx4 v7,s[40:43],s100 offen offset:3072 lds\ns_waitcnt lgkmcnt(1)\n" \
"v_pk_fma_f32 v[92:93],v[92:93],v[28:29],v[124:125]\nv_pk_add_f32 v[100:101],v[100:101],v[92:93]\nv_pk_fma_f32 v[102:103],v[92:93],v[92:93],v[102:103]\nv_pk_add_f32 v[126:127],v[126:127],s[24:25] neg_lo:[0,1] neg_hi:[0,1]\n" \
"v_pk_fma_f32 v[94:95],v[94:95],v[30:31],v[126:127]\nv_pk_add_f32 v[100:101],v[100:101],v[94:95]\nv_pk_fma_f32 v[102:103],v[94:95],v[94:95],v[102:103]\nv_pk_add_f32 v[98:99],v[98:99],s[24:25] neg_lo:[0,1] neg_hi:[0,1]\n" \
"s_waitcnt lgkmcnt(0)\nv_pk_fma_f32 v[96:97],v[96:97],v[58:59],v[98:99]\nv_pk_add_f32 v[100:101],v[100:101],v[96:97]\nv_pk_fma_f32 v[102:103],v[96:97],v[96:97],v[102:103]\n" \
"v_add_f32 v100,v100,v101\nv_add_f32 v102,v102,v103\nv_pk_add_f32 v[62:63],v[62:63],v[60:61] op_sel_hi:[1,0]\nv_cvt_pk_bf16_f32 v32,v62,v63\n" \
"v_permlane16_swap_b32 v100,v102\nv_add_f32 v100,v100,v102\nv_pk_add_f32 v[64:65],v[64:65],v[60:61] op_sel_hi:[1,0]\nv_cvt_pk_bf16_f32 v33,v64,v65\n" \
"v_add_f32_dpp v100,v100,v100 quad_perm:[1,0,3,2] row_mask:0xf bank_mask:0xf bound_ctrl:1\nv_pk_add_f32 v[66:67],v[66:67],v[60:61] op_sel_hi:[1,0]\nv_cvt_pk_bf16_f32 v34,v66,v67\nv_add_f32_dpp v100,v100,v100 quad_perm:[2,3,0,1] row_mask:0xf bank_mask:0xf bound_ctrl:1\n" \
"v_pk_add_f32 v[68:69],v[68:69],v[60:61] op_sel_hi:[1,0]\nv_cvt_pk_bf16_f32 v35,v68,v69\nv_add_f32_dpp v100,v100,v100 row_half_mirror row_mask:0xf bank_mask:0xf bound_ctrl:1\nv_pk_add_f32 v[70:71],v[70:71],v[60:61] op_sel_hi:[1,0]\n" \
"v_cvt_pk_bf16_f32 v36,v70,v71\nv_add_f32_dpp v100,v100,v100 row_mirror row_mask:0xf bank_mask:0xf bound_ctrl:1\nv_mul_f32 v104,0x3ae38e39,v100\nv_mul_f32 v102,0x3ae38e39,v100\n" \
"v_pk_add_f32 v[72:73],v[72:73],v[60:61] op_sel_hi:[1,0]\nv_cvt_pk_bf16_f32 v37,v72,v73\nv_permlane16_swap_b32 v104,v102\nv_fma_f32 v102,-v104,v104,v102\n" \
"v_max_f32 v102,0,v102\nv_add_f32 v102,0x3727c5ac,v102\nv_rsq_f32_e64 v100,v102\nv_pk_add_f32 v[74:75],v[74:75],v[60:61] op_sel_hi:[1,0]\n" \
"v_mul_f32 v101,-v104,v100\nv_pk_fma_f32 v[80:81],v[80:81],v[100:101],v[100:101] op_sel:[0,0,1] op_sel_hi:[1,0,1]\nv_pk_fma_f32 v[82:83],v[82:83],v[100:101],v[100:101] op_sel:[0,0,1] op_sel_hi:[1,0,1]\nv_pk_fma_f32 v[84:85],v[84:85],v[100:101],v[100:101] op_sel:[0,0,1] op_sel_hi:[1,0,1]\n" \
"v_pk_fma_f32 v[86:87],v[86:87],v[100:101],v[100:101] op_sel:[0,0,1] op_sel_hi:[1,0,1]\nv_cvt_pk_bf16_f32 v16,v80,v81\nv_cvt_pk_bf16_f32 v17,v82,v83\nv_cvt_pk_bf16_f32 v18,v84,v85\n" \
"v_cvt_pk_bf16_f32 v19,v86,v87\nds_write_b128 v48,v[16:19]\nv_pk_fma_f32 v[88:89],v[88:89],v[100:101],v[100:101] op_sel:[0,0,1] op_sel_hi:[1,0,1]\nv_pk_fma_f32 v[90:91],v[90:91],v[100:101],v[100:101] op_sel:[0,0,1] op_sel_hi:[1,0,1]\n" \
"v_pk_fma_f32 v[92:93],v[92:93],v[100:101],v[100:101] op_sel:[0,0,1] op_sel_hi:[1,0,1]\nv_pk_fma_f32 v[94:95],v[94:95],v[100:101],v[100:101] op_sel:[0,0,1] op_sel_hi:[1,0,1]\nv_cvt_pk_bf16_f32 v20,v88,v89\nv_cvt_pk_bf16_f32 v21,v90,v91\n" \
"v_cvt_pk_bf16_f32 v22,v92,v93\nv_cvt_pk_bf16_f32 v23,v94,v95\nds_write_b128 v48,v[20:23] offset:512\nv_pk_fma_f32 v[96:97],v[96:97],v[100:101],v[100:101] op_sel:[0,0,1] op_sel_hi:[1,0,1]\n" \
"v_cvt_pk_bf16_f32 v24,v96,v97\nds_write_b32 v49,v24 offset:1024\nv_cvt_pk_bf16_f32 v38,v74,v75\nv_pk_add_f32 v[76:77],v[76:77],v[60:61] op_sel_hi:[1,0]\n" \
"v_cvt_pk_bf16_f32 v39,v76,v77\nv_pk_add_f32 v[78:79],v[78:79],v[60:61] op_sel_hi:[1,0]\nv_cvt_pk_bf16_f32 v40,v78,v79\nv_bfe_u32 v50,v0,2,2\n" \
"v_mul_u32_u24 v50,4896,v50\nv_bfe_u32 v51,v0,4,2\nv_lshl_add_u32 v51,v51,2,s3\nv_lshl_add_u32 v50,v51,5,v50\n" \
"v_and_b32 v52,3,v0\nv_lshl_add_u32 v50,v52,3,v50\nv_and_b32 v52,0xf0,v1\nv_lshl_add_u32 v51,v51,16,v52\n" \
"v_bfrev_b32 v52,1\nv_cmp_gt_u32 vcc,256,v1\nv_and_b32 v53,31,v0\nv_mul_u32_u24 v53,12,v53\n" \
"v_cndmask_b32 v52,v52,v51,vcc\nv_sub_u32 v53,v12,v53\ns_lshl_b32 s24,s34,8\ns_add_u32 s24,s24,9764864\n" \
"s_waitcnt lgkmcnt(0)\ns_barrier\nds_read_b64_tr_b16 v[112:113],v50 offset:9216\nds_read_b64_tr_b16 v[114:115],v50 offset:11664\n" \
"ds_read_b64_tr_b16 v[116:117],v50 offset:9728\nds_read_b64_tr_b16 v[118:119],v50 offset:12176\nds_read_b64_tr_b16 v[120:121],v50 offset:10240\nds_read_b64_tr_b16 v[122:123],v50 offset:12688\n" \
"s_add_u32 s25,s24,1048576\ns_add_u32 s26,s24,2097152\ns_add_u32 s13,s13,7405568\ns_load_dwordx2 s[4:5],s[0:1],56\n" \
"s_load_dwordx2 s[16:17],s[0:1],64\ns_waitcnt lgkmcnt(0)\nbuffer_store_dwordx4 v[112:115],v51,s[36:39],s24 offen sc1\nbuffer_store_dwordx4 v[116:119],v51,s[36:39],s25 offen sc1\n" \
"buffer_store_dwordx4 v[120:123],v52,s[36:39],s26 offen sc1\nbuffer_store_dwordx4 v[32:35],v12,s[36:39],s13 offen sc1\nbuffer_store_dwordx4 v[36:39],v12,s[36:39],s13 offen offset:512 sc1\nbuffer_store_dword v40,v53,s[36:39],s13 offen offset:1024 sc1\n" \
"s_lshr_b32 s31,s33,3\ns_lshl1_add_u32 s31,s31,0\ns_sub_u32 s96,s31,7\ns_min_u32 s31,s31,s96\n" \
"s_lshl_b32 s96,s31,12\ns_add_u32 s96,s96,s57\ns_add_u32 m0,s96,0\ns_and_b32 s96,s31,1\n" \
"s_lshr_b32 s31,s31,1\ns_lshl_b32 s31,s31,10\ns_add_u32 s31,s31,s96\ns_lshl_b32 s31,s31,7\n" \
"s_add_u32 s31,s31,s92\ns_add_u32 s31,s31,262144\ns_lshr_b32 s97,s33,3\ns_lshl1_add_u32 s97,s97,1\n" \
"s_sub_u32 s95,s97,7\ns_min_u32 s97,s97,s95\ns_lshl_b32 s95,s97,12\ns_add_u32 s95,s95,s57\n" \
"s_lshr_b32 s100,s33,3\ns_lshl1_add_u32 s100,s100,2\ns_sub_u32 s101,s100,7\ns_min_u32 s100,s100,s101\n" \
"s_lshl_b32 s101,s100,12\ns_add_u32 s101,s101,s57\nv_and_b32 v64,63,v0\ns_and_b32 s25,s33,7\n" \
"v_lshrrev_b32 v66,5,v64\nv_and_b32 v67,1,v0\ns_lshl2_add_u32 s25,s25,s3\ns_and_b32 s26,s33,6\n" \
"s_lshr_b32 s27,s3,1\nv_lshl_add_u32 v66,v66,1,v67\ns_mul_i32 s24,s32,1152\ns_lshl_b32 s25,s25,5\n" \
"s_add_u32 s26,s26,s27\ns_mov_b32 s6,9216\ns_mov_b32 s7,0x20000\nv_lshlrev_b32 v68,3,v66\n" \
"s_add_u32 s25,s25,s24\nv_bfe_u32 v72,v0,1,1\nv_lshrrev_b32 v74,2,v64\ns_lshl4_add_u32 s26,s26,s24\n" \
"v_and_b32 v73,12,v0\nv_lshrrev_b32 v71,4,v64\nv_and_b32 v69,7,v0\nbuffer_load_dwordx2 v[80:81],v68,s[4:7],s25 offen\n" \
"v_lshl_add_u32 v72,v72,2,s21\nv_and_b32 v74,12,v74\nv_add_u32 v71,v71,v73\ns_lshl_b32 s24,s3,4\n" \
"v_lshlrev_b32 v70,1,v69\nv_lshl_add_u32 v72,v72,4,v74\ns_mov_b32 s15,s19\nv_add_u32 v71,s24,v71\n" \
"buffer_load_ushort v82,v70,s[4:7],s26 offen offset:1024\ns_mov_b32 s18,4096\ns_mov_b32 s19,0x20000\nv_lshlrev_b32 v74,1,v71\n" \
"s_lshl_b32 s24,s22,8\nbuffer_load_ushort v83,v74,s[16:19],s24 offen\nv_add_u32 v72,s3,v72\nbuffer_load_ushort v84,v74,s[16:19],s24 offen offset:128\n" \
"v_lshlrev_b32_e64 v75,1,v72\nbuffer_load_ushort v85,v75,s[16:19],s24 offen\nv_lshl_add_u32 v73,v67,1,v73\nv_mul_u32_u24 v91,1152,v72\n" \
"v_lshl_add_u32 v91,v73,1,v91\nv_lshlrev_b32_e64 v73,2,v73\nv_add_u32 v73,163712,v73\nds_read_b64 v[86:87],v73\n" \
"s_mul_i32 s24,s22,147456\ns_mul_i32 s25,s20,288\nv_bfe_u32 v58,v0,1,1\ns_add_u32 s24,s24,s25\n" \
"v_mul_u32_u24 v58,544,v58\nv_bfe_u32 v75,v0,2,1\ns_lshl_b32 s25,s21,6\nv_lshl_add_u32 v58,v75,8,v58\n" \
"s_add_u32 s25,s25,s24\nv_bfe_u32 v75,v0,3,2\ns_add_u32 s24,s24,256\nv_lshl_add_u32 v58,v75,6,v58\n" \
"v_add_u32 v91,s24,v91\nv_mul_u32_u24 v90,1152,v71\nv_and_b32 v74,3,v0\nv_lshl_add_u32 v58,v66,4,v58\n" \
"s_mul_i32 s24,s3,1056\nv_lshl_add_u32 v90,v74,3,v90\nv_add_u32 v58,s24,v58\nv_add_u32 v90,s25,v90\n" \
"s_and_b32 s24,s3,1\nv_mul_u32_u24 v60,288,v75\ns_lshl_b32 s24,s24,2\ns_lshr_b32 s25,s3,1\n" \
"v_lshrrev_b32 v75,5,v64\ns_add_u32 s24,s24,s25\nv_lshl_add_u32 v60,v75,6,v60\ns_lshl_b32 s24,s24,5\n" \
"v_lshl_add_u32 v60,v69,2,v60\ns_add_u32 s24,s24,4224\nv_add_u32 v60,s24,v60\ns_bfe_u32 s24,s33,0x10001\n" \
"s_add_u32 s24,s24,s15\ns_add_u32 s24,s24,17\ns_lshl2_add_u32 s24,s24,s3\ns_waitcnt lgkmcnt(0)\n" \
"v_lshrrev_b32 v86,16,v86\ns_lshl_b32 s24,s24,7\nv_mov_b32 v92,v4\nv_mov_b32 v93,v5\n" \
"v_add_u32 v74,s57,v1\nv_or_b32 v95,v86,v87\nv_lshl_add_u32 v62,v64,1,s24\nv_bfrev_b32 v76,1\n" \
"v_cmp_eq_u32 vcc,0,v64\ns_lshl2_add_u32 s24,s34,s3\ns_lshl2_add_u32 s24,s24,65536\nv_cndmask_b32 v76,v76,0,vcc\n" \
"v_mov_b32 v77,1\ns_barrier\nv_mov_b32 v57,0xc0135761\nv_add_u32 v59,0x80000,v1\n" \
"v_lshl_add_u32 v61,v64,3,s23\nv_mov_b32 v63,0\nv_bfe_u32 v100,v0,4,2\nv_bfe_u32 v101,v0,2,2\n" \
"v_lshl_add_u32 v100,v100,3,v101\nv_bfe_u32 v101,v0,3,2\nv_lshl_add_u32 v100,v100,2,v101\nv_and_b32 v101,3,v0\n" \
"v_lshl_add_u32 v100,v100,2,v101\nv_lshlrev_b32 v100,3,v100\nv_xor_b32 v8,0,v100\nv_add_u32 v8,s57,v8\n" \
"v_mov_b32 v88,v8\nv_xor_b32 v9,32,v100\nv_add_u32 v9,s57,v9\nv_xor_b32 v10,64,v100\n" \
"v_add_u32 v10,s57,v10\nv_xor_b32 v11,96,v100\nv_add_u32 v11,s57,v11\ns_waitcnt vmcnt(5)\n" \
"buffer_store_dword v77,v76,s[36:39],s24 offen sc1\nds_read_b64_tr_b16 v[128:129],v8\nds_read_b64_tr_b16 v[130:131],v8 offset:512\nds_read_b64_tr_b16 v[132:133],v9\n" \
"ds_read_b64_tr_b16 v[134:135],v9 offset:512\nds_read_b64_tr_b16 v[136:137],v10\nds_read_b64_tr_b16 v[138:139],v10 offset:512\nds_read_b64_tr_b16 v[140:141],v11\n" \
"ds_read_b64_tr_b16 v[142:143],v11 offset:512\nds_read_b64_tr_b16 v[144:145],v8 offset:4096\nds_read_b64_tr_b16 v[146:147],v8 offset:4608\nds_read_b64_tr_b16 v[148:149],v9 offset:4096\n" \
"ds_read_b64_tr_b16 v[150:151],v9 offset:4608\nds_read_b64_tr_b16 v[152:153],v10 offset:4096\nds_read_b64_tr_b16 v[154:155],v10 offset:4608\nds_read_b64_tr_b16 v[156:157],v11 offset:4096\n" \
"ds_read_b64_tr_b16 v[158:159],v11 offset:4608\nds_read_b64_tr_b16 v[160:161],v8 offset:8192\nds_read_b64_tr_b16 v[162:163],v8 offset:8704\ns_waitcnt vmcnt(5)\n" \
"v_and_b32 v53,0xffff0000,v80\nv_and_b32 v55,0xffff0000,v81\nv_lshlrev_b32 v52,16,v80\nv_lshlrev_b32 v54,16,v81\n" \
"ds_read_b64_tr_b16 v[164:165],v9 offset:8192\nds_read_b64_tr_b16 v[166:167],v9 offset:8704\nds_read_b64_tr_b16 v[168:169],v10 offset:8192\nds_read_b64_tr_b16 v[170:171],v10 offset:8704\n" \
"ds_read_b64_tr_b16 v[172:173],v11 offset:8192\ns_waitcnt vmcnt(4)\nv_lshlrev_b32 v56,16,v82\nds_read_b64_tr_b16 v[174:175],v11 offset:8704\n" \
"ds_read_b64_tr_b16 v[176:177],v8 offset:12288\nds_read_b64_tr_b16 v[178:179],v8 offset:12800\nds_read_b64_tr_b16 v[180:181],v9 offset:12288\nds_read_b64_tr_b16 v[182:183],v9 offset:12800\n" \
"s_waitcnt vmcnt(2)\nv_lshl_or_b32 v89,v84,16,v83\nds_write_b128 v74,v[88:91] offset:28672\ns_waitcnt vmcnt(1)\n" \
"v_lshlrev_b32_e64 v94,16,v85\nds_write_b128 v74,v[92:95] offset:29696\nds_read_b64_tr_b16 v[184:185],v10 offset:12288\nds_read_b64_tr_b16 v[186:187],v10 offset:12800\n" \
"ds_read_b64_tr_b16 v[188:189],v11 offset:12288\nds_read_b64_tr_b16 v[190:191],v11 offset:12800\ns_waitcnt lgkmcnt(0)\nbuffer_load_dwordx4 v4,s[40:43],s31 offen lds\n" \
"buffer_load_dwordx4 v5,s[40:43],s31 offen offset:1024 lds\nbuffer_load_dwordx4 v6,s[40:43],s31 offen offset:2048 lds\nbuffer_load_dwordx4 v7,s[40:43],s31 offen offset:3072 lds\ns_add_u32 m0,s95,0\n" \
"s_and_b32 s95,s97,1\ns_lshr_b32 s97,s97,1\ns_lshl_b32 s97,s97,10\ns_add_u32 s97,s97,s95\n" \
"s_lshl_b32 s97,s97,7\ns_add_u32 s97,s97,s92\ns_add_u32 s97,s97,262144\nbuffer_load_dwordx4 v4,s[40:43],s97 offen lds\n" \
"buffer_load_dwordx4 v5,s[40:43],s97 offen offset:1024 lds\nbuffer_load_dwordx4 v6,s[40:43],s97 offen offset:2048 lds\nbuffer_load_dwordx4 v7,s[40:43],s97 offen offset:3072 lds\ns_add_u32 m0,s101,0\n" \
"s_and_b32 s101,s100,1\ns_lshr_b32 s100,s100,1\ns_lshl_b32 s100,s100,10\ns_add_u32 s100,s100,s101\n" \
"s_lshl_b32 s100,s100,7\ns_add_u32 s100,s100,s92\ns_add_u32 s100,s100,262144\nbuffer_load_dwordx4 v4,s[40:43],s100 offen lds\n" \
"buffer_load_dwordx4 v5,s[40:43],s100 offen offset:1024 lds\nbuffer_load_dwordx4 v6,s[40:43],s100 offen offset:2048 lds\nbuffer_load_dwordx4 v7,s[40:43],s100 offen offset:3072 lds\ns_lshr_b32 s31,s33,3\n" \
"s_lshl1_add_u32 s31,s31,3\ns_sub_u32 s96,s31,7\ns_min_u32 s31,s31,s96\ns_lshl_b32 s96,s31,12\n" \
"s_add_u32 s96,s96,s57\ns_add_u32 m0,s96,0\ns_and_b32 s96,s31,1\ns_lshr_b32 s31,s31,1\n" \
"s_lshl_b32 s31,s31,10\ns_add_u32 s31,s31,s96\ns_lshl_b32 s31,s31,7\ns_add_u32 s31,s31,s92\n" \
"s_add_u32 s31,s31,262144\nbuffer_load_dwordx4 v4,s[40:43],s31 offen lds\nbuffer_load_dwordx4 v5,s[40:43],s31 offen offset:1024 lds\nbuffer_load_dwordx4 v6,s[40:43],s31 offen offset:2048 lds\n" \
"buffer_load_dwordx4 v7,s[40:43],s31 offen offset:3072 lds\ns_lshr_b32 s97,s33,3\ns_lshl1_add_u32 s97,s97,4\ns_sub_u32 s95,s97,7\n" \
"s_min_u32 s97,s97,s95\ns_lshl_b32 s95,s97,12\ns_add_u32 s95,s95,s57\ns_add_u32 m0,s95,0\n" \
"s_and_b32 s95,s97,1\ns_lshr_b32 s97,s97,1\ns_lshl_b32 s97,s97,10\ns_add_u32 s97,s97,s95\n" \
"s_lshl_b32 s97,s97,7\ns_add_u32 s97,s97,s92\ns_add_u32 s97,s97,262144\nbuffer_load_dwordx4 v4,s[40:43],s97 offen lds\n" \
"buffer_load_dwordx4 v5,s[40:43],s97 offen offset:1024 lds\nbuffer_load_dwordx4 v6,s[40:43],s97 offen offset:2048 lds\nbuffer_load_dwordx4 v7,s[40:43],s97 offen offset:3072 lds\ns_lshr_b32 s100,s33,3\n" \
"s_lshl1_add_u32 s100,s100,5\ns_sub_u32 s101,s100,7\ns_min_u32 s100,s100,s101\ns_lshl_b32 s101,s100,12\n" \
"s_add_u32 s101,s101,s57\ns_add_u32 m0,s101,0\ns_and_b32 s101,s100,1\ns_lshr_b32 s100,s100,1\n" \
"s_lshl_b32 s100,s100,10\ns_add_u32 s100,s100,s101\ns_lshl_b32 s100,s100,7\ns_add_u32 s100,s100,s92\n" \
"s_add_u32 s100,s100,262144\nbuffer_load_dwordx4 v4,s[40:43],s100 offen lds\nbuffer_load_dwordx4 v5,s[40:43],s100 offen offset:1024 lds\nbuffer_load_dwordx4 v6,s[40:43],s100 offen offset:2048 lds\n" \
"buffer_load_dwordx4 v7,s[40:43],s100 offen offset:3072 lds\ns_lshr_b32 s31,s33,3\ns_lshl1_add_u32 s31,s31,6\ns_sub_u32 s96,s31,7\n" \
"s_min_u32 s31,s31,s96\ns_lshl_b32 s96,s31,12\ns_add_u32 s96,s96,s57\ns_add_u32 m0,s96,0\n" \
"s_and_b32 s96,s31,1\ns_lshr_b32 s31,s31,1\ns_lshl_b32 s31,s31,10\ns_add_u32 s31,s31,s96\n" \
"s_lshl_b32 s31,s31,7\ns_add_u32 s31,s31,s92\ns_add_u32 s31,s31,262144\nbuffer_load_dwordx4 v4,s[40:43],s31 offen lds\n" \
"buffer_load_dwordx4 v5,s[40:43],s31 offen offset:1024 lds\nbuffer_load_dwordx4 v6,s[40:43],s31 offen offset:2048 lds\nbuffer_load_dwordx4 v7,s[40:43],s31 offen offset:3072 lds\ns_branch .Lgemm_%=\n" \
".LC_%=:\nv_bfe_u32 v112,v0,3,2\nv_bfe_u32 v113,v0,5,1\nv_lshl_add_u32 v112,v112,8,v113\n" \
"s_lshl_b32 s48,s33,2\nv_lshrrev_b32 v113,3,v0\ns_add_u32 s48,s48,s3\nv_and_b32 v113,6,v113\n" \
"v_and_b32 v114,7,v0\ns_mul_i32 s48,s48,18432\ns_mul_i32 s49,s32,1152\nv_mul_u32_u24 v112,9216,v112\n" \
"v_xor_b32 v113,v113,v114\ns_add_u32 s48,s48,s49\nv_lshl_add_u32 v4,v113,4,v112\ns_mov_b32 s10,18874368\n" \
"s_mov_b32 s11,0x20000\ns_add_u32 s48,s48,1024\ns_add_u32 m0,s88,20224\ns_add_u32 s50,s48,9437184\n" \
"buffer_load_dwordx4 v4,s[8:11],s48 offen lds\ns_add_u32 m0,s88,24320\ns_mov_b32 s51,4096\nbuffer_load_dwordx4 v4,s[8:11],s50 offen lds\n" \
"s_add_u32 m0,s88,28416\ns_mov_b32 s52,8192\nbuffer_load_dwordx4 v2,s[4:7],0 offen lds\ns_add_u32 m0,s88,32512\n" \
"s_mov_b32 s53,14336\nbuffer_load_dwordx4 v2,s[4:7],s51 offen lds\nbuffer_load_dwordx4 v[234:237],v1,s[4:7],s52 offen\nbuffer_load_dwordx4 v[238:241],v1,s[4:7],s52 offen offset:1024\n" \
"buffer_load_dwordx4 v[242:245],v2,s[4:7],s53 offen\ns_mov_b32 s52,18432\nbuffer_load_dwordx4 v[252:255],v2,s[4:7],s52 offen\ns_and_b32 s60,s33,7\n" \
"s_mul_i32 s92,s3,589824\ns_mul_i32 s57,s3,30720\ns_add_u32 s92,s92,s49\ns_lshl_b32 s62,s60,7\n" \
"v_add_u32 v5,17408,v4\nv_add_u32 v6,34816,v4\nv_add_u32 v7,52224,v4\ns_add_u32 s57,s57,38272\n" \
"s_add_u32 s92,s92,s62\ns_load_dwordx4 s[44:47],s[0:1],72\nv_and_b32 v120,0x3c0,v1\ns_lshr_b32 s59,s33,3\n" \
"v_bfe_u32 v121,v0,3,2\nv_lshlrev_b32 v120,1,v120\ns_lshl_b32 s56,s34,8\nv_bfe_u32 v73,v0,4,2\n" \
"s_lshl_b32 s59,s59,3\nv_bfe_u32 v113,v0,4,2\nv_lshl_add_u32 v120,v121,5,v120\nv_and_b32 v122,3,v0\n" \
"s_mul_i32 s54,s34,9344\ns_add_u32 s56,s56,10289152\ns_add_u32 s59,s59,s32\nv_and_b32 v72,15,v0\n" \
"v_cvt_f32_u32 v73,v73\nv_cvt_f32_u32 v74,s3\nv_mul_u32_u24 v113,589824,v113\nv_and_b32 v112,15,v0\n" \
"v_lshl_add_u32 v120,v122,3,v120\ns_add_u32 s35,s54,524288\ns_cmp_eq_u32 s3,2\ns_cselect_b32 s56,s56,2147483648\n" \
"s_lshl_b32 s59,s59,3\nv_bfe_u32 v233,v0,3,3\nv_mul_u32_u24 v233,8192,v233\nv_lshl_add_u32 v233,v1,3,v233\n" \
"s_lshl2_add_u32 s100,s33,s3\ns_add_u32 s100,s100,4294967260\ns_and_b32 s101,s100,31\ns_lshl_b32 s101,s101,3\n" \
"s_cmp_lt_u32 s100,256\ns_cselect_b32 s100,s100,0x40000\ns_lshr_b32 s100,s100,5\ns_add_u32 s100,s100,s101\n" \
"s_lshl_b32 s100,s100,6\ns_add_u32 s100,s100,s32\ns_mul_i32 s100,s100,1152\ns_mov_b32 m0,162176\n" \
"v_cvt_f32_u32_e64 v72,v72\nv_fmamk_f32 v73,v73,0x41800000,v74\nv_lshl_add_u32 v113,v112,4,v113\ns_add_u32 s58,s35,8192\n" \
"v_bfe_u32 v119,v0,4,2\nv_xor_b32 v8,0,v120\nv_xor_b32 v9,32,v120\nv_xor_b32 v10,64,v120\n" \
"v_xor_b32 v11,96,v120\ns_add_u32 s59,s59,s60\nv_mul_f32 v72,0xbb800000,v72\nv_and_b32 v118,15,v0\n" \
"v_mul_f32 v73,4.0,v73\nv_and_b32 v116,0x70,v2\nv_add_u32 v113,s56,v113\nv_add_u32 v114,s58,v2\n" \
"v_cmp_gt_u32 vcc,68,v0\nv_lshrrev_b32 v115,3,v0\nv_cvt_f32_u32 v120,s34\ns_lshl_b32 s59,s59,7\n" \
"s_lshl_b32 s61,s34,4\nv_lshrrev_b32 v48,1,v118\nv_and_b32 v49,1,v119\nv_mul_f32 v73,v73,v72\n" \
"v_cndmask_b32 v113,v113,v114,vcc\nv_lshl_add_u32 v115,v115,16,v116\nv_mul_f32 v50,0x3b800000,v120\ns_add_u32 s87,s59,262144\n" \
"s_add_u32 s61,s61,65536\nv_cvt_f32_u32 v48,v48\nv_cvt_f32_u32 v49,v49\nv_fmamk_f32 v79,v72,0x41800000,v73\n" \
"v_bfe_u32 v100,v0,1,2\nv_and_b32 v101,1,v0\nv_add_u32 v115,s87,v115\nv_mov_b32 v116,s61\n" \
"v_cmp_gt_u32 vcc,24,v0\nv_mul_f32 v48,0xbe000000,v48\nv_fma_f32 v49,v49,2.0,v50\nv_cos_f32 v78,v79\n" \
"v_lshl_add_u32 v100,v101,2,v100\nv_cndmask_b32 v115,v116,v115,vcc\nv_mul_f32 v49,v48,v49\nv_fmamk_f32 v81,v72,0x42000000,v73\n" \
"v_fmamk_f32 v83,v72,0x42400000,v73\nv_mul_f32 v101,0x3a000000,v120\nv_cvt_f32_u32 v100,v100\nv_bfrev_b32 v116,1\n" \
"v_cmp_gt_u32 vcc,25,v0\nv_fmamk_f32 v63,v48,0x40800000,v49\nv_cos_f32 v80,v81\nv_cos_f32 v82,v83\n" \
"v_fmamk_f32 v100,v100,0x3e000000,v101\nv_cndmask_b32 v115,v116,v115,vcc\nv_mov_b32 v16,-1\nv_mov_b32 v17,-1\n" \
"v_mov_b32 v18,-1\nv_mov_b32 v19,-1\nv_lshrrev_b32 v50,1,v119\nv_and_b32 v51,1,v0\n" \
"v_cos_f32 v62,v63\nv_cos_f32 v76,v73\nv_sin_f32 v77,v73\nv_sin_f32 v79,v79\n" \
"v_cos_f32 v84,v72\nv_sin_f32 v85,v72\nv_cvt_f32_u32 v101,v119\nbuffer_store_dwordx4 v[16:19],v2,s[36:39],s35 offen sc1\n" \
"v_cmp_eq_u32 vcc,v50,v51\nv_add_f32 v101,v101,v101\nv_mov_b32 v50,0x3e800000\nv_mul_f32 v101,v101,v100\n" \
"v_sin_f32 v81,v81\nv_sin_f32 v83,v83\nv_cndmask_b32 v52,0,v50,vcc\nv_cos_f32 v56,v49\n" \
"v_sin_f32 v57,v49\nv_sin_f32 v63,v63\nv_cos_f32 v106,v101\nv_sin_f32 v107,v101\n" \
"v_cmp_gt_u32 vcc,8,v118\nv_cvt_pk_f16_f32 v128,v76,-v77\nv_cvt_pk_f16_f32 v132,v77,v76\nv_cvt_pk_f16_f32 v129,v78,-v79\n" \
"v_cvt_pk_f16_f32 v133,v79,v78\nv_pk_mul_f32 v[92:93],v[76:77],v[84:85] op_sel:[1,1] op_sel_hi:[0,1] neg_lo:[1,0]\nv_pk_mul_f32 v[94:95],v[78:79],v[84:85] op_sel:[1,1] op_sel_hi:[0,1] neg_lo:[1,0]\ns_add_u32 s55,s35,4096\n" \
"v_cndmask_b32 v102,0,0.5,vcc\nv_lshlrev_b32 v51,31,v0\nbuffer_store_dwordx4 v[16:19],v2,s[36:39],s55 offen sc1\nv_pk_fma_f32 v[76:77],v[76:77],v[84:85],v[92:93] op_sel_hi:[1,0,1]\n" \
"v_pk_fma_f32 v[78:79],v[78:79],v[84:85],v[94:95] op_sel_hi:[1,0,1]\nv_xor_b32 v54,v51,v52\nv_sub_f32_e64 v104,0.5,v102\nv_cvt_pk_f16_f32 v130,v80,-v81\n" \
"v_cvt_pk_f16_f32 v134,v81,v80\nv_cvt_pk_f16_f32 v131,v82,-v83\nv_cvt_pk_f16_f32 v135,v83,v82\nv_pk_mul_f32 v[92:93],v[80:81],v[84:85] op_sel:[1,1] op_sel_hi:[0,1] neg_lo:[1,0]\n" \
"v_pk_mul_f32 v[94:95],v[82:83],v[84:85] op_sel:[1,1] op_sel_hi:[0,1] neg_lo:[1,0]\nv_pk_mul_f32 v[58:59],v[56:57],v[52:53] op_sel_hi:[1,0]\nv_pk_mul_f32 v[60:61],v[56:57],v[54:55] op_sel_hi:[1,0]\nv_pk_mul_f32 v[64:65],v[62:63],v[52:53] op_sel_hi:[1,0]\n" \
"v_pk_mul_f32 v[66:67],v[62:63],v[54:55] op_sel_hi:[1,0]\nv_pk_mul_f32 v[108:109],v[106:107],v[102:103] op_sel_hi:[1,0]\nv_pk_mul_f32 v[110:111],v[106:107],v[104:105] op_sel_hi:[1,0]\nv_pk_fma_f32 v[80:81],v[80:81],v[84:85],v[92:93] op_sel_hi:[1,0,1]\n" \
"v_pk_fma_f32 v[82:83],v[82:83],v[84:85],v[94:95] op_sel_hi:[1,0,1]\nv_fmamk_f32 v57,v48,0x3f800000,v49\nv_fmamk_f32 v63,v48,0x40a00000,v49\nv_fmamk_f32 v107,v100,0x3f800000,v101\n" \
"v_cvt_pk_f16_f32 v136,v76,-v77\nv_cvt_pk_f16_f32 v140,v77,v76\nv_cvt_pk_f16_f32 v137,v78,-v79\nv_cvt_pk_f16_f32 v141,v79,v78\n" \
"v_pk_mul_f32 v[92:93],v[76:77],v[84:85] op_sel:[1,1] op_sel_hi:[0,1] neg_lo:[1,0]\nv_pk_mul_f32 v[94:95],v[78:79],v[84:85] op_sel:[1,1] op_sel_hi:[0,1] neg_lo:[1,0]\nv_cos_f32 v56,v57\nv_cos_f32 v62,v63\n" \
"v_cos_f32_e64 v106,v107\nbuffer_store_dwordx4 v[16:19],v113,s[36:39],0 offen sc1\nv_pk_fma_f32 v[76:77],v[76:77],v[84:85],v[92:93] op_sel_hi:[1,0,1]\nv_pk_fma_f32 v[78:79],v[78:79],v[84:85],v[94:95] op_sel_hi:[1,0,1]\n" \
"v_cvt_pk_f16_f32 v138,v80,-v81\nv_cvt_pk_f16_f32 v142,v81,v80\nv_cvt_pk_f16_f32 v139,v82,-v83\nv_cvt_pk_f16_f32 v143,v83,v82\n" \
"v_pk_mul_f32 v[92:93],v[80:81],v[84:85] op_sel:[1,1] op_sel_hi:[0,1] neg_lo:[1,0]\nv_pk_mul_f32 v[94:95],v[82:83],v[84:85] op_sel:[1,1] op_sel_hi:[0,1] neg_lo:[1,0]\nv_pk_fma_f32 v[80:81],v[80:81],v[84:85],v[92:93] op_sel_hi:[1,0,1]\nv_pk_fma_f32 v[82:83],v[82:83],v[84:85],v[94:95] op_sel_hi:[1,0,1]\n" \
"v_sin_f32 v57,v57\nv_sin_f32 v63,v63\nv_sin_f32_e64 v107,v107\nv_cvt_pk_f16_f32 v144,v76,-v77\n" \
"v_cvt_pk_f16_f32 v148,v77,v76\nv_cvt_pk_f16_f32 v145,v78,-v79\nv_cvt_pk_f16_f32 v149,v79,v78\nv_pk_mul_f32 v[92:93],v[76:77],v[84:85] op_sel:[1,1] op_sel_hi:[0,1] neg_lo:[1,0]\n" \
"v_pk_mul_f32 v[94:95],v[78:79],v[84:85] op_sel:[1,1] op_sel_hi:[0,1] neg_lo:[1,0]\nv_mov_b32 v20,0\nv_mov_b32 v21,0\nv_mov_b32 v22,0\n" \
"v_mov_b32 v23,0\nbuffer_store_dwordx4 v[20:23],v115,s[36:39],0 offen sc1\nv_pk_fma_f32 v[76:77],v[76:77],v[84:85],v[92:93] op_sel_hi:[1,0,1]\nv_pk_fma_f32 v[78:79],v[78:79],v[84:85],v[94:95] op_sel_hi:[1,0,1]\n" \
"v_cvt_pk_f16_f32 v40,v58,-v61\nv_cvt_pk_f16_f32 v44,v59,v60\nv_cvt_pk_f16_f32 v41,v64,-v67\nv_cvt_pk_f16_f32 v45,v65,v66\n" \
"v_cvt_pk_f16_f32 v216,v108,-v109\nv_cvt_pk_f16_f32 v220,v109,v108\nv_cvt_pk_f16_f32 v217,v110,-v111\nv_cvt_pk_f16_f32 v221,-v111,-v110\n" \
"v_cvt_pk_f16_f32 v146,v80,-v81\nv_cvt_pk_f16_f32 v150,v81,v80\nv_cvt_pk_f16_f32 v147,v82,-v83\nv_cvt_pk_f16_f32 v151,v83,v82\n" \
"v_pk_mul_f32 v[92:93],v[80:81],v[84:85] op_sel:[1,1] op_sel_hi:[0,1] neg_lo:[1,0]\nv_pk_mul_f32 v[94:95],v[82:83],v[84:85] op_sel:[1,1] op_sel_hi:[0,1] neg_lo:[1,0]\nbuffer_load_dword v233,s[8:11],s100 offen lds\nv_pk_mul_f32 v[58:59],v[56:57],v[52:53] op_sel_hi:[1,0]\n" \
"v_pk_mul_f32 v[60:61],v[56:57],v[54:55] op_sel_hi:[1,0]\nv_pk_mul_f32 v[64:65],v[62:63],v[52:53] op_sel_hi:[1,0]\nv_pk_mul_f32 v[66:67],v[62:63],v[54:55] op_sel_hi:[1,0]\nv_pk_mul_f32 v[108:109],v[106:107],v[102:103] op_sel_hi:[1,0]\n" \
"v_pk_mul_f32 v[110:111],v[106:107],v[104:105] op_sel_hi:[1,0]\nv_pk_fma_f32 v[80:81],v[80:81],v[84:85],v[92:93] op_sel_hi:[1,0,1]\nv_pk_fma_f32 v[82:83],v[82:83],v[84:85],v[94:95] op_sel_hi:[1,0,1]\nv_lshlrev_b32 v3,4,v119\n" \
"v_lshrrev_b32 v15,1,v1\nv_lshl_add_u32 v3,v118,6,v3\nv_cvt_pk_f16_f32 v42,v58,-v61\nv_cvt_pk_f16_f32 v46,v59,v60\n" \
"v_cvt_pk_f16_f32 v43,v64,-v67\nv_cvt_pk_f16_f32 v47,v65,v66\nv_cvt_pk_f16_f32 v218,v108,-v109\nv_cvt_pk_f16_f32 v222,v109,v108\n" \
"v_cvt_pk_f16_f32 v219,v110,-v111\nv_cvt_pk_f16_f32 v223,-v111,-v110\nv_cvt_pk_f16_f32 v152,v76,-v77\nv_cvt_pk_f16_f32 v156,v77,v76\n" \
"v_cvt_pk_f16_f32 v153,v78,-v79\nv_cvt_pk_f16_f32 v157,v79,v78\nv_cvt_pk_f16_f32 v154,v80,-v81\nv_cvt_pk_f16_f32 v158,v81,v80\n" \
"v_cvt_pk_f16_f32 v155,v82,-v83\nv_cvt_pk_f16_f32 v159,v83,v82\nv_add_u32 v15,s57,v15\nv_add_u32 v8,s57,v8\n" \
"v_add_u32 v9,s57,v9\nv_add_u32 v10,s57,v10\nv_add_u32 v11,s57,v11\nv_lshrrev_b32 v77,5,v0\n" \
"v_bfe_u32 v113,v0,1,5\nv_and_b32 v115,1,v0\ns_lshl_b32 s63,s3,1\nv_mul_u32_u24 v77,1160,v77\n" \
"v_and_b32 v76,31,v0\nv_and_b32 v114,6,v113\nv_add_u32 v115,s63,v115\nv_mad_u32_u24 v78,v76,36,v77\n" \
"v_xor_b32 v114,v114,v115\nv_lshlrev_b32 v113,7,v113\nv_lshl_add_u32 v113,v114,4,v113\ns_add_u32 s64,s54,2916352\n" \
"s_add_u32 s65,s64,4096\nv_bfrev_b32 v114,1\nv_cmp_gt_u32 vcc,72,v0\ns_lshl_b32 s67,s32,2\n" \
"s_add_u32 s66,s64,8192\nv_cndmask_b32 v114,v114,v2,vcc\ns_add_u32 s67,s67,s3\ns_lshl_b32 s67,s67,5\n" \
"s_add_u32 s67,s67,s33\ns_lshl_b32 s67,s67,11\ns_add_u32 s67,s67,5308416\ns_lshl2_add_u32 s69,s3,161664\n" \
"s_waitcnt vmcnt(20)\nv_readfirstlane_b32 s28,v126\nv_readfirstlane_b32 s29,v127\ns_mul_i32 s72,s3,2320\n" \
"v_mov_b32 v251,s69\nv_cmp_gt_u32 vcc,32,v0\ns_add_u32 s28,s28,1\ns_addc_u32 s29,s29,0\n" \
"s_lshl_b32 s7,s32,11\nv_cndmask_b32 v74,2.0,0,vcc\ns_mov_b32 s93,128\ns_mov_b32 s73,8192\n" \
"s_mov_b32 s74,12288\ns_mov_b32 s75,17408\ns_mov_b32 s76,21504\ns_mov_b32 s77,16384\n" \
"s_mov_b32 s78,25600\ns_add_u32 s7,s7,40960\ns_mov_b32 s81,0x3d800000\ns_mov_b32 s42,18874368\n" \
"s_mov_b32 s43,0x20000\ns_waitcnt lgkmcnt(0)\ns_mov_b64 s[40:41],s[44:45]\ns_mov_b64 s[44:45],s[46:47]\n" \
"s_mov_b32 s46,2359296\ns_mov_b32 s47,0x20000\ns_waitcnt vmcnt(19)\nv_lshlrev_b32_e64 v48,16,v224\n" \
"v_and_b32 v49,0xffff0000,v224\nv_readlane_b32 s70,v48,0\nv_readlane_b32 s71,v48,32\nv_and_b32 v51,0xffff0000,v225\n" \
"v_mov_b32 v72,s70\nv_mov_b32 v73,s71\nv_lshlrev_b32 v50,16,v225\nv_and_b32 v53,0xffff0000,v226\n" \
"v_permlane32_swap_b32 v72,v73\nv_pk_add_f32 v[48:49],v[48:49],v[72:73] op_sel_hi:[1,0] neg_lo:[0,1] neg_hi:[0,1]\nv_pk_add_f32 v[50:51],v[50:51],v[72:73] op_sel_hi:[1,0] neg_lo:[0,1] neg_hi:[0,1]\nv_pk_mul_f32 v[68:69],v[48:49],v[48:49]\n" \
"v_lshlrev_b32_e64 v52,16,v226\nv_and_b32 v55,0xffff0000,v227\nv_pk_add_f32 v[52:53],v[52:53],v[72:73] op_sel_hi:[1,0] neg_lo:[0,1] neg_hi:[0,1]\nv_pk_add_f32 v[66:67],v[48:49],v[50:51]\n" \
"v_pk_fma_f32 v[68:69],v[50:51],v[50:51],v[68:69]\nv_lshlrev_b32 v54,16,v227\ns_waitcnt vmcnt(18)\nv_and_b32 v57,0xffff0000,v228\n" \
"v_pk_add_f32 v[54:55],v[54:55],v[72:73] op_sel_hi:[1,0] neg_lo:[0,1] neg_hi:[0,1]\nv_pk_add_f32 v[66:67],v[66:67],v[52:53]\nv_pk_fma_f32 v[68:69],v[52:53],v[52:53],v[68:69]\nv_lshlrev_b32_e64 v56,16,v228\n" \
"v_and_b32 v59,0xffff0000,v229\nv_pk_add_f32 v[56:57],v[56:57],v[72:73] op_sel_hi:[1,0] neg_lo:[0,1] neg_hi:[0,1]\nv_pk_add_f32 v[66:67],v[66:67],v[54:55]\nv_pk_fma_f32 v[68:69],v[54:55],v[54:55],v[68:69]\n" \
"v_lshlrev_b32_e64 v58,16,v229\nv_and_b32 v61,0xffff0000,v230\nv_pk_add_f32 v[58:59],v[58:59],v[72:73] op_sel_hi:[1,0] neg_lo:[0,1] neg_hi:[0,1]\nv_pk_add_f32 v[66:67],v[66:67],v[56:57]\n" \
"v_pk_fma_f32 v[68:69],v[56:57],v[56:57],v[68:69]\nv_lshlrev_b32_e64 v60,16,v230\nv_and_b32 v63,0xffff0000,v231\nv_pk_add_f32 v[60:61],v[60:61],v[72:73] op_sel_hi:[1,0] neg_lo:[0,1] neg_hi:[0,1]\n" \
"v_pk_add_f32 v[66:67],v[66:67],v[58:59]\nv_pk_fma_f32 v[68:69],v[58:59],v[58:59],v[68:69]\nv_lshlrev_b32 v62,16,v231\ns_waitcnt vmcnt(17)\n" \
"v_and_b32 v65,0xffff0000,v232\nv_pk_add_f32 v[62:63],v[62:63],v[72:73] op_sel_hi:[1,0] neg_lo:[0,1] neg_hi:[0,1]\nv_pk_add_f32 v[66:67],v[66:67],v[60:61]\nv_pk_fma_f32 v[68:69],v[60:61],v[60:61],v[68:69]\n" \
"v_lshlrev_b32_e64 v64,16,v232\nv_pk_add_f32 v[64:65],v[64:65],v[72:73] op_sel_hi:[1,0] neg_lo:[0,1] neg_hi:[0,1]\nv_pk_add_f32 v[66:67],v[66:67],v[62:63]\nv_pk_fma_f32 v[68:69],v[62:63],v[62:63],v[68:69]\n" \
"v_pk_add_f32 v[66:67],v[66:67],v[64:65]\nv_pk_fma_f32 v[68:69],v[64:65],v[64:65],v[68:69]\nv_add_f32 v66,v66,v67\nv_add_f32 v68,v68,v69\n" \
"s_nop 1\nv_permlane16_swap_b32 v66,v68\nv_add_f32 v66,v66,v68\ns_nop 1\n" \
"v_add_f32_dpp v66,v66,v66 quad_perm:[1,0,3,2] row_mask:0xf bank_mask:0xf bound_ctrl:1\ns_nop 1\nv_add_f32_dpp v66,v66,v66 quad_perm:[2,3,0,1] row_mask:0xf bank_mask:0xf bound_ctrl:1\ns_nop 1\n" \
"v_add_f32_dpp v66,v66,v66 row_half_mirror row_mask:0xf bank_mask:0xf bound_ctrl:1\ns_nop 1\nv_add_f32_dpp v66,v66,v66 row_mirror row_mask:0xf bank_mask:0xf bound_ctrl:1\nv_mul_f32 v70,0x3ae38e39,v66\n" \
"v_mul_f32 v68,0x3ae38e39,v66\ns_nop 1\nv_permlane16_swap_b32 v70,v68\nv_fma_f32 v68,-v70,v70,v68\n" \
"v_max_f32 v68,0,v68\nv_add_f32 v68,0x3727c5ac,v68\nv_rsq_f32 v66,v68\nv_add_f32 v250,v70,v72\n" \
"v_mul_f32 v67,-v70,v66\nv_pk_fma_f32 v[48:49],v[48:49],v[66:67],v[66:67] op_sel:[0,0,1] op_sel_hi:[1,0,1]\nv_pk_fma_f32 v[50:51],v[50:51],v[66:67],v[66:67] op_sel:[0,0,1] op_sel_hi:[1,0,1]\nv_cvt_pk_f16_f32 v80,v48,v49\n" \
"v_cvt_pk_f16_f32 v81,v50,v51\nv_pk_fma_f32 v[52:53],v[52:53],v[66:67],v[66:67] op_sel:[0,0,1] op_sel_hi:[1,0,1]\nv_pk_fma_f32 v[54:55],v[54:55],v[66:67],v[66:67] op_sel:[0,0,1] op_sel_hi:[1,0,1]\nds_write2_b32 v78,v80,v81 offset0:0 offset1:1\n" \
"v_cvt_pk_f16_f32 v82,v52,v53\nv_cvt_pk_f16_f32 v83,v54,v55\nv_pk_fma_f32 v[56:57],v[56:57],v[66:67],v[66:67] op_sel:[0,0,1] op_sel_hi:[1,0,1]\nv_pk_fma_f32 v[58:59],v[58:59],v[66:67],v[66:67] op_sel:[0,0,1] op_sel_hi:[1,0,1]\n" \
"ds_write2_b32 v78,v82,v83 offset0:2 offset1:3\nv_cvt_pk_f16_f32 v84,v56,v57\nv_cvt_pk_f16_f32 v85,v58,v59\nv_pk_fma_f32 v[60:61],v[60:61],v[66:67],v[66:67] op_sel:[0,0,1] op_sel_hi:[1,0,1]\n" \
"v_pk_fma_f32 v[62:63],v[62:63],v[66:67],v[66:67] op_sel:[0,0,1] op_sel_hi:[1,0,1]\nds_write2_b32 v78,v84,v85 offset0:4 offset1:5\nv_cvt_pk_f16_f32 v86,v60,v61\nv_cvt_pk_f16_f32 v87,v62,v63\n" \
"v_pk_fma_f32 v[64:65],v[64:65],v[66:67],v[66:67] op_sel:[0,0,1] op_sel_hi:[1,0,1]\nds_write2_b32 v78,v86,v87 offset0:6 offset1:7\nv_cvt_pk_f16_f32 v88,v64,v65\nds_write_b32 v78,v88 offset:32\n" \
"s_waitcnt vmcnt(11)\ns_barrier\nds_read_u16 v16,v246\nds_read_u16 v17,v246 offset:5216\n" \
"ds_read_u16 v18,v246 offset:578\nds_read_u16 v19,v246 offset:5794\nds_read_u16 v20,v246 offset:1156\nds_read_u16 v21,v246 offset:6372\n" \
"ds_read_u16 v22,v246 offset:1734\nds_read_u16 v23,v246 offset:6950\ns_waitcnt vmcnt(8)\nv_lshrrev_b32 v96,1,v234\n" \
"ds_read_u16 v24,v247\nv_lshrrev_b32 v97,1,v235\nv_lshrrev_b32 v98,1,v236\ns_waitcnt lgkmcnt(7)\n" \
"v_lshl_or_b32 v16,v17,16,v16\nv_lshrrev_b32 v99,1,v237\nds_read_u16 v25,v247 offset:5216\ns_waitcnt vmcnt(7)\n" \
"v_lshrrev_b32 v100,1,v238\nv_lshrrev_b32 v101,1,v239\nv_lshrrev_b32 v102,1,v240\nv_lshrrev_b32 v103,1,v241\n" \
"ds_read_u16 v26,v247 offset:578\nv_add_u32 v96,s72,v96\nv_add_u32 v97,s72,v97\ns_waitcnt lgkmcnt(7)\n" \
"v_lshl_or_b32 v17,v19,16,v18\nv_add_u32 v98,s72,v98\nds_read_u16 v27,v247 offset:5794\nv_add_u32 v99,s72,v99\n" \
"v_add_u32 v100,s72,v100\nv_add_u32 v101,s72,v101\nv_add_u32 v102,s72,v102\nds_read_u16 v28,v247 offset:1156\n" \
"v_add_u32 v103,s72,v103\nv_pk_maximum3_f16 v124,v16,v17,v16\ns_waitcnt lgkmcnt(7)\nv_lshl_or_b32 v18,v21,16,v20\n" \
"v_pk_minimum3_f16 v125,v16,v17,v16\nds_read_u16 v29,v247 offset:6372\nds_read_u16 v30,v247 offset:1734\ns_waitcnt lgkmcnt(7)\n" \
"v_lshl_or_b32 v19,v23,16,v22\nbuffer_store_dwordx4 v[16:19],v2,s[36:39],s64 offen sc1\nds_read_u16 v31,v247 offset:6950\nds_read_u16 v104,v248\n" \
"v_pk_maximum3_f16 v124,v18,v19,v124\nv_pk_minimum3_f16 v125,v18,v19,v125\ns_waitcnt lgkmcnt(7)\nv_lshl_or_b32 v24,v25,16,v24\n" \
"ds_read_u16 v105,v248 offset:5216\nds_read_u16 v106,v248 offset:578\ns_waitcnt lgkmcnt(7)\nv_lshl_or_b32 v25,v27,16,v26\n" \
"v_pk_maximum3_f16 v124,v24,v25,v124\nds_read_u16 v107,v248 offset:5794\nv_pk_minimum3_f16 v125,v24,v25,v125\nds_read_u16 v108,v248 offset:1156\n" \
"s_waitcnt lgkmcnt(7)\nv_lshl_or_b32 v26,v29,16,v28\nds_read_u16 v109,v248 offset:6372\nds_read_u16 v110,v248 offset:1734\n" \
"s_waitcnt lgkmcnt(7)\nv_lshl_or_b32 v27,v31,16,v30\nbuffer_store_dwordx4 v[24:27],v2,s[36:39],s65 offen sc1\nds_read_u16 v111,v248 offset:6950\n" \
"ds_read_b128 v[116:119],v113 offset:20224\nv_pk_maximum3_f16 v124,v26,v27,v124\nv_pk_minimum3_f16 v125,v26,v27,v125\nds_read_b128 v[120:123],v113 offset:24320\n" \
"s_waitcnt lgkmcnt(8)\nv_lshl_or_b32 v104,v105,16,v104\nds_read_b32 v16,v96\nds_read_b32 v24,v96 offset:1160\n" \
"ds_read_b32 v17,v97\nds_read_b32 v25,v97 offset:1160\ns_waitcnt lgkmcnt(10)\nv_lshl_or_b32 v105,v107,16,v106\n" \
"ds_read_b32 v18,v98\nv_pk_maximum3_f16 v124,v104,v105,v124\nds_read_b32 v26,v98 offset:1160\nv_pk_minimum3_f16 v125,v104,v105,v125\n" \
"ds_read_b32 v19,v99\nds_read_b32 v27,v99 offset:1160\nds_read_b32 v20,v100\ns_waitcnt lgkmcnt(13)\n" \
"v_lshl_or_b32 v106,v109,16,v108\nds_read_b32 v28,v100 offset:1160\nds_read_b32 v21,v101\nds_read_b32 v29,v101 offset:1160\n" \
"s_waitcnt lgkmcnt(14)\nv_lshl_or_b32 v107,v111,16,v110\nbuffer_store_dwordx4 v[104:107],v114,s[36:39],s66 offen sc1\ns_waitcnt lgkmcnt(13)\n" \
"buffer_store_dwordx4 v[116:119],v1,s[36:39],s67 offen sc1\ns_waitcnt lgkmcnt(12)\nbuffer_store_dwordx4 v[120:123],v1,s[36:39],s67 offen offset:1024 sc1\nds_read_b32 v22,v102\n" \
"ds_read_b32 v30,v102 offset:1160\nds_read_b32 v23,v103\nds_read_b32 v31,v103 offset:1160\nv_pk_maximum3_f16 v124,v106,v107,v124\n" \
"v_and_b32 v124,0x7fff7fff,v124\nv_pk_minimum3_f16 v125,v106,v107,v125\nv_and_b32 v125,0x7fff7fff,v125\nv_pk_max_u16 v124,v124,v125\n" \
"v_lshlrev_b32 v125,16,v124\nv_max_u32 v124,v124,v125\ns_nop 1\nv_max_u32_dpp v124,v124,v124 quad_perm:[1,0,3,2] row_mask:0xf bank_mask:0xf bound_ctrl:1\n" \
"s_nop 1\nv_max_u32_dpp v124,v124,v124 quad_perm:[2,3,0,1] row_mask:0xf bank_mask:0xf bound_ctrl:1\ns_nop 1\nv_max_u32_dpp v124,v124,v124 row_half_mirror row_mask:0xf bank_mask:0xf bound_ctrl:1\n" \
"s_nop 1\nv_max_u32_dpp v124,v124,v124 row_mirror row_mask:0xf bank_mask:0xf bound_ctrl:1\ns_nop 0\nv_readlane_b32 s68,v124,0\n" \
"v_readlane_b32 s31,v124,16\nv_readlane_b32 s96,v124,32\nv_readlane_b32 s97,v124,48\ns_max_u32 s31,s31,s96\n" \
"s_max_u32 s68,s68,s97\ns_max_u32 s68,s68,s31\nv_mov_b32 v124,s68\nds_write_b32 v251,v124 offset:32\n" \
"v_mov_b32 v114,161664\nv_and_b32 v80,15,v0\nv_bfe_u32 v81,v0,4,2\nv_cvt_f32_u32 v80,v80\n" \
"v_cvt_f32_u32 v82,s3\nv_fma_f32 v80,v80,4.0,v82\nv_cvt_f32_u32 v81,v81\nbuffer_load_dwordx4 v[48:51],v2,s[36:39],s73 offen sc1\n" \
"v_mul_f32 v82,0xbc800000,v81\nv_cos_f32 v92,v82\nv_sin_f32 v93,v82\nv_mul_f32 v80,0xbc800000,v80\n" \
"v_mul_f32 v81,4.0,v81\nbuffer_load_dwordx4 v[60:63],v2,s[36:39],s75 offen sc1\ns_mov_b32 s70,0x3f7fb10f\ns_mov_b32 s71,0xbd48fb30\n" \
"v_mul_f32 v81,v81,v80\nv_pk_mul_f32 v[100:101],v[92:93],s[70:71] op_sel:[1,1] op_sel_hi:[0,1] neg_lo:[1,0]\nv_fmamk_f32 v87,v80,0x40000000,v81\nv_pk_fma_f32 v[94:95],v[92:93],s[70:71],v[100:101] op_sel_hi:[1,0,1]\n" \
"v_cos_f32 v86,v87\ns_mov_b32 s70,0x3f7fec43\ns_mov_b32 s71,0xbcc90ab0\nv_fmamk_f32 v89,v80,0x3f800000,v81\n" \
"v_fmamk_f32 v91,v80,0x40400000,v81\nv_pk_mul_f32 v[100:101],v[92:93],s[70:71] op_sel:[1,1] op_sel_hi:[0,1] neg_lo:[1,0]\nbuffer_load_dwordx4 v[52:55],v2,s[36:39],s74 offen sc1\nv_cos_f32 v88,v89\n" \
"v_cos_f32 v90,v91\nv_pk_fma_f32 v[96:97],v[92:93],s[70:71],v[100:101] op_sel_hi:[1,0,1]\nv_cos_f32 v84,v81\nv_sin_f32 v85,v81\n" \
"v_sin_f32 v87,v87\ns_mov_b32 s70,0x3f7f4e6d\ns_mov_b32 s71,0xbd96a905\nv_pk_mul_f32 v[100:101],v[92:93],s[70:71] op_sel:[1,1] op_sel_hi:[0,1] neg_lo:[1,0]\n" \
"v_sin_f32 v89,v89\nv_sin_f32_e64 v91,v91\nv_pk_fma_f32 v[98:99],v[92:93],s[70:71],v[100:101] op_sel_hi:[1,0,1]\nbuffer_load_dwordx4 v[64:67],v2,s[36:39],s76 offen sc1\n" \
"v_cvt_pk_f16_f32 v160,v84,-v85\nv_cvt_pk_f16_f32 v164,v85,v84\nv_cvt_pk_f16_f32 v161,v86,-v87\nv_cvt_pk_f16_f32 v165,v87,v86\n" \
"v_pk_mul_f32 v[100:101],v[84:85],v[92:93] op_sel:[1,1] op_sel_hi:[0,1] neg_lo:[1,0]\nv_pk_mul_f32 v[102:103],v[86:87],v[94:95] op_sel:[1,1] op_sel_hi:[0,1] neg_lo:[1,0]\nv_pk_fma_f32 v[84:85],v[84:85],v[92:93],v[100:101] op_sel_hi:[1,0,1]\nv_pk_fma_f32 v[86:87],v[86:87],v[94:95],v[102:103] op_sel_hi:[1,0,1]\n" \
"v_cvt_pk_f16_f32 v162,v88,-v89\nv_cvt_pk_f16_f32 v166,v89,v88\nv_cvt_pk_f16_f32 v163,v90,-v91\nv_cvt_pk_f16_f32 v167,v91,v90\n" \
"v_pk_mul_f32 v[100:101],v[88:89],v[96:97] op_sel:[1,1] op_sel_hi:[0,1] neg_lo:[1,0]\nv_pk_mul_f32 v[102:103],v[90:91],v[98:99] op_sel:[1,1] op_sel_hi:[0,1] neg_lo:[1,0]\nv_pk_fma_f32 v[88:89],v[88:89],v[96:97],v[100:101] op_sel_hi:[1,0,1]\nv_pk_fma_f32 v[90:91],v[90:91],v[98:99],v[102:103] op_sel_hi:[1,0,1]\n" \
"buffer_load_dwordx4 v[56:59],v1,s[36:39],s77 offen sc1\nv_cvt_pk_f16_f32 v168,v84,-v85\nv_cvt_pk_f16_f32 v172,v85,v84\nv_cvt_pk_f16_f32 v169,v86,-v87\n" \
"v_cvt_pk_f16_f32 v173,v87,v86\nv_pk_mul_f32 v[100:101],v[84:85],v[92:93] op_sel:[1,1] op_sel_hi:[0,1] neg_lo:[1,0]\nv_pk_mul_f32 v[102:103],v[86:87],v[94:95] op_sel:[1,1] op_sel_hi:[0,1] neg_lo:[1,0]\nv_pk_fma_f32 v[84:85],v[84:85],v[92:93],v[100:101] op_sel_hi:[1,0,1]\n" \
"v_pk_fma_f32 v[86:87],v[86:87],v[94:95],v[102:103] op_sel_hi:[1,0,1]\nv_cvt_pk_f16_f32 v170,v88,-v89\nv_cvt_pk_f16_f32 v174,v89,v88\nv_cvt_pk_f16_f32 v171,v90,-v91\n" \
"v_cvt_pk_f16_f32 v175,v91,v90\nv_pk_mul_f32 v[100:101],v[88:89],v[96:97] op_sel:[1,1] op_sel_hi:[0,1] neg_lo:[1,0]\nv_pk_mul_f32 v[102:103],v[90:91],v[98:99] op_sel:[1,1] op_sel_hi:[0,1] neg_lo:[1,0]\ns_waitcnt vmcnt(5) lgkmcnt(0)\n" \
"s_barrier\nds_read_b128 v[116:119],v114 offset:32\nv_pk_fma_f32 v[88:89],v[88:89],v[96:97],v[100:101] op_sel_hi:[1,0,1]\nv_pk_fma_f32 v[90:91],v[90:91],v[98:99],v[102:103] op_sel_hi:[1,0,1]\n" \
"buffer_load_dwordx4 v[68:71],v1,s[36:39],s78 offen sc1\nv_cvt_pk_f16_f32 v176,v84,-v85\nv_cvt_pk_f16_f32 v180,v85,v84\nv_cvt_pk_f16_f32 v177,v86,-v87\n" \
"v_cvt_pk_f16_f32 v181,v87,v86\nv_pk_mul_f32 v[100:101],v[84:85],v[92:93] op_sel:[1,1] op_sel_hi:[0,1] neg_lo:[1,0]\nv_pk_mul_f32 v[102:103],v[86:87],v[94:95] op_sel:[1,1] op_sel_hi:[0,1] neg_lo:[1,0]\ns_mov_b32 s79,0xffffff\n" \
"s_lshl3_add_u32 s80,s2,4294615040\nv_pk_fma_f32 v[84:85],v[84:85],v[92:93],v[100:101] op_sel_hi:[1,0,1]\nv_pk_fma_f32 v[86:87],v[86:87],v[94:95],v[102:103] op_sel_hi:[1,0,1]\nv_cvt_pk_f16_f32 v178,v88,-v89\n" \
"v_cvt_pk_f16_f32 v182,v89,v88\nv_cvt_pk_f16_f32 v179,v90,-v91\nv_cvt_pk_f16_f32 v183,v91,v90\nv_pk_mul_f32 v[100:101],v[88:89],v[96:97] op_sel:[1,1] op_sel_hi:[0,1] neg_lo:[1,0]\n" \
"v_pk_mul_f32 v[102:103],v[90:91],v[98:99] op_sel:[1,1] op_sel_hi:[0,1] neg_lo:[1,0]\nv_pk_fma_f32 v[88:89],v[88:89],v[96:97],v[100:101] op_sel_hi:[1,0,1]\ns_waitcnt lgkmcnt(0)\nv_max3_u32 v116,v116,v117,v118\n" \
"v_max_u32 v116,v116,v119\nv_and_b32 v119,0xf8,v0\nv_cmp_ne_u32 vcc,192,v119\nv_lshlrev_b32 v116,1,v116\n" \
"v_mov_b32 v117,s29\nv_lshlrev_b32 v118,11,v0\nv_bfi_b32 v117,s79,v117,v116\nv_add_u32 v118,s80,v118\n" \
"v_bfrev_b32 v119,1\nv_cndmask_b32 v118,v118,v119,vcc\nv_mov_b32_e64 v116,s28\nv_pk_fma_f32 v[90:91],v[90:91],v[98:99],v[102:103] op_sel_hi:[1,0,1]\n" \
"buffer_store_dwordx2 v[116:117],v118,s[36:39],0 offen sc1\nv_add_u32 v112,28416,v1\nv_cvt_pk_f16_f32 v184,v84,-v85\nv_cvt_pk_f16_f32 v188,v85,v84\n" \
"v_cvt_pk_f16_f32 v185,v86,-v87\nv_cvt_pk_f16_f32 v189,v87,v86\nv_cvt_pk_f16_f32 v186,v88,-v89\nv_cvt_pk_f16_f32 v190,v89,v88\n" \
"v_cvt_pk_f16_f32 v187,v90,-v91\nv_cvt_pk_f16_f32 v191,v91,v90\nds_read_b128 v[192:195],v112\nds_read_b128 v[196:199],v112 offset:1024\n" \
"ds_read_b128 v[200:203],v112 offset:2048\nds_read_b128 v[204:207],v112 offset:3072\nds_read_b128 v[208:211],v112 offset:4096\ns_lshl1_add_u32 s95,s33,s3\n" \
"s_add_u32 s95,s95,74\ns_cmp_lt_u32 s3,2\ns_cselect_b32 s95,s95,256\nds_read_b128 v[212:215],v112 offset:5120\n" \
"s_and_b32 s100,s95,31\ns_lshl_b32 s100,s100,3\nds_read_b128 v[32:35],v112 offset:6144\ns_cmp_lt_u32 s95,256\n" \
"s_cselect_b32 s95,s95,0x40000\nds_read_b128 v[36:39],v112 offset:7168\ns_lshr_b32 s95,s95,5\ns_add_u32 s95,s95,s100\n" \
"s_lshl_b32 s95,s95,6\ns_add_u32 s95,s95,s32\ns_mul_i32 s95,s95,1152\ns_mov_b32 m0,162176\n" \
"s_lshl_b32 s101,s33,7\nbuffer_load_dword v233,s[8:11],s95 offen lds\ns_sub_u32 s101,s101,1280\nv_add_u32 v111,s101,v0\n" \
"v_mul_u32_u24 v110,52429,v111\nv_lshrrev_b32 v110,18,v110\nv_lshlrev_b32 v111,7,v111\ns_mov_b32 s101,6272\n" \
"v_mad_u32_u24 v111,v110,s101,v111\ns_mul_i32 s31,s32,576\ns_add_u32 s31,s31,2304\ns_andn2_b32 s31,s31,127\n" \
"s_cmp_gt_u32 s3,1\ns_cselect_b32 s31,s31,2147483648\ns_mov_b32 s26,3981312\ns_mov_b32 s27,0x20000\n" \
"s_mov_b32 m0,162176\ns_nop 0\nbuffer_load_dword v111,s[24:27],s31 offen lds\ns_waitcnt vmcnt(8)\n" \
"v_cmp_ne_u64 vcc,s[28:29],v[48:49]\ns_waitcnt vmcnt(6)\nv_cmp_ne_u64 s[84:85],s[28:29],v[52:53]\ns_waitcnt vmcnt(4)\n" \
"v_cmp_ne_u64 s[82:83],s[28:29],v[56:57]\ns_or_b64 vcc,vcc,s[84:85]\nv_cmp_ne_u64 s[84:85],s[28:29],v[60:61]\ns_or_b64 vcc,vcc,s[82:83]\n" \
"v_cmp_ne_u64 s[82:83],s[28:29],v[64:65]\ns_or_b64 vcc,vcc,s[84:85]\ns_waitcnt vmcnt(3)\nv_cmp_ne_u64 s[84:85],s[28:29],v[68:69]\n" \
"s_or_b64 vcc,vcc,s[82:83]\ns_or_b64 vcc,vcc,s[84:85]\ns_cbranch_vccnz .LsmC_%=\n.LbmC_%=:\n" \
"v_pk_add_f32 v[50:51],v[50:51],v[62:63]\nv_pk_add_f32 v[54:55],v[54:55],v[66:67]\nv_pk_mul_f32 v[62:63],v[50:51],2.0 op_sel_hi:[1,0]\nv_pk_add_f32 v[58:59],v[58:59],v[70:71]\n" \
"v_max3_f32 v112,|v62|,|v63|,0\nv_pk_fma_f32 v[66:67],v[54:55],2.0,v[74:75] op_sel_hi:[1,0,0]\nv_max3_f32 v112,|v66|,|v67|,v112\nv_pk_fma_f32 v[70:71],v[58:59],2.0,2.0 op_sel_hi:[1,0,0]\n" \
"v_max3_f32 v112,|v70|,|v71|,v112\nv_cmp_lt_f32 s[82:83],s81,v112\nv_cmp_ngt_f32 vcc,0x41000000,v112\ns_cmp_lg_u64 s[82:83],0\n" \
"s_cselect_b32 s82,2,0\nv_cvt_pk_f16_f32 v62,v62,v63\ns_cmp_lg_u64 vcc,0\ns_addc_u32 s82,s82,0\n" \
"ds_write_b32 v252,v62 offset:62848\nv_cvt_pk_f16_f32 v66,v66,v67\nds_write_b32 v253,v66 offset:62848\nv_cvt_pk_f16_f32 v70,v70,v71\n" \
"v_mov_b32 v113,s82\nds_write_b32 v254,v70 offset:62848\nds_write_b32 v251,v113\ns_waitcnt lgkmcnt(0)\n" \
"s_barrier\nds_read_b128 v[124:127],v114\nds_read_b64 v[96:97],v234 offset:62848\nds_read_b64 v[98:99],v235 offset:62848\n" \
"ds_read_b64 v[100:101],v236 offset:62848\nds_read_b64 v[102:103],v237 offset:62848\nds_read_b64 v[104:105],v238 offset:62848\nds_read_b64 v[106:107],v239 offset:62848\n" \
"ds_read_b64 v[108:109],v240 offset:62848\nds_read_b64 v[110:111],v241 offset:62848\ns_waitcnt lgkmcnt(8)\nv_or3_b32 v124,v124,v125,v126\n" \
"v_or_b32 v124,v124,v127\nv_cmp_ne_u32 vcc,2,v124\ns_nop 0\ns_cbranch_vccnz .LsxC_%=\n" \
".LbxC_%=:\ns_waitcnt lgkmcnt(7)\nv_pk_fma_f16 v80,v16,v96,v97\ns_waitcnt lgkmcnt(6)\n" \
"v_pk_fma_f16 v81,v17,v98,v99\ns_waitcnt lgkmcnt(5)\nv_pk_fma_f16 v82,v18,v100,v101\ns_waitcnt lgkmcnt(4)\n" \
"v_pk_fma_f16 v83,v19,v102,v103\nv_pk_fma_f16 v88,v24,v96,v97\ns_waitcnt lgkmcnt(3)\nv_pk_fma_f16 v84,v20,v104,v105\n" \
"s_waitcnt lgkmcnt(2)\nv_pk_fma_f16 v85,v21,v106,v107\nv_pk_fma_f16 v89,v25,v98,v99\ns_waitcnt lgkmcnt(1)\n" \
"v_pk_fma_f16 v86,v22,v108,v109\nv_pk_fma_f16 v90,v26,v100,v101\ns_waitcnt lgkmcnt(0)\nv_pk_fma_f16 v87,v23,v110,v111\n" \
"v_pk_fma_f16 v91,v27,v102,v103\nv_pk_fma_f16 v92,v28,v104,v105\nv_pk_fma_f16 v93,v29,v106,v107\nv_pk_fma_f16 v94,v30,v108,v109\n" \
"s_add_u32 s96,s92,0\ns_add_u32 m0,s57,0\ns_add_u32 s97,s92,9437184\nbuffer_load_dwordx4 v4,s[8:11],s96 offen lds\n" \
"v_pk_fma_f16 v95,v31,v110,v111\nv_mfma_f32_16x16x32_f16 v[48:51],v[80:83],v[192:195],0\nv_and_b32 v112,15,v0\nv_bfrev_b32 v113,1\n" \
"v_mfma_f32_16x16x32_f16 v[52:55],v[80:83],v[196:199],0\nv_cmp_eq_u32 vcc,0,v112\ns_add_u32 s73,s35,s88\nbuffer_load_dwordx4 v5,s[8:11],s96 offen offset:1024 lds\n" \
"v_mfma_f32_16x16x32_f16 v[56:59],v[84:87],v[32:35],0\nv_cndmask_b32 v113,v113,v3,vcc\nv_cmp_gt_u32 vcc,64,v0\nv_mfma_f32_16x16x32_f16 v[60:63],v[84:87],v[36:39],0\n" \
"s_add_u32 s75,s73,8192\nv_cndmask_b32 v113,v113,v3,vcc\nv_mfma_f32_16x16x32_f16 v[64:67],v[88:91],v[192:195],0\nbuffer_load_dwordx4 v6,s[8:11],s96 offen offset:2048 lds\n" \
"v_mfma_f32_16x16x32_f16 v[68:71],v[88:91],v[196:199],0\nv_pk_add_f32 v[56:57],v[48:49],v[56:57] neg_lo:[0,1] neg_hi:[0,1]\nv_sub_f32 v58,v50,v58\nv_mfma_f32_16x16x32_f16 v[72:75],v[92:95],v[32:35],0\n" \
"v_pk_add_f32 v[60:61],v[52:53],v[60:61] neg_lo:[0,1] neg_hi:[0,1]\nv_sub_f32 v62,v54,v62\nv_mfma_f32_16x16x32_f16 v[76:79],v[92:95],v[36:39],0\nbuffer_load_dwordx4 v7,s[8:11],s96 offen offset:3072 lds\n" \
"v_pk_fma_f32 v[48:49],v[48:49],2.0,v[56:57] op_sel_hi:[1,0,1] neg_lo:[0,0,1] neg_hi:[0,0,1]\nv_fma_f32 v50,v50,2.0,-v58\nv_pk_fma_f32 v[52:53],v[52:53],2.0,v[60:61] op_sel_hi:[1,0,1] neg_lo:[0,0,1] neg_hi:[0,0,1]\nv_fma_f32 v54,v54,2.0,-v62\n" \
"v_pk_add_f32 v[72:73],v[64:65],v[72:73] neg_lo:[0,1] neg_hi:[0,1]\nv_sub_f32_e64 v74,v66,v74\nv_pk_fma_f32 v[64:65],v[64:65],2.0,v[72:73] op_sel_hi:[1,0,1] neg_lo:[0,0,1] neg_hi:[0,0,1]\nv_pk_add_f32 v[76:77],v[68:69],v[76:77] neg_lo:[0,1] neg_hi:[0,1]\n" \
"s_add_u32 m0,s57,4096\ns_add_u32 s95,s92,73728\nbuffer_load_dwordx4 v4,s[8:11],s97 offen lds\nv_sub_f32_e64 v78,v70,v78\n" \
"v_fma_f32 v66,v66,2.0,-v74\nv_pk_fma_f32 v[68:69],v[68:69],2.0,v[76:77] op_sel_hi:[1,0,1] neg_lo:[0,0,1] neg_hi:[0,0,1]\nv_fma_f32 v70,v70,2.0,-v78\nv_cvt_pk_f16_f32 v16,v48,v52\n" \
"v_cvt_pk_f16_f32 v17,v49,v53\nv_cvt_pk_f16_f32 v18,v50,v54\nv_cvt_pk_f16_f32 v24,v64,v68\nbuffer_load_dwordx4 v5,s[8:11],s97 offen offset:1024 lds\n" \
"v_cvt_pk_f16_f32 v25,v65,v69\nv_cvt_pk_f16_f32 v26,v66,v70\nv_mfma_f32_16x16x32_f16 v[48:51],v[200:203],v[16:19],0\nv_cvt_pk_f16_f32 v20,v56,v60\n" \
"v_cvt_pk_f16_f32 v21,v57,v61\nv_mfma_f32_16x16x32_f16 v[52:55],v[204:207],v[16:19],0\nbuffer_load_dwordx4 v6,s[8:11],s97 offen offset:2048 lds\nv_mfma_f32_16x16x32_f16 v[64:67],v[200:203],v[24:27],0\n" \
"v_cvt_pk_f16_f32 v22,v58,v62\nv_cvt_pk_f16_f32 v28,v72,v76\nv_mfma_f32_16x16x32_f16 v[68:71],v[204:207],v[24:27],0\nv_cvt_pk_f16_f32 v29,v73,v77\n" \
"v_cvt_pk_f16_f32 v30,v74,v78\nbuffer_load_dwordx4 v7,s[8:11],s97 offen offset:3072 lds\nv_mfma_f32_16x16x32_f16 v[56:59],v[200:203],v[20:23],0\nv_cvt_pk_f16_f32 v80,v48,v52\n" \
"v_cvt_pk_f16_f32 v82,v49,v53\nv_mfma_f32_16x16x32_f16 v[60:63],v[204:207],v[20:23],0\nv_cvt_pk_f16_f32 v84,v50,v54\nv_cvt_pk_f16_f32 v81,v64,v68\n" \
"s_add_u32 m0,s57,8192\ns_add_u32 s100,s92,9510912\nbuffer_load_dwordx4 v4,s[8:11],s95 offen lds\nv_mfma_f32_16x16x32_f16 v[72:75],v[200:203],v[28:31],0\n" \
"ds_write_b64 v255,v[80:81]\nv_mfma_f32_16x16x32_f16 v[76:79],v[204:207],v[28:31],0\nv_cvt_pk_f16_f32 v83,v65,v69\nds_write_b64 v255,v[82:83] offset:512\n" \
"buffer_load_dwordx4 v5,s[8:11],s95 offen offset:1024 lds\nv_cvt_pk_f16_f32 v85,v66,v70\nds_write_b64 v255,v[84:85] offset:1024\nv_cvt_pk_f16_f32 v86,v56,v60\n" \
"v_cvt_pk_f16_f32 v88,v57,v61\nv_cvt_pk_f16_f32 v90,v58,v62\nv_cvt_pk_f16_f32 v87,v72,v76\nbuffer_load_dwordx4 v6,s[8:11],s95 offen offset:2048 lds\n" \
"ds_write_b64 v255,v[86:87] offset:256\nv_cvt_pk_f16_f32 v89,v73,v77\nds_write_b64 v255,v[88:89] offset:768\nv_cvt_pk_f16_f32 v91,v74,v78\n" \
"ds_write_b64 v255,v[90:91] offset:1280\nbuffer_load_dwordx4 v7,s[8:11],s95 offen offset:3072 lds\ns_cmp_lt_u32 s3,2\ns_cselect_b32 s75,s75,2147483648\n" \
"s_add_u32 s74,s73,4096\ns_add_u32 m0,s57,12288\ns_add_u32 s101,s92,147456\ns_mul_i32 s76,s32,18\n" \
"s_add_u32 s76,s76,s33\ns_sub_u32 s76,s76,14\ns_cmp_lt_u32 s33,14\ns_cselect_b32 s76,144,s76\n" \
"buffer_load_dwordx4 v4,s[8:11],s100 offen lds\ns_waitcnt lgkmcnt(0)\ns_barrier\nds_read_b128 v[16:19],v243\n" \
"ds_read_b128 v[20:23],v244\nds_read_b128 v[24:27],v245\nv_lshrrev_b32 v124,2,v0\nv_mul_u32_u24 v124,9344,v124\n" \
"v_and_b32 v125,48,v2\nbuffer_load_dwordx4 v5,s[8:11],s100 offen offset:1024 lds\nv_add_u32 v124,v124,v125\nv_xor_b32 v125,32,v124\n" \
"s_lshl_b32 s77,s76,6\ns_add_u32 s66,s77,2916352\ns_add_u32 s67,s77,3514368\ns_add_u32 s68,s77,4112384\n" \
"s_add_u32 s69,s77,4710400\nbuffer_load_dwordx4 v6,s[8:11],s100 offen offset:2048 lds\ns_waitcnt lgkmcnt(2)\nv_mfma_f32_16x16x32_f16 v[48:51],v[40:43],v[16:19],0\n" \
"v_mfma_f32_16x16x32_f16 v[52:55],v[44:47],v[16:19],0\ns_waitcnt lgkmcnt(1)\nv_mfma_f32_16x16x32_f16 v[56:59],v[40:43],v[20:23],0\nbuffer_load_dwordx4 v7,s[8:11],s100 offen offset:3072 lds\n" \
"buffer_load_dwordx4 v[104:107],v1,s[36:39],s7 offen sc1\nbuffer_load_dwordx4 v[108:111],v1,s[36:39],s7 offen offset:1024 sc1\ns_add_u32 m0,s57,16384\nv_mfma_f32_16x16x32_f16 v[60:63],v[44:47],v[20:23],0\n" \
"s_waitcnt lgkmcnt(0)\nv_mfma_f32_16x16x32_f16 v[64:67],v[40:43],v[24:27],0\nv_cvt_pk_f16_f32 v80,v48,v52\nv_mfma_f32_16x16x32_f16 v[68:71],v[44:47],v[24:27],0\n" \
"v_cvt_pk_f16_f32 v81,v49,v53\nv_cvt_pk_f16_f32 v82,v50,v54\nbuffer_load_dwordx4 v4,s[8:11],s101 offen lds\nv_cvt_pk_f16_f32 v83,v51,v55\n" \
"v_and_b32 v80,-2,v80\nv_cvt_pk_f16_f32 v84,v56,v60\nv_cvt_pk_f16_f32 v85,v57,v61\nv_cvt_pk_f16_f32 v86,v58,v62\n" \
"v_cvt_pk_f16_f32 v88,v64,v68\nv_cvt_pk_f16_f32 v87,v59,v63\nv_cvt_pk_f16_f32 v89,v65,v69\nbuffer_load_dwordx4 v5,s[8:11],s101 offen offset:1024 lds\n" \
"v_cvt_pk_f16_f32 v90,v66,v70\nv_cvt_pk_f16_f32 v91,v67,v71\nv_and_b32 v84,-2,v84\nv_and_b32 v88,-2,v88\n" \
"buffer_load_dwordx4 v6,s[8:11],s101 offen offset:2048 lds\ns_waitcnt vmcnt(4)\nv_xor_b32 v116,s29,v105\nv_xor_b32 v117,s29,v107\n" \
"buffer_load_dwordx4 v7,s[8:11],s101 offen offset:3072 lds\ns_waitcnt vmcnt(4)\nv_xor_b32 v118,s29,v109\nv_or3_b32 v116,v116,v117,v118\n" \
"v_xor_b32 v119,s29,v111\nv_xor_b32 v117,s28,v104\nv_or_b32 v116,v116,v119\nv_lshl_or_b32 v116,v116,8,v117\n" \
"v_xor_b32 v118,s28,v106\nv_xor_b32 v119,s28,v108\nv_or3_b32 v116,v116,v118,v119\nv_xor_b32 v117,s28,v110\n" \
"v_or_b32 v116,v116,v117\nv_cmp_ne_u32 vcc,0,v116\ns_nop 0\ns_cbranch_vccnz .Lsw_%=\n" \
"buffer_store_dwordx4 v[80:83],v3,s[36:39],s73 offen sc1\nbuffer_store_dwordx4 v[84:87],v3,s[36:39],s74 offen sc1\nbuffer_store_dwordx4 v[88:91],v113,s[36:39],s75 offen sc1\ns_add_u32 m0,s88,16384\n" \
"s_nop 0\nbuffer_load_dwordx4 v124,s[36:39],s66 offen sc1 lds\ns_add_u32 m0,s88,20480\ns_nop 0\n" \
"buffer_load_dwordx4 v125,s[36:39],s67 offen sc1 lds\ns_add_u32 m0,s88,24576\ns_nop 0\nbuffer_load_dwordx4 v124,s[36:39],s68 offen sc1 lds\n" \
"s_add_u32 m0,s88,28672\ns_nop 0\nbuffer_load_dwordx4 v125,s[36:39],s69 offen sc1 lds\n.Lbw_%=:\n" \
"s_cmp_lt_u32 s3,2\ns_cselect_b32 s18,s75,s73\ns_mov_b32 s12,s88\ns_mov_b32 s13,s89\n" \
"s_mov_b32 s14,s87\ns_mov_b32 s16,s73\ns_mov_b32 s17,s74\nv_bfe_u32 v86,v0,4,2\n" \
"v_and_b32 v88,1,v86\nv_bfe_u32 v85,v0,3,1\nv_and_b32 v84,7,v0\nv_lshlrev_b32 v88,3,v88\n" \
"v_lshl_add_u32 v87,v86,6,v85\ns_lshl_b32 s84,s3,2\nv_xor_b32 v89,v84,v88\nv_add_u32 v87,s84,v87\n" \
"v_lshlrev_b32_e64 v89,2,v89\nv_lshl_add_u32 v80,v87,6,v89\nv_and_b32 v87,0xf0,v1\nv_add_u32 v80,16384,v80\n" \
"v_lshl_add_u32 v246,v86,12,v87\nv_sub_u32 v82,46076,v80\nv_lshl_add_u32 v87,v84,1,v85\nv_xor_b32 v82,32,v82\n" \
"v_lshlrev_b32 v87,4,v87\nv_add_u32 v81,128,v80\nv_subrev_u32 v83,128,v82\nv_lshl_add_u32 v87,v85,10,v87\n" \
"v_lshl_add_u32 v87,v86,8,v87\ns_lshl_b32 s86,s3,12\nv_add_u32 v252,s86,v87\nv_lshlrev_b32 v87,3,v85\n" \
"v_lshl_add_u32 v87,v84,4,v87\ns_lshr_b32 s84,s76,5\nv_lshl_add_u32 v87,v86,12,v87\ns_lshl_b32 s85,s3,8\n" \
"s_add_u32 s84,s84,1\nv_add_u32 v246,s85,v246\nv_add_u32 v248,s85,v87\ns_cmp_lt_u32 s76,128\n" \
"s_cselect_b32 s84,s84,0\nv_max3_u32 v126,v105,v107,v109\ns_and_b32 s85,s76,31\nv_max_u32 v126,v126,v111\n" \
"s_lshl_b32 s84,s84,6\ns_mul_i32 s85,s85,225\nv_max_u32_dpp v126,v126,v126 quad_perm:[1,0,3,2] row_mask:0xf bank_mask:0xf bound_ctrl:1\ns_add_u32 s84,s84,s85\n" \
"s_mul_i32 s85,s84,58255\nv_max_u32_dpp v126,v126,v126 quad_perm:[2,3,0,1] row_mask:0xf bank_mask:0xf bound_ctrl:1\ns_lshr_b32 s85,s85,24\ns_mul_i32 s85,s85,288\n" \
"v_max_u32_dpp v126,v126,v126 row_half_mirror row_mask:0xf bank_mask:0xf bound_ctrl:1\ns_sub_u32 s84,s84,s85\nv_cvt_f32_u32 v92,s84\nv_max_u32_dpp v126,v126,v126 row_mirror row_mask:0xf bank_mask:0xf bound_ctrl:1\n" \
"v_mul_f32 v92,0x3ae38e39,v92\nv_readlane_b32 s94,v126,0\nv_readlane_b32 s31,v126,16\nv_readlane_b32 s96,v126,32\n" \
"v_readlane_b32 s97,v126,48\ns_max_u32 s31,s31,s96\ns_max_u32 s94,s94,s97\ns_max_u32 s94,s94,s31\n" \
"v_cos_f32 v90,v92\ns_lshr_b32 s94,s94,24\nv_bfe_u32 v87,v0,1,3\nv_and_b32 v88,1,v0\n" \
"s_max_u32 s94,s94,64\nv_lshl_add_u32 v87,v88,3,v87\ns_min_u32 s94,s94,250\nv_lshl_add_u32 v87,v86,4,v87\n" \
"s_sub_u32 s85,251,s94\nv_sin_f32 v92,v92\nv_lshlrev_b32 v87,4,v87\ns_cmp_eq_u32 s76,128\n" \
"s_subb_u32 s85,s85,0\nv_bfe_u32 v120,v0,4,2\nv_add_u32 v254,s86,v87\ns_mul_i32 s80,s3,37376\n" \
"v_mul_u32_u24 v120,598016,v120\nv_and_b32 v121,15,v0\nv_and_b32 v87,15,v0\ns_lshl_b32 s85,s85,23\n" \
"s_add_u32 s80,s80,s77\nv_lshl_add_u32 v120,v121,2,v120\nv_mul_u32_u24 v87,149504,v87\nv_fma_f32 v94,v92,s85,s85\n" \
"s_mov_b32 s79,0x34003400\ns_add_u32 s80,s80,524288\nv_add_u32 v121,149504,v120\nv_add_u32 v122,299008,v120\n" \
"v_add_u32 v123,448512,v120\nv_lshl_add_u32 v255,v86,4,v87\nv_pk_mul_f16 v112,v192,s79\nv_pk_mul_f16 v113,v193,s79\n" \
"v_pk_mul_f16 v114,v194,s79\nv_pk_mul_f16 v115,v195,s79\nv_pk_mul_f16 v116,v196,s79\nv_pk_mul_f16 v117,v197,s79\n" \
"v_pk_mul_f16 v118,v198,s79\nv_pk_mul_f16 v119,v199,s79\nv_fma_f32 v92,-v92,s85,s85\ns_add_u32 s81,s80,9344\n" \
"s_add_u32 s82,s80,18688\ns_add_u32 s83,s80,28032\nv_xor_b32 v247,16,v246\nv_xor_b32 v253,16,v252\n" \
"v_mul_f32 v90,s85,v90\nds_read_b64_tr_b16 a[0:1],v8\nds_read_b64_tr_b16 a[4:5],v9\nds_read_b64_tr_b16 a[8:9],v10\n" \
"ds_read_b64_tr_b16 a[12:13],v11\nds_read_b64_tr_b16 a[16:17],v8 offset:2048\nds_read_b64_tr_b16 a[20:21],v9 offset:2048\nds_read_b64_tr_b16 a[24:25],v10 offset:2048\n" \
"ds_read_b64_tr_b16 a[28:29],v11 offset:2048\nds_read_b64_tr_b16 a[2:3],v8 offset:4096\nds_read_b64_tr_b16 a[6:7],v9 offset:4096\nds_read_b64_tr_b16 a[10:11],v10 offset:4096\n" \
"ds_read_b64_tr_b16 a[14:15],v11 offset:4096\nds_read_b64_tr_b16 a[18:19],v8 offset:6144\nds_read_b64_tr_b16 a[22:23],v9 offset:6144\nds_read_b64_tr_b16 a[26:27],v10 offset:6144\n" \
"ds_read_b64_tr_b16 a[30:31],v11 offset:6144\nds_read_b64_tr_b16 a[32:33],v8 offset:8192\nds_read_b64_tr_b16 a[36:37],v9 offset:8192\nds_read_b64_tr_b16 a[40:41],v10 offset:8192\n" \
"ds_read_b64_tr_b16 a[44:45],v11 offset:8192\nds_read_b64_tr_b16 a[48:49],v8 offset:10240\nds_read_b64_tr_b16 a[52:53],v9 offset:10240\nds_read_b64_tr_b16 a[56:57],v10 offset:10240\n" \
"ds_read_b64_tr_b16 a[60:61],v11 offset:10240\nds_read_b64_tr_b16 a[34:35],v8 offset:12288\nds_read_b64_tr_b16 a[38:39],v9 offset:12288\nds_read_b64_tr_b16 a[42:43],v10 offset:12288\n" \
"ds_read_b64_tr_b16 a[46:47],v11 offset:12288\nds_read_b64_tr_b16 a[50:51],v8 offset:14336\nds_read_b64_tr_b16 a[54:55],v9 offset:14336\nds_read_b64_tr_b16 a[58:59],v10 offset:14336\n" \
"ds_read_b64_tr_b16 a[62:63],v11 offset:14336\ns_waitcnt vmcnt(0)\ns_barrier\nds_read2st64_b32 v[64:65],v80 offset0:0 offset1:4\n" \
"ds_read2st64_b32 v[72:73],v82 offset0:12 offset1:8\nds_read2st64_b32 v[66:67],v80 offset0:8 offset1:12\nds_read2st64_b32 v[74:75],v82 offset0:4 offset1:0\nds_read2st64_b32 v[68:69],v81 offset0:0 offset1:4\n" \
"ds_read2st64_b32 v[76:77],v83 offset0:12 offset1:8\nds_read2st64_b32 v[70:71],v81 offset0:8 offset1:12\nds_read2st64_b32 v[78:79],v83 offset0:4 offset1:0\ns_waitcnt lgkmcnt(7)\n" \
"v_and_b32 v20,0xffff0000,v64\nv_and_b32 v21,0xffff0000,v65\nv_lshlrev_b32 v16,16,v64\nv_lshlrev_b32 v17,16,v65\n" \
"s_waitcnt lgkmcnt(6)\nv_and_b32 v36,0xffff0000,v72\nv_and_b32 v37,0xffff0000,v73\nv_lshlrev_b32 v32,16,v72\n" \
"s_waitcnt lgkmcnt(5)\nv_and_b32 v22,0xffff0000,v66\nv_and_b32 v23,0xffff0000,v67\nv_lshlrev_b32 v33,16,v73\n" \
"v_lshlrev_b32 v18,16,v66\ns_waitcnt lgkmcnt(4)\nv_and_b32 v38,0xffff0000,v74\nv_and_b32 v39,0xffff0000,v75\n" \
"v_lshlrev_b32 v34,16,v74\ns_waitcnt lgkmcnt(3)\nv_and_b32 v44,0xffff0000,v68\nv_and_b32 v45,0xffff0000,v69\n" \
"v_lshlrev_b32 v19,16,v67\ns_waitcnt lgkmcnt(2)\nv_and_b32 v60,0xffff0000,v76\nv_and_b32 v61,0xffff0000,v77\n" \
"v_lshlrev_b32 v35,16,v75\nv_lshlrev_b32 v40,16,v68\ns_waitcnt lgkmcnt(1)\nv_and_b32 v46,0xffff0000,v70\n" \
"v_and_b32 v47,0xffff0000,v71\nv_lshlrev_b32 v56,16,v76\ns_waitcnt lgkmcnt(0)\nv_and_b32 v62,0xffff0000,v78\n" \
"v_and_b32 v63,0xffff0000,v79\nv_lshlrev_b32 v41,16,v69\nv_lshlrev_b32 v57,16,v77\nv_lshlrev_b32 v42,16,v70\n" \
"v_lshlrev_b32 v58,16,v78\nv_lshlrev_b32 v43,16,v71\nv_lshlrev_b32 v59,16,v79\ns_cmp_eq_u32 s76,128\n" \
"s_cbranch_scc1 .Lsz_%=\n.Lbz_%=:\nv_pk_mul_f32 v[24:25],v[16:17],v[94:95] op_sel_hi:[1,0]\nv_pk_mul_f32 v[28:29],v[20:21],v[94:95] op_sel_hi:[1,0]\n" \
"v_pk_add_f32 v[64:65],v[20:21],v[36:37]\nv_pk_add_f32 v[66:67],v[16:17],v[32:33] neg_lo:[0,1] neg_hi:[0,1]\nv_pk_mul_f32 v[26:27],v[18:19],v[94:95] op_sel_hi:[1,0]\nv_pk_mul_f32 v[30:31],v[22:23],v[94:95] op_sel_hi:[1,0]\n" \
"v_pk_add_f32 v[68:69],v[22:23],v[38:39]\nv_pk_add_f32 v[70:71],v[18:19],v[34:35] neg_lo:[0,1] neg_hi:[0,1]\nv_pk_mul_f32 v[48:49],v[40:41],v[94:95] op_sel_hi:[1,0]\nv_pk_mul_f32 v[52:53],v[44:45],v[94:95] op_sel_hi:[1,0]\n" \
"v_pk_add_f32 v[72:73],v[44:45],v[60:61]\nv_pk_add_f32 v[74:75],v[40:41],v[56:57] neg_lo:[0,1] neg_hi:[0,1]\nv_pk_mul_f32 v[50:51],v[42:43],v[94:95] op_sel_hi:[1,0]\nv_pk_mul_f32 v[54:55],v[46:47],v[94:95] op_sel_hi:[1,0]\n" \
"v_pk_add_f32 v[76:77],v[46:47],v[62:63]\nv_pk_add_f32 v[78:79],v[42:43],v[58:59] neg_lo:[0,1] neg_hi:[0,1]\nv_pk_mul_f32 v[16:17],v[16:17],v[92:93] op_sel_hi:[1,0]\nv_pk_mul_f32 v[20:21],v[20:21],v[92:93] op_sel_hi:[1,0]\n" \
"v_pk_mul_f32 v[18:19],v[18:19],v[92:93] op_sel_hi:[1,0]\nv_pk_mul_f32 v[22:23],v[22:23],v[92:93] op_sel_hi:[1,0]\nbuffer_load_dword v96,v120,s[36:39],s80 offen sc1\nbuffer_load_dword v97,v121,s[36:39],s80 offen sc1\n" \
"buffer_load_dword v98,v122,s[36:39],s80 offen sc1\nbuffer_load_dword v99,v123,s[36:39],s80 offen sc1\nbuffer_load_dword v100,v120,s[36:39],s81 offen sc1\nbuffer_load_dword v101,v121,s[36:39],s81 offen sc1\n" \
"buffer_load_dword v102,v122,s[36:39],s81 offen sc1\nbuffer_load_dword v103,v123,s[36:39],s81 offen sc1\nbuffer_load_dword v104,v120,s[36:39],s82 offen sc1\nbuffer_load_dword v105,v121,s[36:39],s82 offen sc1\n" \
"buffer_load_dword v106,v122,s[36:39],s82 offen sc1\nbuffer_load_dword v107,v123,s[36:39],s82 offen sc1\nbuffer_load_dword v108,v120,s[36:39],s83 offen sc1\nbuffer_load_dword v109,v121,s[36:39],s83 offen sc1\n" \
"buffer_load_dword v110,v122,s[36:39],s83 offen sc1\nbuffer_load_dword v111,v123,s[36:39],s83 offen sc1\nv_pk_mul_f32 v[40:41],v[40:41],v[92:93] op_sel_hi:[1,0]\nv_pk_mul_f32 v[44:45],v[44:45],v[92:93] op_sel_hi:[1,0]\n" \
"v_pk_mul_f32 v[42:43],v[42:43],v[92:93] op_sel_hi:[1,0]\nv_pk_mul_f32 v[46:47],v[46:47],v[92:93] op_sel_hi:[1,0]\nv_pk_fma_f32 v[24:25],v[32:33],v[92:93],v[24:25] op_sel_hi:[1,0,1]\nv_pk_fma_f32 v[28:29],v[36:37],v[92:93],v[28:29] op_sel_hi:[1,0,1] neg_lo:[1,0,0] neg_hi:[1,0,0]\n" \
"v_pk_fma_f32 v[16:17],v[32:33],v[94:95],v[16:17] op_sel_hi:[1,0,1]\nv_pk_fma_f32 v[20:21],v[36:37],v[94:95],v[20:21] op_sel_hi:[1,0,1] neg_lo:[1,0,0] neg_hi:[1,0,0]\nv_pk_fma_f32 v[26:27],v[34:35],v[92:93],v[26:27] op_sel_hi:[1,0,1]\nv_pk_fma_f32 v[30:31],v[38:39],v[92:93],v[30:31] op_sel_hi:[1,0,1] neg_lo:[1,0,0] neg_hi:[1,0,0]\n" \
"v_pk_fma_f32 v[18:19],v[34:35],v[94:95],v[18:19] op_sel_hi:[1,0,1]\nv_pk_fma_f32 v[22:23],v[38:39],v[94:95],v[22:23] op_sel_hi:[1,0,1] neg_lo:[1,0,0] neg_hi:[1,0,0]\nv_pk_fma_f32 v[48:49],v[56:57],v[92:93],v[48:49] op_sel_hi:[1,0,1]\nv_pk_fma_f32 v[52:53],v[60:61],v[92:93],v[52:53] op_sel_hi:[1,0,1] neg_lo:[1,0,0] neg_hi:[1,0,0]\n" \
"v_pk_fma_f32 v[40:41],v[56:57],v[94:95],v[40:41] op_sel_hi:[1,0,1]\nv_pk_fma_f32 v[44:45],v[60:61],v[94:95],v[44:45] op_sel_hi:[1,0,1] neg_lo:[1,0,0] neg_hi:[1,0,0]\nv_pk_fma_f32 v[50:51],v[58:59],v[92:93],v[50:51] op_sel_hi:[1,0,1]\nv_pk_fma_f32 v[54:55],v[62:63],v[92:93],v[54:55] op_sel_hi:[1,0,1] neg_lo:[1,0,0] neg_hi:[1,0,0]\n" \
"v_pk_fma_f32 v[42:43],v[58:59],v[94:95],v[42:43] op_sel_hi:[1,0,1]\nv_pk_fma_f32 v[46:47],v[62:63],v[94:95],v[46:47] op_sel_hi:[1,0,1] neg_lo:[1,0,0] neg_hi:[1,0,0]\nv_pk_mul_f32 v[32:33],v[64:65],v[90:91] op_sel_hi:[1,0] neg_lo:[1,0] neg_hi:[1,0]\nv_pk_mul_f32 v[36:37],v[66:67],v[90:91] op_sel_hi:[1,0]\n" \
"v_pk_mul_f32 v[34:35],v[68:69],v[90:91] op_sel_hi:[1,0] neg_lo:[1,0] neg_hi:[1,0]\nv_pk_mul_f32 v[38:39],v[70:71],v[90:91] op_sel_hi:[1,0]\nv_pk_mul_f32 v[56:57],v[72:73],v[90:91] op_sel_hi:[1,0] neg_lo:[1,0] neg_hi:[1,0]\nv_pk_mul_f32 v[60:61],v[74:75],v[90:91] op_sel_hi:[1,0]\n" \
"v_pk_mul_f32 v[58:59],v[76:77],v[90:91] op_sel_hi:[1,0] neg_lo:[1,0] neg_hi:[1,0]\nv_pk_mul_f32 v[62:63],v[78:79],v[90:91] op_sel_hi:[1,0]\ns_mov_b32 s78,0x11111111\ns_mov_b32 s79,0x11111111\n" \
"ds_read_b64_tr_b16 a[64:65],v8 offset:16384\nds_read_b64_tr_b16 a[68:69],v9 offset:16384\nds_read_b64_tr_b16 a[72:73],v10 offset:16384\nds_read_b64_tr_b16 a[76:77],v11 offset:16384\n" \
"ds_read_b64_tr_b16 a[80:81],v8 offset:18432\nds_read_b64_tr_b16 a[84:85],v9 offset:18432\nds_read_b64_tr_b16 a[88:89],v10 offset:18432\nds_read_b64_tr_b16 a[92:93],v11 offset:18432\n" \
"s_waitcnt vmcnt(13)\nv_or3_b32 v124,v96,v97,v98\ns_waitcnt vmcnt(11)\nv_or3_b32 v124,v124,v99,v100\n" \
"s_waitcnt vmcnt(9)\nv_or3_b32 v124,v124,v101,v102\ns_waitcnt vmcnt(7)\nv_or3_b32 v124,v124,v103,v104\n" \
"s_waitcnt vmcnt(5)\nv_or3_b32 v124,v124,v105,v106\ns_waitcnt vmcnt(3)\nv_or3_b32 v124,v124,v107,v108\n" \
"s_waitcnt vmcnt(1)\nv_or3_b32 v124,v124,v109,v110\ns_waitcnt vmcnt(0)\nv_or_b32 v124,v124,v111\n" \
"v_and_b32 v124,1,v124\nv_cmp_ne_u32 vcc,0,v124\ns_nop 0\ns_and_b64 vcc,vcc,s[78:79]\n" \
"s_cbranch_vccnz .Lst_%=\n.Lbt_%=:\nv_mfma_f32_16x16x32_f16 v[64:67],v[128:131],v[96:99],0\ns_add_u32 s95,s92,9584640\n" \
"s_add_u32 m0,s57,20480\nv_mfma_f32_16x16x32_f16 v[68:71],v[132:135],v[96:99],0\nbuffer_load_dwordx4 v4,s[8:11],s95 offen lds\nv_mfma_f32_16x16x32_f16 v[72:75],v[136:139],v[100:103],0\n" \
"s_add_u32 s100,s92,221184\ns_add_u32 s101,s92,9658368\nv_mfma_f32_16x16x32_f16 v[76:79],v[140:143],v[100:103],0\ns_add_u32 s31,s92,294912\n" \
"s_add_u32 s96,s92,9732096\nv_mfma_f32_16x16x32_f16 v[80:83],v[144:147],v[104:107],0\nbuffer_load_dwordx4 v5,s[8:11],s95 offen offset:1024 lds\nv_mfma_f32_16x16x32_f16 v[84:87],v[148:151],v[104:107],0\n" \
"v_cvt_pk_f16_f32 v96,v64,v68\nv_cvt_pk_f16_f32 v100,v65,v69\nv_mfma_f32_16x16x32_f16 v[88:91],v[152:155],v[108:111],0\nv_cvt_pk_f16_f32 v97,v72,v76\n" \
"v_cvt_pk_f16_f32 v101,v73,v77\nbuffer_load_dwordx4 v6,s[8:11],s95 offen offset:2048 lds\nv_mfma_f32_16x16x32_f16 v[92:95],v[156:159],v[108:111],0\nv_cvt_pk_f16_f32 v104,v66,v70\n" \
"v_cvt_pk_f16_f32 v98,v80,v84\nv_cvt_pk_f16_f32 v102,v81,v85\nv_cvt_pk_f16_f32 v105,v74,v78\nv_cvt_pk_f16_f32 v106,v82,v86\n" \
"v_cvt_pk_f16_f32 v108,v67,v71\nbuffer_load_dwordx4 v7,s[8:11],s95 offen offset:3072 lds\nv_cvt_pk_f16_f32 v109,v75,v79\nv_cvt_pk_f16_f32 v99,v88,v92\n" \
"ds_write_b128 v246,v[96:99]\nv_cvt_pk_f16_f32 v103,v89,v93\ns_add_u32 m0,s57,24576\ns_add_u32 s97,s92,368640\n" \
"buffer_load_dwordx4 v4,s[8:11],s100 offen lds\nds_write_b128 v247,v[100:103] offset:1024\nv_cvt_pk_f16_f32 v107,v90,v94\nds_write_b128 v246,v[104:107] offset:2048\n" \
"buffer_load_dwordx4 v5,s[8:11],s100 offen offset:1024 lds\nv_cvt_pk_f16_f32 v110,v83,v87\nv_cvt_pk_f16_f32 v111,v91,v95\nds_write_b128 v247,v[108:111] offset:3072\n" \
"s_add_u32 s95,s92,9805824\ns_mov_b32 s5,26624\nbuffer_load_dwordx4 v6,s[8:11],s100 offen offset:2048 lds\ns_mov_b32 s6,31232\n" \
"buffer_load_dwordx4 v7,s[8:11],s100 offen offset:3072 lds\ns_add_u32 m0,s57,0\ns_add_u32 s100,s92,442368\ns_waitcnt lgkmcnt(0)\n" \
"s_barrier\nds_read_b128 v[96:99],v252\nds_read_b128 v[100:103],v253\nbuffer_load_dwordx4 v4,s[8:11],s101 offen lds\n" \
"ds_read_b128 v[104:107],v252 offset:2048\nds_read_b128 v[108:111],v253 offset:2048\nbuffer_load_dwordx4 v5,s[8:11],s101 offen offset:1024 lds\ns_waitcnt lgkmcnt(3)\n" \
"v_mfma_f32_16x16x32_f16 v[64:67],v[112:115],v[96:99],0\nv_mfma_f32_16x16x32_f16 v[68:71],v[116:119],v[96:99],0\nbuffer_load_dwordx4 v6,s[8:11],s101 offen offset:2048 lds\ns_waitcnt lgkmcnt(2)\n" \
"v_mfma_f32_16x16x32_f16 v[72:75],v[112:115],v[100:103],0\nv_mfma_f32_16x16x32_f16 v[76:79],v[116:119],v[100:103],0\ns_waitcnt lgkmcnt(1)\nv_mfma_f32_16x16x32_f16 v[80:83],v[112:115],v[104:107],0\n" \
"buffer_load_dwordx4 v7,s[8:11],s101 offen offset:3072 lds\nv_mfma_f32_16x16x32_f16 v[84:87],v[116:119],v[104:107],0\ns_add_u32 m0,s57,4096\nv_pk_mul_f32 v[122:123],v[16:17],v[64:65]\n" \
"v_pk_mul_f32 v[120:121],v[16:17],v[68:69]\nv_pk_mul_f32 v[124:125],v[18:19],v[70:71]\nv_pk_mul_f32 v[126:127],v[18:19],v[66:67]\nv_pk_fma_f32 v[120:121],v[20:21],v[64:65],v[120:121]\n" \
"buffer_load_dwordx4 v4,s[8:11],s31 offen lds\nv_pk_fma_f32 v[122:123],v[20:21],v[68:69],v[122:123] neg_lo:[1,0,0] neg_hi:[1,0,0]\nv_pk_fma_f32 v[124:125],v[22:23],v[66:67],v[124:125]\nv_pk_fma_f32 v[126:127],v[22:23],v[70:71],v[126:127] neg_lo:[1,0,0] neg_hi:[1,0,0]\n" \
"v_pk_fma_f32 v[120:121],v[32:33],v[76:77],v[120:121]\nv_pk_fma_f32 v[122:123],v[32:33],v[72:73],v[122:123]\nv_pk_fma_f32 v[124:125],v[34:35],v[78:79],v[124:125]\nv_pk_fma_f32 v[126:127],v[34:35],v[74:75],v[126:127]\n" \
"buffer_load_dwordx4 v5,s[8:11],s31 offen offset:1024 lds\nv_pk_fma_f32 v[120:121],v[36:37],v[72:73],v[120:121]\nv_pk_fma_f32 v[122:123],v[36:37],v[76:77],v[122:123] neg_lo:[1,0,0] neg_hi:[1,0,0]\nv_pk_fma_f32 v[124:125],v[38:39],v[74:75],v[124:125]\n" \
"v_pk_fma_f32 v[126:127],v[38:39],v[78:79],v[126:127] neg_lo:[1,0,0] neg_hi:[1,0,0]\ns_waitcnt lgkmcnt(0)\nv_mfma_f32_16x16x32_f16 v[88:91],v[112:115],v[108:111],0\nv_cvt_pk_f16_f32 v96,v120,v122\n" \
"v_cvt_pk_f16_f32 v97,v121,v123\nbuffer_load_dwordx4 v6,s[8:11],s31 offen offset:2048 lds\nv_mfma_f32_16x16x32_f16 v[92:95],v[116:119],v[108:111],0\nv_cvt_pk_f16_f32 v98,v124,v126\n" \
"v_cvt_pk_f16_f32 v99,v125,v127\nv_pk_mul_f32 v[120:121],v[24:25],v[76:77]\nv_pk_mul_f32 v[122:123],v[24:25],v[72:73]\nv_pk_mul_f32 v[124:125],v[26:27],v[78:79]\n" \
"v_pk_mul_f32 v[126:127],v[26:27],v[74:75]\nbuffer_load_dwordx4 v7,s[8:11],s31 offen offset:3072 lds\nv_pk_fma_f32 v[120:121],v[28:29],v[72:73],v[120:121]\nv_pk_fma_f32 v[122:123],v[28:29],v[76:77],v[122:123] neg_lo:[1,0,0] neg_hi:[1,0,0]\n" \
"v_pk_fma_f32 v[124:125],v[30:31],v[74:75],v[124:125]\nv_pk_fma_f32 v[126:127],v[30:31],v[78:79],v[126:127] neg_lo:[1,0,0] neg_hi:[1,0,0]\nv_pk_fma_f32 v[120:121],v[32:33],v[68:69],v[120:121] neg_lo:[1,0,0] neg_hi:[1,0,0]\nv_pk_fma_f32 v[122:123],v[32:33],v[64:65],v[122:123] neg_lo:[1,0,0] neg_hi:[1,0,0]\n" \
"v_pk_fma_f32 v[124:125],v[34:35],v[70:71],v[124:125] neg_lo:[1,0,0] neg_hi:[1,0,0]\ns_add_u32 m0,s57,8192\ns_add_u32 s101,s92,9879552\nbuffer_load_dwordx4 v4,s[8:11],s96 offen lds\n" \
"v_pk_fma_f32 v[126:127],v[34:35],v[66:67],v[126:127] neg_lo:[1,0,0] neg_hi:[1,0,0]\nv_pk_fma_f32 v[120:121],v[36:37],v[64:65],v[120:121] neg_lo:[1,0,0] neg_hi:[1,0,0]\nv_pk_fma_f32 v[122:123],v[36:37],v[68:69],v[122:123]\nv_pk_fma_f32 v[124:125],v[38:39],v[66:67],v[124:125] neg_lo:[1,0,0] neg_hi:[1,0,0]\n" \
"v_pk_fma_f32 v[126:127],v[38:39],v[70:71],v[126:127]\nv_cvt_pk_f16_f32 v100,v120,v122\nv_cvt_pk_f16_f32 v101,v121,v123\nbuffer_load_dwordx4 v5,s[8:11],s96 offen offset:1024 lds\n" \
"v_cvt_pk_f16_f32 v102,v124,v126\nv_cvt_pk_f16_f32 v103,v125,v127\nv_pk_mul_f32 v[120:121],v[40:41],v[84:85]\nv_pk_mul_f32 v[122:123],v[40:41],v[80:81]\n" \
"v_pk_mul_f32 v[124:125],v[42:43],v[86:87]\nv_pk_mul_f32 v[126:127],v[42:43],v[82:83]\nv_pk_fma_f32 v[120:121],v[44:45],v[80:81],v[120:121]\nbuffer_load_dwordx4 v6,s[8:11],s96 offen offset:2048 lds\n" \
"v_pk_fma_f32 v[122:123],v[44:45],v[84:85],v[122:123] neg_lo:[1,0,0] neg_hi:[1,0,0]\nv_pk_fma_f32 v[124:125],v[46:47],v[82:83],v[124:125]\nv_pk_fma_f32 v[126:127],v[46:47],v[86:87],v[126:127] neg_lo:[1,0,0] neg_hi:[1,0,0]\nv_pk_fma_f32 v[120:121],v[56:57],v[92:93],v[120:121]\n" \
"v_pk_fma_f32 v[122:123],v[56:57],v[88:89],v[122:123]\nv_pk_fma_f32 v[124:125],v[58:59],v[94:95],v[124:125]\nv_pk_fma_f32 v[126:127],v[58:59],v[90:91],v[126:127]\nbuffer_load_dwordx4 v7,s[8:11],s96 offen offset:3072 lds\n" \
"v_pk_fma_f32 v[120:121],v[60:61],v[88:89],v[120:121]\nv_pk_fma_f32 v[122:123],v[60:61],v[92:93],v[122:123] neg_lo:[1,0,0] neg_hi:[1,0,0]\nv_pk_fma_f32 v[124:125],v[62:63],v[90:91],v[124:125]\nv_pk_fma_f32 v[126:127],v[62:63],v[94:95],v[126:127] neg_lo:[1,0,0] neg_hi:[1,0,0]\n" \
"v_cvt_pk_f16_f32 v104,v120,v122\nv_cvt_pk_f16_f32 v105,v121,v123\nv_cvt_pk_f16_f32 v106,v124,v126\ns_add_u32 m0,s57,12288\n" \
"v_cvt_pk_f16_f32 v107,v125,v127\nbuffer_load_dwordx4 v4,s[8:11],s97 offen lds\nv_pk_mul_f32 v[120:121],v[48:49],v[92:93]\nv_pk_mul_f32 v[122:123],v[48:49],v[88:89]\n" \
"v_pk_mul_f32 v[124:125],v[50:51],v[94:95]\nv_pk_mul_f32 v[126:127],v[50:51],v[90:91]\nv_pk_fma_f32 v[120:121],v[52:53],v[88:89],v[120:121]\nv_pk_fma_f32 v[122:123],v[52:53],v[92:93],v[122:123] neg_lo:[1,0,0] neg_hi:[1,0,0]\n" \
"v_pk_fma_f32 v[124:125],v[54:55],v[90:91],v[124:125]\nbuffer_load_dwordx4 v5,s[8:11],s97 offen offset:1024 lds\nv_pk_fma_f32 v[126:127],v[54:55],v[94:95],v[126:127] neg_lo:[1,0,0] neg_hi:[1,0,0]\nv_pk_fma_f32 v[120:121],v[56:57],v[84:85],v[120:121] neg_lo:[1,0,0] neg_hi:[1,0,0]\n" \
"v_pk_fma_f32 v[122:123],v[56:57],v[80:81],v[122:123] neg_lo:[1,0,0] neg_hi:[1,0,0]\nv_pk_fma_f32 v[124:125],v[58:59],v[86:87],v[124:125] neg_lo:[1,0,0] neg_hi:[1,0,0]\nv_pk_fma_f32 v[126:127],v[58:59],v[82:83],v[126:127] neg_lo:[1,0,0] neg_hi:[1,0,0]\nv_pk_fma_f32 v[120:121],v[60:61],v[80:81],v[120:121] neg_lo:[1,0,0] neg_hi:[1,0,0]\n" \
"v_pk_fma_f32 v[122:123],v[60:61],v[84:85],v[122:123]\nbuffer_load_dwordx4 v6,s[8:11],s97 offen offset:2048 lds\nv_pk_fma_f32 v[124:125],v[62:63],v[82:83],v[124:125] neg_lo:[1,0,0] neg_hi:[1,0,0]\nv_pk_fma_f32 v[126:127],v[62:63],v[86:87],v[126:127]\n" \
"v_mfma_f32_16x16x32_f16 v[64:67],v[112:115],v[96:99],0\nv_cvt_pk_f16_f32 v108,v120,v122\nv_cvt_pk_f16_f32 v109,v121,v123\nv_mfma_f32_16x16x32_f16 v[68:71],v[116:119],v[96:99],0\n" \
"buffer_load_dwordx4 v7,s[8:11],s97 offen offset:3072 lds\nv_mfma_f32_16x16x32_f16 v[80:83],v[112:115],v[104:107],0\nv_cvt_pk_f16_f32 v110,v124,v126\nv_cvt_pk_f16_f32 v111,v125,v127\n" \
"v_mfma_f32_16x16x32_f16 v[84:87],v[116:119],v[104:107],0\ns_add_u32 m0,s57,16384\ns_waitcnt vmcnt(20)\nds_read_b64_tr_b16 a[66:67],v8 offset:20480\n" \
"v_mfma_f32_16x16x32_f16 v[72:75],v[112:115],v[100:103],0\nbuffer_load_dwordx4 v4,s[8:11],s95 offen lds\nv_mfma_f32_16x16x32_f16 v[76:79],v[116:119],v[100:103],0\nv_cvt_pk_f16_f32 v96,v64,v68\n" \
"v_cvt_pk_f16_f32 v98,v65,v69\nv_mfma_f32_16x16x32_f16 v[88:91],v[112:115],v[108:111],0\nv_cvt_pk_f16_f32 v97,v80,v84\nds_write_b64 v248,v[96:97] offset:16384\n" \
"buffer_load_dwordx4 v5,s[8:11],s95 offen offset:1024 lds\nv_mfma_f32_16x16x32_f16 v[92:95],v[116:119],v[108:111],0\nv_cvt_pk_f16_f32 v99,v81,v85\nds_write_b64 v248,v[98:99] offset:17408\n" \
"v_cvt_pk_f16_f32 v100,v66,v70\nv_cvt_pk_f16_f32 v101,v82,v86\nds_write_b64 v248,v[100:101] offset:18432\nbuffer_load_dwordx4 v6,s[8:11],s95 offen offset:2048 lds\n" \
"v_cvt_pk_f16_f32 v102,v67,v71\nv_cvt_pk_f16_f32 v103,v83,v87\nds_write_b64 v248,v[102:103] offset:19456\nv_cvt_pk_f16_f32 v104,v72,v76\n" \
"v_cvt_pk_f16_f32 v105,v88,v92\nds_write_b64 v248,v[104:105] offset:16512\nbuffer_load_dwordx4 v7,s[8:11],s95 offen offset:3072 lds\nv_cvt_pk_f16_f32 v106,v73,v77\n" \
"v_cvt_pk_f16_f32 v107,v89,v93\nds_write_b64 v248,v[106:107] offset:17536\nv_cvt_pk_f16_f32 v108,v74,v78\nv_cvt_pk_f16_f32 v109,v90,v94\n" \
"ds_write_b64 v248,v[108:109] offset:18560\ns_add_u32 m0,s57,20480\nv_cvt_pk_f16_f32 v110,v75,v79\nv_cvt_pk_f16_f32 v111,v91,v95\n" \
"ds_write_b64 v248,v[110:111] offset:19584\nds_read_b64_tr_b16 a[70:71],v9 offset:20480\nds_read_b64_tr_b16 a[74:75],v10 offset:20480\nds_read_b64_tr_b16 a[78:79],v11 offset:20480\n" \
"ds_read_b64_tr_b16 a[82:83],v8 offset:22528\nds_read_b64_tr_b16 a[86:87],v9 offset:22528\nds_read_b64_tr_b16 a[90:91],v10 offset:22528\nds_read_b64_tr_b16 a[94:95],v11 offset:22528\n" \
"s_waitcnt lgkmcnt(0)\nbuffer_load_dwordx4 v4,s[8:11],s100 offen lds\ns_waitcnt vmcnt(21)\nds_read_b64_tr_b16 a[96:97],v8 offset:24576\n" \
"ds_read_b64_tr_b16 a[100:101],v9 offset:24576\nds_read_b64_tr_b16 a[104:105],v10 offset:24576\nds_read_b64_tr_b16 a[108:109],v11 offset:24576\nds_read_b64_tr_b16 a[112:113],v8 offset:26624\n" \
"ds_read_b64_tr_b16 a[116:117],v9 offset:26624\nds_read_b64_tr_b16 a[120:121],v10 offset:26624\nv_min_u32 v32,2288,v2\nbuffer_load_dwordx4 v5,s[8:11],s100 offen offset:1024 lds\n" \
"s_barrier\nds_read_b128 v[96:99],v254 offset:16384\nds_read_b128 v[100:103],v254 offset:17408\nds_read_b128 v[104:107],v254 offset:18432\n" \
"ds_read_b128 v[108:111],v254 offset:19456\nds_read_b64_tr_b16 a[124:125],v11 offset:26624\nbuffer_load_dwordx4 v6,s[8:11],s100 offen offset:2048 lds\nbuffer_load_dwordx4 v7,s[8:11],s100 offen offset:3072 lds\n" \
"s_add_u32 m0,s57,24576\ns_waitcnt lgkmcnt(4)\nv_mfma_f32_16x16x32_f16 v[64:67],v[96:99],v[160:163],0\nv_mfma_f32_16x16x32_f16 v[68:71],v[96:99],v[164:167],0\n" \
"s_waitcnt lgkmcnt(0)\nbuffer_load_dwordx4 v4,s[8:11],s101 offen lds\nv_mfma_f32_16x16x32_f16 v[72:75],v[100:103],v[168:171],0\nv_mfma_f32_16x16x32_f16 v[76:79],v[100:103],v[172:175],0\n" \
"v_mfma_f32_16x16x32_f16 v[80:83],v[104:107],v[176:179],0\nbuffer_load_dwordx4 v5,s[8:11],s101 offen offset:1024 lds\nv_mfma_f32_16x16x32_f16 v[84:87],v[104:107],v[180:183],0\nv_mfma_f32_16x16x32_f16 v[88:91],v[108:111],v[184:187],0\n" \
"v_cvt_pk_f16_f32 v96,v68,v64\nv_cvt_pk_f16_f32 v97,v69,v65\nbuffer_load_dwordx4 v6,s[8:11],s101 offen offset:2048 lds\nv_cvt_pk_f16_f32 v98,v70,v66\n" \
"v_cvt_pk_f16_f32 v99,v71,v67\nv_or_b32_e64 v96,1,v96\nv_mfma_f32_16x16x32_f16 v[92:95],v[108:111],v[188:191],0\nv_cvt_pk_f16_f32 v100,v76,v72\n" \
"v_cvt_pk_f16_f32 v101,v77,v73\nv_cvt_pk_f16_f32 v102,v78,v74\nbuffer_load_dwordx4 v7,s[8:11],s101 offen offset:3072 lds\nv_cvt_pk_f16_f32 v103,v79,v75\n" \
"v_or_b32_e64 v100,1,v100\nv_cvt_pk_f16_f32 v104,v84,v80\nv_cvt_pk_f16_f32 v105,v85,v81\nv_cvt_pk_f16_f32 v106,v86,v82\n" \
"v_cvt_pk_f16_f32 v107,v87,v83\nv_or_b32_e64 v104,1,v104\nv_cvt_pk_f16_f32 v108,v92,v88\nbuffer_load_dwordx4 v[16:19],v32,s[36:39],s5 offen sc1\n" \
"v_cvt_pk_f16_f32 v109,v93,v89\nv_cvt_pk_f16_f32 v110,v94,v90\nv_cvt_pk_f16_f32 v111,v95,v91\nv_or_b32_e64 v108,1,v108\n" \
"buffer_load_dwordx4 v[20:23],v32,s[36:39],s5 offen offset:2304 sc1\nbuffer_load_dwordx4 v[24:27],v32,s[36:39],s6 offen sc1\nbuffer_load_dwordx4 v[28:31],v32,s[36:39],s6 offen offset:2304 sc1\nbuffer_store_dwordx4 v[96:99],v255,s[36:39],s80 offen sc1\n" \
"buffer_store_dwordx4 v[100:103],v255,s[36:39],s81 offen sc1\nbuffer_store_dwordx4 v[104:107],v255,s[36:39],s82 offen sc1\nbuffer_store_dwordx4 v[108:111],v255,s[36:39],s83 offen sc1\ns_barrier\n" \
"s_waitcnt vmcnt(32)\nds_read_b64_tr_b16 a[98:99],v8\nds_read_b64_tr_b16 a[102:103],v9\nds_read_b64_tr_b16 a[106:107],v10\n" \
"ds_read_b64_tr_b16 a[110:111],v11\nds_read_b64_tr_b16 a[114:115],v8 offset:2048\nds_read_b64_tr_b16 a[118:119],v9 offset:2048\nds_read_b64_tr_b16 a[122:123],v10 offset:2048\n" \
"ds_read_b64_tr_b16 a[126:127],v11 offset:2048\ns_waitcnt vmcnt(28)\nds_read_b64_tr_b16 a[128:129],v8 offset:4096\nds_read_b64_tr_b16 a[132:133],v9 offset:4096\n" \
"ds_read_b64_tr_b16 a[136:137],v10 offset:4096\nds_read_b64_tr_b16 a[140:141],v11 offset:4096\nds_read_b64_tr_b16 a[144:145],v8 offset:6144\nds_read_b64_tr_b16 a[148:149],v9 offset:6144\n" \
"ds_read_b64_tr_b16 a[152:153],v10 offset:6144\nds_read_b64_tr_b16 a[156:157],v11 offset:6144\ns_waitcnt vmcnt(24)\nds_read_b64_tr_b16 a[130:131],v8 offset:8192\n" \
"ds_read_b64_tr_b16 a[134:135],v9 offset:8192\nds_read_b64_tr_b16 a[138:139],v10 offset:8192\nds_read_b64_tr_b16 a[142:143],v11 offset:8192\nds_read_b64_tr_b16 a[146:147],v8 offset:10240\n" \
"ds_read_b64_tr_b16 a[150:151],v9 offset:10240\nds_read_b64_tr_b16 a[154:155],v10 offset:10240\nds_read_b64_tr_b16 a[158:159],v11 offset:10240\ns_waitcnt vmcnt(7)\n" \
"v_cmp_ne_u64 vcc,s[28:29],v[16:17]\ns_waitcnt vmcnt(6)\nv_cmp_ne_u64 s[24:25],s[28:29],v[20:21]\ns_waitcnt vmcnt(5)\n" \
"v_cmp_ne_u64 s[26:27],s[28:29],v[24:25]\ns_or_b64 vcc,vcc,s[24:25]\ns_waitcnt vmcnt(4)\nv_cmp_ne_u64 s[24:25],s[28:29],v[28:29]\n" \
"s_or_b64 vcc,vcc,s[26:27]\ns_or_b64 vcc,vcc,s[24:25]\ns_cbranch_vccnz .LsnC_%=\n.LbnC_%=:\n" \
"v_bfe_u32 v112,v0,1,5\nv_mul_u32_u24 v112,72,v112\nv_lshrrev_b32 v113,6,v0\nv_mul_u32_u24 v114,144,v0\n" \
"v_lshl_add_u32 v112,v113,5,v112\nv_subrev_u32 v114,18368,v114\nv_mov_b32 v115,1048576\nv_cmp_gt_u32 vcc,144,v0\n" \
"v_and_b32 v113,1,v0\nv_lshl_add_u32 v112,v113,4,v112\nv_cndmask_b32 v114,v115,v114,vcc\nv_and_b32 v97,0xffff0000,v18\n" \
"v_lshlrev_b32 v96,16,v18\ns_add_u32 s24,s93,s94\nv_cmp_gt_u32 vcc,128,v0\nv_add_u32 v113,8,v112\n" \
"v_mul_f32 v96,0x3ee38e39,v96\nv_mul_f32 v97,0x3ee38e39,v97\ns_sub_u32 s24,s24,253\nv_cndmask_b32 v112,v114,v112,vcc\n" \
"v_and_b32 v99,0xffff0000,v19\nv_ldexp_f32 v96,v96,s24\nv_ldexp_f32 v97,v97,s24\nv_lshlrev_b32_e64 v98,16,v19\n" \
"ds_write_b64 v112,v[96:97] offset:28800\nv_add_u32 v115,72,v114\nv_mul_f32 v98,0x3ee38e39,v98\nv_mul_f32 v99,0x3ee38e39,v99\n" \
"v_cndmask_b32_e64 v113,v115,v113,vcc\nv_ldexp_f32 v98,v98,s24\nv_ldexp_f32 v99,v99,s24\nds_write_b64 v113,v[98:99] offset:28800\n" \
"v_and_b32 v101,0xffff0000,v22\nv_lshlrev_b32 v100,16,v22\nds_write_b64 v112,v[100:101] offset:31104\nv_and_b32 v103,0xffff0000,v23\n" \
"v_lshlrev_b32 v102,16,v23\ns_lshr_b32 s25,s33,3\nds_write_b64 v113,v[102:103] offset:31104\ns_mul_i32 s25,s25,576\n" \
"v_and_b32 v105,0xffff0000,v26\nv_lshlrev_b32 v104,16,v26\ns_sub_u32 s25,163200,s25\nds_write_b64 v112,v[104:105] offset:33408\n" \
"v_add_u32 v114,s25,v2\nv_subrev_u32 v115,163200,v114\nv_and_b32 v107,0xffff0000,v27\nv_lshlrev_b32 v106,16,v27\n" \
"v_mov_b32 v116,1048576\nv_cmp_gt_u32 vcc,576,v115\nds_write_b64 v113,v[106:107] offset:33408\nv_and_b32 v109,0xffff0000,v30\n" \
"v_cndmask_b32 v114,v116,v114,vcc\nv_and_b32 v111,0xffff0000,v31\nv_lshlrev_b32 v108,16,v30\nv_lshlrev_b32 v110,16,v31\n" \
"ds_write_b128 v114,v[108:111]\nv_and_b32 v112,3,v0\ns_mul_i32 s27,s3,4896\nv_and_b32 v113,12,v0\n" \
"v_lshlrev_b32 v112,2,v112\ns_add_u32 s27,s27,9216\nv_max_u32 v112,v112,v113\nv_mov_b32 v114,1048576\n" \
"v_mov_b32 v113,s27\nv_cmp_gt_u32 vcc,12,v112\nv_and_b32 v124,48,v0\ns_and_b32 s22,s33,7\n" \
"v_cndmask_b32 v112,v114,v113,vcc\nv_add_u32 v48,v234,v112\nv_add_u32 v49,v235,v112\nv_add_u32 v50,v236,v112\n" \
"v_add_u32 v51,v237,v112\nv_add_u32 v52,v238,v112\nv_add_u32 v53,v239,v112\nv_add_u32 v54,v240,v112\n" \
"v_add_u32 v55,v241,v112\nv_cmp_gt_u32 vcc,64,v0\ns_and_b32 s24,s32,1\ns_lshl3_add_u32 s22,s24,s22\n" \
"v_cndmask_b32 v124,v124,v3,vcc\ns_bfe_u32 s24,s33,0x10002\ns_lshl1_add_u32 s24,s32,s24\ns_waitcnt lgkmcnt(0)\n" \
"s_barrier\ns_mul_i32 s19,s24,18\ns_and_b32 s24,s33,3\ns_lshl2_add_u32 s23,s24,s19\n" \
"s_lshl_b32 s24,s3,5\ns_lshr_b32 s20,s33,3\ns_lshr_b32 s21,s32,1\ns_add_u32 s54,s14,s24\n" \
"s_lshl2_add_u32 s24,s20,s21\ns_add_u32 s23,s23,s3\ns_lshl2_add_u32 s24,s24,s3\ns_add_u32 s23,s23,1\n" \
"s_lshl_b32 s24,s24,9\ns_lshl_b32 s23,s23,9\ns_add_u32 s24,s24,262144\ns_sub_u32 s59,s24,s23\n" \
"s_lshr_b32 s25,s3,1\ns_mul_i32 s24,s21,1152\ns_mul_i32 s25,s25,576\ns_add_u32 s24,s24,s25\n" \
"s_lshl4_add_u32 s24,s24,s22\ns_and_b32 s25,s3,1\ns_lshl_b32 s24,s24,8\ns_lshl_b32 s65,s25,17\n" \
"s_add_u32 s65,s65,s24\ns_lshl_b32 s25,s25,20\ns_add_u32 s24,s24,s25\ns_add_u32 s60,s24,655488\n" \
"s_add_u32 s61,s24,786432\ns_add_u32 s62,s24,786560\ns_add_u32 s63,s24,917504\ns_add_u32 s64,s24,917632\n" \
"s_mul_i32 s52,s20,4505600\ns_lshl2_add_u32 s24,s21,s3\ns_add_u32 s52,s52,12123648\ns_mul_i32 s24,s24,9216\n" \
"s_add_u32 s24,s24,s52\ns_add_u32 s68,s24,819712\ns_lshl_b32 s24,s22,14\ns_add_u32 s24,s24,s52\n" \
"s_lshl2_add_u32 s25,s21,s3\ns_add_u32 s24,s24,967168\ns_lshl_b32 s25,s25,8\ns_add_u32 s79,s25,s24\n" \
"s_add_u32 s25,s21,4\ns_and_b32 s25,s25,3\ns_add_u32 s24,s24,s12\ns_lshl_b32 s25,s25,12\n" \
"s_add_u32 s25,s25,s24\ns_sub_u32 s74,s25,819200\ns_add_u32 s25,s21,3\ns_and_b32 s25,s25,3\n" \
"s_lshl_b32 s25,s25,12\ns_add_u32 s25,s25,s24\ns_sub_u32 s71,s25,2048000\ns_sub_u32 s76,s25,1638400\n" \
"s_add_u32 s25,s21,2\ns_and_b32 s25,s25,3\ns_lshl_b32 s25,s25,12\ns_add_u32 s25,s25,s24\n" \
"s_sub_u32 s72,s25,1638400\ns_sub_u32 s77,s25,1638400\nbuffer_load_dwordx4 v[112:115],v3,s[36:39],s16 offen sc1\nbuffer_load_dwordx4 v[116:119],v3,s[36:39],s17 offen sc1\n" \
"buffer_load_dwordx4 v[120:123],v124,s[36:39],s18 offen sc1\ns_add_u32 s25,s21,1\ns_and_b32 s25,s25,3\ns_lshl_b32 s25,s25,12\n" \
"s_add_u32 s25,s25,s24\ns_sub_u32 s73,s25,1638400\ns_sub_u32 s78,s25,1228800\ns_lshl2_add_u32 s26,s33,s3\n" \
"s_and_b32 s24,s26,15\ns_and_b32 s25,s26,8\ns_lshl2_add_u32 s24,s24,192\ns_mul_i32 s25,s25,28\n" \
"s_add_u32 s25,s25,s24\ns_sub_u32 s25,s25,32\ns_bitcmp1_b32 s26,4\ns_cselect_b32 s24,s25,s24\n" \
"s_and_b32 s25,s26,31\ns_lshl2_add_u32 s25,s25,448\ns_bitcmp1_b32 s26,5\ns_cselect_b32 s24,s25,s24\n" \
"s_lshr_b32 s25,s26,6\ns_lshl1_add_u32 s25,s21,s25\ns_mul_i32 s25,s25,576\ns_add_u32 s24,s24,s25\n" \
"s_and_b32 s25,s32,1\ns_lshl1_add_u32 s24,s24,s25\ns_lshl_b32 s24,s24,11\ns_bfe_u32 s25,s33,131072\n" \
"s_add_u32 s24,s24,s25\nv_bfe_u32 v127,v0,5,1\nv_and_b32 v13,31,v0\ns_mul_i32 s48,s20,589824\n" \
"s_add_u32 s89,s24,1\nv_mul_u32_u24 v127,2448,v127\nv_mul_u32_u24 v13,72,v13\ns_mul_i32 s27,s3,4896\n" \
"s_lshl4_add_u32 s48,s12,s48\ns_lshl_b32 s86,s21,11\ns_lshl2_add_u32 s24,s2,s3\nv_and_b32 v63,0xffff0000,v224\n" \
"v_and_b32 v65,0xffff0000,v225\nv_and_b32 v67,0xffff0000,v226\nv_and_b32 v69,0xffff0000,v227\nv_and_b32 v71,0xffff0000,v228\n" \
"v_and_b32 v73,0xffff0000,v229\nv_and_b32 v75,0xffff0000,v230\nv_and_b32 v77,0xffff0000,v231\nv_and_b32 v79,0xffff0000,v232\n" \
"v_lshlrev_b32 v62,16,v224\nv_lshlrev_b32 v64,16,v225\nv_lshlrev_b32 v66,16,v226\nv_lshlrev_b32 v68,16,v227\n" \
"v_lshlrev_b32 v70,16,v228\nv_lshlrev_b32 v72,16,v229\nv_lshlrev_b32 v74,16,v230\nv_lshlrev_b32 v76,16,v231\n" \
"v_lshlrev_b32 v78,16,v232\nv_add3_u32 v13,v13,v127,s27\ns_add_u32 s48,s48,9764864\ns_add_u32 s65,s65,2097152\n" \
"s_lshl_b32 s67,s21,7\ns_lshl_b32 s85,s21,17\ns_sub_u32 s86,s14,s86\ns_lshl_b32 s24,s24,8\n" \
"v_pk_add_f32 v[62:63],v[62:63],v[250:251] op_sel_hi:[1,0] neg_lo:[0,1] neg_hi:[0,1]\nv_pk_add_f32 v[64:65],v[64:65],v[250:251] op_sel_hi:[1,0] neg_lo:[0,1] neg_hi:[0,1]\nv_pk_add_f32 v[66:67],v[66:67],v[250:251] op_sel_hi:[1,0] neg_lo:[0,1] neg_hi:[0,1]\nv_pk_add_f32 v[68:69],v[68:69],v[250:251] op_sel_hi:[1,0] neg_lo:[0,1] neg_hi:[0,1]\n" \
"v_pk_add_f32 v[70:71],v[70:71],v[250:251] op_sel_hi:[1,0] neg_lo:[0,1] neg_hi:[0,1]\nv_pk_add_f32 v[72:73],v[72:73],v[250:251] op_sel_hi:[1,0] neg_lo:[0,1] neg_hi:[0,1]\nv_pk_add_f32 v[74:75],v[74:75],v[250:251] op_sel_hi:[1,0] neg_lo:[0,1] neg_hi:[0,1]\nv_pk_add_f32 v[76:77],v[76:77],v[250:251] op_sel_hi:[1,0] neg_lo:[0,1] neg_hi:[0,1]\n" \
"v_pk_add_f32 v[78:79],v[78:79],v[250:251] op_sel_hi:[1,0] neg_lo:[0,1] neg_hi:[0,1]\nv_mov_b64 v[16:17],v[192:193]\nv_mov_b64 v[18:19],v[194:195]\nv_mov_b64 v[20:21],v[196:197]\n" \
"v_mov_b64 v[22:23],v[198:199]\nv_mov_b64 v[24:25],v[208:209]\nv_mov_b64 v[26:27],v[210:211]\nv_mov_b64 v[28:29],v[212:213]\n" \
"v_mov_b64 v[30:31],v[214:215]\nv_mov_b64 v[32:33],v[216:217]\nv_mov_b64 v[34:35],v[218:219]\nv_mov_b64 v[36:37],v[220:221]\n" \
"v_mov_b64 v[38:39],v[222:223]\nv_mov_b64 v[40:41],v[200:201]\nv_mov_b64 v[42:43],v[202:203]\nv_mov_b64 v[44:45],v[204:205]\n" \
"v_mov_b64 v[46:47],v[206:207]\nv_mov_b64 v[56:57],v[242:243]\nv_mov_b64 v[58:59],v[244:245]\nv_mov_b32 v60,v249\n" \
"v_mov_b32 v61,v250\nds_read_b64_tr_b16 a[160:161],v8 offset:12288\nds_read_b64_tr_b16 a[164:165],v9 offset:12288\nds_read_b64_tr_b16 a[168:169],v10 offset:12288\n" \
"ds_read_b64_tr_b16 a[172:173],v11 offset:12288\nds_read_b64_tr_b16 a[176:177],v8 offset:14336\nds_read_b64_tr_b16 a[180:181],v9 offset:14336\nds_read_b64_tr_b16 a[184:185],v10 offset:14336\n" \
"ds_read_b64_tr_b16 a[188:189],v11 offset:14336\nds_read_b64_tr_b16 a[162:163],v8 offset:16384\nds_read_b64_tr_b16 a[166:167],v9 offset:16384\nds_read_b64_tr_b16 a[170:171],v10 offset:16384\n" \
"ds_read_b64_tr_b16 a[174:175],v11 offset:16384\nds_read_b64_tr_b16 a[178:179],v8 offset:18432\nv_mov_b32_e64 v127,0\nds_read_b64_tr_b16 a[182:183],v9 offset:18432\n" \
"ds_read_b64_tr_b16 a[186:187],v10 offset:18432\ns_add_u32 s49,s48,4096\ns_add_u32 s50,s48,8192\ns_add_u32 s51,s48,12288\n" \
"s_add_u32 s53,s52,3276800\ns_add_u32 s83,s52,2048000\ns_mov_b32 s55,0xbdd2d3e7\ns_mul_i32 s56,s3,9568\n" \
"s_lshl_b32 s58,s3,13\ns_add_u32 s75,s59,196608\ns_add_u32 s66,s65,128\nds_read_b64_tr_b16 a[190:191],v11 offset:18432\n" \
"ds_read_b64_tr_b16 a[192:193],v8 offset:20480\ns_add_u32 s67,s67,163200\nds_read_b64_tr_b16 a[196:197],v9 offset:20480\ns_add_u32 s69,s68,3072\n" \
"ds_read_b64_tr_b16 a[200:201],v10 offset:20480\ns_add_u32 s70,s68,6144\nds_read_b64_tr_b16 a[204:205],v11 offset:20480\ns_add_u32 s80,s79,4096\n" \
"ds_read_b64_tr_b16 a[208:209],v8 offset:22528\ns_add_u32 s81,s79,8192\nds_read_b64_tr_b16 a[212:213],v9 offset:22528\ns_add_u32 s82,s79,12288\n" \
"ds_read_b64_tr_b16 a[216:217],v10 offset:22528\nds_read_b64_tr_b16 a[220:221],v11 offset:22528\ns_and_b32 s84,s33,1\nds_read_b64_tr_b16 a[194:195],v8 offset:24576\n" \
"s_add_u32 s85,s85,65536\nds_read_b64_tr_b16 a[198:199],v9 offset:24576\ns_add_u32 s87,s12,65536\nds_read_b64_tr_b16 a[202:203],v10 offset:24576\n" \
"s_add_u32 s88,s74,819200\nds_read_b64_tr_b16 a[206:207],v11 offset:24576\ns_add_u32 s86,s86,0x10000\nds_read_b64_tr_b16 a[210:211],v8 offset:26624\n" \
"s_mov_b32 s90,7405568\nds_read_b64_tr_b16 a[214:215],v9 offset:26624\ns_add_u32 s91,s24,69206016\nds_read_b64_tr_b16 a[218:219],v10 offset:26624\n" \
"ds_read_b64_tr_b16 a[222:223],v11 offset:26624\ns_sub_u32 s98,s57,s12\ns_mov_b32 s99,s12\ns_waitcnt vmcnt(1)\n" \
"v_and_b32 v125,v112,v116\ns_waitcnt vmcnt(0)\nv_bitop3_b32 v125,v125,v120,1 bitop3:0x80\nv_cmp_eq_u32 vcc,0,v125\n" \
"s_nop 0\ns_cbranch_vccnz .LsuC_%=\n.LbuC_%=:\nv_mfma_f32_16x16x32_f16 v[80:83],v[32:35],v[112:115],0\n" \
"v_and_b32 v3,31,v0\nv_mul_u32_u24 v3,72,v3\nv_mfma_f32_16x16x32_f16 v[84:87],v[36:39],v[112:115],0\nv_add_u32 v13,9216,v13\n" \
"v_mov_b32 v104,-1\nv_mfma_f32_16x16x32_f16 v[88:91],v[32:35],v[116:119],0\nv_mov_b32 v105,-1\nv_mov_b32 v106,-1\n" \
"v_mfma_f32_16x16x32_f16 v[92:95],v[36:39],v[116:119],0\nv_mov_b32 v107,-1\ns_add_u32 s27,s2,192\nv_mfma_f32_16x16x32_f16 v[96:99],v[32:35],v[120:123],0\n" \
"s_lshr_b32 s31,s27,4\ns_mul_i32 s96,s31,37\ns_lshr_b32 s96,s96,8\ns_lshl2_add_u32 s31,s96,s31\n" \
"s_mul_i32 s31,s31,409600\ns_and_b32 s96,s27,15\ns_lshl_b32 s96,s96,14\ns_add_u32 s31,s31,s96\n" \
"s_add_u32 s31,s31,13090816\nbuffer_store_dwordx4 v[104:107],v2,s[36:39],s31 offen sc1\nv_mfma_f32_16x16x32_f16 v[100:103],v[36:39],v[120:123],0\nv_cvt_pk_f16_f32 v112,v84,v80\n" \
"v_cvt_pk_f16_f32 v113,v85,v81\nv_cvt_pk_f16_f32 v114,v86,v82\nv_cvt_pk_f16_f32 v115,v87,v83\nds_write_b128 v57,v[112:115]\n" \
"v_cvt_pk_f16_f32 v116,v92,v88\nv_cvt_pk_f16_f32 v117,v93,v89\ns_add_u32 s31,s31,4096\nbuffer_store_dwordx4 v[104:107],v2,s[36:39],s31 offen sc1\n" \
"v_cvt_pk_f16_f32 v118,v94,v90\nv_cvt_pk_f16_f32 v119,v95,v91\nds_write_b128 v58,v[116:119]\nv_cvt_pk_f16_f32 v120,v100,v96\n" \
"v_cvt_pk_f16_f32 v121,v101,v97\nv_cvt_pk_f16_f32 v122,v102,v98\nv_cvt_pk_f16_f32 v123,v103,v99\nds_write_b128 v60,v[120:123]\n" \
"s_add_u32 s31,s31,4096\nbuffer_store_dwordx4 v[104:107],v2,s[36:39],s31 offen sc1\nv_add_u32 v60,31104,v3\nv_add_u32 v3,28800,v3\n" \
"ds_read2_b64 v[32:35],v3 offset0:0 offset1:1\nds_read2_b64 v[36:39],v3 offset0:2 offset1:3\ns_add_u32 s31,s31,4096\ns_add_u32 s27,s2,47\n" \
"s_lshr_b32 s97,s27,4\ns_mul_i32 s95,s97,37\ns_lshr_b32 s95,s95,8\ns_lshl2_add_u32 s97,s95,s97\n" \
"s_mul_i32 s97,s97,409600\ns_and_b32 s95,s27,15\ns_lshl_b32 s95,s95,14\nbuffer_store_dwordx4 v[104:107],v2,s[36:39],s31 offen sc1\n" \
"s_add_u32 s97,s97,s95\ns_add_u32 s97,s97,13090816\ns_add_u32 s100,s92,516096\ns_waitcnt lgkmcnt(2)\n" \
"s_barrier\nds_read_b64 v[112:113],v56\nds_read_b64 v[116:117],v56 offset:512\nds_read_b64 v[120:121],v56 offset:256\n" \
"ds_read_b64 v[124:125],v56 offset:768\nds_read_b64 v[114:115],v56 offset:1024\nds_read_b64 v[122:123],v56 offset:1280\nds_read_b64 v[56:57],v3 offset:64\n" \
"s_add_u32 m0,s57,0\nbuffer_store_dwordx4 v[104:107],v2,s[36:39],s97 offen sc1\ns_add_u32 s97,s97,4096\ns_add_u32 s101,s92,9953280\n" \
"s_bfe_u32 s26,s33,0x20001\ns_lshl2_add_u32 s15,s32,s26\ns_lshl2_add_u32 s15,s15,s3\ns_lshl_b32 s15,s15,14\n" \
"s_add_u32 s15,s15,5308416\ns_add_u32 s31,s15,0\ns_add_u32 s96,s15,4096\ns_add_u32 s95,s15,12288\n" \
"v_bfe_u32 v110,v0,1,2\nv_bfe_u32 v111,v0,4,1\nv_xor_b32 v110,v110,v111\nbuffer_store_dwordx4 v[104:107],v2,s[36:39],s97 offen sc1\n" \
"s_waitcnt lgkmcnt(5)\nv_swap_b32 v113,v116\ns_waitcnt lgkmcnt(3)\nv_swap_b32 v121,v124\n" \
"s_waitcnt lgkmcnt(2)\nv_mov_b32 v118,v115\nv_mfma_f32_16x16x32_f16 v[80:83],v[112:115],v[40:43],0\ns_waitcnt lgkmcnt(1)\n" \
"v_mov_b32 v126,v123\ns_add_u32 s97,s97,4096\nv_mfma_f32_16x16x32_f16 v[84:87],v[112:115],v[44:47],0\nv_and_b32_e64 v111,1,v0\n" \
"v_lshl_add_u32 v110,v110,1,v111\nv_mfma_f32_16x16x32_f16 v[88:91],v[120:123],v[40:43],0\nv_bfe_u32 v111,v0,3,3\nbuffer_store_dwordx4 v[104:107],v2,s[36:39],s97 offen sc1\n" \
"v_mfma_f32_16x16x32_f16 v[92:95],v[120:123],v[44:47],0\ns_add_u32 s97,s97,4096\nv_lshlrev_b32 v111,12,v111\nv_mfma_f32_16x16x32_f16 v[96:99],v[116:119],v[40:43],0\n" \
"s_lshr_b32 s26,s32,1\ns_mul_i32 s92,s26,1152\nv_pk_add_f32 v[88:89],v[80:81],v[88:89] neg_lo:[0,1] neg_hi:[0,1]\nv_pk_add_f32 v[90:91],v[82:83],v[90:91] neg_lo:[0,1] neg_hi:[0,1]\n" \
"v_pk_fma_f32 v[80:81],v[80:81],2.0,v[88:89] op_sel_hi:[1,0,1] neg_lo:[0,0,1] neg_hi:[0,0,1]\nv_pk_add_f32 v[92:93],v[84:85],v[92:93] neg_lo:[0,1] neg_hi:[0,1]\nv_pk_add_f32 v[94:95],v[86:87],v[94:95] neg_lo:[0,1] neg_hi:[0,1]\nbuffer_store_dwordx4 v[104:107],v2,s[36:39],s97 offen sc1\n" \
"v_pk_fma_f32 v[82:83],v[82:83],2.0,v[90:91] op_sel_hi:[1,0,1] neg_lo:[0,0,1] neg_hi:[0,0,1]\nv_pk_fma_f32 v[84:85],v[84:85],2.0,v[92:93] op_sel_hi:[1,0,1] neg_lo:[0,0,1] neg_hi:[0,0,1]\nv_pk_fma_f32 v[86:87],v[86:87],2.0,v[94:95] op_sel_hi:[1,0,1] neg_lo:[0,0,1] neg_hi:[0,0,1]\nv_cvt_pk_f16_f32 v112,v80,v84\n" \
"v_cvt_pk_f16_f32 v113,v81,v85\nv_cvt_pk_f16_f32 v114,v82,v86\nv_cvt_pk_f16_f32 v115,v83,v87\nv_mfma_f32_16x16x32_f16 v[100:103],v[116:119],v[44:47],0\n" \
"v_cvt_pk_f16_f32 v116,v88,v92\nv_cvt_pk_f16_f32 v117,v89,v93\nv_mfma_f32_16x16x32_f16 v[80:83],v[16:19],v[112:115],0\nbuffer_load_dwordx4 v4,s[8:11],s100 offen lds\n" \
"v_mfma_f32_16x16x32_f16 v[84:87],v[20:23],v[112:115],0\nv_cvt_pk_f16_f32 v118,v90,v94\nv_cvt_pk_f16_f32 v119,v91,v95\nv_mfma_f32_16x16x32_f16 v[104:107],v[124:127],v[40:43],0\n" \
"ds_read2_b64 v[40:43],v3 offset0:4 offset1:5\nds_read2_b64 v[112:115],v60 offset0:0 offset1:1\nv_mfma_f32_16x16x32_f16 v[88:91],v[24:27],v[116:119],0\nbuffer_load_dwordx4 v5,s[8:11],s100 offen offset:1024 lds\n" \
"v_mfma_f32_16x16x32_f16 v[92:95],v[28:31],v[116:119],0\nds_write2_b32 v48,v84,v80 offset0:0 offset1:1\nds_write2_b32 v49,v85,v81 offset0:0 offset1:1\nds_write2_b32 v50,v86,v82 offset0:0 offset1:1\n" \
"buffer_load_dwordx4 v6,s[8:11],s100 offen offset:2048 lds\nv_pk_add_f32 v[104:105],v[96:97],v[104:105] neg_lo:[0,1] neg_hi:[0,1]\nv_pk_add_f32 v[106:107],v[98:99],v[106:107] neg_lo:[0,1] neg_hi:[0,1]\nv_pk_fma_f32 v[96:97],v[96:97],2.0,v[104:105] op_sel_hi:[1,0,1] neg_lo:[0,0,1] neg_hi:[0,0,1]\n" \
"v_pk_fma_f32 v[98:99],v[98:99],2.0,v[106:107] op_sel_hi:[1,0,1] neg_lo:[0,0,1] neg_hi:[0,0,1]\nds_write2_b32 v51,v87,v83 offset0:0 offset1:1\nds_write2_b32 v52,v92,v88 offset0:0 offset1:1\nbuffer_load_dwordx4 v7,s[8:11],s100 offen offset:3072 lds\n" \
"ds_write2_b32 v53,v93,v89 offset0:0 offset1:1\nds_write2_b32 v54,v94,v90 offset0:0 offset1:1\nds_write2_b32 v55,v95,v91 offset0:0 offset1:1\nv_add_u32 v48,2448,v48\n" \
"s_add_u32 m0,s57,4096\ns_add_u32 s97,s15,8192\nbuffer_load_dwordx4 v4,s[8:11],s101 offen lds\nv_add_u32 v49,2448,v49\n" \
"v_add_u32 v50,2448,v50\nv_add_u32 v51,2448,v51\nv_add_u32 v52,2448,v52\nv_add_u32 v53,2448,v53\n" \
"v_add_u32 v54,2448,v54\nv_add_u32 v55,2448,v55\nbuffer_load_dwordx4 v5,s[8:11],s101 offen offset:1024 lds\nds_read2_b64 v[116:119],v60 offset0:2 offset1:3\n" \
"v_lshl_add_u32 v4,v110,4,v111\nv_mfma_f32_16x16x32_f16 v[108:111],v[124:127],v[44:47],0\nds_read2_b64 v[44:47],v3 offset0:6 offset1:7\nv_xor_b32 v5,64,v4\n" \
"v_add_u32 v5,31744,v5\ns_lshr_b32 s26,s3,1\nbuffer_load_dwordx4 v6,s[8:11],s101 offen offset:2048 lds\nv_add_u32 v6,63488,v4\n" \
"s_lshl_b32 s26,s26,6\ns_add_u32 s92,s92,s26\nv_pk_add_f32 v[108:109],v[100:101],v[108:109] neg_lo:[0,1] neg_hi:[0,1]\nv_pk_add_f32 v[110:111],v[102:103],v[110:111] neg_lo:[0,1] neg_hi:[0,1]\n" \
"v_pk_fma_f32 v[100:101],v[100:101],2.0,v[108:109] op_sel_hi:[1,0,1] neg_lo:[0,0,1] neg_hi:[0,0,1]\nv_pk_fma_f32 v[102:103],v[102:103],2.0,v[110:111] op_sel_hi:[1,0,1] neg_lo:[0,0,1] neg_hi:[0,0,1]\nv_cvt_pk_f16_f32 v120,v96,v100\nbuffer_load_dwordx4 v7,s[8:11],s101 offen offset:3072 lds\n" \
"v_cvt_pk_f16_f32 v121,v97,v101\nv_cvt_pk_f16_f32 v122,v98,v102\nv_cvt_pk_f16_f32 v123,v99,v103\nv_cvt_pk_f16_f32 v124,v104,v108\n" \
"v_cvt_pk_f16_f32 v125,v105,v109\nv_mfma_f32_16x16x32_f16 v[96:99],v[16:19],v[120:123],0\nv_cvt_pk_f16_f32 v126,v106,v110\ns_add_u32 m0,s57,8192\n" \
"s_lshr_b32 s100,s33,3\nbuffer_load_dwordx4 v1,s[36:39],s31 offen sc1 lds\nv_mfma_f32_16x16x32_f16 v[100:103],v[20:23],v[120:123],0\nv_cvt_pk_f16_f32 v127,v107,v111\n" \
"ds_read2_b64 v[120:123],v60 offset0:4 offset1:5\nv_add_u32 v7,63488,v5\nv_mfma_f32_16x16x32_f16 v[104:107],v[24:27],v[124:127],0\ns_lshl_b32 s26,s3,8\n" \
"s_add_u32 s92,s92,s26\nbuffer_load_dwordx4 v1,s[36:39],s31 offen offset:1024 sc1 lds\nv_mfma_f32_16x16x32_f16 v[108:111],v[28:31],v[124:127],0\nds_write2_b32 v48,v100,v96 offset0:0 offset1:1\n" \
"ds_write2_b32 v49,v101,v97 offset0:0 offset1:1\nds_write2_b32 v50,v102,v98 offset0:0 offset1:1\nbuffer_load_dwordx4 v1,s[36:39],s31 offen offset:2048 sc1 lds\nds_write2_b32 v51,v103,v99 offset0:0 offset1:1\n" \
"ds_read2_b64 v[124:127],v60 offset0:6 offset1:7\nds_read_b64 v[98:99],v60 offset:64\nv_add_u32 v60,2304,v60\nds_write2_b32 v52,v108,v104 offset0:0 offset1:1\n" \
"ds_write2_b32 v53,v109,v105 offset0:0 offset1:1\nbuffer_load_dwordx4 v1,s[36:39],s31 offen offset:3072 sc1 lds\nds_write2_b32 v54,v110,v106 offset0:0 offset1:1\nds_write2_b32 v55,v111,v107 offset0:0 offset1:1\n" \
"ds_read2_b64 v[80:83],v13 offset0:0 offset1:1\nds_read2_b64 v[84:87],v13 offset0:2 offset1:3\ns_add_u32 m0,s57,12288\ns_lshl_b32 s92,s92,4\n" \
"buffer_load_dwordx4 v1,s[36:39],s96 offen sc1 lds\nds_read2_b64 v[88:91],v13 offset0:4 offset1:5\ns_and_b32 s26,s32,1\nds_read2_b64 v[92:95],v13 offset0:6 offset1:7\n" \
"s_lshl3_add_u32 s92,s26,s92\ns_and_b32 s26,s33,7\ns_add_u32 s92,s92,s26\ns_lshl_b32 s92,s92,8\n" \
"s_add_u32 s100,s100,0\ns_sub_u32 s101,s100,4\nbuffer_load_dwordx4 v1,s[36:39],s96 offen offset:1024 sc1 lds\nds_read_b64 v[96:97],v13 offset:64\n" \
"ds_read2_b64 v[16:19],v60 offset0:0 offset1:1\nds_read2_b64 v[20:23],v60 offset0:2 offset1:3\ns_waitcnt lgkmcnt(6)\nv_pk_fma_f32 v[62:63],v[80:81],v[32:33],v[62:63]\n" \
"v_pk_fma_f32 v[64:65],v[82:83],v[34:35],v[64:65]\nv_pk_mul_f32 v[102:103],v[62:63],v[62:63]\nv_pk_add_f32 v[100:101],v[62:63],v[64:65]\nv_pk_fma_f32 v[102:103],v[64:65],v[64:65],v[102:103]\n" \
"buffer_load_dwordx4 v1,s[36:39],s96 offen offset:2048 sc1 lds\ns_waitcnt lgkmcnt(5)\nv_pk_fma_f32 v[66:67],v[84:85],v[36:37],v[66:67]\nv_pk_fma_f32 v[68:69],v[86:87],v[38:39],v[68:69]\n" \
"v_pk_add_f32 v[100:101],v[100:101],v[66:67]\nv_pk_fma_f32 v[102:103],v[66:67],v[66:67],v[102:103]\ns_waitcnt lgkmcnt(4)\nv_pk_fma_f32 v[70:71],v[88:89],v[40:41],v[70:71]\n" \
"v_pk_add_f32 v[100:101],v[100:101],v[68:69]\nv_pk_fma_f32 v[102:103],v[68:69],v[68:69],v[102:103]\nbuffer_load_dwordx4 v1,s[36:39],s96 offen offset:3072 sc1 lds\nv_pk_fma_f32 v[72:73],v[90:91],v[42:43],v[72:73]\n" \
"v_pk_add_f32 v[100:101],v[100:101],v[70:71]\nv_pk_fma_f32 v[102:103],v[70:71],v[70:71],v[102:103]\ns_waitcnt lgkmcnt(3)\nv_pk_fma_f32 v[74:75],v[92:93],v[44:45],v[74:75]\n" \
"v_pk_add_f32 v[100:101],v[100:101],v[72:73]\nv_pk_fma_f32 v[102:103],v[72:73],v[72:73],v[102:103]\nv_pk_fma_f32 v[76:77],v[94:95],v[46:47],v[76:77]\ns_add_u32 m0,s57,16384\n" \
"s_min_u32 s100,s100,s101\nbuffer_load_dwordx4 v1,s[36:39],s97 offen sc1 lds\nv_pk_add_f32 v[100:101],v[100:101],v[74:75]\nv_pk_fma_f32 v[102:103],v[74:75],v[74:75],v[102:103]\n" \
"s_waitcnt lgkmcnt(2)\nv_pk_fma_f32 v[78:79],v[96:97],v[56:57],v[78:79]\nv_pk_add_f32 v[100:101],v[100:101],v[76:77]\nv_pk_fma_f32 v[102:103],v[76:77],v[76:77],v[102:103]\n" \
"v_pk_add_f32 v[100:101],v[100:101],v[78:79]\nv_pk_fma_f32 v[102:103],v[78:79],v[78:79],v[102:103]\nbuffer_load_dwordx4 v1,s[36:39],s97 offen offset:1024 sc1 lds\nds_read2_b64 v[24:27],v60 offset0:4 offset1:5\n" \
"v_add_f32 v100,v100,v101\nv_add_f32 v102,v102,v103\nds_read2_b64 v[28:31],v60 offset0:6 offset1:7\nds_read_b64 v[58:59],v60 offset:64\n" \
"v_permlane16_swap_b32 v100,v102\nv_add_f32 v100,v100,v102\ns_waitcnt vmcnt(14)\nds_read_b64_tr_b16 a[224:225],v8\n" \
"buffer_load_dwordx4 v1,s[36:39],s97 offen offset:2048 sc1 lds\nv_add_f32_dpp v100,v100,v100 quad_perm:[1,0,3,2] row_mask:0xf bank_mask:0xf bound_ctrl:1\nds_read_b64_tr_b16 a[228:229],v9\nds_read_b64_tr_b16 a[232:233],v10\n" \
"v_add_f32_dpp v100,v100,v100 quad_perm:[2,3,0,1] row_mask:0xf bank_mask:0xf bound_ctrl:1\nds_read_b64_tr_b16 a[236:237],v11\nds_read_b64_tr_b16 a[240:241],v8 offset:2048\nv_add_f32_dpp v100,v100,v100 row_half_mirror row_mask:0xf bank_mask:0xf bound_ctrl:1\n" \
"ds_read_b64_tr_b16 a[244:245],v9 offset:2048\nbuffer_load_dwordx4 v1,s[36:39],s97 offen offset:3072 sc1 lds\nv_add_f32_dpp v100,v100,v100 row_mirror row_mask:0xf bank_mask:0xf bound_ctrl:1\nv_mul_f32 v104,0x3ae38e39,v100\n" \
"v_mul_f32 v102,0x3ae38e39,v100\nds_read_b64_tr_b16 a[248:249],v10 offset:2048\nds_read_b64_tr_b16 a[252:253],v11 offset:2048\nv_permlane16_swap_b32 v104,v102\n" \
"v_fma_f32 v102,-v104,v104,v102\ns_add_u32 m0,s57,20480\ns_lshl_b32 s101,s100,12\nbuffer_load_dwordx4 v1,s[36:39],s95 offen sc1 lds\n" \
"v_max_f32 v102,0,v102\nv_add_f32 v102,0x3727c5ac,v102\nv_rsq_f32 v100,v102\ns_waitcnt vmcnt(13)\n" \
"ds_read_b64_tr_b16 a[226:227],v8 offset:4096\nv_mul_f32 v101,-v104,v100\nv_pk_fma_f32 v[80:81],v[62:63],v[100:101],v[100:101] op_sel:[0,0,1] op_sel_hi:[1,0,1]\nv_pk_fma_f32 v[82:83],v[64:65],v[100:101],v[100:101] op_sel:[0,0,1] op_sel_hi:[1,0,1]\n" \
"buffer_load_dwordx4 v1,s[36:39],s95 offen offset:1024 sc1 lds\nv_pk_fma_f32 v[84:85],v[66:67],v[100:101],v[100:101] op_sel:[0,0,1] op_sel_hi:[1,0,1]\nv_pk_fma_f32 v[86:87],v[68:69],v[100:101],v[100:101] op_sel:[0,0,1] op_sel_hi:[1,0,1]\nv_pk_fma_f32 v[88:89],v[70:71],v[100:101],v[100:101] op_sel:[0,0,1] op_sel_hi:[1,0,1]\n" \
"v_pk_fma_f32 v[90:91],v[72:73],v[100:101],v[100:101] op_sel:[0,0,1] op_sel_hi:[1,0,1]\nv_pk_fma_f32 v[92:93],v[74:75],v[100:101],v[100:101] op_sel:[0,0,1] op_sel_hi:[1,0,1]\nv_pk_fma_f32 v[94:95],v[76:77],v[100:101],v[100:101] op_sel:[0,0,1] op_sel_hi:[1,0,1]\nv_pk_fma_f32 v[96:97],v[78:79],v[100:101],v[100:101] op_sel:[0,0,1] op_sel_hi:[1,0,1]\n" \
"buffer_load_dwordx4 v1,s[36:39],s95 offen offset:2048 sc1 lds\nds_read_b64_tr_b16 a[230:231],v9 offset:4096\nds_read_b64_tr_b16 a[234:235],v10 offset:4096\nds_read_b64_tr_b16 a[238:239],v11 offset:4096\n" \
"ds_read_b64_tr_b16 a[242:243],v8 offset:6144\nds_read_b64_tr_b16 a[246:247],v9 offset:6144\nds_read_b64_tr_b16 a[250:251],v10 offset:6144\nds_read_b64_tr_b16 a[254:255],v11 offset:6144\n" \
"s_waitcnt vmcnt(11)\nds_read_b64_tr_b16 v[192:193],v15 offset:8192\nds_read_b64_tr_b16 v[196:197],v15 offset:8704\nbuffer_load_dwordx4 v1,s[36:39],s95 offen offset:3072 sc1 lds\n" \
"ds_read_b64_tr_b16 v[194:195],v15 offset:9216\nds_read_b64_tr_b16 v[198:199],v15 offset:9728\nds_read_b64_tr_b16 v[200:201],v15 offset:10240\nds_read_b64_tr_b16 v[204:205],v15 offset:10752\n" \
"ds_read_b64_tr_b16 v[202:203],v15 offset:11264\nds_read_b64_tr_b16 v[206:207],v15 offset:11776\ns_add_u32 s101,s101,s57\ns_add_u32 m0,s101,0\n" \
"s_and_b32 s101,s100,1\ns_lshr_b32 s100,s100,1\ns_lshl_b32 s100,s100,10\ns_add_u32 s100,s100,s101\n" \
"s_lshl_b32 s100,s100,7\ns_add_u32 s100,s100,s92\ns_lshr_b32 s31,s33,3\ns_add_u32 s31,s31,1\n" \
"s_sub_u32 s96,s31,4\ns_min_u32 s31,s31,s96\ns_lshl_b32 s96,s31,12\ns_add_u32 s96,s96,s57\n" \
"s_lshr_b32 s97,s33,3\ns_add_u32 s97,s97,2\ns_sub_u32 s95,s97,4\ns_min_u32 s97,s97,s95\n" \
"s_lshl_b32 s95,s97,12\ns_add_u32 s95,s95,s57\nv_readlane_b32 s24,v112,0\nv_mov_b32 v60,v61\n" \
"s_mov_b32 s25,s24\nv_pk_add_f32 v[112:113],v[112:113],s[24:25] neg_lo:[0,1] neg_hi:[0,1]\ns_waitcnt lgkmcnt(15)\nv_pk_fma_f32 v[80:81],v[80:81],v[16:17],v[112:113]\n" \
"v_pk_mul_f32 v[102:103],v[80:81],v[80:81]\nv_pk_add_f32 v[114:115],v[114:115],s[24:25] neg_lo:[0,1] neg_hi:[0,1]\nv_pk_fma_f32 v[82:83],v[82:83],v[18:19],v[114:115]\nv_pk_add_f32 v[100:101],v[80:81],v[82:83]\n" \
"v_pk_fma_f32 v[102:103],v[82:83],v[82:83],v[102:103]\nv_pk_add_f32 v[116:117],v[116:117],s[24:25] neg_lo:[0,1] neg_hi:[0,1]\nv_pk_fma_f32 v[84:85],v[84:85],v[20:21],v[116:117]\nv_pk_add_f32 v[100:101],v[100:101],v[84:85]\n" \
"v_pk_fma_f32 v[102:103],v[84:85],v[84:85],v[102:103]\nv_pk_add_f32 v[118:119],v[118:119],s[24:25] neg_lo:[0,1] neg_hi:[0,1]\nv_pk_fma_f32 v[86:87],v[86:87],v[22:23],v[118:119]\nv_pk_add_f32 v[100:101],v[100:101],v[86:87]\n" \
"v_pk_fma_f32 v[102:103],v[86:87],v[86:87],v[102:103]\nv_pk_add_f32 v[120:121],v[120:121],s[24:25] neg_lo:[0,1] neg_hi:[0,1]\nv_pk_fma_f32 v[88:89],v[88:89],v[24:25],v[120:121]\nv_pk_add_f32 v[100:101],v[100:101],v[88:89]\n" \
"v_pk_fma_f32 v[102:103],v[88:89],v[88:89],v[102:103]\nv_pk_add_f32 v[122:123],v[122:123],s[24:25] neg_lo:[0,1] neg_hi:[0,1]\nv_pk_fma_f32 v[90:91],v[90:91],v[26:27],v[122:123]\nv_pk_add_f32 v[100:101],v[100:101],v[90:91]\n" \
"v_pk_fma_f32 v[102:103],v[90:91],v[90:91],v[102:103]\ns_waitcnt vmcnt(8)\nds_read_b64_tr_b16 v[208:209],v15 offset:12288\nds_read_b64_tr_b16 v[212:213],v15 offset:12800\n" \
"ds_read_b64_tr_b16 v[210:211],v15 offset:13312\nds_read_b64_tr_b16 v[214:215],v15 offset:13824\nds_read_b64_tr_b16 v[216:217],v15 offset:14336\nds_read_b64_tr_b16 v[220:221],v15 offset:14848\n" \
"ds_read_b64_tr_b16 v[218:219],v15 offset:15360\nds_read_b64_tr_b16 v[222:223],v15 offset:15872\ns_waitcnt lgkmcnt(0)\nbuffer_load_dwordx4 v4,s[40:43],s100 offen lds\n" \
"v_pk_add_f32 v[124:125],v[124:125],s[24:25] neg_lo:[0,1] neg_hi:[0,1]\nv_pk_fma_f32 v[92:93],v[92:93],v[28:29],v[124:125]\nv_pk_add_f32 v[100:101],v[100:101],v[92:93]\nv_pk_fma_f32 v[102:103],v[92:93],v[92:93],v[102:103]\n" \
"v_pk_add_f32 v[126:127],v[126:127],s[24:25] neg_lo:[0,1] neg_hi:[0,1]\nv_pk_fma_f32 v[94:95],v[94:95],v[30:31],v[126:127]\nv_pk_add_f32 v[100:101],v[100:101],v[94:95]\nbuffer_load_dwordx4 v5,s[40:43],s100 offen offset:1024 lds\n" \
"v_pk_fma_f32 v[102:103],v[94:95],v[94:95],v[102:103]\nv_pk_add_f32 v[98:99],v[98:99],s[24:25] neg_lo:[0,1] neg_hi:[0,1]\nv_pk_fma_f32 v[96:97],v[96:97],v[58:59],v[98:99]\nv_pk_add_f32 v[100:101],v[100:101],v[96:97]\n" \
"v_pk_fma_f32 v[102:103],v[96:97],v[96:97],v[102:103]\nv_add_f32 v100,v100,v101\nv_add_f32 v102,v102,v103\nv_pk_add_f32 v[62:63],v[62:63],v[60:61] op_sel_hi:[1,0]\n" \
"buffer_load_dwordx4 v6,s[40:43],s100 offen offset:2048 lds\nv_permlane16_swap_b32 v100,v102\nv_add_f32_e64 v100,v100,v102\nv_cvt_pk_bf16_f32 v32,v62,v63\n" \
"v_pk_add_f32 v[64:65],v[64:65],v[60:61] op_sel_hi:[1,0]\nv_add_f32_dpp v100,v100,v100 quad_perm:[1,0,3,2] row_mask:0xf bank_mask:0xf bound_ctrl:1\nv_cvt_pk_bf16_f32 v33,v64,v65\nv_pk_add_f32 v[66:67],v[66:67],v[60:61] op_sel_hi:[1,0]\n" \
"buffer_load_dwordx4 v7,s[40:43],s100 offen offset:3072 lds\nv_add_f32_dpp v100,v100,v100 quad_perm:[2,3,0,1] row_mask:0xf bank_mask:0xf bound_ctrl:1\ns_add_u32 m0,s96,0\ns_and_b32 s96,s31,1\n" \
"v_add_f32_dpp v100,v100,v100 row_half_mirror row_mask:0xf bank_mask:0xf bound_ctrl:1\ns_lshr_b32 s31,s31,1\ns_lshl_b32 s31,s31,10\nv_add_f32_dpp v100,v100,v100 row_mirror row_mask:0xf bank_mask:0xf bound_ctrl:1\n" \
"v_mul_f32 v104,0x3ae38e39,v100\ns_add_u32 s31,s31,s96\ns_lshl_b32 s31,s31,7\ns_add_u32 s31,s31,s92\n" \
"buffer_load_dwordx4 v4,s[40:43],s31 offen lds\nv_mul_f32 v102,0x3ae38e39,v100\ns_lshr_b32 s100,s33,3\ns_add_u32 s100,s100,3\n" \
"v_permlane16_swap_b32 v104,v102\nv_fma_f32 v102,-v104,v104,v102\nv_max_f32 v102,0,v102\nv_add_f32 v102,0x3727c5ac,v102\n" \
"buffer_load_dwordx4 v5,s[40:43],s31 offen offset:1024 lds\nv_rsq_f32 v100,v102\ns_sub_u32 s101,s100,4\nv_mul_f32 v101,-v104,v100\n" \
"v_pk_fma_f32 v[80:81],v[80:81],v[100:101],v[100:101] op_sel:[0,0,1] op_sel_hi:[1,0,1]\nv_pk_fma_f32 v[82:83],v[82:83],v[100:101],v[100:101] op_sel:[0,0,1] op_sel_hi:[1,0,1]\nv_pk_fma_f32 v[84:85],v[84:85],v[100:101],v[100:101] op_sel:[0,0,1] op_sel_hi:[1,0,1]\nv_pk_fma_f32 v[86:87],v[86:87],v[100:101],v[100:101] op_sel:[0,0,1] op_sel_hi:[1,0,1]\n" \
"buffer_load_dwordx4 v6,s[40:43],s31 offen offset:2048 lds\nv_cvt_pk_bf16_f32 v16,v80,v81\nv_cvt_pk_bf16_f32 v17,v82,v83\nv_cvt_pk_bf16_f32 v18,v84,v85\n" \
"v_cvt_pk_bf16_f32 v19,v86,v87\nv_pk_fma_f32 v[88:89],v[88:89],v[100:101],v[100:101] op_sel:[0,0,1] op_sel_hi:[1,0,1]\nv_pk_fma_f32 v[90:91],v[90:91],v[100:101],v[100:101] op_sel:[0,0,1] op_sel_hi:[1,0,1]\nv_pk_fma_f32 v[92:93],v[92:93],v[100:101],v[100:101] op_sel:[0,0,1] op_sel_hi:[1,0,1]\n" \
"buffer_load_dwordx4 v7,s[40:43],s31 offen offset:3072 lds\nv_pk_fma_f32 v[94:95],v[94:95],v[100:101],v[100:101] op_sel:[0,0,1] op_sel_hi:[1,0,1]\nv_cvt_pk_bf16_f32 v20,v88,v89\nv_cvt_pk_bf16_f32 v21,v90,v91\n" \
"v_cvt_pk_bf16_f32 v22,v92,v93\nv_cvt_pk_bf16_f32 v23,v94,v95\nv_pk_fma_f32 v[96:97],v[96:97],v[100:101],v[100:101] op_sel:[0,0,1] op_sel_hi:[1,0,1]\nv_cvt_pk_bf16_f32 v24,v96,v97\n" \
"s_add_u32 m0,s95,0\ns_and_b32 s95,s97,1\ns_lshr_b32 s97,s97,1\ns_lshl_b32 s97,s97,10\n" \
"s_add_u32 s97,s97,s95\ns_lshl_b32 s97,s97,7\ns_add_u32 s97,s97,s92\nbuffer_load_dwordx4 v4,s[40:43],s97 offen lds\n" \
"s_min_u32 s100,s100,s101\ns_lshl_b32 s101,s100,12\ns_add_u32 s101,s101,s57\nv_cvt_pk_bf16_f32 v34,v66,v67\n" \
"v_pk_add_f32 v[68:69],v[68:69],v[60:61] op_sel_hi:[1,0]\nv_cvt_pk_bf16_f32 v35,v68,v69\nv_pk_add_f32 v[70:71],v[70:71],v[60:61] op_sel_hi:[1,0]\nv_cvt_pk_bf16_f32 v36,v70,v71\n" \
"buffer_load_dwordx4 v5,s[40:43],s97 offen offset:1024 lds\nv_pk_add_f32 v[72:73],v[72:73],v[60:61] op_sel_hi:[1,0]\nv_cvt_pk_bf16_f32 v37,v72,v73\nv_pk_add_f32 v[74:75],v[74:75],v[60:61] op_sel_hi:[1,0]\n" \
"v_cvt_pk_bf16_f32 v38,v74,v75\nv_pk_add_f32 v[76:77],v[76:77],v[60:61] op_sel_hi:[1,0]\nv_cvt_pk_bf16_f32 v39,v76,v77\nv_pk_add_f32 v[78:79],v[78:79],v[60:61] op_sel_hi:[1,0]\n" \
"buffer_load_dwordx4 v6,s[40:43],s97 offen offset:2048 lds\nv_cvt_pk_bf16_f32 v40,v78,v79\nv_and_b32 v48,31,v0\nv_mad_u32_u24 v49,v48,56,0\n" \
"v_sub_u32 v48,v13,v49\nds_write_b128 v48,v[16:19]\nbuffer_load_dwordx4 v7,s[40:43],s97 offen offset:3072 lds\nds_write_b128 v48,v[20:23] offset:512\n" \
"s_add_u32 m0,s101,0\ns_and_b32 s101,s100,1\ns_lshr_b32 s100,s100,1\ns_lshl_b32 s100,s100,10\n" \
"s_add_u32 s100,s100,s101\ns_lshl_b32 s100,s100,7\ns_add_u32 s100,s100,s92\nbuffer_load_dwordx4 v4,s[40:43],s100 offen lds\n" \
"v_and_b32 v49,31,v0\nv_mul_u32_u24 v49,68,v49\nv_sub_u32 v49,v13,v49\nds_write_b32 v49,v24 offset:1024\n" \
"v_bfe_u32 v50,v0,2,2\nv_mul_u32_u24 v50,4896,v50\nv_bfe_u32 v51,v0,4,2\nv_lshl_add_u32 v51,v51,2,s3\n" \
"buffer_load_dwordx4 v5,s[40:43],s100 offen offset:1024 lds\nv_lshl_add_u32 v50,v51,5,v50\nv_and_b32 v52,3,v0\nv_lshl_add_u32 v50,v52,3,v50\n" \
"v_and_b32 v52,0xf0,v1\nv_lshl_add_u32 v51,v51,16,v52\nv_bfrev_b32 v52,1\nv_cmp_gt_u32 vcc,256,v1\n" \
"v_and_b32 v53,31,v0\nbuffer_load_dwordx4 v6,s[40:43],s100 offen offset:2048 lds\ns_waitcnt lgkmcnt(0)\ns_barrier\n" \
"ds_read_b64_tr_b16 v[112:113],v50 offset:9216\nds_read_b64_tr_b16 v[114:115],v50 offset:11664\nds_read_b64_tr_b16 v[116:117],v50 offset:9728\nds_read_b64_tr_b16 v[118:119],v50 offset:12176\n" \
"ds_read_b64_tr_b16 v[120:121],v50 offset:10240\nds_read_b64_tr_b16 v[122:123],v50 offset:12688\nbuffer_load_dwordx4 v7,s[40:43],s100 offen offset:3072 lds\nv_cndmask_b32 v52,v52,v51,vcc\n" \
"v_mul_u32_u24 v53,12,v53\nv_sub_u32 v53,v12,v53\ns_lshl_b32 s24,s34,8\ns_add_u32 s24,s24,9764864\n" \
"s_add_u32 s25,s24,1048576\ns_add_u32 s26,s24,2097152\ns_add_u32 s13,s13,7405568\ns_load_dwordx2 s[4:5],s[0:1],56\n" \
"s_load_dwordx2 s[16:17],s[0:1],64\ns_waitcnt lgkmcnt(0)\nbuffer_store_dwordx4 v[112:115],v51,s[36:39],s24 offen sc1\nbuffer_store_dwordx4 v[116:119],v51,s[36:39],s25 offen sc1\n" \
"buffer_store_dwordx4 v[120:123],v52,s[36:39],s26 offen sc1\nbuffer_store_dwordx4 v[32:35],v12,s[36:39],s13 offen sc1\nbuffer_store_dwordx4 v[36:39],v12,s[36:39],s13 offen offset:512 sc1\nbuffer_store_dword v40,v53,s[36:39],s13 offen offset:1024 sc1\n" \
"s_lshr_b32 s31,s33,3\ns_lshl1_add_u32 s31,s31,0\ns_sub_u32 s96,s31,7\ns_min_u32 s31,s31,s96\n" \
"s_lshl_b32 s96,s31,12\ns_add_u32 s96,s96,s57\ns_add_u32 m0,s96,0\ns_and_b32 s96,s31,1\n" \
"s_lshr_b32 s31,s31,1\ns_lshl_b32 s31,s31,10\ns_add_u32 s31,s31,s96\ns_lshl_b32 s31,s31,7\n" \
"s_add_u32 s31,s31,s92\ns_add_u32 s31,s31,262144\ns_lshr_b32 s97,s33,3\ns_lshl1_add_u32 s97,s97,1\n" \
"s_sub_u32 s95,s97,7\ns_min_u32 s97,s97,s95\ns_lshl_b32 s95,s97,12\ns_add_u32 s95,s95,s57\n" \
"s_lshr_b32 s100,s33,3\ns_lshl1_add_u32 s100,s100,2\ns_sub_u32 s101,s100,7\ns_min_u32 s100,s100,s101\n" \
"s_lshl_b32 s101,s100,12\ns_add_u32 s101,s101,s57\nv_and_b32 v64,63,v0\ns_and_b32 s25,s33,7\n" \
"v_lshrrev_b32 v66,5,v64\nv_and_b32 v67,1,v0\ns_lshl2_add_u32 s25,s25,s3\ns_and_b32 s26,s33,6\n" \
"s_lshr_b32 s27,s3,1\nv_lshl_add_u32 v66,v66,1,v67\ns_mul_i32 s24,s32,1152\ns_lshl_b32 s25,s25,5\n" \
"s_add_u32 s26,s26,s27\ns_mov_b32 s6,9216\ns_mov_b32 s7,0x20000\nv_lshlrev_b32 v68,3,v66\n" \
"s_add_u32 s25,s25,s24\nv_bfe_u32 v72,v0,1,1\nv_lshrrev_b32 v74,2,v64\ns_lshl4_add_u32 s26,s26,s24\n" \
"v_and_b32 v73,12,v0\nv_lshrrev_b32 v71,4,v64\nv_and_b32 v69,7,v0\nbuffer_load_dwordx2 v[80:81],v68,s[4:7],s25 offen\n" \
"v_lshl_add_u32 v72,v72,2,s21\nv_and_b32 v74,12,v74\nv_add_u32 v71,v71,v73\ns_lshl_b32 s24,s3,4\n" \
"v_lshlrev_b32 v70,1,v69\nv_lshl_add_u32 v72,v72,4,v74\ns_mov_b32 s15,s19\nv_add_u32 v71,s24,v71\n" \
"buffer_load_ushort v82,v70,s[4:7],s26 offen offset:1024\ns_mov_b32 s18,4096\ns_mov_b32 s19,0x20000\nv_lshlrev_b32 v74,1,v71\n" \
"s_lshl_b32 s24,s22,8\nbuffer_load_ushort v83,v74,s[16:19],s24 offen\nv_add_u32 v72,s3,v72\nbuffer_load_ushort v84,v74,s[16:19],s24 offen offset:128\n" \
"v_lshlrev_b32_e64 v75,1,v72\nbuffer_load_ushort v85,v75,s[16:19],s24 offen\nv_lshl_add_u32 v73,v67,1,v73\nv_mul_u32_u24 v91,1152,v72\n" \
"v_lshl_add_u32 v91,v73,1,v91\nv_lshlrev_b32_e64 v73,2,v73\nv_add_u32 v73,163712,v73\nds_read_b64 v[86:87],v73\n" \
"s_mul_i32 s24,s22,147456\ns_mul_i32 s25,s20,288\nv_bfe_u32 v58,v0,1,1\ns_add_u32 s24,s24,s25\n" \
"v_mul_u32_u24 v58,544,v58\nv_bfe_u32 v75,v0,2,1\ns_lshl_b32 s25,s21,6\nv_lshl_add_u32 v58,v75,8,v58\n" \
"s_add_u32 s25,s25,s24\nv_bfe_u32 v75,v0,3,2\ns_add_u32 s24,s24,256\nv_lshl_add_u32 v58,v75,6,v58\n" \
"v_add_u32 v91,s24,v91\nv_mul_u32_u24 v90,1152,v71\nv_and_b32 v74,3,v0\nv_lshl_add_u32 v58,v66,4,v58\n" \
"s_mul_i32 s24,s3,1056\nv_lshl_add_u32 v90,v74,3,v90\nv_add_u32 v58,s24,v58\nv_add_u32 v90,s25,v90\n" \
"s_and_b32 s24,s3,1\nv_mul_u32_u24 v60,288,v75\ns_lshl_b32 s24,s24,2\ns_lshr_b32 s25,s3,1\n" \
"v_lshrrev_b32 v75,5,v64\ns_add_u32 s24,s24,s25\nv_lshl_add_u32 v60,v75,6,v60\ns_lshl_b32 s24,s24,5\n" \
"v_lshl_add_u32 v60,v69,2,v60\ns_add_u32 s24,s24,4224\nv_add_u32 v60,s24,v60\ns_bfe_u32 s24,s33,0x10001\n" \
"s_add_u32 s24,s24,s15\ns_add_u32 s24,s24,17\ns_lshl2_add_u32 s24,s24,s3\ns_waitcnt lgkmcnt(0)\n" \
"v_lshrrev_b32 v86,16,v86\ns_lshl_b32 s24,s24,7\nv_mov_b32 v92,v4\nv_mov_b32 v93,v5\n" \
"v_add_u32 v74,s57,v1\nv_or_b32 v95,v86,v87\nv_lshl_add_u32 v62,v64,1,s24\nv_bfrev_b32 v76,1\n" \
"v_cmp_eq_u32 vcc,0,v64\ns_lshl2_add_u32 s24,s34,s3\ns_lshl2_add_u32 s24,s24,65536\nv_cndmask_b32 v76,v76,0,vcc\n" \
"v_mov_b32 v77,1\ns_barrier\nv_mov_b32 v57,0xc0135761\nv_add_u32 v59,0x80000,v1\n" \
"v_lshl_add_u32 v61,v64,3,s23\nv_mov_b32 v63,0\ns_waitcnt vmcnt(31)\nds_read_b64_tr_b16 v[224:225],v15 offset:16384\n" \
"ds_read_b64_tr_b16 v[228:229],v15 offset:16896\nds_read_b64_tr_b16 v[226:227],v15 offset:17408\nds_read_b64_tr_b16 v[230:231],v15 offset:17920\nds_read_b64_tr_b16 v[232:233],v15 offset:18432\n" \
"ds_read_b64_tr_b16 v[236:237],v15 offset:18944\nds_read_b64_tr_b16 v[234:235],v15 offset:19456\nds_read_b64_tr_b16 v[238:239],v15 offset:19968\ns_waitcnt vmcnt(27)\n" \
"ds_read_b64_tr_b16 v[240:241],v15 offset:20480\nds_read_b64_tr_b16 v[244:245],v15 offset:20992\nds_read_b64_tr_b16 v[242:243],v15 offset:21504\nds_read_b64_tr_b16 v[246:247],v15 offset:22016\n" \
"ds_read_b64_tr_b16 v[248:249],v15 offset:22528\nv_bfe_u32 v100,v0,4,2\nds_read_b64_tr_b16 v[252:253],v15 offset:23040\nds_read_b64_tr_b16 v[250:251],v15 offset:23552\n" \
"v_bfe_u32 v101,v0,2,2\nv_lshl_add_u32 v100,v100,3,v101\nv_bfe_u32 v101,v0,3,2\nv_lshl_add_u32 v100,v100,2,v101\n" \
"v_and_b32 v101,3,v0\nv_lshl_add_u32 v100,v100,2,v101\nv_lshlrev_b32 v100,3,v100\nv_xor_b32 v8,0,v100\n" \
"v_add_u32 v8,s57,v8\nv_mov_b32 v88,v8\ns_waitcnt vmcnt(5)\nbuffer_store_dword v77,v76,s[36:39],s24 offen sc1\n" \
"ds_read_b64_tr_b16 v[254:255],v15 offset:24064\nv_xor_b32 v9,32,v100\nv_add_u32 v9,s57,v9\nv_xor_b32 v10,64,v100\n" \
"v_add_u32 v10,s57,v10\nv_xor_b32 v11,96,v100\nv_add_u32 v11,s57,v11\ns_waitcnt vmcnt(5)\n" \
"v_and_b32 v53,0xffff0000,v80\nv_and_b32 v55,0xffff0000,v81\nv_lshlrev_b32 v52,16,v80\nv_lshlrev_b32 v54,16,v81\n" \
"s_waitcnt vmcnt(4)\nv_lshlrev_b32 v56,16,v82\ns_waitcnt vmcnt(2)\nv_lshl_or_b32 v89,v84,16,v83\n" \
"ds_write_b128 v74,v[88:91] offset:28672\ns_waitcnt vmcnt(1)\nv_lshlrev_b32_e64 v94,16,v85\nds_write_b128 v74,v[92:95] offset:29696\n" \
"ds_read_b64_tr_b16 v[128:129],v8\nds_read_b64_tr_b16 v[130:131],v8 offset:512\nds_read_b64_tr_b16 v[132:133],v9\nds_read_b64_tr_b16 v[134:135],v9 offset:512\n" \
"ds_read_b64_tr_b16 v[136:137],v10\nds_read_b64_tr_b16 v[138:139],v10 offset:512\nds_read_b64_tr_b16 v[140:141],v11\nds_read_b64_tr_b16 v[142:143],v11 offset:512\n" \
"ds_read_b64_tr_b16 v[144:145],v8 offset:4096\nds_read_b64_tr_b16 v[146:147],v8 offset:4608\nds_read_b64_tr_b16 v[148:149],v9 offset:4096\nds_read_b64_tr_b16 v[150:151],v9 offset:4608\n" \
"ds_read_b64_tr_b16 v[152:153],v10 offset:4096\nds_read_b64_tr_b16 v[154:155],v10 offset:4608\nds_read_b64_tr_b16 v[156:157],v11 offset:4096\nds_read_b64_tr_b16 v[158:159],v11 offset:4608\n" \
"ds_read_b64_tr_b16 v[160:161],v8 offset:8192\nds_read_b64_tr_b16 v[162:163],v8 offset:8704\nds_read_b64_tr_b16 v[164:165],v9 offset:8192\nds_read_b64_tr_b16 v[166:167],v9 offset:8704\n" \
"ds_read_b64_tr_b16 v[168:169],v10 offset:8192\nds_read_b64_tr_b16 v[170:171],v10 offset:8704\nds_read_b64_tr_b16 v[172:173],v11 offset:8192\nds_read_b64_tr_b16 v[174:175],v11 offset:8704\n" \
"ds_read_b64_tr_b16 v[176:177],v8 offset:12288\nds_read_b64_tr_b16 v[178:179],v8 offset:12800\nds_read_b64_tr_b16 v[180:181],v9 offset:12288\nds_read_b64_tr_b16 v[182:183],v9 offset:12800\n" \
"ds_read_b64_tr_b16 v[184:185],v10 offset:12288\nds_read_b64_tr_b16 v[186:187],v10 offset:12800\nds_read_b64_tr_b16 v[188:189],v11 offset:12288\nds_read_b64_tr_b16 v[190:191],v11 offset:12800\n" \
"s_waitcnt lgkmcnt(0)\nbuffer_load_dwordx4 v4,s[40:43],s31 offen lds\nbuffer_load_dwordx4 v5,s[40:43],s31 offen offset:1024 lds\nbuffer_load_dwordx4 v6,s[40:43],s31 offen offset:2048 lds\n" \
"buffer_load_dwordx4 v7,s[40:43],s31 offen offset:3072 lds\ns_add_u32 m0,s95,0\ns_and_b32 s95,s97,1\ns_lshr_b32 s97,s97,1\n" \
"s_lshl_b32 s97,s97,10\ns_add_u32 s97,s97,s95\ns_lshl_b32 s97,s97,7\ns_add_u32 s97,s97,s92\n" \
"s_add_u32 s97,s97,262144\nbuffer_load_dwordx4 v4,s[40:43],s97 offen lds\nbuffer_load_dwordx4 v5,s[40:43],s97 offen offset:1024 lds\nbuffer_load_dwordx4 v6,s[40:43],s97 offen offset:2048 lds\n" \
"buffer_load_dwordx4 v7,s[40:43],s97 offen offset:3072 lds\ns_add_u32 m0,s101,0\ns_and_b32 s101,s100,1\ns_lshr_b32 s100,s100,1\n" \
"s_lshl_b32 s100,s100,10\ns_add_u32 s100,s100,s101\ns_lshl_b32 s100,s100,7\ns_add_u32 s100,s100,s92\n" \
"s_add_u32 s100,s100,262144\nbuffer_load_dwordx4 v4,s[40:43],s100 offen lds\nbuffer_load_dwordx4 v5,s[40:43],s100 offen offset:1024 lds\nbuffer_load_dwordx4 v6,s[40:43],s100 offen offset:2048 lds\n" \
"buffer_load_dwordx4 v7,s[40:43],s100 offen offset:3072 lds\ns_lshr_b32 s31,s33,3\ns_lshl1_add_u32 s31,s31,3\ns_sub_u32 s96,s31,7\n" \
"s_min_u32 s31,s31,s96\ns_lshl_b32 s96,s31,12\ns_add_u32 s96,s96,s57\ns_add_u32 m0,s96,0\n" \
"s_and_b32 s96,s31,1\ns_lshr_b32 s31,s31,1\ns_lshl_b32 s31,s31,10\ns_add_u32 s31,s31,s96\n" \
"s_lshl_b32 s31,s31,7\ns_add_u32 s31,s31,s92\ns_add_u32 s31,s31,262144\nbuffer_load_dwordx4 v4,s[40:43],s31 offen lds\n" \
"buffer_load_dwordx4 v5,s[40:43],s31 offen offset:1024 lds\nbuffer_load_dwordx4 v6,s[40:43],s31 offen offset:2048 lds\nbuffer_load_dwordx4 v7,s[40:43],s31 offen offset:3072 lds\ns_lshr_b32 s97,s33,3\n" \
"s_lshl1_add_u32 s97,s97,4\ns_sub_u32 s95,s97,7\ns_min_u32 s97,s97,s95\ns_lshl_b32 s95,s97,12\n" \
"s_add_u32 s95,s95,s57\ns_add_u32 m0,s95,0\ns_and_b32 s95,s97,1\ns_lshr_b32 s97,s97,1\n" \
"s_lshl_b32 s97,s97,10\ns_add_u32 s97,s97,s95\ns_lshl_b32 s97,s97,7\ns_add_u32 s97,s97,s92\n" \
"s_add_u32 s97,s97,262144\nbuffer_load_dwordx4 v4,s[40:43],s97 offen lds\nbuffer_load_dwordx4 v5,s[40:43],s97 offen offset:1024 lds\nbuffer_load_dwordx4 v6,s[40:43],s97 offen offset:2048 lds\n" \
"buffer_load_dwordx4 v7,s[40:43],s97 offen offset:3072 lds\ns_lshr_b32 s100,s33,3\ns_lshl1_add_u32 s100,s100,5\ns_sub_u32 s101,s100,7\n" \
"s_min_u32 s100,s100,s101\ns_lshl_b32 s101,s100,12\ns_add_u32 s101,s101,s57\ns_add_u32 m0,s101,0\n" \
"s_and_b32 s101,s100,1\ns_lshr_b32 s100,s100,1\ns_lshl_b32 s100,s100,10\ns_add_u32 s100,s100,s101\n" \
"s_lshl_b32 s100,s100,7\ns_add_u32 s100,s100,s92\ns_add_u32 s100,s100,262144\nbuffer_load_dwordx4 v4,s[40:43],s100 offen lds\n" \
"buffer_load_dwordx4 v5,s[40:43],s100 offen offset:1024 lds\nbuffer_load_dwordx4 v6,s[40:43],s100 offen offset:2048 lds\nbuffer_load_dwordx4 v7,s[40:43],s100 offen offset:3072 lds\ns_lshr_b32 s31,s33,3\n" \
"s_lshl1_add_u32 s31,s31,6\ns_sub_u32 s96,s31,7\ns_min_u32 s31,s31,s96\ns_lshl_b32 s96,s31,12\n" \
"s_add_u32 s96,s96,s57\ns_add_u32 m0,s96,0\ns_and_b32 s96,s31,1\ns_lshr_b32 s31,s31,1\n" \
"s_lshl_b32 s31,s31,10\ns_add_u32 s31,s31,s96\ns_lshl_b32 s31,s31,7\ns_add_u32 s31,s31,s92\n" \
"s_add_u32 s31,s31,262144\nbuffer_load_dwordx4 v4,s[40:43],s31 offen lds\nbuffer_load_dwordx4 v5,s[40:43],s31 offen offset:1024 lds\nbuffer_load_dwordx4 v6,s[40:43],s31 offen offset:2048 lds\n" \
"buffer_load_dwordx4 v7,s[40:43],s31 offen offset:3072 lds\n.Lgemm_%=:\ns_add_u32 s4,s36,s48\ns_addc_u32 s5,s37,0\n" \
"s_add_u32 s6,s36,s49\ns_addc_u32 s7,s37,0\ns_add_u32 s8,s36,s50\ns_addc_u32 s9,s37,0\n" \
"s_add_u32 s10,s36,s51\ns_addc_u32 s11,s37,0\ns_add_u32 s12,s36,s68\ns_addc_u32 s13,s37,0\n" \
"s_add_u32 s14,s36,s69\ns_addc_u32 s15,s37,0\ns_add_u32 s16,s36,s70\ns_addc_u32 s17,s37,0\n" \
"s_memrealtime s[94:95]\nglobal_load_dwordx4 v[64:67],v59,s[4:5] sc1\nglobal_load_dwordx4 v[68:71],v59,s[4:5] offset:1024 sc1\nglobal_load_dwordx4 v[72:75],v59,s[4:5] offset:2048 sc1\n" \
"global_load_dwordx4 v[76:79],v59,s[4:5] offset:3072 sc1\nglobal_load_dwordx4 v[80:83],v59,s[6:7] sc1\nglobal_load_dwordx4 v[84:87],v59,s[6:7] offset:1024 sc1\nglobal_load_dwordx4 v[88:91],v59,s[6:7] offset:2048 sc1\n" \
"global_load_dwordx4 v[92:95],v59,s[6:7] offset:3072 sc1\nglobal_load_dwordx4 v[96:99],v59,s[8:9] sc1\nglobal_load_dwordx4 v[100:103],v59,s[8:9] offset:1024 sc1\nglobal_load_dwordx4 v[104:107],v59,s[8:9] offset:2048 sc1\n" \
"global_load_dwordx4 v[108:111],v59,s[8:9] offset:3072 sc1\nglobal_load_dwordx4 v[112:115],v59,s[10:11] sc1\nglobal_load_dwordx4 v[116:119],v59,s[10:11] offset:1024 sc1\nglobal_load_dwordx4 v[120:123],v59,s[10:11] offset:2048 sc1\n" \
"global_load_dwordx4 v[124:127],v59,s[10:11] offset:3072 sc1\ns_branch .Lz0_%=\n.Lzr_%=:\nv_and_b32 v0,0x3ff,v59\n" \
"v_lshlrev_b32 v0,0,v0\nv_bfrev_b32 v1,1\ns_waitcnt lgkmcnt(0)\n.Lh_%=:\n" \
"buffer_load_dwordx4 v[4:7],v0,s[36:39],s87 offen sc1\ns_memrealtime s[96:97]\ns_waitcnt vmcnt(0) lgkmcnt(0)\nv_bitop3_b32 v4,v4,v5,v6 bitop3:0x80\n" \
"v_and_b32 v4,v4,v7\nv_cmp_ne_u32 vcc,0,v4\nv_max3_u32 v2,v64,v68,v72\nv_max3_u32 v2,v2,v76,v80\n" \
"v_max3_u32 v2,v2,v84,v88\nv_max3_u32 v2,v2,v92,v96\nv_max3_u32 v2,v2,v100,v104\nv_max3_u32 v2,v2,v108,v112\n" \
"v_max3_u32 v2,v2,v116,v120\nv_max_u32 v2,v2,v124\nv_cndmask_b32 v0,v0,v1,vcc\nv_cmp_eq_u32 vcc,-1,v2\n" \
"v_cmp_ne_u32 s[100:101],v0,v1\ns_sub_u32 s96,s96,s94\ns_cbranch_vccz .Lj_%=\nv_cmpx_eq_u32 vcc,-1,v64\n" \
"global_load_dwordx4 v[64:67],v59,s[4:5] sc1\ns_mov_b64 exec,-1\nv_cmpx_eq_u32 vcc,-1,v68\nglobal_load_dwordx4 v[68:71],v59,s[4:5] offset:1024 sc1\n" \
"s_mov_b64 exec,-1\nv_cmpx_eq_u32 vcc,-1,v72\nglobal_load_dwordx4 v[72:75],v59,s[4:5] offset:2048 sc1\ns_mov_b64 exec,-1\n" \
"v_cmpx_eq_u32 vcc,-1,v76\nglobal_load_dwordx4 v[76:79],v59,s[4:5] offset:3072 sc1\ns_mov_b64 exec,-1\nv_cmpx_eq_u32 vcc,-1,v80\n" \
"global_load_dwordx4 v[80:83],v59,s[6:7] sc1\ns_mov_b64 exec,-1\nv_cmpx_eq_u32 vcc,-1,v84\nglobal_load_dwordx4 v[84:87],v59,s[6:7] offset:1024 sc1\n" \
"s_mov_b64 exec,-1\nv_cmpx_eq_u32 vcc,-1,v88\nglobal_load_dwordx4 v[88:91],v59,s[6:7] offset:2048 sc1\ns_mov_b64 exec,-1\n" \
"v_cmpx_eq_u32 vcc,-1,v92\nglobal_load_dwordx4 v[92:95],v59,s[6:7] offset:3072 sc1\ns_mov_b64 exec,-1\nv_cmpx_eq_u32 vcc,-1,v96\n" \
"global_load_dwordx4 v[96:99],v59,s[8:9] sc1\ns_mov_b64 exec,-1\nv_cmpx_eq_u32 vcc,-1,v100\nglobal_load_dwordx4 v[100:103],v59,s[8:9] offset:1024 sc1\n" \
"s_mov_b64 exec,-1\nv_cmpx_eq_u32 vcc,-1,v104\nglobal_load_dwordx4 v[104:107],v59,s[8:9] offset:2048 sc1\ns_mov_b64 exec,-1\n" \
"v_cmpx_eq_u32 vcc,-1,v108\nglobal_load_dwordx4 v[108:111],v59,s[8:9] offset:3072 sc1\ns_mov_b64 exec,-1\nv_cmpx_eq_u32 vcc,-1,v112\n" \
"global_load_dwordx4 v[112:115],v59,s[10:11] sc1\ns_mov_b64 exec,-1\nv_cmpx_eq_u32 vcc,-1,v116\nglobal_load_dwordx4 v[116:119],v59,s[10:11] offset:1024 sc1\n" \
"s_mov_b64 exec,-1\nv_cmpx_eq_u32 vcc,-1,v120\nglobal_load_dwordx4 v[120:123],v59,s[10:11] offset:2048 sc1\ns_mov_b64 exec,-1\n" \
"v_cmpx_eq_u32 vcc,-1,v124\nglobal_load_dwordx4 v[124:127],v59,s[10:11] offset:3072 sc1\ns_mov_b64 exec,-1\n.Lj_%=:\n" \
"s_cmp_gt_u32 s96,0x4c4b40\ns_cbranch_scc1 .Lg_%=\ns_cmp_lg_u64 s[100:101],0\ns_cbranch_scc1 .Lh_%=\n" \
".Lg_%=:\ns_waitcnt vmcnt(0)\ns_mov_b32 s33,s86\ns_nop 0\n" \
"s_mov_b32 m0,s56\ns_and_b32 s95,s89,7\ns_andn2_b32 s101,s89,7\ns_mul_i32 s95,s95,409600\n" \
"s_add_u32 s95,s95,s52\ns_mov_b32 s86,0x11111111\ns_mov_b32 s87,0x11111111\nv_add_u32 v59,s85,v59\n" \
"s_add_u32 s100,s90,73728\ns_mov_b32 s35,73728\ns_mov_b32 s34,s91\ns_waitcnt lgkmcnt(0)\n" \
"s_cmp_eq_u32 s84,0\ns_cbranch_scc0 .Ls_%=\n.Lf_%=:\ns_waitcnt vmcnt(15)\n" \
"v_mfma_f32_16x16x32_bf16 v[0:3],v[64:67],a[0:3],0\nds_read_b32 v44,v60\ns_waitcnt vmcnt(14)\nv_mfma_f32_16x16x32_bf16 v[0:3],v[68:71],a[16:19],v[0:3]\n" \
"ds_read_b32 v45,v60 offset:9568\nv_add_f32 v40,1.0,v40\nv_mfma_f32_16x16x32_bf16 v[4:7],v[64:67],a[4:7],0\nds_read_b32 v46,v60 offset:19136\n" \
"v_add_u32 v59,0x10000,v59\nv_mfma_f32_16x16x32_bf16 v[4:7],v[68:71],a[20:23],v[4:7]\nds_read_b32 v47,v60 offset:28704\nv_and_b32 v59,0x7ffff,v59\n" \
"v_mfma_f32_16x16x32_bf16 v[8:11],v[64:67],a[8:11],0\nv_add_f32 v41,1.0,v41\nv_add_f32 v42,1.0,v42\nv_mfma_f32_16x16x32_bf16 v[8:11],v[68:71],a[24:27],v[8:11]\n" \
"v_add_f32 v43,1.0,v43\nds_write_addtid_b32 v20 offset:5344\nv_mfma_f32_16x16x32_bf16 v[12:15],v[64:67],a[12:15],0\nv_rcp_f32 v40,v40\n" \
"v_mfma_f32_16x16x32_bf16 v[12:15],v[68:71],a[28:31],v[12:15]\nv_rcp_f32 v41,v41\nv_mfma_f32_16x16x32_bf16 v[16:19],v[64:67],v[192:195],0\nglobal_load_dwordx4 v[64:67],v59,s[4:5] sc1\n" \
"ds_write_addtid_b32 v21 offset:5888\nv_mfma_f32_16x16x32_bf16 v[16:19],v[68:71],v[196:199],v[16:19]\nds_write_addtid_b32 v22 offset:5600\ns_waitcnt vmcnt(14)\n" \
"v_mfma_f32_16x16x32_bf16 v[0:3],v[72:75],a[32:35],v[0:3]\nds_write_addtid_b32 v23 offset:6144\ns_waitcnt vmcnt(13)\nv_mfma_f32_16x16x32_bf16 v[0:3],v[76:79],a[48:51],v[0:3]\n" \
"v_rcp_f32 v42,v42\nv_mfma_f32_16x16x32_bf16 v[4:7],v[72:75],a[36:39],v[4:7]\nv_rcp_f32 v43,v43\nv_mfma_f32_16x16x32_bf16 v[4:7],v[76:79],a[52:55],v[4:7]\n" \
"global_load_dwordx4 v[68:71],v59,s[4:5] offset:1024 sc1\nv_mul_f32 v36,v36,v40\nv_mfma_f32_16x16x32_bf16 v[8:11],v[72:75],a[40:43],v[8:11]\nv_mul_f32 v37,v37,v41\n" \
"v_mul_f32 v38,v38,v42\nv_mfma_f32_16x16x32_bf16 v[8:11],v[76:79],a[56:59],v[8:11]\nv_mul_f32 v39,v39,v43\nv_cvt_pk_bf16_f32 v40,v36,v37\n" \
"v_mfma_f32_16x16x32_bf16 v[12:15],v[72:75],a[44:47],v[12:15]\nds_write_addtid_b32 v24 offset:6400\nv_cvt_pk_bf16_f32 v41,v38,v39\nv_mfma_f32_16x16x32_bf16 v[12:15],v[76:79],a[60:63],v[12:15]\n" \
"ds_write_addtid_b32 v25 offset:6944\nbuffer_store_dwordx2 v[40:41],v61,s[36:39],s52 offen sc1\nv_mfma_f32_16x16x32_bf16 v[16:19],v[72:75],v[200:203],v[16:19]\nglobal_load_dwordx4 v[72:75],v59,s[4:5] offset:2048 sc1\n" \
"ds_write_addtid_b32 v26 offset:6656\nv_mfma_f32_16x16x32_bf16 v[16:19],v[76:79],v[204:207],v[16:19]\nds_write_addtid_b32 v27 offset:7200\ns_waitcnt vmcnt(15)\n" \
"v_mfma_f32_16x16x32_bf16 v[0:3],v[80:83],a[64:67],v[0:3]\nds_write_addtid_b32 v28 offset:7456\ns_waitcnt vmcnt(14)\nv_mfma_f32_16x16x32_bf16 v[0:3],v[84:87],a[80:83],v[0:3]\n" \
"ds_write_addtid_b32 v29 offset:8000\ns_waitcnt lgkmcnt(10)\nv_mfma_f32_16x16x32_bf16 v[4:7],v[80:83],a[68:71],v[4:7]\nds_write_addtid_b32 v30 offset:7712\n" \
"v_add_f32 v44,v44,v45\nv_mfma_f32_16x16x32_bf16 v[4:7],v[84:87],a[84:87],v[4:7]\nglobal_load_dwordx4 v[76:79],v59,s[4:5] offset:3072 sc1\nds_write_addtid_b32 v31 offset:8256\n" \
"v_mfma_f32_16x16x32_bf16 v[8:11],v[80:83],a[72:75],v[8:11]\nds_write_addtid_b32 v32 offset:8512\nv_add_f32 v46,v46,v47\nv_mfma_f32_16x16x32_bf16 v[8:11],v[84:87],a[88:91],v[8:11]\n" \
"ds_write_addtid_b32 v33 offset:9056\nv_add_f32 v44,v44,v56\nv_mfma_f32_16x16x32_bf16 v[12:15],v[80:83],a[76:79],v[12:15]\nds_write_addtid_b32 v34 offset:8768\n" \
"v_add_f32 v44,v44,v46\nv_mfma_f32_16x16x32_bf16 v[12:15],v[84:87],a[92:95],v[12:15]\nds_write_addtid_b32 v35 offset:9312\nv_mul_f32 v45,v44,v44\n" \
"v_mfma_f32_16x16x32_bf16 v[16:19],v[80:83],v[208:211],v[16:19]\nglobal_load_dwordx4 v[80:83],v59,s[6:7] sc1\nv_fma_f32 v45,v45,s55,v57\nv_mfma_f32_16x16x32_bf16 v[16:19],v[84:87],v[212:215],v[16:19]\n" \
"v_mul_f32 v45,v44,v45\ns_waitcnt vmcnt(15)\nv_mfma_f32_16x16x32_bf16 v[0:3],v[88:91],a[96:99],v[0:3]\ns_waitcnt vmcnt(14)\n" \
"v_mfma_f32_16x16x32_bf16 v[0:3],v[92:95],a[112:115],v[0:3]\nv_exp_f32 v45,v45\nv_mfma_f32_16x16x32_bf16 v[4:7],v[88:91],a[100:103],v[4:7]\nv_add_f32 v45,1.0,v45\n" \
"v_mfma_f32_16x16x32_bf16 v[4:7],v[92:95],a[116:119],v[4:7]\nglobal_load_dwordx4 v[84:87],v59,s[6:7] offset:1024 sc1\nv_mfma_f32_16x16x32_bf16 v[8:11],v[88:91],a[104:107],v[8:11]\ns_waitcnt lgkmcnt(0)\n" \
"s_barrier\nv_mfma_f32_16x16x32_bf16 v[8:11],v[92:95],a[120:123],v[8:11]\nv_rcp_f32 v45,v45\nv_mfma_f32_16x16x32_bf16 v[12:15],v[88:91],a[108:111],v[12:15]\n" \
"v_mul_f32 v44,v44,v45\nv_cvt_pk_bf16_f32 v45,v44,v44\nv_mfma_f32_16x16x32_bf16 v[12:15],v[92:95],a[124:127],v[12:15]\nbuffer_store_short v45,v62,s[36:39],s52 offen sc1\n" \
"ds_read_b128 v[36:39],v58 offset:5344\nv_mfma_f32_16x16x32_bf16 v[16:19],v[88:91],v[216:219],v[16:19]\nglobal_load_dwordx4 v[88:91],v59,s[6:7] offset:2048 sc1\nds_read_b128 v[40:43],v58 offset:14912\n" \
"v_mfma_f32_16x16x32_bf16 v[16:19],v[92:95],v[220:223],v[16:19]\nds_read_b128 v[44:47],v58 offset:24480\ns_waitcnt vmcnt(16)\nv_mfma_f32_16x16x32_bf16 v[0:3],v[96:99],a[128:131],v[0:3]\n" \
"ds_read_b128 v[48:51],v58 offset:34048\ns_waitcnt vmcnt(15)\nv_mfma_f32_16x16x32_bf16 v[0:3],v[100:103],a[144:147],v[0:3]\nv_mfma_f32_16x16x32_bf16 v[4:7],v[96:99],a[132:135],v[4:7]\n" \
"v_mfma_f32_16x16x32_bf16 v[4:7],v[100:103],a[148:151],v[4:7]\nglobal_load_dwordx4 v[92:95],v59,s[6:7] offset:3072 sc1\nv_mfma_f32_16x16x32_bf16 v[8:11],v[96:99],a[136:139],v[8:11]\nv_mfma_f32_16x16x32_bf16 v[8:11],v[100:103],a[152:155],v[8:11]\n" \
"s_cmp_eq_u32 s52,s83\ns_cbranch_scc1 .Lxq5_%=\n.Lyq5_%=:\nv_mfma_f32_16x16x32_bf16 v[12:15],v[96:99],a[140:143],v[12:15]\n" \
"s_waitcnt lgkmcnt(2)\nv_add_f32 v36,v36,v40\nv_mfma_f32_16x16x32_bf16 v[12:15],v[100:103],a[156:159],v[12:15]\nv_add_f32 v37,v37,v41\n" \
"v_add_f32 v38,v38,v42\nv_mfma_f32_16x16x32_bf16 v[16:19],v[96:99],v[224:227],v[16:19]\nglobal_load_dwordx4 v[96:99],v59,s[8:9] sc1\nv_add_f32 v39,v39,v43\n" \
"v_mfma_f32_16x16x32_bf16 v[16:19],v[100:103],v[228:231],v[16:19]\nv_add_f32 v36,v36,v52\ns_waitcnt vmcnt(16)\nv_mfma_f32_16x16x32_bf16 v[0:3],v[104:107],a[160:163],v[0:3]\n" \
"v_add_f32 v37,v37,v53\ns_waitcnt vmcnt(15)\nv_mfma_f32_16x16x32_bf16 v[0:3],v[108:111],a[176:179],v[0:3]\nv_add_f32 v38,v38,v54\n" \
"v_add_f32 v39,v39,v55\nv_mfma_f32_16x16x32_bf16 v[4:7],v[104:107],a[164:167],v[4:7]\ns_waitcnt lgkmcnt(0)\nv_add_f32 v44,v44,v48\n" \
"v_mfma_f32_16x16x32_bf16 v[4:7],v[108:111],a[180:183],v[4:7]\nglobal_load_dwordx4 v[100:103],v59,s[8:9] offset:1024 sc1\nv_add_f32 v45,v45,v49\nv_mfma_f32_16x16x32_bf16 v[8:11],v[104:107],a[168:171],v[8:11]\n" \
"v_add_f32 v46,v46,v50\nv_add_f32 v47,v47,v51\nv_mfma_f32_16x16x32_bf16 v[8:11],v[108:111],a[184:187],v[8:11]\nv_add_f32 v36,v36,v44\n" \
"v_add_f32 v37,v37,v45\nv_mfma_f32_16x16x32_bf16 v[12:15],v[104:107],a[172:175],v[12:15]\nv_add_f32 v38,v38,v46\nv_add_f32 v39,v39,v47\n" \
"v_mfma_f32_16x16x32_bf16 v[12:15],v[108:111],a[188:191],v[12:15]\nv_mul_f32 v40,v36,v36\nv_mul_f32 v41,v37,v37\nv_mfma_f32_16x16x32_bf16 v[16:19],v[104:107],v[232:235],v[16:19]\n" \
"global_load_dwordx4 v[104:107],v59,s[8:9] offset:2048 sc1\nv_mul_f32 v42,v38,v38\nv_mfma_f32_16x16x32_bf16 v[16:19],v[108:111],v[236:239],v[16:19]\nv_mul_f32 v43,v39,v39\n" \
"s_waitcnt vmcnt(16)\nv_mfma_f32_16x16x32_bf16 v[0:3],v[112:115],a[192:195],v[0:3]\nv_fma_f32 v40,v40,s55,v57\ns_waitcnt vmcnt(15)\n" \
"v_mfma_f32_16x16x32_bf16 v[0:3],v[116:119],a[208:211],v[0:3]\nv_fma_f32 v41,v41,s55,v57\nv_fma_f32 v42,v42,s55,v57\nv_mfma_f32_16x16x32_bf16 v[4:7],v[112:115],a[196:199],v[4:7]\n" \
"v_fma_f32 v43,v43,s55,v57\nv_mul_f32 v40,v36,v40\nv_mfma_f32_16x16x32_bf16 v[4:7],v[116:119],a[212:215],v[4:7]\nglobal_load_dwordx4 v[108:111],v59,s[8:9] offset:3072 sc1\n" \
"v_mul_f32 v41,v37,v41\nv_mfma_f32_16x16x32_bf16 v[8:11],v[112:115],a[200:203],v[8:11]\nv_mul_f32 v42,v38,v42\nv_mul_f32 v43,v39,v43\n" \
"v_mfma_f32_16x16x32_bf16 v[8:11],v[116:119],a[216:219],v[8:11]\nv_exp_f32 v40,v40\nv_mfma_f32_16x16x32_bf16 v[12:15],v[112:115],a[204:207],v[12:15]\nv_exp_f32 v41,v41\n" \
"v_mfma_f32_16x16x32_bf16 v[12:15],v[116:119],a[220:223],v[12:15]\nv_exp_f32 v42,v42\nv_mfma_f32_16x16x32_bf16 v[16:19],v[112:115],v[240:243],v[16:19]\nglobal_load_dwordx4 v[112:115],v59,s[10:11] sc1\n" \
"v_mfma_f32_16x16x32_bf16 v[16:19],v[116:119],v[244:247],v[16:19]\ns_waitcnt vmcnt(16)\nv_mfma_f32_16x16x32_bf16 v[0:3],v[120:123],a[224:227],v[0:3]\ns_waitcnt vmcnt(15)\n" \
"v_mfma_f32_16x16x32_bf16 v[0:3],v[124:127],a[240:243],v[0:3]\nv_exp_f32 v43,v43\nv_mfma_f32_16x16x32_bf16 v[4:7],v[120:123],a[228:231],v[4:7]\nv_add_f32 v40,1.0,v40\n" \
"v_add_f32 v41,1.0,v41\nv_mfma_f32_16x16x32_bf16 v[4:7],v[124:127],a[244:247],v[4:7]\nglobal_load_dwordx4 v[116:119],v59,s[10:11] offset:1024 sc1\nv_add_f32 v42,1.0,v42\n" \
"v_mfma_f32_16x16x32_bf16 v[8:11],v[120:123],a[232:235],v[8:11]\nv_add_f32 v43,1.0,v43\nv_mfma_f32_16x16x32_bf16 v[8:11],v[124:127],a[248:251],v[8:11]\nv_rcp_f32 v40,v40\n" \
"v_mfma_f32_16x16x32_bf16 v[12:15],v[120:123],a[236:239],v[12:15]\ns_cmp_eq_u32 s52,s95\ns_cbranch_scc1 .Lxt5_%=\n.Lyt5_%=:\n" \
"v_mfma_f32_16x16x32_bf16 v[12:15],v[124:127],a[252:255],v[12:15]\nv_rcp_f32 v41,v41\nv_mfma_f32_16x16x32_bf16 v[16:19],v[120:123],v[248:251],v[16:19]\nglobal_load_dwordx4 v[120:123],v59,s[10:11] offset:2048 sc1\n" \
"v_rcp_f32 v42,v42\nv_mfma_f32_16x16x32_bf16 v[16:19],v[124:127],v[252:255],v[16:19]\nglobal_load_dwordx4 v[124:127],v59,s[10:11] offset:3072 sc1\nv_rcp_f32 v43,v43\n" \
"s_add_u32 s52,s52,409600\ns_cmp_eq_u32 s52,s53\ns_cbranch_scc1 .Lls_%=\n.Ls_%=:\n" \
"s_waitcnt vmcnt(17)\nv_mfma_f32_16x16x32_bf16 v[20:23],v[64:67],a[0:3],0\nv_mul_f32 v36,v36,v40\ns_waitcnt vmcnt(16)\n" \
"v_mfma_f32_16x16x32_bf16 v[20:23],v[68:71],a[16:19],v[20:23]\nv_add_u32 v59,0x10000,v59\nv_mul_f32 v37,v37,v41\nv_mfma_f32_16x16x32_bf16 v[24:27],v[64:67],a[4:7],0\n" \
"v_and_b32 v59,0x7ffff,v59\nv_mul_f32 v38,v38,v42\nv_mfma_f32_16x16x32_bf16 v[24:27],v[68:71],a[20:23],v[24:27]\nv_mul_f32 v39,v39,v43\n" \
"v_cvt_pk_bf16_f32 v40,v36,v37\nv_mfma_f32_16x16x32_bf16 v[28:31],v[64:67],a[8:11],0\nds_write_addtid_b32 v0\nv_cvt_pk_bf16_f32 v41,v38,v39\n" \
"v_mfma_f32_16x16x32_bf16 v[28:31],v[68:71],a[24:27],v[28:31]\nds_write_addtid_b32 v1 offset:544\nbuffer_store_dwordx2 v[40:41],v61,s[36:39],s52 offen sc1\nv_mfma_f32_16x16x32_bf16 v[32:35],v[64:67],a[12:15],0\n" \
"global_load_dwordx4 v[64:67],v59,s[4:5] sc1\nds_write_addtid_b32 v2 offset:256\nv_mfma_f32_16x16x32_bf16 v[32:35],v[68:71],a[28:31],v[32:35]\nds_write_addtid_b32 v3 offset:800\n" \
"s_waitcnt vmcnt(16)\nv_mfma_f32_16x16x32_bf16 v[20:23],v[72:75],a[32:35],v[20:23]\nds_write_addtid_b32 v4 offset:1056\ns_waitcnt vmcnt(15)\n" \
"v_mfma_f32_16x16x32_bf16 v[20:23],v[76:79],a[48:51],v[20:23]\nds_write_addtid_b32 v5 offset:1600\nv_mfma_f32_16x16x32_bf16 v[24:27],v[72:75],a[36:39],v[24:27]\nglobal_load_dwordx4 v[68:71],v59,s[4:5] offset:1024 sc1\n" \
"ds_write_addtid_b32 v6 offset:1312\nv_mfma_f32_16x16x32_bf16 v[24:27],v[76:79],a[52:55],v[24:27]\nds_write_addtid_b32 v7 offset:1856\nv_mfma_f32_16x16x32_bf16 v[28:31],v[72:75],a[40:43],v[28:31]\n" \
"ds_write_addtid_b32 v8 offset:2112\nv_mfma_f32_16x16x32_bf16 v[28:31],v[76:79],a[56:59],v[28:31]\nds_write_addtid_b32 v9 offset:2656\nv_mfma_f32_16x16x32_bf16 v[32:35],v[72:75],a[44:47],v[32:35]\n" \
"global_load_dwordx4 v[72:75],v59,s[4:5] offset:2048 sc1\nds_write_addtid_b32 v10 offset:2368\nv_mfma_f32_16x16x32_bf16 v[32:35],v[76:79],a[60:63],v[32:35]\nds_write_addtid_b32 v11 offset:2912\n" \
"s_waitcnt vmcnt(16)\nv_mfma_f32_16x16x32_bf16 v[20:23],v[80:83],a[64:67],v[20:23]\nds_write_addtid_b32 v12 offset:3168\ns_waitcnt vmcnt(15)\n" \
"v_mfma_f32_16x16x32_bf16 v[20:23],v[84:87],a[80:83],v[20:23]\nds_write_addtid_b32 v13 offset:3712\nv_mfma_f32_16x16x32_bf16 v[24:27],v[80:83],a[68:71],v[24:27]\nglobal_load_dwordx4 v[76:79],v59,s[4:5] offset:3072 sc1\n" \
"ds_write_addtid_b32 v14 offset:3424\nv_mfma_f32_16x16x32_bf16 v[24:27],v[84:87],a[84:87],v[24:27]\nds_write_addtid_b32 v15 offset:3968\nv_mfma_f32_16x16x32_bf16 v[28:31],v[80:83],a[72:75],v[28:31]\n" \
"ds_write_addtid_b32 v16 offset:4224\nv_mfma_f32_16x16x32_bf16 v[28:31],v[84:87],a[88:91],v[28:31]\nds_write_addtid_b32 v17 offset:4512\nv_mfma_f32_16x16x32_bf16 v[32:35],v[80:83],a[76:79],v[32:35]\n" \
"global_load_dwordx4 v[80:83],v59,s[6:7] sc1\nds_write_addtid_b32 v18 offset:4800\nv_mfma_f32_16x16x32_bf16 v[32:35],v[84:87],a[92:95],v[32:35]\nds_write_addtid_b32 v19 offset:5088\n" \
"s_waitcnt vmcnt(15)\nv_mfma_f32_16x16x32_bf16 v[20:23],v[88:91],a[96:99],v[20:23]\ns_waitcnt vmcnt(14)\nv_mfma_f32_16x16x32_bf16 v[20:23],v[92:95],a[112:115],v[20:23]\n" \
"v_mfma_f32_16x16x32_bf16 v[24:27],v[88:91],a[100:103],v[24:27]\nglobal_load_dwordx4 v[84:87],v59,s[6:7] offset:1024 sc1\nv_mfma_f32_16x16x32_bf16 v[24:27],v[92:95],a[116:119],v[24:27]\nv_mfma_f32_16x16x32_bf16 v[28:31],v[88:91],a[104:107],v[28:31]\n" \
"v_mfma_f32_16x16x32_bf16 v[28:31],v[92:95],a[120:123],v[28:31]\ns_waitcnt lgkmcnt(0)\ns_barrier\nv_mfma_f32_16x16x32_bf16 v[32:35],v[88:91],a[108:111],v[32:35]\n" \
"global_load_dwordx4 v[88:91],v59,s[6:7] offset:2048 sc1\nds_read_b128 v[36:39],v58\nv_mfma_f32_16x16x32_bf16 v[32:35],v[92:95],a[124:127],v[32:35]\nds_read_b128 v[40:43],v58 offset:9568\n" \
"s_waitcnt vmcnt(15)\nv_mfma_f32_16x16x32_bf16 v[20:23],v[96:99],a[128:131],v[20:23]\nds_read_b128 v[44:47],v58 offset:19136\ns_waitcnt vmcnt(14)\n" \
"v_mfma_f32_16x16x32_bf16 v[20:23],v[100:103],a[144:147],v[20:23]\nds_read_b128 v[48:51],v58 offset:28704\nv_mfma_f32_16x16x32_bf16 v[24:27],v[96:99],a[132:135],v[24:27]\nglobal_load_dwordx4 v[92:95],v59,s[6:7] offset:3072 sc1\n" \
"v_mfma_f32_16x16x32_bf16 v[24:27],v[100:103],a[148:151],v[24:27]\nv_mfma_f32_16x16x32_bf16 v[28:31],v[96:99],a[136:139],v[28:31]\ns_cmp_eq_u32 s52,s83\ns_cbranch_scc1 .Lxq4_%=\n" \
".Lyq4_%=:\nv_mfma_f32_16x16x32_bf16 v[28:31],v[100:103],a[152:155],v[28:31]\nv_mfma_f32_16x16x32_bf16 v[32:35],v[96:99],a[140:143],v[32:35]\nglobal_load_dwordx4 v[96:99],v59,s[8:9] sc1\n" \
"v_mfma_f32_16x16x32_bf16 v[32:35],v[100:103],a[156:159],v[32:35]\ns_waitcnt lgkmcnt(2)\ns_waitcnt vmcnt(15)\nv_mfma_f32_16x16x32_bf16 v[20:23],v[104:107],a[160:163],v[20:23]\n" \
"v_add_f32 v36,v36,v40\ns_waitcnt vmcnt(14)\nv_mfma_f32_16x16x32_bf16 v[20:23],v[108:111],a[176:179],v[20:23]\nv_add_f32 v37,v37,v41\n" \
"v_add_f32 v38,v38,v42\nv_mfma_f32_16x16x32_bf16 v[24:27],v[104:107],a[164:167],v[24:27]\nglobal_load_dwordx4 v[100:103],v59,s[8:9] offset:1024 sc1\nv_add_f32 v39,v39,v43\n" \
"v_mfma_f32_16x16x32_bf16 v[24:27],v[108:111],a[180:183],v[24:27]\nv_add_f32 v36,v36,v52\nv_add_f32 v37,v37,v53\nv_mfma_f32_16x16x32_bf16 v[28:31],v[104:107],a[168:171],v[28:31]\n" \
"v_add_f32 v38,v38,v54\nv_add_f32 v39,v39,v55\nv_mfma_f32_16x16x32_bf16 v[28:31],v[108:111],a[184:187],v[28:31]\ns_waitcnt lgkmcnt(0)\n" \
"v_add_f32 v44,v44,v48\nv_mfma_f32_16x16x32_bf16 v[32:35],v[104:107],a[172:175],v[32:35]\nglobal_load_dwordx4 v[104:107],v59,s[8:9] offset:2048 sc1\nv_add_f32 v45,v45,v49\n" \
"v_mfma_f32_16x16x32_bf16 v[32:35],v[108:111],a[188:191],v[32:35]\nv_add_f32 v46,v46,v50\ns_waitcnt vmcnt(15)\nv_mfma_f32_16x16x32_bf16 v[20:23],v[112:115],a[192:195],v[20:23]\n" \
"v_add_f32 v47,v47,v51\ns_waitcnt vmcnt(14)\nv_mfma_f32_16x16x32_bf16 v[20:23],v[116:119],a[208:211],v[20:23]\nv_add_f32 v36,v36,v44\n" \
"v_add_f32 v37,v37,v45\nv_mfma_f32_16x16x32_bf16 v[24:27],v[112:115],a[196:199],v[24:27]\nglobal_load_dwordx4 v[108:111],v59,s[8:9] offset:3072 sc1\nv_add_f32 v38,v38,v46\n" \
"v_mfma_f32_16x16x32_bf16 v[24:27],v[116:119],a[212:215],v[24:27]\nv_add_f32 v39,v39,v47\nv_mul_f32 v40,v36,v36\nv_mfma_f32_16x16x32_bf16 v[28:31],v[112:115],a[200:203],v[28:31]\n" \
"v_mul_f32 v41,v37,v37\nv_mul_f32 v42,v38,v38\nv_mfma_f32_16x16x32_bf16 v[28:31],v[116:119],a[216:219],v[28:31]\nv_mul_f32 v43,v39,v39\n" \
"v_fma_f32 v40,v40,s55,v57\nv_mfma_f32_16x16x32_bf16 v[32:35],v[112:115],a[204:207],v[32:35]\nglobal_load_dwordx4 v[112:115],v59,s[10:11] sc1\nv_fma_f32 v41,v41,s55,v57\n" \
"v_mfma_f32_16x16x32_bf16 v[32:35],v[116:119],a[220:223],v[32:35]\nv_fma_f32 v42,v42,s55,v57\ns_waitcnt vmcnt(15)\nv_mfma_f32_16x16x32_bf16 v[20:23],v[120:123],a[224:227],v[20:23]\n" \
"v_fma_f32 v43,v43,s55,v57\ns_waitcnt vmcnt(14)\nv_mfma_f32_16x16x32_bf16 v[20:23],v[124:127],a[240:243],v[20:23]\nv_mul_f32 v40,v36,v40\n" \
"v_mul_f32 v41,v37,v41\nv_mfma_f32_16x16x32_bf16 v[24:27],v[120:123],a[228:231],v[24:27]\nglobal_load_dwordx4 v[116:119],v59,s[10:11] offset:1024 sc1\nv_mul_f32 v42,v38,v42\n" \
"v_mfma_f32_16x16x32_bf16 v[24:27],v[124:127],a[244:247],v[24:27]\nv_mul_f32 v43,v39,v43\nv_mfma_f32_16x16x32_bf16 v[28:31],v[120:123],a[232:235],v[28:31]\ns_cmp_eq_u32 s52,s95\n" \
"s_cbranch_scc1 .Lxt4_%=\n.Lyt4_%=:\nv_mfma_f32_16x16x32_bf16 v[28:31],v[124:127],a[248:251],v[28:31]\nv_exp_f32 v40,v40\n" \
"v_mfma_f32_16x16x32_bf16 v[32:35],v[120:123],a[236:239],v[32:35]\nglobal_load_dwordx4 v[120:123],v59,s[10:11] offset:2048 sc1\nv_exp_f32 v41,v41\nv_mfma_f32_16x16x32_bf16 v[32:35],v[124:127],a[252:255],v[32:35]\n" \
"global_load_dwordx4 v[124:127],v59,s[10:11] offset:3072 sc1\nv_exp_f32 v42,v42\nv_exp_f32 v43,v43\ns_add_u32 s52,s52,409600\n" \
"s_cmp_eq_u32 s52,s53\ns_cbranch_scc0 .Lf_%=\nv_mfma_f32_16x16x32_bf16 v[0:3],v[64:67],a[0:3],0\nds_read_b32 v44,v60\n" \
"s_waitcnt vmcnt(16)\nv_mfma_f32_16x16x32_bf16 v[0:3],v[68:71],a[16:19],v[0:3]\nds_read_b32 v45,v60 offset:9568\nv_and_b32 v59,0x3ff,v59\n" \
"v_mfma_f32_16x16x32_bf16 v[4:7],v[64:67],a[4:7],0\nds_read_b32 v46,v60 offset:19136\nds_write_addtid_b32 v20 offset:5344\nv_mfma_f32_16x16x32_bf16 v[4:7],v[68:71],a[20:23],v[4:7]\n" \
"ds_read_b32 v47,v60 offset:28704\nds_write_addtid_b32 v21 offset:5888\nv_mfma_f32_16x16x32_bf16 v[8:11],v[64:67],a[8:11],0\ns_add_u32 s4,s12,409600\n" \
"s_addc_u32 s5,s13,0\nv_mfma_f32_16x16x32_bf16 v[8:11],v[68:71],a[24:27],v[8:11]\ns_add_u32 s6,s14,409600\ns_addc_u32 s7,s15,0\n" \
"v_mfma_f32_16x16x32_bf16 v[12:15],v[64:67],a[12:15],0\ns_add_u32 s8,s16,409600\ns_addc_u32 s9,s17,0\nv_mfma_f32_16x16x32_bf16 v[12:15],v[68:71],a[28:31],v[12:15]\n" \
"ds_write_addtid_b32 v22 offset:5600\nds_write_addtid_b32 v23 offset:6144\nv_mfma_f32_16x16x32_bf16 v[16:19],v[64:67],v[192:195],0\nds_write_addtid_b32 v24 offset:6400\n" \
"ds_write_addtid_b32 v25 offset:6944\nv_mfma_f32_16x16x32_bf16 v[16:19],v[68:71],v[196:199],v[16:19]\nds_write_addtid_b32 v26 offset:6656\ns_waitcnt vmcnt(15)\n" \
"v_mfma_f32_16x16x32_bf16 v[0:3],v[72:75],a[32:35],v[0:3]\nv_add_u32 v71,s57,v59\ns_waitcnt vmcnt(14)\nv_mfma_f32_16x16x32_bf16 v[0:3],v[76:79],a[48:51],v[0:3]\n" \
"s_waitcnt vmcnt(8)\nv_cmp_eq_u32 vcc,0,v63\nv_mfma_f32_16x16x32_bf16 v[4:7],v[72:75],a[36:39],v[4:7]\nds_read_b32 v64,v71 offset:28672\n" \
"ds_read_b64 v[68:69],v71 offset:29696\nv_mfma_f32_16x16x32_bf16 v[4:7],v[76:79],a[52:55],v[4:7]\ns_and_b64 vcc,vcc,s[86:87]\ns_cbranch_scc1 .LsaF_%=\n" \
".LbaF_%=:\nv_mfma_f32_16x16x32_bf16 v[8:11],v[72:75],a[40:43],v[8:11]\nds_write_addtid_b32 v27 offset:7200\nds_write_addtid_b32 v28 offset:7456\n" \
"v_mfma_f32_16x16x32_bf16 v[8:11],v[76:79],a[56:59],v[8:11]\nds_write_addtid_b32 v29 offset:8000\nds_write_addtid_b32 v30 offset:7712\nv_mfma_f32_16x16x32_bf16 v[12:15],v[72:75],a[44:47],v[12:15]\n" \
"s_waitcnt lgkmcnt(4)\nv_xor_b32 v65,32,v64\nv_mfma_f32_16x16x32_bf16 v[12:15],v[76:79],a[60:63],v[12:15]\nv_xor_b32 v66,64,v64\n" \
"v_xor_b32 v67,96,v64\nv_mfma_f32_16x16x32_bf16 v[16:19],v[72:75],v[200:203],v[16:19]\nv_add_u32 v70,0xf800,v68\nv_add_u32 v71,0xf800,v69\n" \
"v_mfma_f32_16x16x32_bf16 v[16:19],v[76:79],v[204:207],v[16:19]\nds_write_addtid_b32 v31 offset:8256\nv_mfma_f32_16x16x32_bf16 v[0:3],v[80:83],a[64:67],v[0:3]\nglobal_load_dwordx4 v[72:75],v59,s[12:13] sc1\n" \
"v_mfma_f32_16x16x32_bf16 v[0:3],v[84:87],a[80:83],v[0:3]\nds_write_addtid_b32 v32 offset:8512\nds_write_addtid_b32 v33 offset:9056\nv_mfma_f32_16x16x32_bf16 v[4:7],v[80:83],a[68:71],v[4:7]\n" \
"global_load_dwordx4 v[76:79],v59,s[12:13] offset:1024 sc1\nds_write_addtid_b32 v34 offset:8768\nv_mfma_f32_16x16x32_bf16 v[4:7],v[84:87],a[84:87],v[4:7]\nds_write_addtid_b32 v35 offset:9312\n" \
"ds_read_b64_tr_b16 a[0:1],v64\nv_mfma_f32_16x16x32_bf16 v[8:11],v[80:83],a[72:75],v[8:11]\nds_read_b64_tr_b16 a[2:3],v64 offset:512\nds_read_b64_tr_b16 a[4:5],v65\n" \
"v_mfma_f32_16x16x32_bf16 v[8:11],v[84:87],a[88:91],v[8:11]\nds_read_b64_tr_b16 a[6:7],v65 offset:512\nds_read_b64_tr_b16 a[8:9],v66\nv_mfma_f32_16x16x32_bf16 v[12:15],v[80:83],a[76:79],v[12:15]\n" \
"ds_read_b64_tr_b16 a[10:11],v66 offset:512\nds_read_b64_tr_b16 a[12:13],v67\nv_mfma_f32_16x16x32_bf16 v[12:15],v[84:87],a[92:95],v[12:15]\nds_read_b64_tr_b16 a[14:15],v67 offset:512\n" \
"ds_read_b64_tr_b16 a[16:17],v64 offset:4096\nv_mfma_f32_16x16x32_bf16 v[16:19],v[80:83],v[208:211],v[16:19]\nds_read_b64_tr_b16 a[18:19],v64 offset:4608\nds_read_b64_tr_b16 a[20:21],v65 offset:4096\n" \
"v_mfma_f32_16x16x32_bf16 v[16:19],v[84:87],v[212:215],v[16:19]\nds_read_b64_tr_b16 a[22:23],v65 offset:4608\ns_waitcnt vmcnt(13)\nv_mfma_f32_16x16x32_bf16 v[0:3],v[88:91],a[96:99],v[0:3]\n" \
"ds_read_b64_tr_b16 a[24:25],v66 offset:4096\ns_waitcnt vmcnt(12)\nv_mfma_f32_16x16x32_bf16 v[0:3],v[92:95],a[112:115],v[0:3]\ns_waitcnt lgkmcnt(5)\n" \
"s_add_u32 m0,s57,0\nv_mfma_f32_16x16x32_bf16 v[4:7],v[88:91],a[100:103],v[4:7]\nbuffer_load_dwordx4 v68,s[40:43],s60 offen lds\nds_read_b64_tr_b16 a[26:27],v66 offset:4608\n" \
"v_mfma_f32_16x16x32_bf16 v[4:7],v[92:95],a[116:119],v[4:7]\ns_waitcnt lgkmcnt(0)\ns_barrier\nv_mfma_f32_16x16x32_bf16 v[8:11],v[88:91],a[104:107],v[8:11]\n" \
"buffer_load_dwordx4 v69,s[40:43],s60 offen offset:1024 lds\nds_read_b64_tr_b16 a[28:29],v67 offset:4096\nv_mfma_f32_16x16x32_bf16 v[8:11],v[92:95],a[120:123],v[8:11]\nds_read_b64_tr_b16 a[30:31],v67 offset:4608\n" \
"ds_read_b64_tr_b16 a[32:33],v64 offset:8192\nv_mfma_f32_16x16x32_bf16 v[12:15],v[88:91],a[108:111],v[12:15]\nbuffer_load_dwordx4 v70,s[40:43],s60 offen offset:2048 lds\nds_read_b64_tr_b16 a[34:35],v64 offset:8704\n" \
"v_mfma_f32_16x16x32_bf16 v[12:15],v[92:95],a[124:127],v[12:15]\nds_read_b64_tr_b16 a[36:37],v65 offset:8192\nds_read_b64_tr_b16 a[38:39],v65 offset:8704\nv_mfma_f32_16x16x32_bf16 v[16:19],v[88:91],v[216:219],v[16:19]\n" \
"buffer_load_dwordx4 v71,s[40:43],s60 offen offset:3072 lds\nds_read_b64_tr_b16 a[40:41],v66 offset:8192\nv_mfma_f32_16x16x32_bf16 v[16:19],v[92:95],v[220:223],v[16:19]\nds_read_b64_tr_b16 a[42:43],v66 offset:8704\n" \
"s_waitcnt vmcnt(13)\nv_mfma_f32_16x16x32_bf16 v[0:3],v[96:99],a[128:131],v[0:3]\nglobal_load_dwordx4 v[80:83],v59,s[12:13] offset:2048 sc1\ns_waitcnt vmcnt(13)\n" \
"v_mfma_f32_16x16x32_bf16 v[0:3],v[100:103],a[144:147],v[0:3]\ns_waitcnt lgkmcnt(6)\ns_add_u32 m0,s57,4096\nv_mfma_f32_16x16x32_bf16 v[4:7],v[96:99],a[132:135],v[4:7]\n" \
"buffer_load_dwordx4 v68,s[40:43],s61 offen lds\nds_read_b64_tr_b16 a[44:45],v67 offset:8192\nv_mfma_f32_16x16x32_bf16 v[4:7],v[100:103],a[148:151],v[4:7]\nds_read_b64_tr_b16 a[46:47],v67 offset:8704\n" \
"ds_read_b64_tr_b16 a[48:49],v64 offset:12288\nv_mfma_f32_16x16x32_bf16 v[8:11],v[96:99],a[136:139],v[8:11]\nbuffer_load_dwordx4 v69,s[40:43],s61 offen offset:1024 lds\nds_read_b64_tr_b16 a[50:51],v64 offset:12800\n" \
"v_mfma_f32_16x16x32_bf16 v[8:11],v[100:103],a[152:155],v[8:11]\nds_read_b64_tr_b16 a[52:53],v65 offset:12288\nds_read_b64_tr_b16 a[54:55],v65 offset:12800\nv_mfma_f32_16x16x32_bf16 v[12:15],v[96:99],a[140:143],v[12:15]\n" \
"buffer_load_dwordx4 v70,s[40:43],s61 offen offset:2048 lds\nds_read_b64_tr_b16 a[56:57],v66 offset:12288\nv_mfma_f32_16x16x32_bf16 v[12:15],v[100:103],a[156:159],v[12:15]\nds_read_b64_tr_b16 a[58:59],v66 offset:12800\n" \
"ds_read_b64_tr_b16 a[60:61],v67 offset:12288\nv_mfma_f32_16x16x32_bf16 v[16:19],v[96:99],v[224:227],v[16:19]\nbuffer_load_dwordx4 v71,s[40:43],s61 offen offset:3072 lds\nds_read_b64_tr_b16 a[62:63],v67 offset:12800\n" \
"v_mfma_f32_16x16x32_bf16 v[16:19],v[100:103],v[228:231],v[16:19]\nds_read_b64_tr_b16 a[64:65],v64 offset:16384\ns_waitcnt vmcnt(16)\nv_mfma_f32_16x16x32_bf16 v[0:3],v[104:107],a[160:163],v[0:3]\n" \
"global_load_dwordx4 v[84:87],v59,s[14:15] sc1\ns_waitcnt vmcnt(16)\nv_mfma_f32_16x16x32_bf16 v[0:3],v[108:111],a[176:179],v[0:3]\ns_waitcnt lgkmcnt(9)\n" \
"s_add_u32 m0,s57,8192\nv_mfma_f32_16x16x32_bf16 v[4:7],v[104:107],a[164:167],v[4:7]\nbuffer_load_dwordx4 v68,s[40:43],s62 offen lds\nds_read_b64_tr_b16 a[66:67],v64 offset:16896\n" \
"v_mfma_f32_16x16x32_bf16 v[4:7],v[108:111],a[180:183],v[4:7]\nds_read_b64_tr_b16 a[68:69],v65 offset:16384\nds_read_b64_tr_b16 a[70:71],v65 offset:16896\nv_mfma_f32_16x16x32_bf16 v[8:11],v[104:107],a[168:171],v[8:11]\n" \
"buffer_load_dwordx4 v69,s[40:43],s62 offen offset:1024 lds\nds_read_b64_tr_b16 a[72:73],v66 offset:16384\nv_mfma_f32_16x16x32_bf16 v[8:11],v[108:111],a[184:187],v[8:11]\nds_read_b64_tr_b16 a[74:75],v66 offset:16896\n" \
"ds_read_b64_tr_b16 a[76:77],v67 offset:16384\nv_mfma_f32_16x16x32_bf16 v[12:15],v[104:107],a[172:175],v[12:15]\nbuffer_load_dwordx4 v70,s[40:43],s62 offen offset:2048 lds\nds_read_b64_tr_b16 a[78:79],v67 offset:16896\n" \
"v_mfma_f32_16x16x32_bf16 v[12:15],v[108:111],a[188:191],v[12:15]\nds_read_b64_tr_b16 a[80:81],v64 offset:20480\nds_read_b64_tr_b16 a[82:83],v64 offset:20992\nv_mfma_f32_16x16x32_bf16 v[16:19],v[104:107],v[232:235],v[16:19]\n" \
"buffer_load_dwordx4 v71,s[40:43],s62 offen offset:3072 lds\nds_read_b64_tr_b16 a[84:85],v65 offset:20480\nv_mfma_f32_16x16x32_bf16 v[16:19],v[108:111],v[236:239],v[16:19]\nds_read_b64_tr_b16 a[86:87],v65 offset:20992\n" \
"s_waitcnt vmcnt(19)\nv_mfma_f32_16x16x32_bf16 v[0:3],v[112:115],a[192:195],v[0:3]\nglobal_load_dwordx4 v[88:91],v59,s[14:15] offset:1024 sc1\ns_waitcnt vmcnt(19)\n" \
"v_mfma_f32_16x16x32_bf16 v[0:3],v[116:119],a[208:211],v[0:3]\ns_waitcnt lgkmcnt(12)\ns_add_u32 m0,s57,12288\nv_mfma_f32_16x16x32_bf16 v[4:7],v[112:115],a[196:199],v[4:7]\n" \
"buffer_load_dwordx4 v68,s[40:43],s63 offen lds\nds_read_b64_tr_b16 a[88:89],v66 offset:20480\nv_mfma_f32_16x16x32_bf16 v[4:7],v[116:119],a[212:215],v[4:7]\nds_read_b64_tr_b16 a[90:91],v66 offset:20992\n" \
"ds_read_b64_tr_b16 a[92:93],v67 offset:20480\nv_mfma_f32_16x16x32_bf16 v[8:11],v[112:115],a[200:203],v[8:11]\nbuffer_load_dwordx4 v69,s[40:43],s63 offen offset:1024 lds\nds_read_b64_tr_b16 a[94:95],v67 offset:20992\n" \
"v_mfma_f32_16x16x32_bf16 v[8:11],v[116:119],a[216:219],v[8:11]\nds_read_b64_tr_b16 a[96:97],v64 offset:24576\nds_read_b64_tr_b16 a[98:99],v64 offset:25088\nv_mfma_f32_16x16x32_bf16 v[12:15],v[112:115],a[204:207],v[12:15]\n" \
"buffer_load_dwordx4 v70,s[40:43],s63 offen offset:2048 lds\nds_read_b64_tr_b16 a[100:101],v65 offset:24576\nv_mfma_f32_16x16x32_bf16 v[12:15],v[116:119],a[220:223],v[12:15]\nds_read_b64_tr_b16 a[102:103],v65 offset:25088\n" \
"ds_read_b64_tr_b16 a[104:105],v66 offset:24576\nv_mfma_f32_16x16x32_bf16 v[16:19],v[112:115],v[240:243],v[16:19]\nbuffer_load_dwordx4 v71,s[40:43],s63 offen offset:3072 lds\nds_read_b64_tr_b16 a[106:107],v66 offset:25088\n" \
"v_mfma_f32_16x16x32_bf16 v[16:19],v[116:119],v[244:247],v[16:19]\nds_read_b64_tr_b16 a[108:109],v67 offset:24576\ns_waitcnt vmcnt(22)\nv_mfma_f32_16x16x32_bf16 v[0:3],v[120:123],a[224:227],v[0:3]\n" \
"global_load_dwordx4 v[92:95],v59,s[14:15] offset:2048 sc1\ns_waitcnt vmcnt(22)\nv_mfma_f32_16x16x32_bf16 v[0:3],v[124:127],a[240:243],v[0:3]\ns_add_u32 m0,s57,16384\n" \
"v_mfma_f32_16x16x32_bf16 v[4:7],v[120:123],a[228:231],v[4:7]\nbuffer_load_dwordx4 v68,s[40:43],s64 offen lds\nds_read_b64_tr_b16 a[110:111],v67 offset:25088\nv_mfma_f32_16x16x32_bf16 v[4:7],v[124:127],a[244:247],v[4:7]\n" \
"v_add_f32 v40,1.0,v40\nv_add_f32 v41,1.0,v41\nv_mfma_f32_16x16x32_bf16 v[8:11],v[120:123],a[232:235],v[8:11]\nbuffer_load_dwordx4 v69,s[40:43],s64 offen offset:1024 lds\n" \
"v_add_f32 v42,1.0,v42\nv_mfma_f32_16x16x32_bf16 v[8:11],v[124:127],a[248:251],v[8:11]\nv_add_f32 v43,1.0,v43\ns_waitcnt lgkmcnt(15)\n" \
"v_mfma_f32_16x16x32_bf16 v[12:15],v[120:123],a[236:239],v[12:15]\nbuffer_load_dwordx4 v70,s[40:43],s64 offen offset:2048 lds\nv_add_f32 v44,v44,v45\nv_mfma_f32_16x16x32_bf16 v[12:15],v[124:127],a[252:255],v[12:15]\n" \
"v_rcp_f32 v40,v40\nv_mfma_f32_16x16x32_bf16 v[16:19],v[120:123],v[248:251],v[16:19]\nbuffer_load_dwordx4 v71,s[40:43],s64 offen offset:3072 lds\nv_add_f32 v46,v46,v47\n" \
"v_mfma_f32_16x16x32_bf16 v[16:19],v[124:127],v[252:255],v[16:19]\ns_mov_b32 m0,s56\ns_waitcnt vmcnt(25)\nv_mfma_f32_16x16x32_bf16 v[208:211],v[128:131],v[72:75],0\n" \
"global_load_dwordx4 v[108:111],v59,s[4:5] sc1\ns_waitcnt vmcnt(25)\nv_mfma_f32_16x16x32_bf16 v[208:211],v[160:163],v[76:79],v[208:211]\nds_write_addtid_b32 v0\n" \
"ds_write_addtid_b32 v1 offset:544\nv_mfma_f32_16x16x32_bf16 v[212:215],v[132:135],v[72:75],0\nglobal_load_dwordx4 v[112:115],v59,s[4:5] offset:1024 sc1\nds_write_addtid_b32 v2 offset:256\n" \
"v_mfma_f32_16x16x32_bf16 v[212:215],v[164:167],v[76:79],v[212:215]\nds_write_addtid_b32 v3 offset:800\nds_write_addtid_b32 v4 offset:1056\nv_mfma_f32_16x16x32_bf16 v[216:219],v[136:139],v[72:75],0\n" \
"global_load_dwordx4 v[116:119],v59,s[4:5] offset:2048 sc1\nds_write_addtid_b32 v5 offset:1600\nv_mfma_f32_16x16x32_bf16 v[216:219],v[168:171],v[76:79],v[216:219]\nds_write_addtid_b32 v6 offset:1312\n" \
"ds_write_addtid_b32 v7 offset:1856\nv_mfma_f32_16x16x32_bf16 v[220:223],v[140:143],v[72:75],0\nds_write_addtid_b32 v8 offset:2112\nds_write_addtid_b32 v9 offset:2656\n" \
"v_mfma_f32_16x16x32_bf16 v[220:223],v[172:175],v[76:79],v[220:223]\nds_write_addtid_b32 v10 offset:2368\nds_write_addtid_b32 v11 offset:2912\nv_mfma_f32_16x16x32_bf16 v[224:227],v[144:147],v[72:75],0\n" \
"ds_write_addtid_b32 v12 offset:3168\nds_write_addtid_b32 v13 offset:3712\nv_mfma_f32_16x16x32_bf16 v[224:227],v[176:179],v[76:79],v[224:227]\nds_write_addtid_b32 v14 offset:3424\n" \
"ds_write_addtid_b32 v15 offset:3968\nv_mfma_f32_16x16x32_bf16 v[228:231],v[148:151],v[72:75],0\nds_write_addtid_b32 v16 offset:4224\nds_write_addtid_b32 v17 offset:4512\n" \
"v_mfma_f32_16x16x32_bf16 v[228:231],v[180:183],v[76:79],v[228:231]\nds_write_addtid_b32 v18 offset:4800\nds_write_addtid_b32 v19 offset:5088\nv_mfma_f32_16x16x32_bf16 v[232:235],v[152:155],v[72:75],0\n" \
"s_waitcnt lgkmcnt(15)\ns_add_u32 m0,s57,20480\nv_mfma_f32_16x16x32_bf16 v[232:235],v[184:187],v[76:79],v[232:235]\nbuffer_load_dwordx4 v68,s[40:43],s65 offen lds\n" \
"s_waitcnt vmcnt(24)\nv_mfma_f32_16x16x32_bf16 v[236:239],v[156:159],v[72:75],0\nds_read_b64_tr_b16 a[112:113],v64\nds_read_b64_tr_b16 a[114:115],v64 offset:512\n" \
"v_mfma_f32_16x16x32_bf16 v[236:239],v[188:191],v[76:79],v[236:239]\ns_waitcnt lgkmcnt(15)\ns_waitcnt vmcnt(23)\nv_mfma_f32_16x16x32_bf16 v[208:211],a[0:3],v[80:83],v[208:211]\n" \
"s_waitcnt vmcnt(18)\nv_mfma_f32_16x16x32_bf16 v[208:211],a[32:35],v[84:87],v[208:211]\nbuffer_load_dwordx4 v69,s[40:43],s65 offen offset:1024 lds\nds_read_b64_tr_b16 a[116:117],v65\n" \
"v_mfma_f32_16x16x32_bf16 v[212:215],a[4:7],v[80:83],v[212:215]\ns_waitcnt lgkmcnt(0)\ns_barrier\nv_mfma_f32_16x16x32_bf16 v[212:215],a[36:39],v[84:87],v[212:215]\n" \
"buffer_load_dwordx4 v70,s[40:43],s65 offen offset:2048 lds\nds_read_b64_tr_b16 a[118:119],v65 offset:512\nv_mfma_f32_16x16x32_bf16 v[216:219],a[8:11],v[80:83],v[216:219]\nds_read_b64_tr_b16 a[120:121],v66\n" \
"ds_read_b64_tr_b16 a[122:123],v66 offset:512\nv_mfma_f32_16x16x32_bf16 v[216:219],a[40:43],v[84:87],v[216:219]\nbuffer_load_dwordx4 v71,s[40:43],s65 offen offset:3072 lds\nds_read_b64_tr_b16 a[124:125],v67\n" \
"v_mfma_f32_16x16x32_bf16 v[220:223],a[12:15],v[80:83],v[220:223]\nds_read_b64_tr_b16 a[126:127],v67 offset:512\nv_mfma_f32_16x16x32_bf16 v[220:223],a[44:47],v[84:87],v[220:223]\nglobal_load_dwordx4 v[120:123],v59,s[6:7] sc1\n" \
"v_mfma_f32_16x16x32_bf16 v[224:227],a[16:19],v[80:83],v[224:227]\nds_read_b64_tr_b16 a[128:129],v64 offset:4096\nv_mfma_f32_16x16x32_bf16 v[224:227],a[48:51],v[84:87],v[224:227]\ns_add_u32 m0,s57,24576\n" \
"v_mfma_f32_16x16x32_bf16 v[228:231],a[20:23],v[80:83],v[228:231]\nbuffer_load_dwordx4 v68,s[40:43],s66 offen lds\nds_read_b64_tr_b16 a[130:131],v64 offset:4608\nv_mfma_f32_16x16x32_bf16 v[228:231],a[52:55],v[84:87],v[228:231]\n" \
"ds_read_b64_tr_b16 a[132:133],v65 offset:4096\nds_read_b64_tr_b16 a[134:135],v65 offset:4608\nv_mfma_f32_16x16x32_bf16 v[232:235],a[24:27],v[80:83],v[232:235]\nbuffer_load_dwordx4 v69,s[40:43],s66 offen offset:1024 lds\n" \
"ds_read_b64_tr_b16 a[136:137],v66 offset:4096\nv_mfma_f32_16x16x32_bf16 v[232:235],a[56:59],v[84:87],v[232:235]\nds_read_b64_tr_b16 a[138:139],v66 offset:4608\nds_read_b64_tr_b16 a[140:141],v67 offset:4096\n" \
"v_mfma_f32_16x16x32_bf16 v[236:239],a[28:31],v[80:83],v[236:239]\nbuffer_load_dwordx4 v70,s[40:43],s66 offen offset:2048 lds\nds_read_b64_tr_b16 a[142:143],v67 offset:4608\nv_mfma_f32_16x16x32_bf16 v[236:239],a[60:63],v[84:87],v[236:239]\n" \
"s_waitcnt vmcnt(20)\nv_mfma_f32_16x16x32_bf16 v[208:211],a[64:67],v[88:91],v[208:211]\ns_waitcnt vmcnt(15)\nv_mfma_f32_16x16x32_bf16 v[208:211],a[96:99],v[92:95],v[208:211]\n" \
"buffer_load_dwordx4 v71,s[40:43],s66 offen offset:3072 lds\nv_mfma_f32_16x16x32_bf16 v[212:215],a[68:71],v[88:91],v[212:215]\nds_read_b64_tr_b16 a[144:145],v64 offset:8192\nds_read_b64_tr_b16 a[146:147],v64 offset:8704\n" \
"v_mfma_f32_16x16x32_bf16 v[212:215],a[100:103],v[92:95],v[212:215]\nglobal_load_dwordx4 v[124:127],v59,s[6:7] offset:1024 sc1\nds_read_b64_tr_b16 a[148:149],v65 offset:8192\nv_mfma_f32_16x16x32_bf16 v[216:219],a[72:75],v[88:91],v[216:219]\n" \
"ds_read_b64_tr_b16 a[150:151],v65 offset:8704\nds_read_b64_tr_b16 a[152:153],v66 offset:8192\nv_mfma_f32_16x16x32_bf16 v[216:219],a[104:107],v[92:95],v[216:219]\nglobal_load_dwordx4 v[192:195],v59,s[6:7] offset:2048 sc1\n" \
"ds_read_b64_tr_b16 a[154:155],v66 offset:8704\nv_mfma_f32_16x16x32_bf16 v[220:223],a[76:79],v[88:91],v[220:223]\nds_read_b64_tr_b16 a[156:157],v67 offset:8192\nds_read_b64_tr_b16 a[158:159],v67 offset:8704\n" \
"v_mfma_f32_16x16x32_bf16 v[220:223],a[108:111],v[92:95],v[220:223]\nglobal_load_dwordx4 v[96:99],v59,s[16:17] sc1\ns_waitcnt lgkmcnt(15)\nv_mfma_f32_16x16x32_bf16 v[224:227],a[80:83],v[88:91],v[224:227]\n" \
"ds_read_b64_tr_b16 a[160:161],v64 offset:12288\nv_mfma_f32_16x16x32_bf16 v[228:231],a[84:87],v[88:91],v[228:231]\nglobal_load_dwordx4 v[100:103],v59,s[16:17] offset:1024 sc1\nds_read_b64_tr_b16 a[162:163],v64 offset:12800\n" \
"v_mfma_f32_16x16x32_bf16 v[232:235],a[88:91],v[88:91],v[232:235]\nds_read_b64_tr_b16 a[164:165],v65 offset:12288\nds_read_b64_tr_b16 a[166:167],v65 offset:12800\nv_mfma_f32_16x16x32_bf16 v[236:239],a[92:95],v[88:91],v[236:239]\n" \
"global_load_dwordx4 v[104:107],v59,s[16:17] offset:2048 sc1\ns_waitcnt vmcnt(16)\nv_mfma_f32_16x16x32_bf16 v[0:3],v[128:131],v[108:111],0\nds_read_b64_tr_b16 a[168:169],v66 offset:12288\n" \
"s_waitcnt vmcnt(15)\nv_mfma_f32_16x16x32_bf16 v[0:3],v[160:163],v[112:115],v[0:3]\nglobal_load_dwordx4 v[196:199],v59,s[8:9] sc1\nds_read_b64_tr_b16 a[170:171],v66 offset:12800\n" \
"v_mfma_f32_16x16x32_bf16 v[4:7],v[132:135],v[108:111],0\nds_read_b64_tr_b16 a[172:173],v67 offset:12288\nds_read_b64_tr_b16 a[174:175],v67 offset:12800\nv_mfma_f32_16x16x32_bf16 v[4:7],v[164:167],v[112:115],v[4:7]\n" \
"global_load_dwordx4 v[200:203],v59,s[8:9] offset:1024 sc1\nds_read_b32 v252,v60\nv_mfma_f32_16x16x32_bf16 v[8:11],v[136:139],v[108:111],0\ns_waitcnt vmcnt(19)\n" \
"ds_read_b64_tr_b16 a[176:177],v64 offset:16384\nv_mfma_f32_16x16x32_bf16 v[8:11],v[168:171],v[112:115],v[8:11]\nglobal_load_dwordx4 v[204:207],v59,s[8:9] offset:2048 sc1\nds_read_b64_tr_b16 a[178:179],v64 offset:16896\n" \
"v_mfma_f32_16x16x32_bf16 v[12:15],v[140:143],v[108:111],0\ns_add_u32 s59,s59,0x10000\nbuffer_load_dword v63,v61,s[36:39],s59 offen sc1\nv_mfma_f32_16x16x32_bf16 v[12:15],v[172:175],v[112:115],v[12:15]\n" \
"v_add_u32 v59,819200,v59\nds_read_b64_tr_b16 a[180:181],v65 offset:16384\nv_mfma_f32_16x16x32_bf16 v[16:19],v[144:147],v[108:111],0\nds_read_b64_tr_b16 a[182:183],v65 offset:16896\n" \
"ds_read_b64_tr_b16 a[184:185],v66 offset:16384\nv_mfma_f32_16x16x32_bf16 v[16:19],v[176:179],v[112:115],v[16:19]\nds_read_b64_tr_b16 a[186:187],v66 offset:16896\nds_read_b64_tr_b16 a[188:189],v67 offset:16384\n" \
"v_mfma_f32_16x16x32_bf16 v[20:23],v[148:151],v[108:111],0\nds_read_b64_tr_b16 a[190:191],v67 offset:16896\nds_read_b32 v253,v60 offset:9568\nv_mfma_f32_16x16x32_bf16 v[20:23],v[180:183],v[112:115],v[20:23]\n" \
"ds_read_b32 v254,v60 offset:19136\nds_read_b32 v255,v60 offset:28704\nv_mfma_f32_16x16x32_bf16 v[24:27],v[152:155],v[108:111],0\nv_rcp_f32 v41,v41\n" \
"v_mfma_f32_16x16x32_bf16 v[24:27],v[184:187],v[112:115],v[24:27]\nv_add_f32 v44,v44,v56\nv_add_f32 v44,v44,v46\nv_mfma_f32_16x16x32_bf16 v[28:31],v[156:159],v[108:111],0\n" \
"v_rcp_f32 v42,v42\nv_mfma_f32_16x16x32_bf16 v[28:31],v[188:191],v[112:115],v[28:31]\nv_mul_f32 v45,v44,v44\ns_waitcnt vmcnt(18)\n" \
"v_mfma_f32_16x16x32_bf16 v[0:3],a[0:3],v[116:119],v[0:3]\nv_fma_f32 v45,v45,s55,v57\ns_waitcnt vmcnt(13)\nv_mfma_f32_16x16x32_bf16 v[0:3],a[32:35],v[120:123],v[0:3]\n" \
"s_waitcnt lgkmcnt(0)\nv_add_f32 v252,v252,v253\nv_mfma_f32_16x16x32_bf16 v[4:7],a[4:7],v[116:119],v[4:7]\nv_add_f32 v254,v254,v255\n" \
"v_add_f32 v252,v252,v56\nv_mfma_f32_16x16x32_bf16 v[4:7],a[36:39],v[120:123],v[4:7]\nv_add_f32 v252,v252,v254\nv_mul_f32 v253,v252,v252\n" \
"v_mfma_f32_16x16x32_bf16 v[8:11],a[8:11],v[116:119],v[8:11]\nv_fma_f32 v253,v253,s55,v57\nv_mul_f32 v45,v44,v45\nv_mfma_f32_16x16x32_bf16 v[8:11],a[40:43],v[120:123],v[8:11]\n" \
"v_rcp_f32 v43,v43\nv_mfma_f32_16x16x32_bf16 v[12:15],a[12:15],v[116:119],v[12:15]\nv_mul_f32 v253,v252,v253\nv_mul_f32 v36,v36,v40\n" \
"v_mfma_f32_16x16x32_bf16 v[12:15],a[44:47],v[120:123],v[12:15]\nv_exp_f32 v45,v45\nv_mfma_f32_16x16x32_bf16 v[16:19],a[16:19],v[116:119],v[16:19]\nv_exp_f32 v253,v253\n" \
"v_mfma_f32_16x16x32_bf16 v[16:19],a[48:51],v[120:123],v[16:19]\nv_mul_f32 v37,v37,v41\nv_add_f32 v45,1.0,v45\nv_mfma_f32_16x16x32_bf16 v[20:23],a[20:23],v[116:119],v[20:23]\n" \
"v_add_f32 v253,1.0,v253\nv_mul_f32 v38,v38,v42\nv_mfma_f32_16x16x32_bf16 v[20:23],a[52:55],v[120:123],v[20:23]\nds_read_b64_tr_b16 a[192:193],v64 offset:20480\n" \
"v_mfma_f32_16x16x32_bf16 v[24:27],a[24:27],v[116:119],v[24:27]\nds_read_b64_tr_b16 a[194:195],v64 offset:20992\nds_read_b64_tr_b16 a[196:197],v65 offset:20480\nv_mfma_f32_16x16x32_bf16 v[24:27],a[56:59],v[120:123],v[24:27]\n" \
"ds_read_b64_tr_b16 a[198:199],v65 offset:20992\nds_read_b64_tr_b16 a[200:201],v66 offset:20480\nv_mfma_f32_16x16x32_bf16 v[28:31],a[28:31],v[116:119],v[28:31]\nds_read_b64_tr_b16 a[202:203],v66 offset:20992\n" \
"ds_read_b64_tr_b16 a[204:205],v67 offset:20480\nv_mfma_f32_16x16x32_bf16 v[28:31],a[60:63],v[120:123],v[28:31]\nds_read_b64_tr_b16 a[206:207],v67 offset:20992\ns_waitcnt vmcnt(8)\n" \
"v_mfma_f32_16x16x32_bf16 v[0:3],a[64:67],v[124:127],v[0:3]\nv_mul_f32 v39,v39,v43\ns_waitcnt vmcnt(7)\nv_mfma_f32_16x16x32_bf16 v[0:3],a[96:99],v[192:195],v[0:3]\n" \
"v_rcp_f32 v45,v45\nv_mfma_f32_16x16x32_bf16 v[4:7],a[68:71],v[124:127],v[4:7]\nv_rcp_f32 v253,v253\nv_mfma_f32_16x16x32_bf16 v[4:7],a[100:103],v[192:195],v[4:7]\n" \
"v_cvt_pk_bf16_f32 v40,v36,v37\nv_mul_f32 v44,v44,v45\nv_mfma_f32_16x16x32_bf16 v[8:11],a[72:75],v[124:127],v[8:11]\nv_mul_f32 v252,v252,v253\n" \
"v_cvt_pk_bf16_f32 v41,v38,v39\nv_mfma_f32_16x16x32_bf16 v[8:11],a[104:107],v[192:195],v[8:11]\nv_cvt_pk_bf16_f32 v45,v44,v44\nv_cvt_pk_bf16_f32 v253,v252,v252\n" \
"v_mfma_f32_16x16x32_bf16 v[12:15],a[76:79],v[124:127],v[12:15]\nbuffer_store_dwordx2 v[40:41],v61,s[36:39],s52 offen sc1\nbuffer_store_short v45,v62,s[36:39],s52 offen sc1\nds_read_b128 v[36:39],v58 offset:5344\n" \
"ds_read_b128 v[40:43],v58 offset:14912\nds_read_b128 v[44:47],v58 offset:24480\nds_read_b128 v[48:51],v58 offset:34048\ns_waitcnt lgkmcnt(2)\n" \
"v_add_f32 v36,v36,v40\nv_add_f32 v37,v37,v41\nv_add_f32 v38,v38,v42\nv_add_f32 v39,v39,v43\n" \
"v_add_f32 v36,v36,v52\nv_add_f32 v37,v37,v53\nv_add_f32 v38,v38,v54\nv_add_f32 v39,v39,v55\n" \
"s_waitcnt lgkmcnt(0)\nv_add_f32 v44,v44,v48\nv_add_f32 v45,v45,v49\nv_add_f32 v46,v46,v50\n" \
"v_add_f32 v47,v47,v51\nv_add_f32 v36,v36,v44\nv_add_f32 v37,v37,v45\nv_add_f32 v38,v38,v46\n" \
"v_add_f32 v39,v39,v47\nv_mul_f32 v40,v36,v36\nv_mul_f32 v41,v37,v37\nv_mul_f32 v42,v38,v38\n" \
"v_mul_f32 v43,v39,v39\nv_fma_f32 v40,v40,s55,v57\nv_fma_f32 v41,v41,s55,v57\nv_fma_f32 v42,v42,s55,v57\n" \
"v_fma_f32 v43,v43,s55,v57\nv_mul_f32 v40,v36,v40\nv_mul_f32 v41,v37,v41\nv_mul_f32 v42,v38,v42\n" \
"v_mul_f32 v43,v39,v43\nv_exp_f32 v40,v40\nv_exp_f32 v41,v41\nv_exp_f32 v42,v42\n" \
"v_exp_f32 v43,v43\nv_add_f32 v40,1.0,v40\nv_add_f32 v41,1.0,v41\nv_add_f32 v42,1.0,v42\n" \
"v_add_f32 v43,1.0,v43\nv_rcp_f32 v40,v40\nv_rcp_f32 v41,v41\nv_rcp_f32 v42,v42\n" \
"v_rcp_f32 v43,v43\nv_mul_f32 v36,v36,v40\nv_mul_f32 v37,v37,v41\nv_mul_f32 v38,v38,v42\n" \
"v_mul_f32 v39,v39,v43\nv_cvt_pk_bf16_f32 v40,v36,v37\nv_cvt_pk_bf16_f32 v41,v38,v39\nv_mfma_f32_16x16x32_bf16 v[12:15],a[108:111],v[192:195],v[12:15]\n" \
"s_add_u32 s52,s52,409600\nbuffer_store_dwordx2 v[40:41],v61,s[36:39],s52 offen sc1\nds_read_b128 v[36:39],v58\nv_mfma_f32_16x16x32_bf16 v[16:19],a[80:83],v[124:127],v[16:19]\n" \
"ds_read_b128 v[40:43],v58 offset:9568\nds_read_b128 v[44:47],v58 offset:19136\nv_mfma_f32_16x16x32_bf16 v[20:23],a[84:87],v[124:127],v[20:23]\nds_read_b128 v[48:51],v58 offset:28704\n" \
"s_add_u32 s52,s52,409600\nv_mfma_f32_16x16x32_bf16 v[24:27],a[88:91],v[124:127],v[24:27]\nds_read_b64_tr_b16 a[208:209],v64 offset:24576\nv_mfma_f32_16x16x32_bf16 v[28:31],a[92:95],v[124:127],v[28:31]\n" \
"ds_read_b64_tr_b16 a[210:211],v64 offset:25088\nv_mfma_f32_16x16x32_bf16 v[224:227],a[112:115],v[92:95],v[224:227]\ns_waitcnt vmcnt(9)\nv_mfma_f32_16x16x32_bf16 v[224:227],a[144:147],v[96:99],v[224:227]\n" \
"ds_read_b64_tr_b16 a[212:213],v65 offset:24576\nds_read_b64_tr_b16 a[214:215],v65 offset:25088\nv_mfma_f32_16x16x32_bf16 v[228:231],a[116:119],v[92:95],v[228:231]\nds_read_b64_tr_b16 a[216:217],v66 offset:24576\n" \
"ds_read_b64_tr_b16 a[218:219],v66 offset:25088\nv_mfma_f32_16x16x32_bf16 v[228:231],a[148:151],v[96:99],v[228:231]\nds_read_b64_tr_b16 a[220:221],v67 offset:24576\nds_read_b64_tr_b16 a[222:223],v67 offset:25088\n" \
"v_mfma_f32_16x16x32_bf16 v[232:235],a[120:123],v[92:95],v[232:235]\ns_waitcnt lgkmcnt(10)\nv_add_f32 v36,v36,v40\nv_add_f32 v37,v37,v41\n" \
"v_mfma_f32_16x16x32_bf16 v[232:235],a[152:155],v[96:99],v[232:235]\nv_add_f32 v38,v38,v42\nv_add_f32 v39,v39,v43\nv_add_f32 v36,v36,v52\n" \
"v_mfma_f32_16x16x32_bf16 v[236:239],a[124:127],v[92:95],v[236:239]\nv_add_f32 v37,v37,v53\nv_add_f32 v38,v38,v54\nv_add_f32 v39,v39,v55\n" \
"v_mfma_f32_16x16x32_bf16 v[236:239],a[156:159],v[96:99],v[236:239]\ns_waitcnt lgkmcnt(8)\nv_add_f32 v44,v44,v48\nv_mfma_f32_16x16x32_bf16 v[208:211],a[128:131],v[96:99],v[208:211]\n" \
"v_add_f32 v45,v45,v49\ns_waitcnt vmcnt(8)\nv_mfma_f32_16x16x32_bf16 v[208:211],a[160:163],v[100:103],v[208:211]\nv_add_f32 v46,v46,v50\n" \
"v_add_f32 v47,v47,v51\nv_add_f32 v36,v36,v44\nv_mfma_f32_16x16x32_bf16 v[212:215],a[132:135],v[96:99],v[212:215]\nv_add_f32 v37,v37,v45\n" \
"v_add_f32 v38,v38,v46\nv_add_f32 v39,v39,v47\nv_mfma_f32_16x16x32_bf16 v[212:215],a[164:167],v[100:103],v[212:215]\nv_mul_f32 v40,v36,v36\n" \
"v_mul_f32 v41,v37,v37\nv_mul_f32 v42,v38,v38\nv_mfma_f32_16x16x32_bf16 v[216:219],a[136:139],v[96:99],v[216:219]\nv_mul_f32 v43,v39,v39\n" \
"v_fma_f32 v40,v40,s55,v57\nv_fma_f32 v41,v41,s55,v57\nv_mfma_f32_16x16x32_bf16 v[216:219],a[168:171],v[100:103],v[216:219]\nv_fma_f32 v42,v42,s55,v57\n" \
"v_fma_f32 v43,v43,s55,v57\nv_mul_f32 v40,v36,v40\nv_mfma_f32_16x16x32_bf16 v[220:223],a[140:143],v[96:99],v[220:223]\nv_mul_f32 v41,v37,v41\n" \
"v_mul_f32 v42,v38,v42\nv_mul_f32 v43,v39,v43\nv_mfma_f32_16x16x32_bf16 v[220:223],a[172:175],v[100:103],v[220:223]\nv_exp_f32 v40,v40\n" \
"v_mfma_f32_16x16x32_bf16 v[224:227],a[176:179],v[100:103],v[224:227]\ns_waitcnt lgkmcnt(0)\ns_waitcnt vmcnt(7)\nv_mfma_f32_16x16x32_bf16 v[224:227],a[208:211],v[104:107],v[224:227]\n" \
"v_exp_f32 v41,v41\nv_mfma_f32_16x16x32_bf16 v[228:231],a[180:183],v[100:103],v[228:231]\nv_exp_f32 v42,v42\nv_mfma_f32_16x16x32_bf16 v[228:231],a[212:215],v[104:107],v[228:231]\n" \
"v_exp_f32 v43,v43\nv_add_f32 v40,1.0,v40\nv_mfma_f32_16x16x32_bf16 v[232:235],a[184:187],v[100:103],v[232:235]\nv_add_f32 v41,1.0,v41\n" \
"v_add_f32 v42,1.0,v42\nv_add_f32 v43,1.0,v43\nv_mfma_f32_16x16x32_bf16 v[232:235],a[216:219],v[104:107],v[232:235]\nv_rcp_f32 v40,v40\n" \
"v_mfma_f32_16x16x32_bf16 v[236:239],a[188:191],v[100:103],v[236:239]\nv_rcp_f32 v41,v41\nv_rcp_f32 v42,v42\nv_rcp_f32 v43,v43\n" \
"v_mul_f32 v36,v36,v40\nv_mul_f32 v37,v37,v41\nv_mul_f32 v38,v38,v42\nv_mul_f32 v39,v39,v43\n" \
"v_cvt_pk_bf16_f32 v40,v36,v37\nv_cvt_pk_bf16_f32 v41,v38,v39\nv_mfma_f32_16x16x32_bf16 v[236:239],a[220:223],v[104:107],v[236:239]\nbuffer_store_short v253,v62,s[36:39],s52 offen sc1\n" \
"v_mfma_f32_16x16x32_bf16 v[208:211],a[192:195],v[104:107],v[208:211]\nv_and_b32 v58,0x3ff,v59\nv_add_u32 v58,s99,v58\nv_mfma_f32_16x16x32_bf16 v[212:215],a[196:199],v[104:107],v[212:215]\n" \
"v_add_u32 v46,s98,v58\nds_read_b64 v[56:57],v46 offset:28680\nv_mfma_f32_16x16x32_bf16 v[216:219],a[200:203],v[104:107],v[216:219]\nbuffer_store_dwordx2 v[40:41],v61,s[36:39],s52 offen sc1\n" \
"global_load_dwordx4 v[72:75],v59,s[12:13] sc1\nv_mfma_f32_16x16x32_bf16 v[220:223],a[204:207],v[104:107],v[220:223]\nv_mfma_f32_16x16x32_bf16 v[16:19],a[112:115],v[192:195],v[16:19]\nglobal_load_dwordx4 v[76:79],v59,s[12:13] offset:1024 sc1\n" \
"s_waitcnt vmcnt(10)\nv_mfma_f32_16x16x32_bf16 v[16:19],a[144:147],v[196:199],v[16:19]\ns_waitcnt lgkmcnt(0)\ns_barrier\n" \
"s_mov_b32 m0,s58\nv_mfma_f32_16x16x32_bf16 v[20:23],a[116:119],v[192:195],v[20:23]\nglobal_load_dwordx4 v[80:83],v59,s[12:13] offset:2048 sc1\nds_write_addtid_b32 v208\n" \
"v_mfma_f32_16x16x32_bf16 v[20:23],a[148:151],v[196:199],v[20:23]\nds_write_addtid_b32 v209 offset:256\nds_write_addtid_b32 v210 offset:512\nv_mfma_f32_16x16x32_bf16 v[24:27],a[120:123],v[192:195],v[24:27]\n" \
"global_load_dwordx4 v[84:87],v59,s[14:15] sc1\nds_write_addtid_b32 v211 offset:768\nv_mfma_f32_16x16x32_bf16 v[24:27],a[152:155],v[196:199],v[24:27]\nds_write_addtid_b32 v212 offset:1024\n" \
"ds_write_addtid_b32 v213 offset:1280\nv_mfma_f32_16x16x32_bf16 v[28:31],a[124:127],v[192:195],v[28:31]\nglobal_load_dwordx4 v[88:91],v59,s[14:15] offset:1024 sc1\nds_write_addtid_b32 v214 offset:1536\n" \
"v_mfma_f32_16x16x32_bf16 v[28:31],a[156:159],v[196:199],v[28:31]\nds_write_addtid_b32 v215 offset:1792\nds_write_addtid_b32 v216 offset:2048\nv_mfma_f32_16x16x32_bf16 v[0:3],a[128:131],v[196:199],v[0:3]\n" \
"global_load_dwordx4 v[92:95],v59,s[14:15] offset:2048 sc1\ns_waitcnt vmcnt(13)\nv_mfma_f32_16x16x32_bf16 v[0:3],a[160:163],v[200:203],v[0:3]\nds_write_addtid_b32 v217 offset:2304\n" \
"ds_write_addtid_b32 v218 offset:2560\nv_mfma_f32_16x16x32_bf16 v[4:7],a[132:135],v[196:199],v[4:7]\nglobal_load_dwordx4 v[96:99],v59,s[16:17] sc1\nds_write_addtid_b32 v219 offset:2816\n" \
"v_mfma_f32_16x16x32_bf16 v[4:7],a[164:167],v[200:203],v[4:7]\nds_write_addtid_b32 v220 offset:3072\nds_write_addtid_b32 v221 offset:3328\nv_mfma_f32_16x16x32_bf16 v[8:11],a[136:139],v[196:199],v[8:11]\n" \
"global_load_dwordx4 v[100:103],v59,s[16:17] offset:1024 sc1\nds_write_addtid_b32 v222 offset:3584\nv_mfma_f32_16x16x32_bf16 v[8:11],a[168:171],v[200:203],v[8:11]\nds_write_addtid_b32 v223 offset:3840\n" \
"ds_write_addtid_b32 v224 offset:4096\nv_mfma_f32_16x16x32_bf16 v[12:15],a[140:143],v[196:199],v[12:15]\nglobal_load_dwordx4 v[104:107],v59,s[16:17] offset:2048 sc1\nds_write_addtid_b32 v225 offset:4352\n" \
"v_mfma_f32_16x16x32_bf16 v[12:15],a[172:175],v[200:203],v[12:15]\nds_write_addtid_b32 v226 offset:4608\nds_write_addtid_b32 v227 offset:4864\nv_mfma_f32_16x16x32_bf16 v[16:19],a[176:179],v[200:203],v[16:19]\n" \
"ds_write_addtid_b32 v228 offset:5120\ns_waitcnt vmcnt(15)\nv_mfma_f32_16x16x32_bf16 v[16:19],a[208:211],v[204:207],v[16:19]\nds_write_addtid_b32 v229 offset:5376\n" \
"ds_write_addtid_b32 v230 offset:5632\nv_mfma_f32_16x16x32_bf16 v[20:23],a[180:183],v[200:203],v[20:23]\nds_write_addtid_b32 v231 offset:5888\nds_write_addtid_b32 v232 offset:6144\n" \
"v_mfma_f32_16x16x32_bf16 v[20:23],a[212:215],v[204:207],v[20:23]\nds_write_addtid_b32 v233 offset:6400\nds_write_addtid_b32 v234 offset:6656\nv_mfma_f32_16x16x32_bf16 v[24:27],a[184:187],v[200:203],v[24:27]\n" \
"ds_write_addtid_b32 v235 offset:6912\nds_write_addtid_b32 v236 offset:7168\nv_mfma_f32_16x16x32_bf16 v[24:27],a[216:219],v[204:207],v[24:27]\nds_write_addtid_b32 v237 offset:7424\n" \
"ds_write_addtid_b32 v238 offset:7680\nv_mfma_f32_16x16x32_bf16 v[28:31],a[188:191],v[200:203],v[28:31]\nds_write_addtid_b32 v239 offset:7936\nv_mfma_f32_16x16x32_bf16 v[28:31],a[220:223],v[204:207],v[28:31]\n" \
"v_mfma_f32_16x16x32_bf16 v[0:3],a[192:195],v[204:207],v[0:3]\nv_mfma_f32_16x16x32_bf16 v[4:7],a[196:199],v[204:207],v[4:7]\nv_mfma_f32_16x16x32_bf16 v[8:11],a[200:203],v[204:207],v[8:11]\nv_mfma_f32_16x16x32_bf16 v[12:15],a[204:207],v[204:207],v[12:15]\n" \
"s_waitcnt vmcnt(7)\nv_mfma_f32_16x16x32_bf16 v[208:211],v[128:131],v[72:75],0\ns_mov_b64 exec,1\nbuffer_store_dword v61,off,s[36:39],s54 sc1\n" \
"s_mov_b64 exec,-1\nv_mfma_f32_16x16x32_bf16 v[208:211],v[160:163],v[76:79],v[208:211]\nv_cmp_eq_u32 vcc,0,v63\nv_mfma_f32_16x16x32_bf16 v[212:215],v[132:135],v[72:75],0\n" \
"s_waitcnt lgkmcnt(0)\ns_barrier\nv_mfma_f32_16x16x32_bf16 v[212:215],v[164:167],v[76:79],v[212:215]\ns_and_b64 vcc,vcc,s[86:87]\n" \
"s_cbranch_scc1 .LsbF_%=\n.LbbF_%=:\nv_mfma_f32_16x16x32_bf16 v[216:219],v[136:139],v[72:75],0\nglobal_load_dwordx4 v[108:111],v59,s[4:5] sc1\n" \
"ds_write_addtid_b32 v0 offset:32768\nv_mfma_f32_16x16x32_bf16 v[216:219],v[168:171],v[76:79],v[216:219]\nds_write_addtid_b32 v1 offset:33024\nds_write_addtid_b32 v2 offset:33280\n" \
"v_mfma_f32_16x16x32_bf16 v[220:223],v[140:143],v[72:75],0\nglobal_load_dwordx4 v[112:115],v59,s[4:5] offset:1024 sc1\nds_write_addtid_b32 v3 offset:33536\nv_mfma_f32_16x16x32_bf16 v[220:223],v[172:175],v[76:79],v[220:223]\n" \
"ds_write_addtid_b32 v4 offset:33792\nds_write_addtid_b32 v5 offset:34048\nv_mfma_f32_16x16x32_bf16 v[224:227],v[144:147],v[72:75],0\nglobal_load_dwordx4 v[116:119],v59,s[4:5] offset:2048 sc1\n" \
"ds_write_addtid_b32 v6 offset:34304\nv_mfma_f32_16x16x32_bf16 v[224:227],v[176:179],v[76:79],v[224:227]\nds_write_addtid_b32 v7 offset:34560\nds_write_addtid_b32 v8 offset:34816\n" \
"v_mfma_f32_16x16x32_bf16 v[228:231],v[148:151],v[72:75],0\nglobal_load_dwordx4 v[120:123],v59,s[6:7] sc1\nds_write_addtid_b32 v9 offset:35072\nv_mfma_f32_16x16x32_bf16 v[228:231],v[180:183],v[76:79],v[228:231]\n" \
"ds_write_addtid_b32 v10 offset:35328\nds_write_addtid_b32 v11 offset:35584\nv_mfma_f32_16x16x32_bf16 v[232:235],v[152:155],v[72:75],0\nglobal_load_dwordx4 v[124:127],v59,s[6:7] offset:1024 sc1\n" \
"ds_write_addtid_b32 v12 offset:35840\nv_mfma_f32_16x16x32_bf16 v[232:235],v[184:187],v[76:79],v[232:235]\nds_write_addtid_b32 v13 offset:36096\nds_write_addtid_b32 v14 offset:36352\n" \
"v_mfma_f32_16x16x32_bf16 v[236:239],v[156:159],v[72:75],0\nglobal_load_dwordx4 v[192:195],v59,s[6:7] offset:2048 sc1\nds_write_addtid_b32 v15 offset:36608\nv_mfma_f32_16x16x32_bf16 v[236:239],v[188:191],v[76:79],v[236:239]\n" \
"ds_write_addtid_b32 v16 offset:36864\ns_waitcnt vmcnt(12)\nv_mfma_f32_16x16x32_bf16 v[208:211],a[0:3],v[80:83],v[208:211]\nglobal_load_dwordx4 v[196:199],v59,s[8:9] sc1\n" \
"ds_write_addtid_b32 v17 offset:37120\nv_mfma_f32_16x16x32_bf16 v[208:211],a[32:35],v[84:87],v[208:211]\nds_write_addtid_b32 v18 offset:37376\nds_write_addtid_b32 v19 offset:37632\n" \
"v_mfma_f32_16x16x32_bf16 v[212:215],a[4:7],v[80:83],v[212:215]\nglobal_load_dwordx4 v[200:203],v59,s[8:9] offset:1024 sc1\nds_write_addtid_b32 v20 offset:37888\nv_mfma_f32_16x16x32_bf16 v[212:215],a[36:39],v[84:87],v[212:215]\n" \
"ds_write_addtid_b32 v21 offset:38144\nds_write_addtid_b32 v22 offset:38400\nv_mfma_f32_16x16x32_bf16 v[216:219],a[8:11],v[80:83],v[216:219]\nglobal_load_dwordx4 v[204:207],v59,s[8:9] offset:2048 sc1\n" \
"ds_write_addtid_b32 v23 offset:38656\nv_mfma_f32_16x16x32_bf16 v[216:219],a[40:43],v[84:87],v[216:219]\nds_write_addtid_b32 v24 offset:38912\nds_write_addtid_b32 v25 offset:39168\n" \
"v_mfma_f32_16x16x32_bf16 v[220:223],a[12:15],v[80:83],v[220:223]\nds_write_addtid_b32 v26 offset:39424\nds_write_addtid_b32 v27 offset:39680\nv_mfma_f32_16x16x32_bf16 v[220:223],a[44:47],v[84:87],v[220:223]\n" \
"ds_write_addtid_b32 v28 offset:39936\nds_write_addtid_b32 v29 offset:40192\nv_mfma_f32_16x16x32_bf16 v[224:227],a[16:19],v[80:83],v[224:227]\nds_write_addtid_b32 v30 offset:40448\n" \
"ds_write_addtid_b32 v31 offset:40704\nv_mfma_f32_16x16x32_bf16 v[224:227],a[48:51],v[84:87],v[224:227]\nds_read_b128 v[32:35],v58\nds_read_b128 v[36:39],v58 offset:8192\n" \
"v_mfma_f32_16x16x32_bf16 v[228:231],a[20:23],v[80:83],v[228:231]\nds_read_b128 v[40:43],v58 offset:16384\nds_read_b128 v[44:47],v58 offset:24576\nv_mfma_f32_16x16x32_bf16 v[228:231],a[52:55],v[84:87],v[228:231]\n" \
"ds_read_b128 v[48:51],v58 offset:4096\nds_read_b128 v[240:243],v58 offset:12288\nv_mfma_f32_16x16x32_bf16 v[232:235],a[24:27],v[80:83],v[232:235]\nds_read_b128 v[244:247],v58 offset:20480\n" \
"ds_read_b128 v[248:251],v58 offset:28672\nv_mfma_f32_16x16x32_bf16 v[232:235],a[56:59],v[84:87],v[232:235]\nv_mfma_f32_16x16x32_bf16 v[236:239],a[28:31],v[80:83],v[236:239]\ns_waitcnt lgkmcnt(0)\n" \
"s_barrier\nv_mfma_f32_16x16x32_bf16 v[236:239],a[60:63],v[84:87],v[236:239]\ns_waitcnt vmcnt(13)\nv_mfma_f32_16x16x32_bf16 v[208:211],a[64:67],v[88:91],v[208:211]\n" \
"v_mfma_f32_16x16x32_bf16 v[208:211],a[96:99],v[92:95],v[208:211]\nv_mfma_f32_16x16x32_bf16 v[212:215],a[68:71],v[88:91],v[212:215]\nv_add_f32 v32,v32,v36\nv_mfma_f32_16x16x32_bf16 v[212:215],a[100:103],v[92:95],v[212:215]\n" \
"v_add_f32 v33,v33,v37\nv_add_f32 v34,v34,v38\nv_mfma_f32_16x16x32_bf16 v[216:219],a[72:75],v[88:91],v[216:219]\nv_add_f32 v35,v35,v39\n" \
"v_add_f32 v40,v40,v44\nv_mfma_f32_16x16x32_bf16 v[216:219],a[104:107],v[92:95],v[216:219]\nv_add_f32 v41,v41,v45\nv_add_f32 v42,v42,v46\n" \
"v_mfma_f32_16x16x32_bf16 v[220:223],a[76:79],v[88:91],v[220:223]\nv_add_f32 v43,v43,v47\nv_add_f32 v32,v32,v40\nv_mfma_f32_16x16x32_bf16 v[220:223],a[108:111],v[92:95],v[220:223]\n" \
"v_add_f32 v33,v33,v41\nv_add_f32 v34,v34,v42\nv_mfma_f32_16x16x32_bf16 v[224:227],a[80:83],v[88:91],v[224:227]\nv_add_f32 v35,v35,v43\n" \
"v_cvt_pk_bf16_f32 v36,v32,v33\nv_mfma_f32_16x16x32_bf16 v[224:227],a[112:115],v[92:95],v[224:227]\nv_cvt_pk_bf16_f32 v37,v34,v35\nv_mfma_f32_16x16x32_bf16 v[228:231],a[84:87],v[88:91],v[228:231]\n" \
"v_add_f32 v48,v48,v240\nv_add_f32 v49,v49,v241\nv_mfma_f32_16x16x32_bf16 v[228:231],a[116:119],v[92:95],v[228:231]\nv_add_f32 v50,v50,v242\n" \
"v_add_f32 v51,v51,v243\nv_mfma_f32_16x16x32_bf16 v[232:235],a[88:91],v[88:91],v[232:235]\nv_add_f32 v244,v244,v248\nv_add_f32 v245,v245,v249\n" \
"v_mfma_f32_16x16x32_bf16 v[232:235],a[120:123],v[92:95],v[232:235]\nv_add_f32 v246,v246,v250\nv_add_f32 v247,v247,v251\nv_mfma_f32_16x16x32_bf16 v[236:239],a[92:95],v[88:91],v[236:239]\n" \
"v_add_f32 v48,v48,v244\nv_add_f32 v49,v49,v245\nv_mfma_f32_16x16x32_bf16 v[236:239],a[124:127],v[92:95],v[236:239]\nv_add_f32 v50,v50,v246\n" \
"s_waitcnt vmcnt(11)\nv_mfma_f32_16x16x32_bf16 v[208:211],a[128:131],v[96:99],v[208:211]\nv_add_f32 v51,v51,v247\nv_cvt_pk_bf16_f32 v38,v48,v49\n" \
"v_mfma_f32_16x16x32_bf16 v[208:211],a[160:163],v[100:103],v[208:211]\nv_cvt_pk_bf16_f32 v39,v50,v51\nbuffer_store_dwordx4 v[36:39],v59,s[36:39],s74 offen sc1\nds_read_b128 v[32:35],v58 offset:32768\n" \
"v_mfma_f32_16x16x32_bf16 v[212:215],a[132:135],v[96:99],v[212:215]\nds_read_b128 v[36:39],v58 offset:40960\nds_read_b128 v[40:43],v58 offset:49152\nv_mfma_f32_16x16x32_bf16 v[212:215],a[164:167],v[100:103],v[212:215]\n" \
"ds_read_b128 v[44:47],v58 offset:57344\nds_read_b128 v[48:51],v58 offset:36864\nv_mfma_f32_16x16x32_bf16 v[216:219],a[136:139],v[96:99],v[216:219]\nds_read_b128 v[240:243],v58 offset:45056\n" \
"ds_read_b128 v[244:247],v58 offset:53248\nv_mfma_f32_16x16x32_bf16 v[216:219],a[168:171],v[100:103],v[216:219]\nds_read_b128 v[248:251],v58 offset:61440\nv_mfma_f32_16x16x32_bf16 v[220:223],a[140:143],v[96:99],v[220:223]\n" \
"v_mfma_f32_16x16x32_bf16 v[220:223],a[172:175],v[100:103],v[220:223]\nv_mfma_f32_16x16x32_bf16 v[224:227],a[144:147],v[96:99],v[224:227]\nv_mfma_f32_16x16x32_bf16 v[224:227],a[176:179],v[100:103],v[224:227]\nv_mfma_f32_16x16x32_bf16 v[228:231],a[148:151],v[96:99],v[228:231]\n" \
"v_mfma_f32_16x16x32_bf16 v[228:231],a[180:183],v[100:103],v[228:231]\ns_waitcnt lgkmcnt(4)\nv_add_f32 v32,v32,v36\nv_add_f32 v33,v33,v37\n" \
"v_mfma_f32_16x16x32_bf16 v[232:235],a[152:155],v[96:99],v[232:235]\nv_add_f32 v34,v34,v38\nv_add_f32 v35,v35,v39\nv_add_f32 v40,v40,v44\n" \
"v_mfma_f32_16x16x32_bf16 v[232:235],a[184:187],v[100:103],v[232:235]\nv_add_f32 v41,v41,v45\nv_add_f32 v42,v42,v46\nv_add_f32 v43,v43,v47\n" \
"v_mfma_f32_16x16x32_bf16 v[236:239],a[156:159],v[96:99],v[236:239]\nv_add_f32 v32,v32,v40\nv_add_f32 v33,v33,v41\nv_add_f32 v34,v34,v42\n" \
"v_mfma_f32_16x16x32_bf16 v[236:239],a[188:191],v[100:103],v[236:239]\nv_add_f32 v35,v35,v43\nv_cvt_pk_bf16_f32 v36,v32,v33\ns_waitcnt vmcnt(11)\n" \
"v_mfma_f32_16x16x32_bf16 v[208:211],a[192:195],v[104:107],v[208:211]\nv_cvt_pk_bf16_f32 v37,v34,v35\ns_waitcnt lgkmcnt(0)\nv_add_f32 v48,v48,v240\n" \
"v_mfma_f32_16x16x32_bf16 v[212:215],a[196:199],v[104:107],v[212:215]\nv_add_f32 v49,v49,v241\nv_add_f32 v50,v50,v242\nv_add_f32 v51,v51,v243\n" \
"v_mfma_f32_16x16x32_bf16 v[216:219],a[200:203],v[104:107],v[216:219]\nv_add_f32 v244,v244,v248\nv_add_f32 v245,v245,v249\nv_mfma_f32_16x16x32_bf16 v[220:223],a[204:207],v[104:107],v[220:223]\n" \
"v_add_f32 v246,v246,v250\nv_add_f32 v247,v247,v251\nv_mfma_f32_16x16x32_bf16 v[224:227],a[208:211],v[104:107],v[224:227]\nv_add_f32 v48,v48,v244\n" \
"v_add_f32 v49,v49,v245\nv_mfma_f32_16x16x32_bf16 v[228:231],a[212:215],v[104:107],v[228:231]\nv_add_f32 v50,v50,v246\nv_add_f32 v51,v51,v247\n" \
"v_mfma_f32_16x16x32_bf16 v[232:235],a[216:219],v[104:107],v[232:235]\nv_cvt_pk_bf16_f32 v38,v48,v49\nv_cvt_pk_bf16_f32 v39,v50,v51\nv_mfma_f32_16x16x32_bf16 v[236:239],a[220:223],v[104:107],v[236:239]\n" \
"v_add_u32 v59,409600,v59\nbuffer_store_dwordx4 v[36:39],v59,s[36:39],s74 offen sc1\ns_branch .Le_%=\n.Lzl_%=:\n" \
"s_branch .Lzr_%=\n.Lls_%=:\nv_mfma_f32_16x16x32_bf16 v[20:23],v[64:67],a[0:3],0\nv_and_b32 v59,0x3ff,v59\n" \
"v_mfma_f32_16x16x32_bf16 v[20:23],v[68:71],a[16:19],v[20:23]\ns_add_u32 s4,s12,409600\ns_addc_u32 s5,s13,0\nv_mfma_f32_16x16x32_bf16 v[24:27],v[64:67],a[4:7],0\n" \
"s_add_u32 s6,s14,409600\ns_addc_u32 s7,s15,0\nv_mfma_f32_16x16x32_bf16 v[24:27],v[68:71],a[20:23],v[24:27]\ns_add_u32 s8,s16,409600\n" \
"s_addc_u32 s9,s17,0\nv_mfma_f32_16x16x32_bf16 v[28:31],v[64:67],a[8:11],0\nds_write_addtid_b32 v0\nds_write_addtid_b32 v1 offset:544\n" \
"v_mfma_f32_16x16x32_bf16 v[28:31],v[68:71],a[24:27],v[28:31]\nds_write_addtid_b32 v2 offset:256\nds_write_addtid_b32 v3 offset:800\nv_mfma_f32_16x16x32_bf16 v[32:35],v[64:67],a[12:15],0\n" \
"ds_write_addtid_b32 v4 offset:1056\nds_write_addtid_b32 v5 offset:1600\nv_mfma_f32_16x16x32_bf16 v[32:35],v[68:71],a[28:31],v[32:35]\nds_write_addtid_b32 v6 offset:1312\n" \
"s_waitcnt vmcnt(16)\nv_mfma_f32_16x16x32_bf16 v[20:23],v[72:75],a[32:35],v[20:23]\nv_add_u32 v71,s57,v59\ns_waitcnt vmcnt(15)\n" \
"v_mfma_f32_16x16x32_bf16 v[20:23],v[76:79],a[48:51],v[20:23]\ns_waitcnt vmcnt(8)\nv_cmp_eq_u32 vcc,0,v63\nv_mfma_f32_16x16x32_bf16 v[24:27],v[72:75],a[36:39],v[24:27]\n" \
"ds_read_b32 v64,v71 offset:28672\nds_read_b64 v[68:69],v71 offset:29696\nv_mfma_f32_16x16x32_bf16 v[24:27],v[76:79],a[52:55],v[24:27]\ns_and_b64 vcc,vcc,s[86:87]\n" \
"s_cbranch_scc1 .LsaS_%=\n.LbaS_%=:\nv_mfma_f32_16x16x32_bf16 v[28:31],v[72:75],a[40:43],v[28:31]\nds_write_addtid_b32 v7 offset:1856\n" \
"ds_write_addtid_b32 v8 offset:2112\nv_mfma_f32_16x16x32_bf16 v[28:31],v[76:79],a[56:59],v[28:31]\nds_write_addtid_b32 v9 offset:2656\nds_write_addtid_b32 v10 offset:2368\n" \
"v_mfma_f32_16x16x32_bf16 v[32:35],v[72:75],a[44:47],v[32:35]\ns_waitcnt lgkmcnt(4)\nv_xor_b32 v65,32,v64\nv_mfma_f32_16x16x32_bf16 v[32:35],v[76:79],a[60:63],v[32:35]\n" \
"v_xor_b32 v66,64,v64\nv_mfma_f32_16x16x32_bf16 v[20:23],v[80:83],a[64:67],v[20:23]\nglobal_load_dwordx4 v[72:75],v59,s[12:13] sc1\nv_mfma_f32_16x16x32_bf16 v[20:23],v[84:87],a[80:83],v[20:23]\n" \
"v_xor_b32 v67,96,v64\nv_add_u32 v70,0xf800,v68\nv_mfma_f32_16x16x32_bf16 v[24:27],v[80:83],a[68:71],v[24:27]\nglobal_load_dwordx4 v[76:79],v59,s[12:13] offset:1024 sc1\n" \
"v_add_u32 v71,0xf800,v69\nv_mfma_f32_16x16x32_bf16 v[24:27],v[84:87],a[84:87],v[24:27]\nds_write_addtid_b32 v11 offset:2912\nds_write_addtid_b32 v12 offset:3168\n" \
"v_mfma_f32_16x16x32_bf16 v[28:31],v[80:83],a[72:75],v[28:31]\nds_write_addtid_b32 v13 offset:3712\nds_write_addtid_b32 v14 offset:3424\nv_mfma_f32_16x16x32_bf16 v[28:31],v[84:87],a[88:91],v[28:31]\n" \
"ds_write_addtid_b32 v15 offset:3968\nds_write_addtid_b32 v16 offset:4224\nv_mfma_f32_16x16x32_bf16 v[32:35],v[80:83],a[76:79],v[32:35]\nds_write_addtid_b32 v17 offset:4512\n" \
"ds_write_addtid_b32 v18 offset:4800\nv_mfma_f32_16x16x32_bf16 v[32:35],v[84:87],a[92:95],v[32:35]\nds_write_addtid_b32 v19 offset:5088\nv_mfma_f32_16x16x32_bf16 v[20:23],v[88:91],a[96:99],v[20:23]\n" \
"global_load_dwordx4 v[80:83],v59,s[12:13] offset:2048 sc1\nv_mfma_f32_16x16x32_bf16 v[20:23],v[92:95],a[112:115],v[20:23]\nds_read_b64_tr_b16 a[0:1],v64\nds_read_b64_tr_b16 a[2:3],v64 offset:512\n" \
"v_mfma_f32_16x16x32_bf16 v[24:27],v[88:91],a[100:103],v[24:27]\nds_read_b64_tr_b16 a[4:5],v65\nds_read_b64_tr_b16 a[6:7],v65 offset:512\nv_mfma_f32_16x16x32_bf16 v[24:27],v[92:95],a[116:119],v[24:27]\n" \
"ds_read_b64_tr_b16 a[8:9],v66\nds_read_b64_tr_b16 a[10:11],v66 offset:512\nv_mfma_f32_16x16x32_bf16 v[28:31],v[88:91],a[104:107],v[28:31]\nds_read_b64_tr_b16 a[12:13],v67\n" \
"ds_read_b64_tr_b16 a[14:15],v67 offset:512\nv_mfma_f32_16x16x32_bf16 v[28:31],v[92:95],a[120:123],v[28:31]\ns_waitcnt lgkmcnt(0)\ns_barrier\n" \
"v_mfma_f32_16x16x32_bf16 v[32:35],v[88:91],a[108:111],v[32:35]\nds_read_b64_tr_b16 a[16:17],v64 offset:4096\nds_read_b64_tr_b16 a[18:19],v64 offset:4608\nv_mfma_f32_16x16x32_bf16 v[32:35],v[92:95],a[124:127],v[32:35]\n" \
"ds_read_b64_tr_b16 a[20:21],v65 offset:4096\ns_waitcnt vmcnt(10)\nv_mfma_f32_16x16x32_bf16 v[20:23],v[96:99],a[128:131],v[20:23]\nds_read_b64_tr_b16 a[22:23],v65 offset:4608\n" \
"s_waitcnt vmcnt(9)\nv_mfma_f32_16x16x32_bf16 v[20:23],v[100:103],a[144:147],v[20:23]\ns_add_u32 m0,s57,0\nv_mfma_f32_16x16x32_bf16 v[24:27],v[96:99],a[132:135],v[24:27]\n" \
"buffer_load_dwordx4 v68,s[40:43],s60 offen lds\nds_read_b64_tr_b16 a[24:25],v66 offset:4096\nv_mfma_f32_16x16x32_bf16 v[24:27],v[100:103],a[148:151],v[24:27]\nds_read_b64_tr_b16 a[26:27],v66 offset:4608\n" \
"ds_read_b64_tr_b16 a[28:29],v67 offset:4096\nv_mfma_f32_16x16x32_bf16 v[28:31],v[96:99],a[136:139],v[28:31]\nbuffer_load_dwordx4 v69,s[40:43],s60 offen offset:1024 lds\nds_read_b64_tr_b16 a[30:31],v67 offset:4608\n" \
"v_mfma_f32_16x16x32_bf16 v[28:31],v[100:103],a[152:155],v[28:31]\nds_read_b64_tr_b16 a[32:33],v64 offset:8192\nds_read_b64_tr_b16 a[34:35],v64 offset:8704\nv_mfma_f32_16x16x32_bf16 v[32:35],v[96:99],a[140:143],v[32:35]\n" \
"buffer_load_dwordx4 v70,s[40:43],s60 offen offset:2048 lds\nds_read_b64_tr_b16 a[36:37],v65 offset:8192\nv_mfma_f32_16x16x32_bf16 v[32:35],v[100:103],a[156:159],v[32:35]\nds_read_b64_tr_b16 a[38:39],v65 offset:8704\n" \
"s_waitcnt vmcnt(11)\nv_mfma_f32_16x16x32_bf16 v[20:23],v[104:107],a[160:163],v[20:23]\nbuffer_load_dwordx4 v71,s[40:43],s60 offen offset:3072 lds\ns_waitcnt vmcnt(11)\n" \
"v_mfma_f32_16x16x32_bf16 v[20:23],v[108:111],a[176:179],v[20:23]\nds_read_b64_tr_b16 a[40:41],v66 offset:8192\nds_read_b64_tr_b16 a[42:43],v66 offset:8704\nv_mfma_f32_16x16x32_bf16 v[24:27],v[104:107],a[164:167],v[24:27]\n" \
"global_load_dwordx4 v[84:87],v59,s[14:15] sc1\nds_read_b64_tr_b16 a[44:45],v67 offset:8192\nv_mfma_f32_16x16x32_bf16 v[24:27],v[108:111],a[180:183],v[24:27]\ns_waitcnt lgkmcnt(7)\n" \
"s_add_u32 m0,s57,4096\nv_mfma_f32_16x16x32_bf16 v[28:31],v[104:107],a[168:171],v[28:31]\nbuffer_load_dwordx4 v68,s[40:43],s61 offen lds\nds_read_b64_tr_b16 a[46:47],v67 offset:8704\n" \
"v_mfma_f32_16x16x32_bf16 v[28:31],v[108:111],a[184:187],v[28:31]\nds_read_b64_tr_b16 a[48:49],v64 offset:12288\nds_read_b64_tr_b16 a[50:51],v64 offset:12800\nv_mfma_f32_16x16x32_bf16 v[32:35],v[104:107],a[172:175],v[32:35]\n" \
"buffer_load_dwordx4 v69,s[40:43],s61 offen offset:1024 lds\nds_read_b64_tr_b16 a[52:53],v65 offset:12288\nv_mfma_f32_16x16x32_bf16 v[32:35],v[108:111],a[188:191],v[32:35]\nds_read_b64_tr_b16 a[54:55],v65 offset:12800\n" \
"s_waitcnt vmcnt(13)\nv_mfma_f32_16x16x32_bf16 v[20:23],v[112:115],a[192:195],v[20:23]\nbuffer_load_dwordx4 v70,s[40:43],s61 offen offset:2048 lds\ns_waitcnt vmcnt(13)\n" \
"v_mfma_f32_16x16x32_bf16 v[20:23],v[116:119],a[208:211],v[20:23]\nds_read_b64_tr_b16 a[56:57],v66 offset:12288\nds_read_b64_tr_b16 a[58:59],v66 offset:12800\nv_mfma_f32_16x16x32_bf16 v[24:27],v[112:115],a[196:199],v[24:27]\n" \
"buffer_load_dwordx4 v71,s[40:43],s61 offen offset:3072 lds\nds_read_b64_tr_b16 a[60:61],v67 offset:12288\nv_mfma_f32_16x16x32_bf16 v[24:27],v[116:119],a[212:215],v[24:27]\nds_read_b64_tr_b16 a[62:63],v67 offset:12800\n" \
"ds_read_b64_tr_b16 a[64:65],v64 offset:16384\nv_mfma_f32_16x16x32_bf16 v[28:31],v[112:115],a[200:203],v[28:31]\nglobal_load_dwordx4 v[88:91],v59,s[14:15] offset:1024 sc1\nds_read_b64_tr_b16 a[66:67],v64 offset:16896\n" \
"v_mfma_f32_16x16x32_bf16 v[28:31],v[116:119],a[216:219],v[28:31]\ns_waitcnt lgkmcnt(10)\ns_add_u32 m0,s57,8192\nv_mfma_f32_16x16x32_bf16 v[32:35],v[112:115],a[204:207],v[32:35]\n" \
"buffer_load_dwordx4 v68,s[40:43],s62 offen lds\nds_read_b64_tr_b16 a[68:69],v65 offset:16384\nv_mfma_f32_16x16x32_bf16 v[32:35],v[116:119],a[220:223],v[32:35]\nds_read_b64_tr_b16 a[70:71],v65 offset:16896\n" \
"s_waitcnt vmcnt(15)\nv_mfma_f32_16x16x32_bf16 v[20:23],v[120:123],a[224:227],v[20:23]\nbuffer_load_dwordx4 v69,s[40:43],s62 offen offset:1024 lds\ns_waitcnt vmcnt(15)\n" \
"v_mfma_f32_16x16x32_bf16 v[20:23],v[124:127],a[240:243],v[20:23]\nds_read_b64_tr_b16 a[72:73],v66 offset:16384\nds_read_b64_tr_b16 a[74:75],v66 offset:16896\nv_mfma_f32_16x16x32_bf16 v[24:27],v[120:123],a[228:231],v[24:27]\n" \
"buffer_load_dwordx4 v70,s[40:43],s62 offen offset:2048 lds\nds_read_b64_tr_b16 a[76:77],v67 offset:16384\nv_mfma_f32_16x16x32_bf16 v[24:27],v[124:127],a[244:247],v[24:27]\nds_read_b64_tr_b16 a[78:79],v67 offset:16896\n" \
"ds_read_b64_tr_b16 a[80:81],v64 offset:20480\nv_mfma_f32_16x16x32_bf16 v[28:31],v[120:123],a[232:235],v[28:31]\nbuffer_load_dwordx4 v71,s[40:43],s62 offen offset:3072 lds\nds_read_b64_tr_b16 a[82:83],v64 offset:20992\n" \
"v_mfma_f32_16x16x32_bf16 v[28:31],v[124:127],a[248:251],v[28:31]\nds_read_b64_tr_b16 a[84:85],v65 offset:20480\nds_read_b64_tr_b16 a[86:87],v65 offset:20992\nv_mfma_f32_16x16x32_bf16 v[32:35],v[120:123],a[236:239],v[32:35]\n" \
"global_load_dwordx4 v[92:95],v59,s[14:15] offset:2048 sc1\nds_read_b64_tr_b16 a[88:89],v66 offset:20480\nv_mfma_f32_16x16x32_bf16 v[32:35],v[124:127],a[252:255],v[32:35]\nds_read_b64_tr_b16 a[90:91],v66 offset:20992\n" \
"s_waitcnt vmcnt(17)\nv_mfma_f32_16x16x32_bf16 v[208:211],v[128:131],v[72:75],0\nglobal_load_dwordx4 v[108:111],v59,s[4:5] sc1\ns_waitcnt vmcnt(17)\n" \
"v_mfma_f32_16x16x32_bf16 v[208:211],v[160:163],v[76:79],v[208:211]\ns_waitcnt lgkmcnt(14)\ns_add_u32 m0,s57,12288\nv_mfma_f32_16x16x32_bf16 v[212:215],v[132:135],v[72:75],0\n" \
"buffer_load_dwordx4 v68,s[40:43],s63 offen lds\nds_read_b64_tr_b16 a[92:93],v67 offset:20480\nv_mfma_f32_16x16x32_bf16 v[212:215],v[164:167],v[76:79],v[212:215]\nds_read_b64_tr_b16 a[94:95],v67 offset:20992\n" \
"ds_read_b64_tr_b16 a[96:97],v64 offset:24576\nv_mfma_f32_16x16x32_bf16 v[216:219],v[136:139],v[72:75],0\nbuffer_load_dwordx4 v69,s[40:43],s63 offen offset:1024 lds\nds_read_b64_tr_b16 a[98:99],v64 offset:25088\n" \
"v_mfma_f32_16x16x32_bf16 v[216:219],v[168:171],v[76:79],v[216:219]\nds_read_b64_tr_b16 a[100:101],v65 offset:24576\nds_read_b64_tr_b16 a[102:103],v65 offset:25088\nv_mfma_f32_16x16x32_bf16 v[220:223],v[140:143],v[72:75],0\n" \
"buffer_load_dwordx4 v70,s[40:43],s63 offen offset:2048 lds\nds_read_b64_tr_b16 a[104:105],v66 offset:24576\nv_mfma_f32_16x16x32_bf16 v[220:223],v[172:175],v[76:79],v[220:223]\nds_read_b64_tr_b16 a[106:107],v66 offset:25088\n" \
"ds_read_b64_tr_b16 a[108:109],v67 offset:24576\nv_mfma_f32_16x16x32_bf16 v[224:227],v[144:147],v[72:75],0\nbuffer_load_dwordx4 v71,s[40:43],s63 offen offset:3072 lds\ns_mov_b32 m0,s56\n" \
"v_mfma_f32_16x16x32_bf16 v[224:227],v[176:179],v[76:79],v[224:227]\nds_write_addtid_b32 v20 offset:5344\nds_write_addtid_b32 v21 offset:5888\nv_mfma_f32_16x16x32_bf16 v[228:231],v[148:151],v[72:75],0\n" \
"global_load_dwordx4 v[112:115],v59,s[4:5] offset:1024 sc1\nds_write_addtid_b32 v22 offset:5600\nv_mfma_f32_16x16x32_bf16 v[228:231],v[180:183],v[76:79],v[228:231]\nds_write_addtid_b32 v23 offset:6144\n" \
"ds_write_addtid_b32 v24 offset:6400\nv_mfma_f32_16x16x32_bf16 v[232:235],v[152:155],v[72:75],0\nglobal_load_dwordx4 v[116:119],v59,s[4:5] offset:2048 sc1\nds_write_addtid_b32 v25 offset:6944\n" \
"v_mfma_f32_16x16x32_bf16 v[232:235],v[184:187],v[76:79],v[232:235]\nds_write_addtid_b32 v26 offset:6656\nds_write_addtid_b32 v27 offset:7200\nv_mfma_f32_16x16x32_bf16 v[236:239],v[156:159],v[72:75],0\n" \
"ds_write_addtid_b32 v28 offset:7456\nds_write_addtid_b32 v29 offset:8000\nv_mfma_f32_16x16x32_bf16 v[236:239],v[188:191],v[76:79],v[236:239]\ns_waitcnt lgkmcnt(15)\n" \
"s_waitcnt vmcnt(22)\nv_mfma_f32_16x16x32_bf16 v[208:211],a[0:3],v[80:83],v[208:211]\ns_waitcnt vmcnt(17)\nv_mfma_f32_16x16x32_bf16 v[208:211],a[32:35],v[84:87],v[208:211]\n" \
"ds_write_addtid_b32 v30 offset:7712\nds_write_addtid_b32 v31 offset:8256\nv_mfma_f32_16x16x32_bf16 v[212:215],a[4:7],v[80:83],v[212:215]\nds_write_addtid_b32 v32 offset:8512\n" \
"ds_write_addtid_b32 v33 offset:9056\nv_mfma_f32_16x16x32_bf16 v[212:215],a[36:39],v[84:87],v[212:215]\nds_write_addtid_b32 v34 offset:8768\nds_write_addtid_b32 v35 offset:9312\n" \
"v_mfma_f32_16x16x32_bf16 v[216:219],a[8:11],v[80:83],v[216:219]\ns_waitcnt lgkmcnt(15)\ns_add_u32 m0,s57,16384\nv_mfma_f32_16x16x32_bf16 v[216:219],a[40:43],v[84:87],v[216:219]\n" \
"buffer_load_dwordx4 v68,s[40:43],s64 offen lds\nds_read_b64_tr_b16 a[110:111],v67 offset:25088\nv_mfma_f32_16x16x32_bf16 v[220:223],a[12:15],v[80:83],v[220:223]\nv_mul_f32 v36,v36,v40\n" \
"v_mul_f32 v37,v37,v41\nv_mfma_f32_16x16x32_bf16 v[220:223],a[44:47],v[84:87],v[220:223]\nbuffer_load_dwordx4 v69,s[40:43],s64 offen offset:1024 lds\ns_waitcnt lgkmcnt(15)\n" \
"v_mfma_f32_16x16x32_bf16 v[224:227],a[16:19],v[80:83],v[224:227]\nv_mul_f32 v38,v38,v42\nv_mfma_f32_16x16x32_bf16 v[224:227],a[48:51],v[84:87],v[224:227]\nbuffer_load_dwordx4 v70,s[40:43],s64 offen offset:2048 lds\n" \
"v_mul_f32 v39,v39,v43\nv_mfma_f32_16x16x32_bf16 v[228:231],a[20:23],v[80:83],v[228:231]\ns_waitcnt lgkmcnt(0)\ns_barrier\n" \
"v_mfma_f32_16x16x32_bf16 v[228:231],a[52:55],v[84:87],v[228:231]\nbuffer_load_dwordx4 v71,s[40:43],s64 offen offset:3072 lds\nv_mfma_f32_16x16x32_bf16 v[232:235],a[24:27],v[80:83],v[232:235]\nds_read_b64_tr_b16 a[112:113],v64\n" \
"ds_read_b64_tr_b16 a[114:115],v64 offset:512\nv_mfma_f32_16x16x32_bf16 v[232:235],a[56:59],v[84:87],v[232:235]\nglobal_load_dwordx4 v[120:123],v59,s[6:7] sc1\nds_read_b64_tr_b16 a[116:117],v65\n" \
"v_mfma_f32_16x16x32_bf16 v[236:239],a[28:31],v[80:83],v[236:239]\ns_add_u32 m0,s57,20480\nv_mfma_f32_16x16x32_bf16 v[236:239],a[60:63],v[84:87],v[236:239]\ns_waitcnt vmcnt(17)\n" \
"v_mfma_f32_16x16x32_bf16 v[208:211],a[64:67],v[88:91],v[208:211]\ns_waitcnt vmcnt(12)\nv_mfma_f32_16x16x32_bf16 v[208:211],a[96:99],v[92:95],v[208:211]\nbuffer_load_dwordx4 v68,s[40:43],s65 offen lds\n" \
"ds_read_b64_tr_b16 a[118:119],v65 offset:512\nv_mfma_f32_16x16x32_bf16 v[212:215],a[68:71],v[88:91],v[212:215]\nds_read_b64_tr_b16 a[120:121],v66\nds_read_b64_tr_b16 a[122:123],v66 offset:512\n" \
"v_mfma_f32_16x16x32_bf16 v[212:215],a[100:103],v[92:95],v[212:215]\nbuffer_load_dwordx4 v69,s[40:43],s65 offen offset:1024 lds\nds_read_b64_tr_b16 a[124:125],v67\nv_mfma_f32_16x16x32_bf16 v[216:219],a[72:75],v[88:91],v[216:219]\n" \
"ds_read_b64_tr_b16 a[126:127],v67 offset:512\nv_mfma_f32_16x16x32_bf16 v[216:219],a[104:107],v[92:95],v[216:219]\nbuffer_load_dwordx4 v70,s[40:43],s65 offen offset:2048 lds\nds_read_b64_tr_b16 a[128:129],v64 offset:4096\n" \
"v_mfma_f32_16x16x32_bf16 v[220:223],a[76:79],v[88:91],v[220:223]\nds_read_b64_tr_b16 a[130:131],v64 offset:4608\nds_read_b64_tr_b16 a[132:133],v65 offset:4096\nv_mfma_f32_16x16x32_bf16 v[220:223],a[108:111],v[92:95],v[220:223]\n" \
"buffer_load_dwordx4 v71,s[40:43],s65 offen offset:3072 lds\nv_mfma_f32_16x16x32_bf16 v[224:227],a[80:83],v[88:91],v[224:227]\nds_read_b64_tr_b16 a[134:135],v65 offset:4608\nds_read_b64_tr_b16 a[136:137],v66 offset:4096\n" \
"v_mfma_f32_16x16x32_bf16 v[228:231],a[84:87],v[88:91],v[228:231]\nglobal_load_dwordx4 v[124:127],v59,s[6:7] offset:1024 sc1\nds_read_b64_tr_b16 a[138:139],v66 offset:4608\nv_mfma_f32_16x16x32_bf16 v[232:235],a[88:91],v[88:91],v[232:235]\n" \
"s_add_u32 m0,s57,24576\nv_mfma_f32_16x16x32_bf16 v[236:239],a[92:95],v[88:91],v[236:239]\nbuffer_load_dwordx4 v68,s[40:43],s66 offen lds\ns_waitcnt vmcnt(17)\n" \
"v_mfma_f32_16x16x32_bf16 v[0:3],v[128:131],v[108:111],0\nds_read_b64_tr_b16 a[140:141],v67 offset:4096\ns_waitcnt vmcnt(12)\nv_mfma_f32_16x16x32_bf16 v[0:3],v[160:163],v[112:115],v[0:3]\n" \
"buffer_load_dwordx4 v69,s[40:43],s66 offen offset:1024 lds\nds_read_b64_tr_b16 a[142:143],v67 offset:4608\nv_mfma_f32_16x16x32_bf16 v[4:7],v[132:135],v[108:111],0\ns_waitcnt vmcnt(20)\n" \
"ds_read_b64_tr_b16 a[144:145],v64 offset:8192\nv_mfma_f32_16x16x32_bf16 v[4:7],v[164:167],v[112:115],v[4:7]\nbuffer_load_dwordx4 v70,s[40:43],s66 offen offset:2048 lds\nds_read_b64_tr_b16 a[146:147],v64 offset:8704\n" \
"v_mfma_f32_16x16x32_bf16 v[8:11],v[136:139],v[108:111],0\nds_read_b64_tr_b16 a[148:149],v65 offset:8192\nds_read_b64_tr_b16 a[150:151],v65 offset:8704\nv_mfma_f32_16x16x32_bf16 v[8:11],v[168:171],v[112:115],v[8:11]\n" \
"buffer_load_dwordx4 v71,s[40:43],s66 offen offset:3072 lds\nds_read_b64_tr_b16 a[152:153],v66 offset:8192\nv_mfma_f32_16x16x32_bf16 v[12:15],v[140:143],v[108:111],0\nds_read_b64_tr_b16 a[154:155],v66 offset:8704\n" \
"ds_read_b64_tr_b16 a[156:157],v67 offset:8192\nv_mfma_f32_16x16x32_bf16 v[12:15],v[172:175],v[112:115],v[12:15]\nglobal_load_dwordx4 v[192:195],v59,s[6:7] offset:2048 sc1\nds_read_b64_tr_b16 a[158:159],v67 offset:8704\n" \
"v_mfma_f32_16x16x32_bf16 v[16:19],v[144:147],v[108:111],0\nv_cvt_pk_bf16_f32 v40,v36,v37\nv_cvt_pk_bf16_f32 v41,v38,v39\nv_mfma_f32_16x16x32_bf16 v[16:19],v[176:179],v[112:115],v[16:19]\n" \
"global_load_dwordx4 v[96:99],v59,s[16:17] sc1\nbuffer_store_dwordx2 v[40:41],v61,s[36:39],s52 offen sc1\nds_read_b128 v[36:39],v58\nds_read_b128 v[40:43],v58 offset:9568\n" \
"ds_read_b128 v[44:47],v58 offset:19136\nds_read_b128 v[48:51],v58 offset:28704\ns_waitcnt lgkmcnt(2)\nv_add_f32 v36,v36,v40\n" \
"v_add_f32 v37,v37,v41\nv_add_f32 v38,v38,v42\nv_add_f32 v39,v39,v43\nv_add_f32 v36,v36,v52\n" \
"v_add_f32 v37,v37,v53\nv_add_f32 v38,v38,v54\nv_add_f32 v39,v39,v55\ns_waitcnt lgkmcnt(0)\n" \
"v_add_f32 v44,v44,v48\nv_add_f32 v45,v45,v49\nv_add_f32 v46,v46,v50\nv_add_f32 v47,v47,v51\n" \
"v_add_f32 v36,v36,v44\nv_add_f32 v37,v37,v45\nv_add_f32 v38,v38,v46\nv_add_f32 v39,v39,v47\n" \
"v_mul_f32 v40,v36,v36\nv_mul_f32 v41,v37,v37\nv_mul_f32 v42,v38,v38\nv_mul_f32 v43,v39,v39\n" \
"v_fma_f32 v40,v40,s55,v57\nv_fma_f32 v41,v41,s55,v57\nv_fma_f32 v42,v42,s55,v57\nv_fma_f32 v43,v43,s55,v57\n" \
"v_mul_f32 v40,v36,v40\nv_mul_f32 v41,v37,v41\nv_mul_f32 v42,v38,v42\nv_mul_f32 v43,v39,v43\n" \
"v_exp_f32 v40,v40\nv_exp_f32 v41,v41\nv_exp_f32 v42,v42\nv_exp_f32 v43,v43\n" \
"v_add_f32 v40,1.0,v40\nv_add_f32 v41,1.0,v41\nv_add_f32 v42,1.0,v42\nv_add_f32 v43,1.0,v43\n" \
"v_rcp_f32 v40,v40\nv_rcp_f32 v41,v41\nv_rcp_f32 v42,v42\nv_rcp_f32 v43,v43\n" \
"v_mul_f32 v36,v36,v40\nv_mul_f32 v37,v37,v41\nv_mul_f32 v38,v38,v42\nv_mul_f32 v39,v39,v43\n" \
"v_cvt_pk_bf16_f32 v40,v36,v37\nv_cvt_pk_bf16_f32 v41,v38,v39\nv_mfma_f32_16x16x32_bf16 v[20:23],v[148:151],v[108:111],0\ns_add_u32 s52,s52,409600\n" \
"buffer_store_dwordx2 v[40:41],v61,s[36:39],s52 offen sc1\nv_mfma_f32_16x16x32_bf16 v[20:23],v[180:183],v[112:115],v[20:23]\nglobal_load_dwordx4 v[100:103],v59,s[16:17] offset:1024 sc1\nds_read_b32 v48,v60\n" \
"v_mfma_f32_16x16x32_bf16 v[24:27],v[152:155],v[108:111],0\nds_read_b32 v49,v60 offset:9568\nds_read_b32 v50,v60 offset:19136\nv_mfma_f32_16x16x32_bf16 v[24:27],v[184:187],v[112:115],v[24:27]\n" \
"global_load_dwordx4 v[104:107],v59,s[16:17] offset:2048 sc1\nds_read_b32 v51,v60 offset:28704\nv_mfma_f32_16x16x32_bf16 v[28:31],v[156:159],v[108:111],0\nds_read_b64_tr_b16 a[160:161],v64 offset:12288\n" \
"v_mfma_f32_16x16x32_bf16 v[28:31],v[188:191],v[112:115],v[28:31]\nglobal_load_dwordx4 v[196:199],v59,s[8:9] sc1\ns_waitcnt vmcnt(21)\nv_mfma_f32_16x16x32_bf16 v[0:3],a[0:3],v[116:119],v[0:3]\n" \
"ds_read_b64_tr_b16 a[162:163],v64 offset:12800\ns_waitcnt vmcnt(16)\nv_mfma_f32_16x16x32_bf16 v[0:3],a[32:35],v[120:123],v[0:3]\nglobal_load_dwordx4 v[200:203],v59,s[8:9] offset:1024 sc1\n" \
"ds_read_b64_tr_b16 a[164:165],v65 offset:12288\nv_mfma_f32_16x16x32_bf16 v[4:7],a[4:7],v[116:119],v[4:7]\nds_read_b64_tr_b16 a[166:167],v65 offset:12800\nds_read_b64_tr_b16 a[168:169],v66 offset:12288\n" \
"v_mfma_f32_16x16x32_bf16 v[4:7],a[36:39],v[120:123],v[4:7]\nglobal_load_dwordx4 v[204:207],v59,s[8:9] offset:2048 sc1\nds_read_b64_tr_b16 a[170:171],v66 offset:12800\nv_mfma_f32_16x16x32_bf16 v[8:11],a[8:11],v[116:119],v[8:11]\n" \
"s_add_u32 s59,s59,0x10000\nbuffer_load_dword v63,v61,s[36:39],s59 offen sc1\nv_mfma_f32_16x16x32_bf16 v[8:11],a[40:43],v[120:123],v[8:11]\nv_add_u32 v59,819200,v59\n" \
"ds_read_b64_tr_b16 a[172:173],v67 offset:12288\nv_mfma_f32_16x16x32_bf16 v[12:15],a[12:15],v[116:119],v[12:15]\nds_read_b64_tr_b16 a[174:175],v67 offset:12800\ns_waitcnt lgkmcnt(8)\n" \
"v_mfma_f32_16x16x32_bf16 v[12:15],a[44:47],v[120:123],v[12:15]\nv_add_f32 v48,v48,v49\nv_add_f32 v50,v50,v51\nv_mfma_f32_16x16x32_bf16 v[16:19],a[16:19],v[116:119],v[16:19]\n" \
"v_add_f32 v48,v48,v56\nv_add_f32 v48,v48,v50\nv_mfma_f32_16x16x32_bf16 v[16:19],a[48:51],v[120:123],v[16:19]\nv_mul_f32 v49,v48,v48\n" \
"v_fma_f32 v49,v49,s55,v57\nv_mfma_f32_16x16x32_bf16 v[20:23],a[20:23],v[116:119],v[20:23]\nv_mul_f32 v49,v48,v49\nv_mfma_f32_16x16x32_bf16 v[20:23],a[52:55],v[120:123],v[20:23]\n" \
"v_exp_f32 v49,v49\nv_mfma_f32_16x16x32_bf16 v[24:27],a[24:27],v[116:119],v[24:27]\nv_add_f32 v49,1.0,v49\nv_mfma_f32_16x16x32_bf16 v[24:27],a[56:59],v[120:123],v[24:27]\n" \
"v_rcp_f32 v49,v49\nv_mfma_f32_16x16x32_bf16 v[28:31],a[28:31],v[116:119],v[28:31]\nv_mul_f32 v48,v48,v49\nv_cvt_pk_bf16_f32 v49,v48,v48\n" \
"v_mfma_f32_16x16x32_bf16 v[28:31],a[60:63],v[120:123],v[28:31]\nbuffer_store_short v49,v62,s[36:39],s52 offen sc1\ns_waitcnt vmcnt(15)\nv_mfma_f32_16x16x32_bf16 v[0:3],a[64:67],v[124:127],v[0:3]\n" \
"ds_read_b128 v[36:39],v58 offset:5344\ns_waitcnt vmcnt(10)\nv_mfma_f32_16x16x32_bf16 v[0:3],a[96:99],v[192:195],v[0:3]\nds_read_b128 v[40:43],v58 offset:14912\n" \
"ds_read_b128 v[44:47],v58 offset:24480\nv_mfma_f32_16x16x32_bf16 v[4:7],a[68:71],v[124:127],v[4:7]\nds_read_b128 v[48:51],v58 offset:34048\ns_add_u32 s52,s52,409600\n" \
"v_mfma_f32_16x16x32_bf16 v[4:7],a[100:103],v[192:195],v[4:7]\nds_read_b64_tr_b16 a[176:177],v64 offset:16384\nv_mfma_f32_16x16x32_bf16 v[8:11],a[72:75],v[124:127],v[8:11]\nds_read_b64_tr_b16 a[178:179],v64 offset:16896\n" \
"ds_read_b64_tr_b16 a[180:181],v65 offset:16384\nv_mfma_f32_16x16x32_bf16 v[8:11],a[104:107],v[192:195],v[8:11]\nds_read_b64_tr_b16 a[182:183],v65 offset:16896\nds_read_b64_tr_b16 a[184:185],v66 offset:16384\n" \
"v_mfma_f32_16x16x32_bf16 v[12:15],a[76:79],v[124:127],v[12:15]\nds_read_b64_tr_b16 a[186:187],v66 offset:16896\nds_read_b64_tr_b16 a[188:189],v67 offset:16384\nv_mfma_f32_16x16x32_bf16 v[12:15],a[108:111],v[192:195],v[12:15]\n" \
"ds_read_b64_tr_b16 a[190:191],v67 offset:16896\nv_mfma_f32_16x16x32_bf16 v[16:19],a[80:83],v[124:127],v[16:19]\nv_mfma_f32_16x16x32_bf16 v[20:23],a[84:87],v[124:127],v[20:23]\ns_waitcnt lgkmcnt(10)\n" \
"v_add_f32 v36,v36,v40\nv_add_f32 v37,v37,v41\nv_mfma_f32_16x16x32_bf16 v[24:27],a[88:91],v[124:127],v[24:27]\nv_add_f32 v38,v38,v42\n" \
"v_add_f32 v39,v39,v43\nv_add_f32 v36,v36,v52\nv_mfma_f32_16x16x32_bf16 v[28:31],a[92:95],v[124:127],v[28:31]\nv_add_f32 v37,v37,v53\n" \
"v_add_f32 v38,v38,v54\nv_mfma_f32_16x16x32_bf16 v[224:227],a[112:115],v[92:95],v[224:227]\nv_add_f32 v39,v39,v55\ns_waitcnt vmcnt(9)\n" \
"v_mfma_f32_16x16x32_bf16 v[224:227],a[144:147],v[96:99],v[224:227]\nds_read_b64_tr_b16 a[192:193],v64 offset:20480\ns_waitcnt lgkmcnt(9)\nv_mfma_f32_16x16x32_bf16 v[228:231],a[116:119],v[92:95],v[228:231]\n" \
"ds_read_b64_tr_b16 a[194:195],v64 offset:20992\nds_read_b64_tr_b16 a[196:197],v65 offset:20480\nv_add_f32 v44,v44,v48\nv_mfma_f32_16x16x32_bf16 v[228:231],a[148:151],v[96:99],v[228:231]\n" \
"ds_read_b64_tr_b16 a[198:199],v65 offset:20992\nds_read_b64_tr_b16 a[200:201],v66 offset:20480\nv_add_f32 v45,v45,v49\nv_mfma_f32_16x16x32_bf16 v[232:235],a[120:123],v[92:95],v[232:235]\n" \
"ds_read_b64_tr_b16 a[202:203],v66 offset:20992\nds_read_b64_tr_b16 a[204:205],v67 offset:20480\nv_add_f32 v46,v46,v50\nv_mfma_f32_16x16x32_bf16 v[232:235],a[152:155],v[96:99],v[232:235]\n" \
"ds_read_b64_tr_b16 a[206:207],v67 offset:20992\nv_add_f32 v47,v47,v51\nv_add_f32 v36,v36,v44\nv_mfma_f32_16x16x32_bf16 v[236:239],a[124:127],v[92:95],v[236:239]\n" \
"v_add_f32 v37,v37,v45\nv_add_f32 v38,v38,v46\nv_add_f32 v39,v39,v47\nv_mfma_f32_16x16x32_bf16 v[236:239],a[156:159],v[96:99],v[236:239]\n" \
"v_mul_f32 v40,v36,v36\nv_mul_f32 v41,v37,v37\ns_waitcnt lgkmcnt(15)\nv_mfma_f32_16x16x32_bf16 v[208:211],a[128:131],v[96:99],v[208:211]\n" \
"v_mul_f32 v42,v38,v38\ns_waitcnt vmcnt(6)\nv_mfma_f32_16x16x32_bf16 v[208:211],a[160:163],v[100:103],v[208:211]\nds_read_b64_tr_b16 a[208:209],v64 offset:24576\n" \
"ds_read_b64_tr_b16 a[210:211],v64 offset:25088\nv_mfma_f32_16x16x32_bf16 v[212:215],a[132:135],v[96:99],v[212:215]\nds_read_b64_tr_b16 a[212:213],v65 offset:24576\nds_read_b64_tr_b16 a[214:215],v65 offset:25088\n" \
"v_mul_f32 v43,v39,v39\nv_mfma_f32_16x16x32_bf16 v[212:215],a[164:167],v[100:103],v[212:215]\nds_read_b64_tr_b16 a[216:217],v66 offset:24576\nds_read_b64_tr_b16 a[218:219],v66 offset:25088\n" \
"v_fma_f32 v40,v40,s55,v57\nv_mfma_f32_16x16x32_bf16 v[216:219],a[136:139],v[96:99],v[216:219]\nds_read_b64_tr_b16 a[220:221],v67 offset:24576\nds_read_b64_tr_b16 a[222:223],v67 offset:25088\n" \
"v_fma_f32 v41,v41,s55,v57\nv_mfma_f32_16x16x32_bf16 v[216:219],a[168:171],v[100:103],v[216:219]\nv_fma_f32 v42,v42,s55,v57\nv_fma_f32 v43,v43,s55,v57\n" \
"v_mul_f32 v40,v36,v40\nv_mfma_f32_16x16x32_bf16 v[220:223],a[140:143],v[96:99],v[220:223]\nv_mul_f32 v41,v37,v41\nv_mul_f32 v42,v38,v42\n" \
"v_mul_f32 v43,v39,v43\nv_mfma_f32_16x16x32_bf16 v[220:223],a[172:175],v[100:103],v[220:223]\nv_exp_f32 v40,v40\ns_waitcnt lgkmcnt(15)\n" \
"v_mfma_f32_16x16x32_bf16 v[224:227],a[176:179],v[100:103],v[224:227]\ns_waitcnt lgkmcnt(0)\ns_waitcnt vmcnt(5)\nv_mfma_f32_16x16x32_bf16 v[224:227],a[208:211],v[104:107],v[224:227]\n" \
"v_exp_f32 v41,v41\nv_mfma_f32_16x16x32_bf16 v[228:231],a[180:183],v[100:103],v[228:231]\nv_exp_f32 v42,v42\nv_mfma_f32_16x16x32_bf16 v[228:231],a[212:215],v[104:107],v[228:231]\n" \
"v_exp_f32 v43,v43\nv_add_f32 v40,1.0,v40\nv_mfma_f32_16x16x32_bf16 v[232:235],a[184:187],v[100:103],v[232:235]\nv_add_f32 v41,1.0,v41\n" \
"v_add_f32 v42,1.0,v42\nv_add_f32 v43,1.0,v43\nv_mfma_f32_16x16x32_bf16 v[232:235],a[216:219],v[104:107],v[232:235]\nv_rcp_f32 v40,v40\n" \
"v_mfma_f32_16x16x32_bf16 v[236:239],a[188:191],v[100:103],v[236:239]\nv_rcp_f32 v41,v41\nv_rcp_f32 v42,v42\nv_rcp_f32 v43,v43\n" \
"v_mul_f32 v36,v36,v40\nv_mul_f32 v37,v37,v41\nv_mul_f32 v38,v38,v42\nv_mul_f32 v39,v39,v43\n" \
"v_cvt_pk_bf16_f32 v40,v36,v37\nv_cvt_pk_bf16_f32 v41,v38,v39\nv_mfma_f32_16x16x32_bf16 v[236:239],a[220:223],v[104:107],v[236:239]\nv_and_b32 v58,0x3ff,v59\n" \
"v_mfma_f32_16x16x32_bf16 v[208:211],a[192:195],v[104:107],v[208:211]\nv_add_u32 v58,s99,v58\nv_add_u32 v46,s98,v58\nv_mfma_f32_16x16x32_bf16 v[212:215],a[196:199],v[104:107],v[212:215]\n" \
"ds_read_b64 v[56:57],v46 offset:28680\nbuffer_store_dwordx2 v[40:41],v61,s[36:39],s52 offen sc1\nv_mfma_f32_16x16x32_bf16 v[216:219],a[200:203],v[104:107],v[216:219]\nglobal_load_dwordx4 v[72:75],v59,s[12:13] sc1\n" \
"v_mfma_f32_16x16x32_bf16 v[220:223],a[204:207],v[104:107],v[220:223]\nv_mfma_f32_16x16x32_bf16 v[16:19],a[112:115],v[192:195],v[16:19]\nglobal_load_dwordx4 v[76:79],v59,s[12:13] offset:1024 sc1\ns_waitcnt vmcnt(7)\n" \
"v_mfma_f32_16x16x32_bf16 v[16:19],a[144:147],v[196:199],v[16:19]\ns_waitcnt lgkmcnt(0)\ns_barrier\ns_mov_b32 m0,s58\n" \
"v_mfma_f32_16x16x32_bf16 v[20:23],a[116:119],v[192:195],v[20:23]\nglobal_load_dwordx4 v[80:83],v59,s[12:13] offset:2048 sc1\nds_write_addtid_b32 v208\nv_mfma_f32_16x16x32_bf16 v[20:23],a[148:151],v[196:199],v[20:23]\n" \
"ds_write_addtid_b32 v209 offset:256\nds_write_addtid_b32 v210 offset:512\nv_mfma_f32_16x16x32_bf16 v[24:27],a[120:123],v[192:195],v[24:27]\nglobal_load_dwordx4 v[84:87],v59,s[14:15] sc1\n" \
"ds_write_addtid_b32 v211 offset:768\nv_mfma_f32_16x16x32_bf16 v[24:27],a[152:155],v[196:199],v[24:27]\nds_write_addtid_b32 v212 offset:1024\nds_write_addtid_b32 v213 offset:1280\n" \
"v_mfma_f32_16x16x32_bf16 v[28:31],a[124:127],v[192:195],v[28:31]\nglobal_load_dwordx4 v[88:91],v59,s[14:15] offset:1024 sc1\nds_write_addtid_b32 v214 offset:1536\nv_mfma_f32_16x16x32_bf16 v[28:31],a[156:159],v[196:199],v[28:31]\n" \
"ds_write_addtid_b32 v215 offset:1792\nds_write_addtid_b32 v216 offset:2048\nv_mfma_f32_16x16x32_bf16 v[0:3],a[128:131],v[196:199],v[0:3]\nglobal_load_dwordx4 v[92:95],v59,s[14:15] offset:2048 sc1\n" \
"s_waitcnt vmcnt(10)\nv_mfma_f32_16x16x32_bf16 v[0:3],a[160:163],v[200:203],v[0:3]\nds_write_addtid_b32 v217 offset:2304\nds_write_addtid_b32 v218 offset:2560\n" \
"v_mfma_f32_16x16x32_bf16 v[4:7],a[132:135],v[196:199],v[4:7]\nglobal_load_dwordx4 v[96:99],v59,s[16:17] sc1\nds_write_addtid_b32 v219 offset:2816\nv_mfma_f32_16x16x32_bf16 v[4:7],a[164:167],v[200:203],v[4:7]\n" \
"ds_write_addtid_b32 v220 offset:3072\nds_write_addtid_b32 v221 offset:3328\nv_mfma_f32_16x16x32_bf16 v[8:11],a[136:139],v[196:199],v[8:11]\nglobal_load_dwordx4 v[100:103],v59,s[16:17] offset:1024 sc1\n" \
"ds_write_addtid_b32 v222 offset:3584\nv_mfma_f32_16x16x32_bf16 v[8:11],a[168:171],v[200:203],v[8:11]\nds_write_addtid_b32 v223 offset:3840\nds_write_addtid_b32 v224 offset:4096\n" \
"v_mfma_f32_16x16x32_bf16 v[12:15],a[140:143],v[196:199],v[12:15]\nglobal_load_dwordx4 v[104:107],v59,s[16:17] offset:2048 sc1\nds_write_addtid_b32 v225 offset:4352\nv_mfma_f32_16x16x32_bf16 v[12:15],a[172:175],v[200:203],v[12:15]\n" \
"ds_write_addtid_b32 v226 offset:4608\nds_write_addtid_b32 v227 offset:4864\nv_mfma_f32_16x16x32_bf16 v[16:19],a[176:179],v[200:203],v[16:19]\nds_write_addtid_b32 v228 offset:5120\n" \
"s_waitcnt vmcnt(12)\nv_mfma_f32_16x16x32_bf16 v[16:19],a[208:211],v[204:207],v[16:19]\nds_write_addtid_b32 v229 offset:5376\nds_write_addtid_b32 v230 offset:5632\n" \
"v_mfma_f32_16x16x32_bf16 v[20:23],a[180:183],v[200:203],v[20:23]\nds_write_addtid_b32 v231 offset:5888\nds_write_addtid_b32 v232 offset:6144\nv_mfma_f32_16x16x32_bf16 v[20:23],a[212:215],v[204:207],v[20:23]\n" \
"ds_write_addtid_b32 v233 offset:6400\nds_write_addtid_b32 v234 offset:6656\nv_mfma_f32_16x16x32_bf16 v[24:27],a[184:187],v[200:203],v[24:27]\nds_write_addtid_b32 v235 offset:6912\n" \
"ds_write_addtid_b32 v236 offset:7168\nv_mfma_f32_16x16x32_bf16 v[24:27],a[216:219],v[204:207],v[24:27]\nds_write_addtid_b32 v237 offset:7424\nds_write_addtid_b32 v238 offset:7680\n" \
"v_mfma_f32_16x16x32_bf16 v[28:31],a[188:191],v[200:203],v[28:31]\nds_write_addtid_b32 v239 offset:7936\nv_mfma_f32_16x16x32_bf16 v[28:31],a[220:223],v[204:207],v[28:31]\nv_mfma_f32_16x16x32_bf16 v[0:3],a[192:195],v[204:207],v[0:3]\n" \
"v_mfma_f32_16x16x32_bf16 v[4:7],a[196:199],v[204:207],v[4:7]\nv_mfma_f32_16x16x32_bf16 v[8:11],a[200:203],v[204:207],v[8:11]\nv_mfma_f32_16x16x32_bf16 v[12:15],a[204:207],v[204:207],v[12:15]\ns_waitcnt vmcnt(7)\n" \
"v_mfma_f32_16x16x32_bf16 v[208:211],v[128:131],v[72:75],0\ns_mov_b64 exec,1\nbuffer_store_dword v61,off,s[36:39],s54 sc1\ns_mov_b64 exec,-1\n" \
"v_mfma_f32_16x16x32_bf16 v[208:211],v[160:163],v[76:79],v[208:211]\nv_cmp_eq_u32 vcc,0,v63\nv_mfma_f32_16x16x32_bf16 v[212:215],v[132:135],v[72:75],0\ns_waitcnt lgkmcnt(0)\n" \
"s_barrier\nv_mfma_f32_16x16x32_bf16 v[212:215],v[164:167],v[76:79],v[212:215]\ns_and_b64 vcc,vcc,s[86:87]\ns_cbranch_scc1 .LsbS_%=\n" \
".LbbS_%=:\nv_mfma_f32_16x16x32_bf16 v[216:219],v[136:139],v[72:75],0\nglobal_load_dwordx4 v[108:111],v59,s[4:5] sc1\nds_write_addtid_b32 v0 offset:32768\n" \
"v_mfma_f32_16x16x32_bf16 v[216:219],v[168:171],v[76:79],v[216:219]\nds_write_addtid_b32 v1 offset:33024\nds_write_addtid_b32 v2 offset:33280\nv_mfma_f32_16x16x32_bf16 v[220:223],v[140:143],v[72:75],0\n" \
"global_load_dwordx4 v[112:115],v59,s[4:5] offset:1024 sc1\nds_write_addtid_b32 v3 offset:33536\nv_mfma_f32_16x16x32_bf16 v[220:223],v[172:175],v[76:79],v[220:223]\nds_write_addtid_b32 v4 offset:33792\n" \
"ds_write_addtid_b32 v5 offset:34048\nv_mfma_f32_16x16x32_bf16 v[224:227],v[144:147],v[72:75],0\nglobal_load_dwordx4 v[116:119],v59,s[4:5] offset:2048 sc1\nds_write_addtid_b32 v6 offset:34304\n" \
"v_mfma_f32_16x16x32_bf16 v[224:227],v[176:179],v[76:79],v[224:227]\nds_write_addtid_b32 v7 offset:34560\nds_write_addtid_b32 v8 offset:34816\nv_mfma_f32_16x16x32_bf16 v[228:231],v[148:151],v[72:75],0\n" \
"global_load_dwordx4 v[120:123],v59,s[6:7] sc1\nds_write_addtid_b32 v9 offset:35072\nv_mfma_f32_16x16x32_bf16 v[228:231],v[180:183],v[76:79],v[228:231]\nds_write_addtid_b32 v10 offset:35328\n" \
"ds_write_addtid_b32 v11 offset:35584\nv_mfma_f32_16x16x32_bf16 v[232:235],v[152:155],v[72:75],0\nglobal_load_dwordx4 v[124:127],v59,s[6:7] offset:1024 sc1\nds_write_addtid_b32 v12 offset:35840\n" \
"v_mfma_f32_16x16x32_bf16 v[232:235],v[184:187],v[76:79],v[232:235]\nds_write_addtid_b32 v13 offset:36096\nds_write_addtid_b32 v14 offset:36352\nv_mfma_f32_16x16x32_bf16 v[236:239],v[156:159],v[72:75],0\n" \
"global_load_dwordx4 v[192:195],v59,s[6:7] offset:2048 sc1\nds_write_addtid_b32 v15 offset:36608\nv_mfma_f32_16x16x32_bf16 v[236:239],v[188:191],v[76:79],v[236:239]\nds_write_addtid_b32 v16 offset:36864\n" \
"s_waitcnt vmcnt(12)\nv_mfma_f32_16x16x32_bf16 v[208:211],a[0:3],v[80:83],v[208:211]\nglobal_load_dwordx4 v[196:199],v59,s[8:9] sc1\nds_write_addtid_b32 v17 offset:37120\n" \
"v_mfma_f32_16x16x32_bf16 v[208:211],a[32:35],v[84:87],v[208:211]\nds_write_addtid_b32 v18 offset:37376\nds_write_addtid_b32 v19 offset:37632\nv_mfma_f32_16x16x32_bf16 v[212:215],a[4:7],v[80:83],v[212:215]\n" \
"global_load_dwordx4 v[200:203],v59,s[8:9] offset:1024 sc1\nds_write_addtid_b32 v20 offset:37888\nv_mfma_f32_16x16x32_bf16 v[212:215],a[36:39],v[84:87],v[212:215]\nds_write_addtid_b32 v21 offset:38144\n" \
"ds_write_addtid_b32 v22 offset:38400\nv_mfma_f32_16x16x32_bf16 v[216:219],a[8:11],v[80:83],v[216:219]\nglobal_load_dwordx4 v[204:207],v59,s[8:9] offset:2048 sc1\nds_write_addtid_b32 v23 offset:38656\n" \
"v_mfma_f32_16x16x32_bf16 v[216:219],a[40:43],v[84:87],v[216:219]\nds_write_addtid_b32 v24 offset:38912\nds_write_addtid_b32 v25 offset:39168\nv_mfma_f32_16x16x32_bf16 v[220:223],a[12:15],v[80:83],v[220:223]\n" \
"ds_write_addtid_b32 v26 offset:39424\nds_write_addtid_b32 v27 offset:39680\nv_mfma_f32_16x16x32_bf16 v[220:223],a[44:47],v[84:87],v[220:223]\nds_write_addtid_b32 v28 offset:39936\n" \
"ds_write_addtid_b32 v29 offset:40192\nv_mfma_f32_16x16x32_bf16 v[224:227],a[16:19],v[80:83],v[224:227]\nds_write_addtid_b32 v30 offset:40448\nds_write_addtid_b32 v31 offset:40704\n" \
"v_mfma_f32_16x16x32_bf16 v[224:227],a[48:51],v[84:87],v[224:227]\nds_read_b128 v[32:35],v58\nds_read_b128 v[36:39],v58 offset:8192\nv_mfma_f32_16x16x32_bf16 v[228:231],a[20:23],v[80:83],v[228:231]\n" \
"ds_read_b128 v[40:43],v58 offset:16384\nds_read_b128 v[44:47],v58 offset:24576\nv_mfma_f32_16x16x32_bf16 v[228:231],a[52:55],v[84:87],v[228:231]\nds_read_b128 v[48:51],v58 offset:4096\n" \
"ds_read_b128 v[240:243],v58 offset:12288\nv_mfma_f32_16x16x32_bf16 v[232:235],a[24:27],v[80:83],v[232:235]\nds_read_b128 v[244:247],v58 offset:20480\nds_read_b128 v[248:251],v58 offset:28672\n" \
"v_mfma_f32_16x16x32_bf16 v[232:235],a[56:59],v[84:87],v[232:235]\nv_mfma_f32_16x16x32_bf16 v[236:239],a[28:31],v[80:83],v[236:239]\ns_waitcnt lgkmcnt(0)\ns_barrier\n" \
"v_mfma_f32_16x16x32_bf16 v[236:239],a[60:63],v[84:87],v[236:239]\ns_waitcnt vmcnt(13)\nv_mfma_f32_16x16x32_bf16 v[208:211],a[64:67],v[88:91],v[208:211]\nv_mfma_f32_16x16x32_bf16 v[208:211],a[96:99],v[92:95],v[208:211]\n" \
"v_mfma_f32_16x16x32_bf16 v[212:215],a[68:71],v[88:91],v[212:215]\nv_add_f32 v32,v32,v36\nv_mfma_f32_16x16x32_bf16 v[212:215],a[100:103],v[92:95],v[212:215]\nv_add_f32 v33,v33,v37\n" \
"v_add_f32 v34,v34,v38\nv_mfma_f32_16x16x32_bf16 v[216:219],a[72:75],v[88:91],v[216:219]\nv_add_f32 v35,v35,v39\nv_add_f32 v40,v40,v44\n" \
"v_mfma_f32_16x16x32_bf16 v[216:219],a[104:107],v[92:95],v[216:219]\nv_add_f32 v41,v41,v45\nv_add_f32 v42,v42,v46\nv_mfma_f32_16x16x32_bf16 v[220:223],a[76:79],v[88:91],v[220:223]\n" \
"v_add_f32 v43,v43,v47\nv_add_f32 v32,v32,v40\nv_mfma_f32_16x16x32_bf16 v[220:223],a[108:111],v[92:95],v[220:223]\nv_add_f32 v33,v33,v41\n" \
"v_add_f32 v34,v34,v42\nv_mfma_f32_16x16x32_bf16 v[224:227],a[80:83],v[88:91],v[224:227]\nv_add_f32 v35,v35,v43\nv_cvt_pk_bf16_f32 v36,v32,v33\n" \
"v_mfma_f32_16x16x32_bf16 v[224:227],a[112:115],v[92:95],v[224:227]\nv_cvt_pk_bf16_f32 v37,v34,v35\nv_mfma_f32_16x16x32_bf16 v[228:231],a[84:87],v[88:91],v[228:231]\nv_add_f32 v48,v48,v240\n" \
"v_add_f32 v49,v49,v241\nv_mfma_f32_16x16x32_bf16 v[228:231],a[116:119],v[92:95],v[228:231]\nv_add_f32 v50,v50,v242\nv_add_f32 v51,v51,v243\n" \
"v_mfma_f32_16x16x32_bf16 v[232:235],a[88:91],v[88:91],v[232:235]\nv_add_f32 v244,v244,v248\nv_add_f32 v245,v245,v249\nv_mfma_f32_16x16x32_bf16 v[232:235],a[120:123],v[92:95],v[232:235]\n" \
"v_add_f32 v246,v246,v250\nv_add_f32 v247,v247,v251\nv_mfma_f32_16x16x32_bf16 v[236:239],a[92:95],v[88:91],v[236:239]\nv_add_f32 v48,v48,v244\n" \
"v_add_f32 v49,v49,v245\nv_mfma_f32_16x16x32_bf16 v[236:239],a[124:127],v[92:95],v[236:239]\nv_add_f32 v50,v50,v246\ns_waitcnt vmcnt(11)\n" \
"v_mfma_f32_16x16x32_bf16 v[208:211],a[128:131],v[96:99],v[208:211]\nv_add_f32 v51,v51,v247\nv_cvt_pk_bf16_f32 v38,v48,v49\nv_mfma_f32_16x16x32_bf16 v[208:211],a[160:163],v[100:103],v[208:211]\n" \
"v_cvt_pk_bf16_f32 v39,v50,v51\nbuffer_store_dwordx4 v[36:39],v59,s[36:39],s74 offen sc1\nds_read_b128 v[32:35],v58 offset:32768\nv_mfma_f32_16x16x32_bf16 v[212:215],a[132:135],v[96:99],v[212:215]\n" \
"ds_read_b128 v[36:39],v58 offset:40960\nds_read_b128 v[40:43],v58 offset:49152\nv_mfma_f32_16x16x32_bf16 v[212:215],a[164:167],v[100:103],v[212:215]\nds_read_b128 v[44:47],v58 offset:57344\n" \
"ds_read_b128 v[48:51],v58 offset:36864\nv_mfma_f32_16x16x32_bf16 v[216:219],a[136:139],v[96:99],v[216:219]\nds_read_b128 v[240:243],v58 offset:45056\nds_read_b128 v[244:247],v58 offset:53248\n" \
"v_mfma_f32_16x16x32_bf16 v[216:219],a[168:171],v[100:103],v[216:219]\nds_read_b128 v[248:251],v58 offset:61440\nv_mfma_f32_16x16x32_bf16 v[220:223],a[140:143],v[96:99],v[220:223]\nv_mfma_f32_16x16x32_bf16 v[220:223],a[172:175],v[100:103],v[220:223]\n" \
"v_mfma_f32_16x16x32_bf16 v[224:227],a[144:147],v[96:99],v[224:227]\nv_mfma_f32_16x16x32_bf16 v[224:227],a[176:179],v[100:103],v[224:227]\nv_mfma_f32_16x16x32_bf16 v[228:231],a[148:151],v[96:99],v[228:231]\nv_mfma_f32_16x16x32_bf16 v[228:231],a[180:183],v[100:103],v[228:231]\n" \
"s_waitcnt lgkmcnt(4)\nv_add_f32 v32,v32,v36\nv_add_f32 v33,v33,v37\nv_mfma_f32_16x16x32_bf16 v[232:235],a[152:155],v[96:99],v[232:235]\n" \
"v_add_f32 v34,v34,v38\nv_add_f32 v35,v35,v39\nv_add_f32 v40,v40,v44\nv_mfma_f32_16x16x32_bf16 v[232:235],a[184:187],v[100:103],v[232:235]\n" \
"v_add_f32 v41,v41,v45\nv_add_f32 v42,v42,v46\nv_add_f32 v43,v43,v47\nv_mfma_f32_16x16x32_bf16 v[236:239],a[156:159],v[96:99],v[236:239]\n" \
"v_add_f32 v32,v32,v40\nv_add_f32 v33,v33,v41\nv_add_f32 v34,v34,v42\nv_mfma_f32_16x16x32_bf16 v[236:239],a[188:191],v[100:103],v[236:239]\n" \
"v_add_f32 v35,v35,v43\nv_cvt_pk_bf16_f32 v36,v32,v33\ns_waitcnt vmcnt(11)\nv_mfma_f32_16x16x32_bf16 v[208:211],a[192:195],v[104:107],v[208:211]\n" \
"v_cvt_pk_bf16_f32 v37,v34,v35\ns_waitcnt lgkmcnt(0)\nv_add_f32 v48,v48,v240\nv_mfma_f32_16x16x32_bf16 v[212:215],a[196:199],v[104:107],v[212:215]\n" \
"v_add_f32 v49,v49,v241\nv_add_f32 v50,v50,v242\nv_add_f32 v51,v51,v243\nv_mfma_f32_16x16x32_bf16 v[216:219],a[200:203],v[104:107],v[216:219]\n" \
"v_add_f32 v244,v244,v248\nv_add_f32 v245,v245,v249\nv_mfma_f32_16x16x32_bf16 v[220:223],a[204:207],v[104:107],v[220:223]\nv_add_f32 v246,v246,v250\n" \
"v_add_f32 v247,v247,v251\nv_mfma_f32_16x16x32_bf16 v[224:227],a[208:211],v[104:107],v[224:227]\nv_add_f32 v48,v48,v244\nv_add_f32 v49,v49,v245\n" \
"v_mfma_f32_16x16x32_bf16 v[228:231],a[212:215],v[104:107],v[228:231]\nv_add_f32 v50,v50,v246\nv_add_f32 v51,v51,v247\nv_mfma_f32_16x16x32_bf16 v[232:235],a[216:219],v[104:107],v[232:235]\n" \
"v_cvt_pk_bf16_f32 v38,v48,v49\nv_cvt_pk_bf16_f32 v39,v50,v51\nv_mfma_f32_16x16x32_bf16 v[236:239],a[220:223],v[104:107],v[236:239]\nv_add_u32 v59,409600,v59\n" \
"buffer_store_dwordx4 v[36:39],v59,s[36:39],s74 offen sc1\n.Le_%=:\ns_waitcnt vmcnt(9)\nv_mfma_f32_16x16x32_bf16 v[0:3],v[128:131],v[108:111],0\n" \
"v_add_u32 v59,409600,v59\nv_mfma_f32_16x16x32_bf16 v[0:3],v[160:163],v[112:115],v[0:3]\nds_write_addtid_b32 v208\nv_mfma_f32_16x16x32_bf16 v[4:7],v[132:135],v[108:111],0\n" \
"global_load_dwordx4 v[72:75],v59,s[12:13] sc1\nds_write_addtid_b32 v209 offset:256\nv_mfma_f32_16x16x32_bf16 v[4:7],v[164:167],v[112:115],v[4:7]\nds_write_addtid_b32 v210 offset:512\n" \
"v_mfma_f32_16x16x32_bf16 v[8:11],v[136:139],v[108:111],0\nds_write_addtid_b32 v211 offset:768\nv_mfma_f32_16x16x32_bf16 v[8:11],v[168:171],v[112:115],v[8:11]\nds_write_addtid_b32 v212 offset:1024\n" \
"v_mfma_f32_16x16x32_bf16 v[12:15],v[140:143],v[108:111],0\nds_write_addtid_b32 v213 offset:1280\nv_mfma_f32_16x16x32_bf16 v[12:15],v[172:175],v[112:115],v[12:15]\nds_write_addtid_b32 v214 offset:1536\n" \
"v_mfma_f32_16x16x32_bf16 v[16:19],v[144:147],v[108:111],0\nglobal_load_dwordx4 v[76:79],v59,s[12:13] offset:1024 sc1\nds_write_addtid_b32 v215 offset:1792\nv_mfma_f32_16x16x32_bf16 v[16:19],v[176:179],v[112:115],v[16:19]\n" \
"ds_write_addtid_b32 v216 offset:2048\nv_mfma_f32_16x16x32_bf16 v[20:23],v[148:151],v[108:111],0\nds_write_addtid_b32 v217 offset:2304\nv_mfma_f32_16x16x32_bf16 v[20:23],v[180:183],v[112:115],v[20:23]\n" \
"ds_write_addtid_b32 v218 offset:2560\nv_mfma_f32_16x16x32_bf16 v[24:27],v[152:155],v[108:111],0\nds_write_addtid_b32 v219 offset:2816\nv_mfma_f32_16x16x32_bf16 v[24:27],v[184:187],v[112:115],v[24:27]\n" \
"ds_write_addtid_b32 v220 offset:3072\nv_mfma_f32_16x16x32_bf16 v[28:31],v[156:159],v[108:111],0\nglobal_load_dwordx4 v[80:83],v59,s[12:13] offset:2048 sc1\nds_write_addtid_b32 v221 offset:3328\n" \
"v_mfma_f32_16x16x32_bf16 v[28:31],v[188:191],v[112:115],v[28:31]\nds_write_addtid_b32 v222 offset:3584\ns_waitcnt vmcnt(10)\nv_mfma_f32_16x16x32_bf16 v[0:3],a[0:3],v[116:119],v[0:3]\n" \
"ds_write_addtid_b32 v223 offset:3840\nv_bfe_u32 v69,v59,2,8\nv_mfma_f32_16x16x32_bf16 v[0:3],a[32:35],v[120:123],v[0:3]\nds_write_addtid_b32 v224 offset:4096\n" \
"buffer_load_dword v64,v69,s[36:39],s79 offen sc1\nv_mfma_f32_16x16x32_bf16 v[4:7],a[4:7],v[116:119],v[4:7]\nds_write_addtid_b32 v225 offset:4352\nbuffer_load_dword v65,v69,s[36:39],s80 offen sc1\n" \
"v_mfma_f32_16x16x32_bf16 v[4:7],a[36:39],v[120:123],v[4:7]\nds_write_addtid_b32 v226 offset:4608\nbuffer_load_dword v66,v69,s[36:39],s81 offen sc1\nv_mfma_f32_16x16x32_bf16 v[8:11],a[8:11],v[116:119],v[8:11]\n" \
"global_load_dwordx4 v[84:87],v59,s[14:15] sc1\nds_write_addtid_b32 v227 offset:4864\nv_mfma_f32_16x16x32_bf16 v[8:11],a[40:43],v[120:123],v[8:11]\nds_write_addtid_b32 v228 offset:5120\n" \
"buffer_load_dword v67,v69,s[36:39],s82 offen sc1\nv_mfma_f32_16x16x32_bf16 v[12:15],a[12:15],v[116:119],v[12:15]\nds_write_addtid_b32 v229 offset:5376\nbuffer_load_dword v68,v57,s[36:39],s90 offen sc1\n" \
"v_mfma_f32_16x16x32_bf16 v[12:15],a[44:47],v[120:123],v[12:15]\nds_write_addtid_b32 v230 offset:5632\nv_add_u32 v70,s98,v58\nv_mfma_f32_16x16x32_bf16 v[16:19],a[16:19],v[116:119],v[16:19]\n" \
"s_add_u32 s59,s59,0x10000\nbuffer_load_dword v63,v61,s[36:39],s59 offen sc1\nv_mfma_f32_16x16x32_bf16 v[16:19],a[48:51],v[120:123],v[16:19]\nds_write_addtid_b32 v231 offset:5888\n" \
"v_mfma_f32_16x16x32_bf16 v[20:23],a[20:23],v[116:119],v[20:23]\nglobal_load_dwordx4 v[88:91],v59,s[14:15] offset:1024 sc1\nds_write_addtid_b32 v232 offset:6144\nv_mfma_f32_16x16x32_bf16 v[20:23],a[52:55],v[120:123],v[20:23]\n" \
"ds_write_addtid_b32 v233 offset:6400\nv_mfma_f32_16x16x32_bf16 v[24:27],a[24:27],v[116:119],v[24:27]\nds_write_addtid_b32 v234 offset:6656\nv_mfma_f32_16x16x32_bf16 v[24:27],a[56:59],v[120:123],v[24:27]\n" \
"ds_write_addtid_b32 v235 offset:6912\nv_mfma_f32_16x16x32_bf16 v[28:31],a[28:31],v[116:119],v[28:31]\nds_write_addtid_b32 v236 offset:7168\nv_mfma_f32_16x16x32_bf16 v[28:31],a[60:63],v[120:123],v[28:31]\n" \
"ds_write_addtid_b32 v237 offset:7424\ns_waitcnt vmcnt(16)\nv_mfma_f32_16x16x32_bf16 v[0:3],a[64:67],v[124:127],v[0:3]\nglobal_load_dwordx4 v[92:95],v59,s[14:15] offset:2048 sc1\n" \
"ds_write_addtid_b32 v238 offset:7680\nv_mfma_f32_16x16x32_bf16 v[0:3],a[96:99],v[192:195],v[0:3]\nds_write_addtid_b32 v239 offset:7936\nv_mfma_f32_16x16x32_bf16 v[4:7],a[68:71],v[124:127],v[4:7]\n" \
"ds_read_b64 v[70:71],v70 offset:29704\nv_mfma_f32_16x16x32_bf16 v[4:7],a[100:103],v[192:195],v[4:7]\nv_mfma_f32_16x16x32_bf16 v[8:11],a[72:75],v[124:127],v[8:11]\nv_mfma_f32_16x16x32_bf16 v[8:11],a[104:107],v[192:195],v[8:11]\n" \
"v_mfma_f32_16x16x32_bf16 v[12:15],a[76:79],v[124:127],v[12:15]\nglobal_load_dwordx4 v[96:99],v59,s[16:17] sc1\nv_mfma_f32_16x16x32_bf16 v[12:15],a[108:111],v[192:195],v[12:15]\ns_waitcnt lgkmcnt(0)\n" \
"s_barrier\nv_mfma_f32_16x16x32_bf16 v[16:19],a[80:83],v[124:127],v[16:19]\nds_read_b128 v[32:35],v58\nv_mfma_f32_16x16x32_bf16 v[16:19],a[112:115],v[192:195],v[16:19]\n" \
"ds_read_b128 v[36:39],v58 offset:8192\nv_mfma_f32_16x16x32_bf16 v[20:23],a[84:87],v[124:127],v[20:23]\nds_read_b128 v[40:43],v58 offset:16384\nv_mfma_f32_16x16x32_bf16 v[20:23],a[116:119],v[192:195],v[20:23]\n" \
"ds_read_b128 v[44:47],v58 offset:24576\nv_mfma_f32_16x16x32_bf16 v[24:27],a[88:91],v[124:127],v[24:27]\nglobal_load_dwordx4 v[100:103],v59,s[16:17] offset:1024 sc1\nds_read_b128 v[48:51],v58 offset:4096\n" \
"v_mfma_f32_16x16x32_bf16 v[24:27],a[120:123],v[192:195],v[24:27]\nds_read_b128 v[240:243],v58 offset:12288\nv_mfma_f32_16x16x32_bf16 v[28:31],a[92:95],v[124:127],v[28:31]\nds_read_b128 v[244:247],v58 offset:20480\n" \
"v_mfma_f32_16x16x32_bf16 v[28:31],a[124:127],v[192:195],v[28:31]\nds_read_b128 v[248:251],v58 offset:28672\ns_waitcnt vmcnt(17)\nv_mfma_f32_16x16x32_bf16 v[0:3],a[128:131],v[196:199],v[0:3]\n" \
"v_mfma_f32_16x16x32_bf16 v[0:3],a[160:163],v[200:203],v[0:3]\nv_mfma_f32_16x16x32_bf16 v[4:7],a[132:135],v[196:199],v[4:7]\nglobal_load_dwordx4 v[104:107],v59,s[16:17] offset:2048 sc1\nv_mfma_f32_16x16x32_bf16 v[4:7],a[164:167],v[200:203],v[4:7]\n" \
"s_waitcnt lgkmcnt(4)\nv_add_f32 v32,v32,v36\nv_mfma_f32_16x16x32_bf16 v[8:11],a[136:139],v[196:199],v[8:11]\nv_add_f32 v33,v33,v37\n" \
"v_add_f32 v34,v34,v38\nv_mfma_f32_16x16x32_bf16 v[8:11],a[168:171],v[200:203],v[8:11]\nv_add_f32 v35,v35,v39\nv_add_f32 v40,v40,v44\n" \
"v_mfma_f32_16x16x32_bf16 v[12:15],a[140:143],v[196:199],v[12:15]\nv_add_f32 v41,v41,v45\nv_add_f32 v42,v42,v46\nv_mfma_f32_16x16x32_bf16 v[12:15],a[172:175],v[200:203],v[12:15]\n" \
"v_add_f32 v43,v43,v47\nv_add_f32 v32,v32,v40\nv_mfma_f32_16x16x32_bf16 v[16:19],a[144:147],v[196:199],v[16:19]\nv_add_f32 v33,v33,v41\n" \
"v_add_f32 v34,v34,v42\nv_mfma_f32_16x16x32_bf16 v[16:19],a[176:179],v[200:203],v[16:19]\nv_add_f32 v35,v35,v43\nv_cvt_pk_bf16_f32 v36,v32,v33\n" \
"v_mfma_f32_16x16x32_bf16 v[20:23],a[148:151],v[196:199],v[20:23]\nv_cvt_pk_bf16_f32 v37,v34,v35\ns_waitcnt lgkmcnt(0)\nv_mfma_f32_16x16x32_bf16 v[20:23],a[180:183],v[200:203],v[20:23]\n" \
"v_add_f32 v48,v48,v240\nv_add_f32 v49,v49,v241\nv_mfma_f32_16x16x32_bf16 v[24:27],a[152:155],v[196:199],v[24:27]\nv_add_f32 v50,v50,v242\n" \
"v_add_f32 v51,v51,v243\nv_mfma_f32_16x16x32_bf16 v[24:27],a[184:187],v[200:203],v[24:27]\nv_add_f32 v244,v244,v248\nv_add_f32 v245,v245,v249\n" \
"v_mfma_f32_16x16x32_bf16 v[28:31],a[156:159],v[196:199],v[28:31]\nv_add_f32 v246,v246,v250\nv_add_f32 v247,v247,v251\nv_mfma_f32_16x16x32_bf16 v[28:31],a[188:191],v[200:203],v[28:31]\n" \
"v_add_f32 v48,v48,v244\ns_waitcnt vmcnt(17)\nv_mfma_f32_16x16x32_bf16 v[0:3],a[192:195],v[204:207],v[0:3]\nv_add_f32 v49,v49,v245\n" \
"v_add_f32 v50,v50,v246\nv_mfma_f32_16x16x32_bf16 v[4:7],a[196:199],v[204:207],v[4:7]\nv_add_f32 v51,v51,v247\nv_cvt_pk_bf16_f32 v38,v48,v49\n" \
"v_mfma_f32_16x16x32_bf16 v[8:11],a[200:203],v[204:207],v[8:11]\nv_cvt_pk_bf16_f32 v39,v50,v51\nbuffer_store_dwordx4 v[36:39],v59,s[36:39],s74 offen sc1\nv_mfma_f32_16x16x32_bf16 v[12:15],a[204:207],v[204:207],v[12:15]\n" \
"v_mfma_f32_16x16x32_bf16 v[16:19],a[208:211],v[204:207],v[16:19]\nv_mfma_f32_16x16x32_bf16 v[20:23],a[212:215],v[204:207],v[20:23]\nv_mfma_f32_16x16x32_bf16 v[24:27],a[216:219],v[204:207],v[24:27]\nv_mfma_f32_16x16x32_bf16 v[28:31],a[220:223],v[204:207],v[28:31]\n" \
"s_waitcnt vmcnt(14)\nv_mfma_f32_16x16x32_bf16 v[208:211],v[128:131],v[72:75],0\nv_add_u32 v59,409600,v59\nv_mfma_f32_16x16x32_bf16 v[208:211],v[160:163],v[76:79],v[208:211]\n" \
"s_waitcnt vmcnt(6)\nv_cmp_eq_u32 vcc,0,v63\nv_mfma_f32_16x16x32_bf16 v[212:215],v[132:135],v[72:75],0\nds_write_addtid_b32 v0 offset:32768\n" \
"v_mfma_f32_16x16x32_bf16 v[212:215],v[164:167],v[76:79],v[212:215]\ns_and_b64 vcc,vcc,s[86:87]\ns_cbranch_scc1 .Lsg4_%=\n.Lbg4_%=:\n" \
"v_mfma_f32_16x16x32_bf16 v[216:219],v[136:139],v[72:75],0\nglobal_load_dwordx4 v[108:111],v59,s[12:13] sc1\nds_write_addtid_b32 v1 offset:33024\nv_mfma_f32_16x16x32_bf16 v[216:219],v[168:171],v[76:79],v[216:219]\n" \
"ds_write_addtid_b32 v2 offset:33280\nv_mfma_f32_16x16x32_bf16 v[220:223],v[140:143],v[72:75],0\nds_write_addtid_b32 v3 offset:33536\nv_max3_u32 v52,v64,v65,v66\n" \
"v_mfma_f32_16x16x32_bf16 v[220:223],v[172:175],v[76:79],v[220:223]\nds_write_addtid_b32 v4 offset:33792\nv_max_u32 v52,v52,v67\nv_mfma_f32_16x16x32_bf16 v[224:227],v[144:147],v[72:75],0\n" \
"global_load_dwordx4 v[112:115],v59,s[12:13] offset:1024 sc1\nds_write_addtid_b32 v5 offset:34048\nv_mfma_f32_16x16x32_bf16 v[224:227],v[176:179],v[76:79],v[224:227]\nds_write_addtid_b32 v6 offset:34304\n" \
"v_cmp_eq_u32 vcc,-1,v52\nv_mfma_f32_16x16x32_bf16 v[228:231],v[148:151],v[72:75],0\nds_write_addtid_b32 v7 offset:34560\nv_mfma_f32_16x16x32_bf16 v[228:231],v[180:183],v[76:79],v[228:231]\n" \
"ds_write_addtid_b32 v8 offset:34816\ns_cbranch_vccnz .Lse_%=\n.Lbe_%=:\nv_mfma_f32_16x16x32_bf16 v[232:235],v[152:155],v[72:75],0\n" \
"ds_write_addtid_b32 v9 offset:35072\nv_lshlrev_b32 v52,16,v64\nv_mfma_f32_16x16x32_bf16 v[232:235],v[184:187],v[76:79],v[232:235]\nds_write_addtid_b32 v10 offset:35328\n" \
"v_and_b32 v53,0xffff0000,v64\nv_mfma_f32_16x16x32_bf16 v[236:239],v[156:159],v[72:75],0\nglobal_load_dwordx4 v[116:119],v59,s[12:13] offset:2048 sc1\nds_write_addtid_b32 v11 offset:35584\n" \
"v_mfma_f32_16x16x32_bf16 v[236:239],v[188:191],v[76:79],v[236:239]\nds_write_addtid_b32 v12 offset:35840\nv_mfma_f32_16x16x32_bf16 v[208:211],a[0:3],v[80:83],v[208:211]\nds_write_addtid_b32 v13 offset:36096\n" \
"v_lshlrev_b32 v54,16,v65\nv_mfma_f32_16x16x32_bf16 v[208:211],a[32:35],v[84:87],v[208:211]\nds_write_addtid_b32 v14 offset:36352\nv_and_b32 v55,0xffff0000,v65\n" \
"v_mfma_f32_16x16x32_bf16 v[212:215],a[4:7],v[80:83],v[212:215]\nds_write_addtid_b32 v15 offset:36608\nv_lshlrev_b32 v252,16,v66\nv_mfma_f32_16x16x32_bf16 v[212:215],a[36:39],v[84:87],v[212:215]\n" \
"ds_write_addtid_b32 v16 offset:36864\nv_and_b32 v253,0xffff0000,v66\nv_mfma_f32_16x16x32_bf16 v[216:219],a[8:11],v[80:83],v[216:219]\nglobal_load_dwordx4 v[120:123],v59,s[14:15] sc1\n" \
"ds_write_addtid_b32 v17 offset:37120\nv_mfma_f32_16x16x32_bf16 v[216:219],a[40:43],v[84:87],v[216:219]\nds_write_addtid_b32 v18 offset:37376\nv_lshlrev_b32 v254,16,v67\n" \
"v_mfma_f32_16x16x32_bf16 v[220:223],a[12:15],v[80:83],v[220:223]\nds_write_addtid_b32 v19 offset:37632\nv_and_b32 v255,0xffff0000,v67\nv_mfma_f32_16x16x32_bf16 v[220:223],a[44:47],v[84:87],v[220:223]\n" \
"ds_write_addtid_b32 v20 offset:37888\nv_add_f32 v52,v52,v54\nv_mfma_f32_16x16x32_bf16 v[224:227],a[16:19],v[80:83],v[224:227]\nds_write_addtid_b32 v21 offset:38144\n" \
"v_add_f32 v53,v53,v55\nv_mfma_f32_16x16x32_bf16 v[224:227],a[48:51],v[84:87],v[224:227]\nds_write_addtid_b32 v22 offset:38400\nv_add_f32 v252,v252,v254\n" \
"v_mfma_f32_16x16x32_bf16 v[228:231],a[20:23],v[80:83],v[228:231]\nglobal_load_dwordx4 v[124:127],v59,s[14:15] offset:1024 sc1\nds_write_addtid_b32 v23 offset:38656\nv_mfma_f32_16x16x32_bf16 v[228:231],a[52:55],v[84:87],v[228:231]\n" \
"ds_write_addtid_b32 v24 offset:38912\nv_add_f32 v253,v253,v255\nv_mfma_f32_16x16x32_bf16 v[232:235],a[24:27],v[80:83],v[232:235]\nds_write_addtid_b32 v25 offset:39168\n" \
"s_waitcnt lgkmcnt(15)\nv_mfma_f32_16x16x32_bf16 v[232:235],a[56:59],v[84:87],v[232:235]\nds_write_addtid_b32 v26 offset:39424\nv_add_f32 v52,v52,v70\n" \
"v_mfma_f32_16x16x32_bf16 v[236:239],a[28:31],v[80:83],v[236:239]\nds_write_addtid_b32 v27 offset:39680\nv_add_f32 v53,v53,v70\nv_mfma_f32_16x16x32_bf16 v[236:239],a[60:63],v[84:87],v[236:239]\n" \
"ds_write_addtid_b32 v28 offset:39936\ns_waitcnt vmcnt(9)\nv_mfma_f32_16x16x32_bf16 v[208:211],a[64:67],v[88:91],v[208:211]\nglobal_load_dwordx4 v[192:195],v59,s[14:15] offset:2048 sc1\n" \
"ds_write_addtid_b32 v29 offset:40192\nv_mfma_f32_16x16x32_bf16 v[208:211],a[96:99],v[92:95],v[208:211]\nds_write_addtid_b32 v30 offset:40448\nv_lshlrev_b32 v54,16,v71\n" \
"v_mfma_f32_16x16x32_bf16 v[212:215],a[68:71],v[88:91],v[212:215]\nds_write_addtid_b32 v31 offset:40704\nv_and_b32 v55,0xffff0000,v71\nv_mfma_f32_16x16x32_bf16 v[212:215],a[100:103],v[92:95],v[212:215]\n" \
"v_lshlrev_b32 v254,16,v68\nv_and_b32 v255,0xffff0000,v68\nv_mfma_f32_16x16x32_bf16 v[216:219],a[72:75],v[88:91],v[216:219]\nv_add_f32 v52,v52,v252\n" \
"v_add_f32 v53,v53,v253\nv_mfma_f32_16x16x32_bf16 v[216:219],a[104:107],v[92:95],v[216:219]\nv_fma_f32 v52,v54,v52,v254\nv_fma_f32 v53,v55,v53,v255\n" \
"v_mfma_f32_16x16x32_bf16 v[220:223],a[76:79],v[88:91],v[220:223]\nglobal_load_dwordx4 v[196:199],v59,s[16:17] sc1\nv_cvt_pk_bf16_f32 v52,v52,v53\nv_mfma_f32_16x16x32_bf16 v[220:223],a[108:111],v[92:95],v[220:223]\n" \
"buffer_store_dword v52,v57,s[44:47],0 offen sc1\nv_mfma_f32_16x16x32_bf16 v[224:227],a[80:83],v[88:91],v[224:227]\ns_waitcnt lgkmcnt(0)\ns_barrier\n" \
"v_mfma_f32_16x16x32_bf16 v[224:227],a[112:115],v[92:95],v[224:227]\nds_read_b128 v[32:35],v58 offset:32768\nv_mfma_f32_16x16x32_bf16 v[228:231],a[84:87],v[88:91],v[228:231]\nds_read_b128 v[36:39],v58 offset:40960\n" \
"v_mfma_f32_16x16x32_bf16 v[228:231],a[116:119],v[92:95],v[228:231]\nds_read_b128 v[40:43],v58 offset:49152\nv_mfma_f32_16x16x32_bf16 v[232:235],a[88:91],v[88:91],v[232:235]\nglobal_load_dwordx4 v[200:203],v59,s[16:17] offset:1024 sc1\n" \
"ds_read_b128 v[44:47],v58 offset:57344\nv_mfma_f32_16x16x32_bf16 v[232:235],a[120:123],v[92:95],v[232:235]\nds_read_b128 v[48:51],v58 offset:36864\nv_mfma_f32_16x16x32_bf16 v[236:239],a[92:95],v[88:91],v[236:239]\n" \
"ds_read_b128 v[240:243],v58 offset:45056\nv_mfma_f32_16x16x32_bf16 v[236:239],a[124:127],v[92:95],v[236:239]\nds_read_b128 v[244:247],v58 offset:53248\ns_waitcnt vmcnt(11)\n" \
"v_mfma_f32_16x16x32_bf16 v[208:211],a[128:131],v[96:99],v[208:211]\nds_read_b128 v[248:251],v58 offset:61440\nv_mfma_f32_16x16x32_bf16 v[208:211],a[160:163],v[100:103],v[208:211]\nv_mfma_f32_16x16x32_bf16 v[212:215],a[132:135],v[96:99],v[212:215]\n" \
"global_load_dwordx4 v[204:207],v59,s[16:17] offset:2048 sc1\nv_mfma_f32_16x16x32_bf16 v[212:215],a[164:167],v[100:103],v[212:215]\nv_mfma_f32_16x16x32_bf16 v[216:219],a[136:139],v[96:99],v[216:219]\ns_waitcnt lgkmcnt(4)\n" \
"v_add_f32 v32,v32,v36\nv_mfma_f32_16x16x32_bf16 v[216:219],a[168:171],v[100:103],v[216:219]\nv_add_f32 v33,v33,v37\nv_add_f32 v34,v34,v38\n" \
"v_mfma_f32_16x16x32_bf16 v[220:223],a[140:143],v[96:99],v[220:223]\nv_add_f32 v35,v35,v39\nv_add_f32 v40,v40,v44\nv_mfma_f32_16x16x32_bf16 v[220:223],a[172:175],v[100:103],v[220:223]\n" \
"v_add_f32 v41,v41,v45\nv_add_f32 v42,v42,v46\nv_mfma_f32_16x16x32_bf16 v[224:227],a[144:147],v[96:99],v[224:227]\nv_add_f32 v43,v43,v47\n" \
"v_add_f32 v32,v32,v40\nv_mfma_f32_16x16x32_bf16 v[224:227],a[176:179],v[100:103],v[224:227]\nv_add_f32 v33,v33,v41\nv_add_f32 v34,v34,v42\n" \
"v_mfma_f32_16x16x32_bf16 v[228:231],a[148:151],v[96:99],v[228:231]\nv_add_f32 v35,v35,v43\nv_cvt_pk_bf16_f32 v36,v32,v33\nv_mfma_f32_16x16x32_bf16 v[228:231],a[180:183],v[100:103],v[228:231]\n" \
"v_cvt_pk_bf16_f32 v37,v34,v35\ns_waitcnt lgkmcnt(0)\nv_mfma_f32_16x16x32_bf16 v[232:235],a[152:155],v[96:99],v[232:235]\nv_add_f32 v48,v48,v240\n" \
"v_add_f32 v49,v49,v241\nv_mfma_f32_16x16x32_bf16 v[232:235],a[184:187],v[100:103],v[232:235]\nv_add_f32 v50,v50,v242\nv_add_f32 v51,v51,v243\n" \
"v_mfma_f32_16x16x32_bf16 v[236:239],a[156:159],v[96:99],v[236:239]\nv_add_f32 v244,v244,v248\nv_add_f32 v245,v245,v249\nv_mfma_f32_16x16x32_bf16 v[236:239],a[188:191],v[100:103],v[236:239]\n" \
"v_add_f32 v246,v246,v250\ns_waitcnt vmcnt(11)\nv_mfma_f32_16x16x32_bf16 v[208:211],a[192:195],v[104:107],v[208:211]\nv_add_f32 v247,v247,v251\n" \
"v_add_f32 v48,v48,v244\nv_mfma_f32_16x16x32_bf16 v[212:215],a[196:199],v[104:107],v[212:215]\nv_add_f32 v49,v49,v245\nv_add_f32 v50,v50,v246\n" \
"v_mfma_f32_16x16x32_bf16 v[216:219],a[200:203],v[104:107],v[216:219]\nv_add_f32 v51,v51,v247\nv_cvt_pk_bf16_f32 v38,v48,v49\nv_mfma_f32_16x16x32_bf16 v[220:223],a[204:207],v[104:107],v[220:223]\n" \
"v_cvt_pk_bf16_f32 v39,v50,v51\nbuffer_store_dwordx4 v[36:39],v59,s[36:39],s74 offen sc1\nv_mfma_f32_16x16x32_bf16 v[224:227],a[208:211],v[104:107],v[224:227]\nv_mfma_f32_16x16x32_bf16 v[228:231],a[212:215],v[104:107],v[228:231]\n" \
"v_mfma_f32_16x16x32_bf16 v[232:235],a[216:219],v[104:107],v[232:235]\nv_mfma_f32_16x16x32_bf16 v[236:239],a[220:223],v[104:107],v[236:239]\ns_waitcnt vmcnt(9)\nv_mfma_f32_16x16x32_bf16 v[0:3],v[128:131],v[108:111],0\n" \
"v_add_u32 v59,409600,v59\nv_add_u32 v60,s98,v58\nv_mfma_f32_16x16x32_bf16 v[0:3],v[160:163],v[112:115],v[0:3]\nds_write_addtid_b32 v208\n" \
"v_mfma_f32_16x16x32_bf16 v[4:7],v[132:135],v[108:111],0\nglobal_load_dwordx4 v[72:75],v59,s[12:13] sc1\nds_write_addtid_b32 v209 offset:256\nv_mfma_f32_16x16x32_bf16 v[4:7],v[164:167],v[112:115],v[4:7]\n" \
"ds_write_addtid_b32 v210 offset:512\nv_mfma_f32_16x16x32_bf16 v[8:11],v[136:139],v[108:111],0\nds_write_addtid_b32 v211 offset:768\nv_mfma_f32_16x16x32_bf16 v[8:11],v[168:171],v[112:115],v[8:11]\n" \
"buffer_load_dwordx4 v[64:67],v59,s[36:39],s71 offen sc1\nds_write_addtid_b32 v212 offset:1024\nv_mfma_f32_16x16x32_bf16 v[12:15],v[140:143],v[108:111],0\nds_write_addtid_b32 v213 offset:1280\n" \
"v_mfma_f32_16x16x32_bf16 v[12:15],v[172:175],v[112:115],v[12:15]\nds_write_addtid_b32 v214 offset:1536\nv_mfma_f32_16x16x32_bf16 v[16:19],v[144:147],v[108:111],0\nglobal_load_dwordx4 v[76:79],v59,s[12:13] offset:1024 sc1\n" \
"ds_write_addtid_b32 v215 offset:1792\nv_mfma_f32_16x16x32_bf16 v[16:19],v[176:179],v[112:115],v[16:19]\nds_write_addtid_b32 v216 offset:2048\nv_mfma_f32_16x16x32_bf16 v[20:23],v[148:151],v[108:111],0\n" \
"ds_write_addtid_b32 v217 offset:2304\nv_mfma_f32_16x16x32_bf16 v[20:23],v[180:183],v[112:115],v[20:23]\nds_write_addtid_b32 v218 offset:2560\nv_mfma_f32_16x16x32_bf16 v[24:27],v[152:155],v[108:111],0\n" \
"ds_write_addtid_b32 v219 offset:2816\nv_mfma_f32_16x16x32_bf16 v[24:27],v[184:187],v[112:115],v[24:27]\nds_write_addtid_b32 v220 offset:3072\nv_mfma_f32_16x16x32_bf16 v[28:31],v[156:159],v[108:111],0\n" \
"global_load_dwordx4 v[80:83],v59,s[12:13] offset:2048 sc1\nds_write_addtid_b32 v221 offset:3328\nv_mfma_f32_16x16x32_bf16 v[28:31],v[188:191],v[112:115],v[28:31]\nds_write_addtid_b32 v222 offset:3584\n" \
"s_waitcnt vmcnt(11)\nv_mfma_f32_16x16x32_bf16 v[0:3],a[0:3],v[116:119],v[0:3]\nds_write_addtid_b32 v223 offset:3840\nv_mfma_f32_16x16x32_bf16 v[0:3],a[32:35],v[120:123],v[0:3]\n" \
"buffer_load_dwordx4 v[52:55],v59,s[36:39],s76 offen sc1\nds_write_addtid_b32 v224 offset:4096\nv_mfma_f32_16x16x32_bf16 v[4:7],a[4:7],v[116:119],v[4:7]\nds_write_addtid_b32 v225 offset:4352\n" \
"v_mfma_f32_16x16x32_bf16 v[4:7],a[36:39],v[120:123],v[4:7]\nds_write_addtid_b32 v226 offset:4608\nv_mfma_f32_16x16x32_bf16 v[8:11],a[8:11],v[116:119],v[8:11]\nglobal_load_dwordx4 v[84:87],v59,s[14:15] sc1\n" \
"ds_write_addtid_b32 v227 offset:4864\nv_mfma_f32_16x16x32_bf16 v[8:11],a[40:43],v[120:123],v[8:11]\nds_write_addtid_b32 v228 offset:5120\nv_mfma_f32_16x16x32_bf16 v[12:15],a[12:15],v[116:119],v[12:15]\n" \
"ds_write_addtid_b32 v229 offset:5376\nv_mfma_f32_16x16x32_bf16 v[12:15],a[44:47],v[120:123],v[12:15]\nds_write_addtid_b32 v230 offset:5632\nv_mfma_f32_16x16x32_bf16 v[16:19],a[16:19],v[116:119],v[16:19]\n" \
"ds_write_addtid_b32 v231 offset:5888\nv_mfma_f32_16x16x32_bf16 v[16:19],a[48:51],v[120:123],v[16:19]\nds_write_addtid_b32 v232 offset:6144\nv_mfma_f32_16x16x32_bf16 v[20:23],a[20:23],v[116:119],v[20:23]\n" \
"global_load_dwordx4 v[88:91],v59,s[14:15] offset:1024 sc1\nds_write_addtid_b32 v233 offset:6400\nv_mfma_f32_16x16x32_bf16 v[20:23],a[52:55],v[120:123],v[20:23]\nds_write_addtid_b32 v234 offset:6656\n" \
"v_mfma_f32_16x16x32_bf16 v[24:27],a[24:27],v[116:119],v[24:27]\nds_write_addtid_b32 v235 offset:6912\nv_mfma_f32_16x16x32_bf16 v[24:27],a[56:59],v[120:123],v[24:27]\nds_write_addtid_b32 v236 offset:7168\n" \
"v_mfma_f32_16x16x32_bf16 v[28:31],a[28:31],v[116:119],v[28:31]\nds_write_addtid_b32 v237 offset:7424\nv_mfma_f32_16x16x32_bf16 v[28:31],a[60:63],v[120:123],v[28:31]\nds_write_addtid_b32 v238 offset:7680\n" \
"s_waitcnt vmcnt(12)\nv_mfma_f32_16x16x32_bf16 v[0:3],a[64:67],v[124:127],v[0:3]\nglobal_load_dwordx4 v[92:95],v59,s[14:15] offset:2048 sc1\nds_write_addtid_b32 v239 offset:7936\n" \
"v_mfma_f32_16x16x32_bf16 v[0:3],a[96:99],v[192:195],v[0:3]\nds_read_b32 v61,v60 offset:28676\nv_mfma_f32_16x16x32_bf16 v[4:7],a[68:71],v[124:127],v[4:7]\nv_mfma_f32_16x16x32_bf16 v[4:7],a[100:103],v[192:195],v[4:7]\n" \
"v_mfma_f32_16x16x32_bf16 v[8:11],a[72:75],v[124:127],v[8:11]\nv_mfma_f32_16x16x32_bf16 v[8:11],a[104:107],v[192:195],v[8:11]\nv_mfma_f32_16x16x32_bf16 v[12:15],a[76:79],v[124:127],v[12:15]\nglobal_load_dwordx4 v[96:99],v59,s[16:17] sc1\n" \
"v_mfma_f32_16x16x32_bf16 v[12:15],a[108:111],v[192:195],v[12:15]\ns_waitcnt lgkmcnt(0)\ns_barrier\nv_mfma_f32_16x16x32_bf16 v[16:19],a[80:83],v[124:127],v[16:19]\n" \
"ds_read_b128 v[32:35],v58\nv_mfma_f32_16x16x32_bf16 v[16:19],a[112:115],v[192:195],v[16:19]\nds_read_b128 v[36:39],v58 offset:8192\nv_mfma_f32_16x16x32_bf16 v[20:23],a[84:87],v[124:127],v[20:23]\n" \
"ds_read_b128 v[40:43],v58 offset:16384\nv_lshlrev_b32 v60,16,v61\nv_mfma_f32_16x16x32_bf16 v[20:23],a[116:119],v[192:195],v[20:23]\nds_read_b128 v[44:47],v58 offset:24576\n" \
"v_and_b32 v61,0xffff0000,v61\nv_mfma_f32_16x16x32_bf16 v[24:27],a[88:91],v[124:127],v[24:27]\nglobal_load_dwordx4 v[100:103],v59,s[16:17] offset:1024 sc1\nds_read_b128 v[48:51],v58 offset:4096\n" \
"v_mfma_f32_16x16x32_bf16 v[24:27],a[120:123],v[192:195],v[24:27]\nds_read_b128 v[240:243],v58 offset:12288\nv_mfma_f32_16x16x32_bf16 v[28:31],a[92:95],v[124:127],v[28:31]\nds_read_b128 v[244:247],v58 offset:20480\n" \
"v_mfma_f32_16x16x32_bf16 v[28:31],a[124:127],v[192:195],v[28:31]\nds_read_b128 v[248:251],v58 offset:28672\ns_waitcnt vmcnt(12)\nv_mfma_f32_16x16x32_bf16 v[0:3],a[128:131],v[196:199],v[0:3]\n" \
"v_mfma_f32_16x16x32_bf16 v[0:3],a[160:163],v[200:203],v[0:3]\nv_mfma_f32_16x16x32_bf16 v[4:7],a[132:135],v[196:199],v[4:7]\nglobal_load_dwordx4 v[104:107],v59,s[16:17] offset:2048 sc1\nv_mfma_f32_16x16x32_bf16 v[4:7],a[164:167],v[200:203],v[4:7]\n" \
"s_waitcnt lgkmcnt(4)\nv_add_f32 v32,v32,v36\nv_mfma_f32_16x16x32_bf16 v[8:11],a[136:139],v[196:199],v[8:11]\nv_add_f32 v33,v33,v37\n" \
"v_add_f32 v34,v34,v38\nv_mfma_f32_16x16x32_bf16 v[8:11],a[168:171],v[200:203],v[8:11]\nv_add_f32 v35,v35,v39\nv_add_f32 v40,v40,v44\n" \
"v_mfma_f32_16x16x32_bf16 v[12:15],a[140:143],v[196:199],v[12:15]\nv_add_f32 v41,v41,v45\nv_add_f32 v42,v42,v46\nv_mfma_f32_16x16x32_bf16 v[12:15],a[172:175],v[200:203],v[12:15]\n" \
"v_add_f32 v43,v43,v47\nv_add_f32 v32,v32,v40\nv_mfma_f32_16x16x32_bf16 v[16:19],a[144:147],v[196:199],v[16:19]\nv_add_f32 v33,v33,v41\n" \
"v_add_f32 v34,v34,v42\nv_mfma_f32_16x16x32_bf16 v[16:19],a[176:179],v[200:203],v[16:19]\nv_add_f32 v35,v35,v43\nv_cvt_pk_bf16_f32 v36,v32,v33\n" \
"v_mfma_f32_16x16x32_bf16 v[20:23],a[148:151],v[196:199],v[20:23]\nv_cvt_pk_bf16_f32 v37,v34,v35\ns_waitcnt lgkmcnt(0)\nv_mfma_f32_16x16x32_bf16 v[20:23],a[180:183],v[200:203],v[20:23]\n" \
"v_add_f32 v48,v48,v240\nv_add_f32 v49,v49,v241\nv_mfma_f32_16x16x32_bf16 v[24:27],a[152:155],v[196:199],v[24:27]\nv_add_f32 v50,v50,v242\n" \
"v_add_f32 v51,v51,v243\nv_mfma_f32_16x16x32_bf16 v[24:27],a[184:187],v[200:203],v[24:27]\nv_add_f32 v244,v244,v248\nv_add_f32 v245,v245,v249\n" \
"v_mfma_f32_16x16x32_bf16 v[28:31],a[156:159],v[196:199],v[28:31]\nv_add_f32 v246,v246,v250\nv_add_f32 v247,v247,v251\nv_mfma_f32_16x16x32_bf16 v[28:31],a[188:191],v[200:203],v[28:31]\n" \
"v_add_f32 v48,v48,v244\ns_waitcnt vmcnt(12)\nv_mfma_f32_16x16x32_bf16 v[0:3],a[192:195],v[204:207],v[0:3]\nv_add_f32 v49,v49,v245\n" \
"v_add_f32 v50,v50,v246\nv_mfma_f32_16x16x32_bf16 v[4:7],a[196:199],v[204:207],v[4:7]\nv_add_f32 v51,v51,v247\nv_cvt_pk_bf16_f32 v38,v48,v49\n" \
"v_mfma_f32_16x16x32_bf16 v[8:11],a[200:203],v[204:207],v[8:11]\nv_cvt_pk_bf16_f32 v39,v50,v51\nbuffer_store_dwordx4 v[36:39],v59,s[36:39],s74 offen sc1\nv_mfma_f32_16x16x32_bf16 v[12:15],a[204:207],v[204:207],v[12:15]\n" \
"v_mfma_f32_16x16x32_bf16 v[16:19],a[208:211],v[204:207],v[16:19]\nv_mfma_f32_16x16x32_bf16 v[20:23],a[212:215],v[204:207],v[20:23]\nv_mfma_f32_16x16x32_bf16 v[24:27],a[216:219],v[204:207],v[24:27]\nv_mfma_f32_16x16x32_bf16 v[28:31],a[220:223],v[204:207],v[28:31]\n" \
"s_waitcnt vmcnt(9)\nv_mfma_f32_16x16x32_bf16 v[208:211],v[128:131],v[72:75],0\nv_add_u32 v59,409600,v59\ns_waitcnt vmcnt(7)\n" \
"v_mfma_f32_16x16x32_bf16 v[208:211],v[160:163],v[76:79],v[208:211]\nds_write_addtid_b32 v0 offset:32768\nv_max_u32 v62,v64,v52\nv_mfma_f32_16x16x32_bf16 v[212:215],v[132:135],v[72:75],0\n" \
"global_load_dwordx4 v[108:111],v59,s[12:13] sc1\nds_write_addtid_b32 v1 offset:33024\nv_mfma_f32_16x16x32_bf16 v[212:215],v[164:167],v[76:79],v[212:215]\nds_write_addtid_b32 v2 offset:33280\n" \
"v_cmp_eq_u32 vcc,-1,v62\nv_mfma_f32_16x16x32_bf16 v[216:219],v[136:139],v[72:75],0\nds_write_addtid_b32 v3 offset:33536\nv_mfma_f32_16x16x32_bf16 v[216:219],v[168:171],v[76:79],v[216:219]\n" \
"ds_write_addtid_b32 v4 offset:33792\ns_cbranch_vccnz .Lsp1_%=\n.Lbp1_%=:\nv_mfma_f32_16x16x32_bf16 v[220:223],v[140:143],v[72:75],0\n" \
"ds_write_addtid_b32 v5 offset:34048\nv_and_b32 v71,0xffff0000,v67\nv_mfma_f32_16x16x32_bf16 v[220:223],v[172:175],v[76:79],v[220:223]\nds_write_addtid_b32 v6 offset:34304\n" \
"v_lshlrev_b32 v70,16,v67\nv_mfma_f32_16x16x32_bf16 v[224:227],v[144:147],v[72:75],0\nglobal_load_dwordx4 v[112:115],v59,s[12:13] offset:1024 sc1\nds_write_addtid_b32 v7 offset:34560\n" \
"v_mfma_f32_16x16x32_bf16 v[224:227],v[176:179],v[76:79],v[224:227]\nds_write_addtid_b32 v8 offset:34816\nv_and_b32 v69,0xffff0000,v66\nv_mfma_f32_16x16x32_bf16 v[228:231],v[148:151],v[72:75],0\n" \
"ds_write_addtid_b32 v9 offset:35072\nv_lshlrev_b32 v68,16,v66\nv_mfma_f32_16x16x32_bf16 v[228:231],v[180:183],v[76:79],v[228:231]\nds_write_addtid_b32 v10 offset:35328\n" \
"v_and_b32 v67,0xffff0000,v65\nv_mfma_f32_16x16x32_bf16 v[232:235],v[152:155],v[72:75],0\nds_write_addtid_b32 v11 offset:35584\nv_lshlrev_b32 v66,16,v65\n" \
"v_mfma_f32_16x16x32_bf16 v[232:235],v[184:187],v[76:79],v[232:235]\nds_write_addtid_b32 v12 offset:35840\nv_and_b32 v65,0xffff0000,v64\nv_mfma_f32_16x16x32_bf16 v[236:239],v[156:159],v[72:75],0\n" \
"global_load_dwordx4 v[116:119],v59,s[12:13] offset:2048 sc1\nds_write_addtid_b32 v13 offset:36096\nv_mfma_f32_16x16x32_bf16 v[236:239],v[188:191],v[76:79],v[236:239]\nds_write_addtid_b32 v14 offset:36352\n" \
"s_waitcnt vmcnt(9)\nv_mfma_f32_16x16x32_bf16 v[208:211],a[0:3],v[80:83],v[208:211]\nds_write_addtid_b32 v15 offset:36608\nv_lshlrev_b32 v64,16,v64\n" \
"v_mfma_f32_16x16x32_bf16 v[208:211],a[32:35],v[84:87],v[208:211]\nds_write_addtid_b32 v16 offset:36864\nv_add_f32 v64,v64,v60\nv_mfma_f32_16x16x32_bf16 v[212:215],a[4:7],v[80:83],v[212:215]\n" \
"ds_write_addtid_b32 v17 offset:37120\nv_add_f32 v65,v65,v60\nv_mfma_f32_16x16x32_bf16 v[212:215],a[36:39],v[84:87],v[212:215]\nds_write_addtid_b32 v18 offset:37376\n" \
"v_add_f32 v66,v66,v60\nv_mfma_f32_16x16x32_bf16 v[216:219],a[8:11],v[80:83],v[216:219]\nglobal_load_dwordx4 v[120:123],v59,s[14:15] sc1\nds_write_addtid_b32 v19 offset:37632\n" \
"v_mfma_f32_16x16x32_bf16 v[216:219],a[40:43],v[84:87],v[216:219]\nds_write_addtid_b32 v20 offset:37888\nv_add_f32 v67,v67,v60\nv_mfma_f32_16x16x32_bf16 v[220:223],a[12:15],v[80:83],v[220:223]\n" \
"ds_write_addtid_b32 v21 offset:38144\nv_add_f32 v68,v68,v61\nv_mfma_f32_16x16x32_bf16 v[220:223],a[44:47],v[84:87],v[220:223]\nds_write_addtid_b32 v22 offset:38400\n" \
"v_add_f32 v69,v69,v61\nv_mfma_f32_16x16x32_bf16 v[224:227],a[16:19],v[80:83],v[224:227]\nds_write_addtid_b32 v23 offset:38656\nv_add_f32 v70,v70,v61\n" \
"v_mfma_f32_16x16x32_bf16 v[224:227],a[48:51],v[84:87],v[224:227]\nds_write_addtid_b32 v24 offset:38912\nv_add_f32 v71,v71,v61\nv_mfma_f32_16x16x32_bf16 v[228:231],a[20:23],v[80:83],v[228:231]\n" \
"global_load_dwordx4 v[124:127],v59,s[14:15] offset:1024 sc1\nds_write_addtid_b32 v25 offset:39168\nv_mfma_f32_16x16x32_bf16 v[228:231],a[52:55],v[84:87],v[228:231]\nds_write_addtid_b32 v26 offset:39424\n" \
"v_and_b32 v255,0xffff0000,v55\nv_mfma_f32_16x16x32_bf16 v[232:235],a[24:27],v[80:83],v[232:235]\nds_write_addtid_b32 v27 offset:39680\nv_lshlrev_b32 v254,16,v55\n" \
"v_mfma_f32_16x16x32_bf16 v[232:235],a[56:59],v[84:87],v[232:235]\nds_write_addtid_b32 v28 offset:39936\nv_and_b32 v253,0xffff0000,v54\nv_mfma_f32_16x16x32_bf16 v[236:239],a[28:31],v[80:83],v[236:239]\n" \
"ds_write_addtid_b32 v29 offset:40192\nv_lshlrev_b32 v252,16,v54\nv_mfma_f32_16x16x32_bf16 v[236:239],a[60:63],v[84:87],v[236:239]\nds_write_addtid_b32 v30 offset:40448\n" \
"s_waitcnt vmcnt(9)\nv_mfma_f32_16x16x32_bf16 v[208:211],a[64:67],v[88:91],v[208:211]\nglobal_load_dwordx4 v[192:195],v59,s[14:15] offset:2048 sc1\nds_write_addtid_b32 v31 offset:40704\n" \
"v_mfma_f32_16x16x32_bf16 v[208:211],a[96:99],v[92:95],v[208:211]\nv_and_b32 v55,0xffff0000,v53\nv_lshlrev_b32 v54,16,v53\nv_mfma_f32_16x16x32_bf16 v[212:215],a[68:71],v[88:91],v[212:215]\n" \
"v_and_b32 v53,0xffff0000,v52\nv_lshlrev_b32 v52,16,v52\nv_mfma_f32_16x16x32_bf16 v[212:215],a[100:103],v[92:95],v[212:215]\nv_add_f32 v52,v52,v60\n" \
"v_add_f32 v53,v53,v60\nv_mfma_f32_16x16x32_bf16 v[216:219],a[72:75],v[88:91],v[216:219]\nv_add_f32 v54,v54,v60\nv_add_f32 v55,v55,v60\n" \
"v_mfma_f32_16x16x32_bf16 v[216:219],a[104:107],v[92:95],v[216:219]\nv_add_f32 v252,v252,v61\nv_add_f32 v253,v253,v61\nv_mfma_f32_16x16x32_bf16 v[220:223],a[76:79],v[88:91],v[220:223]\n" \
"global_load_dwordx4 v[196:199],v59,s[16:17] sc1\nv_add_f32 v254,v254,v61\nv_mfma_f32_16x16x32_bf16 v[220:223],a[108:111],v[92:95],v[220:223]\ns_waitcnt lgkmcnt(0)\n" \
"s_barrier\nv_mfma_f32_16x16x32_bf16 v[224:227],a[80:83],v[88:91],v[224:227]\nds_read_b128 v[32:35],v58 offset:32768\nv_add_f32 v255,v255,v61\n" \
"v_mfma_f32_16x16x32_bf16 v[224:227],a[112:115],v[92:95],v[224:227]\nds_read_b128 v[36:39],v58 offset:40960\nv_mfma_f32_16x16x32_bf16 v[228:231],a[84:87],v[88:91],v[228:231]\nds_read_b128 v[40:43],v58 offset:49152\n" \
"v_mfma_f32_16x16x32_bf16 v[228:231],a[116:119],v[92:95],v[228:231]\nds_read_b128 v[44:47],v58 offset:57344\nv_mfma_f32_16x16x32_bf16 v[232:235],a[88:91],v[88:91],v[232:235]\nglobal_load_dwordx4 v[200:203],v59,s[16:17] offset:1024 sc1\n" \
"ds_read_b128 v[48:51],v58 offset:36864\nv_mfma_f32_16x16x32_bf16 v[232:235],a[120:123],v[92:95],v[232:235]\nds_read_b128 v[240:243],v58 offset:45056\nv_mfma_f32_16x16x32_bf16 v[236:239],a[92:95],v[88:91],v[236:239]\n" \
"ds_read_b128 v[244:247],v58 offset:53248\nv_mfma_f32_16x16x32_bf16 v[236:239],a[124:127],v[92:95],v[236:239]\nbuffer_load_dwordx4 v[60:63],v59,s[36:39],s72 offen sc1\ns_waitcnt vmcnt(11)\n" \
"v_mfma_f32_16x16x32_bf16 v[208:211],a[128:131],v[96:99],v[208:211]\nds_read_b128 v[248:251],v58 offset:61440\nv_mfma_f32_16x16x32_bf16 v[208:211],a[160:163],v[100:103],v[208:211]\nv_mfma_f32_16x16x32_bf16 v[212:215],a[132:135],v[96:99],v[212:215]\n" \
"global_load_dwordx4 v[204:207],v59,s[16:17] offset:2048 sc1\nv_mfma_f32_16x16x32_bf16 v[212:215],a[164:167],v[100:103],v[212:215]\ns_waitcnt lgkmcnt(4)\nv_add_f32 v32,v32,v36\n" \
"v_mfma_f32_16x16x32_bf16 v[216:219],a[136:139],v[96:99],v[216:219]\nv_add_f32 v33,v33,v37\nv_add_f32 v34,v34,v38\nv_mfma_f32_16x16x32_bf16 v[216:219],a[168:171],v[100:103],v[216:219]\n" \
"v_add_f32 v35,v35,v39\nv_add_f32 v40,v40,v44\nv_mfma_f32_16x16x32_bf16 v[220:223],a[140:143],v[96:99],v[220:223]\nv_add_f32 v41,v41,v45\n" \
"v_add_f32 v42,v42,v46\nv_mfma_f32_16x16x32_bf16 v[220:223],a[172:175],v[100:103],v[220:223]\nv_add_f32 v43,v43,v47\nv_add_f32 v32,v32,v40\n" \
"v_mfma_f32_16x16x32_bf16 v[224:227],a[144:147],v[96:99],v[224:227]\nv_add_f32 v33,v33,v41\nv_add_f32 v34,v34,v42\nv_mfma_f32_16x16x32_bf16 v[224:227],a[176:179],v[100:103],v[224:227]\n" \
"v_add_f32 v35,v35,v43\nv_cvt_pk_bf16_f32 v36,v32,v33\nv_mfma_f32_16x16x32_bf16 v[228:231],a[148:151],v[96:99],v[228:231]\nv_cvt_pk_bf16_f32 v37,v34,v35\n" \
"s_waitcnt lgkmcnt(0)\nv_mfma_f32_16x16x32_bf16 v[228:231],a[180:183],v[100:103],v[228:231]\nv_add_f32 v48,v48,v240\nv_add_f32 v49,v49,v241\n" \
"v_mfma_f32_16x16x32_bf16 v[232:235],a[152:155],v[96:99],v[232:235]\nv_add_f32 v50,v50,v242\nv_add_f32 v51,v51,v243\nv_mfma_f32_16x16x32_bf16 v[232:235],a[184:187],v[100:103],v[232:235]\n" \
"v_add_f32 v244,v244,v248\nv_add_f32 v245,v245,v249\nv_mfma_f32_16x16x32_bf16 v[236:239],a[156:159],v[96:99],v[236:239]\nv_add_f32 v246,v246,v250\n" \
"v_add_f32 v247,v247,v251\nv_mfma_f32_16x16x32_bf16 v[236:239],a[188:191],v[100:103],v[236:239]\nv_add_f32 v48,v48,v244\ns_waitcnt vmcnt(11)\n" \
"v_mfma_f32_16x16x32_bf16 v[208:211],a[192:195],v[104:107],v[208:211]\nv_add_f32 v49,v49,v245\nv_add_f32 v50,v50,v246\nv_mfma_f32_16x16x32_bf16 v[212:215],a[196:199],v[104:107],v[212:215]\n" \
"v_add_f32 v51,v51,v247\nv_cvt_pk_bf16_f32 v38,v48,v49\nv_mfma_f32_16x16x32_bf16 v[216:219],a[200:203],v[104:107],v[216:219]\nv_cvt_pk_bf16_f32 v39,v50,v51\n" \
"buffer_store_dwordx4 v[36:39],v59,s[36:39],s74 offen sc1\nv_mfma_f32_16x16x32_bf16 v[220:223],a[204:207],v[104:107],v[220:223]\nv_mfma_f32_16x16x32_bf16 v[224:227],a[208:211],v[104:107],v[224:227]\nv_mfma_f32_16x16x32_bf16 v[228:231],a[212:215],v[104:107],v[228:231]\n" \
"v_mfma_f32_16x16x32_bf16 v[232:235],a[216:219],v[104:107],v[232:235]\nv_mfma_f32_16x16x32_bf16 v[236:239],a[220:223],v[104:107],v[236:239]\ns_waitcnt vmcnt(9)\nv_mfma_f32_16x16x32_bf16 v[0:3],v[128:131],v[108:111],0\n" \
"v_add_u32 v59,409600,v59\nv_mfma_f32_16x16x32_bf16 v[0:3],v[160:163],v[112:115],v[0:3]\nds_write_addtid_b32 v208\nds_write_addtid_b32 v209 offset:256\n" \
"v_mfma_f32_16x16x32_bf16 v[4:7],v[132:135],v[108:111],0\nglobal_load_dwordx4 v[72:75],v59,s[12:13] sc1\nds_write_addtid_b32 v210 offset:512\nv_mfma_f32_16x16x32_bf16 v[4:7],v[164:167],v[112:115],v[4:7]\n" \
"ds_write_addtid_b32 v211 offset:768\nds_write_addtid_b32 v212 offset:1024\nv_mfma_f32_16x16x32_bf16 v[8:11],v[136:139],v[108:111],0\nds_write_addtid_b32 v213 offset:1280\n" \
"ds_write_addtid_b32 v214 offset:1536\nv_mfma_f32_16x16x32_bf16 v[8:11],v[168:171],v[112:115],v[8:11]\nds_write_addtid_b32 v215 offset:1792\nds_write_addtid_b32 v216 offset:2048\n" \
"v_mfma_f32_16x16x32_bf16 v[12:15],v[140:143],v[108:111],0\nds_write_addtid_b32 v217 offset:2304\nds_write_addtid_b32 v218 offset:2560\nv_mfma_f32_16x16x32_bf16 v[12:15],v[172:175],v[112:115],v[12:15]\n" \
"ds_write_addtid_b32 v219 offset:2816\nds_write_addtid_b32 v220 offset:3072\nv_mfma_f32_16x16x32_bf16 v[16:19],v[144:147],v[108:111],0\nglobal_load_dwordx4 v[76:79],v59,s[12:13] offset:1024 sc1\n" \
"ds_write_addtid_b32 v221 offset:3328\nv_mfma_f32_16x16x32_bf16 v[16:19],v[176:179],v[112:115],v[16:19]\nds_write_addtid_b32 v222 offset:3584\nds_write_addtid_b32 v223 offset:3840\n" \
"v_mfma_f32_16x16x32_bf16 v[20:23],v[148:151],v[108:111],0\nds_write_addtid_b32 v224 offset:4096\nds_write_addtid_b32 v225 offset:4352\nv_mfma_f32_16x16x32_bf16 v[20:23],v[180:183],v[112:115],v[20:23]\n" \
"ds_write_addtid_b32 v226 offset:4608\nds_write_addtid_b32 v227 offset:4864\nv_mfma_f32_16x16x32_bf16 v[24:27],v[152:155],v[108:111],0\nds_write_addtid_b32 v228 offset:5120\n" \
"ds_write_addtid_b32 v229 offset:5376\nv_mfma_f32_16x16x32_bf16 v[24:27],v[184:187],v[112:115],v[24:27]\nds_write_addtid_b32 v230 offset:5632\nds_write_addtid_b32 v231 offset:5888\n" \
"v_mfma_f32_16x16x32_bf16 v[28:31],v[156:159],v[108:111],0\nglobal_load_dwordx4 v[80:83],v59,s[12:13] offset:2048 sc1\nds_write_addtid_b32 v232 offset:6144\nv_mfma_f32_16x16x32_bf16 v[28:31],v[188:191],v[112:115],v[28:31]\n" \
"ds_write_addtid_b32 v233 offset:6400\ns_waitcnt vmcnt(10)\nv_mfma_f32_16x16x32_bf16 v[0:3],a[0:3],v[116:119],v[0:3]\nds_write_addtid_b32 v234 offset:6656\n" \
"ds_write_addtid_b32 v235 offset:6912\nv_mfma_f32_16x16x32_bf16 v[0:3],a[32:35],v[120:123],v[0:3]\nbuffer_load_dwordx4 v[108:111],v59,s[36:39],s77 offen sc1\nds_write_addtid_b32 v236 offset:7168\n" \
"v_mfma_f32_16x16x32_bf16 v[4:7],a[4:7],v[116:119],v[4:7]\nds_write_addtid_b32 v237 offset:7424\nds_write_addtid_b32 v238 offset:7680\nv_mfma_f32_16x16x32_bf16 v[4:7],a[36:39],v[120:123],v[4:7]\n" \
"ds_write_addtid_b32 v239 offset:7936\nv_mfma_f32_16x16x32_bf16 v[8:11],a[8:11],v[116:119],v[8:11]\nglobal_load_dwordx4 v[84:87],v59,s[14:15] sc1\nv_mfma_f32_16x16x32_bf16 v[8:11],a[40:43],v[120:123],v[8:11]\n" \
"v_mfma_f32_16x16x32_bf16 v[12:15],a[12:15],v[116:119],v[12:15]\nv_mfma_f32_16x16x32_bf16 v[12:15],a[44:47],v[120:123],v[12:15]\nv_mfma_f32_16x16x32_bf16 v[16:19],a[16:19],v[116:119],v[16:19]\nv_mfma_f32_16x16x32_bf16 v[16:19],a[48:51],v[120:123],v[16:19]\n" \
"s_waitcnt lgkmcnt(0)\ns_barrier\nv_mfma_f32_16x16x32_bf16 v[20:23],a[20:23],v[116:119],v[20:23]\nglobal_load_dwordx4 v[88:91],v59,s[14:15] offset:1024 sc1\n" \
"ds_read_b128 v[32:35],v58\nv_mfma_f32_16x16x32_bf16 v[20:23],a[52:55],v[120:123],v[20:23]\nds_read_b128 v[36:39],v58 offset:8192\nds_read_b128 v[40:43],v58 offset:16384\n" \
"v_mfma_f32_16x16x32_bf16 v[24:27],a[24:27],v[116:119],v[24:27]\nds_read_b128 v[44:47],v58 offset:24576\nds_read_b128 v[48:51],v58 offset:4096\nv_mfma_f32_16x16x32_bf16 v[24:27],a[56:59],v[120:123],v[24:27]\n" \
"ds_read_b128 v[240:243],v58 offset:12288\nds_read_b128 v[244:247],v58 offset:20480\nv_mfma_f32_16x16x32_bf16 v[28:31],a[28:31],v[116:119],v[28:31]\nds_read_b128 v[248:251],v58 offset:28672\n" \
"s_waitcnt vmcnt(8)\nv_mfma_f32_16x16x32_bf16 v[28:31],a[60:63],v[120:123],v[28:31]\nv_cmp_eq_u32 vcc,-1,v60\nv_mfma_f32_16x16x32_bf16 v[0:3],a[64:67],v[124:127],v[0:3]\n" \
"global_load_dwordx4 v[92:95],v59,s[14:15] offset:2048 sc1\nv_mfma_f32_16x16x32_bf16 v[0:3],a[96:99],v[192:195],v[0:3]\nbuffer_load_dwordx2 v[116:117],v56,s[36:39],s90 offen sc1\ns_cbranch_vccnz .Lsp2A_%=\n" \
".Lbp2A_%=:\nv_mfma_f32_16x16x32_bf16 v[4:7],a[68:71],v[124:127],v[4:7]\nbuffer_load_dwordx2 v[118:119],v56,s[36:39],s90 offen offset:32 sc1\nv_lshlrev_b32 v112,16,v60\n" \
"v_mfma_f32_16x16x32_bf16 v[4:7],a[100:103],v[192:195],v[4:7]\nv_and_b32 v113,0xffff0000,v60\nv_add_f32 v64,v64,v112\nv_mfma_f32_16x16x32_bf16 v[8:11],a[72:75],v[124:127],v[8:11]\n" \
"s_waitcnt lgkmcnt(4)\nv_add_f32 v32,v32,v36\nv_mfma_f32_16x16x32_bf16 v[8:11],a[104:107],v[192:195],v[8:11]\nv_add_f32 v33,v33,v37\n" \
"v_add_f32 v34,v34,v38\nv_mfma_f32_16x16x32_bf16 v[12:15],a[76:79],v[124:127],v[12:15]\nglobal_load_dwordx4 v[96:99],v59,s[16:17] sc1\nv_add_f32 v35,v35,v39\n" \
"v_mfma_f32_16x16x32_bf16 v[12:15],a[108:111],v[192:195],v[12:15]\nbuffer_load_dwordx2 v[120:121],v56,s[36:39],s100 offen sc1\nv_add_f32 v40,v40,v44\nv_mfma_f32_16x16x32_bf16 v[16:19],a[80:83],v[124:127],v[16:19]\n" \
"buffer_load_dwordx2 v[122:123],v56,s[36:39],s100 offen offset:32 sc1\nv_add_f32 v41,v41,v45\nv_mfma_f32_16x16x32_bf16 v[16:19],a[112:115],v[192:195],v[16:19]\nv_add_f32 v42,v42,v46\n" \
"v_add_f32 v43,v43,v47\nv_mfma_f32_16x16x32_bf16 v[20:23],a[84:87],v[124:127],v[20:23]\nv_add_f32 v32,v32,v40\nv_add_f32 v33,v33,v41\n" \
"v_mfma_f32_16x16x32_bf16 v[20:23],a[116:119],v[192:195],v[20:23]\nv_add_f32 v34,v34,v42\nv_add_f32 v35,v35,v43\nv_mfma_f32_16x16x32_bf16 v[24:27],a[88:91],v[124:127],v[24:27]\n" \
"global_load_dwordx4 v[100:103],v59,s[16:17] offset:1024 sc1\nv_cvt_pk_bf16_f32 v36,v32,v33\nv_mfma_f32_16x16x32_bf16 v[24:27],a[120:123],v[192:195],v[24:27]\nv_cvt_pk_bf16_f32 v37,v34,v35\n" \
"s_waitcnt lgkmcnt(0)\nv_mfma_f32_16x16x32_bf16 v[28:31],a[92:95],v[124:127],v[28:31]\nv_add_f32 v48,v48,v240\nv_add_f32 v49,v49,v241\n" \
"v_mfma_f32_16x16x32_bf16 v[28:31],a[124:127],v[192:195],v[28:31]\nv_add_f32 v50,v50,v242\nv_mfma_f32_16x16x32_bf16 v[0:3],a[128:131],v[196:199],v[0:3]\nv_add_f32 v65,v65,v113\n" \
"v_add_f32 v51,v51,v243\nv_mfma_f32_16x16x32_bf16 v[0:3],a[160:163],v[200:203],v[0:3]\nv_lshlrev_b32 v112,16,v61\nv_add_f32 v244,v244,v248\n" \
"v_mfma_f32_16x16x32_bf16 v[4:7],a[132:135],v[196:199],v[4:7]\nglobal_load_dwordx4 v[104:107],v59,s[16:17] offset:2048 sc1\nv_and_b32 v113,0xffff0000,v61\nv_mfma_f32_16x16x32_bf16 v[4:7],a[164:167],v[200:203],v[4:7]\n" \
"v_add_f32 v245,v245,v249\nv_add_f32 v66,v66,v112\nv_mfma_f32_16x16x32_bf16 v[8:11],a[136:139],v[196:199],v[8:11]\nv_add_f32 v246,v246,v250\n" \
"v_add_f32 v67,v67,v113\nv_mfma_f32_16x16x32_bf16 v[8:11],a[168:171],v[200:203],v[8:11]\nv_add_f32 v247,v247,v251\nv_lshlrev_b32 v112,16,v62\n" \
"v_mfma_f32_16x16x32_bf16 v[12:15],a[140:143],v[196:199],v[12:15]\nv_add_f32 v48,v48,v244\nv_and_b32 v113,0xffff0000,v62\nv_mfma_f32_16x16x32_bf16 v[12:15],a[172:175],v[200:203],v[12:15]\n" \
"v_add_f32 v49,v49,v245\nv_add_f32 v68,v68,v112\nv_mfma_f32_16x16x32_bf16 v[16:19],a[144:147],v[196:199],v[16:19]\nv_add_f32 v50,v50,v246\n" \
"v_add_f32 v69,v69,v113\nv_mfma_f32_16x16x32_bf16 v[16:19],a[176:179],v[200:203],v[16:19]\nv_add_f32 v51,v51,v247\nv_lshlrev_b32 v112,16,v63\n" \
"v_mfma_f32_16x16x32_bf16 v[20:23],a[148:151],v[196:199],v[20:23]\nv_cvt_pk_bf16_f32 v38,v48,v49\nv_and_b32 v113,0xffff0000,v63\nv_mfma_f32_16x16x32_bf16 v[20:23],a[180:183],v[200:203],v[20:23]\n" \
"v_cvt_pk_bf16_f32 v39,v50,v51\nv_add_f32 v70,v70,v112\nv_mfma_f32_16x16x32_bf16 v[24:27],a[152:155],v[196:199],v[24:27]\nbuffer_store_dwordx4 v[36:39],v59,s[36:39],s74 offen sc1\n" \
"v_add_f32 v71,v71,v113\nv_mfma_f32_16x16x32_bf16 v[24:27],a[184:187],v[200:203],v[24:27]\nv_mfma_f32_16x16x32_bf16 v[28:31],a[156:159],v[196:199],v[28:31]\nv_mfma_f32_16x16x32_bf16 v[28:31],a[188:191],v[200:203],v[28:31]\n" \
"s_waitcnt vmcnt(16)\nv_mfma_f32_16x16x32_bf16 v[0:3],a[192:195],v[204:207],v[0:3]\nv_mfma_f32_16x16x32_bf16 v[4:7],a[196:199],v[204:207],v[4:7]\nv_mfma_f32_16x16x32_bf16 v[8:11],a[200:203],v[204:207],v[8:11]\n" \
"v_mfma_f32_16x16x32_bf16 v[12:15],a[204:207],v[204:207],v[12:15]\nv_mfma_f32_16x16x32_bf16 v[16:19],a[208:211],v[204:207],v[16:19]\nv_mfma_f32_16x16x32_bf16 v[20:23],a[212:215],v[204:207],v[20:23]\nv_mfma_f32_16x16x32_bf16 v[24:27],a[216:219],v[204:207],v[24:27]\n" \
"v_mfma_f32_16x16x32_bf16 v[28:31],a[220:223],v[204:207],v[28:31]\ns_waitcnt vmcnt(13)\nv_mfma_f32_16x16x32_bf16 v[208:211],v[128:131],v[72:75],0\nv_add_u32 v59,409600,v59\n" \
"s_waitcnt vmcnt(11)\nv_mfma_f32_16x16x32_bf16 v[208:211],v[160:163],v[76:79],v[208:211]\nds_write_addtid_b32 v0 offset:32768\nds_write_addtid_b32 v1 offset:33024\n" \
"v_mfma_f32_16x16x32_bf16 v[212:215],v[132:135],v[72:75],0\nds_write_addtid_b32 v2 offset:33280\nds_write_addtid_b32 v3 offset:33536\nv_mfma_f32_16x16x32_bf16 v[212:215],v[164:167],v[76:79],v[212:215]\n" \
"buffer_load_dwordx4 v[112:115],v59,s[36:39],s73 offen sc1\nds_write_addtid_b32 v4 offset:33792\nv_mfma_f32_16x16x32_bf16 v[216:219],v[136:139],v[72:75],0\nds_write_addtid_b32 v5 offset:34048\n" \
"ds_write_addtid_b32 v6 offset:34304\nv_mfma_f32_16x16x32_bf16 v[216:219],v[168:171],v[76:79],v[216:219]\nds_write_addtid_b32 v7 offset:34560\nds_write_addtid_b32 v8 offset:34816\n" \
"v_mfma_f32_16x16x32_bf16 v[220:223],v[140:143],v[72:75],0\nds_write_addtid_b32 v9 offset:35072\nds_write_addtid_b32 v10 offset:35328\nv_mfma_f32_16x16x32_bf16 v[220:223],v[172:175],v[76:79],v[220:223]\n" \
"ds_write_addtid_b32 v11 offset:35584\ns_waitcnt vmcnt(11)\nv_mfma_f32_16x16x32_bf16 v[208:211],a[0:3],v[80:83],v[208:211]\nds_write_addtid_b32 v12 offset:35840\n" \
"ds_write_addtid_b32 v13 offset:36096\nv_mfma_f32_16x16x32_bf16 v[208:211],a[32:35],v[84:87],v[208:211]\nds_write_addtid_b32 v14 offset:36352\nds_write_addtid_b32 v15 offset:36608\n" \
"v_mfma_f32_16x16x32_bf16 v[212:215],a[4:7],v[80:83],v[212:215]\nds_write_addtid_b32 v16 offset:36864\nds_write_addtid_b32 v17 offset:37120\nv_mfma_f32_16x16x32_bf16 v[212:215],a[36:39],v[84:87],v[212:215]\n" \
"ds_write_addtid_b32 v18 offset:37376\nds_write_addtid_b32 v19 offset:37632\nv_mfma_f32_16x16x32_bf16 v[216:219],a[8:11],v[80:83],v[216:219]\nds_write_addtid_b32 v20 offset:37888\n" \
"ds_write_addtid_b32 v21 offset:38144\nv_mfma_f32_16x16x32_bf16 v[216:219],a[40:43],v[84:87],v[216:219]\nds_write_addtid_b32 v22 offset:38400\nds_write_addtid_b32 v23 offset:38656\n" \
"v_mfma_f32_16x16x32_bf16 v[220:223],a[12:15],v[80:83],v[220:223]\nds_write_addtid_b32 v24 offset:38912\nds_write_addtid_b32 v25 offset:39168\nv_mfma_f32_16x16x32_bf16 v[220:223],a[44:47],v[84:87],v[220:223]\n" \
"ds_write_addtid_b32 v26 offset:39424\ns_waitcnt vmcnt(9)\nv_mfma_f32_16x16x32_bf16 v[208:211],a[64:67],v[88:91],v[208:211]\nds_write_addtid_b32 v27 offset:39680\n" \
"ds_write_addtid_b32 v28 offset:39936\nv_mfma_f32_16x16x32_bf16 v[208:211],a[96:99],v[92:95],v[208:211]\nds_write_addtid_b32 v29 offset:40192\nds_write_addtid_b32 v30 offset:40448\n" \
"v_mfma_f32_16x16x32_bf16 v[212:215],a[68:71],v[88:91],v[212:215]\nds_write_addtid_b32 v31 offset:40704\nv_cmp_eq_u32 vcc,-1,v108\nv_mfma_f32_16x16x32_bf16 v[212:215],a[100:103],v[92:95],v[212:215]\n" \
"v_and_b32 v62,0x30,v59\nv_add_u32 v62,s67,v62\nv_mfma_f32_16x16x32_bf16 v[216:219],a[72:75],v[88:91],v[216:219]\ns_cbranch_vccnz .Lsp2B_%=\n" \
".Lbp2B_%=:\nv_lshlrev_b32 v60,16,v108\nv_mfma_f32_16x16x32_bf16 v[216:219],a[104:107],v[92:95],v[216:219]\nv_and_b32 v61,0xffff0000,v108\n" \
"v_add_f32 v52,v52,v60\nv_mfma_f32_16x16x32_bf16 v[220:223],a[76:79],v[88:91],v[220:223]\nv_add_f32 v53,v53,v61\nv_lshlrev_b32 v60,16,v109\n" \
"v_mfma_f32_16x16x32_bf16 v[220:223],a[108:111],v[92:95],v[220:223]\nv_and_b32 v61,0xffff0000,v109\ns_waitcnt vmcnt(3)\nv_mfma_f32_16x16x32_bf16 v[208:211],a[128:131],v[96:99],v[208:211]\n" \
"s_waitcnt lgkmcnt(0)\ns_barrier\nv_mfma_f32_16x16x32_bf16 v[208:211],a[160:163],v[100:103],v[208:211]\nds_read_b128 v[32:35],v58 offset:32768\n" \
"ds_read_b128 v[36:39],v58 offset:40960\nv_mfma_f32_16x16x32_bf16 v[212:215],a[132:135],v[96:99],v[212:215]\nds_read_b128 v[40:43],v58 offset:49152\nds_read_b128 v[44:47],v58 offset:57344\n" \
"v_mfma_f32_16x16x32_bf16 v[212:215],a[164:167],v[100:103],v[212:215]\nds_read_b128 v[48:51],v58 offset:36864\nds_read_b128 v[240:243],v58 offset:45056\nv_mfma_f32_16x16x32_bf16 v[216:219],a[136:139],v[96:99],v[216:219]\n" \
"ds_read_b128 v[244:247],v58 offset:53248\nds_read_b128 v[248:251],v58 offset:61440\nv_mfma_f32_16x16x32_bf16 v[216:219],a[168:171],v[100:103],v[216:219]\nv_add_f32 v54,v54,v60\n" \
"v_add_f32 v55,v55,v61\nv_mfma_f32_16x16x32_bf16 v[220:223],a[140:143],v[96:99],v[220:223]\nv_lshlrev_b32 v60,16,v110\nv_and_b32 v61,0xffff0000,v110\n" \
"v_mfma_f32_16x16x32_bf16 v[220:223],a[172:175],v[100:103],v[220:223]\nv_add_f32 v252,v252,v60\ns_waitcnt vmcnt(2)\nv_mfma_f32_16x16x32_bf16 v[208:211],a[192:195],v[104:107],v[208:211]\n" \
"v_add_f32 v253,v253,v61\nv_lshlrev_b32 v60,16,v111\nv_mfma_f32_16x16x32_bf16 v[212:215],a[196:199],v[104:107],v[212:215]\nv_and_b32 v61,0xffff0000,v111\n" \
"v_add_f32 v254,v254,v60\nv_mfma_f32_16x16x32_bf16 v[216:219],a[200:203],v[104:107],v[216:219]\ns_waitcnt lgkmcnt(4)\nv_add_f32 v32,v32,v36\n" \
"v_mfma_f32_16x16x32_bf16 v[220:223],a[204:207],v[104:107],v[220:223]\nv_add_f32 v33,v33,v37\nv_add_f32 v34,v34,v38\nv_mfma_f32_16x16x32_bf16 v[224:227],v[144:147],v[72:75],0\n" \
"v_add_f32 v35,v35,v39\nv_add_f32 v40,v40,v44\nv_mfma_f32_16x16x32_bf16 v[224:227],v[176:179],v[76:79],v[224:227]\nv_add_f32 v41,v41,v45\n" \
"v_add_f32 v42,v42,v46\nv_mfma_f32_16x16x32_bf16 v[228:231],v[148:151],v[72:75],0\nds_write_addtid_b32 v208\nds_write_addtid_b32 v209 offset:256\n" \
"v_mfma_f32_16x16x32_bf16 v[228:231],v[180:183],v[76:79],v[228:231]\nds_write_addtid_b32 v210 offset:512\nds_write_addtid_b32 v211 offset:768\nv_mfma_f32_16x16x32_bf16 v[232:235],v[152:155],v[72:75],0\n" \
"ds_write_addtid_b32 v212 offset:1024\nds_write_addtid_b32 v213 offset:1280\nv_mfma_f32_16x16x32_bf16 v[232:235],v[184:187],v[76:79],v[232:235]\nds_write_addtid_b32 v214 offset:1536\n" \
"ds_write_addtid_b32 v215 offset:1792\nv_mfma_f32_16x16x32_bf16 v[236:239],v[156:159],v[72:75],0\nds_write_addtid_b32 v216 offset:2048\nds_write_addtid_b32 v217 offset:2304\n" \
"v_mfma_f32_16x16x32_bf16 v[236:239],v[188:191],v[76:79],v[236:239]\nds_write_addtid_b32 v218 offset:2560\nds_write_addtid_b32 v219 offset:2816\nv_mfma_f32_16x16x32_bf16 v[224:227],a[16:19],v[80:83],v[224:227]\n" \
"ds_write_addtid_b32 v220 offset:3072\nds_write_addtid_b32 v221 offset:3328\nv_mfma_f32_16x16x32_bf16 v[224:227],a[48:51],v[84:87],v[224:227]\nds_write_addtid_b32 v222 offset:3584\n" \
"ds_write_addtid_b32 v223 offset:3840\nv_mfma_f32_16x16x32_bf16 v[228:231],a[20:23],v[80:83],v[228:231]\nv_add_f32 v43,v43,v47\nv_add_f32 v32,v32,v40\n" \
"v_mfma_f32_16x16x32_bf16 v[228:231],a[52:55],v[84:87],v[228:231]\nv_add_f32 v33,v33,v41\nv_add_f32 v34,v34,v42\nv_mfma_f32_16x16x32_bf16 v[232:235],a[24:27],v[80:83],v[232:235]\n" \
"v_add_f32 v255,v255,v61\nv_add_f32 v35,v35,v43\nv_mfma_f32_16x16x32_bf16 v[232:235],a[56:59],v[84:87],v[232:235]\ns_waitcnt vmcnt(0)\n" \
"v_mfma_f32_16x16x32_bf16 v[236:239],a[28:31],v[80:83],v[236:239]\nbuffer_load_dwordx4 v[124:127],v59,s[36:39],s78 offen sc1\nv_cmp_eq_u32 vcc,-1,v112\nv_mfma_f32_16x16x32_bf16 v[236:239],a[60:63],v[84:87],v[236:239]\n" \
"s_waitcnt lgkmcnt(0)\ns_barrier\nv_mfma_f32_16x16x32_bf16 v[224:227],a[80:83],v[88:91],v[224:227]\nds_read_b128 v[16:19],v58\n" \
"ds_read_b128 v[20:23],v58 offset:8192\nv_mfma_f32_16x16x32_bf16 v[224:227],a[112:115],v[92:95],v[224:227]\nds_read_b128 v[24:27],v58 offset:16384\nds_read_b128 v[28:31],v58 offset:24576\n" \
"v_mfma_f32_16x16x32_bf16 v[228:231],a[84:87],v[88:91],v[228:231]\nv_add_f32 v48,v48,v240\ns_cbranch_vccnz .Lsp3A_%=\n.Lbp3A_%=:\n" \
"v_mfma_f32_16x16x32_bf16 v[228:231],a[116:119],v[92:95],v[228:231]\nv_add_f32 v49,v49,v241\nv_lshlrev_b32 v60,16,v112\nv_mfma_f32_16x16x32_bf16 v[232:235],a[88:91],v[88:91],v[232:235]\n" \
"v_add_f32 v50,v50,v242\nv_and_b32 v61,0xffff0000,v112\nv_mfma_f32_16x16x32_bf16 v[232:235],a[120:123],v[92:95],v[232:235]\nv_add_f32 v51,v51,v243\n" \
"v_add_f32 v64,v64,v60\nv_mfma_f32_16x16x32_bf16 v[236:239],a[92:95],v[88:91],v[236:239]\nv_add_f32 v244,v244,v248\nv_add_f32 v65,v65,v61\n" \
"v_mfma_f32_16x16x32_bf16 v[236:239],a[124:127],v[92:95],v[236:239]\nv_add_f32 v245,v245,v249\nv_lshlrev_b32 v60,16,v113\nv_mfma_f32_16x16x32_bf16 v[224:227],a[144:147],v[96:99],v[224:227]\n" \
"v_add_f32 v246,v246,v250\nv_and_b32 v61,0xffff0000,v113\nv_mfma_f32_16x16x32_bf16 v[224:227],a[176:179],v[100:103],v[224:227]\nv_add_f32 v247,v247,v251\n" \
"v_add_f32 v66,v66,v60\nv_mfma_f32_16x16x32_bf16 v[228:231],a[148:151],v[96:99],v[228:231]\nv_add_f32 v48,v48,v244\nv_add_f32 v67,v67,v61\n" \
"v_mfma_f32_16x16x32_bf16 v[228:231],a[180:183],v[100:103],v[228:231]\nv_add_f32 v49,v49,v245\nv_lshlrev_b32 v60,16,v114\nv_add_f32 v50,v50,v246\n" \
"v_mfma_f32_16x16x32_bf16 v[232:235],a[152:155],v[96:99],v[232:235]\nv_and_b32 v61,0xffff0000,v114\nv_add_f32 v51,v51,v247\nv_add_f32 v68,v68,v60\n" \
"v_mfma_f32_16x16x32_bf16 v[232:235],a[184:187],v[100:103],v[232:235]\nv_add_f32 v69,v69,v61\nv_lshlrev_b32 v60,16,v115\nv_and_b32 v61,0xffff0000,v115\n" \
"v_mfma_f32_16x16x32_bf16 v[236:239],a[156:159],v[96:99],v[236:239]\nv_add_f32 v70,v70,v60\nds_read_b128 v[8:11],v62\nv_mfma_f32_16x16x32_bf16 v[236:239],a[188:191],v[100:103],v[236:239]\n" \
"v_add_f32 v71,v71,v61\nds_read_b128 v[12:15],v62 offset:64\nv_mfma_f32_16x16x32_bf16 v[224:227],a[208:211],v[104:107],v[224:227]\nv_lshlrev_b32 v0,16,v116\n" \
"v_and_b32 v1,0xffff0000,v116\nv_mfma_f32_16x16x32_bf16 v[228:231],a[212:215],v[104:107],v[228:231]\nv_lshlrev_b32 v2,16,v117\nv_and_b32 v3,0xffff0000,v117\n" \
"v_mfma_f32_16x16x32_bf16 v[232:235],a[216:219],v[104:107],v[232:235]\nv_lshlrev_b32 v4,16,v120\nv_and_b32 v5,0xffff0000,v120\nv_mfma_f32_16x16x32_bf16 v[236:239],a[220:223],v[104:107],v[236:239]\n" \
"v_lshlrev_b32 v6,16,v121\nv_and_b32 v7,0xffff0000,v121\ns_waitcnt lgkmcnt(0)\nv_fma_f32 v0,v8,v64,v0\n" \
"v_fma_f32 v1,v9,v65,v1\nv_fma_f32 v2,v10,v66,v2\nv_fma_f32 v3,v11,v67,v3\nv_fma_f32 v4,v8,v68,v4\n" \
"v_fma_f32 v5,v9,v69,v5\nv_fma_f32 v6,v10,v70,v6\nv_fma_f32 v7,v11,v71,v7\nv_fma_f32 v0,v8,v32,v0\n" \
"v_fma_f32 v1,v9,v33,v1\nv_fma_f32 v2,v10,v34,v2\nv_fma_f32 v3,v11,v35,v3\nv_cvt_pk_bf16_f32 v0,v0,v1\n" \
"v_cvt_pk_bf16_f32 v1,v2,v3\nbuffer_store_dwordx2 v[0:1],v56,s[44:47],0 offen sc1\nv_fma_f32 v4,v8,v48,v4\nv_fma_f32 v5,v9,v49,v5\n" \
"v_fma_f32 v6,v10,v50,v6\nv_fma_f32 v7,v11,v51,v7\nv_cvt_pk_bf16_f32 v4,v4,v5\nv_cvt_pk_bf16_f32 v5,v6,v7\n" \
"buffer_store_dwordx2 v[4:5],v56,s[44:47],s35 offen sc1\nv_lshlrev_b32 v64,16,v118\nv_and_b32 v65,0xffff0000,v118\nv_lshlrev_b32 v66,16,v119\n" \
"v_and_b32 v67,0xffff0000,v119\nds_write_addtid_b32 v224 offset:4096\nds_write_addtid_b32 v225 offset:4352\nds_write_addtid_b32 v226 offset:4608\n" \
"ds_write_addtid_b32 v227 offset:4864\nds_write_addtid_b32 v228 offset:5120\nds_write_addtid_b32 v229 offset:5376\nds_write_addtid_b32 v230 offset:5632\n" \
"ds_write_addtid_b32 v231 offset:5888\nds_write_addtid_b32 v232 offset:6144\nds_write_addtid_b32 v233 offset:6400\nds_write_addtid_b32 v234 offset:6656\n" \
"ds_write_addtid_b32 v235 offset:6912\nds_write_addtid_b32 v236 offset:7168\nds_write_addtid_b32 v237 offset:7424\nds_write_addtid_b32 v238 offset:7680\n" \
"ds_write_addtid_b32 v239 offset:7936\nv_lshlrev_b32 v68,16,v122\nv_and_b32 v69,0xffff0000,v122\nv_lshlrev_b32 v70,16,v123\n" \
"v_and_b32 v71,0xffff0000,v123\ns_waitcnt lgkmcnt(15)\nv_pk_add_f32 v[16:17],v[16:17],v[20:21]\nv_pk_add_f32 v[18:19],v[18:19],v[22:23]\n" \
"v_pk_add_f32 v[24:25],v[24:25],v[28:29]\nv_pk_add_f32 v[26:27],v[26:27],v[30:31]\nv_pk_add_f32 v[16:17],v[16:17],v[24:25]\nv_pk_add_f32 v[18:19],v[18:19],v[26:27]\n" \
"s_waitcnt lgkmcnt(0)\ns_barrier\nds_read_b128 v[32:35],v58 offset:4096\nds_read_b128 v[36:39],v58 offset:12288\n" \
"ds_read_b128 v[40:43],v58 offset:20480\nds_read_b128 v[44:47],v58 offset:28672\ns_waitcnt vmcnt(2)\nv_cmp_eq_u32 vcc,-1,v124\n" \
"s_cbranch_vccnz .Lsp3B_%=\n.Lbp3B_%=:\nv_lshlrev_b32 v20,16,v124\nv_and_b32 v21,0xffff0000,v124\n" \
"v_lshlrev_b32 v22,16,v125\nv_and_b32 v23,0xffff0000,v125\nv_lshlrev_b32 v24,16,v126\nv_and_b32 v25,0xffff0000,v126\n" \
"v_lshlrev_b32 v26,16,v127\nv_and_b32 v27,0xffff0000,v127\nv_pk_add_f32 v[52:53],v[52:53],v[20:21]\nv_pk_add_f32 v[54:55],v[54:55],v[22:23]\n" \
"v_pk_add_f32 v[252:253],v[252:253],v[24:25]\nv_pk_add_f32 v[254:255],v[254:255],v[26:27]\nv_pk_fma_f32 v[64:65],v[12:13],v[52:53],v[64:65]\nv_pk_fma_f32 v[66:67],v[14:15],v[54:55],v[66:67]\n" \
"v_pk_fma_f32 v[68:69],v[12:13],v[252:253],v[68:69]\nv_pk_fma_f32 v[70:71],v[14:15],v[254:255],v[70:71]\nv_pk_fma_f32 v[64:65],v[12:13],v[16:17],v[64:65]\nv_pk_fma_f32 v[66:67],v[14:15],v[18:19],v[66:67]\n" \
"v_cvt_pk_bf16_f32 v64,v64,v65\nv_cvt_pk_bf16_f32 v65,v66,v67\nbuffer_store_dwordx2 v[64:65],v56,s[44:47],0 offen offset:32 sc1\ns_waitcnt lgkmcnt(0)\n" \
"v_pk_add_f32 v[32:33],v[32:33],v[36:37]\nv_pk_add_f32 v[34:35],v[34:35],v[38:39]\nv_pk_add_f32 v[40:41],v[40:41],v[44:45]\nv_pk_add_f32 v[42:43],v[42:43],v[46:47]\n" \
"v_pk_add_f32 v[32:33],v[32:33],v[40:41]\nv_pk_add_f32 v[34:35],v[34:35],v[42:43]\nv_pk_fma_f32 v[68:69],v[12:13],v[32:33],v[68:69]\nv_pk_fma_f32 v[70:71],v[14:15],v[34:35],v[70:71]\n" \
"v_cvt_pk_bf16_f32 v68,v68,v69\nv_cvt_pk_bf16_f32 v69,v70,v71\nbuffer_store_dwordx2 v[68:69],v56,s[44:47],s35 offen offset:32 sc1\ns_branch .Lend_%=\n" \
".LsaF_%=:\ns_memrealtime s[94:95]\ns_waitcnt lgkmcnt(0)\n.LpaF_%=:\n" \
"s_sleep 2\nbuffer_load_dword v63,v61,s[36:39],s59 offen sc1\ns_memrealtime s[96:97]\ns_waitcnt vmcnt(0) lgkmcnt(0)\n" \
"s_sub_u32 s96,s96,s94\ns_cmp_gt_u32 s96,0x4c4b40\ns_cbranch_scc1 .LbaF_%=\nv_cmp_eq_u32 vcc,0,v63\n" \
"s_and_b64 vcc,vcc,s[86:87]\ns_cbranch_scc1 .LpaF_%=\ns_branch .LbaF_%=\n.LsbF_%=:\n" \
"s_memrealtime s[94:95]\ns_waitcnt lgkmcnt(0)\n.LpbF_%=:\ns_sleep 2\n" \
"buffer_load_dword v63,v61,s[36:39],s59 offen sc1\ns_memrealtime s[96:97]\ns_waitcnt vmcnt(0) lgkmcnt(0)\ns_sub_u32 s96,s96,s94\n" \
"s_cmp_gt_u32 s96,0x4c4b40\ns_cbranch_scc1 .LbbF_%=\nv_cmp_eq_u32 vcc,0,v63\ns_and_b64 vcc,vcc,s[86:87]\n" \
"s_cbranch_scc1 .LpbF_%=\ns_branch .LbbF_%=\n.LsaS_%=:\ns_memrealtime s[94:95]\n" \
"s_waitcnt lgkmcnt(0)\n.LpaS_%=:\ns_sleep 2\nbuffer_load_dword v63,v61,s[36:39],s59 offen sc1\n" \
"s_memrealtime s[96:97]\ns_waitcnt vmcnt(0) lgkmcnt(0)\ns_sub_u32 s96,s96,s94\ns_cmp_gt_u32 s96,0x4c4b40\n" \
"s_cbranch_scc1 .LbaS_%=\nv_cmp_eq_u32 vcc,0,v63\ns_and_b64 vcc,vcc,s[86:87]\ns_cbranch_scc1 .LpaS_%=\n" \
"s_branch .LbaS_%=\n.LsbS_%=:\ns_memrealtime s[94:95]\ns_waitcnt lgkmcnt(0)\n" \
".LpbS_%=:\ns_sleep 2\nbuffer_load_dword v63,v61,s[36:39],s59 offen sc1\ns_memrealtime s[96:97]\n" \
"s_waitcnt vmcnt(0) lgkmcnt(0)\ns_sub_u32 s96,s96,s94\ns_cmp_gt_u32 s96,0x4c4b40\ns_cbranch_scc1 .LbbS_%=\n" \
"v_cmp_eq_u32 vcc,0,v63\ns_and_b64 vcc,vcc,s[86:87]\ns_cbranch_scc1 .LpbS_%=\ns_branch .LbbS_%=\n" \
".Lsp3B_%=:\ns_memrealtime s[94:95]\ns_waitcnt lgkmcnt(0)\n.Lpp3B_%=:\n" \
"s_sub_u32 s101,s78,0\nbuffer_load_dwordx4 v[124:127],v59,s[36:39],s101 offen sc1\ns_memrealtime s[96:97]\ns_waitcnt vmcnt(0) lgkmcnt(0)\n" \
"s_sub_u32 s96,s96,s94\ns_cmp_gt_u32 s96,0x4c4b40\ns_cbranch_scc1 .Lbp3B_%=\nv_cmp_eq_u32 vcc,-1,v124\n" \
"s_cbranch_vccnz .Lpp3B_%=\ns_branch .Lbp3B_%=\n.Lsg4_%=:\ns_memrealtime s[94:95]\n" \
"s_waitcnt lgkmcnt(0)\n.Lpg4_%=:\ns_sleep 2\nbuffer_load_dword v63,v61,s[36:39],s59 offen sc1\n" \
"s_memrealtime s[96:97]\ns_waitcnt vmcnt(0) lgkmcnt(0)\ns_sub_u32 s96,s96,s94\ns_cmp_gt_u32 s96,0x4c4b40\n" \
"s_cbranch_scc1 .Lbg4_%=\nv_cmp_eq_u32 vcc,0,v63\ns_and_b64 vcc,vcc,s[86:87]\ns_cbranch_scc1 .Lpg4_%=\n" \
"s_branch .Lbg4_%=\n.Lse_%=:\ns_memrealtime s[94:95]\ns_waitcnt lgkmcnt(0)\n" \
".Lpe_%=:\nbuffer_load_dword v64,v69,s[36:39],s79 offen sc1\nbuffer_load_dword v65,v69,s[36:39],s80 offen sc1\nbuffer_load_dword v66,v69,s[36:39],s81 offen sc1\n" \
"buffer_load_dword v67,v69,s[36:39],s82 offen sc1\ns_memrealtime s[96:97]\ns_waitcnt vmcnt(0) lgkmcnt(0)\ns_sub_u32 s96,s96,s94\n" \
"s_cmp_gt_u32 s96,0x4c4b40\ns_cbranch_scc1 .Lbe_%=\nv_max3_u32 v52,v64,v65,v66\nv_max_u32 v52,v52,v67\n" \
"v_cmp_eq_u32 vcc,-1,v52\ns_cbranch_vccnz .Lpe_%=\ns_branch .Lbe_%=\n.Lsp1_%=:\n" \
"s_memrealtime s[94:95]\ns_waitcnt lgkmcnt(0)\n.Lpp1_%=:\ns_sub_u32 s101,s71,409600\n" \
"buffer_load_dwordx4 v[64:67],v59,s[36:39],s101 offen sc1\ns_sub_u32 s101,s76,409600\nbuffer_load_dwordx4 v[52:55],v59,s[36:39],s101 offen sc1\ns_memrealtime s[96:97]\n" \
"s_waitcnt vmcnt(0) lgkmcnt(0)\ns_sub_u32 s96,s96,s94\ns_cmp_gt_u32 s96,0x4c4b40\ns_cbranch_scc1 .Lbp1_%=\n" \
"v_max_u32 v62,v64,v52\nv_cmp_eq_u32 vcc,-1,v62\ns_cbranch_vccnz .Lpp1_%=\ns_branch .Lbp1_%=\n" \
".Lsp2A_%=:\ns_memrealtime s[94:95]\ns_waitcnt lgkmcnt(0)\n.Lpp2A_%=:\n" \
"s_sub_u32 s101,s72,409600\nbuffer_load_dwordx4 v[60:63],v59,s[36:39],s101 offen sc1\ns_memrealtime s[96:97]\ns_waitcnt vmcnt(0) lgkmcnt(0)\n" \
"s_sub_u32 s96,s96,s94\ns_cmp_gt_u32 s96,0x4c4b40\ns_cbranch_scc1 .Lbp2A_%=\nv_cmp_eq_u32 vcc,-1,v60\n" \
"s_cbranch_vccnz .Lpp2A_%=\ns_branch .Lbp2A_%=\n.Lsp2B_%=:\ns_memrealtime s[94:95]\n" \
"s_waitcnt lgkmcnt(0)\n.Lpp2B_%=:\ns_sub_u32 s101,s77,409600\nbuffer_load_dwordx4 v[108:111],v59,s[36:39],s101 offen sc1\n" \
"s_memrealtime s[96:97]\ns_waitcnt vmcnt(0) lgkmcnt(0)\ns_sub_u32 s96,s96,s94\ns_cmp_gt_u32 s96,0x4c4b40\n" \
"s_cbranch_scc1 .Lbp2B_%=\nv_cmp_eq_u32 vcc,-1,v108\ns_cbranch_vccnz .Lpp2B_%=\ns_branch .Lbp2B_%=\n" \
".Lsp3A_%=:\ns_memrealtime s[94:95]\ns_waitcnt lgkmcnt(0)\n.Lpp3A_%=:\n" \
"s_sub_u32 s101,s73,0\nbuffer_load_dwordx4 v[112:115],v59,s[36:39],s101 offen sc1\ns_memrealtime s[96:97]\ns_waitcnt vmcnt(0) lgkmcnt(0)\n" \
"s_sub_u32 s96,s96,s94\ns_cmp_gt_u32 s96,0x4c4b40\ns_cbranch_scc1 .Lbp3A_%=\nv_cmp_eq_u32 vcc,-1,v112\n" \
"s_cbranch_vccnz .Lpp3A_%=\ns_branch .Lbp3A_%=\n.Lz0_%=:\ns_branch .Lz1_%=\n" \
".Lxq5_%=:\ns_mov_b64 exec,1\nbuffer_store_dword v61,off,s[36:39],s54 sc1\ns_mov_b64 exec,-1\n" \
"buffer_load_dword v63,v61,s[36:39],s75 offen sc1\ns_mov_b32 s75,s59\ns_add_u32 s54,s54,0x10000\ns_add_u32 s83,s83,819200\n" \
"s_branch .Lyq5_%=\n.Lz1_%=:\ns_branch .Lz2_%=\n.Lxt5_%=:\n" \
"v_and_b32 v20,0x300,v59\nv_and_b32 v21,0xf0,v59\nv_lshlrev_b32 v20,4,v20\ns_mov_b32 m0,162176\n" \
"v_lshl_or_b32 v20,v21,3,v20\nbuffer_load_dword v20,s[40:43],s101 offen lds\ns_mov_b32 m0,s56\ns_branch .Lyt5_%=\n" \
".Lz2_%=:\ns_branch .Lz3_%=\n.Lxq4_%=:\ns_mov_b64 exec,1\n" \
"buffer_store_dword v61,off,s[36:39],s54 sc1\ns_mov_b64 exec,-1\nbuffer_load_dword v63,v61,s[36:39],s75 offen sc1\ns_mov_b32 s75,s59\n" \
"s_add_u32 s54,s54,0x10000\ns_add_u32 s83,s83,819200\ns_branch .Lyq4_%=\n.Lz3_%=:\n" \
"s_branch .Lz4_%=\n.Lxt4_%=:\nv_and_b32 v0,0x300,v59\nv_and_b32 v1,0xf0,v59\n" \
"v_lshlrev_b32 v0,4,v0\ns_mov_b32 m0,162176\nv_lshl_or_b32 v0,v1,3,v0\nbuffer_load_dword v0,s[40:43],s101 offen lds\n" \
"s_mov_b32 m0,s56\ns_branch .Lyt4_%=\n.Lz4_%=:\ns_branch .Lzl_%=\n" \
".LsmP_%=:\ns_sleep 2\nbuffer_load_dwordx4 v[48:51],v2,s[36:39],s73 offen sc1\nbuffer_load_dwordx4 v[60:63],v2,s[36:39],s75 offen sc1\n" \
"buffer_load_dwordx4 v[52:55],v2,s[36:39],s74 offen sc1\nbuffer_load_dwordx4 v[64:67],v2,s[36:39],s76 offen sc1\nbuffer_load_dwordx4 v[56:59],v1,s[36:39],s77 offen sc1\nbuffer_load_dwordx4 v[68:71],v1,s[36:39],s78 offen sc1\n" \
"s_memrealtime s[96:97]\ns_waitcnt vmcnt(0) lgkmcnt(0)\ns_sub_u32 s96,s96,s30\ns_cmp_gt_u32 s96,0x4c4b40\n" \
"s_cbranch_scc1 .LbmP_%=\nv_cmp_ne_u64 vcc,s[28:29],v[48:49]\nv_cmp_ne_u64 s[84:85],s[28:29],v[52:53]\nv_cmp_ne_u64 s[82:83],s[28:29],v[56:57]\n" \
"s_or_b64 vcc,vcc,s[84:85]\nv_cmp_ne_u64 s[84:85],s[28:29],v[60:61]\ns_or_b64 vcc,vcc,s[82:83]\nv_cmp_ne_u64 s[82:83],s[28:29],v[64:65]\n" \
"s_or_b64 vcc,vcc,s[84:85]\nv_cmp_ne_u64 s[84:85],s[28:29],v[68:69]\ns_or_b64 vcc,vcc,s[82:83]\ns_or_b64 vcc,vcc,s[84:85]\n" \
"s_cbranch_vccnz .LsmP_%=\ns_branch .LbmP_%=\n.LsxP_%=:\ns_waitcnt lgkmcnt(0)\n" \
"v_mul_f32 v74,0.5,v74\nv_pk_add_f32 v[124:125],v[54:55],v[74:75] op_sel_hi:[1,0]\nv_pk_add_f32 v[126:127],v[58:59],1.0 op_sel_hi:[1,0]\nv_max3_f32 v112,|v50|,|v51|,|v124|\n" \
"v_max3_f32 v112,|v125|,|v126|,v112\nv_max_f32 v112,|v127|,v112\ns_nop 1\nv_max_u32_dpp v112,v112,v112 quad_perm:[1,0,3,2] row_mask:0xf bank_mask:0xf bound_ctrl:1\n" \
"s_nop 1\nv_max_u32_dpp v112,v112,v112 quad_perm:[2,3,0,1] row_mask:0xf bank_mask:0xf bound_ctrl:1\ns_nop 1\nv_max_u32_dpp v112,v112,v112 row_half_mirror row_mask:0xf bank_mask:0xf bound_ctrl:1\n" \
"s_nop 1\nv_max_u32_dpp v112,v112,v112 row_mirror row_mask:0xf bank_mask:0xf bound_ctrl:1\ns_nop 0\nv_readlane_b32 s82,v112,0\n" \
"v_readlane_b32 s100,v112,16\nv_readlane_b32 s101,v112,32\nv_readlane_b32 s31,v112,48\ns_max_u32 s100,s100,s101\n" \
"s_max_u32 s82,s82,s31\ns_max_u32 s82,s82,s100\nv_mov_b32 v113,s82\nds_write_b32 v251,v113 offset:16\n" \
"s_waitcnt lgkmcnt(0)\ns_barrier\nds_read_b128 v[124:127],v114 offset:16\ns_waitcnt lgkmcnt(0)\n" \
"v_max3_u32 v124,v124,v125,v126\nv_max_u32 v124,v124,v127\ns_nop 0\nv_readfirstlane_b32 s93,v124\n" \
"s_nop 0\ns_lshr_b32 s93,s93,23\ns_max_u32 s93,s93,64\ns_min_u32 s93,s93,254\n" \
"s_sub_u32 s82,256,s93\ns_lshl_b32 s82,s82,23\nv_mov_b32 v124,s82\nv_mul_f32 v74,v74,v124\n" \
"v_pk_mul_f32 v[62:63],v[50:51],v[124:125] op_sel_hi:[1,0]\nv_max3_f32 v112,|v62|,|v63|,0\nv_cvt_pk_f16_f32 v62,v62,v63\nds_write_b32 v252,v62 offset:62848\n" \
"v_pk_fma_f32 v[66:67],v[54:55],v[124:125],v[74:75] op_sel_hi:[1,0,0]\nv_max3_f32 v112,|v66|,|v67|,v112\nv_cvt_pk_f16_f32 v66,v66,v67\nds_write_b32 v253,v66 offset:62848\n" \
"v_pk_fma_f32 v[70:71],v[58:59],v[124:125],v[124:125] op_sel_hi:[1,0,0]\nv_max3_f32 v112,|v70|,|v71|,v112\nv_cvt_pk_f16_f32 v70,v70,v71\nds_write_b32 v254,v70 offset:62848\n" \
"s_waitcnt lgkmcnt(0)\ns_barrier\nds_read_b64 v[96:97],v234 offset:62848\nds_read_b64 v[98:99],v235 offset:62848\n" \
"ds_read_b64 v[100:101],v236 offset:62848\nds_read_b64 v[102:103],v237 offset:62848\nds_read_b64 v[104:105],v238 offset:62848\nds_read_b64 v[106:107],v239 offset:62848\n" \
"ds_read_b64 v[108:109],v240 offset:62848\nds_read_b64 v[110:111],v241 offset:62848\ns_waitcnt lgkmcnt(0)\ns_branch .LbxP_%=\n" \
".LsrP_%=:\ns_sleep 2\nbuffer_load_dwordx4 v[104:107],v1,s[36:39],s7 offen sc1\nbuffer_load_dwordx4 v[108:111],v1,s[36:39],s7 offen offset:1024 sc1\n" \
"s_memrealtime s[96:97]\ns_waitcnt vmcnt(0) lgkmcnt(0)\ns_sub_u32 s96,s96,s30\ns_cmp_gt_u32 s96,0x4c4b40\n" \
"s_cbranch_scc1 .LbrP_%=\nv_xor_b32 v112,s29,v105\nv_xor_b32 v113,s29,v107\nv_xor_b32 v114,s29,v109\n" \
"v_xor_b32 v115,s29,v111\nv_or3_b32 v112,v112,v113,v114\nv_xor_b32 v113,s28,v104\nv_xor_b32 v114,s28,v106\n" \
"v_or_b32 v112,v112,v115\nv_xor_b32 v115,s28,v108\nv_lshl_or_b32 v112,v112,8,v113\nv_xor_b32 v113,s28,v110\n" \
"v_or3_b32 v112,v112,v114,v115\nv_or_b32 v112,v112,v113\nv_cmp_ne_u32 vcc,0,v112\ns_nop 0\n" \
"s_cbranch_vccnz .LsrP_%=\ns_branch .LbrP_%=\n.LsnP_%=:\ns_sleep 2\n" \
"buffer_load_dwordx4 v[80:83],v120,s[36:39],s5 offen sc1\nbuffer_load_dwordx4 v[84:87],v120,s[36:39],s5 offen offset:2304 sc1\nbuffer_load_dwordx4 v[88:91],v120,s[36:39],s6 offen sc1\nbuffer_load_dwordx4 v[92:95],v120,s[36:39],s6 offen offset:2304 sc1\n" \
"s_memrealtime s[96:97]\ns_waitcnt vmcnt(0) lgkmcnt(0)\ns_sub_u32 s96,s96,s30\ns_cmp_gt_u32 s96,0x4c4b40\n" \
"s_cbranch_scc1 .LbnP_%=\nv_cmp_ne_u64 vcc,s[28:29],v[80:81]\nv_cmp_ne_u64 s[24:25],s[28:29],v[84:85]\nv_cmp_ne_u64 s[26:27],s[28:29],v[88:89]\n" \
"s_or_b64 vcc,vcc,s[24:25]\nv_cmp_ne_u64 s[24:25],s[28:29],v[92:93]\ns_or_b64 vcc,vcc,s[26:27]\ns_or_b64 vcc,vcc,s[24:25]\n" \
"s_cbranch_vccnz .LsnP_%=\ns_branch .LbnP_%=\n.LsuP_%=:\nv_and_b32 v125,1,v112\n" \
"v_cmpx_eq_u32 vcc,0,v125\nbuffer_load_dwordx4 v[112:115],v3,s[36:39],s16 offen sc1\ns_mov_b64 exec,-1\nv_and_b32 v125,1,v116\n" \
"v_cmpx_eq_u32 vcc,0,v125\nbuffer_load_dwordx4 v[116:119],v3,s[36:39],s17 offen sc1\ns_mov_b64 exec,-1\nv_and_b32 v125,1,v120\n" \
"v_cmpx_eq_u32 vcc,0,v125\nbuffer_load_dwordx4 v[120:123],v124,s[36:39],s18 offen sc1\ns_mov_b64 exec,-1\ns_memrealtime s[96:97]\n" \
"s_waitcnt vmcnt(0) lgkmcnt(0)\ns_sub_u32 s96,s96,s30\ns_cmp_gt_u32 s96,0x4c4b40\ns_cbranch_scc1 .LbuP_%=\n" \
"v_and_b32 v125,v112,v116\nv_bitop3_b32 v125,v125,v120,1 bitop3:0x80\nv_cmp_eq_u32 vcc,0,v125\ns_nop 0\n" \
"s_cbranch_vccnz .LsuP_%=\ns_branch .LbuP_%=\n.LsmN_%=:\ns_sleep 2\n" \
"buffer_load_dwordx4 v[48:51],v2,s[36:39],s73 offen sc1\nbuffer_load_dwordx4 v[60:63],v2,s[36:39],s75 offen sc1\nbuffer_load_dwordx4 v[52:55],v2,s[36:39],s74 offen sc1\nbuffer_load_dwordx4 v[64:67],v2,s[36:39],s76 offen sc1\n" \
"buffer_load_dwordx4 v[56:59],v1,s[36:39],s77 offen sc1\nbuffer_load_dwordx4 v[68:71],v1,s[36:39],s78 offen sc1\ns_memrealtime s[96:97]\ns_waitcnt vmcnt(0) lgkmcnt(0)\n" \
"s_sub_u32 s96,s96,s30\ns_cmp_gt_u32 s96,0x4c4b40\ns_cbranch_scc1 .LbmN_%=\nv_cmp_ne_u64 vcc,s[28:29],v[48:49]\n" \
"v_cmp_ne_u64 s[84:85],s[28:29],v[52:53]\nv_cmp_ne_u64 s[82:83],s[28:29],v[56:57]\ns_or_b64 vcc,vcc,s[84:85]\nv_cmp_ne_u64 s[84:85],s[28:29],v[60:61]\n" \
"s_or_b64 vcc,vcc,s[82:83]\nv_cmp_ne_u64 s[82:83],s[28:29],v[64:65]\ns_or_b64 vcc,vcc,s[84:85]\nv_cmp_ne_u64 s[84:85],s[28:29],v[68:69]\n" \
"s_or_b64 vcc,vcc,s[82:83]\ns_or_b64 vcc,vcc,s[84:85]\ns_cbranch_vccnz .LsmN_%=\ns_branch .LbmN_%=\n" \
".LsxN_%=:\ns_waitcnt lgkmcnt(0)\nv_mul_f32 v74,0.5,v74\nv_pk_add_f32 v[124:125],v[54:55],v[74:75] op_sel_hi:[1,0]\n" \
"v_pk_add_f32 v[126:127],v[58:59],1.0 op_sel_hi:[1,0]\nv_max3_f32 v112,|v50|,|v51|,|v124|\nv_max3_f32 v112,|v125|,|v126|,v112\nv_max_f32 v112,|v127|,v112\n" \
"s_nop 1\nv_max_u32_dpp v112,v112,v112 quad_perm:[1,0,3,2] row_mask:0xf bank_mask:0xf bound_ctrl:1\ns_nop 1\nv_max_u32_dpp v112,v112,v112 quad_perm:[2,3,0,1] row_mask:0xf bank_mask:0xf bound_ctrl:1\n" \
"s_nop 1\nv_max_u32_dpp v112,v112,v112 row_half_mirror row_mask:0xf bank_mask:0xf bound_ctrl:1\ns_nop 1\nv_max_u32_dpp v112,v112,v112 row_mirror row_mask:0xf bank_mask:0xf bound_ctrl:1\n" \
"s_nop 0\nv_readlane_b32 s82,v112,0\nv_readlane_b32 s100,v112,16\nv_readlane_b32 s101,v112,32\n" \
"v_readlane_b32 s31,v112,48\ns_max_u32 s100,s100,s101\ns_max_u32 s82,s82,s31\ns_max_u32 s82,s82,s100\n" \
"v_mov_b32 v113,s82\nds_write_b32 v251,v113 offset:16\ns_waitcnt lgkmcnt(0)\ns_barrier\n" \
"ds_read_b128 v[124:127],v114 offset:16\ns_waitcnt lgkmcnt(0)\nv_max3_u32 v124,v124,v125,v126\nv_max_u32 v124,v124,v127\n" \
"s_nop 0\nv_readfirstlane_b32 s93,v124\ns_nop 0\ns_lshr_b32 s93,s93,23\n" \
"s_max_u32 s93,s93,64\ns_min_u32 s93,s93,254\ns_sub_u32 s82,256,s93\ns_lshl_b32 s82,s82,23\n" \
"v_mov_b32 v124,s82\nv_mul_f32 v74,v74,v124\nv_pk_mul_f32 v[62:63],v[50:51],v[124:125] op_sel_hi:[1,0]\nv_max3_f32 v112,|v62|,|v63|,0\n" \
"v_cvt_pk_f16_f32 v62,v62,v63\nds_write_b32 v252,v62 offset:62848\nv_pk_fma_f32 v[66:67],v[54:55],v[124:125],v[74:75] op_sel_hi:[1,0,0]\nv_max3_f32 v112,|v66|,|v67|,v112\n" \
"v_cvt_pk_f16_f32 v66,v66,v67\nds_write_b32 v253,v66 offset:62848\nv_pk_fma_f32 v[70:71],v[58:59],v[124:125],v[124:125] op_sel_hi:[1,0,0]\nv_max3_f32 v112,|v70|,|v71|,v112\n" \
"v_cvt_pk_f16_f32 v70,v70,v71\nds_write_b32 v254,v70 offset:62848\ns_waitcnt lgkmcnt(0)\ns_barrier\n" \
"ds_read_b64 v[96:97],v234 offset:62848\nds_read_b64 v[98:99],v235 offset:62848\nds_read_b64 v[100:101],v236 offset:62848\nds_read_b64 v[102:103],v237 offset:62848\n" \
"ds_read_b64 v[104:105],v238 offset:62848\nds_read_b64 v[106:107],v239 offset:62848\nds_read_b64 v[108:109],v240 offset:62848\nds_read_b64 v[110:111],v241 offset:62848\n" \
"s_waitcnt lgkmcnt(0)\ns_branch .LbxN_%=\n.LsrN_%=:\ns_sleep 2\n" \
"buffer_load_dwordx4 v[104:107],v1,s[36:39],s7 offen sc1\nbuffer_load_dwordx4 v[108:111],v1,s[36:39],s7 offen offset:1024 sc1\ns_memrealtime s[96:97]\ns_waitcnt vmcnt(0) lgkmcnt(0)\n" \
"s_sub_u32 s96,s96,s30\ns_cmp_gt_u32 s96,0x4c4b40\ns_cbranch_scc1 .LbrN_%=\nv_xor_b32 v112,s29,v105\n" \
"v_xor_b32 v113,s29,v107\nv_xor_b32 v114,s29,v109\nv_xor_b32 v115,s29,v111\nv_or3_b32 v112,v112,v113,v114\n" \
"v_xor_b32 v113,s28,v104\nv_xor_b32 v114,s28,v106\nv_or_b32 v112,v112,v115\nv_xor_b32 v115,s28,v108\n" \
"v_lshl_or_b32 v112,v112,8,v113\nv_xor_b32 v113,s28,v110\nv_or3_b32 v112,v112,v114,v115\nv_or_b32 v112,v112,v113\n" \
"v_cmp_ne_u32 vcc,0,v112\ns_nop 0\ns_cbranch_vccnz .LsrN_%=\ns_branch .LbrN_%=\n" \
".LsnN_%=:\ns_sleep 2\nbuffer_load_dwordx4 v[80:83],v120,s[36:39],s5 offen sc1\nbuffer_load_dwordx4 v[84:87],v120,s[36:39],s5 offen offset:2304 sc1\n" \
"buffer_load_dwordx4 v[88:91],v120,s[36:39],s6 offen sc1\nbuffer_load_dwordx4 v[92:95],v120,s[36:39],s6 offen offset:2304 sc1\ns_memrealtime s[96:97]\ns_waitcnt vmcnt(0) lgkmcnt(0)\n" \
"s_sub_u32 s96,s96,s30\ns_cmp_gt_u32 s96,0x4c4b40\ns_cbranch_scc1 .LbnN_%=\nv_cmp_ne_u64 vcc,s[28:29],v[80:81]\n" \
"v_cmp_ne_u64 s[24:25],s[28:29],v[84:85]\nv_cmp_ne_u64 s[26:27],s[28:29],v[88:89]\ns_or_b64 vcc,vcc,s[24:25]\nv_cmp_ne_u64 s[24:25],s[28:29],v[92:93]\n" \
"s_or_b64 vcc,vcc,s[26:27]\ns_or_b64 vcc,vcc,s[24:25]\ns_cbranch_vccnz .LsnN_%=\ns_branch .LbnN_%=\n" \
".LsuN_%=:\nv_and_b32 v125,1,v112\nv_cmpx_eq_u32 vcc,0,v125\nbuffer_load_dwordx4 v[112:115],v3,s[36:39],s16 offen sc1\n" \
"s_mov_b64 exec,-1\nv_and_b32 v125,1,v116\nv_cmpx_eq_u32 vcc,0,v125\nbuffer_load_dwordx4 v[116:119],v3,s[36:39],s17 offen sc1\n" \
"s_mov_b64 exec,-1\nv_and_b32 v125,1,v120\nv_cmpx_eq_u32 vcc,0,v125\nbuffer_load_dwordx4 v[120:123],v124,s[36:39],s18 offen sc1\n" \
"s_mov_b64 exec,-1\ns_memrealtime s[96:97]\ns_waitcnt vmcnt(0) lgkmcnt(0)\ns_sub_u32 s96,s96,s30\n" \
"s_cmp_gt_u32 s96,0x4c4b40\ns_cbranch_scc1 .LbuN_%=\nv_and_b32 v125,v112,v116\nv_bitop3_b32 v125,v125,v120,1 bitop3:0x80\n" \
"v_cmp_eq_u32 vcc,0,v125\ns_nop 0\ns_cbranch_vccnz .LsuN_%=\ns_branch .LbuN_%=\n" \
".LsmC_%=:\ns_sleep 2\nbuffer_load_dwordx4 v[48:51],v2,s[36:39],s73 offen sc1\nbuffer_load_dwordx4 v[60:63],v2,s[36:39],s75 offen sc1\n" \
"buffer_load_dwordx4 v[52:55],v2,s[36:39],s74 offen sc1\nbuffer_load_dwordx4 v[64:67],v2,s[36:39],s76 offen sc1\nbuffer_load_dwordx4 v[56:59],v1,s[36:39],s77 offen sc1\nbuffer_load_dwordx4 v[68:71],v1,s[36:39],s78 offen sc1\n" \
"s_memrealtime s[96:97]\ns_waitcnt vmcnt(0) lgkmcnt(0)\ns_sub_u32 s96,s96,s30\ns_cmp_gt_u32 s96,0x4c4b40\n" \
"s_cbranch_scc1 .LbmC_%=\nv_cmp_ne_u64 vcc,s[28:29],v[48:49]\nv_cmp_ne_u64 s[84:85],s[28:29],v[52:53]\nv_cmp_ne_u64 s[82:83],s[28:29],v[56:57]\n" \
"s_or_b64 vcc,vcc,s[84:85]\nv_cmp_ne_u64 s[84:85],s[28:29],v[60:61]\ns_or_b64 vcc,vcc,s[82:83]\nv_cmp_ne_u64 s[82:83],s[28:29],v[64:65]\n" \
"s_or_b64 vcc,vcc,s[84:85]\nv_cmp_ne_u64 s[84:85],s[28:29],v[68:69]\ns_or_b64 vcc,vcc,s[82:83]\ns_or_b64 vcc,vcc,s[84:85]\n" \
"s_cbranch_vccnz .LsmC_%=\ns_branch .LbmC_%=\n.LsxC_%=:\ns_waitcnt lgkmcnt(0)\n" \
"v_mul_f32 v74,0.5,v74\nv_pk_add_f32 v[124:125],v[54:55],v[74:75] op_sel_hi:[1,0]\nv_pk_add_f32 v[126:127],v[58:59],1.0 op_sel_hi:[1,0]\nv_max3_f32 v112,|v50|,|v51|,|v124|\n" \
"v_max3_f32 v112,|v125|,|v126|,v112\nv_max_f32 v112,|v127|,v112\ns_nop 1\nv_max_u32_dpp v112,v112,v112 quad_perm:[1,0,3,2] row_mask:0xf bank_mask:0xf bound_ctrl:1\n" \
"s_nop 1\nv_max_u32_dpp v112,v112,v112 quad_perm:[2,3,0,1] row_mask:0xf bank_mask:0xf bound_ctrl:1\ns_nop 1\nv_max_u32_dpp v112,v112,v112 row_half_mirror row_mask:0xf bank_mask:0xf bound_ctrl:1\n" \
"s_nop 1\nv_max_u32_dpp v112,v112,v112 row_mirror row_mask:0xf bank_mask:0xf bound_ctrl:1\ns_nop 0\nv_readlane_b32 s82,v112,0\n" \
"v_readlane_b32 s100,v112,16\nv_readlane_b32 s101,v112,32\nv_readlane_b32 s31,v112,48\ns_max_u32 s100,s100,s101\n" \
"s_max_u32 s82,s82,s31\ns_max_u32 s82,s82,s100\nv_mov_b32 v113,s82\nds_write_b32 v251,v113 offset:16\n" \
"s_waitcnt lgkmcnt(0)\ns_barrier\nds_read_b128 v[124:127],v114 offset:16\ns_waitcnt lgkmcnt(0)\n" \
"v_max3_u32 v124,v124,v125,v126\nv_max_u32 v124,v124,v127\ns_nop 0\nv_readfirstlane_b32 s93,v124\n" \
"s_nop 0\ns_lshr_b32 s93,s93,23\ns_max_u32 s93,s93,64\ns_min_u32 s93,s93,254\n" \
"s_sub_u32 s82,256,s93\ns_lshl_b32 s82,s82,23\nv_mov_b32 v124,s82\nv_mul_f32 v74,v74,v124\n" \
"v_pk_mul_f32 v[62:63],v[50:51],v[124:125] op_sel_hi:[1,0]\nv_max3_f32 v112,|v62|,|v63|,0\nv_cvt_pk_f16_f32 v62,v62,v63\nds_write_b32 v252,v62 offset:62848\n" \
"v_pk_fma_f32 v[66:67],v[54:55],v[124:125],v[74:75] op_sel_hi:[1,0,0]\nv_max3_f32 v112,|v66|,|v67|,v112\nv_cvt_pk_f16_f32 v66,v66,v67\nds_write_b32 v253,v66 offset:62848\n" \
"v_pk_fma_f32 v[70:71],v[58:59],v[124:125],v[124:125] op_sel_hi:[1,0,0]\nv_max3_f32 v112,|v70|,|v71|,v112\nv_cvt_pk_f16_f32 v70,v70,v71\nds_write_b32 v254,v70 offset:62848\n" \
"s_waitcnt lgkmcnt(0)\ns_barrier\nds_read_b64 v[96:97],v234 offset:62848\nds_read_b64 v[98:99],v235 offset:62848\n" \
"ds_read_b64 v[100:101],v236 offset:62848\nds_read_b64 v[102:103],v237 offset:62848\nds_read_b64 v[104:105],v238 offset:62848\nds_read_b64 v[106:107],v239 offset:62848\n" \
"ds_read_b64 v[108:109],v240 offset:62848\nds_read_b64 v[110:111],v241 offset:62848\ns_waitcnt lgkmcnt(0)\ns_branch .LbxC_%=\n" \
".Lsw_%=:\nbuffer_store_dwordx4 v[80:83],v3,s[36:39],s73 offen sc1\nbuffer_store_dwordx4 v[84:87],v3,s[36:39],s74 offen sc1\nbuffer_store_dwordx4 v[88:91],v113,s[36:39],s75 offen sc1\n" \
".Lpw_%=:\nbuffer_load_dwordx4 v[104:107],v1,s[36:39],s7 offen sc1\nbuffer_load_dwordx4 v[108:111],v1,s[36:39],s7 offen offset:1024 sc1\ns_memrealtime s[96:97]\n" \
"s_waitcnt vmcnt(0) lgkmcnt(0)\ns_sub_u32 s96,s96,s30\ns_cmp_gt_u32 s96,0x4c4b40\ns_cbranch_scc1 .Lqw_%=\n" \
"v_xor_b32 v116,s29,v105\nv_xor_b32 v117,s29,v107\nv_xor_b32 v118,s29,v109\nv_xor_b32 v119,s29,v111\n" \
"v_or3_b32 v116,v116,v117,v118\nv_xor_b32 v117,s28,v104\nv_xor_b32 v118,s28,v106\nv_or_b32 v116,v116,v119\n" \
"v_xor_b32 v119,s28,v108\nv_lshl_or_b32 v116,v116,8,v117\nv_xor_b32 v117,s28,v110\nv_or3_b32 v116,v116,v118,v119\n" \
"v_or_b32 v116,v116,v117\nv_cmp_ne_u32 vcc,0,v116\ns_nop 0\ns_cbranch_vccnz .Lpw_%=\n" \
".Lqw_%=:\ns_add_u32 m0,s88,16384\ns_nop 0\nbuffer_load_dwordx4 v124,s[36:39],s66 offen sc1 lds\n" \
"s_add_u32 m0,s88,20480\ns_nop 0\nbuffer_load_dwordx4 v125,s[36:39],s67 offen sc1 lds\ns_add_u32 m0,s88,24576\n" \
"s_nop 0\nbuffer_load_dwordx4 v124,s[36:39],s68 offen sc1 lds\ns_add_u32 m0,s88,28672\ns_nop 0\n" \
"buffer_load_dwordx4 v125,s[36:39],s69 offen sc1 lds\ns_waitcnt vmcnt(0)\ns_branch .Lbw_%=\n.Lsz_%=:\n" \
"v_subrev_u32 v124,16384,v80\nv_lshrrev_b32 v124,6,v124\nv_mul_u32_u24 v125,9344,v124\nv_lshl_add_u32 v124,v84,2,v125\n" \
"v_sub_u32 v125,2382780,v124\ns_mov_b32 s79,2925632\nbuffer_load_dword v64,v124,s[36:39],s79 offen sc1\nv_subrev_u32 v126,0,v125\n" \
"s_mov_b32 s79,2925632\nbuffer_load_dword v72,v126,s[36:39],s79 offen sc1\ns_mov_b32 s79,3075136\nbuffer_load_dword v65,v124,s[36:39],s79 offen sc1\n" \
"v_subrev_u32 v126,149504,v125\ns_mov_b32 s79,2925632\nbuffer_load_dword v73,v126,s[36:39],s79 offen sc1\ns_mov_b32 s79,3224640\n" \
"buffer_load_dword v66,v124,s[36:39],s79 offen sc1\nv_subrev_u32 v126,299008,v125\ns_mov_b32 s79,2925632\nbuffer_load_dword v74,v126,s[36:39],s79 offen sc1\n" \
"s_mov_b32 s79,3374144\nbuffer_load_dword v67,v124,s[36:39],s79 offen sc1\nv_subrev_u32 v126,448512,v125\ns_mov_b32 s79,2925632\n" \
"buffer_load_dword v75,v126,s[36:39],s79 offen sc1\ns_mov_b32 s79,2944320\nbuffer_load_dword v68,v124,s[36:39],s79 offen sc1\nv_subrev_u32 v126,18688,v125\n" \
"s_mov_b32 s79,2925632\nbuffer_load_dword v76,v126,s[36:39],s79 offen sc1\ns_mov_b32 s79,3093824\nbuffer_load_dword v69,v124,s[36:39],s79 offen sc1\n" \
"v_subrev_u32 v126,168192,v125\ns_mov_b32 s79,2925632\nbuffer_load_dword v77,v126,s[36:39],s79 offen sc1\ns_mov_b32 s79,3243328\n" \
"buffer_load_dword v70,v124,s[36:39],s79 offen sc1\nv_subrev_u32 v126,317696,v125\ns_mov_b32 s79,2925632\nbuffer_load_dword v78,v126,s[36:39],s79 offen sc1\n" \
"s_mov_b32 s79,3392832\nbuffer_load_dword v71,v124,s[36:39],s79 offen sc1\nv_subrev_u32 v126,467200,v125\ns_mov_b32 s79,2925632\n" \
"buffer_load_dword v79,v126,s[36:39],s79 offen sc1\ns_waitcnt vmcnt(0)\nv_lshlrev_b32 v124,16,v72\nv_and_b32 v125,0xffff0000,v72\n" \
"v_add_f32 v16,v16,v124\nv_sub_f32 v20,v20,v125\nv_lshlrev_b32 v124,16,v64\nv_and_b32 v125,0xffff0000,v64\n" \
"v_add_f32 v32,v32,v124\nv_sub_f32 v36,v36,v125\nv_lshlrev_b32 v124,16,v73\nv_and_b32 v125,0xffff0000,v73\n" \
"v_add_f32 v17,v17,v124\nv_sub_f32 v21,v21,v125\nv_lshlrev_b32 v124,16,v65\nv_and_b32 v125,0xffff0000,v65\n" \
"v_add_f32 v33,v33,v124\nv_sub_f32 v37,v37,v125\nv_lshlrev_b32 v124,16,v74\nv_and_b32 v125,0xffff0000,v74\n" \
"v_add_f32 v18,v18,v124\nv_sub_f32 v22,v22,v125\nv_lshlrev_b32 v124,16,v66\nv_and_b32 v125,0xffff0000,v66\n" \
"v_add_f32 v34,v34,v124\nv_sub_f32 v38,v38,v125\nv_lshlrev_b32 v124,16,v75\nv_and_b32 v125,0xffff0000,v75\n" \
"v_add_f32 v19,v19,v124\nv_sub_f32 v23,v23,v125\nv_lshlrev_b32 v124,16,v67\nv_and_b32 v125,0xffff0000,v67\n" \
"v_add_f32 v35,v35,v124\nv_sub_f32 v39,v39,v125\nv_lshlrev_b32 v124,16,v76\nv_and_b32 v125,0xffff0000,v76\n" \
"v_add_f32 v40,v40,v124\nv_sub_f32 v44,v44,v125\nv_lshlrev_b32 v124,16,v68\nv_and_b32 v125,0xffff0000,v68\n" \
"v_add_f32 v56,v56,v124\nv_sub_f32 v60,v60,v125\nv_lshlrev_b32 v124,16,v77\nv_and_b32 v125,0xffff0000,v77\n" \
"v_add_f32 v41,v41,v124\nv_sub_f32 v45,v45,v125\nv_lshlrev_b32 v124,16,v69\nv_and_b32 v125,0xffff0000,v69\n" \
"v_add_f32 v57,v57,v124\nv_sub_f32 v61,v61,v125\nv_lshlrev_b32 v124,16,v78\nv_and_b32 v125,0xffff0000,v78\n" \
"v_add_f32 v42,v42,v124\nv_sub_f32 v46,v46,v125\nv_lshlrev_b32 v124,16,v70\nv_and_b32 v125,0xffff0000,v70\n" \
"v_add_f32 v58,v58,v124\nv_sub_f32 v62,v62,v125\nv_lshlrev_b32 v124,16,v79\nv_and_b32 v125,0xffff0000,v79\n" \
"v_add_f32 v43,v43,v124\nv_sub_f32 v47,v47,v125\nv_lshlrev_b32 v124,16,v71\nv_and_b32 v125,0xffff0000,v71\n" \
"v_add_f32 v59,v59,v124\nv_sub_f32 v63,v63,v125\ns_branch .Lbz_%=\n.Lst_%=:\n" \
"s_sleep 1\nbuffer_load_dword v96,v120,s[36:39],s80 offen sc1\nbuffer_load_dword v97,v121,s[36:39],s80 offen sc1\nbuffer_load_dword v98,v122,s[36:39],s80 offen sc1\n" \
"buffer_load_dword v99,v123,s[36:39],s80 offen sc1\nbuffer_load_dword v100,v120,s[36:39],s81 offen sc1\nbuffer_load_dword v101,v121,s[36:39],s81 offen sc1\nbuffer_load_dword v102,v122,s[36:39],s81 offen sc1\n" \
"buffer_load_dword v103,v123,s[36:39],s81 offen sc1\nbuffer_load_dword v104,v120,s[36:39],s82 offen sc1\nbuffer_load_dword v105,v121,s[36:39],s82 offen sc1\nbuffer_load_dword v106,v122,s[36:39],s82 offen sc1\n" \
"buffer_load_dword v107,v123,s[36:39],s82 offen sc1\nbuffer_load_dword v108,v120,s[36:39],s83 offen sc1\nbuffer_load_dword v109,v121,s[36:39],s83 offen sc1\nbuffer_load_dword v110,v122,s[36:39],s83 offen sc1\n" \
"buffer_load_dword v111,v123,s[36:39],s83 offen sc1\ns_memrealtime s[96:97]\ns_waitcnt vmcnt(0) lgkmcnt(0)\ns_sub_u32 s96,s96,s30\n" \
"s_cmp_gt_u32 s96,0x4c4b40\ns_cbranch_scc1 .Lbt_%=\nv_or3_b32 v124,v96,v97,v98\nv_or3_b32 v124,v124,v99,v100\n" \
"v_or3_b32 v124,v124,v101,v102\nv_or3_b32 v124,v124,v103,v104\nv_or3_b32 v124,v124,v105,v106\nv_or3_b32 v124,v124,v107,v108\n" \
"v_or3_b32 v124,v124,v109,v110\nv_or_b32 v124,v124,v111\nv_and_b32 v124,1,v124\nv_cmp_ne_u32 vcc,0,v124\n" \
"s_nop 0\ns_and_b64 vcc,vcc,s[78:79]\ns_cbranch_vccnz .Lst_%=\ns_branch .Lbt_%=\n" \
".LsnC_%=:\ns_sleep 2\nbuffer_load_dwordx4 v[16:19],v32,s[36:39],s5 offen sc1\nbuffer_load_dwordx4 v[20:23],v32,s[36:39],s5 offen offset:2304 sc1\n" \
"buffer_load_dwordx4 v[24:27],v32,s[36:39],s6 offen sc1\nbuffer_load_dwordx4 v[28:31],v32,s[36:39],s6 offen offset:2304 sc1\ns_memrealtime s[96:97]\ns_waitcnt vmcnt(0) lgkmcnt(0)\n" \
"s_sub_u32 s96,s96,s30\ns_cmp_gt_u32 s96,0x4c4b40\ns_cbranch_scc1 .LbnC_%=\nv_cmp_ne_u64 vcc,s[28:29],v[16:17]\n" \
"v_cmp_ne_u64 s[24:25],s[28:29],v[20:21]\nv_cmp_ne_u64 s[26:27],s[28:29],v[24:25]\ns_or_b64 vcc,vcc,s[24:25]\nv_cmp_ne_u64 s[24:25],s[28:29],v[28:29]\n" \
"s_or_b64 vcc,vcc,s[26:27]\ns_or_b64 vcc,vcc,s[24:25]\ns_cbranch_vccnz .LsnC_%=\ns_branch .LbnC_%=\n" \
".LsuC_%=:\nv_and_b32 v125,1,v112\nv_cmpx_eq_u32 vcc,0,v125\nbuffer_load_dwordx4 v[112:115],v3,s[36:39],s16 offen sc1\n" \
"s_mov_b64 exec,-1\nv_and_b32 v125,1,v116\nv_cmpx_eq_u32 vcc,0,v125\nbuffer_load_dwordx4 v[116:119],v3,s[36:39],s17 offen sc1\n" \
"s_mov_b64 exec,-1\nv_and_b32 v125,1,v120\nv_cmpx_eq_u32 vcc,0,v125\nbuffer_load_dwordx4 v[120:123],v124,s[36:39],s18 offen sc1\n" \
"s_mov_b64 exec,-1\ns_memrealtime s[96:97]\ns_waitcnt vmcnt(0) lgkmcnt(0)\ns_sub_u32 s96,s96,s30\n" \
"s_cmp_gt_u32 s96,0x4c4b40\ns_cbranch_scc1 .LbuC_%=\nv_and_b32 v125,v112,v116\nv_bitop3_b32 v125,v125,v120,1 bitop3:0x80\n" \
"v_cmp_eq_u32 vcc,0,v125\ns_nop 0\ns_cbranch_vccnz .LsuC_%=\ns_branch .LbuC_%=\n" \
".Lend_%=:\n"
#define ASM_CLOB "v1", "v2", "v3", "v4", "v5", "v6", "v7", "v8", "v9", "v10", "v11", "v12", "v13", "v14", "v15", "v16", "v17", "v18", "v19", "v20", "v21", "v22", "v23", "v24", "v25", "v26", "v27", "v28", "v29", "v30", "v31", "v32", "v33", "v34", "v35", "v36", "v37", "v38", "v39", "v40", "v41", "v42", "v43", "v44", "v45", "v46", "v47", "v48", "v49", "v50", "v51", "v52", "v53", "v54", "v55", "v56", "v57", "v58", "v59", "v60", "v61", "v62", "v63", "v64", "v65", "v66", "v67", "v68", "v69", "v70", "v71", "v72", "v73", "v74", "v75", "v76", "v77", "v78", "v79", "v80", "v81", "v82", "v83", "v84", "v85", "v86", "v87", "v88", "v89", "v90", "v91", "v92", "v93", "v94", "v95", "v96", "v97", "v98", "v99", "v100", "v101", "v102", "v103", "v104", "v105", "v106", "v107", "v108", "v109", "v110", "v111", "v112", "v113", "v114", "v115", "v116", "v117", "v118", "v119", "v120", "v121", "v122", "v123", "v124", "v125", "v126", "v127", "v128", "v129", "v130", "v131", "v132", "v133", "v134", "v135", "v136", "v137", "v138", "v139", "v140", "v141", "v142", "v143", "v144", "v145", "v146", "v147", "v148", "v149", "v150", "v151", "v152", "v153", "v154", "v155", "v156", "v157", "v158", "v159", "v160", "v161", "v162", "v163", "v164", "v165", "v166", "v167", "v168", "v169", "v170", "v171", "v172", "v173", "v174", "v175", "v176", "v177", "v178", "v179", "v180", "v181", "v182", "v183", "v184", "v185", "v186", "v187", "v188", "v189", "v190", "v191", "v192", "v193", "v194", "v195", "v196", "v197", "v198", "v199", "v200", "v201", "v202", "v203", "v204", "v205", "v206", "v207", "v208", "v209", "v210", "v211", "v212", "v213", "v214", "v215", "v216", "v217", "v218", "v219", "v220", "v221", "v222", "v223", "v224", "v225", "v226", "v227", "v228", "v229", "v230", "v231", "v232", "v233", "v234", "v235", "v236", "v237", "v238", "v239", "v240", "v241", "v242", "v243", "v244", "v245", "v246", "v247", "v248", "v249", "v250", "v251", "v252", "v253", "v254", "v255", "a0", "a1", "a2", "a3", "a4", "a5", "a6", "a7", "a8", "a9", "a10", "a11", "a12", "a13", "a14", "a15", "a16", "a17", "a18", "a19", "a20", "a21", "a22", "a23", "a24", "a25", "a26", "a27", "a28", "a29", "a30", "a31", "a32", "a33", "a34", "a35", "a36", "a37", "a38", "a39", "a40", "a41", "a42", "a43", "a44", "a45", "a46", "a47", "a48", "a49", "a50", "a51", "a52", "a53", "a54", "a55", "a56", "a57", "a58", "a59", "a60", "a61", "a62", "a63", "a64", "a65", "a66", "a67", "a68", "a69", "a70", "a71", "a72", "a73", "a74", "a75", "a76", "a77", "a78", "a79", "a80", "a81", "a82", "a83", "a84", "a85", "a86", "a87", "a88", "a89", "a90", "a91", "a92", "a93", "a94", "a95", "a96", "a97", "a98", "a99", "a100", "a101", "a102", "a103", "a104", "a105", "a106", "a107", "a108", "a109", "a110", "a111", "a112", "a113", "a114", "a115", "a116", "a117", "a118", "a119", "a120", "a121", "a122", "a123", "a124", "a125", "a126", "a127", "a128", "a129", "a130", "a131", "a132", "a133", "a134", "a135", "a136", "a137", "a138", "a139", "a140", "a141", "a142", "a143", "a144", "a145", "a146", "a147", "a148", "a149", "a150", "a151", "a152", "a153", "a154", "a155", "a156", "a157", "a158", "a159", "a160", "a161", "a162", "a163", "a164", "a165", "a166", "a167", "a168", "a169", "a170", "a171", "a172", "a173", "a174", "a175", "a176", "a177", "a178", "a179", "a180", "a181", "a182", "a183", "a184", "a185", "a186", "a187", "a188", "a189", "a190", "a191", "a192", "a193", "a194", "a195", "a196", "a197", "a198", "a199", "a200", "a201", "a202", "a203", "a204", "a205", "a206", "a207", "a208", "a209", "a210", "a211", "a212", "a213", "a214", "a215", "a216", "a217", "a218", "a219", "a220", "a221", "a222", "a223", "a224", "a225", "a226", "a227", "a228", "a229", "a230", "a231", "a232", "a233", "a234", "a235", "a236", "a237", "a238", "a239", "a240", "a241", "a242", "a243", "a244", "a245", "a246", "a247", "a248", "a249", "a250", "a251", "a252", "a253", "a254", "a255", "s3", "s6", "s7", "s8", "s9", "s10", "s11", "s12", "s13", "s14", "s15", "s16", "s17", "s18", "s19", "s20", "s21", "s22", "s23", "s24", "s25", "s26", "s27", "s28", "s29", "s30", "s31", "s32", "s33", "s34", "s35", "s36", "s37", "s38", "s39", "s40", "s41", "s42", "s43", "s44", "s45", "s46", "s47", "s48", "s49", "s50", "s51", "s52", "s53", "s54", "s55", "s56", "s57", "s58", "s59", "s60", "s61", "s62", "s63", "s64", "s65", "s66", "s67", "s68", "s69", "s70", "s71", "s72", "s73", "s74", "s75", "s76", "s77", "s78", "s79", "s80", "s81", "s82", "s83", "s84", "s85", "s86", "s87", "s88", "s89", "s90", "s91", "s92", "s93", "s94", "s95", "s96", "s97", "s98", "s99", "s100", "s101"
extern "C" __global__ __launch_bounds__(256, 1) void adm(
    const u16* x, const u16* wr, const u16* wi, const u16* w1, const u16* cvec, const u16* wmod, char* wsb, const u16* b1, const u16* b2, const u16* w2, u16* outp) {
  unsigned long long ka = (unsigned long long)__builtin_amdgcn_kernarg_segment_ptr(), tab = (unsigned long long)TAB;
  unsigned bid = blockIdx.x, tid = threadIdx.x;
  asm volatile(ASM_ALL : "+{s[0:1]}"(ka), "+{s2}"(bid), "+{s[4:5]}"(tab), "+{v0}"(tid) : : "vcc", "scc", "memory", ASM_CLOB);
}

extern "C" void kernel_launch(void* const* d_in, const int* in_sizes, int n_in, void* d_out, int out_size, void* d_ws,
                              size_t ws_size, hipStream_t stream) {
  (void)hipFuncSetAttribute((const void*)adm, hipFuncAttributeMaxDynamicSharedMemorySize, 163840);
  adm<<<256, 256, 163840, stream>>>((const u16*)d_in[0], (const u16*)d_in[1], (const u16*)d_in[2], (const u16*)d_in[4], (const u16*)d_in[8], (const u16*)d_in[9],
                                    (char*)d_ws, (const u16*)d_in[5], (const u16*)d_in[7], (const u16*)d_in[6], (u16*)d_out);
}
```

```cpp
#include <hip/hip_runtime.h>
typedef unsigned short u16;
static __device__ const unsigned __attribute__((aligned(128))) TAB[5632] = {
13312,13312,13312,13312,13312,0x2e1f3364,0x31a831a8,0x33642e1f,13312,0x31a831a8,0x34000000,0x31a8b1a8,13312,0x33642e1f,0x31a8b1a8,0xae1fb364,
13312,0x34000000,46080,0xb4000000,13312,0x3364ae1f,0xb1a8b1a8,0xae1f3364,13312,0x31a8b1a8,0xb4000000,0x31a831a8,13312,0x2e1fb364,0xb1a831a8,0x3364ae1f,
13312,46080,13312,46080,13312,0xae1fb364,0x31a831a8,0xb364ae1f,13312,0xb1a8b1a8,0x34000000,0xb1a831a8,13312,0xb364ae1f,0x31a8b1a8,0x2e1f3364,
13312,0xb4000000,46080,0x34000000,13312,0xb3642e1f,0xb1a8b1a8,0x2e1fb364,13312,0xb1a831a8,0xb4000000,0xb1a8b1a8,13312,0xae1f3364,0xb1a831a8,0xb3642e1f,
13312,13312,13312,13312,0x34000000,0x3364ae1f,0x31a8b1a8,0x2e1fb364,46080,0xb1a8b1a8,0xb4000000,0xb1a831a8,0xb4000000,0xae1f3364,0x31a831a8,0x3364ae1f,
13312,0x34000000,46080,0xb4000000,0x34000000,0xae1fb364,0xb1a831a8,0x33642e1f,46080,0xb1a831a8,0x34000000,0xb1a8b1a8,0xb4000000,0x33642e1f,0xb1a8b1a8,0x2e1f3364,
13312,46080,13312,46080,0x34000000,0xb3642e1f,0x31a8b1a8,0xae1f3364,46080,0x31a831a8,0xb4000000,0x31a8b1a8,0xb4000000,0x2e1fb364,0x31a831a8,0xb3642e1f,
13312,0xb4000000,46080,0x34000000,0x34000000,0x2e1f3364,0xb1a831a8,0xb364ae1f,46080,0x31a8b1a8,0x34000000,0x31a831a8,0xb4000000,0xb364ae1f,0xb1a8b1a8,0xae1fb364,
13312,13312,13312,13312,46080,0xae1fb364,0xb1a8b1a8,0xb364ae1f,13312,0x31a831a8,0x34000000,0x31a8b1a8,46080,0xb364ae1f,0xb1a831a8,0x2e1f3364,
13312,0x34000000,46080,0xb4000000,46080,0xb3642e1f,0x31a831a8,0x2e1fb364,13312,0x31a8b1a8,0xb4000000,0x31a831a8,46080,0xae1f3364,0x31a8b1a8,0xb3642e1f,
13312,46080,13312,46080,46080,0x2e1f3364,0xb1a8b1a8,0x33642e1f,13312,0xb1a8b1a8,0x34000000,0xb1a831a8,46080,0x33642e1f,0xb1a831a8,0xae1fb364,
13312,0xb4000000,46080,0x34000000,46080,0x3364ae1f,0x31a831a8,0xae1f3364,13312,0xb1a831a8,0xb4000000,0xb1a8b1a8,46080,0x2e1fb364,0x31a8b1a8,0x3364ae1f,
13312,13312,13312,13312,0xb4000000,0xb3642e1f,0xb1a831a8,0xae1f3364,46080,0xb1a8b1a8,0xb4000000,0xb1a831a8,0x34000000,0x2e1fb364,0xb1a8b1a8,0xb3642e1f,
13312,0x34000000,46080,0xb4000000,0xb4000000,0x2e1f3364,0x31a8b1a8,0xb364ae1f,46080,0xb1a831a8,0x34000000,0xb1a8b1a8,0x34000000,0xb364ae1f,0x31a831a8,0xae1fb364,
13312,46080,13312,46080,0xb4000000,0x3364ae1f,0xb1a831a8,0x2e1fb364,46080,0x31a831a8,0xb4000000,0x31a8b1a8,0x34000000,0xae1f3364,0xb1a8b1a8,0x3364ae1f,
13312,0xb4000000,46080,0x34000000,0xb4000000,0xae1fb364,0x31a8b1a8,0x33642e1f,46080,0x31a8b1a8,0x34000000,0x31a831a8,0x34000000,0x33642e1f,0x31a831a8,0x2e1f3364,
0x34000000,0x34000000,0x34000000,0x34000000,0x34000000,0x3364ae1f,0x31a8b1a8,0x2e1fb364,0x34000000,0x31a8b1a8,46080,0xb1a8b1a8,0x34000000,0x2e1fb364,0xb1a8b1a8,0xb3642e1f,
0x34000000,46080,0xb4000000,13312,0x34000000,0xae1fb364,0xb1a831a8,0x33642e1f,0x34000000,0xb1a8b1a8,13312,0x31a8b1a8,0x34000000,0xb364ae1f,0x31a831a8,0xae1fb364,
0x34000000,0xb4000000,0x34000000,0xb4000000,0x34000000,0xb3642e1f,0x31a8b1a8,0xae1f3364,0x34000000,0xb1a831a8,46080,0x31a831a8,0x34000000,0xae1f3364,0xb1a8b1a8,0x3364ae1f,
0x34000000,13312,0xb4000000,46080,0x34000000,0x2e1f3364,0xb1a831a8,0xb364ae1f,0x34000000,0x31a831a8,13312,0xb1a831a8,0x34000000,0x33642e1f,0x31a831a8,0x2e1f3364,
0x34000000,0x34000000,0x34000000,0x34000000,46080,0xae1fb364,0xb1a8b1a8,0xb364ae1f,0xb4000000,0xb1a831a8,13312,0x31a831a8,13312,0x33642e1f,0x31a8b1a8,0xae1fb364,
0x34000000,46080,0xb4000000,13312,46080,0xb3642e1f,0x31a831a8,0x2e1fb364,0xb4000000,0x31a831a8,46080,0xb1a831a8,13312,0x2e1fb364,0xb1a831a8,0x3364ae1f,
0x34000000,0xb4000000,0x34000000,0xb4000000,46080,0x2e1f3364,0xb1a8b1a8,0x33642e1f,0xb4000000,0x31a8b1a8,13312,0xb1a8b1a8,13312,0xb364ae1f,0x31a8b1a8,0x2e1f3364,
0x34000000,13312,0xb4000000,46080,46080,0x3364ae1f,0x31a831a8,0xae1f3364,0xb4000000,0xb1a8b1a8,46080,0x31a8b1a8,13312,0xae1f3364,0xb1a831a8,0xb3642e1f,
0x34000000,0x34000000,0x34000000,0x34000000,0xb4000000,0xb3642e1f,0xb1a831a8,0xae1f3364,0x34000000,0x31a8b1a8,46080,0xb1a8b1a8,0xb4000000,0xae1f3364,0x31a831a8,0x3364ae1f,
0x34000000,46080,0xb4000000,13312,0xb4000000,0x2e1f3364,0x31a8b1a8,0xb364ae1f,0x34000000,0xb1a8b1a8,13312,0x31a8b1a8,0xb4000000,0x33642e1f,0xb1a8b1a8,0x2e1f3364,
0x34000000,0xb4000000,0x34000000,0xb4000000,0xb4000000,0x3364ae1f,0xb1a831a8,0x2e1fb364,0x34000000,0xb1a831a8,46080,0x31a831a8,0xb4000000,0x2e1fb364,0x31a831a8,0xb3642e1f,
0x34000000,13312,0xb4000000,46080,0xb4000000,0xae1fb364,0x31a8b1a8,0x33642e1f,0x34000000,0x31a831a8,13312,0xb1a831a8,0xb4000000,0xb364ae1f,0xb1a8b1a8,0xae1fb364,
0x34000000,0x34000000,0x34000000,0x34000000,13312,0x2e1f3364,0x31a831a8,0x33642e1f,0xb4000000,0xb1a831a8,13312,0x31a831a8,46080,0xb364ae1f,0xb1a831a8,0x2e1f3364,
0x34000000,46080,0xb4000000,13312,13312,0x3364ae1f,0xb1a8b1a8,0xae1f3364,0xb4000000,0x31a831a8,46080,0xb1a831a8,46080,0xae1f3364,0x31a8b1a8,0xb3642e1f,
0x34000000,0xb4000000,0x34000000,0xb4000000,13312,0xae1fb364,0x31a831a8,0xb364ae1f,0xb4000000,0x31a8b1a8,13312,0xb1a8b1a8,46080,0x33642e1f,0xb1a831a8,0xae1fb364,
0x34000000,13312,0xb4000000,46080,13312,0xb3642e1f,0xb1a8b1a8,0x2e1fb364,0xb4000000,0xb1a8b1a8,46080,0x31a8b1a8,46080,0x2e1fb364,0x31a8b1a8,0x3364ae1f,
13312,13312,13312,0,13312,0x31243221,0x33e1298f,0,13312,0x33e1298f,0x2d79b384,0,0,0,0,0,
13312,0x32eeb000,0xb2eeb000,0,13312,0x2d79b384,0xb1243221,0,13312,0xad79b384,0x31243221,0,0,0,0,0,
13312,0xb2eeb000,0x32eeb000,0,13312,0xb3e1298f,0xad79b384,0,13312,0xb1243221,0xb3e1298f,0,0,0,0,0,
0,0,0,0,0,0,0,0,0,0,0,0,0,0,0,0,
13312,13312,13312,0,0x32eeb000,0x2d79b384,0xad79b384,0,0xb2eeb000,0xb1243221,0x31243221,0,0,0,0,0,
13312,0x32eeb000,0xb2eeb000,0,0x32eeb000,0xb3e1298f,0x33e1298f,0,0xb2eeb000,0x33e1298f,0xb3e1298f,0,0,0,0,0,
13312,0xb2eeb000,0x32eeb000,0,0x32eeb000,0x31243221,0xb1243221,0,0xb2eeb000,0xad79b384,0x2d79b384,0,0,0,0,0,
0,0,0,0,0,0,0,0,0,0,0,0,0,0,0,0,
13312,13312,13312,0,0xb2eeb000,0xb3e1298f,0xb1243221,0,0x32eeb000,0xad79b384,0xb3e1298f,0,0,0,0,0,
13312,0x32eeb000,0xb2eeb000,0,0xb2eeb000,0x31243221,0xad79b384,0,0x32eeb000,0xb1243221,0x2d79b384,0,0,0,0,0,
13312,0xb2eeb000,0x32eeb000,0,0xb2eeb000,0x2d79b384,0x33e1298f,0,0x32eeb000,0x33e1298f,0x31243221,0,0,0,0,0,
0,0,0,0,0,0,0,0,0,0,0,0,0,0,0,0,
0,0,0,0,0,0,0,0,0,0,0,0,0,0,0,0,
0,0,0,0,0,0,0,0,0,0,0,0,0,0,0,0,
0,0,0,0,0,0,0,0,0,0,0,0,0,0,0,0,
0,0,0,0,0,0,0,0,0,0,0,0,0,0,0,0,
0x34000000,0x34000000,0x34000000,0,0x34000000,0x3221b124,0x298fb3e1,0,0x34000000,0x298fb3e1,0xb384ad79,0,0,0,0,0,
0x34000000,0xb000b2ee,0xb00032ee,0,0x34000000,0xb384ad79,0x32213124,0,0x34000000,0xb3842d79,0x3221b124,0,0,0,0,0,
0x34000000,0xb00032ee,0xb000b2ee,0,0x34000000,0x298f33e1,0xb3842d79,0,0x34000000,0x32213124,0x298f33e1,0,0,0,0,0,
0,0,0,0,0,0,0,0,0,0,0,0,0,0,0,0,
0x34000000,0x34000000,0x34000000,0,0xb000b2ee,0xb384ad79,0xb3842d79,0,0xb00032ee,0x32213124,0x3221b124,0,0,0,0,0,
0x34000000,0xb000b2ee,0xb00032ee,0,0xb000b2ee,0x298f33e1,0x298fb3e1,0,0xb00032ee,0x298fb3e1,0x298f33e1,0,0,0,0,0,
0x34000000,0xb00032ee,0xb000b2ee,0,0xb000b2ee,0x3221b124,0x32213124,0,0xb00032ee,0xb3842d79,0xb384ad79,0,0,0,0,0,
0,0,0,0,0,0,0,0,0,0,0,0,0,0,0,0,
0x34000000,0x34000000,0x34000000,0,0xb00032ee,0x298f33e1,0x32213124,0,0xb000b2ee,0xb3842d79,0x298f33e1,0,0,0,0,0,
0x34000000,0xb000b2ee,0xb00032ee,0,0xb00032ee,0x3221b124,0xb3842d79,0,0xb000b2ee,0x32213124,0xb384ad79,0,0,0,0,0,
0x34000000,0xb00032ee,0xb000b2ee,0,0xb00032ee,0xb384ad79,0x298fb3e1,0,0xb000b2ee,0x298fb3e1,0x3221b124,0,0,0,0,0,
0,0,0,0,0,0,0,0,0,0,0,0,0,0,0,0,
0,0,0,0,0,0,0,0,0,0,0,0,0,0,0,0,
0,0,0,0,0,0,0,0,0,0,0,0,0,0,0,0,
0,0,0,0,0,0,0,0,0,0,0,0,0,0,0,0,
0,0,0,0,0,0,0,0,0,0,0,0,0,0,0,0,
13312,0x2a3e33d9,0x2e1f3364,0x307232a7,13312,0x307232a7,0x33642e1f,0x33d9aa3e,13312,0x32a73072,0x3364ae1f,0x2a3eb3d9,13312,0x33d92a3e,0x2e1fb364,0xb2a7b072,
13312,0x33d9aa3e,0xae1fb364,0xb2a73072,13312,0x32a7b072,0xb364ae1f,0x2a3e33d9,13312,0x3072b2a7,0xb3642e1f,0x33d92a3e,13312,0x2a3eb3d9,0xae1f3364,0x3072b2a7,
13312,0xaa3eb3d9,0x2e1f3364,0xb072b2a7,13312,0xb072b2a7,0x33642e1f,0xb3d92a3e,13312,0xb2a7b072,0x3364ae1f,0xaa3e33d9,13312,0xb3d9aa3e,0x2e1fb364,0x32a73072,
13312,0xb3d92a3e,0xae1fb364,0x32a7b072,13312,0xb2a73072,0xb364ae1f,0xaa3eb3d9,13312,0xb07232a7,0xb3642e1f,0xb3d9aa3e,13312,0xaa3e33d9,0xae1f3364,0xb07232a7,
0x31a831a8,0x32a73072,0x33642e1f,0x33d92a3e,0x31a8b1a8,0x2a3eb3d9,0xae1fb364,0xb2a7b072,0xb1a8b1a8,0xb3d92a3e,0xae1f3364,0x307232a7,0xb1a831a8,0x307232a7,0x3364ae1f,0xaa3eb3d9,
0x31a831a8,0x3072b2a7,0xb364ae1f,0xaa3e33d9,0x31a8b1a8,0xb3d9aa3e,0x2e1f3364,0x3072b2a7,0xb1a8b1a8,0x2a3e33d9,0x2e1fb364,0xb2a73072,0xb1a831a8,0x32a7b072,0xb3642e1f,0x33d9aa3e,
0x31a831a8,0xb2a7b072,0x33642e1f,0xb3d9aa3e,0x31a8b1a8,0xaa3e33d9,0xae1fb364,0x32a73072,0xb1a8b1a8,0x33d9aa3e,0xae1f3364,0xb072b2a7,0xb1a831a8,0xb072b2a7,0x3364ae1f,0x2a3e33d9,
0x31a831a8,0xb07232a7,0xb364ae1f,0x2a3eb3d9,0x31a8b1a8,0x33d92a3e,0x2e1f3364,0xb07232a7,0xb1a8b1a8,0xaa3eb3d9,0x2e1fb364,0x32a7b072,0xb1a831a8,0xb2a73072,0xb3642e1f,0xb3d92a3e,
0x34000000,0x33d9aa3e,0x3364ae1f,0x32a7b072,0xb4000000,0xb2a73072,0xae1f3364,0x2a3e33d9,0x34000000,0x3072b2a7,0xae1fb364,0xb3d9aa3e,0xb4000000,0xaa3e33d9,0x33642e1f,0x3072b2a7,
0x34000000,0xaa3eb3d9,0xb3642e1f,0x307232a7,0xb4000000,0x307232a7,0x2e1fb364,0xb3d92a3e,0x34000000,0xb2a7b072,0x2e1f3364,0x2a3eb3d9,0xb4000000,0x33d92a3e,0xb364ae1f,0x32a73072,
0x34000000,0xb3d92a3e,0x3364ae1f,0xb2a73072,0xb4000000,0x32a7b072,0xae1f3364,0xaa3eb3d9,0x34000000,0xb07232a7,0xae1fb364,0x33d92a3e,0xb4000000,0x2a3eb3d9,0x33642e1f,0xb07232a7,
0x34000000,0x2a3e33d9,0xb3642e1f,0xb072b2a7,0xb4000000,0xb072b2a7,0x2e1fb364,0x33d9aa3e,0x34000000,0x32a73072,0x2e1f3364,0xaa3e33d9,0xb4000000,0xb3d9aa3e,0xb364ae1f,0xb2a7b072,
0x31a8b1a8,0x3072b2a7,0x2e1fb364,0x2a3eb3d9,0x31a831a8,0x33d92a3e,0x3364ae1f,0x3072b2a7,0xb1a831a8,0x2a3e33d9,0x33642e1f,0x32a7b072,0xb1a8b1a8,0xb2a73072,0x2e1f3364,0x33d9aa3e,
0x31a8b1a8,0xb2a7b072,0xae1f3364,0x33d92a3e,0x31a831a8,0x2a3eb3d9,0xb3642e1f,0x32a73072,0xb1a831a8,0x33d9aa3e,0xb364ae1f,0x307232a7,0xb1a8b1a8,0x307232a7,0xae1fb364,0x2a3e33d9,
0x31a8b1a8,0xb07232a7,0x2e1fb364,0xaa3e33d9,0x31a831a8,0xb3d9aa3e,0x3364ae1f,0xb07232a7,0xb1a831a8,0xaa3eb3d9,0x33642e1f,0xb2a73072,0xb1a8b1a8,0x32a7b072,0x2e1f3364,0xb3d92a3e,
0x31a8b1a8,0x32a73072,0xae1f3364,0xb3d9aa3e,0x31a831a8,0xaa3e33d9,0xb3642e1f,0xb2a7b072,0xb1a831a8,0xb3d92a3e,0xb364ae1f,0xb072b2a7,0xb1a8b1a8,0xb072b2a7,0xae1fb364,0xaa3eb3d9,
0x34000000,0x33d9aa3e,0x3364ae1f,0x32a7b072,0x34000000,0x32a7b072,0x2e1fb364,0xaa3eb3d9,0x34000000,0x3072b2a7,0xae1fb364,0xb3d9aa3e,0x34000000,0x2a3eb3d9,0xb364ae1f,0xb07232a7,
0x34000000,0xaa3eb3d9,0xb3642e1f,0x307232a7,0x34000000,0xb072b2a7,0xae1f3364,0x33d9aa3e,0x34000000,0xb2a7b072,0x2e1f3364,0x2a3eb3d9,0x34000000,0xb3d9aa3e,0x33642e1f,0xb2a7b072,
0x34000000,0xb3d92a3e,0x3364ae1f,0xb2a73072,0x34000000,0xb2a73072,0x2e1fb364,0x2a3e33d9,0x34000000,0xb07232a7,0xae1fb364,0x33d92a3e,0x34000000,0xaa3e33d9,0xb364ae1f,0x3072b2a7,
0x34000000,0x2a3e33d9,0xb3642e1f,0xb072b2a7,0x34000000,0x307232a7,0xae1f3364,0xb3d92a3e,0x34000000,0x32a73072,0x2e1f3364,0xaa3e33d9,0x34000000,0x33d92a3e,0x33642e1f,0x32a73072,
0x31a8b1a8,0x3072b2a7,0x2e1fb364,0x2a3eb3d9,0xb1a8b1a8,0xb3d9aa3e,0xb3642e1f,0xb07232a7,0xb1a831a8,0x2a3e33d9,0x33642e1f,0x32a7b072,0x31a831a8,0x32a7b072,0xae1fb364,0xb3d92a3e,
0x31a8b1a8,0xb2a7b072,0xae1f3364,0x33d92a3e,0xb1a8b1a8,0xaa3e33d9,0x3364ae1f,0xb2a7b072,0xb1a831a8,0x33d9aa3e,0xb364ae1f,0x307232a7,0x31a831a8,0xb072b2a7,0x2e1f3364,0xaa3eb3d9,
0x31a8b1a8,0xb07232a7,0x2e1fb364,0xaa3e33d9,0xb1a8b1a8,0x33d92a3e,0xb3642e1f,0x3072b2a7,0xb1a831a8,0xaa3eb3d9,0x33642e1f,0xb2a73072,0x31a831a8,0xb2a73072,0xae1fb364,0x33d9aa3e,
0x31a8b1a8,0x32a73072,0xae1f3364,0xb3d9aa3e,0xb1a8b1a8,0x2a3eb3d9,0x3364ae1f,0x32a73072,0xb1a831a8,0xb3d92a3e,0xb364ae1f,0xb072b2a7,0x31a831a8,0x307232a7,0x2e1f3364,0x2a3e33d9,
46080,0xaa3eb3d9,0xae1fb364,0xb072b2a7,13312,0x307232a7,0x33642e1f,0x33d9aa3e,46080,0xb2a7b072,0xb3642e1f,0xaa3e33d9,13312,0x33d92a3e,0x2e1fb364,0xb2a7b072,
46080,0xb3d92a3e,0x2e1f3364,0x32a7b072,13312,0x32a7b072,0xb364ae1f,0x2a3e33d9,46080,0xb07232a7,0x3364ae1f,0xb3d9aa3e,13312,0x2a3eb3d9,0xae1f3364,0x3072b2a7,
46080,0x2a3e33d9,0xae1fb364,0x307232a7,13312,0xb072b2a7,0x33642e1f,0xb3d92a3e,46080,0x32a73072,0xb3642e1f,0x2a3eb3d9,13312,0xb3d9aa3e,0x2e1fb364,0x32a73072,
46080,0x33d9aa3e,0x2e1f3364,0xb2a73072,13312,0xb2a73072,0xb364ae1f,0xaa3eb3d9,46080,0x3072b2a7,0x3364ae1f,0x33d92a3e,13312,0xaa3e33d9,0xae1f3364,0xb07232a7,
0xb1a8b1a8,0xb2a7b072,0xb364ae1f,0xb3d9aa3e,0x31a8b1a8,0x2a3eb3d9,0xae1fb364,0xb2a7b072,0x31a831a8,0x33d9aa3e,0x2e1fb364,0xb072b2a7,0xb1a831a8,0x307232a7,0x3364ae1f,0xaa3eb3d9,
0xb1a8b1a8,0xb07232a7,0x33642e1f,0x2a3eb3d9,0x31a8b1a8,0xb3d9aa3e,0x2e1f3364,0x3072b2a7,0x31a831a8,0xaa3eb3d9,0xae1f3364,0x32a7b072,0xb1a831a8,0x32a7b072,0xb3642e1f,0x33d9aa3e,
0xb1a8b1a8,0x32a73072,0xb364ae1f,0x33d92a3e,0x31a8b1a8,0xaa3e33d9,0xae1fb364,0x32a73072,0x31a831a8,0xb3d92a3e,0x2e1fb364,0x307232a7,0xb1a831a8,0xb072b2a7,0x3364ae1f,0x2a3e33d9,
0xb1a8b1a8,0x3072b2a7,0x33642e1f,0xaa3e33d9,0x31a8b1a8,0x33d92a3e,0x2e1f3364,0xb07232a7,0x31a831a8,0x2a3e33d9,0xae1f3364,0xb2a73072,0xb1a831a8,0xb2a73072,0xb3642e1f,0xb3d92a3e,
13312,13312,13312,13312,0x2a3e33d9,0x307232a7,0x32a73072,0x33d92a3e,0x2e1f3364,0x33642e1f,0x3364ae1f,0x2e1fb364,0x307232a7,0x33d9aa3e,0x2a3eb3d9,0xb2a7b072,
0x31a831a8,0x31a8b1a8,0xb1a8b1a8,0xb1a831a8,0x32a73072,0x2a3eb3d9,0xb3d92a3e,0x307232a7,0x33642e1f,0xae1fb364,0xae1f3364,0x3364ae1f,0x33d92a3e,0xb2a7b072,0x307232a7,0xaa3eb3d9,
0x34000000,0xb4000000,0x34000000,0xb4000000,0x33d9aa3e,0xb2a73072,0x3072b2a7,0xaa3e33d9,0x3364ae1f,0xae1f3364,0xae1fb364,0x33642e1f,0x32a7b072,0x2a3e33d9,0xb3d9aa3e,0x3072b2a7,
0x31a8b1a8,0x31a831a8,0xb1a831a8,0xb1a8b1a8,0x3072b2a7,0x33d92a3e,0x2a3e33d9,0xb2a73072,0x2e1fb364,0x3364ae1f,0x33642e1f,0x2e1f3364,0x2a3eb3d9,0x3072b2a7,0x32a7b072,0x33d9aa3e,
13312,13312,13312,13312,0x33d9aa3e,0x32a7b072,0x3072b2a7,0x2a3eb3d9,0xae1fb364,0xb364ae1f,0xb3642e1f,0xae1f3364,0xb2a73072,0x2a3e33d9,0x33d92a3e,0x3072b2a7,
0x31a831a8,0x31a8b1a8,0xb1a8b1a8,0xb1a831a8,0x3072b2a7,0xb3d9aa3e,0x2a3e33d9,0x32a7b072,0xb364ae1f,0x2e1f3364,0x2e1fb364,0xb3642e1f,0xaa3e33d9,0x3072b2a7,0xb2a73072,0x33d9aa3e,
0x34000000,0xb4000000,0x34000000,0xb4000000,0xaa3eb3d9,0x307232a7,0xb2a7b072,0x33d92a3e,0xb3642e1f,0x2e1fb364,0x2e1f3364,0xb364ae1f,0x307232a7,0xb3d92a3e,0x2a3eb3d9,0x32a73072,
0x31a8b1a8,0x31a831a8,0xb1a831a8,0xb1a8b1a8,0xb2a7b072,0x2a3eb3d9,0x33d9aa3e,0x307232a7,0xae1f3364,0xb3642e1f,0xb364ae1f,0xae1fb364,0x33d92a3e,0x32a73072,0x307232a7,0x2a3e33d9,
13312,13312,13312,13312,0xaa3eb3d9,0xb072b2a7,0xb2a7b072,0xb3d9aa3e,0x2e1f3364,0x33642e1f,0x3364ae1f,0x2e1fb364,0xb072b2a7,0xb3d92a3e,0xaa3e33d9,0x32a73072,
0x31a831a8,0x31a8b1a8,0xb1a8b1a8,0xb1a831a8,0xb2a7b072,0xaa3e33d9,0x33d9aa3e,0xb072b2a7,0x33642e1f,0xae1fb364,0xae1f3364,0x3364ae1f,0xb3d9aa3e,0x32a73072,0xb072b2a7,0x2a3e33d9,
0x34000000,0xb4000000,0x34000000,0xb4000000,0xb3d92a3e,0x32a7b072,0xb07232a7,0x2a3eb3d9,0x3364ae1f,0xae1f3364,0xae1fb364,0x33642e1f,0xb2a73072,0xaa3eb3d9,0x33d92a3e,0xb07232a7,
0x31a8b1a8,0x31a831a8,0xb1a831a8,0xb1a8b1a8,0xb07232a7,0xb3d9aa3e,0xaa3eb3d9,0x32a7b072,0x2e1fb364,0x3364ae1f,0x33642e1f,0x2e1f3364,0xaa3e33d9,0xb07232a7,0xb2a73072,0xb3d92a3e,
13312,13312,13312,13312,0xb3d92a3e,0xb2a73072,0xb07232a7,0xaa3e33d9,0xae1fb364,0xb364ae1f,0xb3642e1f,0xae1f3364,0x32a7b072,0xaa3eb3d9,0xb3d9aa3e,0xb07232a7,
0x31a831a8,0x31a8b1a8,0xb1a8b1a8,0xb1a831a8,0xb07232a7,0x33d92a3e,0xaa3eb3d9,0xb2a73072,0xb364ae1f,0x2e1f3364,0x2e1fb364,0xb3642e1f,0x2a3eb3d9,0xb07232a7,0x32a7b072,0xb3d92a3e,
0x34000000,0xb4000000,0x34000000,0xb4000000,0x2a3e33d9,0xb072b2a7,0x32a73072,0xb3d9aa3e,0xb3642e1f,0x2e1fb364,0x2e1f3364,0xb364ae1f,0xb072b2a7,0x33d9aa3e,0xaa3e33d9,0xb2a7b072,
0x31a8b1a8,0x31a831a8,0xb1a831a8,0xb1a8b1a8,0x32a73072,0xaa3e33d9,0xb3d92a3e,0xb072b2a7,0xae1f3364,0xb3642e1f,0xb364ae1f,0xae1fb364,0xb3d9aa3e,0xb2a7b072,0xb072b2a7,0xaa3eb3d9,
0x34000000,0x34000000,0x34000000,0x34000000,0x33d9aa3e,0x32a7b072,0x3072b2a7,0x2a3eb3d9,0x3364ae1f,0x2e1fb364,0xae1fb364,0xb364ae1f,0x32a7b072,0xaa3eb3d9,0xb3d9aa3e,0xb07232a7,
0x31a8b1a8,0xb1a8b1a8,0xb1a831a8,0x31a831a8,0x3072b2a7,0xb3d9aa3e,0x2a3e33d9,0x32a7b072,0x2e1fb364,0xb3642e1f,0x33642e1f,0xae1fb364,0x2a3eb3d9,0xb07232a7,0x32a7b072,0xb3d92a3e,
46080,13312,46080,13312,0xaa3eb3d9,0x307232a7,0xb2a7b072,0x33d92a3e,0xae1fb364,0x33642e1f,0xb3642e1f,0x2e1fb364,0xb072b2a7,0x33d9aa3e,0xaa3e33d9,0xb2a7b072,
0xb1a8b1a8,0x31a8b1a8,0x31a831a8,0xb1a831a8,0xb2a7b072,0x2a3eb3d9,0x33d9aa3e,0x307232a7,0xb364ae1f,0xae1fb364,0x2e1fb364,0x3364ae1f,0xb3d9aa3e,0xb2a7b072,0xb072b2a7,0xaa3eb3d9,
0x34000000,0x34000000,0x34000000,0x34000000,0xaa3eb3d9,0xb072b2a7,0xb2a7b072,0xb3d9aa3e,0xb3642e1f,0xae1f3364,0x2e1f3364,0x33642e1f,0x307232a7,0x33d9aa3e,0x2a3eb3d9,0xb2a7b072,
0x31a8b1a8,0xb1a8b1a8,0xb1a831a8,0x31a831a8,0xb2a7b072,0xaa3e33d9,0x33d9aa3e,0xb072b2a7,0xae1f3364,0x3364ae1f,0xb364ae1f,0x2e1f3364,0x33d92a3e,0xb2a7b072,0x307232a7,0xaa3eb3d9,
46080,13312,46080,13312,0xb3d92a3e,0x32a7b072,0xb07232a7,0x2a3eb3d9,0x2e1f3364,0xb364ae1f,0x3364ae1f,0xae1f3364,0x32a7b072,0x2a3e33d9,0xb3d9aa3e,0x3072b2a7,
0xb1a8b1a8,0x31a8b1a8,0x31a831a8,0xb1a831a8,0xb07232a7,0xb3d9aa3e,0xaa3eb3d9,0x32a7b072,0x33642e1f,0x2e1f3364,0xae1f3364,0xb3642e1f,0x2a3eb3d9,0x3072b2a7,0x32a7b072,0x33d9aa3e,
0x34000000,0x34000000,0x34000000,0x34000000,0xb3d92a3e,0xb2a73072,0xb07232a7,0xaa3e33d9,0x3364ae1f,0x2e1fb364,0xae1fb364,0xb364ae1f,0xb2a73072,0x2a3e33d9,0x33d92a3e,0x3072b2a7,
0x31a8b1a8,0xb1a8b1a8,0xb1a831a8,0x31a831a8,0xb07232a7,0x33d92a3e,0xaa3eb3d9,0xb2a73072,0x2e1fb364,0xb3642e1f,0x33642e1f,0xae1fb364,0xaa3e33d9,0x3072b2a7,0xb2a73072,0x33d9aa3e,
46080,13312,46080,13312,0x2a3e33d9,0xb072b2a7,0x32a73072,0xb3d9aa3e,0xae1fb364,0x33642e1f,0xb3642e1f,0x2e1fb364,0x307232a7,0xb3d92a3e,0x2a3eb3d9,0x32a73072,
0xb1a8b1a8,0x31a8b1a8,0x31a831a8,0xb1a831a8,0x32a73072,0xaa3e33d9,0xb3d92a3e,0xb072b2a7,0xb364ae1f,0xae1fb364,0x2e1fb364,0x3364ae1f,0x33d92a3e,0x32a73072,0x307232a7,0x2a3e33d9,
0x34000000,0x34000000,0x34000000,0x34000000,0x2a3e33d9,0x307232a7,0x32a73072,0x33d92a3e,0xb3642e1f,0xae1f3364,0x2e1f3364,0x33642e1f,0xb072b2a7,0xb3d92a3e,0xaa3e33d9,0x32a73072,
0x31a8b1a8,0xb1a8b1a8,0xb1a831a8,0x31a831a8,0x32a73072,0x2a3eb3d9,0xb3d92a3e,0x307232a7,0xae1f3364,0x3364ae1f,0xb364ae1f,0x2e1f3364,0xb3d9aa3e,0x32a73072,0xb072b2a7,0x2a3e33d9,
46080,13312,46080,13312,0x33d9aa3e,0xb2a73072,0x3072b2a7,0xaa3e33d9,0x2e1f3364,0xb364ae1f,0x3364ae1f,0xae1f3364,0xb2a73072,0xaa3eb3d9,0x33d92a3e,0xb07232a7,
0xb1a8b1a8,0x31a8b1a8,0x31a831a8,0xb1a831a8,0x3072b2a7,0x33d92a3e,0x2a3e33d9,0xb2a73072,0x33642e1f,0x2e1f3364,0xae1f3364,0xb3642e1f,0xaa3e33d9,0xb07232a7,0xb2a73072,0xb3d92a3e,
0,304,648,952,576,880,1224,1528,1152,1456,1800,2104,1728,2032,104,408,
1728,2032,104,408,32,336,680,984,608,912,1256,1560,1184,1488,1832,2136,
1184,1488,1832,2136,1760,2064,352,1648,64,1360,72,376,64,1360,72,376,
64,1360,72,376,64,1360,72,376,64,1360,72,376,64,1360,72,376,
1296,1600,1944,2248,1872,2176,248,552,176,480,824,1128,752,1056,1400,1704,
752,1056,1400,1704,1328,1632,1976,2280,1904,2208,928,2224,640,1936,216,520,
640,1936,216,520,144,448,792,1096,720,1024,1368,1672,720,1024,1368,1672,
720,1024,1368,1672,720,1024,1368,1672,720,1024,1368,1672,720,1024,1368,1672,
320,624,968,1272,896,1200,1544,1848,1472,1776,2120,496,2048,208,1504,88,
2048,208,1504,88,1216,16,360,664,288,592,936,1240,864,1168,1512,1816,
864,1168,1512,1816,1440,1744,2088,120,2016,48,392,696,2016,48,392,696,
2016,48,392,696,2016,48,392,696,2016,48,392,696,2016,48,392,696,
1616,1920,2264,1072,2192,784,2080,232,1792,160,504,808,432,736,1080,1384,
432,736,1080,1384,1008,1312,1656,1960,1584,1888,2232,264,2160,192,536,840,
2160,192,536,840,464,768,1112,1416,1040,1344,1688,1992,1040,1344,1688,1992,
1040,1344,1688,1992,1040,1344,1688,1992,1040,1344,1688,1992,1040,1344,1688,1992,
152,456,800,1104,728,1032,1376,1680,1304,1608,1952,2256,1880,2184,256,560,
1880,2184,256,560,184,488,832,1136,760,1064,1408,1712,1336,1640,1984,2288,
1336,1640,1984,2288,1912,2216,1000,2296,712,2008,224,528,712,2008,224,528,
712,2008,224,528,712,2008,224,528,712,2008,224,528,712,2008,224,528,
1448,1752,2096,128,2024,56,400,704,328,632,976,1280,904,1208,1552,1856,
904,1208,1552,1856,1480,1784,2128,568,2056,280,1576,96,1288,24,368,672,
1288,24,368,672,296,600,944,1248,872,1176,1520,1824,872,1176,1520,1824,
872,1176,1520,1824,872,1176,1520,1824,872,1176,1520,1824,872,1176,1520,1824,
472,776,1120,1424,1048,1352,1696,2000,1624,1928,2272,1144,2200,856,2152,240,
2200,856,2152,240,1864,168,512,816,440,744,1088,1392,1016,1320,1664,1968,
1016,1320,1664,1968,1592,1896,2240,272,2168,200,544,848,2168,200,544,848,
2168,200,544,848,2168,200,544,848,2168,200,544,848,2168,200,544,848,
1768,2072,424,1720,136,1432,80,384,8,312,656,960,584,888,1232,1536,
584,888,1232,1536,1160,1464,1808,2112,1736,2040,112,416,40,344,688,992,
40,344,688,992,616,920,1264,1568,1192,1496,1840,2144,1192,1496,1840,2144,
1192,1496,1840,2144,1192,1496,1840,2144,1192,1496,1840,2144,1192,1496,1840,2144,
9920,10176,9792,0,12232,12488,12104,16,10818,10562,10946,32,13130,12874,13258,48,
9794,10050,10242,64,12106,12362,12554,80,10944,10688,10496,96,13256,13000,12808,112,
10244,9924,10116,128,12556,12236,12428,144,10494,10814,10622,160,12806,13126,12934,176,
10118,9798,9990,192,12430,12110,12302,208,10620,10940,10748,224,12932,13252,13060,240,
9992,10248,9864,4608,12304,12560,12176,4624,10746,10490,10874,4640,13058,12802,13186,4656,
9866,10122,10314,4672,12178,12434,12626,4688,10872,10616,10424,4704,13184,12928,12736,4720,
10316,9996,10188,4736,12628,12308,12500,4752,10422,10742,10550,4768,12734,13054,12862,4784,
10190,9870,10062,4800,12502,12182,12374,4816,10548,10868,10676,4832,12860,13180,12988,4848,
10064,10320,9936,1048576,12376,12632,12248,496,10674,10418,10802,480,12986,12730,13114,464,
9938,10194,9810,448,12250,12506,12122,432,10800,10544,10928,416,13112,12856,13240,400,
9812,10068,10260,384,12124,12380,12572,368,10926,10670,10478,352,13238,12982,12790,336,
10262,9942,10134,320,12574,12254,12446,304,10476,10796,10604,288,12788,13108,12916,272,
10136,9816,10008,1048576,12448,12128,12320,5104,10602,10922,10730,5088,12914,13234,13042,5072,
10010,10266,9882,5056,12322,12578,12194,5040,10728,10472,10856,5024,13040,12784,13168,5008,
9884,10140,10332,4992,12196,12452,12644,4976,10854,10598,10406,4960,13166,12910,12718,4944,
10334,10014,10206,4928,12646,12326,12518,4912,10404,10724,10532,4896,12716,13036,12844,4880,
10208,9888,10080,256,12520,12200,12392,1048576,10530,10850,10658,1048576,12842,13162,12970,1048576,
10082,10338,10368,1048576,12394,12650,12680,1048576,10656,10400,10370,1048576,12968,12712,12682,1048576,
9956,10212,10368,1048576,12268,12524,12680,1048576,10782,10526,10370,1048576,13094,12838,12682,1048576,
9830,10086,10368,1048576,12142,12398,12680,1048576,10908,10652,10370,1048576,13220,12964,12682,1048576,
10280,9960,10368,4864,12592,12272,12680,1048576,10458,10778,10370,1048576,12770,13090,12682,1048576,
10154,9834,10368,1048576,12466,12146,12680,1048576,10584,10904,10370,1048576,12896,13216,12682,1048576,
10028,10284,10368,1048576,12340,12596,12680,1048576,10710,10454,10370,1048576,13022,12766,12682,1048576,
9902,10158,10368,1048576,12214,12470,12680,1048576,10836,10580,10370,1048576,13148,12892,12682,1048576,
10352,10032,10368,1048576,12664,12344,12680,1048576,10386,10706,10370,1048576,12698,13018,12682,1048576,
10226,9906,10368,1048576,12538,12218,12680,1048576,10512,10832,10370,1048576,12824,13144,12682,1048576,
10100,10356,10368,1048576,12412,12668,12680,1048576,10638,10382,10370,1048576,12950,12694,12682,1048576,
9974,10230,10368,1048576,12286,12542,12680,1048576,10764,10508,10370,1048576,13076,12820,12682,1048576,
9848,10104,10368,1048576,12160,12416,12680,1048576,10890,10634,10370,1048576,13202,12946,12682,1048576,
10298,9978,10368,1048576,12610,12290,12680,1048576,10440,10760,10370,1048576,12752,13072,12682,1048576,
10172,9852,10368,1048576,12484,12164,12680,1048576,10566,10886,10370,1048576,12878,13198,12682,1048576,
10046,10302,10368,1048576,12358,12614,12680,1048576,10692,10436,10370,1048576,13004,12748,12682,1048576,
10048,10304,10368,1048576,12360,12616,12680,1048576,10690,10434,10370,1048576,13002,12746,12682,1048576,
9922,10178,10368,1048576,12234,12490,12680,1048576,10816,10560,10370,1048576,13128,12872,12682,1048576,
9796,10052,10368,1048576,12108,12364,12680,1048576,10942,10686,10370,1048576,13254,12998,12682,1048576,
10246,9926,10368,1048576,12558,12238,12680,1048576,10492,10812,10370,1048576,12804,13124,12682,1048576,
10120,9800,10368,1048576,12432,12112,12680,1048576,10618,10938,10370,1048576,12930,13250,12682,1048576,
9994,10250,10368,1048576,12306,12562,12680,1048576,10744,10488,10370,1048576,13056,12800,12682,1048576,
9868,10124,10368,1048576,12180,12436,12680,1048576,10870,10614,10370,1048576,13182,12926,12682,1048576,
10318,9998,10368,1048576,12630,12310,12680,1048576,10420,10740,10370,1048576,12732,13052,12682,1048576,
10192,9872,10368,1048576,12504,12184,12680,1048576,10546,10866,10370,1048576,12858,13178,12682,1048576,
10066,10322,10368,1048576,12378,12634,12680,1048576,10672,10416,10370,1048576,12984,12728,12682,1048576,
9940,10196,10368,1048576,12252,12508,12680,1048576,10798,10542,10370,1048576,13110,12854,12682,1048576,
9814,10070,10368,1048576,12126,12382,12680,1048576,10924,10668,10370,1048576,13236,12980,12682,1048576,
10264,9944,10368,1048576,12576,12256,12680,1048576,10474,10794,10370,1048576,12786,13106,12682,1048576,
10138,9818,10368,1048576,12450,12130,12680,1048576,10600,10920,10370,1048576,12912,13232,12682,1048576,
10012,10268,10368,1048576,12324,12580,12680,1048576,10726,10470,10370,1048576,13038,12782,12682,1048576,
9886,10142,10368,1048576,12198,12454,12680,1048576,10852,10596,10370,1048576,13164,12908,12682,1048576,
10336,10016,10368,1048576,12648,12328,12680,1048576,10402,10722,10370,1048576,12714,13034,12682,1048576,
10210,9890,10368,1048576,12522,12202,12680,1048576,10528,10848,10370,1048576,12840,13160,12682,1048576,
10084,10340,10368,1048576,12396,12652,12680,1048576,10654,10398,10370,1048576,12966,12710,12682,1048576,
9958,10214,10368,1048576,12270,12526,12680,1048576,10780,10524,10370,1048576,13092,12836,12682,1048576,
9832,10088,10368,1048576,12144,12400,12680,1048576,10906,10650,10370,1048576,13218,12962,12682,1048576,
10282,9962,10368,1048576,12594,12274,12680,1048576,10456,10776,10370,1048576,12768,13088,12682,1048576,
10156,9836,10368,1048576,12468,12148,12680,1048576,10582,10902,10370,1048576,12894,13214,12682,1048576,
10030,10286,10368,1048576,12342,12598,12680,1048576,10708,10452,10370,1048576,13020,12764,12682,1048576,
9904,10160,10368,1048576,12216,12472,12680,1048576,10834,10578,10370,1048576,13146,12890,12682,1048576,
10354,10034,10368,1048576,12666,12346,12680,1048576,10384,10704,10370,1048576,12696,13016,12682,1048576,
10228,9908,10368,1048576,12540,12220,12680,1048576,10510,10830,10370,1048576,12822,13142,12682,1048576,
10102,10358,10368,1048576,12414,12670,12680,1048576,10636,10380,10370,1048576,12948,12692,12682,1048576,
9976,10232,10368,1048576,12288,12544,12680,1048576,10762,10506,10370,1048576,13074,12818,12682,1048576,
9850,10106,10368,1048576,12162,12418,12680,1048576,10888,10632,10370,1048576,13200,12944,12682,1048576,
10300,9980,10368,1048576,12612,12292,12680,1048576,10438,10758,10370,1048576,12750,13070,12682,1048576,
10174,9854,10368,1048576,12486,12166,12680,1048576,10564,10884,10370,1048576,12876,13196,12682,1048576,
0,512,1536,0,16,528,1552,16,32,544,1568,32,48,560,1584,48,
64,576,1600,64,80,592,1616,80,96,608,1632,96,112,624,1648,112,
128,640,1664,128,144,656,1680,144,160,672,1696,160,176,688,1712,176,
192,704,1728,192,208,720,1744,208,224,736,1760,224,240,752,1776,240,
1536,5120,6144,4608,1552,5136,6160,4624,1568,5152,6176,4640,1584,5168,6192,4656,
1600,5184,6208,4672,1616,5200,6224,4688,1632,5216,6240,4704,1648,5232,6256,4720,
1664,5248,6272,4736,1680,5264,6288,4752,1696,5280,6304,4768,1712,5296,6320,4784,
1728,5312,6336,4800,1744,5328,6352,4816,1760,5344,6368,4832,1776,5360,6384,4848,
3072,4096,3072,0,3088,4592,3568,496,3104,4576,3552,480,3120,4560,3536,464,
3136,4544,3520,448,3152,4528,3504,432,3168,4512,3488,416,3184,4496,3472,400,
3200,4480,3456,384,3216,4464,3440,368,3232,4448,3424,352,3248,4432,3408,336,
3264,4416,3392,320,3280,4400,3376,304,3296,4384,3360,288,3312,4368,3344,272,
3072,8704,7680,4608,3088,9200,8176,5104,3104,9184,8160,5088,3120,9168,8144,5072,
3136,9152,8128,5056,3152,9136,8112,5040,3168,9120,8096,5024,3184,9104,8080,5008,
3200,9088,8064,4992,3216,9072,8048,4976,3232,9056,8032,4960,3248,9040,8016,4944,
3264,9024,8000,4928,3280,9008,7984,4912,3296,8992,7968,4896,3312,8976,7952,4880,
8,768,1792,256,24,784,1808,272,40,800,1824,288,56,816,1840,304,
72,832,1856,320,88,848,1872,336,104,864,1888,352,120,880,1904,368,
136,896,1920,384,152,912,1936,400,168,928,1952,416,184,944,1968,432,
200,960,1984,448,216,976,2000,464,232,992,2016,480,248,1008,2032,496,
1544,5376,6400,4864,1560,5392,6416,4880,1576,5408,6432,4896,1592,5424,6448,4912,
1608,5440,6464,4928,1624,5456,6480,4944,1640,5472,6496,4960,1656,5488,6512,4976,
1672,5504,6528,4992,1688,5520,6544,5008,1704,5536,6560,5024,1720,5552,6576,5040,
1736,5568,6592,5056,1752,5584,6608,5072,1768,5600,6624,5088,1784,5616,6640,5104,
3080,4352,3328,256,3096,4336,3312,240,3112,4320,3296,224,3128,4304,3280,208,
3144,4288,3264,192,3160,4272,3248,176,3176,4256,3232,160,3192,4240,3216,144,
3208,4224,3200,128,3224,4208,3184,112,3240,4192,3168,96,3256,4176,3152,80,
3272,4160,3136,64,3288,4144,3120,48,3304,4128,3104,32,3320,4112,3088,16,
3080,8960,7936,4864,3096,8944,7920,4848,3112,8928,7904,4832,3128,8912,7888,4816,
3144,8896,7872,4800,3160,8880,7856,4784,3176,8864,7840,4768,3192,8848,7824,4752,
3208,8832,7808,4736,3224,8816,7792,4720,3240,8800,7776,4704,3256,8784,7760,4688,
3272,8768,7744,4672,3288,8752,7728,4656,3304,8736,7712,4640,3320,8720,7696,4624,
4608,1024,2048,0,4624,1040,2064,16,4640,1056,2080,32,4656,1072,2096,48,
4672,1088,2112,64,4688,1104,2128,80,4704,1120,2144,96,4720,1136,2160,112,
4736,1152,2176,128,4752,1168,2192,144,4768,1184,2208,160,4784,1200,2224,176,
4800,1216,2240,192,4816,1232,2256,208,4832,1248,2272,224,4848,1264,2288,240,
6144,5632,6656,4608,6160,5648,6672,4624,6176,5664,6688,4640,6192,5680,6704,4656,
6208,5696,6720,4672,6224,5712,6736,4688,6240,5728,6752,4704,6256,5744,6768,4720,
6272,5760,6784,4736,6288,5776,6800,4752,6304,5792,6816,4768,6320,5808,6832,4784,
6336,5824,6848,4800,6352,5840,6864,4816,6368,5856,6880,4832,6384,5872,6896,4848,
7680,3584,2560,0,7696,4080,3056,496,7712,4064,3040,480,7728,4048,3024,464,
7744,4032,3008,448,7760,4016,2992,432,7776,4000,2976,416,7792,3984,2960,400,
7808,3968,2944,384,7824,3952,2928,368,7840,3936,2912,352,7856,3920,2896,336,
7872,3904,2880,320,7888,3888,2864,304,7904,3872,2848,288,7920,3856,2832,272,
7680,8192,7168,4608,7696,8688,7664,5104,7712,8672,7648,5088,7728,8656,7632,5072,
7744,8640,7616,5056,7760,8624,7600,5040,7776,8608,7584,5024,7792,8592,7568,5008,
7808,8576,7552,4992,7824,8560,7536,4976,7840,8544,7520,4960,7856,8528,7504,4944,
7872,8512,7488,4928,7888,8496,7472,4912,7904,8480,7456,4896,7920,8464,7440,4880,
4616,1280,2304,256,4632,1296,2320,272,4648,1312,2336,288,4664,1328,2352,304,
4680,1344,2368,320,4696,1360,2384,336,4712,1376,2400,352,4728,1392,2416,368,
4744,1408,2432,384,4760,1424,2448,400,4776,1440,2464,416,4792,1456,2480,432,
4808,1472,2496,448,4824,1488,2512,464,4840,1504,2528,480,4856,1520,2544,496,
6152,5888,6912,4864,6168,5904,6928,4880,6184,5920,6944,4896,6200,5936,6960,4912,
6216,5952,6976,4928,6232,5968,6992,4944,6248,5984,7008,4960,6264,6000,7024,4976,
6280,6016,7040,4992,6296,6032,7056,5008,6312,6048,7072,5024,6328,6064,7088,5040,
6344,6080,7104,5056,6360,6096,7120,5072,6376,6112,7136,5088,6392,6128,7152,5104,
7688,3840,2816,256,7704,3824,2800,240,7720,3808,2784,224,7736,3792,2768,208,
7752,3776,2752,192,7768,3760,2736,176,7784,3744,2720,160,7800,3728,2704,144,
7816,3712,2688,128,7832,3696,2672,112,7848,3680,2656,96,7864,3664,2640,80,
7880,3648,2624,64,7896,3632,2608,48,7912,3616,2592,32,7928,3600,2576,16,
7688,8448,7424,4864,7704,8432,7408,4848,7720,8416,7392,4832,7736,8400,7376,4816,
7752,8384,7360,4800,7768,8368,7344,4784,7784,8352,7328,4768,7800,8336,7312,4752,
7816,8320,7296,4736,7832,8304,7280,4720,7848,8288,7264,4704,7864,8272,7248,4688,
7880,8256,7232,4672,7896,8240,7216,4656,7912,8224,7200,4640,7928,8208,7184,4624,
4,68,1760,0,12,140,1768,16,20,212,1776,32,28,284,1784,48,
76,356,1832,64,84,428,1840,80,92,500,1848,96,100,572,1856,112,
148,644,1904,128,156,716,1912,144,164,788,1920,160,172,860,1928,176,
220,932,1976,192,228,1004,1984,208,236,1076,1992,224,244,1148,2000,240,
292,1220,2048,1536,300,1292,2056,1552,308,1364,2064,1568,316,1436,2072,1584,
364,1508,2120,1600,372,1580,2128,1616,380,1652,2136,1632,388,1724,2144,1648,
436,1796,2192,1664,444,1868,2200,1680,452,1940,2208,1696,460,2012,2216,1712,
508,2084,2264,1728,516,2156,2272,1744,524,2228,2280,1760,532,2300,2288,1776,
580,0,64,3072,588,8,136,3088,596,16,208,3104,604,24,280,3120,
652,72,352,3136,660,80,424,3152,668,88,496,3168,676,96,568,3184,
724,144,640,3200,732,152,712,3216,740,160,784,3232,748,168,856,3248,
796,216,928,3264,804,224,1000,3280,812,232,1072,3296,820,240,1144,3312,
868,288,1216,1048576,876,296,1288,1048576,884,304,1360,1048576,892,312,1432,1048576,
940,360,1504,1048576,948,368,1576,1048576,956,376,1648,1048576,964,384,1720,1048576,
1012,432,1792,1048576,1020,440,1864,1048576,1028,448,1936,1048576,1036,456,2008,1048576,
1084,504,2080,1048576,1092,512,2152,1048576,1100,520,2224,1048576,1108,528,2296,1048576,
1156,576,1760,8,1164,584,1768,24,1172,592,1776,40,1180,600,1784,56,
1228,648,1832,72,1236,656,1840,88,1244,664,1848,104,1252,672,1856,120,
1300,720,1904,136,1308,728,1912,152,1316,736,1920,168,1324,744,1928,184,
1372,792,1976,200,1380,800,1984,216,1388,808,1992,232,1396,816,2000,248,
1444,864,2048,1544,1452,872,2056,1560,1460,880,2064,1576,1468,888,2072,1592,
1516,936,2120,1608,1524,944,2128,1624,1532,952,2136,1640,1540,960,2144,1656,
1588,1008,2192,1672,1596,1016,2200,1688,1604,1024,2208,1704,1612,1032,2216,1720,
1660,1080,2264,1736,1668,1088,2272,1752,1676,1096,2280,1768,1684,1104,2288,1784,
1732,1152,64,3080,1740,1160,136,3096,1748,1168,208,3112,1756,1176,280,3128,
1804,1224,352,3144,1812,1232,424,3160,1820,1240,496,3176,1828,1248,568,3192,
1876,1296,640,3208,1884,1304,712,3224,1892,1312,784,3240,1900,1320,856,3256,
1948,1368,928,3272,1956,1376,1000,3288,1964,1384,1072,3304,1972,1392,1144,3320,
2020,1440,1216,1048576,2028,1448,1288,1048576,2036,1456,1360,1048576,2044,1464,1432,1048576,
2092,1512,1504,1048576,2100,1520,1576,1048576,2108,1528,1648,1048576,2116,1536,1720,1048576,
2164,1584,1792,1048576,2172,1592,1864,1048576,2180,1600,1936,1048576,2188,1608,2008,1048576,
2236,1656,2080,1048576,2244,1664,2152,1048576,2252,1672,2224,1048576,2260,1680,2296,1048576,
36,1728,1760,4608,44,1736,1768,4624,52,1744,1776,4640,60,1752,1784,4656,
108,1800,1832,4672,116,1808,1840,4688,124,1816,1848,4704,132,1824,1856,4720,
180,1872,1904,4736,188,1880,1912,4752,196,1888,1920,4768,204,1896,1928,4784,
252,1944,1976,4800,260,1952,1984,4816,268,1960,1992,4832,276,1968,2000,4848,
324,2016,2048,6144,332,2024,2056,6160,340,2032,2064,6176,348,2040,2072,6192,
396,2088,2120,6208,404,2096,2128,6224,412,2104,2136,6240,420,2112,2144,6256,
468,2160,2192,6272,476,2168,2200,6288,484,2176,2208,6304,492,2184,2216,6320,
540,2232,2264,6336,548,2240,2272,6352,556,2248,2280,6368,564,2256,2288,6384,
612,32,64,7680,620,40,136,7696,628,48,208,7712,636,56,280,7728,
684,104,352,7744,692,112,424,7760,700,120,496,7776,708,128,568,7792,
756,176,640,7808,764,184,712,7824,772,192,784,7840,780,200,856,7856,
828,248,928,7872,836,256,1000,7888,844,264,1072,7904,852,272,1144,7920,
900,320,1216,1048576,908,328,1288,1048576,916,336,1360,1048576,924,344,1432,1048576,
972,392,1504,1048576,980,400,1576,1048576,988,408,1648,1048576,996,416,1720,1048576,
1044,464,1792,1048576,1052,472,1864,1048576,1060,480,1936,1048576,1068,488,2008,1048576,
1116,536,2080,1048576,1124,544,2152,1048576,1132,552,2224,1048576,1140,560,2296,1048576,
1188,608,1760,4616,1196,616,1768,4632,1204,624,1776,4648,1212,632,1784,4664,
1260,680,1832,4680,1268,688,1840,4696,1276,696,1848,4712,1284,704,1856,4728,
1332,752,1904,4744,1340,760,1912,4760,1348,768,1920,4776,1356,776,1928,4792,
1404,824,1976,4808,1412,832,1984,4824,1420,840,1992,4840,1428,848,2000,4856,
1476,896,2048,6152,1484,904,2056,6168,1492,912,2064,6184,1500,920,2072,6200,
1548,968,2120,6216,1556,976,2128,6232,1564,984,2136,6248,1572,992,2144,6264,
1620,1040,2192,6280,1628,1048,2200,6296,1636,1056,2208,6312,1644,1064,2216,6328,
1692,1112,2264,6344,1700,1120,2272,6360,1708,1128,2280,6376,1716,1136,2288,6392,
1764,1184,64,7688,1772,1192,136,7704,1780,1200,208,7720,1788,1208,280,7736,
1836,1256,352,7752,1844,1264,424,7768,1852,1272,496,7784,1860,1280,568,7800,
1908,1328,640,7816,1916,1336,712,7832,1924,1344,784,7848,1932,1352,856,7864,
1980,1400,928,7880,1988,1408,1000,7896,1996,1416,1072,7912,2004,1424,1144,7928,
2052,1472,1216,1048576,2060,1480,1288,1048576,2068,1488,1360,1048576,2076,1496,1432,1048576,
2124,1544,1504,1048576,2132,1552,1576,1048576,2140,1560,1648,1048576,2148,1568,1720,1048576,
2196,1616,1792,1048576,2204,1624,1864,1048576,2212,1632,1936,1048576,2220,1640,2008,1048576,
2268,1688,2080,1048576,2276,1696,2152,1048576,2284,1704,2224,1048576,2292,1712,2296,1048576,
};
#define ASM_ALL \
"s_load_dwordx2 s[12:13],s[0:1],0\ns_load_dwordx4 s[16:19],s[0:1],8\ns_load_dwordx2 s[36:37],s[0:1],48\ns_load_dwordx2 s[8:9],s[0:1],24\n" \
"s_load_dwordx2 s[20:21],s[0:1],32\ns_load_dwordx2 s[24:25],s[0:1],40\ns_getpc_b64 s[96:97]\n.Lc0_%=:\n" \
"v_and_b32 v1,0xe0,v0\nv_lshlrev_b32 v2,7,v0\ns_lshr_b32 s33,s2,3\ns_mov_b32 s98,.Lend_%=-.Lc0_%=\n" \
"s_mov_b32 s99,0x20000\ns_sub_u32 s95,s33,9\nv_lshl_or_b32 v1,v1,24,v2\ns_lshl_b32 s95,s95,12\n" \
"s_mov_b32 m0,162176\nv_readfirstlane_b32 s3,v0\nbuffer_load_dword v1,s[96:99],s95 offen lds\ns_and_b32 s32,s2,7\n" \
"s_mov_b32 s15,0x20000\ns_mov_b32 s14,2359296\ns_lshr_b32 s3,s3,6\ns_lshl_b32 s34,s32,5\n" \
"v_and_b32 v13,31,v0\nv_bfe_u32 v12,v0,5,1\ns_or_b32 s34,s34,s33\nv_mul_u32_u24 v12,1179648,v12\n" \
"s_lshl_b32 s100,s3,8\nv_lshlrev_b32 v2,4,v0\ns_add_u32 s100,s100,s34\nv_lshl_add_u32 v15,v13,4,v12\n" \
"s_mul_i32 s89,s100,1152\nv_lshl_add_u32 v13,v13,2,v12\nv_mov_b32 v12,v15\nv_and_b32 v1,0x3f0,v2\n" \
"s_mov_b32 s38,134217728\ns_mov_b32 s39,0x20000\ns_cmp_gt_u32 s2,71\ns_cbranch_scc1 .Lnp_%=\n" \
"s_and_b32 s95,s32,3\ns_lshr_b32 s100,s32,2\ns_mul_i32 s95,s95,9\ns_mul_i32 s101,s100,576\n" \
"s_add_u32 s95,s95,s33\ns_mul_i32 s100,s100,1990656\ns_lshl_b32 s95,s95,6\ns_mov_b32 s22,576\n" \
"s_mov_b32 s23,0x20000\ns_mov_b32 s26,1990656\ns_mov_b32 s27,0x20000\ns_waitcnt lgkmcnt(0)\n" \
"s_add_u32 s20,s20,s101\ns_addc_u32 s21,s21,0\ns_add_u32 s24,s24,s100\ns_addc_u32 s25,s25,0\n" \
"buffer_load_dwordx2 v[126:127],off,s[36:39],0 sc1\nv_lshrrev_b32 v120,2,v0\nv_and_b32 v121,3,v0\nv_mul_u32_u24 v122,6912,v120\n" \
"v_lshlrev_b32 v123,3,v120\nv_lshl_add_u32 v121,v121,4,s95\nbuffer_load_dwordx2 v[24:25],v123,s[20:23],0 offen\nv_lshlrev_b32 v120,1,v120\n" \
"v_lshl_add_u32 v123,v122,2,v121\nbuffer_load_ushort v28,v120,s[20:23],0 offen offset:512\nv_add_u32 v121,v121,v122\nbuffer_load_dwordx4 v[80:83],v123,s[24:27],0 offen\n" \
"s_mov_b32 s95,6912\nbuffer_load_dwordx4 v[84:87],v123,s[24:27],s95 offen\ns_mov_b32 s95,13824\nbuffer_load_dwordx4 v[88:91],v123,s[24:27],s95 offen\n" \
"s_mov_b32 s95,20736\nbuffer_load_dwordx4 v[92:95],v123,s[24:27],s95 offen\ns_mov_b32 s95,1769472\nbuffer_load_dwordx4 v[96:99],v121,s[24:27],s95 offen\n" \
"s_branch .Lnq_%=\n.Lnp_%=:\ns_waitcnt lgkmcnt(0)\nbuffer_load_dwordx2 v[126:127],off,s[36:39],0 sc1\n" \
".Lnq_%=:\ns_memrealtime s[30:31]\nbuffer_load_dwordx4 v[224:227],v12,s[12:15],s89 offen\nbuffer_load_dwordx4 v[228:231],v12,s[12:15],s89 offen offset:512\n" \
"buffer_load_dword v232,v13,s[12:15],s89 offen offset:1024\ns_cmp_lt_u32 s3,2\ns_cselect_b64 s[16:17],s[16:17],s[18:19]\ns_mov_b32 s18,1183744\n" \
"s_mov_b32 s19,0x20000\ns_mul_i32 s100,s34,4624\nv_and_b32 v16,0x7f0,v2\ns_lshr_b32 s95,s3,1\n" \
"s_mul_i32 s95,s95,3168\ns_lshl_b32 s88,s3,10\ns_add_u32 s95,s95,s88\ns_add_u32 m0,s95,9792\n" \
"v_add_u32 v17,s100,v16\nbuffer_load_dwordx4 v16,s[16:19],s100 offen lds\nbuffer_load_dwordx4 v16,s[16:19],s100 offen offset:2048 lds\nv_add_u32 v17,4096,v17\n" \
"v_subrev_u32 v18,1183744,v17\ns_add_u32 m0,s95,13888\nv_min_u32 v17,v17,v18\ns_bitcmp1_b32 s3,0\n" \
"s_cselect_b64 exec,63,-1\nbuffer_load_dwordx4 v17,s[16:19],0 offen lds\ns_mov_b64 exec,-1\ns_mov_b32 s6,22528\n" \
"s_mov_b32 s7,0x20000\ns_mov_b32 s95,10240\nbuffer_load_dwordx4 v[246:249],v2,s[4:7],s95 offen\ns_cmp_gt_u32 s2,110\n" \
"s_cbranch_scc1 .LC_%=\ns_cmp_gt_u32 s2,71\ns_cbranch_scc1 .LN_%=\n.LP_%=:\n" \
"v_bfe_u32 v112,v0,3,2\nv_bfe_u32 v113,v0,5,1\nv_lshl_add_u32 v112,v112,8,v113\ns_lshl_b32 s48,s33,2\n" \
"v_lshrrev_b32 v113,3,v0\ns_add_u32 s48,s48,s3\nv_and_b32 v113,6,v113\nv_and_b32 v114,7,v0\n" \
"s_mul_i32 s48,s48,18432\ns_mul_i32 s49,s32,1152\nv_mul_u32_u24 v112,9216,v112\nv_xor_b32 v113,v113,v114\n" \
"s_add_u32 s48,s48,s49\nv_lshl_add_u32 v4,v113,4,v112\ns_mov_b32 s10,18874368\ns_mov_b32 s11,0x20000\n" \
"s_add_u32 s48,s48,1024\ns_add_u32 m0,s88,20224\ns_add_u32 s50,s48,9437184\nbuffer_load_dwordx4 v4,s[8:11],s48 offen lds\n" \
"s_add_u32 m0,s88,24320\nv_add_u32 v5,17408,v4\nbuffer_load_dwordx4 v4,s[8:11],s50 offen lds\nv_add_u32 v6,34816,v4\n" \
"v_add_u32 v7,52224,v4\nv_lshlrev_b32 v158,3,v0\nv_add_u32 v158,161152,v158\ns_mul_i32 s63,s32,9\n" \
"s_add_u32 s63,s63,s33\nv_bfrev_b32 v159,1\nv_cmp_gt_u32 s[64:65],16,v0\ns_lshl_b32 s86,s63,8\n" \
"s_add_u32 s63,s86,8192\nv_cndmask_b32 v159,v159,v2,s[64:65]\ns_mov_b32 s51,4096\ns_add_u32 m0,s88,28416\n" \
"s_waitcnt vmcnt(15)\nv_lshlrev_b32 v128,16,v24\nv_mul_f32 v129,0xbfb8aa3b,v128\nv_exp_f32 v129,v129\n" \
"v_and_b32 v130,0xffff0000,v24\nv_add_f32 v129,1.0,v129\nv_mul_f32 v131,0xbfb8aa3b,v130\nv_lshlrev_b32 v132,16,v25\n" \
"v_rcp_f32 v129,v129\nv_exp_f32 v131,v131\nv_mul_f32 v133,0xbfb8aa3b,v132\nv_exp_f32 v133,v133\n" \
"v_mul_f32 v128,v128,v129\nv_add_f32 v131,1.0,v131\nv_and_b32 v134,0xffff0000,v25\nv_cvt_pk_bf16_f32 v128,v128,v128\n" \
"v_rcp_f32 v131,v131\nv_add_f32 v133,1.0,v133\nv_mul_f32 v135,0xbfb8aa3b,v134\nv_lshlrev_b32 v128,16,v128\n" \
"s_waitcnt vmcnt(14)\nv_lshlrev_b32 v136,16,v28\nv_rcp_f32 v133,v133\nv_exp_f32 v135,v135\n" \
"v_mul_f32 v137,0xbfb8aa3b,v136\nv_mul_f32 v130,v130,v131\nv_exp_f32 v137,v137\nv_cvt_pk_bf16_f32 v130,v130,v130\n" \
"v_mul_f32 v132,v132,v133\nv_add_f32 v135,1.0,v135\nv_lshlrev_b32 v130,16,v130\nv_cvt_pk_bf16_f32 v132,v132,v132\n" \
"v_rcp_f32 v135,v135\nv_add_f32 v137,1.0,v137\nv_lshlrev_b32 v132,16,v132\nv_rcp_f32 v137,v137\n" \
"v_mul_f32 v134,v134,v135\nv_cvt_pk_bf16_f32 v134,v134,v134\nv_mul_f32 v136,v136,v137\nv_lshlrev_b32 v134,16,v134\n" \
"v_cvt_pk_bf16_f32 v136,v136,v136\nv_lshlrev_b32 v136,16,v136\ns_mov_b32 s52,8192\ns_mov_b32 s53,14336\n" \
"s_mul_i32 s57,s3,30720\ns_add_u32 s57,s57,38272\ns_and_b32 s60,s33,7\ns_mul_i32 s92,s3,589824\n" \
"s_add_u32 s92,s92,s49\ns_lshl_b32 s62,s60,7\ns_add_u32 s92,s92,s62\ns_load_dwordx4 s[44:47],s[0:1],72\n" \
"v_mov_b32 v16,-1\nv_mov_b32 v20,0\nv_mov_b32 v17,-1\nv_mov_b32 v21,0\n" \
"v_mov_b32 v18,-1\nv_mov_b32 v22,0\nv_mov_b32 v19,-1\nv_mov_b32 v23,0\n" \
"s_mul_i32 s54,s34,9344\ns_add_u32 s35,s54,524288\ns_add_u32 s55,s35,4096\nv_and_b32 v112,15,v0\n" \
"v_bfe_u32 v113,v0,4,2\nv_mul_u32_u24 v113,589824,v113\nv_lshl_add_u32 v113,v112,4,v113\ns_lshl_b32 s56,s34,8\n" \
"s_add_u32 s56,s56,10289152\ns_cmp_eq_u32 s3,2\ns_cselect_b32 s56,s56,2147483648\nv_add_u32 v113,s56,v113\n" \
"s_add_u32 s58,s35,8192\nv_add_u32 v114,s58,v2\nv_cmp_gt_u32 vcc,68,v0\ns_lshr_b32 s59,s33,3\n" \
"s_lshl_b32 s59,s59,3\nv_cndmask_b32 v113,v113,v114,vcc\ns_add_u32 s59,s59,s32\ns_lshl_b32 s59,s59,3\n" \
"s_add_u32 s59,s59,s60\ns_lshl_b32 s59,s59,7\ns_add_u32 s87,s59,262144\nv_lshrrev_b32 v115,3,v0\n" \
"v_and_b32 v116,0x70,v2\nv_lshl_add_u32 v115,v115,16,v116\nv_add_u32 v115,s87,v115\ns_lshl_b32 s61,s34,4\n" \
"s_add_u32 s61,s61,65536\nv_mov_b32 v116,s61\nv_cmp_gt_u32 vcc,24,v0\nv_and_b32 v118,15,v0\n" \
"v_bfe_u32 v119,v0,4,2\nv_cndmask_b32 v115,v116,v115,vcc\nv_bfrev_b32 v116,1\nv_cmp_gt_u32 vcc,25,v0\n" \
"v_lshlrev_b32 v3,4,v119\nv_lshl_add_u32 v3,v118,6,v3\nv_cndmask_b32 v115,v116,v115,vcc\nv_lshrrev_b32 v15,1,v1\n" \
"v_add_u32 v15,s57,v15\nv_and_b32 v120,0x3c0,v1\nv_bfe_u32 v121,v0,3,2\nv_and_b32 v122,3,v0\n" \
"v_lshlrev_b32 v120,1,v120\nv_lshl_add_u32 v120,v121,5,v120\nv_lshl_add_u32 v120,v122,3,v120\nv_xor_b32 v8,0,v120\n" \
"v_add_u32 v8,s57,v8\nv_xor_b32 v9,32,v120\nv_add_u32 v9,s57,v9\nv_xor_b32 v10,64,v120\n" \
"v_add_u32 v10,s57,v10\nv_xor_b32 v11,96,v120\nv_add_u32 v11,s57,v11\nv_cvt_f32_u32 v120,s34\n" \
"v_lshrrev_b32 v48,1,v118\nv_cvt_f32_u32 v48,v48\nv_and_b32 v49,1,v119\nv_cvt_f32_u32 v49,v49\n" \
"v_mul_f32 v48,0xbe000000,v48\nv_mul_f32 v50,0x3b800000,v120\nv_fma_f32 v49,v49,2.0,v50\nv_mul_f32 v49,v48,v49\n" \
"v_lshrrev_b32 v50,1,v119\nv_and_b32 v51,1,v0\nv_cmp_eq_u32 vcc,v50,v51\nv_mov_b32 v50,0x3e800000\n" \
"v_lshlrev_b32 v51,31,v0\nv_cndmask_b32 v52,0,v50,vcc\nv_xor_b32 v54,v51,v52\nv_cos_f32 v56,v49\n" \
"v_sin_f32 v57,v49\nv_fmamk_f32 v63,v48,0x40800000,v49\nv_pk_mul_f32 v[58:59],v[56:57],v[52:53] op_sel_hi:[1,0]\nv_pk_mul_f32 v[60:61],v[56:57],v[54:55] op_sel_hi:[1,0]\n" \
"v_cvt_pk_f16_f32 v40,v58,-v61\nv_cvt_pk_f16_f32 v44,v59,v60\nv_cos_f32 v62,v63\nv_sin_f32 v63,v63\n" \
"v_fmamk_f32 v57,v48,0x3f800000,v49\nv_pk_mul_f32 v[64:65],v[62:63],v[52:53] op_sel_hi:[1,0]\nv_pk_mul_f32 v[66:67],v[62:63],v[54:55] op_sel_hi:[1,0]\nv_cvt_pk_f16_f32 v41,v64,-v67\n" \
"v_cvt_pk_f16_f32 v45,v65,v66\nv_cos_f32 v56,v57\nv_sin_f32 v57,v57\nv_fmamk_f32 v63,v48,0x40a00000,v49\n" \
"v_pk_mul_f32 v[58:59],v[56:57],v[52:53] op_sel_hi:[1,0]\nv_pk_mul_f32 v[60:61],v[56:57],v[54:55] op_sel_hi:[1,0]\nv_cvt_pk_f16_f32 v42,v58,-v61\nv_cvt_pk_f16_f32 v46,v59,v60\n" \
"v_cos_f32 v62,v63\nv_sin_f32 v63,v63\nv_bfe_u32 v100,v0,1,2\nv_pk_mul_f32 v[64:65],v[62:63],v[52:53] op_sel_hi:[1,0]\n" \
"v_pk_mul_f32 v[66:67],v[62:63],v[54:55] op_sel_hi:[1,0]\nv_cvt_pk_f16_f32 v43,v64,-v67\nv_cvt_pk_f16_f32 v47,v65,v66\nv_and_b32 v101,1,v0\n" \
"v_lshl_add_u32 v100,v101,2,v100\nv_cvt_f32_u32 v100,v100\nv_mul_f32 v101,0x3a000000,v120\nv_fmamk_f32 v100,v100,0x3e000000,v101\n" \
"v_cvt_f32_u32 v101,v119\nv_add_f32 v101,v101,v101\nv_mul_f32 v101,v101,v100\nv_cmp_gt_u32 vcc,8,v118\n" \
"v_cos_f32 v106,v101\nv_sin_f32 v107,v101\nv_cndmask_b32 v102,0,0.5,vcc\nv_sub_f32_e64 v104,0.5,v102\n" \
"v_pk_mul_f32 v[108:109],v[106:107],v[102:103] op_sel_hi:[1,0]\nv_pk_mul_f32 v[110:111],v[106:107],v[104:105] op_sel_hi:[1,0]\nv_cvt_pk_f16_f32 v216,v108,-v109\nv_cvt_pk_f16_f32 v220,v109,v108\n" \
"v_cvt_pk_f16_f32 v217,v110,-v111\nv_cvt_pk_f16_f32 v221,-v111,-v110\nv_fmamk_f32 v107,v100,0x3f800000,v101\nv_cos_f32 v106,v107\n" \
"v_sin_f32 v107,v107\ns_nop 0\nv_pk_mul_f32 v[108:109],v[106:107],v[102:103] op_sel_hi:[1,0]\nv_pk_mul_f32 v[110:111],v[106:107],v[104:105] op_sel_hi:[1,0]\n" \
"v_cvt_pk_f16_f32 v218,v108,-v109\nv_cvt_pk_f16_f32 v222,v109,v108\nv_cvt_pk_f16_f32 v219,v110,-v111\nv_cvt_pk_f16_f32 v223,-v111,-v110\n" \
"v_readfirstlane_b32 s28,v126\nv_readfirstlane_b32 s29,v127\ns_add_u32 s28,s28,1\ns_addc_u32 s29,s29,0\n" \
"v_mov_b32 v168,s28\nv_mov_b32 v169,s29\ns_waitcnt vmcnt(13)\nv_and_b32 v149,0xffff0000,v80\n" \
"v_and_b32 v151,0xffff0000,v81\nv_lshlrev_b32 v148,16,v80\nv_lshlrev_b32 v150,16,v81\nv_pk_mul_f32 v[140:141],v[148:149],v[128:129] op_sel_hi:[1,0]\n" \
"v_pk_mul_f32 v[142:143],v[150:151],v[128:129] op_sel_hi:[1,0]\nv_and_b32 v149,0xffff0000,v82\nv_and_b32 v151,0xffff0000,v83\nv_lshlrev_b32 v148,16,v82\n" \
"v_lshlrev_b32 v150,16,v83\nv_pk_mul_f32 v[144:145],v[148:149],v[128:129] op_sel_hi:[1,0]\nv_pk_mul_f32 v[146:147],v[150:151],v[128:129] op_sel_hi:[1,0]\ns_waitcnt vmcnt(12)\n" \
"v_and_b32 v153,0xffff0000,v84\nv_and_b32 v155,0xffff0000,v85\nv_lshlrev_b32 v152,16,v84\nv_lshlrev_b32 v154,16,v85\n" \
"v_pk_fma_f32 v[140:141],v[152:153],v[130:131],v[140:141] op_sel_hi:[1,0,1]\nv_pk_fma_f32 v[142:143],v[154:155],v[130:131],v[142:143] op_sel_hi:[1,0,1]\ns_waitcnt vmcnt(11)\nv_and_b32 v149,0xffff0000,v88\n" \
"v_and_b32 v151,0xffff0000,v89\nv_lshlrev_b32 v148,16,v88\nv_lshlrev_b32 v150,16,v89\nv_and_b32 v153,0xffff0000,v86\n" \
"v_and_b32 v155,0xffff0000,v87\nv_pk_fma_f32 v[140:141],v[148:149],v[132:133],v[140:141] op_sel_hi:[1,0,1]\nv_pk_fma_f32 v[142:143],v[150:151],v[132:133],v[142:143] op_sel_hi:[1,0,1]\nv_lshlrev_b32 v152,16,v86\n" \
"v_lshlrev_b32 v154,16,v87\nv_pk_fma_f32 v[144:145],v[152:153],v[130:131],v[144:145] op_sel_hi:[1,0,1]\nv_pk_fma_f32 v[146:147],v[154:155],v[130:131],v[146:147] op_sel_hi:[1,0,1]\nv_and_b32 v149,0xffff0000,v90\n" \
"v_and_b32 v151,0xffff0000,v91\nv_lshlrev_b32 v148,16,v90\nv_lshlrev_b32 v150,16,v91\nv_pk_fma_f32 v[144:145],v[148:149],v[132:133],v[144:145] op_sel_hi:[1,0,1]\n" \
"v_pk_fma_f32 v[146:147],v[150:151],v[132:133],v[146:147] op_sel_hi:[1,0,1]\ns_waitcnt vmcnt(10)\nv_and_b32 v153,0xffff0000,v92\nv_and_b32 v155,0xffff0000,v93\n" \
"v_lshlrev_b32 v152,16,v92\nv_lshlrev_b32 v154,16,v93\nv_pk_fma_f32 v[140:141],v[152:153],v[134:135],v[140:141] op_sel_hi:[1,0,1]\nv_pk_fma_f32 v[142:143],v[154:155],v[134:135],v[142:143] op_sel_hi:[1,0,1]\n" \
"s_waitcnt vmcnt(9)\nv_and_b32 v149,0xffff0000,v96\nv_and_b32 v151,0xffff0000,v97\nv_lshlrev_b32 v148,16,v96\n" \
"v_lshlrev_b32 v150,16,v97\nv_and_b32 v153,0xffff0000,v94\nv_and_b32 v155,0xffff0000,v95\nv_pk_fma_f32 v[140:141],v[148:149],v[136:137],v[140:141] op_sel_hi:[1,0,1]\n" \
"v_pk_fma_f32 v[142:143],v[150:151],v[136:137],v[142:143] op_sel_hi:[1,0,1]\nv_lshlrev_b32 v152,16,v94\nv_lshlrev_b32 v154,16,v95\nv_pk_fma_f32 v[144:145],v[152:153],v[134:135],v[144:145] op_sel_hi:[1,0,1]\n" \
"v_pk_fma_f32 v[146:147],v[154:155],v[134:135],v[146:147] op_sel_hi:[1,0,1]\nv_and_b32 v149,0xffff0000,v98\nv_and_b32 v151,0xffff0000,v99\nv_lshlrev_b32 v148,16,v98\n" \
"v_lshlrev_b32 v150,16,v99\nv_pk_fma_f32 v[144:145],v[148:149],v[136:137],v[144:145] op_sel_hi:[1,0,1]\nv_pk_fma_f32 v[146:147],v[150:151],v[136:137],v[146:147] op_sel_hi:[1,0,1]\nv_and_b32 v148,3,v0\n" \
"v_permlane32_swap_b32 v140,v144\nv_permlane32_swap_b32 v141,v145\nv_permlane32_swap_b32 v142,v146\nv_permlane32_swap_b32 v143,v147\n" \
"v_pk_add_f32 v[140:141],v[140:141],v[144:145]\nv_pk_add_f32 v[142:143],v[142:143],v[146:147]\nv_lshrrev_b32 v149,4,v0\nv_and_b32 v150,12,v149\n" \
"v_permlane16_swap_b32 v140,v142\nv_permlane16_swap_b32 v141,v143\nv_lshlrev_b32 v148,5,v148\nv_pk_add_f32 v[140:141],v[140:141],v[142:143]\n" \
"v_mad_u32_u24 v148,v150,24,v148\nv_lshl_add_u32 v148,v149,3,v148\nv_add_f32_dpp v140,v140,v140 row_ror:8 row_mask:0xf bank_mask:0xf bound_ctrl:1\nv_add_f32_dpp v141,v141,v141 row_ror:8 row_mask:0xf bank_mask:0xf bound_ctrl:1\n" \
"v_add_u32 v148,161152,v148\nv_add_f32_dpp v140,v140,v140 row_ror:4 row_mask:0xf bank_mask:0xf bound_ctrl:1\nv_add_f32_dpp v141,v141,v141 row_ror:4 row_mask:0xf bank_mask:0xf bound_ctrl:1\nds_write_b64 v148,v[140:141]\n" \
"s_waitcnt lgkmcnt(0)\ns_barrier\nds_read_b64 v[160:161],v158\nds_read_b64 v[162:163],v158 offset:128\n" \
"ds_read_b64 v[164:165],v158 offset:256\nds_read_b64 v[166:167],v158 offset:384\ns_waitcnt lgkmcnt(1)\nv_pk_add_f32 v[160:161],v[160:161],v[162:163]\n" \
"s_waitcnt lgkmcnt(0)\nv_pk_add_f32 v[164:165],v[164:165],v[166:167]\nv_pk_add_f32 v[170:171],v[160:161],v[164:165]\nbuffer_store_dwordx4 v[168:171],v159,s[36:39],s63 offen sc1\n" \
"buffer_load_dwordx4 v2,s[4:7],0 offen lds\ns_add_u32 m0,s88,32512\ns_nop 0\nbuffer_load_dwordx4 v2,s[4:7],s51 offen lds\n" \
"buffer_load_dwordx4 v[234:237],v1,s[4:7],s52 offen\nbuffer_load_dwordx4 v[238:241],v1,s[4:7],s52 offen offset:1024\nbuffer_load_dwordx4 v[242:245],v2,s[4:7],s53 offen\ns_mov_b32 s52,18432\n" \
"buffer_load_dwordx4 v[252:255],v2,s[4:7],s52 offen\nbuffer_store_dwordx4 v[16:19],v2,s[36:39],s35 offen sc1\nbuffer_store_dwordx4 v[16:19],v2,s[36:39],s55 offen sc1\nbuffer_store_dwordx4 v[16:19],v113,s[36:39],0 offen sc1\n" \
"buffer_store_dwordx4 v[20:23],v115,s[36:39],0 offen sc1\nv_lshrrev_b32 v77,5,v0\nv_bfe_u32 v113,v0,1,5\nv_and_b32 v115,1,v0\n" \
"s_lshl_b32 s63,s3,1\nv_mul_u32_u24 v77,1160,v77\nv_and_b32 v76,31,v0\nv_and_b32 v114,6,v113\n" \
"v_add_u32 v115,s63,v115\nv_mad_u32_u24 v78,v76,36,v77\nv_xor_b32 v114,v114,v115\nv_lshlrev_b32 v113,7,v113\n" \
"v_lshl_add_u32 v113,v114,4,v113\ns_add_u32 s64,s54,2916352\ns_add_u32 s65,s64,4096\nv_bfrev_b32 v114,1\n" \
"v_cmp_gt_u32 vcc,72,v0\ns_lshl_b32 s67,s32,2\ns_add_u32 s66,s64,8192\nv_cndmask_b32 v114,v114,v2,vcc\n" \
"s_add_u32 s67,s67,s3\ns_lshl_b32 s67,s67,5\ns_add_u32 s67,s67,s33\ns_lshl_b32 s67,s67,11\n" \
"s_add_u32 s67,s67,5308416\ns_lshl2_add_u32 s69,s3,161664\ns_mul_i32 s72,s3,2320\nv_mov_b32 v251,s69\n" \
"v_cmp_gt_u32 vcc,32,v0\ns_lshl_b32 s7,s32,11\ns_mov_b32 s93,128\nv_cndmask_b32 v74,2.0,0,vcc\n" \
"s_mov_b32 s73,8192\ns_mov_b32 s74,12288\ns_mov_b32 s75,17408\ns_mov_b32 s76,21504\n" \
"s_mov_b32 s77,16384\ns_mov_b32 s78,25600\ns_add_u32 s7,s7,40960\ns_mov_b32 s81,0x3d800000\n" \
"s_mov_b32 s42,18874368\ns_mov_b32 s43,0x20000\ns_waitcnt vmcnt(7)\nv_lshrrev_b32 v96,1,v234\n" \
"v_lshrrev_b32 v97,1,v235\nv_lshrrev_b32 v98,1,v236\nv_lshrrev_b32 v99,1,v237\nv_add_u32 v96,s72,v96\n" \
"s_waitcnt vmcnt(6)\nv_lshrrev_b32 v100,1,v238\nv_lshrrev_b32 v101,1,v239\nv_lshrrev_b32 v102,1,v240\n" \
"v_lshrrev_b32 v103,1,v241\nv_add_u32 v97,s72,v97\nv_add_u32 v98,s72,v98\nv_add_u32 v99,s72,v99\n" \
"v_add_u32 v100,s72,v100\nv_add_u32 v101,s72,v101\nv_add_u32 v102,s72,v102\nv_add_u32 v103,s72,v103\n" \
"v_lshlrev_b32 v48,16,v224\nv_and_b32 v49,0xffff0000,v224\nv_readlane_b32 s70,v48,0\nv_readlane_b32 s71,v48,32\n" \
"v_and_b32 v51,0xffff0000,v225\nv_mov_b32 v72,s70\nv_mov_b32 v73,s71\nv_lshlrev_b32 v50,16,v225\n" \
"v_and_b32 v53,0xffff0000,v226\nv_permlane32_swap_b32 v72,v73\nv_pk_add_f32 v[48:49],v[48:49],v[72:73] op_sel_hi:[1,0] neg_lo:[0,1] neg_hi:[0,1]\nv_pk_add_f32 v[50:51],v[50:51],v[72:73] op_sel_hi:[1,0] neg_lo:[0,1] neg_hi:[0,1]\n" \
"v_pk_mul_f32 v[68:69],v[48:49],v[48:49]\nv_lshlrev_b32_e64 v52,16,v226\nv_and_b32 v55,0xffff0000,v227\nv_pk_add_f32 v[52:53],v[52:53],v[72:73] op_sel_hi:[1,0] neg_lo:[0,1] neg_hi:[0,1]\n" \
"v_pk_add_f32 v[66:67],v[48:49],v[50:51]\nv_pk_fma_f32 v[68:69],v[50:51],v[50:51],v[68:69]\nv_lshlrev_b32_e64 v54,16,v227\nv_and_b32 v57,0xffff0000,v228\n" \
"v_pk_add_f32 v[54:55],v[54:55],v[72:73] op_sel_hi:[1,0] neg_lo:[0,1] neg_hi:[0,1]\nv_pk_add_f32 v[66:67],v[66:67],v[52:53]\nv_pk_fma_f32 v[68:69],v[52:53],v[52:53],v[68:69]\nv_lshlrev_b32_e64 v56,16,v228\n" \
"v_and_b32 v59,0xffff0000,v229\nv_pk_add_f32 v[56:57],v[56:57],v[72:73] op_sel_hi:[1,0] neg_lo:[0,1] neg_hi:[0,1]\nv_pk_add_f32 v[66:67],v[66:67],v[54:55]\nv_pk_fma_f32 v[68:69],v[54:55],v[54:55],v[68:69]\n" \
"v_lshlrev_b32_e64 v58,16,v229\nv_and_b32 v61,0xffff0000,v230\nv_pk_add_f32 v[58:59],v[58:59],v[72:73] op_sel_hi:[1,0] neg_lo:[0,1] neg_hi:[0,1]\nv_pk_add_f32 v[66:67],v[66:67],v[56:57]\n" \
"v_pk_fma_f32 v[68:69],v[56:57],v[56:57],v[68:69]\nv_lshlrev_b32_e64 v60,16,v230\nv_and_b32 v63,0xffff0000,v231\nv_pk_add_f32 v[60:61],v[60:61],v[72:73] op_sel_hi:[1,0] neg_lo:[0,1] neg_hi:[0,1]\n" \
"v_pk_add_f32 v[66:67],v[66:67],v[58:59]\nv_pk_fma_f32 v[68:69],v[58:59],v[58:59],v[68:69]\nv_lshlrev_b32_e64 v62,16,v231\nv_and_b32 v65,0xffff0000,v232\n" \
"v_pk_add_f32 v[62:63],v[62:63],v[72:73] op_sel_hi:[1,0] neg_lo:[0,1] neg_hi:[0,1]\nv_pk_add_f32 v[66:67],v[66:67],v[60:61]\nv_pk_fma_f32 v[68:69],v[60:61],v[60:61],v[68:69]\nv_lshlrev_b32_e64 v64,16,v232\n" \
"v_pk_add_f32 v[64:65],v[64:65],v[72:73] op_sel_hi:[1,0] neg_lo:[0,1] neg_hi:[0,1]\nv_pk_add_f32 v[66:67],v[66:67],v[62:63]\nv_pk_fma_f32 v[68:69],v[62:63],v[62:63],v[68:69]\nv_pk_add_f32 v[66:67],v[66:67],v[64:65]\n" \
"v_pk_fma_f32 v[68:69],v[64:65],v[64:65],v[68:69]\nv_add_f32 v66,v66,v67\nv_add_f32 v68,v68,v69\ns_mov_b64 s[40:41],s[44:45]\n" \
"s_mov_b64 s[44:45],s[46:47]\nv_permlane16_swap_b32 v66,v68\nv_add_f32 v66,v66,v68\ns_mov_b32 s46,2359296\n" \
"s_mov_b32 s47,0x20000\nv_add_f32_dpp v66,v66,v66 quad_perm:[1,0,3,2] row_mask:0xf bank_mask:0xf bound_ctrl:1\ns_nop 1\nv_add_f32_dpp v66,v66,v66 quad_perm:[2,3,0,1] row_mask:0xf bank_mask:0xf bound_ctrl:1\n" \
"s_nop 1\nv_add_f32_dpp v66,v66,v66 row_half_mirror row_mask:0xf bank_mask:0xf bound_ctrl:1\ns_nop 1\nv_add_f32_dpp v66,v66,v66 row_mirror row_mask:0xf bank_mask:0xf bound_ctrl:1\n" \
"v_mul_f32 v70,0x3ae38e39,v66\nv_mul_f32 v68,0x3ae38e39,v66\ns_nop 1\nv_permlane16_swap_b32 v70,v68\n" \
"v_fma_f32 v68,-v70,v70,v68\nv_max_f32 v68,0,v68\nv_add_f32 v68,0x3727c5ac,v68\nv_rsq_f32 v66,v68\n" \
"v_add_f32 v250,v70,v72\nv_mul_f32 v67,-v70,v66\nv_pk_fma_f32 v[48:49],v[48:49],v[66:67],v[66:67] op_sel:[0,0,1] op_sel_hi:[1,0,1]\nv_pk_fma_f32 v[50:51],v[50:51],v[66:67],v[66:67] op_sel:[0,0,1] op_sel_hi:[1,0,1]\n" \
"v_cvt_pk_f16_f32 v80,v48,v49\nv_cvt_pk_f16_f32 v81,v50,v51\nv_pk_fma_f32 v[52:53],v[52:53],v[66:67],v[66:67] op_sel:[0,0,1] op_sel_hi:[1,0,1]\nv_pk_fma_f32 v[54:55],v[54:55],v[66:67],v[66:67] op_sel:[0,0,1] op_sel_hi:[1,0,1]\n" \
"ds_write2_b32 v78,v80,v81 offset0:0 offset1:1\nv_cvt_pk_f16_f32 v82,v52,v53\nv_cvt_pk_f16_f32 v83,v54,v55\nv_pk_fma_f32 v[56:57],v[56:57],v[66:67],v[66:67] op_sel:[0,0,1] op_sel_hi:[1,0,1]\n" \
"v_pk_fma_f32 v[58:59],v[58:59],v[66:67],v[66:67] op_sel:[0,0,1] op_sel_hi:[1,0,1]\nds_write2_b32 v78,v82,v83 offset0:2 offset1:3\nv_cvt_pk_f16_f32 v84,v56,v57\nv_cvt_pk_f16_f32 v85,v58,v59\n" \
"v_pk_fma_f32 v[60:61],v[60:61],v[66:67],v[66:67] op_sel:[0,0,1] op_sel_hi:[1,0,1]\nv_pk_fma_f32 v[62:63],v[62:63],v[66:67],v[66:67] op_sel:[0,0,1] op_sel_hi:[1,0,1]\nds_write2_b32 v78,v84,v85 offset0:4 offset1:5\nv_cvt_pk_f16_f32 v86,v60,v61\n" \
"v_cvt_pk_f16_f32 v87,v62,v63\nv_pk_fma_f32 v[64:65],v[64:65],v[66:67],v[66:67] op_sel:[0,0,1] op_sel_hi:[1,0,1]\nds_write2_b32 v78,v86,v87 offset0:6 offset1:7\nv_cvt_pk_f16_f32 v88,v64,v65\n" \
"ds_write_b32 v78,v88 offset:32\ns_barrier\nds_read_u16 v16,v246\nds_read_u16 v17,v246 offset:5216\n" \
"ds_read_u16 v18,v246 offset:578\nds_read_u16 v19,v246 offset:5794\nds_read_u16 v20,v246 offset:1156\nds_read_u16 v21,v246 offset:6372\n" \
"ds_read_u16 v22,v246 offset:1734\nds_read_u16 v23,v246 offset:6950\nds_read_u16 v24,v247\ns_waitcnt lgkmcnt(7)\n" \
"v_lshl_or_b32 v16,v17,16,v16\nds_read_u16 v25,v247 offset:5216\nds_read_u16 v26,v247 offset:578\ns_waitcnt lgkmcnt(7)\n" \
"v_lshl_or_b32 v17,v19,16,v18\nv_pk_maximum3_f16 v124,v16,v17,v16\nds_read_u16 v27,v247 offset:5794\nv_pk_minimum3_f16 v125,v16,v17,v16\n" \
"ds_read_u16 v28,v247 offset:1156\ns_waitcnt lgkmcnt(7)\nv_lshl_or_b32 v18,v21,16,v20\nds_read_u16 v29,v247 offset:6372\n" \
"ds_read_u16 v30,v247 offset:1734\ns_waitcnt lgkmcnt(7)\nv_lshl_or_b32 v19,v23,16,v22\nbuffer_store_dwordx4 v[16:19],v2,s[36:39],s64 offen sc1\n" \
"ds_read_u16 v31,v247 offset:6950\nds_read_u16 v104,v248\nv_pk_maximum3_f16 v124,v18,v19,v124\nv_pk_minimum3_f16 v125,v18,v19,v125\n" \
"s_waitcnt lgkmcnt(7)\nv_lshl_or_b32 v24,v25,16,v24\nds_read_u16 v105,v248 offset:5216\nds_read_u16 v106,v248 offset:578\n" \
"s_waitcnt lgkmcnt(7)\nv_lshl_or_b32 v25,v27,16,v26\nv_pk_maximum3_f16 v124,v24,v25,v124\nds_read_u16 v107,v248 offset:5794\n" \
"v_pk_minimum3_f16 v125,v24,v25,v125\nds_read_u16 v108,v248 offset:1156\ns_waitcnt lgkmcnt(7)\nv_lshl_or_b32 v26,v29,16,v28\n" \
"ds_read_u16 v109,v248 offset:6372\nds_read_u16 v110,v248 offset:1734\ns_waitcnt lgkmcnt(7)\nv_lshl_or_b32 v27,v31,16,v30\n" \
"buffer_store_dwordx4 v[24:27],v2,s[36:39],s65 offen sc1\nds_read_u16 v111,v248 offset:6950\nds_read_b128 v[116:119],v113 offset:20224\nv_pk_maximum3_f16 v124,v26,v27,v124\n" \
"v_pk_minimum3_f16 v125,v26,v27,v125\nds_read_b128 v[120:123],v113 offset:24320\ns_waitcnt lgkmcnt(8)\nv_lshl_or_b32 v104,v105,16,v104\n" \
"ds_read_b32 v16,v96\nds_read_b32 v24,v96 offset:1160\nds_read_b32 v17,v97\nds_read_b32 v25,v97 offset:1160\n" \
"s_waitcnt lgkmcnt(10)\nv_lshl_or_b32 v105,v107,16,v106\nds_read_b32 v18,v98\nv_pk_maximum3_f16 v124,v104,v105,v124\n" \
"ds_read_b32 v26,v98 offset:1160\nv_pk_minimum3_f16 v125,v104,v105,v125\nds_read_b32 v19,v99\nds_read_b32 v27,v99 offset:1160\n" \
"ds_read_b32 v20,v100\ns_waitcnt lgkmcnt(13)\nv_lshl_or_b32 v106,v109,16,v108\nds_read_b32 v28,v100 offset:1160\n" \
"ds_read_b32 v21,v101\nds_read_b32 v29,v101 offset:1160\ns_waitcnt lgkmcnt(14)\nv_lshl_or_b32 v107,v111,16,v110\n" \
"buffer_store_dwordx4 v[104:107],v114,s[36:39],s66 offen sc1\ns_waitcnt lgkmcnt(12)\nbuffer_store_dwordx4 v[116:119],v1,s[36:39],s67 offen sc1\nbuffer_store_dwordx4 v[120:123],v1,s[36:39],s67 offen offset:1024 sc1\n" \
"ds_read_b32 v22,v102\nds_read_b32 v30,v102 offset:1160\nds_read_b32 v23,v103\nds_read_b32 v31,v103 offset:1160\n" \
"v_pk_maximum3_f16 v124,v106,v107,v124\nv_and_b32 v124,0x7fff7fff,v124\nv_pk_minimum3_f16 v125,v106,v107,v125\nv_and_b32 v125,0x7fff7fff,v125\n" \
"v_pk_max_u16 v124,v124,v125\nv_lshlrev_b32 v125,16,v124\nv_max_u32 v124,v124,v125\ns_nop 1\n" \
"v_max_u32_dpp v124,v124,v124 quad_perm:[1,0,3,2] row_mask:0xf bank_mask:0xf bound_ctrl:1\ns_nop 1\nv_max_u32_dpp v124,v124,v124 quad_perm:[2,3,0,1] row_mask:0xf bank_mask:0xf bound_ctrl:1\ns_nop 1\n" \
"v_max_u32_dpp v124,v124,v124 row_half_mirror row_mask:0xf bank_mask:0xf bound_ctrl:1\ns_nop 1\nv_max_u32_dpp v124,v124,v124 row_mirror row_mask:0xf bank_mask:0xf bound_ctrl:1\ns_nop 0\n" \
"v_readlane_b32 s68,v124,0\nv_readlane_b32 s100,v124,16\nv_readlane_b32 s101,v124,32\nv_readlane_b32 s31,v124,48\n" \
"s_max_u32 s100,s100,s101\ns_max_u32 s68,s68,s31\ns_max_u32 s68,s68,s100\nv_mov_b32_e64 v124,s68\n" \
"ds_write_b32 v251,v124 offset:32\nv_mov_b32 v114,161664\nbuffer_load_dwordx4 v[48:51],v2,s[36:39],s73 offen sc1\nbuffer_load_dwordx4 v[60:63],v2,s[36:39],s75 offen sc1\n" \
"buffer_load_dwordx4 v[52:55],v2,s[36:39],s74 offen sc1\nbuffer_load_dwordx4 v[64:67],v2,s[36:39],s76 offen sc1\nbuffer_load_dwordx4 v[56:59],v1,s[36:39],s77 offen sc1\ns_lshl3_add_u32 s80,s2,4294615040\n" \
"buffer_load_dwordx4 v[68:71],v1,s[36:39],s78 offen sc1\nv_and_b32 v113,15,v0\ns_mov_b32 s79,0xffffff\nv_bfrev_b32 v115,1\n" \
"s_add_u32 s70,s35,s88\ns_add_u32 s72,s70,8192\ns_cmp_lt_u32 s3,2\ns_cselect_b32 s72,s72,2147483648\n" \
"v_add_u32 v112,28416,v1\ns_add_u32 s71,s70,4096\ns_waitcnt vmcnt(6) lgkmcnt(0)\ns_barrier\n" \
"ds_read_b128 v[116:119],v114 offset:32\nds_read_b128 v[192:195],v112\nds_read_b128 v[196:199],v112 offset:1024\nds_read_b128 v[200:203],v112 offset:2048\n" \
"ds_read_b128 v[204:207],v112 offset:3072\nds_read_b128 v[208:211],v112 offset:4096\nds_read_b128 v[212:215],v112 offset:5120\nds_read_b128 v[32:35],v112 offset:6144\n" \
"ds_read_b128 v[36:39],v112 offset:7168\ns_waitcnt lgkmcnt(8)\nv_max3_u32 v116,v116,v117,v118\nv_max_u32 v116,v116,v119\n" \
"v_and_b32 v119,0xf8,v0\nv_cmp_ne_u32 vcc,192,v119\nv_lshlrev_b32 v118,11,v0\nv_add_u32 v118,s80,v118\n" \
"v_bfrev_b32 v119,1\nv_cndmask_b32 v118,v118,v119,vcc\nv_lshlrev_b32 v116,1,v116\nv_mov_b32 v117,s29\n" \
"v_cmp_eq_u32 vcc,0,v113\nv_bfi_b32 v117,s79,v117,v116\nv_mov_b32 v116,s28\nv_cndmask_b32 v115,v115,v3,vcc\n" \
"v_cmp_gt_u32 vcc,64,v0\nbuffer_store_dwordx2 v[116:117],v118,s[36:39],0 offen sc1\ns_nop 0\nv_cndmask_b32 v115,v115,v3,vcc\n" \
"s_waitcnt vmcnt(6)\nv_cmp_ne_u64 vcc,s[28:29],v[48:49]\ns_waitcnt vmcnt(4)\nv_cmp_ne_u64 s[84:85],s[28:29],v[52:53]\n" \
"s_waitcnt vmcnt(2)\nv_cmp_ne_u64 s[82:83],s[28:29],v[56:57]\ns_or_b64 vcc,vcc,s[84:85]\nv_cmp_ne_u64 s[84:85],s[28:29],v[60:61]\n" \
"s_or_b64 vcc,vcc,s[82:83]\nv_cmp_ne_u64 s[82:83],s[28:29],v[64:65]\ns_or_b64 vcc,vcc,s[84:85]\ns_waitcnt vmcnt(1)\n" \
"v_cmp_ne_u64 s[84:85],s[28:29],v[68:69]\ns_or_b64 vcc,vcc,s[82:83]\ns_or_b64 vcc,vcc,s[84:85]\ns_cbranch_vccnz .LsmP_%=\n" \
".LbmP_%=:\nv_pk_add_f32 v[50:51],v[50:51],v[62:63]\nv_pk_add_f32 v[54:55],v[54:55],v[66:67]\nv_pk_mul_f32 v[62:63],v[50:51],2.0 op_sel_hi:[1,0]\n" \
"v_pk_add_f32 v[58:59],v[58:59],v[70:71]\nv_max3_f32 v112,|v62|,|v63|,0\nv_pk_fma_f32 v[66:67],v[54:55],2.0,v[74:75] op_sel_hi:[1,0,0]\nv_max3_f32 v112,|v66|,|v67|,v112\n" \
"v_pk_fma_f32 v[70:71],v[58:59],2.0,2.0 op_sel_hi:[1,0,0]\nv_max3_f32 v112,|v70|,|v71|,v112\nv_cmp_lt_f32 s[82:83],s81,v112\nv_cmp_ngt_f32 vcc,0x41000000,v112\n" \
"s_cmp_lg_u64 s[82:83],0\ns_cselect_b32 s82,2,0\nv_cvt_pk_f16_f32 v62,v62,v63\ns_cmp_lg_u64 vcc,0\n" \
"s_addc_u32 s82,s82,0\nds_write_b32 v252,v62 offset:62848\nv_cvt_pk_f16_f32 v66,v66,v67\nds_write_b32 v253,v66 offset:62848\n" \
"v_cvt_pk_f16_f32 v70,v70,v71\nv_mov_b32 v113,s82\nds_write_b32 v254,v70 offset:62848\nds_write_b32 v251,v113\n" \
"s_waitcnt lgkmcnt(0)\ns_barrier\nds_read_b128 v[124:127],v114\nds_read_b64 v[96:97],v234 offset:62848\n" \
"ds_read_b64 v[98:99],v235 offset:62848\nds_read_b64 v[100:101],v236 offset:62848\nds_read_b64 v[102:103],v237 offset:62848\nds_read_b64 v[104:105],v238 offset:62848\n" \
"ds_read_b64 v[106:107],v239 offset:62848\nds_read_b64 v[108:109],v240 offset:62848\nds_read_b64 v[110:111],v241 offset:62848\ns_waitcnt lgkmcnt(7)\n" \
"v_or3_b32 v124,v124,v125,v126\nv_or_b32 v124,v124,v127\nv_cmp_ne_u32 vcc,2,v124\ns_nop 0\n" \
"s_cbranch_vccnz .LsxP_%=\n.LbxP_%=:\nv_pk_fma_f16 v80,v16,v96,v97\ns_waitcnt lgkmcnt(4)\n" \
"v_pk_fma_f16 v81,v17,v98,v99\nv_pk_fma_f16 v82,v18,v100,v101\nv_pk_fma_f16 v83,v19,v102,v103\nv_pk_fma_f16 v88,v24,v96,v97\n" \
"s_waitcnt lgkmcnt(2)\nv_pk_fma_f16 v84,v20,v104,v105\nv_pk_fma_f16 v85,v21,v106,v107\nv_pk_fma_f16 v89,v25,v98,v99\n" \
"s_waitcnt lgkmcnt(0)\nv_pk_fma_f16 v86,v22,v108,v109\nv_pk_fma_f16 v90,v26,v100,v101\nv_pk_fma_f16 v87,v23,v110,v111\n" \
"v_pk_fma_f16 v91,v27,v102,v103\nv_pk_fma_f16 v92,v28,v104,v105\nv_pk_fma_f16 v93,v29,v106,v107\nv_pk_fma_f16 v94,v30,v108,v109\n" \
"v_bfe_u32 v233,v0,3,3\nv_mul_u32_u24 v233,8192,v233\nv_lshl_add_u32 v233,v1,3,v233\ns_lshl2_add_u32 s96,s33,s3\n" \
"s_add_u32 s96,s96,138\ns_and_b32 s97,s96,31\ns_lshl_b32 s97,s97,3\ns_cmp_lt_u32 s96,256\n" \
"s_cselect_b32 s96,s96,0x40000\ns_lshr_b32 s96,s96,5\ns_add_u32 s96,s96,s97\ns_lshl_b32 s96,s96,6\n" \
"s_add_u32 s96,s96,s32\ns_mul_i32 s96,s96,1152\ns_mov_b32 m0,162176\nv_pk_fma_f16 v95,v31,v110,v111\n" \
"buffer_load_dword v233,s[8:11],s96 offen lds\nv_mfma_f32_16x16x32_f16 v[48:51],v[80:83],v[192:195],0\nv_mfma_f32_16x16x32_f16 v[52:55],v[80:83],v[196:199],0\nv_mfma_f32_16x16x32_f16 v[56:59],v[84:87],v[32:35],0\n" \
"v_mfma_f32_16x16x32_f16 v[60:63],v[84:87],v[36:39],0\nv_mfma_f32_16x16x32_f16 v[64:67],v[88:91],v[192:195],0\nv_mfma_f32_16x16x32_f16 v[68:71],v[88:91],v[196:199],0\nv_mfma_f32_16x16x32_f16 v[72:75],v[92:95],v[32:35],0\n" \
"v_mfma_f32_16x16x32_f16 v[76:79],v[92:95],v[36:39],0\ns_nop 2\nv_pk_add_f32 v[56:57],v[48:49],v[56:57] neg_lo:[0,1] neg_hi:[0,1]\nv_sub_f32 v58,v50,v58\n" \
"v_pk_fma_f32 v[48:49],v[48:49],2.0,v[56:57] op_sel_hi:[1,0,1] neg_lo:[0,0,1] neg_hi:[0,0,1]\nv_fma_f32 v50,v50,2.0,-v58\nv_pk_add_f32 v[60:61],v[52:53],v[60:61] neg_lo:[0,1] neg_hi:[0,1]\nv_sub_f32 v62,v54,v62\n" \
"v_pk_fma_f32 v[52:53],v[52:53],2.0,v[60:61] op_sel_hi:[1,0,1] neg_lo:[0,0,1] neg_hi:[0,0,1]\nv_pk_add_f32 v[72:73],v[64:65],v[72:73] neg_lo:[0,1] neg_hi:[0,1]\nv_pk_add_f32 v[76:77],v[68:69],v[76:77] neg_lo:[0,1] neg_hi:[0,1]\nv_sub_f32 v74,v66,v74\n" \
"v_sub_f32 v78,v70,v78\nv_fma_f32 v54,v54,2.0,-v62\nv_pk_fma_f32 v[64:65],v[64:65],2.0,v[72:73] op_sel_hi:[1,0,1] neg_lo:[0,0,1] neg_hi:[0,0,1]\nv_fma_f32 v66,v66,2.0,-v74\n" \
"v_pk_fma_f32 v[68:69],v[68:69],2.0,v[76:77] op_sel_hi:[1,0,1] neg_lo:[0,0,1] neg_hi:[0,0,1]\nv_fma_f32 v70,v70,2.0,-v78\nv_cvt_pk_f16_f32 v16,v48,v52\nv_cvt_pk_f16_f32 v17,v49,v53\n" \
"v_cvt_pk_f16_f32 v18,v50,v54\nv_cvt_pk_f16_f32 v24,v64,v68\nv_cvt_pk_f16_f32 v25,v65,v69\nv_cvt_pk_f16_f32 v26,v66,v70\n" \
"v_mfma_f32_16x16x32_f16 v[48:51],v[200:203],v[16:19],0\nv_cvt_pk_f16_f32 v20,v56,v60\nv_cvt_pk_f16_f32 v21,v57,v61\nv_mfma_f32_16x16x32_f16 v[52:55],v[204:207],v[16:19],0\n" \
"v_cvt_pk_f16_f32 v22,v58,v62\nv_cvt_pk_f16_f32 v28,v72,v76\nv_mfma_f32_16x16x32_f16 v[64:67],v[200:203],v[24:27],0\nv_cvt_pk_f16_f32 v29,v73,v77\n" \
"v_cvt_pk_f16_f32 v30,v74,v78\nv_mfma_f32_16x16x32_f16 v[68:71],v[204:207],v[24:27],0\nv_mfma_f32_16x16x32_f16 v[56:59],v[200:203],v[20:23],0\ns_nop 0\n" \
"v_cvt_pk_f16_f32 v80,v48,v52\nv_mfma_f32_16x16x32_f16 v[60:63],v[204:207],v[20:23],0\nv_cvt_pk_f16_f32 v82,v49,v53\nv_cvt_pk_f16_f32 v84,v50,v54\n" \
"v_mfma_f32_16x16x32_f16 v[72:75],v[200:203],v[28:31],0\nv_mfma_f32_16x16x32_f16 v[76:79],v[204:207],v[28:31],0\nv_cvt_pk_f16_f32 v81,v64,v68\nds_write_b64 v255,v[80:81]\n" \
"v_cvt_pk_f16_f32 v83,v65,v69\nds_write_b64 v255,v[82:83] offset:512\nv_cvt_pk_f16_f32 v85,v66,v70\nds_write_b64 v255,v[84:85] offset:1024\n" \
"v_cvt_pk_f16_f32 v86,v56,v60\nv_cvt_pk_f16_f32 v88,v57,v61\nv_cvt_pk_f16_f32 v87,v72,v76\nds_write_b64 v255,v[86:87] offset:256\n" \
"v_cvt_pk_f16_f32 v89,v73,v77\nds_write_b64 v255,v[88:89] offset:768\nv_cvt_pk_f16_f32 v90,v58,v62\nv_cvt_pk_f16_f32 v91,v74,v78\n" \
"ds_write_b64 v255,v[90:91] offset:1280\ns_waitcnt lgkmcnt(0)\ns_barrier\nds_read_b128 v[16:19],v243\n" \
"ds_read_b128 v[20:23],v244\nds_read_b128 v[24:27],v245\ns_waitcnt lgkmcnt(2)\nv_mfma_f32_16x16x32_f16 v[48:51],v[40:43],v[16:19],0\n" \
"v_mfma_f32_16x16x32_f16 v[52:55],v[44:47],v[16:19],0\ns_waitcnt lgkmcnt(0)\nv_mfma_f32_16x16x32_f16 v[56:59],v[40:43],v[20:23],0\nv_mfma_f32_16x16x32_f16 v[60:63],v[44:47],v[20:23],0\n" \
"v_mfma_f32_16x16x32_f16 v[64:67],v[40:43],v[24:27],0\nv_mfma_f32_16x16x32_f16 v[68:71],v[44:47],v[24:27],0\ns_nop 2\nv_cvt_pk_f16_f32 v80,v48,v52\n" \
"v_and_b32_e64 v80,-2,v80\nv_cvt_pk_f16_f32 v81,v49,v53\nv_cvt_pk_f16_f32 v82,v50,v54\nv_cvt_pk_f16_f32 v83,v51,v55\n" \
"buffer_store_dwordx4 v[80:83],v3,s[36:39],s70 offen sc1\nv_cvt_pk_f16_f32 v84,v56,v60\nv_cvt_pk_f16_f32 v85,v57,v61\nv_cvt_pk_f16_f32 v86,v58,v62\n" \
"v_cvt_pk_f16_f32 v87,v59,v63\nv_and_b32_e64 v84,-2,v84\nbuffer_store_dwordx4 v[84:87],v3,s[36:39],s71 offen sc1\nv_cvt_pk_f16_f32 v88,v64,v68\n" \
"v_cvt_pk_f16_f32 v89,v65,v69\nv_cvt_pk_f16_f32 v90,v66,v70\nv_cvt_pk_f16_f32 v91,v67,v71\nv_and_b32 v88,-2,v88\n" \
"buffer_store_dwordx4 v[88:91],v115,s[36:39],s72 offen sc1\ns_cmp_lt_u32 s3,2\ns_cselect_b32 s18,s72,s70\ns_mov_b32 s12,s88\n" \
"s_mov_b32 s13,s89\ns_mov_b32 s14,s87\ns_mov_b32 s16,s70\ns_mov_b32 s17,s71\n" \
"s_load_dwordx2 s[20:21],s[0:1],32\ns_load_dwordx2 s[24:25],s[0:1],40\ns_mov_b32 s22,1152\ns_mov_b32 s26,3981312\n" \
"s_lshr_b32 s95,s86,2\ns_add_u32 s95,s95,2304\ns_waitcnt lgkmcnt(0)\nv_lshrrev_b32 v120,2,v0\n" \
"v_and_b32 v121,3,v0\nv_mul_u32_u24 v122,6912,v120\nv_lshlrev_b32 v123,4,v120\nv_lshl_add_u32 v121,v121,4,s95\n" \
"buffer_load_dwordx4 v[112:115],v123,s[20:23],0 offen\nv_lshlrev_b32 v120,1,v120\nv_lshl_add_u32 v123,v122,3,v121\nbuffer_load_ushort v116,v120,s[20:23],0 offen offset:1024\n" \
"v_add_u32_e64 v121,v121,v122\nbuffer_load_dwordx4 v[16:19],v123,s[24:27],0 offen\ns_mov_b32 s95,6912\nbuffer_load_dwordx4 v[20:23],v123,s[24:27],s95 offen\n" \
"s_mov_b32 s95,13824\nbuffer_load_dwordx4 v[24:27],v123,s[24:27],s95 offen\ns_mov_b32 s95,20736\nbuffer_load_dwordx4 v[28:31],v123,s[24:27],s95 offen\n" \
"s_mov_b32 s95,27648\nbuffer_load_dwordx4 v[32:35],v123,s[24:27],s95 offen\ns_mov_b32 s95,34560\nbuffer_load_dwordx4 v[36:39],v123,s[24:27],s95 offen\n" \
"s_mov_b32 s95,41472\nbuffer_load_dwordx4 v[40:43],v123,s[24:27],s95 offen\ns_mov_b32 s95,48384\nbuffer_load_dwordx4 v[44:47],v123,s[24:27],s95 offen\n" \
"s_mov_b32 s95,3538944\nbuffer_load_dwordx4 v[48:51],v121,s[24:27],s95 offen\ns_lshr_b32 s26,s32,1\ns_cmp_eq_u32 s26,2\n" \
"s_cselect_b32 s26,1.0,0\nv_cmp_gt_u32 vcc,8,v0\nv_mov_b32 v100,s28\nv_mov_b32 v101,s29\n" \
"s_waitcnt vmcnt(10)\nv_lshlrev_b32 v52,16,v112\nv_mul_f32 v53,0xbfb8aa3b,v52\nv_exp_f32 v53,v53\n" \
"v_and_b32 v54,0xffff0000,v112\nv_add_f32 v53,1.0,v53\nv_mul_f32 v55,0xbfb8aa3b,v54\nv_lshlrev_b32 v56,16,v113\n" \
"v_rcp_f32 v53,v53\nv_exp_f32 v55,v55\nv_mul_f32 v57,0xbfb8aa3b,v56\nv_exp_f32 v57,v57\n" \
"v_mul_f32 v52,v52,v53\nv_add_f32 v55,1.0,v55\nv_and_b32 v58,0xffff0000,v113\nv_cvt_pk_bf16_f32 v52,v52,v52\n" \
"v_rcp_f32 v55,v55\nv_add_f32 v57,1.0,v57\nv_mul_f32 v59,0xbfb8aa3b,v58\nv_lshlrev_b32 v52,16,v52\n" \
"v_lshlrev_b32 v60,16,v114\nv_rcp_f32 v57,v57\nv_exp_f32 v59,v59\nv_mul_f32 v61,0xbfb8aa3b,v60\n" \
"v_mul_f32 v54,v54,v55\nv_exp_f32 v61,v61\nv_cvt_pk_bf16_f32 v54,v54,v54\nv_mul_f32 v56,v56,v57\n" \
"v_add_f32 v59,1.0,v59\nv_and_b32 v62,0xffff0000,v114\nv_lshlrev_b32 v54,16,v54\nv_cvt_pk_bf16_f32 v56,v56,v56\n" \
"v_rcp_f32 v59,v59\nv_add_f32 v61,1.0,v61\nv_mul_f32 v63,0xbfb8aa3b,v62\nv_lshlrev_b32 v56,16,v56\n" \
"v_lshlrev_b32 v64,16,v115\nv_rcp_f32 v61,v61\nv_exp_f32 v63,v63\nv_mul_f32 v65,0xbfb8aa3b,v64\n" \
"v_mul_f32 v58,v58,v59\nv_exp_f32 v65,v65\nv_cvt_pk_bf16_f32 v58,v58,v58\nv_mul_f32 v60,v60,v61\n" \
"v_add_f32 v63,1.0,v63\nv_and_b32 v66,0xffff0000,v115\nv_lshlrev_b32 v58,16,v58\nv_cvt_pk_bf16_f32 v60,v60,v60\n" \
"v_rcp_f32 v63,v63\nv_add_f32 v65,1.0,v65\nv_mul_f32 v67,0xbfb8aa3b,v66\nv_lshlrev_b32 v60,16,v60\n" \
"s_waitcnt vmcnt(9)\nv_lshlrev_b32 v68,16,v116\nv_rcp_f32 v65,v65\nv_exp_f32 v67,v67\n" \
"v_mul_f32 v69,0xbfb8aa3b,v68\nv_mul_f32 v62,v62,v63\nv_exp_f32 v69,v69\nv_cvt_pk_bf16_f32 v62,v62,v62\n" \
"v_mul_f32 v64,v64,v65\nv_add_f32 v67,1.0,v67\nv_lshlrev_b32 v62,16,v62\nv_cvt_pk_bf16_f32 v64,v64,v64\n" \
"v_rcp_f32 v67,v67\nv_add_f32 v69,1.0,v69\nv_lshlrev_b32 v64,16,v64\nv_rcp_f32 v69,v69\n" \
"v_mul_f32 v66,v66,v67\nv_cvt_pk_bf16_f32 v66,v66,v66\nv_mul_f32 v68,v68,v69\nv_lshlrev_b32 v66,16,v66\n" \
"v_cvt_pk_bf16_f32 v68,v68,v68\nv_lshlrev_b32 v68,16,v68\ns_waitcnt vmcnt(8)\nv_and_b32 v81,0xffff0000,v16\n" \
"v_and_b32 v83,0xffff0000,v17\nv_lshlrev_b32 v80,16,v16\nv_lshlrev_b32_e64 v82,16,v17\nv_pk_mul_f32 v[70:71],v[80:81],v[52:53] op_sel_hi:[1,0]\n" \
"v_pk_mul_f32 v[72:73],v[82:83],v[52:53] op_sel_hi:[1,0]\nv_and_b32 v81,0xffff0000,v18\nv_and_b32 v83,0xffff0000,v19\nv_lshlrev_b32 v80,16,v18\n" \
"v_lshlrev_b32 v82,16,v19\nv_pk_mul_f32 v[74:75],v[80:81],v[52:53] op_sel_hi:[1,0]\nv_pk_mul_f32 v[76:77],v[82:83],v[52:53] op_sel_hi:[1,0]\ns_waitcnt vmcnt(7)\n" \
"v_and_b32 v85,0xffff0000,v20\nv_and_b32 v87,0xffff0000,v21\nv_lshlrev_b32 v84,16,v20\nv_lshlrev_b32 v86,16,v21\n" \
"v_pk_fma_f32 v[70:71],v[84:85],v[54:55],v[70:71] op_sel_hi:[1,0,1]\nv_pk_fma_f32 v[72:73],v[86:87],v[54:55],v[72:73] op_sel_hi:[1,0,1]\ns_waitcnt vmcnt(6)\nv_and_b32 v81,0xffff0000,v24\n" \
"v_and_b32 v83,0xffff0000,v25\nv_lshlrev_b32 v80,16,v24\nv_lshlrev_b32 v82,16,v25\nv_and_b32 v85,0xffff0000,v22\n" \
"v_and_b32 v87,0xffff0000,v23\nv_pk_fma_f32 v[70:71],v[80:81],v[56:57],v[70:71] op_sel_hi:[1,0,1]\nv_pk_fma_f32 v[72:73],v[82:83],v[56:57],v[72:73] op_sel_hi:[1,0,1]\nv_lshlrev_b32 v84,16,v22\n" \
"v_lshlrev_b32 v86,16,v23\nv_pk_fma_f32 v[74:75],v[84:85],v[54:55],v[74:75] op_sel_hi:[1,0,1]\nv_pk_fma_f32 v[76:77],v[86:87],v[54:55],v[76:77] op_sel_hi:[1,0,1]\nv_and_b32 v81,0xffff0000,v26\n" \
"v_and_b32 v83,0xffff0000,v27\nv_lshlrev_b32 v80,16,v26\nv_lshlrev_b32 v82,16,v27\nv_pk_fma_f32 v[74:75],v[80:81],v[56:57],v[74:75] op_sel_hi:[1,0,1]\n" \
"v_pk_fma_f32 v[76:77],v[82:83],v[56:57],v[76:77] op_sel_hi:[1,0,1]\ns_waitcnt vmcnt(5)\nv_and_b32 v85,0xffff0000,v28\nv_and_b32 v87,0xffff0000,v29\n" \
"v_lshlrev_b32 v84,16,v28\nv_lshlrev_b32 v86,16,v29\nv_pk_fma_f32 v[70:71],v[84:85],v[58:59],v[70:71] op_sel_hi:[1,0,1]\nv_pk_fma_f32 v[72:73],v[86:87],v[58:59],v[72:73] op_sel_hi:[1,0,1]\n" \
"s_waitcnt vmcnt(4)\nv_and_b32 v81,0xffff0000,v32\nv_and_b32 v83,0xffff0000,v33\nv_lshlrev_b32 v80,16,v32\n" \
"v_lshlrev_b32 v82,16,v33\nv_and_b32 v85,0xffff0000,v30\nv_and_b32 v87,0xffff0000,v31\nv_pk_fma_f32 v[70:71],v[80:81],v[60:61],v[70:71] op_sel_hi:[1,0,1]\n" \
"v_pk_fma_f32 v[72:73],v[82:83],v[60:61],v[72:73] op_sel_hi:[1,0,1]\nv_lshlrev_b32 v84,16,v30\nv_lshlrev_b32 v86,16,v31\nv_pk_fma_f32 v[74:75],v[84:85],v[58:59],v[74:75] op_sel_hi:[1,0,1]\n" \
"v_pk_fma_f32 v[76:77],v[86:87],v[58:59],v[76:77] op_sel_hi:[1,0,1]\nv_and_b32 v81,0xffff0000,v34\nv_and_b32 v83,0xffff0000,v35\nv_lshlrev_b32 v80,16,v34\n" \
"v_lshlrev_b32 v82,16,v35\nv_pk_fma_f32 v[74:75],v[80:81],v[60:61],v[74:75] op_sel_hi:[1,0,1]\nv_pk_fma_f32 v[76:77],v[82:83],v[60:61],v[76:77] op_sel_hi:[1,0,1]\ns_waitcnt vmcnt(3)\n" \
"v_and_b32 v85,0xffff0000,v36\nv_and_b32 v87,0xffff0000,v37\nv_lshlrev_b32 v84,16,v36\nv_lshlrev_b32 v86,16,v37\n" \
"v_pk_fma_f32 v[70:71],v[84:85],v[62:63],v[70:71] op_sel_hi:[1,0,1]\nv_pk_fma_f32 v[72:73],v[86:87],v[62:63],v[72:73] op_sel_hi:[1,0,1]\ns_waitcnt vmcnt(2)\nv_and_b32 v81,0xffff0000,v40\n" \
"v_and_b32 v83,0xffff0000,v41\nv_lshlrev_b32 v80,16,v40\nv_lshlrev_b32 v82,16,v41\nv_and_b32 v85,0xffff0000,v38\n" \
"v_and_b32 v87,0xffff0000,v39\nv_pk_fma_f32 v[70:71],v[80:81],v[64:65],v[70:71] op_sel_hi:[1,0,1]\nv_pk_fma_f32 v[72:73],v[82:83],v[64:65],v[72:73] op_sel_hi:[1,0,1]\nv_lshlrev_b32 v84,16,v38\n" \
"v_lshlrev_b32 v86,16,v39\nv_pk_fma_f32 v[74:75],v[84:85],v[62:63],v[74:75] op_sel_hi:[1,0,1]\nv_pk_fma_f32 v[76:77],v[86:87],v[62:63],v[76:77] op_sel_hi:[1,0,1]\nv_and_b32 v81,0xffff0000,v42\n" \
"v_and_b32 v83,0xffff0000,v43\nv_lshlrev_b32 v80,16,v42\nv_lshlrev_b32 v82,16,v43\nv_pk_fma_f32 v[74:75],v[80:81],v[64:65],v[74:75] op_sel_hi:[1,0,1]\n" \
"v_pk_fma_f32 v[76:77],v[82:83],v[64:65],v[76:77] op_sel_hi:[1,0,1]\ns_waitcnt vmcnt(1)\nv_and_b32 v85,0xffff0000,v44\nv_and_b32 v87,0xffff0000,v45\n" \
"v_lshlrev_b32 v84,16,v44\nv_lshlrev_b32 v86,16,v45\nv_pk_fma_f32 v[70:71],v[84:85],v[66:67],v[70:71] op_sel_hi:[1,0,1]\nv_pk_fma_f32 v[72:73],v[86:87],v[66:67],v[72:73] op_sel_hi:[1,0,1]\n" \
"s_waitcnt vmcnt(0)\nv_and_b32 v81,0xffff0000,v48\nv_and_b32 v83,0xffff0000,v49\nv_lshlrev_b32 v80,16,v48\n" \
"v_lshlrev_b32 v82,16,v49\nv_and_b32 v85,0xffff0000,v46\nv_and_b32 v87,0xffff0000,v47\nv_pk_fma_f32 v[70:71],v[80:81],v[68:69],v[70:71] op_sel_hi:[1,0,1]\n" \
"v_pk_fma_f32 v[72:73],v[82:83],v[68:69],v[72:73] op_sel_hi:[1,0,1]\nv_lshlrev_b32 v84,16,v46\nv_lshlrev_b32 v86,16,v47\nv_pk_fma_f32 v[74:75],v[84:85],v[66:67],v[74:75] op_sel_hi:[1,0,1]\n" \
"v_pk_fma_f32 v[76:77],v[86:87],v[66:67],v[76:77] op_sel_hi:[1,0,1]\nv_and_b32 v81,0xffff0000,v50\nv_and_b32 v83,0xffff0000,v51\nv_lshlrev_b32 v80,16,v50\n" \
"v_lshlrev_b32 v82,16,v51\nv_pk_fma_f32 v[74:75],v[80:81],v[68:69],v[74:75] op_sel_hi:[1,0,1]\nv_pk_fma_f32 v[76:77],v[82:83],v[68:69],v[76:77] op_sel_hi:[1,0,1]\nv_and_b32 v80,3,v0\n" \
"v_permlane32_swap_b32 v70,v74\nv_permlane32_swap_b32 v71,v75\nv_permlane32_swap_b32 v72,v76\nv_permlane32_swap_b32 v73,v77\n" \
"v_pk_add_f32 v[70:71],v[70:71],v[74:75]\nv_pk_add_f32 v[72:73],v[72:73],v[76:77]\nv_lshrrev_b32 v81,4,v0\nv_and_b32 v82,12,v81\n" \
"v_permlane16_swap_b32 v70,v72\nv_permlane16_swap_b32 v71,v73\nv_lshlrev_b32 v80,5,v80\nv_pk_add_f32 v[70:71],v[70:71],v[72:73]\n" \
"v_mad_u32_u24 v80,v82,24,v80\nv_lshl_add_u32 v80,v81,3,v80\nv_add_f32_dpp v70,v70,v70 row_ror:8 row_mask:0xf bank_mask:0xf bound_ctrl:1\nv_add_f32_dpp v71,v71,v71 row_ror:8 row_mask:0xf bank_mask:0xf bound_ctrl:1\n" \
"v_add_u32 v80,161152,v80\nv_add_f32_dpp v70,v70,v70 row_ror:4 row_mask:0xf bank_mask:0xf bound_ctrl:1\nv_add_f32_dpp v71,v71,v71 row_ror:4 row_mask:0xf bank_mask:0xf bound_ctrl:1\nds_write_b64 v80,v[70:71]\n" \
"v_and_b32 v80,7,v0\nv_lshlrev_b32 v80,4,v80\nv_add_u32 v80,161152,v80\nv_bfrev_b32 v81,1\n" \
"v_cndmask_b32 v81,v81,v2,vcc\ns_waitcnt lgkmcnt(0)\ns_barrier\nds_read_b128 v[84:87],v80\n" \
"ds_read_b128 v[88:91],v80 offset:128\nds_read_b128 v[92:95],v80 offset:256\nds_read_b128 v[96:99],v80 offset:384\ns_waitcnt lgkmcnt(2)\n" \
"v_pk_add_f32 v[84:85],v[84:85],v[88:89]\nv_pk_add_f32 v[86:87],v[86:87],v[90:91]\ns_waitcnt lgkmcnt(0)\nv_pk_add_f32 v[92:93],v[92:93],v[96:97]\n" \
"v_pk_add_f32 v[94:95],v[94:95],v[98:99]\nv_pk_add_f32 v[84:85],v[84:85],v[92:93]\nv_pk_add_f32 v[86:87],v[86:87],v[94:95]\nv_cvt_pk_bf16_f32 v88,v84,v85\n" \
"v_cvt_pk_bf16_f32 v90,v86,v87\nv_and_b32 v85,0xffff0000,v88\nv_and_b32 v87,0xffff0000,v90\nv_lshlrev_b32 v84,16,v88\n" \
"v_lshlrev_b32 v86,16,v90\nv_add_f32 v84,s26,v84\nv_add_f32 v85,s26,v85\nv_add_f32 v86,s26,v86\n" \
"v_add_f32 v87,s26,v87\ns_lshr_b32 s26,s86,1\nv_cvt_pk_bf16_f32 v102,v84,v85\nv_cvt_pk_bf16_f32 v103,v86,v87\n" \
"s_add_u32 s26,s26,26624\nbuffer_store_dwordx4 v[100:103],v81,s[36:39],s26 offen sc1\ns_and_b32 s22,s33,7\ns_and_b32 s24,s32,1\n" \
"s_lshl3_add_u32 s22,s24,s22\ns_bfe_u32 s24,s33,0x10002\ns_lshl1_add_u32 s24,s32,s24\ns_mul_i32 s19,s24,18\n" \
"s_and_b32 s24,s33,3\ns_lshl2_add_u32 s23,s24,s19\ns_lshl_b32 s24,s3,5\ns_lshr_b32 s20,s33,3\n" \
"s_lshr_b32 s21,s32,1\ns_add_u32 s54,s14,s24\ns_lshl2_add_u32 s24,s20,s21\ns_add_u32 s23,s23,s3\n" \
"s_lshl2_add_u32 s24,s24,s3\ns_add_u32 s23,s23,1\ns_lshl_b32 s24,s24,9\ns_lshl_b32 s23,s23,9\n" \
"v_bfe_u32 v127,v0,3,3\nv_mul_u32_u24 v127,8192,v127\nv_lshl_add_u32 v127,v1,3,v127\ns_lshl2_add_u32 s95,s33,s3\n" \
"s_add_u32 s95,s95,190\ns_and_b32 s100,s95,31\ns_lshl_b32 s100,s100,3\ns_cmp_lt_u32 s95,256\n" \
"s_cselect_b32 s95,s95,0x40000\ns_lshr_b32 s95,s95,5\ns_add_u32 s95,s95,s100\ns_lshl_b32 s95,s95,6\n" \
"s_add_u32 s95,s95,s32\ns_mul_i32 s95,s95,1152\ns_mov_b32 m0,162176\ns_add_u32 s101,s92,0\n" \
"buffer_load_dword v127,s[8:11],s95 offen lds\ns_add_u32 s24,s24,262144\ns_sub_u32 s59,s24,s23\ns_lshr_b32 s25,s3,1\n" \
"s_mul_i32 s24,s21,1152\ns_mul_i32 s25,s25,576\ns_add_u32 s24,s24,s25\ns_lshl4_add_u32 s24,s24,s22\n" \
"s_and_b32 s25,s3,1\ns_lshl_b32 s24,s24,8\ns_lshl_b32 s65,s25,17\ns_add_u32 s65,s65,s24\n" \
"s_lshl_b32 s25,s25,20\ns_add_u32 s24,s24,s25\ns_add_u32 s60,s24,655488\ns_add_u32 s61,s24,786432\n" \
"s_add_u32 s62,s24,786560\ns_add_u32 m0,s57,0\ns_add_u32 s31,s92,9437184\nbuffer_load_dwordx4 v4,s[8:11],s101 offen lds\n" \
"s_add_u32 s63,s24,917504\ns_add_u32 s64,s24,917632\ns_mul_i32 s52,s20,4505600\ns_lshl2_add_u32 s24,s21,s3\n" \
"s_add_u32 s52,s52,12123648\ns_mul_i32 s24,s24,9216\ns_add_u32 s24,s24,s52\ns_add_u32 s68,s24,819712\n" \
"s_lshl_b32 s24,s22,14\nbuffer_load_dwordx4 v5,s[8:11],s101 offen offset:1024 lds\ns_add_u32 s24,s24,s52\ns_lshl2_add_u32 s25,s21,s3\n" \
"s_add_u32 s24,s24,967168\ns_lshl_b32 s25,s25,8\ns_add_u32 s79,s25,s24\ns_add_u32 s25,s21,4\n" \
"s_and_b32 s25,s25,3\ns_add_u32 s24,s24,s12\ns_lshl_b32 s25,s25,12\nbuffer_load_dwordx4 v6,s[8:11],s101 offen offset:2048 lds\n" \
"s_add_u32 s25,s25,s24\ns_sub_u32 s74,s25,819200\ns_add_u32 s25,s21,3\ns_and_b32 s25,s25,3\n" \
"s_lshl_b32 s25,s25,12\ns_add_u32 s25,s25,s24\ns_sub_u32 s71,s25,2048000\ns_sub_u32 s76,s25,1638400\n" \
"s_add_u32 s25,s21,2\nbuffer_load_dwordx4 v7,s[8:11],s101 offen offset:3072 lds\ns_and_b32 s25,s25,3\ns_lshl_b32 s25,s25,12\n" \
"s_add_u32 s25,s25,s24\ns_sub_u32 s72,s25,1638400\ns_sub_u32 s77,s25,1638400\ns_add_u32 s25,s21,1\n" \
"s_and_b32 s25,s25,3\ns_lshl_b32 s25,s25,12\ns_add_u32 s25,s25,s24\ns_add_u32 m0,s57,4096\n" \
"s_add_u32 s96,s92,73728\nbuffer_load_dwordx4 v4,s[8:11],s31 offen lds\ns_sub_u32 s73,s25,1638400\ns_sub_u32 s78,s25,1228800\n" \
"s_lshl2_add_u32 s26,s33,s3\ns_and_b32 s24,s26,15\ns_and_b32 s25,s26,8\ns_lshl2_add_u32 s24,s24,192\n" \
"s_mul_i32 s25,s25,28\ns_add_u32 s25,s25,s24\ns_sub_u32 s25,s25,32\nbuffer_load_dwordx4 v5,s[8:11],s31 offen offset:1024 lds\n" \
"s_bitcmp1_b32 s26,4\ns_cselect_b32 s24,s25,s24\ns_and_b32 s25,s26,31\ns_lshl2_add_u32 s25,s25,448\n" \
"s_bitcmp1_b32 s26,5\ns_cselect_b32 s24,s25,s24\ns_lshr_b32 s25,s26,6\ns_lshl1_add_u32 s25,s21,s25\n" \
"s_mul_i32 s25,s25,576\nbuffer_load_dwordx4 v6,s[8:11],s31 offen offset:2048 lds\ns_add_u32 s24,s24,s25\ns_and_b32 s25,s32,1\n" \
"s_lshl1_add_u32 s24,s24,s25\nv_and_b32 v112,3,v0\ns_lshl_b32 s24,s24,11\ns_bfe_u32 s25,s33,131072\n" \
"s_mul_i32 s27,s3,4896\nv_and_b32 v113,12,v0\nv_lshlrev_b32 v112,2,v112\nbuffer_load_dwordx4 v7,s[8:11],s31 offen offset:3072 lds\n" \
"s_add_u32 s24,s24,s25\ns_add_u32 s27,s27,9216\nv_max_u32 v112,v112,v113\ns_mul_i32 s48,s20,589824\n" \
"s_add_u32 s89,s24,1\nv_mov_b32 v114,1048576\nv_mov_b32 v113,s27\nv_cmp_gt_u32 vcc,12,v112\n" \
"s_add_u32 m0,s57,8192\ns_add_u32 s97,s92,9510912\nbuffer_load_dwordx4 v4,s[8:11],s96 offen lds\ns_lshl4_add_u32 s48,s12,s48\n" \
"s_lshl_b32 s86,s21,11\ns_lshl2_add_u32 s24,s2,s3\nv_and_b32 v63,0xffff0000,v224\nv_and_b32 v65,0xffff0000,v225\n" \
"v_and_b32 v67,0xffff0000,v226\nv_and_b32 v69,0xffff0000,v227\nv_and_b32 v71,0xffff0000,v228\nbuffer_load_dwordx4 v5,s[8:11],s96 offen offset:1024 lds\n" \
"v_and_b32 v73,0xffff0000,v229\nv_and_b32 v75,0xffff0000,v230\nv_and_b32 v77,0xffff0000,v231\nv_and_b32 v79,0xffff0000,v232\n" \
"v_cndmask_b32 v112,v114,v113,vcc\nv_lshlrev_b32 v62,16,v224\nv_lshlrev_b32 v64,16,v225\nv_lshlrev_b32 v66,16,v226\n" \
"buffer_load_dwordx4 v6,s[8:11],s96 offen offset:2048 lds\nv_lshlrev_b32 v68,16,v227\nv_lshlrev_b32 v70,16,v228\nv_lshlrev_b32 v72,16,v229\n" \
"v_lshlrev_b32 v74,16,v230\nv_lshlrev_b32 v76,16,v231\nv_lshlrev_b32 v78,16,v232\ns_add_u32 s48,s48,9764864\n" \
"s_add_u32 s65,s65,2097152\ns_lshl_b32 s67,s21,7\nbuffer_load_dwordx4 v7,s[8:11],s96 offen offset:3072 lds\ns_lshl_b32 s85,s21,17\n" \
"s_sub_u32 s86,s14,s86\ns_lshl_b32 s24,s24,8\nv_pk_add_f32 v[62:63],v[62:63],v[250:251] op_sel_hi:[1,0] neg_lo:[0,1] neg_hi:[0,1]\nv_pk_add_f32 v[64:65],v[64:65],v[250:251] op_sel_hi:[1,0] neg_lo:[0,1] neg_hi:[0,1]\n" \
"v_pk_add_f32 v[66:67],v[66:67],v[250:251] op_sel_hi:[1,0] neg_lo:[0,1] neg_hi:[0,1]\nv_pk_add_f32 v[68:69],v[68:69],v[250:251] op_sel_hi:[1,0] neg_lo:[0,1] neg_hi:[0,1]\nv_pk_add_f32 v[70:71],v[70:71],v[250:251] op_sel_hi:[1,0] neg_lo:[0,1] neg_hi:[0,1]\ns_add_u32 m0,s57,12288\n" \
"s_add_u32 s100,s92,9584640\nbuffer_load_dwordx4 v4,s[8:11],s97 offen lds\nv_pk_add_f32 v[72:73],v[72:73],v[250:251] op_sel_hi:[1,0] neg_lo:[0,1] neg_hi:[0,1]\nv_pk_add_f32 v[74:75],v[74:75],v[250:251] op_sel_hi:[1,0] neg_lo:[0,1] neg_hi:[0,1]\n" \
"v_pk_add_f32 v[76:77],v[76:77],v[250:251] op_sel_hi:[1,0] neg_lo:[0,1] neg_hi:[0,1]\nv_pk_add_f32 v[78:79],v[78:79],v[250:251] op_sel_hi:[1,0] neg_lo:[0,1] neg_hi:[0,1]\nv_mov_b64 v[16:17],v[192:193]\nv_mov_b64 v[18:19],v[194:195]\n" \
"v_mov_b64 v[20:21],v[196:197]\nv_mov_b64 v[22:23],v[198:199]\nbuffer_load_dwordx4 v5,s[8:11],s97 offen offset:1024 lds\nv_mov_b64 v[24:25],v[208:209]\n" \
"v_mov_b64 v[26:27],v[210:211]\nv_mov_b64 v[28:29],v[212:213]\nv_mov_b64 v[30:31],v[214:215]\nv_mov_b64 v[32:33],v[216:217]\n" \
"v_mov_b64 v[34:35],v[218:219]\nv_mov_b64 v[36:37],v[220:221]\nv_mov_b64 v[38:39],v[222:223]\nv_mov_b64 v[40:41],v[200:201]\n" \
"buffer_load_dwordx4 v6,s[8:11],s97 offen offset:2048 lds\nv_mov_b64 v[42:43],v[202:203]\nv_mov_b64 v[44:45],v[204:205]\nv_mov_b64 v[46:47],v[206:207]\n" \
"v_mov_b64 v[56:57],v[242:243]\nv_mov_b64 v[58:59],v[244:245]\nv_mov_b32 v60,v249\nv_mov_b32 v61,v250\n" \
"v_add_u32 v48,v234,v112\nv_add_u32 v49,v235,v112\nbuffer_load_dwordx4 v7,s[8:11],s97 offen offset:3072 lds\nv_add_u32 v50,v236,v112\n" \
"v_add_u32 v51,v237,v112\nv_add_u32 v52,v238,v112\nv_add_u32 v53,v239,v112\nv_add_u32 v54,v240,v112\n" \
"v_add_u32_e64 v55,v241,v112\ns_add_u32 s49,s48,4096\ns_add_u32 s50,s48,8192\ns_add_u32 s51,s48,12288\n" \
"s_add_u32 m0,s57,16384\ns_add_u32 s53,s52,3276800\ns_add_u32 s83,s52,2048000\ns_mov_b32 s55,0xbdd2d3e7\n" \
"s_mul_i32 s56,s3,9568\ns_lshl_b32 s58,s3,13\ns_add_u32 s75,s59,196608\ns_add_u32 s66,s65,128\n" \
"s_add_u32 s67,s67,163200\ns_add_u32 s69,s68,3072\ns_add_u32 s70,s68,6144\ns_add_u32 s80,s79,4096\n" \
"s_add_u32 s81,s79,8192\ns_add_u32 s82,s79,12288\ns_and_b32 s84,s33,1\ns_add_u32 s85,s85,65536\n" \
"s_add_u32 s87,s12,65536\ns_add_u32 s88,s74,819200\ns_add_u32 s86,s86,0x10000\ns_mov_b32 s90,7405568\n" \
"s_add_u32 s91,s24,69206016\ns_sub_u32 s98,s57,s12\ns_mov_b32 s99,s12\ns_add_u32 s95,s92,147456\n" \
"buffer_load_dwordx4 v4,s[8:11],s95 offen lds\ns_add_u32 s101,s92,221184\nbuffer_load_dwordx4 v5,s[8:11],s95 offen offset:1024 lds\nbuffer_load_dwordx4 v6,s[8:11],s95 offen offset:2048 lds\n" \
"buffer_load_dwordx4 v7,s[8:11],s95 offen offset:3072 lds\ns_add_u32 m0,s57,20480\ns_nop 0\nbuffer_load_dwordx4 v4,s[8:11],s100 offen lds\n" \
"buffer_load_dwordx4 v5,s[8:11],s100 offen offset:1024 lds\nbuffer_load_dwordx4 v6,s[8:11],s100 offen offset:2048 lds\nbuffer_load_dwordx4 v7,s[8:11],s100 offen offset:3072 lds\ns_add_u32 m0,s57,24576\n" \
"s_nop 0\nbuffer_load_dwordx4 v4,s[8:11],s101 offen lds\nbuffer_load_dwordx4 v5,s[8:11],s101 offen offset:1024 lds\nbuffer_load_dwordx4 v6,s[8:11],s101 offen offset:2048 lds\n" \
"buffer_load_dwordx4 v7,s[8:11],s101 offen offset:3072 lds\nbuffer_load_dwordx4 v[104:107],v1,s[36:39],s7 offen sc1\nbuffer_load_dwordx4 v[108:111],v1,s[36:39],s7 offen offset:1024 sc1\ns_waitcnt vmcnt(1)\n" \
"v_xor_b32 v112,s29,v105\nv_xor_b32 v113,s29,v107\ns_waitcnt vmcnt(0)\nv_xor_b32 v114,s29,v109\n" \
"v_xor_b32 v115,s29,v111\nv_or3_b32 v112,v112,v113,v114\nv_xor_b32 v113,s28,v104\nv_xor_b32 v114,s28,v106\n" \
"v_or_b32 v112,v112,v115\nv_xor_b32 v115,s28,v108\nv_lshl_or_b32 v112,v112,8,v113\nv_xor_b32 v113,s28,v110\n" \
"v_or3_b32 v112,v112,v114,v115\nv_or_b32 v112,v112,v113\nv_cmp_ne_u32 vcc,0,v112\ns_nop 0\n" \
"s_cbranch_vccnz .LsrP_%=\n.LbrP_%=:\nv_min_u32 v120,2288,v2\ns_mov_b32 s5,26624\n" \
"v_max3_u32 v112,v105,v107,v109\nv_max_u32 v112,v112,v111\ns_mov_b32 s6,31232\nv_or_b32 v102,s2,v0\n" \
"v_max_u32_dpp v112,v112,v112 quad_perm:[1,0,3,2] row_mask:0xf bank_mask:0xf bound_ctrl:1\nv_bfrev_b32 v103,1\nv_cmp_eq_u32 vcc,0,v102\nv_max_u32_dpp v112,v112,v112 quad_perm:[2,3,0,1] row_mask:0xf bank_mask:0xf bound_ctrl:1\n" \
"v_mov_b32 v100,s28\nv_cndmask_b32 v102,v103,v102,vcc\nv_max_u32_dpp v112,v112,v112 row_half_mirror row_mask:0xf bank_mask:0xf bound_ctrl:1\nv_mov_b32 v101,s29\n" \
"buffer_store_dwordx2 v[100:101],v102,s[36:39],0 offen sc1\nv_max_u32_dpp v112,v112,v112 row_mirror row_mask:0xf bank_mask:0xf bound_ctrl:1\nds_read_b64_tr_b16 a[0:1],v8\nv_readlane_b32 s94,v112,0\n" \
"v_readlane_b32 s31,v112,16\nv_readlane_b32 s96,v112,32\nv_readlane_b32 s97,v112,48\ns_max_u32 s31,s31,s96\n" \
"s_max_u32 s94,s94,s97\ns_max_u32 s94,s94,s31\ns_lshr_b32 s94,s94,24\ns_max_u32 s94,s94,64\n" \
"s_min_u32 s94,s94,250\nds_read_b64_tr_b16 a[4:5],v9\nds_read_b64_tr_b16 a[8:9],v10\nds_read_b64_tr_b16 a[12:13],v11\n" \
"ds_read_b64_tr_b16 a[16:17],v8 offset:2048\nds_read_b64_tr_b16 a[20:21],v9 offset:2048\nds_read_b64_tr_b16 a[24:25],v10 offset:2048\nds_read_b64_tr_b16 a[28:29],v11 offset:2048\n" \
"s_add_u32 s95,s92,9658368\ns_add_u32 m0,s57,0\nds_read_b64_tr_b16 a[2:3],v8 offset:4096\ns_waitcnt lgkmcnt(1)\n" \
"buffer_load_dwordx4 v4,s[8:11],s95 offen lds\nbuffer_load_dwordx4 v5,s[8:11],s95 offen offset:1024 lds\nbuffer_load_dwordx4 v6,s[8:11],s95 offen offset:2048 lds\nbuffer_load_dwordx4 v7,s[8:11],s95 offen offset:3072 lds\n" \
"ds_read_b64_tr_b16 a[6:7],v9 offset:4096\nds_read_b64_tr_b16 a[10:11],v10 offset:4096\nds_read_b64_tr_b16 a[14:15],v11 offset:4096\nds_read_b64_tr_b16 a[18:19],v8 offset:6144\n" \
"ds_read_b64_tr_b16 a[22:23],v9 offset:6144\nds_read_b64_tr_b16 a[26:27],v10 offset:6144\nds_read_b64_tr_b16 a[30:31],v11 offset:6144\ns_add_u32 s100,s92,294912\n" \
"s_add_u32 m0,s57,4096\nds_read_b64_tr_b16 a[32:33],v8 offset:8192\ns_waitcnt lgkmcnt(1)\nbuffer_load_dwordx4 v4,s[8:11],s100 offen lds\n" \
"buffer_load_dwordx4 v5,s[8:11],s100 offen offset:1024 lds\nbuffer_load_dwordx4 v6,s[8:11],s100 offen offset:2048 lds\nbuffer_load_dwordx4 v7,s[8:11],s100 offen offset:3072 lds\nds_read_b64_tr_b16 a[36:37],v9 offset:8192\n" \
"ds_read_b64_tr_b16 a[40:41],v10 offset:8192\nds_read_b64_tr_b16 a[44:45],v11 offset:8192\nds_read_b64_tr_b16 a[48:49],v8 offset:10240\nds_read_b64_tr_b16 a[52:53],v9 offset:10240\n" \
"ds_read_b64_tr_b16 a[56:57],v10 offset:10240\nds_read_b64_tr_b16 a[60:61],v11 offset:10240\ns_add_u32 s101,s92,9732096\ns_add_u32 m0,s57,8192\n" \
"ds_read_b64_tr_b16 a[34:35],v8 offset:12288\ns_waitcnt lgkmcnt(1)\nbuffer_load_dwordx4 v4,s[8:11],s101 offen lds\nbuffer_load_dwordx4 v5,s[8:11],s101 offen offset:1024 lds\n" \
"buffer_load_dwordx4 v6,s[8:11],s101 offen offset:2048 lds\nbuffer_load_dwordx4 v7,s[8:11],s101 offen offset:3072 lds\nds_read_b64_tr_b16 a[38:39],v9 offset:12288\nds_read_b64_tr_b16 a[42:43],v10 offset:12288\n" \
"ds_read_b64_tr_b16 a[46:47],v11 offset:12288\nds_read_b64_tr_b16 a[50:51],v8 offset:14336\nds_read_b64_tr_b16 a[54:55],v9 offset:14336\nds_read_b64_tr_b16 a[58:59],v10 offset:14336\n" \
"ds_read_b64_tr_b16 a[62:63],v11 offset:14336\ns_add_u32 s31,s92,368640\ns_add_u32 m0,s57,12288\nds_read_b64_tr_b16 a[64:65],v8 offset:16384\n" \
"s_waitcnt lgkmcnt(1)\nbuffer_load_dwordx4 v4,s[8:11],s31 offen lds\nbuffer_load_dwordx4 v5,s[8:11],s31 offen offset:1024 lds\nbuffer_load_dwordx4 v6,s[8:11],s31 offen offset:2048 lds\n" \
"buffer_load_dwordx4 v7,s[8:11],s31 offen offset:3072 lds\nbuffer_load_dwordx4 v[80:83],v120,s[36:39],s5 offen sc1\nbuffer_load_dwordx4 v[84:87],v120,s[36:39],s5 offen offset:2304 sc1\nbuffer_load_dwordx4 v[88:91],v120,s[36:39],s6 offen sc1\n" \
"buffer_load_dwordx4 v[92:95],v120,s[36:39],s6 offen offset:2304 sc1\nds_read_b64_tr_b16 a[68:69],v9 offset:16384\nds_read_b64_tr_b16 a[72:73],v10 offset:16384\nds_read_b64_tr_b16 a[76:77],v11 offset:16384\n" \
"ds_read_b64_tr_b16 a[80:81],v8 offset:18432\nds_read_b64_tr_b16 a[84:85],v9 offset:18432\nds_read_b64_tr_b16 a[88:89],v10 offset:18432\nds_read_b64_tr_b16 a[92:93],v11 offset:18432\n" \
"s_add_u32 s96,s92,9805824\ns_add_u32 m0,s57,16384\nds_read_b64_tr_b16 a[66:67],v8 offset:20480\ns_waitcnt lgkmcnt(1)\n" \
"buffer_load_dwordx4 v4,s[8:11],s96 offen lds\nbuffer_load_dwordx4 v5,s[8:11],s96 offen offset:1024 lds\nbuffer_load_dwordx4 v6,s[8:11],s96 offen offset:2048 lds\nbuffer_load_dwordx4 v7,s[8:11],s96 offen offset:3072 lds\n" \
"ds_read_b64_tr_b16 a[70:71],v9 offset:20480\nds_read_b64_tr_b16 a[74:75],v10 offset:20480\nds_read_b64_tr_b16 a[78:79],v11 offset:20480\nds_read_b64_tr_b16 a[82:83],v8 offset:22528\n" \
"ds_read_b64_tr_b16 a[86:87],v9 offset:22528\nds_read_b64_tr_b16 a[90:91],v10 offset:22528\nds_read_b64_tr_b16 a[94:95],v11 offset:22528\ns_add_u32 s97,s92,442368\n" \
"s_add_u32 m0,s57,20480\nds_read_b64_tr_b16 a[96:97],v8 offset:24576\ns_waitcnt lgkmcnt(1)\nbuffer_load_dwordx4 v4,s[8:11],s97 offen lds\n" \
"buffer_load_dwordx4 v5,s[8:11],s97 offen offset:1024 lds\nbuffer_load_dwordx4 v6,s[8:11],s97 offen offset:2048 lds\nbuffer_load_dwordx4 v7,s[8:11],s97 offen offset:3072 lds\nds_read_b64_tr_b16 a[100:101],v9 offset:24576\n" \
"ds_read_b64_tr_b16 a[104:105],v10 offset:24576\nds_read_b64_tr_b16 a[108:109],v11 offset:24576\nds_read_b64_tr_b16 a[112:113],v8 offset:26624\nds_read_b64_tr_b16 a[116:117],v9 offset:26624\n" \
"ds_read_b64_tr_b16 a[120:121],v10 offset:26624\nds_read_b64_tr_b16 a[124:125],v11 offset:26624\ns_add_u32 s95,s92,9879552\ns_add_u32 m0,s57,24576\n" \
"s_waitcnt vmcnt(24)\nds_read_b64_tr_b16 a[98:99],v8\ns_waitcnt lgkmcnt(1)\nbuffer_load_dwordx4 v4,s[8:11],s95 offen lds\n" \
"buffer_load_dwordx4 v5,s[8:11],s95 offen offset:1024 lds\nbuffer_load_dwordx4 v6,s[8:11],s95 offen offset:2048 lds\nbuffer_load_dwordx4 v7,s[8:11],s95 offen offset:3072 lds\nds_read_b64_tr_b16 a[102:103],v9\n" \
"ds_read_b64_tr_b16 a[106:107],v10\nds_read_b64_tr_b16 a[110:111],v11\nds_read_b64_tr_b16 a[114:115],v8 offset:2048\nds_read_b64_tr_b16 a[118:119],v9 offset:2048\n" \
"ds_read_b64_tr_b16 a[122:123],v10 offset:2048\nds_read_b64_tr_b16 a[126:127],v11 offset:2048\ns_add_u32 s100,s92,516096\ns_add_u32 m0,s57,0\n" \
"s_waitcnt vmcnt(24)\nds_read_b64_tr_b16 a[128:129],v8 offset:4096\ns_waitcnt lgkmcnt(1)\nbuffer_load_dwordx4 v4,s[8:11],s100 offen lds\n" \
"buffer_load_dwordx4 v5,s[8:11],s100 offen offset:1024 lds\nbuffer_load_dwordx4 v6,s[8:11],s100 offen offset:2048 lds\nbuffer_load_dwordx4 v7,s[8:11],s100 offen offset:3072 lds\nds_read_b64_tr_b16 a[132:133],v9 offset:4096\n" \
"ds_read_b64_tr_b16 a[136:137],v10 offset:4096\nds_read_b64_tr_b16 a[140:141],v11 offset:4096\nds_read_b64_tr_b16 a[144:145],v8 offset:6144\nds_read_b64_tr_b16 a[148:149],v9 offset:6144\n" \
"ds_read_b64_tr_b16 a[152:153],v10 offset:6144\nds_read_b64_tr_b16 a[156:157],v11 offset:6144\ns_add_u32 s101,s92,9953280\ns_add_u32 m0,s57,4096\n" \
"s_waitcnt vmcnt(24)\nds_read_b64_tr_b16 a[130:131],v8 offset:8192\ns_waitcnt lgkmcnt(1)\nbuffer_load_dwordx4 v4,s[8:11],s101 offen lds\n" \
"buffer_load_dwordx4 v5,s[8:11],s101 offen offset:1024 lds\nbuffer_load_dwordx4 v6,s[8:11],s101 offen offset:2048 lds\nbuffer_load_dwordx4 v7,s[8:11],s101 offen offset:3072 lds\nds_read_b64_tr_b16 a[134:135],v9 offset:8192\n" \
"ds_read_b64_tr_b16 a[138:139],v10 offset:8192\nds_read_b64_tr_b16 a[142:143],v11 offset:8192\nds_read_b64_tr_b16 a[146:147],v8 offset:10240\nds_read_b64_tr_b16 a[150:151],v9 offset:10240\n" \
"ds_read_b64_tr_b16 a[154:155],v10 offset:10240\nds_read_b64_tr_b16 a[158:159],v11 offset:10240\ns_bfe_u32 s26,s33,0x20001\ns_lshl2_add_u32 s15,s32,s26\n" \
"s_lshl2_add_u32 s15,s15,s3\ns_lshl_b32 s15,s15,14\ns_add_u32 s15,s15,5308416\ns_add_u32 s31,s15,0\n" \
"s_add_u32 m0,s57,8192\ns_waitcnt vmcnt(24)\nds_read_b64_tr_b16 a[160:161],v8 offset:12288\ns_waitcnt lgkmcnt(1)\n" \
"buffer_load_dwordx4 v1,s[36:39],s31 offen sc1 lds\nbuffer_load_dwordx4 v1,s[36:39],s31 offen offset:1024 sc1 lds\nbuffer_load_dwordx4 v1,s[36:39],s31 offen offset:2048 sc1 lds\nbuffer_load_dwordx4 v1,s[36:39],s31 offen offset:3072 sc1 lds\n" \
"ds_read_b64_tr_b16 a[164:165],v9 offset:12288\nds_read_b64_tr_b16 a[168:169],v10 offset:12288\nds_read_b64_tr_b16 a[172:173],v11 offset:12288\nds_read_b64_tr_b16 a[176:177],v8 offset:14336\n" \
"ds_read_b64_tr_b16 a[180:181],v9 offset:14336\nds_read_b64_tr_b16 a[184:185],v10 offset:14336\nds_read_b64_tr_b16 a[188:189],v11 offset:14336\ns_add_u32 s96,s15,4096\n" \
"s_add_u32 m0,s57,12288\ns_waitcnt vmcnt(20)\nds_read_b64_tr_b16 a[162:163],v8 offset:16384\ns_waitcnt lgkmcnt(1)\n" \
"buffer_load_dwordx4 v1,s[36:39],s96 offen sc1 lds\nbuffer_load_dwordx4 v1,s[36:39],s96 offen offset:1024 sc1 lds\nbuffer_load_dwordx4 v1,s[36:39],s96 offen offset:2048 sc1 lds\nbuffer_load_dwordx4 v1,s[36:39],s96 offen offset:3072 sc1 lds\n" \
"ds_read_b64_tr_b16 a[166:167],v9 offset:16384\nds_read_b64_tr_b16 a[170:171],v10 offset:16384\nds_read_b64_tr_b16 a[174:175],v11 offset:16384\nds_read_b64_tr_b16 a[178:179],v8 offset:18432\n" \
"ds_read_b64_tr_b16 a[182:183],v9 offset:18432\nds_read_b64_tr_b16 a[186:187],v10 offset:18432\nds_read_b64_tr_b16 a[190:191],v11 offset:18432\ns_add_u32 s97,s15,8192\n" \
"s_add_u32 m0,s57,16384\ns_waitcnt vmcnt(20)\nds_read_b64_tr_b16 a[192:193],v8 offset:20480\ns_waitcnt lgkmcnt(1)\n" \
"buffer_load_dwordx4 v1,s[36:39],s97 offen sc1 lds\nbuffer_load_dwordx4 v1,s[36:39],s97 offen offset:1024 sc1 lds\nbuffer_load_dwordx4 v1,s[36:39],s97 offen offset:2048 sc1 lds\nbuffer_load_dwordx4 v1,s[36:39],s97 offen offset:3072 sc1 lds\n" \
"ds_read_b64_tr_b16 a[196:197],v9 offset:20480\nds_read_b64_tr_b16 a[200:201],v10 offset:20480\nds_read_b64_tr_b16 a[204:205],v11 offset:20480\nds_read_b64_tr_b16 a[208:209],v8 offset:22528\n" \
"ds_read_b64_tr_b16 a[212:213],v9 offset:22528\nds_read_b64_tr_b16 a[216:217],v10 offset:22528\nds_read_b64_tr_b16 a[220:221],v11 offset:22528\ns_add_u32 s95,s15,12288\n" \
"s_add_u32 m0,s57,20480\ns_waitcnt lgkmcnt(0)\nbuffer_load_dwordx4 v1,s[36:39],s95 offen sc1 lds\nbuffer_load_dwordx4 v1,s[36:39],s95 offen offset:1024 sc1 lds\n" \
"buffer_load_dwordx4 v1,s[36:39],s95 offen offset:2048 sc1 lds\nbuffer_load_dwordx4 v1,s[36:39],s95 offen offset:3072 sc1 lds\nv_cmp_ne_u64 vcc,s[28:29],v[80:81]\nv_cmp_ne_u64 s[24:25],s[28:29],v[84:85]\n" \
"v_cmp_ne_u64 s[26:27],s[28:29],v[88:89]\ns_or_b64 vcc,vcc,s[24:25]\nv_cmp_ne_u64 s[24:25],s[28:29],v[92:93]\ns_or_b64 vcc,vcc,s[26:27]\n" \
"s_or_b64 vcc,vcc,s[24:25]\ns_cbranch_vccnz .LsnP_%=\n.LbnP_%=:\nv_bfe_u32 v112,v0,1,5\n" \
"v_mul_u32_u24 v112,72,v112\nv_lshrrev_b32 v113,6,v0\nv_mul_u32_u24 v114,144,v0\nv_lshl_add_u32 v112,v113,5,v112\n" \
"v_subrev_u32 v114,18368,v114\nv_mov_b32 v115,1048576\nv_cmp_gt_u32 vcc,144,v0\nv_and_b32 v113,1,v0\n" \
"v_lshl_add_u32 v112,v113,4,v112\nv_cndmask_b32 v114,v115,v114,vcc\nv_and_b32 v97,0xffff0000,v82\nv_lshlrev_b32 v96,16,v82\n" \
"s_add_u32 s24,s93,s94\nv_cmp_gt_u32 vcc,128,v0\nv_add_u32 v113,8,v112\nv_mul_f32 v96,0x3ee38e39,v96\n" \
"v_mul_f32 v97,0x3ee38e39,v97\ns_sub_u32 s24,s24,253\nv_cndmask_b32 v112,v114,v112,vcc\nv_and_b32 v99,0xffff0000,v83\n" \
"v_ldexp_f32 v96,v96,s24\nv_ldexp_f32 v97,v97,s24\nv_lshlrev_b32_e64 v98,16,v83\nds_write_b64 v112,v[96:97] offset:28800\n" \
"v_add_u32 v115,72,v114\nv_mul_f32 v98,0x3ee38e39,v98\nv_mul_f32 v99,0x3ee38e39,v99\nv_cndmask_b32_e64 v113,v115,v113,vcc\n" \
"v_ldexp_f32 v98,v98,s24\nv_ldexp_f32 v99,v99,s24\nds_write_b64 v113,v[98:99] offset:28800\nv_and_b32 v101,0xffff0000,v86\n" \
"v_lshlrev_b32 v100,16,v86\nds_write_b64 v112,v[100:101] offset:31104\nv_and_b32 v103,0xffff0000,v87\nv_lshlrev_b32 v102,16,v87\n" \
"s_lshr_b32 s25,s33,3\nds_write_b64 v113,v[102:103] offset:31104\ns_mul_i32 s25,s25,576\nv_and_b32 v105,0xffff0000,v90\n" \
"v_lshlrev_b32 v104,16,v90\ns_sub_u32 s25,163200,s25\nds_write_b64 v112,v[104:105] offset:33408\nv_add_u32 v114,s25,v2\n" \
"v_subrev_u32 v115,163200,v114\nv_and_b32 v107,0xffff0000,v91\nv_lshlrev_b32 v106,16,v91\nv_mov_b32 v116,1048576\n" \
"v_cmp_gt_u32 vcc,576,v115\nds_write_b64 v113,v[106:107] offset:33408\nv_and_b32 v109,0xffff0000,v94\nv_cndmask_b32 v114,v116,v114,vcc\n" \
"v_and_b32 v111,0xffff0000,v95\nv_lshlrev_b32 v108,16,v94\nv_lshlrev_b32 v110,16,v95\nds_write_b128 v114,v[108:111]\n" \
"buffer_load_dwordx4 v[112:115],v3,s[36:39],s16 offen sc1\nbuffer_load_dwordx4 v[116:119],v3,s[36:39],s17 offen sc1\nv_and_b32 v124,48,v0\nv_cmp_gt_u32 vcc,64,v0\n" \
"v_bfe_u32 v127,v0,5,1\nv_and_b32 v13,31,v0\nv_cndmask_b32_e64 v124,v124,v3,vcc\nbuffer_load_dwordx4 v[120:123],v124,s[36:39],s18 offen sc1\n" \
"v_mul_u32_u24 v127,2448,v127\nv_mul_u32_u24 v13,72,v13\ns_mul_i32 s27,s3,4896\nv_add3_u32 v13,v13,v127,s27\n" \
"v_mov_b32 v127,0\ns_waitcnt vmcnt(27)\nds_read_b64_tr_b16 a[194:195],v8 offset:24576\nds_read_b64_tr_b16 a[198:199],v9 offset:24576\n" \
"s_waitcnt lgkmcnt(2)\ns_barrier\nds_read_b64_tr_b16 a[202:203],v10 offset:24576\nds_read_b64_tr_b16 a[206:207],v11 offset:24576\n" \
"ds_read_b64_tr_b16 a[210:211],v8 offset:26624\nds_read_b64_tr_b16 a[214:215],v9 offset:26624\nds_read_b64_tr_b16 a[218:219],v10 offset:26624\nds_read_b64_tr_b16 a[222:223],v11 offset:26624\n" \
"s_waitcnt vmcnt(23)\nds_read_b64_tr_b16 a[224:225],v8\nds_read_b64_tr_b16 a[228:229],v9\nds_read_b64_tr_b16 a[232:233],v10\n" \
"ds_read_b64_tr_b16 a[236:237],v11\nds_read_b64_tr_b16 a[240:241],v8 offset:2048\nds_read_b64_tr_b16 a[244:245],v9 offset:2048\nds_read_b64_tr_b16 a[248:249],v10 offset:2048\n" \
"ds_read_b64_tr_b16 a[252:253],v11 offset:2048\ns_waitcnt vmcnt(19)\nds_read_b64_tr_b16 a[226:227],v8 offset:4096\nds_read_b64_tr_b16 a[230:231],v9 offset:4096\n" \
"ds_read_b64_tr_b16 a[234:235],v10 offset:4096\nds_read_b64_tr_b16 a[238:239],v11 offset:4096\nds_read_b64_tr_b16 a[242:243],v8 offset:6144\nds_read_b64_tr_b16 a[246:247],v9 offset:6144\n" \
"ds_read_b64_tr_b16 a[250:251],v10 offset:6144\nds_read_b64_tr_b16 a[254:255],v11 offset:6144\ns_waitcnt vmcnt(15)\nds_read_b64_tr_b16 v[192:193],v15 offset:8192\n" \
"ds_read_b64_tr_b16 v[196:197],v15 offset:8704\nds_read_b64_tr_b16 v[194:195],v15 offset:9216\nds_read_b64_tr_b16 v[198:199],v15 offset:9728\nds_read_b64_tr_b16 v[200:201],v15 offset:10240\n" \
"ds_read_b64_tr_b16 v[204:205],v15 offset:10752\nds_read_b64_tr_b16 v[202:203],v15 offset:11264\nds_read_b64_tr_b16 v[206:207],v15 offset:11776\ns_waitcnt vmcnt(11)\n" \
"ds_read_b64_tr_b16 v[208:209],v15 offset:12288\nds_read_b64_tr_b16 v[212:213],v15 offset:12800\nds_read_b64_tr_b16 v[210:211],v15 offset:13312\nds_read_b64_tr_b16 v[214:215],v15 offset:13824\n" \
"ds_read_b64_tr_b16 v[216:217],v15 offset:14336\nds_read_b64_tr_b16 v[220:221],v15 offset:14848\nds_read_b64_tr_b16 v[218:219],v15 offset:15360\nds_read_b64_tr_b16 v[222:223],v15 offset:15872\n" \
"s_waitcnt vmcnt(7)\nds_read_b64_tr_b16 v[224:225],v15 offset:16384\nds_read_b64_tr_b16 v[228:229],v15 offset:16896\nds_read_b64_tr_b16 v[226:227],v15 offset:17408\n" \
"ds_read_b64_tr_b16 v[230:231],v15 offset:17920\nds_read_b64_tr_b16 v[232:233],v15 offset:18432\nds_read_b64_tr_b16 v[236:237],v15 offset:18944\nds_read_b64_tr_b16 v[234:235],v15 offset:19456\n" \
"ds_read_b64_tr_b16 v[238:239],v15 offset:19968\ns_waitcnt vmcnt(3)\nds_read_b64_tr_b16 v[240:241],v15 offset:20480\nds_read_b64_tr_b16 v[244:245],v15 offset:20992\n" \
"ds_read_b64_tr_b16 v[242:243],v15 offset:21504\nds_read_b64_tr_b16 v[246:247],v15 offset:22016\nds_read_b64_tr_b16 v[248:249],v15 offset:22528\nds_read_b64_tr_b16 v[252:253],v15 offset:23040\n" \
"ds_read_b64_tr_b16 v[250:251],v15 offset:23552\nds_read_b64_tr_b16 v[254:255],v15 offset:24064\ns_waitcnt vmcnt(1)\nv_and_b32 v125,v112,v116\n" \
"s_waitcnt vmcnt(0)\nv_bitop3_b32 v125,v125,v120,1 bitop3:0x80\nv_cmp_eq_u32 vcc,0,v125\ns_nop 0\n" \
"s_cbranch_vccnz .LsuP_%=\n.LbuP_%=:\nv_mfma_f32_16x16x32_f16 v[80:83],v[32:35],v[112:115],0\nv_and_b32 v3,31,v0\n" \
"v_mul_u32_u24 v3,72,v3\nv_mfma_f32_16x16x32_f16 v[84:87],v[36:39],v[112:115],0\nv_add_u32 v13,9216,v13\nv_mov_b32 v104,-1\n" \
"v_mfma_f32_16x16x32_f16 v[88:91],v[32:35],v[116:119],0\nv_mov_b32 v105,-1\nv_mov_b32 v106,-1\nv_mfma_f32_16x16x32_f16 v[92:95],v[36:39],v[116:119],0\n" \
"v_mov_b32 v107,-1\ns_add_u32 s27,s2,0\nv_mfma_f32_16x16x32_f16 v[96:99],v[32:35],v[120:123],0\ns_lshr_b32 s100,s27,4\n" \
"s_mul_i32 s101,s100,37\ns_lshr_b32 s101,s101,8\ns_lshl2_add_u32 s100,s101,s100\ns_mul_i32 s100,s100,409600\n" \
"s_and_b32 s101,s27,15\ns_lshl_b32 s101,s101,14\ns_add_u32 s100,s100,s101\ns_add_u32 s100,s100,13090816\n" \
"s_cmp_lt_u32 s27,158\ns_cselect_b32 s100,s100,2147483648\nbuffer_store_dwordx4 v[104:107],v2,s[36:39],s100 offen sc1\nv_mfma_f32_16x16x32_f16 v[100:103],v[36:39],v[120:123],0\n" \
"v_cvt_pk_f16_f32 v112,v84,v80\nv_cvt_pk_f16_f32 v113,v85,v81\nv_cvt_pk_f16_f32 v114,v86,v82\nv_cvt_pk_f16_f32 v115,v87,v83\n" \
"ds_write_b128 v57,v[112:115]\nv_cvt_pk_f16_f32 v116,v92,v88\nv_cvt_pk_f16_f32 v117,v93,v89\ns_add_u32 s100,s100,4096\n" \
"buffer_store_dwordx4 v[104:107],v2,s[36:39],s100 offen sc1\nv_cvt_pk_f16_f32 v118,v94,v90\nv_cvt_pk_f16_f32 v119,v95,v91\nds_write_b128 v58,v[116:119]\n" \
"v_cvt_pk_f16_f32 v120,v100,v96\nv_cvt_pk_f16_f32 v121,v101,v97\nv_cvt_pk_f16_f32 v122,v102,v98\nv_cvt_pk_f16_f32 v123,v103,v99\n" \
"ds_write_b128 v60,v[120:123]\ns_add_u32 s100,s100,4096\nbuffer_store_dwordx4 v[104:107],v2,s[36:39],s100 offen sc1\nv_add_u32 v60,31104,v3\n" \
"v_add_u32 v3,28800,v3\nds_read2_b64 v[32:35],v3 offset0:0 offset1:1\nds_read2_b64 v[36:39],v3 offset0:2 offset1:3\ns_add_u32 s100,s100,4096\n" \
"s_add_u32 s27,s2,111\ns_lshr_b32 s31,s27,4\ns_mul_i32 s96,s31,37\ns_lshr_b32 s96,s96,8\n" \
"s_lshl2_add_u32 s31,s96,s31\ns_mul_i32 s31,s31,409600\ns_and_b32 s96,s27,15\ns_lshl_b32 s96,s96,14\n" \
"buffer_store_dwordx4 v[104:107],v2,s[36:39],s100 offen sc1\ns_add_u32 s31,s31,s96\ns_add_u32 s31,s31,13090816\ns_cmp_lt_u32 s27,158\n" \
"s_cselect_b32 s31,s31,2147483648\ns_waitcnt lgkmcnt(2)\ns_barrier\nds_read_b64 v[112:113],v56\n" \
"ds_read_b64 v[116:117],v56 offset:512\nds_read_b64 v[120:121],v56 offset:256\nds_read_b64 v[124:125],v56 offset:768\nds_read_b64 v[114:115],v56 offset:1024\n" \
"ds_read_b64 v[122:123],v56 offset:1280\nds_read_b64 v[56:57],v3 offset:64\nbuffer_store_dwordx4 v[104:107],v2,s[36:39],s31 offen sc1\ns_add_u32 s31,s31,4096\n" \
"v_bfe_u32 v110,v0,1,2\nv_bfe_u32 v111,v0,4,1\nv_xor_b32 v110,v110,v111\nv_and_b32 v111,1,v0\n" \
"v_lshl_add_u32 v110,v110,1,v111\nv_bfe_u32 v111,v0,3,3\nv_lshlrev_b32 v111,12,v111\nv_lshl_add_u32 v4,v110,4,v111\n" \
"v_xor_b32 v5,64,v4\nv_add_u32 v5,31744,v5\nv_add_u32 v6,63488,v4\nbuffer_store_dwordx4 v[104:107],v2,s[36:39],s31 offen sc1\n" \
"s_waitcnt lgkmcnt(3)\nv_swap_b32 v113,v116\ns_add_u32 s31,s31,4096\nv_swap_b32 v121,v124\n" \
"v_add_u32 v7,63488,v5\ns_waitcnt lgkmcnt(0)\nv_mfma_f32_16x16x32_f16 v[80:83],v[112:115],v[40:43],0\nv_mov_b32 v118,v115\n" \
"v_mov_b32 v126,v123\nv_mfma_f32_16x16x32_f16 v[84:87],v[112:115],v[44:47],0\ns_lshr_b32 s26,s32,1\ns_mul_i32 s92,s26,1152\n" \
"v_mfma_f32_16x16x32_f16 v[88:91],v[120:123],v[40:43],0\nbuffer_store_dwordx4 v[104:107],v2,s[36:39],s31 offen sc1\nv_mfma_f32_16x16x32_f16 v[92:95],v[120:123],v[44:47],0\ns_add_u32 s31,s31,4096\n" \
"s_lshr_b32 s26,s3,1\nv_mfma_f32_16x16x32_f16 v[96:99],v[116:119],v[40:43],0\ns_lshl_b32 s26,s26,6\ns_add_u32 s92,s92,s26\n" \
"v_mfma_f32_16x16x32_f16 v[100:103],v[116:119],v[44:47],0\nv_pk_add_f32 v[88:89],v[80:81],v[88:89] neg_lo:[0,1] neg_hi:[0,1]\nv_pk_add_f32 v[90:91],v[82:83],v[90:91] neg_lo:[0,1] neg_hi:[0,1]\nv_pk_add_f32 v[92:93],v[84:85],v[92:93] neg_lo:[0,1] neg_hi:[0,1]\n" \
"buffer_store_dwordx4 v[104:107],v2,s[36:39],s31 offen sc1\nv_pk_add_f32 v[94:95],v[86:87],v[94:95] neg_lo:[0,1] neg_hi:[0,1]\nv_pk_fma_f32 v[80:81],v[80:81],2.0,v[88:89] op_sel_hi:[1,0,1] neg_lo:[0,0,1] neg_hi:[0,0,1]\nv_pk_fma_f32 v[82:83],v[82:83],2.0,v[90:91] op_sel_hi:[1,0,1] neg_lo:[0,0,1] neg_hi:[0,0,1]\n" \
"v_pk_fma_f32 v[84:85],v[84:85],2.0,v[92:93] op_sel_hi:[1,0,1] neg_lo:[0,0,1] neg_hi:[0,0,1]\nv_pk_fma_f32 v[86:87],v[86:87],2.0,v[94:95] op_sel_hi:[1,0,1] neg_lo:[0,0,1] neg_hi:[0,0,1]\nv_cvt_pk_f16_f32 v112,v80,v84\nv_cvt_pk_f16_f32 v113,v81,v85\n" \
"v_cvt_pk_f16_f32 v114,v82,v86\nv_cvt_pk_f16_f32 v115,v83,v87\nv_mfma_f32_16x16x32_f16 v[104:107],v[124:127],v[40:43],0\nv_cvt_pk_f16_f32 v116,v88,v92\n" \
"s_lshr_b32 s97,s33,3\ns_add_u32 s97,s97,0\ns_sub_u32 s95,s97,4\ns_min_u32 s97,s97,s95\n" \
"s_lshl_b32 s95,s97,12\ns_add_u32 s95,s95,s57\ns_add_u32 m0,s95,0\ns_and_b32 s95,s97,1\n" \
"s_lshr_b32 s97,s97,1\ns_lshl_b32 s97,s97,10\ns_add_u32 s97,s97,s95\ns_lshl_b32 s97,s97,7\n" \
"s_lshr_b32 s100,s33,3\ns_add_u32 s100,s100,1\ns_sub_u32 s101,s100,4\ns_min_u32 s100,s100,s101\n" \
"s_lshl_b32 s101,s100,12\ns_add_u32 s101,s101,s57\ns_lshr_b32 s31,s33,3\ns_add_u32 s31,s31,2\n" \
"s_sub_u32 s96,s31,4\ns_min_u32 s31,s31,s96\ns_lshl_b32 s96,s31,12\ns_add_u32 s96,s96,s57\n" \
"v_mfma_f32_16x16x32_f16 v[80:83],v[16:19],v[112:115],0\nv_cvt_pk_f16_f32 v117,v89,v93\nv_cvt_pk_f16_f32 v118,v90,v94\nv_mfma_f32_16x16x32_f16 v[84:87],v[20:23],v[112:115],0\n" \
"v_cvt_pk_f16_f32 v119,v91,v95\nv_pk_add_f32 v[104:105],v[96:97],v[104:105] neg_lo:[0,1] neg_hi:[0,1]\nv_mfma_f32_16x16x32_f16 v[108:111],v[124:127],v[44:47],0\nv_pk_add_f32 v[106:107],v[98:99],v[106:107] neg_lo:[0,1] neg_hi:[0,1]\n" \
"v_pk_fma_f32 v[96:97],v[96:97],2.0,v[104:105] op_sel_hi:[1,0,1] neg_lo:[0,0,1] neg_hi:[0,0,1]\nv_mfma_f32_16x16x32_f16 v[88:91],v[24:27],v[116:119],0\nv_pk_fma_f32 v[98:99],v[98:99],2.0,v[106:107] op_sel_hi:[1,0,1] neg_lo:[0,0,1] neg_hi:[0,0,1]\nds_read2_b64 v[40:43],v3 offset0:4 offset1:5\n" \
"v_mfma_f32_16x16x32_f16 v[92:95],v[28:31],v[116:119],0\nds_write2_b32 v48,v84,v80 offset0:0 offset1:1\nds_write2_b32 v49,v85,v81 offset0:0 offset1:1\nds_write2_b32 v50,v86,v82 offset0:0 offset1:1\n" \
"v_pk_add_f32 v[108:109],v[100:101],v[108:109] neg_lo:[0,1] neg_hi:[0,1]\nv_pk_add_f32 v[110:111],v[102:103],v[110:111] neg_lo:[0,1] neg_hi:[0,1]\nv_pk_fma_f32 v[100:101],v[100:101],2.0,v[108:109] op_sel_hi:[1,0,1] neg_lo:[0,0,1] neg_hi:[0,0,1]\nv_pk_fma_f32 v[102:103],v[102:103],2.0,v[110:111] op_sel_hi:[1,0,1] neg_lo:[0,0,1] neg_hi:[0,0,1]\n" \
"ds_write2_b32 v51,v87,v83 offset0:0 offset1:1\nv_cvt_pk_f16_f32 v120,v96,v100\nv_cvt_pk_f16_f32 v121,v97,v101\nv_cvt_pk_f16_f32 v122,v98,v102\n" \
"v_cvt_pk_f16_f32 v123,v99,v103\nds_write2_b32 v52,v92,v88 offset0:0 offset1:1\nds_write2_b32 v53,v93,v89 offset0:0 offset1:1\nv_mfma_f32_16x16x32_f16 v[96:99],v[16:19],v[120:123],0\n" \
"ds_write2_b32 v54,v94,v90 offset0:0 offset1:1\nv_mfma_f32_16x16x32_f16 v[100:103],v[20:23],v[120:123],0\nds_write2_b32 v55,v95,v91 offset0:0 offset1:1\nv_cvt_pk_f16_f32 v124,v104,v108\n" \
"v_cvt_pk_f16_f32 v125,v105,v109\nv_cvt_pk_f16_f32 v126,v106,v110\nv_cvt_pk_f16_f32 v127,v107,v111\nv_add_u32 v48,2448,v48\n" \
"v_add_u32 v49,2448,v49\nv_mfma_f32_16x16x32_f16 v[104:107],v[24:27],v[124:127],0\nds_write2_b32 v48,v100,v96 offset0:0 offset1:1\nv_mfma_f32_16x16x32_f16 v[108:111],v[28:31],v[124:127],0\n" \
"ds_write2_b32 v49,v101,v97 offset0:0 offset1:1\nv_add_u32 v50,2448,v50\nds_write2_b32 v50,v102,v98 offset0:0 offset1:1\nv_add_u32 v51,2448,v51\n" \
"ds_write2_b32 v51,v103,v99 offset0:0 offset1:1\nv_add_u32 v52,2448,v52\nv_add_u32 v53,2448,v53\nv_add_u32 v54,2448,v54\n" \
"ds_write2_b32 v52,v108,v104 offset0:0 offset1:1\nds_write2_b32 v53,v109,v105 offset0:0 offset1:1\nds_write2_b32 v54,v110,v106 offset0:0 offset1:1\nv_add_u32 v55,2448,v55\n" \
"ds_write2_b32 v55,v111,v107 offset0:0 offset1:1\nds_read2_b64 v[80:83],v13 offset0:0 offset1:1\nds_read2_b64 v[84:87],v13 offset0:2 offset1:3\ns_lshl_b32 s26,s3,8\n" \
"s_add_u32 s92,s92,s26\ns_lshl_b32 s92,s92,4\ns_and_b32 s26,s32,1\ns_lshl3_add_u32 s92,s26,s92\n" \
"s_and_b32 s26,s33,7\ns_add_u32 s92,s92,s26\nds_read2_b64 v[88:91],v13 offset0:4 offset1:5\ns_lshl_b32 s92,s92,8\n" \
"s_add_u32 s97,s97,s92\nbuffer_load_dwordx4 v4,s[40:43],s97 offen lds\nds_read2_b64 v[44:47],v3 offset0:6 offset1:7\nv_and_b32 v48,31,v0\n" \
"ds_read2_b64 v[92:95],v13 offset0:6 offset1:7\nv_mad_u32_u24 v49,v48,56,0\nv_sub_u32 v48,v13,v49\nv_and_b32 v49,31,v0\n" \
"v_mul_u32_u24 v49,68,v49\nv_sub_u32 v49,v13,v49\nbuffer_load_dwordx4 v5,s[40:43],s97 offen offset:1024 lds\nds_read_b64 v[96:97],v13 offset:64\n" \
"s_waitcnt lgkmcnt(5)\nv_pk_fma_f32 v[62:63],v[80:81],v[32:33],v[62:63]\nv_pk_fma_f32 v[64:65],v[82:83],v[34:35],v[64:65]\nv_pk_mul_f32 v[102:103],v[62:63],v[62:63]\n" \
"s_waitcnt lgkmcnt(4)\nv_pk_fma_f32 v[66:67],v[84:85],v[36:37],v[66:67]\nv_pk_add_f32 v[100:101],v[62:63],v[64:65]\nv_pk_fma_f32 v[102:103],v[64:65],v[64:65],v[102:103]\n" \
"v_pk_fma_f32 v[68:69],v[86:87],v[38:39],v[68:69]\nbuffer_load_dwordx4 v6,s[40:43],s97 offen offset:2048 lds\nv_pk_add_f32 v[100:101],v[100:101],v[66:67]\nv_pk_fma_f32 v[102:103],v[66:67],v[66:67],v[102:103]\n" \
"ds_read2_b64 v[112:115],v60 offset0:0 offset1:1\nds_read2_b64 v[116:119],v60 offset0:2 offset1:3\ns_waitcnt lgkmcnt(4)\nv_pk_fma_f32 v[70:71],v[88:89],v[40:41],v[70:71]\n" \
"v_pk_add_f32 v[100:101],v[100:101],v[68:69]\nds_read2_b64 v[120:123],v60 offset0:4 offset1:5\nv_pk_fma_f32 v[102:103],v[68:69],v[68:69],v[102:103]\nbuffer_load_dwordx4 v7,s[40:43],s97 offen offset:3072 lds\n" \
"ds_read2_b64 v[124:127],v60 offset0:6 offset1:7\nv_pk_fma_f32 v[72:73],v[90:91],v[42:43],v[72:73]\nv_pk_add_f32 v[100:101],v[100:101],v[70:71]\nds_read_b64 v[98:99],v60 offset:64\n" \
"v_add_u32 v60,2304,v60\nv_pk_fma_f32 v[102:103],v[70:71],v[70:71],v[102:103]\nds_read2_b64 v[16:19],v60 offset0:0 offset1:1\ns_waitcnt lgkmcnt(6)\n" \
"v_pk_fma_f32 v[74:75],v[92:93],v[44:45],v[74:75]\ns_add_u32 m0,s101,0\ns_and_b32 s101,s100,1\ns_lshr_b32 s100,s100,1\n" \
"s_lshl_b32 s100,s100,10\ns_add_u32 s100,s100,s101\ns_lshl_b32 s100,s100,7\ns_add_u32 s100,s100,s92\n" \
"buffer_load_dwordx4 v4,s[40:43],s100 offen lds\nv_pk_add_f32 v[100:101],v[100:101],v[72:73]\nv_pk_fma_f32 v[102:103],v[72:73],v[72:73],v[102:103]\nv_pk_fma_f32 v[76:77],v[94:95],v[46:47],v[76:77]\n" \
"v_pk_add_f32 v[100:101],v[100:101],v[74:75]\nv_pk_fma_f32 v[102:103],v[74:75],v[74:75],v[102:103]\nds_read2_b64 v[20:23],v60 offset0:2 offset1:3\nv_pk_fma_f32 v[78:79],v[96:97],v[56:57],v[78:79]\n" \
"v_pk_add_f32 v[100:101],v[100:101],v[76:77]\nbuffer_load_dwordx4 v5,s[40:43],s100 offen offset:1024 lds\nv_pk_fma_f32 v[102:103],v[76:77],v[76:77],v[102:103]\nv_pk_add_f32 v[100:101],v[100:101],v[78:79]\n" \
"v_pk_fma_f32 v[102:103],v[78:79],v[78:79],v[102:103]\nds_read2_b64 v[24:27],v60 offset0:4 offset1:5\nv_add_f32 v100,v100,v101\nv_add_f32 v102,v102,v103\n" \
"ds_read2_b64 v[28:31],v60 offset0:6 offset1:7\nds_read_b64 v[58:59],v60 offset:64\nbuffer_load_dwordx4 v6,s[40:43],s100 offen offset:2048 lds\nv_permlane16_swap_b32 v100,v102\n" \
"v_add_f32 v100,v100,v102\ns_lshr_b32 s97,s33,3\ns_add_u32 s97,s97,3\nv_add_f32_dpp v100,v100,v100 quad_perm:[1,0,3,2] row_mask:0xf bank_mask:0xf bound_ctrl:1\n" \
"s_sub_u32 s95,s97,4\ns_min_u32 s97,s97,s95\nv_add_f32_dpp v100,v100,v100 quad_perm:[2,3,0,1] row_mask:0xf bank_mask:0xf bound_ctrl:1\nbuffer_load_dwordx4 v7,s[40:43],s100 offen offset:3072 lds\n" \
"s_add_u32 m0,s96,0\nv_add_f32_dpp v100,v100,v100 row_half_mirror row_mask:0xf bank_mask:0xf bound_ctrl:1\ns_and_b32 s96,s31,1\ns_lshr_b32 s31,s31,1\n" \
"v_add_f32_dpp v100,v100,v100 row_mirror row_mask:0xf bank_mask:0xf bound_ctrl:1\nv_mul_f32 v104,0x3ae38e39,v100\nv_mul_f32 v102,0x3ae38e39,v100\ns_lshl_b32 s31,s31,10\n" \
"s_add_u32 s31,s31,s96\ns_lshl_b32 s31,s31,7\ns_add_u32 s31,s31,s92\nbuffer_load_dwordx4 v4,s[40:43],s31 offen lds\n" \
"v_permlane16_swap_b32 v104,v102\nv_fma_f32 v102,-v104,v104,v102\nv_max_f32 v102,0,v102\nv_add_f32 v102,0x3727c5ac,v102\n" \
"v_rsq_f32 v100,v102\ns_lshl_b32 s95,s97,12\nbuffer_load_dwordx4 v5,s[40:43],s31 offen offset:1024 lds\nv_mul_f32 v101,-v104,v100\n" \
"v_pk_fma_f32 v[80:81],v[62:63],v[100:101],v[100:101] op_sel:[0,0,1] op_sel_hi:[1,0,1]\nv_pk_fma_f32 v[82:83],v[64:65],v[100:101],v[100:101] op_sel:[0,0,1] op_sel_hi:[1,0,1]\nv_pk_fma_f32 v[84:85],v[66:67],v[100:101],v[100:101] op_sel:[0,0,1] op_sel_hi:[1,0,1]\nv_pk_fma_f32 v[86:87],v[68:69],v[100:101],v[100:101] op_sel:[0,0,1] op_sel_hi:[1,0,1]\n" \
"v_pk_fma_f32 v[88:89],v[70:71],v[100:101],v[100:101] op_sel:[0,0,1] op_sel_hi:[1,0,1]\nv_pk_fma_f32 v[90:91],v[72:73],v[100:101],v[100:101] op_sel:[0,0,1] op_sel_hi:[1,0,1]\nv_pk_fma_f32 v[92:93],v[74:75],v[100:101],v[100:101] op_sel:[0,0,1] op_sel_hi:[1,0,1]\nbuffer_load_dwordx4 v6,s[40:43],s31 offen offset:2048 lds\n" \
"v_pk_fma_f32 v[94:95],v[76:77],v[100:101],v[100:101] op_sel:[0,0,1] op_sel_hi:[1,0,1]\nv_pk_fma_f32 v[96:97],v[78:79],v[100:101],v[100:101] op_sel:[0,0,1] op_sel_hi:[1,0,1]\ns_add_u32 s95,s95,s57\ns_waitcnt lgkmcnt(0)\n" \
"v_readlane_b32 s24,v112,0\nv_mov_b32 v60,v61\ns_mov_b32 s25,s24\nv_pk_add_f32 v[112:113],v[112:113],s[24:25] neg_lo:[0,1] neg_hi:[0,1]\n" \
"v_pk_fma_f32 v[80:81],v[80:81],v[16:17],v[112:113]\nbuffer_load_dwordx4 v7,s[40:43],s31 offen offset:3072 lds\nv_pk_mul_f32 v[102:103],v[80:81],v[80:81]\ns_add_u32 m0,s95,0\n" \
"s_and_b32 s95,s97,1\ns_lshr_b32 s97,s97,1\ns_lshl_b32 s97,s97,10\ns_add_u32 s97,s97,s95\n" \
"s_lshl_b32 s97,s97,7\ns_add_u32 s97,s97,s92\nbuffer_load_dwordx4 v4,s[40:43],s97 offen lds\nv_pk_add_f32 v[114:115],v[114:115],s[24:25] neg_lo:[0,1] neg_hi:[0,1]\n" \
"v_pk_fma_f32 v[82:83],v[82:83],v[18:19],v[114:115]\nv_pk_add_f32 v[100:101],v[80:81],v[82:83]\nv_pk_fma_f32 v[102:103],v[82:83],v[82:83],v[102:103]\nv_pk_add_f32 v[116:117],v[116:117],s[24:25] neg_lo:[0,1] neg_hi:[0,1]\n" \
"v_pk_fma_f32 v[84:85],v[84:85],v[20:21],v[116:117]\nv_pk_add_f32 v[100:101],v[100:101],v[84:85]\nbuffer_load_dwordx4 v5,s[40:43],s97 offen offset:1024 lds\nv_pk_fma_f32 v[102:103],v[84:85],v[84:85],v[102:103]\n" \
"v_pk_add_f32 v[118:119],v[118:119],s[24:25] neg_lo:[0,1] neg_hi:[0,1]\nv_pk_fma_f32 v[86:87],v[86:87],v[22:23],v[118:119]\nv_pk_add_f32 v[100:101],v[100:101],v[86:87]\nv_pk_fma_f32 v[102:103],v[86:87],v[86:87],v[102:103]\n" \
"v_pk_add_f32 v[120:121],v[120:121],s[24:25] neg_lo:[0,1] neg_hi:[0,1]\nv_pk_fma_f32 v[88:89],v[88:89],v[24:25],v[120:121]\nbuffer_load_dwordx4 v6,s[40:43],s97 offen offset:2048 lds\nv_pk_add_f32 v[100:101],v[100:101],v[88:89]\n" \
"v_pk_fma_f32 v[102:103],v[88:89],v[88:89],v[102:103]\nv_pk_add_f32 v[122:123],v[122:123],s[24:25] neg_lo:[0,1] neg_hi:[0,1]\nv_pk_fma_f32 v[90:91],v[90:91],v[26:27],v[122:123]\nv_pk_add_f32 v[100:101],v[100:101],v[90:91]\n" \
"v_pk_fma_f32 v[102:103],v[90:91],v[90:91],v[102:103]\nv_pk_add_f32 v[124:125],v[124:125],s[24:25] neg_lo:[0,1] neg_hi:[0,1]\nbuffer_load_dwordx4 v7,s[40:43],s97 offen offset:3072 lds\nv_pk_fma_f32 v[92:93],v[92:93],v[28:29],v[124:125]\n" \
"v_pk_add_f32 v[100:101],v[100:101],v[92:93]\nv_pk_fma_f32 v[102:103],v[92:93],v[92:93],v[102:103]\nv_pk_add_f32 v[126:127],v[126:127],s[24:25] neg_lo:[0,1] neg_hi:[0,1]\nv_pk_fma_f32 v[94:95],v[94:95],v[30:31],v[126:127]\n" \
"v_pk_add_f32 v[100:101],v[100:101],v[94:95]\nv_pk_fma_f32 v[102:103],v[94:95],v[94:95],v[102:103]\nv_pk_add_f32 v[98:99],v[98:99],s[24:25] neg_lo:[0,1] neg_hi:[0,1]\nv_pk_fma_f32 v[96:97],v[96:97],v[58:59],v[98:99]\n" \
"v_pk_add_f32 v[100:101],v[100:101],v[96:97]\nv_pk_fma_f32 v[102:103],v[96:97],v[96:97],v[102:103]\nv_add_f32 v100,v100,v101\nv_add_f32 v102,v102,v103\n" \
"v_pk_add_f32 v[62:63],v[62:63],v[60:61] op_sel_hi:[1,0]\nv_cvt_pk_bf16_f32 v32,v62,v63\nv_permlane16_swap_b32 v100,v102\nv_add_f32 v100,v100,v102\n" \
"v_pk_add_f32 v[64:65],v[64:65],v[60:61] op_sel_hi:[1,0]\nv_cvt_pk_bf16_f32 v33,v64,v65\nv_add_f32_dpp v100,v100,v100 quad_perm:[1,0,3,2] row_mask:0xf bank_mask:0xf bound_ctrl:1\nv_pk_add_f32 v[66:67],v[66:67],v[60:61] op_sel_hi:[1,0]\n" \
"v_cvt_pk_bf16_f32 v34,v66,v67\nv_add_f32_dpp v100,v100,v100 quad_perm:[2,3,0,1] row_mask:0xf bank_mask:0xf bound_ctrl:1\nv_pk_add_f32 v[68:69],v[68:69],v[60:61] op_sel_hi:[1,0]\nv_cvt_pk_bf16_f32 v35,v68,v69\n" \
"v_add_f32_dpp v100,v100,v100 row_half_mirror row_mask:0xf bank_mask:0xf bound_ctrl:1\nv_pk_add_f32 v[70:71],v[70:71],v[60:61] op_sel_hi:[1,0]\nv_cvt_pk_bf16_f32 v36,v70,v71\nv_add_f32_dpp v100,v100,v100 row_mirror row_mask:0xf bank_mask:0xf bound_ctrl:1\n" \
"v_mul_f32 v104,0x3ae38e39,v100\nv_mul_f32 v102,0x3ae38e39,v100\nv_pk_add_f32 v[72:73],v[72:73],v[60:61] op_sel_hi:[1,0]\nv_cvt_pk_bf16_f32 v37,v72,v73\n" \
"v_permlane16_swap_b32 v104,v102\nv_fma_f32 v102,-v104,v104,v102\nv_max_f32 v102,0,v102\nv_add_f32 v102,0x3727c5ac,v102\n" \
"v_rsq_f32_e64 v100,v102\nv_pk_add_f32 v[74:75],v[74:75],v[60:61] op_sel_hi:[1,0]\nv_mul_f32 v101,-v104,v100\nv_pk_fma_f32 v[80:81],v[80:81],v[100:101],v[100:101] op_sel:[0,0,1] op_sel_hi:[1,0,1]\n" \
"v_pk_fma_f32 v[82:83],v[82:83],v[100:101],v[100:101] op_sel:[0,0,1] op_sel_hi:[1,0,1]\nv_pk_fma_f32 v[84:85],v[84:85],v[100:101],v[100:101] op_sel:[0,0,1] op_sel_hi:[1,0,1]\nv_pk_fma_f32 v[86:87],v[86:87],v[100:101],v[100:101] op_sel:[0,0,1] op_sel_hi:[1,0,1]\nv_cvt_pk_bf16_f32 v16,v80,v81\n" \
"v_cvt_pk_bf16_f32 v17,v82,v83\nv_cvt_pk_bf16_f32 v18,v84,v85\nv_cvt_pk_bf16_f32 v19,v86,v87\nds_write_b128 v48,v[16:19]\n" \
"v_pk_fma_f32 v[88:89],v[88:89],v[100:101],v[100:101] op_sel:[0,0,1] op_sel_hi:[1,0,1]\nv_pk_fma_f32 v[90:91],v[90:91],v[100:101],v[100:101] op_sel:[0,0,1] op_sel_hi:[1,0,1]\nv_pk_fma_f32 v[92:93],v[92:93],v[100:101],v[100:101] op_sel:[0,0,1] op_sel_hi:[1,0,1]\nv_pk_fma_f32 v[94:95],v[94:95],v[100:101],v[100:101] op_sel:[0,0,1] op_sel_hi:[1,0,1]\n" \
"v_cvt_pk_bf16_f32 v20,v88,v89\nv_cvt_pk_bf16_f32 v21,v90,v91\nv_cvt_pk_bf16_f32 v22,v92,v93\nv_cvt_pk_bf16_f32 v23,v94,v95\n" \
"ds_write_b128 v48,v[20:23] offset:512\nv_pk_fma_f32 v[96:97],v[96:97],v[100:101],v[100:101] op_sel:[0,0,1] op_sel_hi:[1,0,1]\nv_cvt_pk_bf16_f32 v24,v96,v97\nds_write_b32 v49,v24 offset:1024\n" \
"v_cvt_pk_bf16_f32 v38,v74,v75\nv_pk_add_f32 v[76:77],v[76:77],v[60:61] op_sel_hi:[1,0]\nv_cvt_pk_bf16_f32 v39,v76,v77\nv_pk_add_f32 v[78:79],v[78:79],v[60:61] op_sel_hi:[1,0]\n" \
"v_cvt_pk_bf16_f32 v40,v78,v79\nv_bfe_u32 v50,v0,2,2\nv_mul_u32_u24 v50,4896,v50\nv_bfe_u32 v51,v0,4,2\n" \
"v_lshl_add_u32 v51,v51,2,s3\nv_lshl_add_u32 v50,v51,5,v50\nv_and_b32 v52,3,v0\nv_lshl_add_u32 v50,v52,3,v50\n" \
"v_and_b32 v52,0xf0,v1\nv_lshl_add_u32 v51,v51,16,v52\nv_bfrev_b32 v52,1\nv_cmp_gt_u32 vcc,256,v1\n" \
"v_and_b32 v53,31,v0\nv_mul_u32_u24 v53,12,v53\nv_cndmask_b32 v52,v52,v51,vcc\nv_sub_u32 v53,v12,v53\n" \
"s_lshl_b32 s24,s34,8\ns_add_u32 s24,s24,9764864\ns_waitcnt lgkmcnt(0)\ns_barrier\n" \
"ds_read_b64_tr_b16 v[112:113],v50 offset:9216\nds_read_b64_tr_b16 v[114:115],v50 offset:11664\nds_read_b64_tr_b16 v[116:117],v50 offset:9728\nds_read_b64_tr_b16 v[118:119],v50 offset:12176\n" \
"ds_read_b64_tr_b16 v[120:121],v50 offset:10240\nds_read_b64_tr_b16 v[122:123],v50 offset:12688\ns_add_u32 s25,s24,1048576\ns_add_u32 s26,s24,2097152\n" \
"s_add_u32 s13,s13,7405568\ns_load_dwordx2 s[4:5],s[0:1],56\ns_load_dwordx2 s[16:17],s[0:1],64\ns_waitcnt lgkmcnt(0)\n" \
"buffer_store_dwordx4 v[112:115],v51,s[36:39],s24 offen sc1\nbuffer_store_dwordx4 v[116:119],v51,s[36:39],s25 offen sc1\nbuffer_store_dwordx4 v[120:123],v52,s[36:39],s26 offen sc1\nbuffer_store_dwordx4 v[32:35],v12,s[36:39],s13 offen sc1\n" \
"buffer_store_dwordx4 v[36:39],v12,s[36:39],s13 offen offset:512 sc1\nbuffer_store_dword v40,v53,s[36:39],s13 offen offset:1024 sc1\ns_lshr_b32 s100,s33,3\ns_lshl1_add_u32 s100,s100,0\n" \
"s_sub_u32 s101,s100,7\ns_min_u32 s100,s100,s101\ns_lshl_b32 s101,s100,12\ns_add_u32 s101,s101,s57\n" \
"s_add_u32 m0,s101,0\ns_and_b32 s101,s100,1\ns_lshr_b32 s100,s100,1\ns_lshl_b32 s100,s100,10\n" \
"s_add_u32 s100,s100,s101\ns_lshl_b32 s100,s100,7\ns_add_u32 s100,s100,s92\ns_add_u32 s100,s100,262144\n" \
"s_lshr_b32 s31,s33,3\ns_lshl1_add_u32 s31,s31,1\ns_sub_u32 s96,s31,7\ns_min_u32 s31,s31,s96\n" \
"s_lshl_b32 s96,s31,12\ns_add_u32 s96,s96,s57\ns_lshr_b32 s97,s33,3\ns_lshl1_add_u32 s97,s97,2\n" \
"s_sub_u32 s95,s97,7\ns_min_u32 s97,s97,s95\ns_lshl_b32 s95,s97,12\ns_add_u32 s95,s95,s57\n" \
"v_and_b32 v64,63,v0\ns_and_b32 s25,s33,7\nv_lshrrev_b32 v66,5,v64\nv_and_b32 v67,1,v0\n" \
"s_lshl2_add_u32 s25,s25,s3\ns_and_b32 s26,s33,6\ns_lshr_b32 s27,s3,1\nv_lshl_add_u32 v66,v66,1,v67\n" \
"s_mul_i32 s24,s32,1152\ns_lshl_b32 s25,s25,5\ns_add_u32 s26,s26,s27\ns_mov_b32 s6,9216\n" \
"s_mov_b32 s7,0x20000\nv_lshlrev_b32 v68,3,v66\ns_add_u32 s25,s25,s24\nv_bfe_u32 v72,v0,1,1\n" \
"v_lshrrev_b32 v74,2,v64\ns_lshl4_add_u32 s26,s26,s24\nv_and_b32 v73,12,v0\nv_lshrrev_b32 v71,4,v64\n" \
"v_and_b32 v69,7,v0\nbuffer_load_dwordx2 v[80:81],v68,s[4:7],s25 offen\nv_lshl_add_u32 v72,v72,2,s21\nv_and_b32 v74,12,v74\n" \
"v_add_u32 v71,v71,v73\ns_lshl_b32 s24,s3,4\nv_lshlrev_b32 v70,1,v69\nv_lshl_add_u32 v72,v72,4,v74\n" \
"s_mov_b32 s15,s19\nv_add_u32 v71,s24,v71\nbuffer_load_ushort v82,v70,s[4:7],s26 offen offset:1024\ns_mov_b32 s18,4096\n" \
"s_mov_b32 s19,0x20000\nv_lshlrev_b32 v74,1,v71\ns_lshl_b32 s24,s22,8\nbuffer_load_ushort v83,v74,s[16:19],s24 offen\n" \
"v_add_u32 v72,s3,v72\nbuffer_load_ushort v84,v74,s[16:19],s24 offen offset:128\nv_lshlrev_b32_e64 v75,1,v72\nbuffer_load_ushort v85,v75,s[16:19],s24 offen\n" \
"v_lshl_add_u32 v73,v67,1,v73\nv_mul_u32_u24 v91,1152,v72\nv_lshl_add_u32 v91,v73,1,v91\nv_lshlrev_b32_e64 v73,2,v73\n" \
"v_add_u32 v73,163712,v73\nds_read_b64 v[86:87],v73\ns_mul_i32 s24,s22,147456\ns_mul_i32 s25,s20,288\n" \
"v_bfe_u32 v58,v0,1,1\ns_add_u32 s24,s24,s25\nv_mul_u32_u24 v58,544,v58\nv_bfe_u32 v75,v0,2,1\n" \
"s_lshl_b32 s25,s21,6\nv_lshl_add_u32 v58,v75,8,v58\ns_add_u32 s25,s25,s24\nv_bfe_u32 v75,v0,3,2\n" \
"s_add_u32 s24,s24,256\nv_lshl_add_u32 v58,v75,6,v58\nv_add_u32 v91,s24,v91\nv_mul_u32_u24 v90,1152,v71\n" \
"v_and_b32 v74,3,v0\nv_lshl_add_u32 v58,v66,4,v58\ns_mul_i32 s24,s3,1056\nv_lshl_add_u32 v90,v74,3,v90\n" \
"v_add_u32 v58,s24,v58\nv_add_u32 v90,s25,v90\ns_and_b32 s24,s3,1\nv_mul_u32_u24 v60,288,v75\n" \
"s_lshl_b32 s24,s24,2\ns_lshr_b32 s25,s3,1\nv_lshrrev_b32 v75,5,v64\ns_add_u32 s24,s24,s25\n" \
"v_lshl_add_u32 v60,v75,6,v60\ns_lshl_b32 s24,s24,5\nv_lshl_add_u32 v60,v69,2,v60\ns_add_u32 s24,s24,4224\n" \
"v_add_u32 v60,s24,v60\ns_bfe_u32 s24,s33,0x10001\ns_add_u32 s24,s24,s15\ns_add_u32 s24,s24,17\n" \
"s_lshl2_add_u32 s24,s24,s3\ns_waitcnt lgkmcnt(0)\nv_lshrrev_b32 v86,16,v86\ns_lshl_b32 s24,s24,7\n" \
"v_mov_b32 v92,v4\nv_mov_b32 v93,v5\nv_add_u32 v74,s57,v1\nv_or_b32 v95,v86,v87\n" \
"v_lshl_add_u32 v62,v64,1,s24\nv_bfrev_b32 v76,1\nv_cmp_eq_u32 vcc,0,v64\ns_lshl2_add_u32 s24,s34,s3\n" \
"s_lshl2_add_u32 s24,s24,65536\nv_cndmask_b32 v76,v76,0,vcc\nv_mov_b32 v77,1\ns_barrier\n" \
"v_mov_b32 v57,0xc0135761\nv_add_u32 v59,0x80000,v1\nv_lshl_add_u32 v61,v64,3,s23\nv_mov_b32 v63,0\n" \
"v_bfe_u32 v100,v0,4,2\nv_bfe_u32 v101,v0,2,2\nv_lshl_add_u32 v100,v100,3,v101\nv_bfe_u32 v101,v0,3,2\n" \
"v_lshl_add_u32 v100,v100,2,v101\nv_and_b32 v101,3,v0\nv_lshl_add_u32 v100,v100,2,v101\nv_lshlrev_b32 v100,3,v100\n" \
"v_xor_b32 v8,0,v100\nv_add_u32 v8,s57,v8\nv_mov_b32 v88,v8\nv_xor_b32 v9,32,v100\n" \
"v_add_u32 v9,s57,v9\nv_xor_b32 v10,64,v100\nv_add_u32 v10,s57,v10\nv_xor_b32 v11,96,v100\n" \
"v_add_u32 v11,s57,v11\ns_waitcnt vmcnt(5)\nbuffer_store_dword v77,v76,s[36:39],s24 offen sc1\nds_read_b64_tr_b16 v[128:129],v8\n" \
"ds_read_b64_tr_b16 v[130:131],v8 offset:512\nds_read_b64_tr_b16 v[132:133],v9\nds_read_b64_tr_b16 v[134:135],v9 offset:512\nds_read_b64_tr_b16 v[136:137],v10\n" \
"ds_read_b64_tr_b16 v[138:139],v10 offset:512\nds_read_b64_tr_b16 v[140:141],v11\nds_read_b64_tr_b16 v[142:143],v11 offset:512\nds_read_b64_tr_b16 v[144:145],v8 offset:4096\n" \
"ds_read_b64_tr_b16 v[146:147],v8 offset:4608\nds_read_b64_tr_b16 v[148:149],v9 offset:4096\nds_read_b64_tr_b16 v[150:151],v9 offset:4608\nds_read_b64_tr_b16 v[152:153],v10 offset:4096\n" \
"ds_read_b64_tr_b16 v[154:155],v10 offset:4608\nds_read_b64_tr_b16 v[156:157],v11 offset:4096\nds_read_b64_tr_b16 v[158:159],v11 offset:4608\nds_read_b64_tr_b16 v[160:161],v8 offset:8192\n" \
"ds_read_b64_tr_b16 v[162:163],v8 offset:8704\ns_waitcnt vmcnt(5)\nv_and_b32 v53,0xffff0000,v80\nv_and_b32 v55,0xffff0000,v81\n" \
"v_lshlrev_b32 v52,16,v80\nv_lshlrev_b32 v54,16,v81\nds_read_b64_tr_b16 v[164:165],v9 offset:8192\nds_read_b64_tr_b16 v[166:167],v9 offset:8704\n" \
"ds_read_b64_tr_b16 v[168:169],v10 offset:8192\nds_read_b64_tr_b16 v[170:171],v10 offset:8704\nds_read_b64_tr_b16 v[172:173],v11 offset:8192\ns_waitcnt vmcnt(4)\n" \
"v_lshlrev_b32 v56,16,v82\nds_read_b64_tr_b16 v[174:175],v11 offset:8704\nds_read_b64_tr_b16 v[176:177],v8 offset:12288\nds_read_b64_tr_b16 v[178:179],v8 offset:12800\n" \
"ds_read_b64_tr_b16 v[180:181],v9 offset:12288\nds_read_b64_tr_b16 v[182:183],v9 offset:12800\ns_waitcnt vmcnt(2)\nv_lshl_or_b32 v89,v84,16,v83\n" \
"ds_write_b128 v74,v[88:91] offset:28672\ns_waitcnt vmcnt(1)\nv_lshlrev_b32_e64 v94,16,v85\nds_write_b128 v74,v[92:95] offset:29696\n" \
"ds_read_b64_tr_b16 v[184:185],v10 offset:12288\nds_read_b64_tr_b16 v[186:187],v10 offset:12800\nds_read_b64_tr_b16 v[188:189],v11 offset:12288\nds_read_b64_tr_b16 v[190:191],v11 offset:12800\n" \
"s_waitcnt lgkmcnt(0)\nbuffer_load_dwordx4 v4,s[40:43],s100 offen lds\nbuffer_load_dwordx4 v5,s[40:43],s100 offen offset:1024 lds\nbuffer_load_dwordx4 v6,s[40:43],s100 offen offset:2048 lds\n" \
"buffer_load_dwordx4 v7,s[40:43],s100 offen offset:3072 lds\ns_add_u32 m0,s96,0\ns_and_b32 s96,s31,1\ns_lshr_b32 s31,s31,1\n" \
"s_lshl_b32 s31,s31,10\ns_add_u32 s31,s31,s96\ns_lshl_b32 s31,s31,7\ns_add_u32 s31,s31,s92\n" \
"s_add_u32 s31,s31,262144\nbuffer_load_dwordx4 v4,s[40:43],s31 offen lds\nbuffer_load_dwordx4 v5,s[40:43],s31 offen offset:1024 lds\nbuffer_load_dwordx4 v6,s[40:43],s31 offen offset:2048 lds\n" \
"buffer_load_dwordx4 v7,s[40:43],s31 offen offset:3072 lds\ns_add_u32 m0,s95,0\ns_and_b32 s95,s97,1\ns_lshr_b32 s97,s97,1\n" \
"s_lshl_b32 s97,s97,10\ns_add_u32 s97,s97,s95\ns_lshl_b32 s97,s97,7\ns_add_u32 s97,s97,s92\n" \
"s_add_u32 s97,s97,262144\nbuffer_load_dwordx4 v4,s[40:43],s97 offen lds\nbuffer_load_dwordx4 v5,s[40:43],s97 offen offset:1024 lds\nbuffer_load_dwordx4 v6,s[40:43],s97 offen offset:2048 lds\n" \
"buffer_load_dwordx4 v7,s[40:43],s97 offen offset:3072 lds\ns_lshr_b32 s100,s33,3\ns_lshl1_add_u32 s100,s100,3\ns_sub_u32 s101,s100,7\n" \
"s_min_u32 s100,s100,s101\ns_lshl_b32 s101,s100,12\ns_add_u32 s101,s101,s57\ns_add_u32 m0,s101,0\n" \
"s_and_b32 s101,s100,1\ns_lshr_b32 s100,s100,1\ns_lshl_b32 s100,s100,10\ns_add_u32 s100,s100,s101\n" \
"s_lshl_b32 s100,s100,7\ns_add_u32 s100,s100,s92\ns_add_u32 s100,s100,262144\nbuffer_load_dwordx4 v4,s[40:43],s100 offen lds\n" \
"buffer_load_dwordx4 v5,s[40:43],s100 offen offset:1024 lds\nbuffer_load_dwordx4 v6,s[40:43],s100 offen offset:2048 lds\nbuffer_load_dwordx4 v7,s[40:43],s100 offen offset:3072 lds\ns_lshr_b32 s31,s33,3\n" \
"s_lshl1_add_u32 s31,s31,4\ns_sub_u32 s96,s31,7\ns_min_u32 s31,s31,s96\ns_lshl_b32 s96,s31,12\n" \
"s_add_u32 s96,s96,s57\ns_add_u32 m0,s96,0\ns_and_b32 s96,s31,1\ns_lshr_b32 s31,s31,1\n" \
"s_lshl_b32 s31,s31,10\ns_add_u32 s31,s31,s96\ns_lshl_b32 s31,s31,7\ns_add_u32 s31,s31,s92\n" \
"s_add_u32 s31,s31,262144\nbuffer_load_dwordx4 v4,s[40:43],s31 offen lds\nbuffer_load_dwordx4 v5,s[40:43],s31 offen offset:1024 lds\nbuffer_load_dwordx4 v6,s[40:43],s31 offen offset:2048 lds\n" \
"buffer_load_dwordx4 v7,s[40:43],s31 offen offset:3072 lds\ns_lshr_b32 s97,s33,3\ns_lshl1_add_u32 s97,s97,5\ns_sub_u32 s95,s97,7\n" \
"s_min_u32 s97,s97,s95\ns_lshl_b32 s95,s97,12\ns_add_u32 s95,s95,s57\ns_add_u32 m0,s95,0\n" \
"s_and_b32 s95,s97,1\ns_lshr_b32 s97,s97,1\ns_lshl_b32 s97,s97,10\ns_add_u32 s97,s97,s95\n" \
"s_lshl_b32 s97,s97,7\ns_add_u32 s97,s97,s92\ns_add_u32 s97,s97,262144\nbuffer_load_dwordx4 v4,s[40:43],s97 offen lds\n" \
"buffer_load_dwordx4 v5,s[40:43],s97 offen offset:1024 lds\nbuffer_load_dwordx4 v6,s[40:43],s97 offen offset:2048 lds\nbuffer_load_dwordx4 v7,s[40:43],s97 offen offset:3072 lds\ns_lshr_b32 s100,s33,3\n" \
"s_lshl1_add_u32 s100,s100,6\ns_sub_u32 s101,s100,7\ns_min_u32 s100,s100,s101\ns_lshl_b32 s101,s100,12\n" \
"s_add_u32 s101,s101,s57\ns_add_u32 m0,s101,0\ns_and_b32 s101,s100,1\ns_lshr_b32 s100,s100,1\n" \
"s_lshl_b32 s100,s100,10\ns_add_u32 s100,s100,s101\ns_lshl_b32 s100,s100,7\ns_add_u32 s100,s100,s92\n" \
"s_add_u32 s100,s100,262144\nbuffer_load_dwordx4 v4,s[40:43],s100 offen lds\nbuffer_load_dwordx4 v5,s[40:43],s100 offen offset:1024 lds\nbuffer_load_dwordx4 v6,s[40:43],s100 offen offset:2048 lds\n" \
"buffer_load_dwordx4 v7,s[40:43],s100 offen offset:3072 lds\ns_branch .Lgemm_%=\n.LN_%=:\nv_bfe_u32 v112,v0,3,2\n" \
"v_bfe_u32 v113,v0,5,1\nv_lshl_add_u32 v112,v112,8,v113\ns_lshl_b32 s48,s33,2\nv_lshrrev_b32 v113,3,v0\n" \
"s_add_u32 s48,s48,s3\nv_and_b32 v113,6,v113\nv_and_b32 v114,7,v0\ns_mul_i32 s48,s48,18432\n" \
"s_mul_i32 s49,s32,1152\nv_mul_u32_u24 v112,9216,v112\nv_xor_b32 v113,v113,v114\ns_add_u32 s48,s48,s49\n" \
"v_lshl_add_u32 v4,v113,4,v112\ns_mov_b32 s10,18874368\ns_mov_b32 s11,0x20000\ns_add_u32 s48,s48,1024\n" \
"s_add_u32 m0,s88,20224\ns_add_u32 s50,s48,9437184\nbuffer_load_dwordx4 v4,s[8:11],s48 offen lds\ns_add_u32 m0,s88,24320\n" \
"s_mov_b32 s51,4096\nbuffer_load_dwordx4 v4,s[8:11],s50 offen lds\ns_add_u32 m0,s88,28416\ns_mov_b32 s52,8192\n" \
"buffer_load_dwordx4 v2,s[4:7],0 offen lds\ns_add_u32 m0,s88,32512\ns_mov_b32 s53,14336\nbuffer_load_dwordx4 v2,s[4:7],s51 offen lds\n" \
"buffer_load_dwordx4 v[234:237],v1,s[4:7],s52 offen\nbuffer_load_dwordx4 v[238:241],v1,s[4:7],s52 offen offset:1024\nbuffer_load_dwordx4 v[242:245],v2,s[4:7],s53 offen\ns_mov_b32 s52,18432\n" \
"buffer_load_dwordx4 v[252:255],v2,s[4:7],s52 offen\ns_and_b32 s60,s33,7\ns_mul_i32 s92,s3,589824\ns_mul_i32 s57,s3,30720\n" \
"s_add_u32 s92,s92,s49\ns_lshl_b32 s62,s60,7\nv_add_u32 v5,17408,v4\nv_add_u32 v6,34816,v4\n" \
"v_add_u32 v7,52224,v4\ns_add_u32 s57,s57,38272\ns_add_u32 s92,s92,s62\ns_load_dwordx4 s[44:47],s[0:1],72\n" \
"v_and_b32 v120,0x3c0,v1\ns_lshr_b32 s59,s33,3\nv_bfe_u32 v121,v0,3,2\nv_lshlrev_b32 v120,1,v120\n" \
"s_lshl_b32 s56,s34,8\ns_lshl_b32 s59,s59,3\nv_bfe_u32 v113,v0,4,2\nv_lshl_add_u32 v120,v121,5,v120\n" \
"v_and_b32 v122,3,v0\ns_mul_i32 s54,s34,9344\ns_add_u32 s56,s56,10289152\ns_add_u32 s59,s59,s32\n" \
"v_mul_u32_u24 v113,589824,v113\nv_and_b32 v112,15,v0\nv_lshl_add_u32 v120,v122,3,v120\ns_add_u32 s35,s54,524288\n" \
"s_cmp_eq_u32 s3,2\ns_cselect_b32 s56,s56,2147483648\ns_lshl_b32 s59,s59,3\nv_lshl_add_u32 v113,v112,4,v113\n" \
"s_add_u32 s58,s35,8192\nv_bfe_u32 v119,v0,4,2\nv_bfe_u32 v233,v0,3,3\nv_mul_u32_u24 v233,8192,v233\n" \
"v_lshl_add_u32 v233,v1,3,v233\ns_lshl2_add_u32 s100,s33,s3\ns_add_u32 s100,s100,4294967260\ns_and_b32 s101,s100,31\n" \
"s_lshl_b32 s101,s101,3\ns_cmp_lt_u32 s100,256\ns_cselect_b32 s100,s100,0x40000\ns_lshr_b32 s100,s100,5\n" \
"s_add_u32 s100,s100,s101\ns_lshl_b32 s100,s100,6\ns_add_u32 s100,s100,s32\ns_mul_i32 s100,s100,1152\n" \
"s_mov_b32 m0,162176\nv_xor_b32 v8,0,v120\nv_xor_b32 v9,32,v120\nv_xor_b32 v10,64,v120\n" \
"v_xor_b32 v11,96,v120\ns_add_u32 s59,s59,s60\nv_and_b32 v118,15,v0\nv_and_b32 v116,0x70,v2\n" \
"v_add_u32 v113,s56,v113\nv_add_u32 v114,s58,v2\nv_cmp_gt_u32 vcc,68,v0\nv_lshrrev_b32 v115,3,v0\n" \
"v_cvt_f32_u32 v120,s34\ns_lshl_b32 s59,s59,7\ns_lshl_b32 s61,s34,4\nv_lshrrev_b32 v48,1,v118\n" \
"v_and_b32 v49,1,v119\nv_cndmask_b32_e64 v113,v113,v114,vcc\nv_lshl_add_u32 v115,v115,16,v116\nv_mul_f32 v50,0x3b800000,v120\n" \
"s_add_u32 s87,s59,262144\ns_add_u32 s61,s61,65536\nv_cvt_f32_u32 v48,v48\nv_cvt_f32_u32 v49,v49\n" \
"v_bfe_u32 v100,v0,1,2\nv_and_b32 v101,1,v0\nv_add_u32 v115,s87,v115\nv_mov_b32 v116,s61\n" \
"v_cmp_gt_u32 vcc,24,v0\nv_mul_f32 v48,0xbe000000,v48\nv_fma_f32 v49,v49,2.0,v50\nv_lshl_add_u32 v100,v101,2,v100\n" \
"v_cndmask_b32 v115,v116,v115,vcc\nv_mul_f32 v49,v48,v49\nv_mul_f32 v101,0x3a000000,v120\nv_cvt_f32_u32 v100,v100\n" \
"v_bfrev_b32 v116,1\nv_cmp_gt_u32 vcc,25,v0\nv_fmamk_f32 v63,v48,0x40800000,v49\nv_fmamk_f32 v100,v100,0x3e000000,v101\n" \
"v_cndmask_b32 v115,v116,v115,vcc\nv_mov_b32 v16,-1\nv_mov_b32 v17,-1\nv_mov_b32 v18,-1\n" \
"v_mov_b32 v19,-1\nv_lshrrev_b32 v50,1,v119\nv_and_b32 v51,1,v0\nv_cos_f32 v62,v63\n" \
"v_cvt_f32_u32 v101,v119\nbuffer_store_dwordx4 v[16:19],v2,s[36:39],s35 offen sc1\nv_cmp_eq_u32 vcc,v50,v51\nv_add_f32 v101,v101,v101\n" \
"v_mov_b32 v50,0x3e800000\nv_mul_f32 v101,v101,v100\nv_cndmask_b32 v52,0,v50,vcc\nv_cos_f32 v56,v49\n" \
"v_sin_f32 v57,v49\nv_sin_f32 v63,v63\nv_cos_f32 v106,v101\nv_sin_f32 v107,v101\n" \
"v_cmp_gt_u32 vcc,8,v118\ns_add_u32 s55,s35,4096\nv_lshlrev_b32 v51,31,v0\nv_cndmask_b32 v102,0,0.5,vcc\n" \
"buffer_store_dwordx4 v[16:19],v2,s[36:39],s55 offen sc1\nv_xor_b32 v54,v51,v52\nv_sub_f32_e64 v104,0.5,v102\nv_pk_mul_f32 v[58:59],v[56:57],v[52:53] op_sel_hi:[1,0]\n" \
"v_pk_mul_f32 v[60:61],v[56:57],v[54:55] op_sel_hi:[1,0]\nv_pk_mul_f32 v[64:65],v[62:63],v[52:53] op_sel_hi:[1,0]\nv_pk_mul_f32 v[66:67],v[62:63],v[54:55] op_sel_hi:[1,0]\nv_pk_mul_f32 v[108:109],v[106:107],v[102:103] op_sel_hi:[1,0]\n" \
"v_pk_mul_f32 v[110:111],v[106:107],v[104:105] op_sel_hi:[1,0]\nv_fmamk_f32 v57,v48,0x3f800000,v49\nv_fmamk_f32 v63,v48,0x40a00000,v49\nv_fmamk_f32 v107,v100,0x3f800000,v101\n" \
"v_cos_f32 v56,v57\nv_cos_f32 v62,v63\nv_cos_f32 v106,v107\nbuffer_store_dwordx4 v[16:19],v113,s[36:39],0 offen sc1\n" \
"v_sin_f32 v57,v57\nv_sin_f32 v63,v63\nv_sin_f32 v107,v107\nv_mov_b32 v20,0\n" \
"v_mov_b32 v21,0\nv_mov_b32 v22,0\nv_mov_b32 v23,0\nbuffer_store_dwordx4 v[20:23],v115,s[36:39],0 offen sc1\n" \
"v_cvt_pk_f16_f32 v40,v58,-v61\nv_cvt_pk_f16_f32 v44,v59,v60\nv_cvt_pk_f16_f32 v41,v64,-v67\nv_cvt_pk_f16_f32 v45,v65,v66\n" \
"v_cvt_pk_f16_f32 v216,v108,-v109\nv_cvt_pk_f16_f32 v220,v109,v108\nv_cvt_pk_f16_f32 v217,v110,-v111\nv_cvt_pk_f16_f32 v221,-v111,-v110\n" \
"v_pk_mul_f32 v[58:59],v[56:57],v[52:53] op_sel_hi:[1,0]\nv_pk_mul_f32 v[60:61],v[56:57],v[54:55] op_sel_hi:[1,0]\nv_pk_mul_f32 v[64:65],v[62:63],v[52:53] op_sel_hi:[1,0]\nv_pk_mul_f32 v[66:67],v[62:63],v[54:55] op_sel_hi:[1,0]\n" \
"v_pk_mul_f32 v[108:109],v[106:107],v[102:103] op_sel_hi:[1,0]\nv_pk_mul_f32 v[110:111],v[106:107],v[104:105] op_sel_hi:[1,0]\nv_lshlrev_b32 v3,4,v119\nv_lshrrev_b32 v15,1,v1\n" \
"v_lshl_add_u32 v3,v118,6,v3\nbuffer_load_dword v233,s[8:11],s100 offen lds\nv_cvt_pk_f16_f32 v42,v58,-v61\nv_cvt_pk_f16_f32 v46,v59,v60\n" \
"v_cvt_pk_f16_f32 v43,v64,-v67\nv_cvt_pk_f16_f32 v47,v65,v66\nv_cvt_pk_f16_f32 v218,v108,-v109\nv_cvt_pk_f16_f32 v222,v109,v108\n" \
"v_cvt_pk_f16_f32 v219,v110,-v111\nv_cvt_pk_f16_f32 v223,-v111,-v110\nv_add_u32 v15,s57,v15\nv_add_u32 v8,s57,v8\n" \
"v_add_u32 v9,s57,v9\nv_add_u32 v10,s57,v10\nv_add_u32 v11,s57,v11\nv_lshrrev_b32 v77,5,v0\n" \
"v_bfe_u32 v113,v0,1,5\nv_and_b32 v115,1,v0\ns_lshl_b32 s63,s3,1\nv_mul_u32_u24 v77,1160,v77\n" \
"v_and_b32 v76,31,v0\nv_and_b32 v114,6,v113\nv_add_u32 v115,s63,v115\nv_xor_b32 v114,v114,v115\n" \
"v_lshlrev_b32 v113,7,v113\nv_lshl_add_u32 v113,v114,4,v113\ns_add_u32 s64,s54,2916352\ns_add_u32 s65,s64,4096\n" \
"v_bfrev_b32 v114,1\ns_lshl1_add_u32 s95,s33,s3\ns_add_u32 s95,s95,74\ns_cmp_lt_u32 s3,2\n" \
"s_cselect_b32 s95,s95,256\ns_and_b32 s100,s95,31\ns_lshl_b32 s100,s100,3\ns_cmp_lt_u32 s95,256\n" \
"s_cselect_b32 s95,s95,0x40000\ns_lshr_b32 s95,s95,5\ns_add_u32 s95,s95,s100\ns_lshl_b32 s95,s95,6\n" \
"s_add_u32 s95,s95,s32\ns_mul_i32 s95,s95,1152\ns_mov_b32 m0,162176\ns_lshl_b32 s101,s33,7\n" \
"s_sub_u32 s101,s101,1280\nv_add_u32 v79,s101,v0\nv_mul_u32_u24 v78,52429,v79\nv_lshrrev_b32 v78,18,v78\n" \
"v_lshlrev_b32 v79,7,v79\ns_mov_b32 s101,6272\nv_mad_u32_u24 v79,v78,s101,v79\ns_mov_b32 s26,3981312\n" \
"s_mov_b32 s27,0x20000\nv_mad_u32_u24 v78,v76,36,v77\nv_cmp_gt_u32 vcc,72,v0\ns_lshl_b32 s67,s32,2\n" \
"s_add_u32 s66,s64,8192\nv_cndmask_b32 v114,v114,v2,vcc\ns_add_u32 s67,s67,s3\ns_lshl_b32 s67,s67,5\n" \
"s_add_u32 s67,s67,s33\ns_lshl_b32 s67,s67,11\ns_add_u32 s67,s67,5308416\ns_lshl2_add_u32 s69,s3,161664\n" \
"s_waitcnt vmcnt(20)\nv_readfirstlane_b32 s28,v126\nv_readfirstlane_b32 s29,v127\ns_mul_i32 s72,s3,2320\n" \
"v_mov_b32 v251,s69\nv_cmp_gt_u32 vcc,32,v0\ns_add_u32 s28,s28,1\ns_addc_u32 s29,s29,0\n" \
"s_lshl_b32 s7,s32,11\nv_cndmask_b32 v74,2.0,0,vcc\ns_mov_b32 s93,128\ns_mov_b32 s73,8192\n" \
"s_mov_b32 s74,12288\ns_mov_b32 s75,17408\ns_mov_b32 s76,21504\ns_mov_b32 s77,16384\n" \
"s_mov_b32 s78,25600\ns_add_u32 s7,s7,40960\ns_mov_b32 s81,0x3d800000\ns_mov_b32 s42,18874368\n" \
"s_mov_b32 s43,0x20000\ns_waitcnt lgkmcnt(0)\ns_mov_b64 s[40:41],s[44:45]\ns_mov_b64 s[44:45],s[46:47]\n" \
"s_mov_b32 s46,2359296\ns_mov_b32 s47,0x20000\ns_waitcnt vmcnt(19)\nv_lshlrev_b32_e64 v48,16,v224\n" \
"v_and_b32 v49,0xffff0000,v224\nv_readlane_b32 s70,v48,0\nv_readlane_b32 s71,v48,32\nv_and_b32 v51,0xffff0000,v225\n" \
"v_mov_b32 v72,s70\nv_mov_b32 v73,s71\nv_lshlrev_b32 v50,16,v225\nv_and_b32 v53,0xffff0000,v226\n" \
"v_permlane32_swap_b32 v72,v73\nv_pk_add_f32 v[48:49],v[48:49],v[72:73] op_sel_hi:[1,0] neg_lo:[0,1] neg_hi:[0,1]\nv_pk_add_f32 v[50:51],v[50:51],v[72:73] op_sel_hi:[1,0] neg_lo:[0,1] neg_hi:[0,1]\nv_pk_mul_f32 v[68:69],v[48:49],v[48:49]\n" \
"v_lshlrev_b32_e64 v52,16,v226\nv_and_b32 v55,0xffff0000,v227\nv_pk_add_f32 v[52:53],v[52:53],v[72:73] op_sel_hi:[1,0] neg_lo:[0,1] neg_hi:[0,1]\nv_pk_add_f32 v[66:67],v[48:49],v[50:51]\n" \
"v_pk_fma_f32 v[68:69],v[50:51],v[50:51],v[68:69]\nv_lshlrev_b32 v54,16,v227\ns_waitcnt vmcnt(18)\nv_and_b32 v57,0xffff0000,v228\n" \
"v_pk_add_f32 v[54:55],v[54:55],v[72:73] op_sel_hi:[1,0] neg_lo:[0,1] neg_hi:[0,1]\nv_pk_add_f32 v[66:67],v[66:67],v[52:53]\nv_pk_fma_f32 v[68:69],v[52:53],v[52:53],v[68:69]\nv_lshlrev_b32_e64 v56,16,v228\n" \
"v_and_b32 v59,0xffff0000,v229\nv_pk_add_f32 v[56:57],v[56:57],v[72:73] op_sel_hi:[1,0] neg_lo:[0,1] neg_hi:[0,1]\nv_pk_add_f32 v[66:67],v[66:67],v[54:55]\nv_pk_fma_f32 v[68:69],v[54:55],v[54:55],v[68:69]\n" \
"v_lshlrev_b32_e64 v58,16,v229\nv_and_b32 v61,0xffff0000,v230\nv_pk_add_f32 v[58:59],v[58:59],v[72:73] op_sel_hi:[1,0] neg_lo:[0,1] neg_hi:[0,1]\nv_pk_add_f32 v[66:67],v[66:67],v[56:57]\n" \
"v_pk_fma_f32 v[68:69],v[56:57],v[56:57],v[68:69]\nv_lshlrev_b32_e64 v60,16,v230\nv_and_b32 v63,0xffff0000,v231\nv_pk_add_f32 v[60:61],v[60:61],v[72:73] op_sel_hi:[1,0] neg_lo:[0,1] neg_hi:[0,1]\n" \
"v_pk_add_f32 v[66:67],v[66:67],v[58:59]\nv_pk_fma_f32 v[68:69],v[58:59],v[58:59],v[68:69]\nv_lshlrev_b32 v62,16,v231\ns_waitcnt vmcnt(17)\n" \
"v_and_b32 v65,0xffff0000,v232\nv_pk_add_f32 v[62:63],v[62:63],v[72:73] op_sel_hi:[1,0] neg_lo:[0,1] neg_hi:[0,1]\nv_pk_add_f32 v[66:67],v[66:67],v[60:61]\nv_pk_fma_f32 v[68:69],v[60:61],v[60:61],v[68:69]\n" \
"v_lshlrev_b32_e64 v64,16,v232\nv_pk_add_f32 v[64:65],v[64:65],v[72:73] op_sel_hi:[1,0] neg_lo:[0,1] neg_hi:[0,1]\nv_pk_add_f32 v[66:67],v[66:67],v[62:63]\nv_pk_fma_f32 v[68:69],v[62:63],v[62:63],v[68:69]\n" \
"v_pk_add_f32 v[66:67],v[66:67],v[64:65]\nv_pk_fma_f32 v[68:69],v[64:65],v[64:65],v[68:69]\nv_add_f32 v66,v66,v67\nv_add_f32 v68,v68,v69\n" \
"s_nop 1\nv_permlane16_swap_b32 v66,v68\nv_add_f32 v66,v66,v68\ns_nop 1\n" \
"v_add_f32_dpp v66,v66,v66 quad_perm:[1,0,3,2] row_mask:0xf bank_mask:0xf bound_ctrl:1\ns_nop 1\nv_add_f32_dpp v66,v66,v66 quad_perm:[2,3,0,1] row_mask:0xf bank_mask:0xf bound_ctrl:1\ns_nop 1\n" \
"v_add_f32_dpp v66,v66,v66 row_half_mirror row_mask:0xf bank_mask:0xf bound_ctrl:1\ns_nop 1\nv_add_f32_dpp v66,v66,v66 row_mirror row_mask:0xf bank_mask:0xf bound_ctrl:1\nv_mul_f32 v70,0x3ae38e39,v66\n" \
"v_mul_f32 v68,0x3ae38e39,v66\ns_nop 1\nv_permlane16_swap_b32 v70,v68\nv_fma_f32 v68,-v70,v70,v68\n" \
"v_max_f32 v68,0,v68\nv_add_f32 v68,0x3727c5ac,v68\nv_rsq_f32 v66,v68\nv_add_f32 v250,v70,v72\n" \
"v_mul_f32 v67,-v70,v66\nv_pk_fma_f32 v[48:49],v[48:49],v[66:67],v[66:67] op_sel:[0,0,1] op_sel_hi:[1,0,1]\nv_pk_fma_f32 v[50:51],v[50:51],v[66:67],v[66:67] op_sel:[0,0,1] op_sel_hi:[1,0,1]\nv_cvt_pk_f16_f32 v80,v48,v49\n" \
"v_cvt_pk_f16_f32 v81,v50,v51\nv_pk_fma_f32 v[52:53],v[52:53],v[66:67],v[66:67] op_sel:[0,0,1] op_sel_hi:[1,0,1]\nv_pk_fma_f32 v[54:55],v[54:55],v[66:67],v[66:67] op_sel:[0,0,1] op_sel_hi:[1,0,1]\nds_write2_b32 v78,v80,v81 offset0:0 offset1:1\n" \
"v_cvt_pk_f16_f32 v82,v52,v53\nv_cvt_pk_f16_f32 v83,v54,v55\nv_pk_fma_f32 v[56:57],v[56:57],v[66:67],v[66:67] op_sel:[0,0,1] op_sel_hi:[1,0,1]\nv_pk_fma_f32 v[58:59],v[58:59],v[66:67],v[66:67] op_sel:[0,0,1] op_sel_hi:[1,0,1]\n" \
"ds_write2_b32 v78,v82,v83 offset0:2 offset1:3\nv_cvt_pk_f16_f32 v84,v56,v57\nv_cvt_pk_f16_f32 v85,v58,v59\nv_pk_fma_f32 v[60:61],v[60:61],v[66:67],v[66:67] op_sel:[0,0,1] op_sel_hi:[1,0,1]\n" \
"v_pk_fma_f32 v[62:63],v[62:63],v[66:67],v[66:67] op_sel:[0,0,1] op_sel_hi:[1,0,1]\nds_write2_b32 v78,v84,v85 offset0:4 offset1:5\nv_cvt_pk_f16_f32 v86,v60,v61\nv_cvt_pk_f16_f32 v87,v62,v63\n" \
"v_pk_fma_f32 v[64:65],v[64:65],v[66:67],v[66:67] op_sel:[0,0,1] op_sel_hi:[1,0,1]\nds_write2_b32 v78,v86,v87 offset0:6 offset1:7\nv_cvt_pk_f16_f32 v88,v64,v65\nds_write_b32 v78,v88 offset:32\n" \
"s_waitcnt vmcnt(11)\ns_barrier\nds_read_u16 v16,v246\nds_read_u16 v17,v246 offset:5216\n" \
"ds_read_u16 v18,v246 offset:578\nds_read_u16 v19,v246 offset:5794\nds_read_u16 v20,v246 offset:1156\nds_read_u16 v21,v246 offset:6372\n" \
"ds_read_u16 v22,v246 offset:1734\nds_read_u16 v23,v246 offset:6950\ns_waitcnt vmcnt(8)\nv_lshrrev_b32 v96,1,v234\n" \
"ds_read_u16 v24,v247\nv_lshrrev_b32 v97,1,v235\nv_lshrrev_b32 v98,1,v236\ns_waitcnt lgkmcnt(7)\n" \
"v_lshl_or_b32 v16,v17,16,v16\nv_lshrrev_b32 v99,1,v237\nds_read_u16 v25,v247 offset:5216\ns_waitcnt vmcnt(7)\n" \
"v_lshrrev_b32 v100,1,v238\nv_lshrrev_b32 v101,1,v239\nv_lshrrev_b32 v102,1,v240\nv_lshrrev_b32 v103,1,v241\n" \
"ds_read_u16 v26,v247 offset:578\nv_add_u32 v96,s72,v96\nv_add_u32 v97,s72,v97\ns_waitcnt lgkmcnt(7)\n" \
"v_lshl_or_b32 v17,v19,16,v18\nv_add_u32 v98,s72,v98\nds_read_u16 v27,v247 offset:5794\nv_add_u32 v99,s72,v99\n" \
"v_add_u32 v100,s72,v100\nv_add_u32 v101,s72,v101\nv_add_u32 v102,s72,v102\nds_read_u16 v28,v247 offset:1156\n" \
"v_add_u32 v103,s72,v103\nv_pk_maximum3_f16 v124,v16,v17,v16\ns_waitcnt lgkmcnt(7)\nv_lshl_or_b32 v18,v21,16,v20\n" \
"v_pk_minimum3_f16 v125,v16,v17,v16\nds_read_u16 v29,v247 offset:6372\nds_read_u16 v30,v247 offset:1734\ns_waitcnt lgkmcnt(7)\n" \
"v_lshl_or_b32 v19,v23,16,v22\nbuffer_store_dwordx4 v[16:19],v2,s[36:39],s64 offen sc1\nds_read_u16 v31,v247 offset:6950\nds_read_u16 v104,v248\n" \
"v_pk_maximum3_f16 v124,v18,v19,v124\nv_pk_minimum3_f16 v125,v18,v19,v125\ns_waitcnt lgkmcnt(7)\nv_lshl_or_b32 v24,v25,16,v24\n" \
"ds_read_u16 v105,v248 offset:5216\nds_read_u16 v106,v248 offset:578\ns_waitcnt lgkmcnt(7)\nv_lshl_or_b32 v25,v27,16,v26\n" \
"v_pk_maximum3_f16 v124,v24,v25,v124\nds_read_u16 v107,v248 offset:5794\nv_pk_minimum3_f16 v125,v24,v25,v125\nds_read_u16 v108,v248 offset:1156\n" \
"s_waitcnt lgkmcnt(7)\nv_lshl_or_b32 v26,v29,16,v28\nds_read_u16 v109,v248 offset:6372\nds_read_u16 v110,v248 offset:1734\n" \
"s_waitcnt lgkmcnt(7)\nv_lshl_or_b32 v27,v31,16,v30\nbuffer_store_dwordx4 v[24:27],v2,s[36:39],s65 offen sc1\nds_read_u16 v111,v248 offset:6950\n" \
"ds_read_b128 v[116:119],v113 offset:20224\nv_pk_maximum3_f16 v124,v26,v27,v124\nv_pk_minimum3_f16 v125,v26,v27,v125\nds_read_b128 v[120:123],v113 offset:24320\n" \
"s_waitcnt lgkmcnt(8)\nv_lshl_or_b32 v104,v105,16,v104\nds_read_b32 v16,v96\nds_read_b32 v24,v96 offset:1160\n" \
"ds_read_b32 v17,v97\nds_read_b32 v25,v97 offset:1160\ns_waitcnt lgkmcnt(10)\nv_lshl_or_b32 v105,v107,16,v106\n" \
"ds_read_b32 v18,v98\nv_pk_maximum3_f16 v124,v104,v105,v124\nds_read_b32 v26,v98 offset:1160\nv_pk_minimum3_f16 v125,v104,v105,v125\n" \
"ds_read_b32 v19,v99\nds_read_b32 v27,v99 offset:1160\nds_read_b32 v20,v100\ns_waitcnt lgkmcnt(13)\n" \
"v_lshl_or_b32 v106,v109,16,v108\nds_read_b32 v28,v100 offset:1160\nds_read_b32 v21,v101\nds_read_b32 v29,v101 offset:1160\n" \
"s_waitcnt lgkmcnt(14)\nv_lshl_or_b32 v107,v111,16,v110\nbuffer_store_dwordx4 v[104:107],v114,s[36:39],s66 offen sc1\ns_waitcnt lgkmcnt(12)\n" \
"buffer_store_dwordx4 v[116:119],v1,s[36:39],s67 offen sc1\nbuffer_store_dwordx4 v[120:123],v1,s[36:39],s67 offen offset:1024 sc1\nds_read_b32 v22,v102\nds_read_b32 v30,v102 offset:1160\n" \
"ds_read_b32 v23,v103\nds_read_b32 v31,v103 offset:1160\nv_pk_maximum3_f16 v124,v106,v107,v124\nbuffer_load_dword v233,s[8:11],s95 offen lds\n" \
"v_and_b32 v124,0x7fff7fff,v124\nv_pk_minimum3_f16 v125,v106,v107,v125\nv_and_b32 v125,0x7fff7fff,v125\nv_pk_max_u16 v124,v124,v125\n" \
"v_lshlrev_b32 v125,16,v124\nv_max_u32 v124,v124,v125\ns_mov_b32 m0,162176\ns_nop 0\n" \
"v_max_u32_dpp v124,v124,v124 quad_perm:[1,0,3,2] row_mask:0xf bank_mask:0xf bound_ctrl:1\ns_nop 1\nv_max_u32_dpp v124,v124,v124 quad_perm:[2,3,0,1] row_mask:0xf bank_mask:0xf bound_ctrl:1\ns_nop 1\n" \
"v_max_u32_dpp v124,v124,v124 row_half_mirror row_mask:0xf bank_mask:0xf bound_ctrl:1\ns_nop 1\nv_max_u32_dpp v124,v124,v124 row_mirror row_mask:0xf bank_mask:0xf bound_ctrl:1\ns_nop 0\n" \
"v_readlane_b32 s68,v124,0\nv_readlane_b32 s31,v124,16\nv_readlane_b32 s96,v124,32\nv_readlane_b32 s97,v124,48\n" \
"s_max_u32 s31,s31,s96\ns_max_u32 s68,s68,s97\ns_max_u32 s68,s68,s31\ns_mul_i32 s31,s32,576\n" \
"s_add_u32 s31,s31,2304\ns_andn2_b32 s31,s31,127\ns_cmp_gt_u32 s3,1\ns_cselect_b32 s31,s31,2147483648\n" \
"buffer_load_dword v79,s[24:27],s31 offen lds\nv_mov_b32_e64 v124,s68\nds_write_b32 v251,v124 offset:32\nv_mov_b32 v114,161664\n" \
"buffer_load_dwordx4 v[48:51],v2,s[36:39],s73 offen sc1\nbuffer_load_dwordx4 v[60:63],v2,s[36:39],s75 offen sc1\nbuffer_load_dwordx4 v[52:55],v2,s[36:39],s74 offen sc1\nbuffer_load_dwordx4 v[64:67],v2,s[36:39],s76 offen sc1\n" \
"buffer_load_dwordx4 v[56:59],v1,s[36:39],s77 offen sc1\ns_lshl3_add_u32 s80,s2,4294615040\nbuffer_load_dwordx4 v[68:71],v1,s[36:39],s78 offen sc1\nv_and_b32 v113,15,v0\n" \
"s_mov_b32 s79,0xffffff\nv_bfrev_b32 v115,1\ns_add_u32 s70,s35,s88\ns_add_u32 s72,s70,8192\n" \
"s_cmp_lt_u32 s3,2\ns_cselect_b32 s72,s72,2147483648\nv_add_u32 v112,28416,v1\ns_add_u32 s71,s70,4096\n" \
"s_waitcnt vmcnt(8) lgkmcnt(0)\ns_barrier\nds_read_b128 v[116:119],v114 offset:32\nds_read_b128 v[192:195],v112\n" \
"ds_read_b128 v[196:199],v112 offset:1024\nds_read_b128 v[200:203],v112 offset:2048\nds_read_b128 v[204:207],v112 offset:3072\nds_read_b128 v[208:211],v112 offset:4096\n" \
"ds_read_b128 v[212:215],v112 offset:5120\nds_read_b128 v[32:35],v112 offset:6144\nds_read_b128 v[36:39],v112 offset:7168\ns_waitcnt lgkmcnt(8)\n" \
"v_max3_u32 v116,v116,v117,v118\nv_max_u32 v116,v116,v119\nv_and_b32 v119,0xf8,v0\nv_cmp_ne_u32 vcc,192,v119\n" \
"v_lshlrev_b32 v118,11,v0\nv_add_u32 v118,s80,v118\nv_bfrev_b32 v119,1\nv_cndmask_b32 v118,v118,v119,vcc\n" \
"v_lshlrev_b32 v116,1,v116\nv_mov_b32 v117,s29\nv_cmp_eq_u32 vcc,0,v113\nv_bfi_b32 v117,s79,v117,v116\n" \
"v_mov_b32 v116,s28\nv_cndmask_b32 v115,v115,v3,vcc\nv_cmp_gt_u32 vcc,64,v0\nbuffer_store_dwordx2 v[116:117],v118,s[36:39],0 offen sc1\n" \
"s_nop 0\nv_cndmask_b32 v115,v115,v3,vcc\ns_waitcnt vmcnt(6)\nv_cmp_ne_u64 vcc,s[28:29],v[48:49]\n" \
"s_waitcnt vmcnt(4)\nv_cmp_ne_u64 s[84:85],s[28:29],v[52:53]\ns_waitcnt vmcnt(2)\nv_cmp_ne_u64 s[82:83],s[28:29],v[56:57]\n" \
"s_or_b64 vcc,vcc,s[84:85]\nv_cmp_ne_u64 s[84:85],s[28:29],v[60:61]\ns_or_b64 vcc,vcc,s[82:83]\nv_cmp_ne_u64 s[82:83],s[28:29],v[64:65]\n" \
"s_or_b64 vcc,vcc,s[84:85]\ns_waitcnt vmcnt(1)\nv_cmp_ne_u64 s[84:85],s[28:29],v[68:69]\ns_or_b64 vcc,vcc,s[82:83]\n" \
"s_or_b64 vcc,vcc,s[84:85]\ns_cbranch_vccnz .LsmN_%=\n.LbmN_%=:\nv_pk_add_f32 v[50:51],v[50:51],v[62:63]\n" \
"v_pk_add_f32 v[54:55],v[54:55],v[66:67]\nv_pk_mul_f32 v[62:63],v[50:51],2.0 op_sel_hi:[1,0]\nv_pk_add_f32 v[58:59],v[58:59],v[70:71]\nv_max3_f32 v112,|v62|,|v63|,0\n" \
"v_pk_fma_f32 v[66:67],v[54:55],2.0,v[74:75] op_sel_hi:[1,0,0]\nv_max3_f32 v112,|v66|,|v67|,v112\nv_pk_fma_f32 v[70:71],v[58:59],2.0,2.0 op_sel_hi:[1,0,0]\nv_max3_f32 v112,|v70|,|v71|,v112\n" \
"v_cmp_lt_f32 s[82:83],s81,v112\nv_cmp_ngt_f32 vcc,0x41000000,v112\ns_cmp_lg_u64 s[82:83],0\ns_cselect_b32 s82,2,0\n" \
"v_cvt_pk_f16_f32 v62,v62,v63\ns_cmp_lg_u64 vcc,0\ns_addc_u32 s82,s82,0\nds_write_b32 v252,v62 offset:62848\n" \
"v_cvt_pk_f16_f32 v66,v66,v67\nds_write_b32 v253,v66 offset:62848\nv_cvt_pk_f16_f32 v70,v70,v71\nv_mov_b32 v113,s82\n" \
"ds_write_b32 v254,v70 offset:62848\nds_write_b32 v251,v113\ns_waitcnt lgkmcnt(0)\ns_barrier\n" \
"ds_read_b128 v[124:127],v114\nds_read_b64 v[96:97],v234 offset:62848\nds_read_b64 v[98:99],v235 offset:62848\nds_read_b64 v[100:101],v236 offset:62848\n" \
"ds_read_b64 v[102:103],v237 offset:62848\nds_read_b64 v[104:105],v238 offset:62848\nds_read_b64 v[106:107],v239 offset:62848\nds_read_b64 v[108:109],v240 offset:62848\n" \
"ds_read_b64 v[110:111],v241 offset:62848\ns_waitcnt lgkmcnt(7)\nv_or3_b32 v124,v124,v125,v126\nv_or_b32 v124,v124,v127\n" \
"v_cmp_ne_u32 vcc,2,v124\ns_nop 0\ns_cbranch_vccnz .LsxN_%=\n.LbxN_%=:\n" \
"v_pk_fma_f16 v80,v16,v96,v97\ns_waitcnt lgkmcnt(4)\nv_pk_fma_f16 v81,v17,v98,v99\nv_pk_fma_f16 v82,v18,v100,v101\n" \
"v_pk_fma_f16 v83,v19,v102,v103\nv_pk_fma_f16 v88,v24,v96,v97\ns_waitcnt lgkmcnt(2)\nv_pk_fma_f16 v84,v20,v104,v105\n" \
"v_pk_fma_f16 v85,v21,v106,v107\nv_pk_fma_f16 v89,v25,v98,v99\ns_waitcnt lgkmcnt(0)\nv_pk_fma_f16 v86,v22,v108,v109\n" \
"v_pk_fma_f16 v90,v26,v100,v101\nv_pk_fma_f16 v87,v23,v110,v111\nv_pk_fma_f16 v91,v27,v102,v103\nv_pk_fma_f16 v92,v28,v104,v105\n" \
"v_pk_fma_f16 v93,v29,v106,v107\nv_pk_fma_f16 v94,v30,v108,v109\nv_bfe_u32 v233,v0,3,3\nv_mul_u32_u24 v233,8192,v233\n" \
"v_lshl_add_u32 v233,v1,3,v233\ns_lshl2_add_u32 s96,s33,s3\ns_add_u32 s96,s96,138\ns_cmp_lt_u32 s33,13\n" \
"s_cselect_b32 s96,s96,256\ns_and_b32 s97,s96,31\ns_lshl_b32 s97,s97,3\ns_cmp_lt_u32 s96,256\n" \
"s_cselect_b32 s96,s96,0x40000\ns_lshr_b32 s96,s96,5\ns_add_u32 s96,s96,s97\ns_lshl_b32 s96,s96,6\n" \
"s_add_u32 s96,s96,s32\ns_mul_i32 s96,s96,1152\ns_mov_b32 m0,162176\nv_pk_fma_f16 v95,v31,v110,v111\n" \
"buffer_load_dword v233,s[8:11],s96 offen lds\nv_mfma_f32_16x16x32_f16 v[48:51],v[80:83],v[192:195],0\nv_mfma_f32_16x16x32_f16 v[52:55],v[80:83],v[196:199],0\nv_mfma_f32_16x16x32_f16 v[56:59],v[84:87],v[32:35],0\n" \
"v_mfma_f32_16x16x32_f16 v[60:63],v[84:87],v[36:39],0\nv_mfma_f32_16x16x32_f16 v[64:67],v[88:91],v[192:195],0\nv_mfma_f32_16x16x32_f16 v[68:71],v[88:91],v[196:199],0\nv_mfma_f32_16x16x32_f16 v[72:75],v[92:95],v[32:35],0\n" \
"v_mfma_f32_16x16x32_f16 v[76:79],v[92:95],v[36:39],0\ns_nop 2\nv_pk_add_f32 v[56:57],v[48:49],v[56:57] neg_lo:[0,1] neg_hi:[0,1]\nv_sub_f32 v58,v50,v58\n" \
"v_pk_fma_f32 v[48:49],v[48:49],2.0,v[56:57] op_sel_hi:[1,0,1] neg_lo:[0,0,1] neg_hi:[0,0,1]\nv_fma_f32 v50,v50,2.0,-v58\nv_pk_add_f32 v[60:61],v[52:53],v[60:61] neg_lo:[0,1] neg_hi:[0,1]\nv_sub_f32 v62,v54,v62\n" \
"v_pk_fma_f32 v[52:53],v[52:53],2.0,v[60:61] op_sel_hi:[1,0,1] neg_lo:[0,0,1] neg_hi:[0,0,1]\nv_pk_add_f32 v[72:73],v[64:65],v[72:73] neg_lo:[0,1] neg_hi:[0,1]\nv_pk_add_f32 v[76:77],v[68:69],v[76:77] neg_lo:[0,1] neg_hi:[0,1]\nv_sub_f32 v74,v66,v74\n" \
"v_sub_f32_e64 v78,v70,v78\nv_fma_f32 v54,v54,2.0,-v62\nv_pk_fma_f32 v[64:65],v[64:65],2.0,v[72:73] op_sel_hi:[1,0,1] neg_lo:[0,0,1] neg_hi:[0,0,1]\nv_fma_f32 v66,v66,2.0,-v74\n" \
"v_pk_fma_f32 v[68:69],v[68:69],2.0,v[76:77] op_sel_hi:[1,0,1] neg_lo:[0,0,1] neg_hi:[0,0,1]\nv_fma_f32 v70,v70,2.0,-v78\nv_cvt_pk_f16_f32 v16,v48,v52\nv_cvt_pk_f16_f32 v17,v49,v53\n" \
"v_cvt_pk_f16_f32 v18,v50,v54\nv_cvt_pk_f16_f32 v24,v64,v68\nv_cvt_pk_f16_f32 v25,v65,v69\nv_cvt_pk_f16_f32 v26,v66,v70\n" \
"v_mfma_f32_16x16x32_f16 v[48:51],v[200:203],v[16:19],0\nv_cvt_pk_f16_f32 v20,v56,v60\nv_cvt_pk_f16_f32 v21,v57,v61\nv_mfma_f32_16x16x32_f16 v[52:55],v[204:207],v[16:19],0\n" \
"v_cvt_pk_f16_f32 v22,v58,v62\nv_cvt_pk_f16_f32 v28,v72,v76\nv_mfma_f32_16x16x32_f16 v[64:67],v[200:203],v[24:27],0\nv_cvt_pk_f16_f32 v29,v73,v77\n" \
"v_cvt_pk_f16_f32 v30,v74,v78\nv_mfma_f32_16x16x32_f16 v[68:71],v[204:207],v[24:27],0\nv_mfma_f32_16x16x32_f16 v[56:59],v[200:203],v[20:23],0\ns_nop 0\n" \
"v_cvt_pk_f16_f32 v80,v48,v52\nv_mfma_f32_16x16x32_f16 v[60:63],v[204:207],v[20:23],0\nv_cvt_pk_f16_f32 v82,v49,v53\nv_cvt_pk_f16_f32 v84,v50,v54\n" \
"v_mfma_f32_16x16x32_f16 v[72:75],v[200:203],v[28:31],0\nv_mfma_f32_16x16x32_f16 v[76:79],v[204:207],v[28:31],0\nv_cvt_pk_f16_f32 v81,v64,v68\nds_write_b64 v255,v[80:81]\n" \
"v_cvt_pk_f16_f32 v83,v65,v69\nds_write_b64 v255,v[82:83] offset:512\nv_cvt_pk_f16_f32 v85,v66,v70\nds_write_b64 v255,v[84:85] offset:1024\n" \
"v_cvt_pk_f16_f32 v86,v56,v60\nv_cvt_pk_f16_f32 v88,v57,v61\nv_cvt_pk_f16_f32 v87,v72,v76\nds_write_b64 v255,v[86:87] offset:256\n" \
"v_cvt_pk_f16_f32 v89,v73,v77\nds_write_b64 v255,v[88:89] offset:768\nv_cvt_pk_f16_f32 v90,v58,v62\nv_cvt_pk_f16_f32 v91,v74,v78\n" \
"ds_write_b64 v255,v[90:91] offset:1280\ns_waitcnt lgkmcnt(0)\ns_barrier\nds_read_b128 v[16:19],v243\n" \
"ds_read_b128 v[20:23],v244\nds_read_b128 v[24:27],v245\ns_waitcnt lgkmcnt(2)\nv_mfma_f32_16x16x32_f16 v[48:51],v[40:43],v[16:19],0\n" \
"v_mfma_f32_16x16x32_f16 v[52:55],v[44:47],v[16:19],0\ns_waitcnt lgkmcnt(0)\nv_mfma_f32_16x16x32_f16 v[56:59],v[40:43],v[20:23],0\nv_mfma_f32_16x16x32_f16 v[60:63],v[44:47],v[20:23],0\n" \
"v_mfma_f32_16x16x32_f16 v[64:67],v[40:43],v[24:27],0\nv_mfma_f32_16x16x32_f16 v[68:71],v[44:47],v[24:27],0\ns_nop 2\nv_cvt_pk_f16_f32 v80,v48,v52\n" \
"v_and_b32_e64 v80,-2,v80\nv_cvt_pk_f16_f32 v81,v49,v53\nv_cvt_pk_f16_f32 v82,v50,v54\nv_cvt_pk_f16_f32 v83,v51,v55\n" \
"buffer_store_dwordx4 v[80:83],v3,s[36:39],s70 offen sc1\nv_cvt_pk_f16_f32 v84,v56,v60\nv_cvt_pk_f16_f32 v85,v57,v61\nv_cvt_pk_f16_f32 v86,v58,v62\n" \
"v_cvt_pk_f16_f32 v87,v59,v63\nv_and_b32_e64 v84,-2,v84\nbuffer_store_dwordx4 v[84:87],v3,s[36:39],s71 offen sc1\nv_cvt_pk_f16_f32 v88,v64,v68\n" \
"v_cvt_pk_f16_f32 v89,v65,v69\nv_cvt_pk_f16_f32 v90,v66,v70\nv_cvt_pk_f16_f32 v91,v67,v71\nv_and_b32 v88,-2,v88\n" \
"buffer_store_dwordx4 v[88:91],v115,s[36:39],s72 offen sc1\ns_load_dwordx4 s[20:23],s[0:1],56\ns_mov_b32 s14,s87\ns_mov_b32 s12,s88\n" \
"s_mov_b32 s17,s71\ns_cmp_lt_u32 s3,2\ns_cselect_b32 s18,s72,s70\ns_mov_b32 s26,4096\n" \
"s_mov_b32 s27,0x20000\nv_and_b32 v112,3,v0\nv_and_b32 v113,12,v0\nv_lshlrev_b32 v112,2,v112\n" \
"s_mov_b32 s13,s89\nv_max_u32 v112,v112,v113\nv_mov_b32 v114,1048576\nv_cmp_gt_u32 vcc,12,v112\n" \
"v_and_b32 v63,0xffff0000,v224\nv_and_b32 v65,0xffff0000,v225\ns_lshl2_add_u32 s95,s33,s3\ns_add_u32 s95,s95,190\n" \
"s_cmp_lt_u32 s33,13\ns_cselect_b32 s95,s95,256\ns_and_b32 s100,s95,31\ns_lshl_b32 s100,s100,3\n" \
"s_cmp_lt_u32 s95,256\ns_cselect_b32 s95,s95,0x40000\ns_lshr_b32 s95,s95,5\ns_add_u32 s95,s95,s100\n" \
"s_lshl_b32 s95,s95,6\ns_add_u32 s95,s95,s32\ns_mul_i32 s95,s95,1152\ns_lshl2_add_u32 s101,s33,s3\n" \
"s_add_u32 s101,s101,206\ns_cmp_lt_u32 s33,13\ns_cselect_b32 s101,s101,256\ns_add_u32 s96,s92,0\n" \
"s_add_u32 s97,s92,9437184\ns_add_u32 s100,s92,9510912\nv_and_b32 v67,0xffff0000,v226\nv_and_b32 v69,0xffff0000,v227\n" \
"v_and_b32 v71,0xffff0000,v228\nv_and_b32 v73,0xffff0000,v229\nv_and_b32 v75,0xffff0000,v230\nv_and_b32 v77,0xffff0000,v231\n" \
"v_and_b32 v79,0xffff0000,v232\nv_lshlrev_b32 v126,3,v1\nv_lshlrev_b32 v62,16,v224\nv_lshlrev_b32 v64,16,v225\n" \
"v_lshlrev_b32 v66,16,v226\nv_lshlrev_b32 v68,16,v227\nv_lshlrev_b32 v70,16,v228\nv_lshlrev_b32 v72,16,v229\n" \
"v_lshlrev_b32 v74,16,v230\nv_lshlrev_b32 v76,16,v231\nv_lshlrev_b32 v78,16,v232\ns_mov_b32 s16,s70\n" \
"v_min_u32 v126,0x480,v126\nv_pk_add_f32 v[62:63],v[62:63],v[250:251] op_sel_hi:[1,0] neg_lo:[0,1] neg_hi:[0,1]\nv_pk_add_f32 v[64:65],v[64:65],v[250:251] op_sel_hi:[1,0] neg_lo:[0,1] neg_hi:[0,1]\nv_pk_add_f32 v[66:67],v[66:67],v[250:251] op_sel_hi:[1,0] neg_lo:[0,1] neg_hi:[0,1]\n" \
"v_pk_add_f32 v[68:69],v[68:69],v[250:251] op_sel_hi:[1,0] neg_lo:[0,1] neg_hi:[0,1]\nv_pk_add_f32 v[70:71],v[70:71],v[250:251] op_sel_hi:[1,0] neg_lo:[0,1] neg_hi:[0,1]\nv_pk_add_f32 v[72:73],v[72:73],v[250:251] op_sel_hi:[1,0] neg_lo:[0,1] neg_hi:[0,1]\nv_pk_add_f32 v[74:75],v[74:75],v[250:251] op_sel_hi:[1,0] neg_lo:[0,1] neg_hi:[0,1]\n" \
"v_pk_add_f32 v[76:77],v[76:77],v[250:251] op_sel_hi:[1,0] neg_lo:[0,1] neg_hi:[0,1]\nv_pk_add_f32 v[78:79],v[78:79],v[250:251] op_sel_hi:[1,0] neg_lo:[0,1] neg_hi:[0,1]\ns_mul_i32 s31,s32,1152\nv_lshlrev_b32 v127,3,v1\n" \
"s_waitcnt lgkmcnt(0)\ns_mov_b64 s[24:25],s[22:23]\ns_mov_b32 s22,9216\ns_mov_b32 s23,0x20000\n" \
"v_mov_b64 v[16:17],v[192:193]\nv_mov_b64 v[18:19],v[194:195]\nv_mov_b64 v[20:21],v[196:197]\nv_mov_b64 v[22:23],v[198:199]\n" \
"v_mov_b64 v[24:25],v[208:209]\nv_mov_b64 v[26:27],v[210:211]\nv_mov_b64 v[28:29],v[212:213]\nv_mov_b64 v[30:31],v[214:215]\n" \
"v_mov_b64 v[32:33],v[216:217]\nv_mov_b64 v[34:35],v[218:219]\nv_mov_b64 v[36:37],v[220:221]\nv_mov_b64 v[38:39],v[222:223]\n" \
"v_mov_b64 v[40:41],v[200:201]\nv_mov_b64 v[42:43],v[202:203]\nv_mov_b64 v[44:45],v[204:205]\nv_mov_b64 v[46:47],v[206:207]\n" \
"v_mov_b64 v[56:57],v[242:243]\nv_mov_b64 v[58:59],v[244:245]\nv_mov_b32 v60,v249\nv_mov_b32 v61,v250\n" \
"s_mov_b32 s55,0xbdd2d3e7\ns_mul_i32 s56,s3,9568\ns_lshl_b32 s58,s3,13\ns_and_b32 s84,s33,1\n" \
"s_add_u32 s87,s12,65536\ns_mov_b32 s90,7405568\ns_sub_u32 s98,s57,s12\ns_mov_b32 s99,s12\n" \
"s_mov_b32 m0,162176\ns_nop 0\nbuffer_load_dword v126,s[20:23],s31 offen lds\ns_and_b32 s22,s33,7\n" \
"s_lshr_b32 s20,s33,3\ns_lshr_b32 s21,s32,1\ns_mul_i32 s52,s20,4505600\ns_add_u32 s52,s52,12123648\n" \
"s_mul_i32 s48,s20,589824\ns_lshl4_add_u32 s48,s12,s48\ns_lshl_b32 s86,s21,11\ns_add_u32 s48,s48,9764864\n" \
"s_lshl_b32 s67,s21,7\ns_lshl_b32 s85,s21,17\ns_sub_u32 s86,s14,s86\ns_add_u32 s49,s48,4096\n" \
"s_add_u32 s50,s48,8192\ns_add_u32 s51,s48,12288\ns_add_u32 s53,s52,3276800\ns_and_b32 s31,s101,31\n" \
"s_lshl_b32 s31,s31,3\ns_cmp_lt_u32 s101,256\ns_cselect_b32 s101,s101,0x40000\ns_lshr_b32 s101,s101,5\n" \
"s_add_u32 s101,s101,s31\ns_lshl_b32 s101,s101,6\ns_add_u32 s101,s101,s32\ns_mul_i32 s101,s101,1152\n" \
"s_add_u32 s31,s92,9584640\ns_add_u32 s83,s52,2048000\ns_add_u32 s67,s67,163200\ns_add_u32 s85,s85,65536\n" \
"s_add_u32 s86,s86,0x10000\ns_mov_b32 m0,162176\ns_nop 0\nbuffer_load_dword v127,s[24:27],0 offen lds\n" \
"s_and_b32 s24,s32,1\ns_lshl3_add_u32 s22,s24,s22\ns_bfe_u32 s24,s33,0x10002\ns_lshl1_add_u32 s24,s32,s24\n" \
"s_mul_i32 s19,s24,18\ns_and_b32 s24,s33,3\ns_lshl2_add_u32 s23,s24,s19\ns_lshl_b32 s24,s3,5\n" \
"s_add_u32 s54,s14,s24\ns_lshl2_add_u32 s24,s20,s21\ns_add_u32 s23,s23,s3\ns_lshl2_add_u32 s24,s24,s3\n" \
"s_add_u32 s23,s23,1\ns_lshl_b32 s24,s24,9\ns_lshl_b32 s23,s23,9\ns_add_u32 s24,s24,262144\n" \
"v_bfe_u32 v127,v0,3,3\nv_mul_u32_u24 v127,8192,v127\nv_lshl_add_u32 v127,v1,3,v127\ns_mov_b32 m0,162176\n" \
"s_sub_u32 s59,s24,s23\nbuffer_load_dword v127,s[8:11],s95 offen lds\ns_lshr_b32 s25,s3,1\ns_mul_i32 s24,s21,1152\n" \
"s_mul_i32 s25,s25,576\ns_add_u32 s24,s24,s25\ns_lshl4_add_u32 s24,s24,s22\ns_and_b32 s25,s3,1\n" \
"s_lshl_b32 s24,s24,8\ns_lshl_b32 s65,s25,17\ns_add_u32 s65,s65,s24\ns_lshl_b32 s25,s25,20\n" \
"s_add_u32 s24,s24,s25\ns_add_u32 s60,s24,655488\ns_add_u32 s61,s24,786432\ns_add_u32 s62,s24,786560\n" \
"s_add_u32 s63,s24,917504\ns_add_u32 s64,s24,917632\ns_mov_b32 m0,162176\ns_add_u32 s95,s92,73728\n" \
"buffer_load_dword v127,s[8:11],s101 offen lds\ns_lshl2_add_u32 s24,s21,s3\ns_mul_i32 s24,s24,9216\ns_add_u32 s24,s24,s52\n" \
"s_add_u32 s68,s24,819712\ns_lshl_b32 s24,s22,14\ns_add_u32 s24,s24,s52\ns_lshl2_add_u32 s25,s21,s3\n" \
"s_add_u32 s24,s24,967168\ns_lshl_b32 s25,s25,8\ns_add_u32 s79,s25,s24\ns_add_u32 s25,s21,4\n" \
"s_and_b32 s25,s25,3\ns_add_u32 s24,s24,s12\ns_lshl_b32 s25,s25,12\ns_add_u32 s25,s25,s24\n" \
"s_sub_u32 s74,s25,819200\ns_add_u32 m0,s57,0\ns_add_u32 s101,s92,147456\nbuffer_load_dwordx4 v4,s[8:11],s96 offen lds\n" \
"s_add_u32 s25,s21,3\ns_and_b32 s25,s25,3\ns_lshl_b32 s25,s25,12\ns_add_u32 s25,s25,s24\n" \
"s_sub_u32 s71,s25,2048000\ns_sub_u32 s76,s25,1638400\ns_add_u32 s25,s21,2\ns_and_b32 s25,s25,3\n" \
"s_lshl_b32 s25,s25,12\nbuffer_load_dwordx4 v5,s[8:11],s96 offen offset:1024 lds\ns_add_u32 s25,s25,s24\ns_sub_u32 s72,s25,1638400\n" \
"s_sub_u32 s77,s25,1638400\ns_add_u32 s25,s21,1\ns_and_b32 s25,s25,3\ns_lshl_b32 s25,s25,12\n" \
"s_add_u32 s25,s25,s24\ns_sub_u32 s73,s25,1638400\ns_sub_u32 s78,s25,1228800\nbuffer_load_dwordx4 v6,s[8:11],s96 offen offset:2048 lds\n" \
"s_lshl2_add_u32 s26,s33,s3\ns_and_b32 s24,s26,15\ns_and_b32 s25,s26,8\ns_lshl2_add_u32 s24,s24,192\n" \
"s_mul_i32 s25,s25,28\ns_add_u32 s25,s25,s24\ns_sub_u32 s25,s25,32\ns_bitcmp1_b32 s26,4\n" \
"s_cselect_b32 s24,s25,s24\nbuffer_load_dwordx4 v7,s[8:11],s96 offen offset:3072 lds\ns_and_b32 s25,s26,31\ns_lshl2_add_u32 s25,s25,448\n" \
"s_bitcmp1_b32 s26,5\ns_cselect_b32 s24,s25,s24\ns_lshr_b32 s25,s26,6\ns_lshl1_add_u32 s25,s21,s25\n" \
"s_mul_i32 s25,s25,576\ns_add_u32 s24,s24,s25\ns_and_b32 s25,s32,1\ns_add_u32 m0,s57,4096\n" \
"s_add_u32 s96,s92,221184\nbuffer_load_dwordx4 v4,s[8:11],s97 offen lds\ns_lshl1_add_u32 s24,s24,s25\ns_lshl_b32 s24,s24,11\n" \
"s_bfe_u32 s25,s33,131072\ns_mul_i32 s27,s3,4896\ns_add_u32 s24,s24,s25\ns_add_u32 s27,s27,9216\n" \
"s_add_u32 s89,s24,1\nv_mov_b32 v113,s27\ns_lshl2_add_u32 s24,s2,s3\nbuffer_load_dwordx4 v5,s[8:11],s97 offen offset:1024 lds\n" \
"v_cndmask_b32 v112,v114,v113,vcc\ns_add_u32 s65,s65,2097152\ns_lshl_b32 s24,s24,8\nv_add_u32 v48,v234,v112\n" \
"v_add_u32 v49,v235,v112\nv_add_u32 v50,v236,v112\nv_add_u32 v51,v237,v112\nv_add_u32 v52,v238,v112\n" \
"buffer_load_dwordx4 v6,s[8:11],s97 offen offset:2048 lds\nv_add_u32 v53,v239,v112\nv_add_u32 v54,v240,v112\nv_add_u32_e64 v55,v241,v112\n" \
"s_add_u32 s75,s59,196608\ns_add_u32 s66,s65,128\ns_add_u32 s69,s68,3072\ns_add_u32 s70,s68,6144\n" \
"s_add_u32 s80,s79,4096\ns_add_u32 s81,s79,8192\nbuffer_load_dwordx4 v7,s[8:11],s97 offen offset:3072 lds\ns_add_u32 s82,s79,12288\n" \
"s_add_u32 s88,s74,819200\ns_add_u32 s91,s24,69206016\ns_add_u32 m0,s57,8192\ns_nop 0\n" \
"buffer_load_dwordx4 v4,s[8:11],s95 offen lds\nbuffer_load_dwordx4 v5,s[8:11],s95 offen offset:1024 lds\nbuffer_load_dwordx4 v6,s[8:11],s95 offen offset:2048 lds\nbuffer_load_dwordx4 v7,s[8:11],s95 offen offset:3072 lds\n" \
"s_add_u32 m0,s57,12288\ns_nop 0\nbuffer_load_dwordx4 v4,s[8:11],s100 offen lds\nbuffer_load_dwordx4 v5,s[8:11],s100 offen offset:1024 lds\n" \
"buffer_load_dwordx4 v6,s[8:11],s100 offen offset:2048 lds\nbuffer_load_dwordx4 v7,s[8:11],s100 offen offset:3072 lds\ns_add_u32 m0,s57,16384\ns_nop 0\n" \
"buffer_load_dwordx4 v4,s[8:11],s101 offen lds\nbuffer_load_dwordx4 v5,s[8:11],s101 offen offset:1024 lds\nbuffer_load_dwordx4 v6,s[8:11],s101 offen offset:2048 lds\nbuffer_load_dwordx4 v7,s[8:11],s101 offen offset:3072 lds\n" \
"s_add_u32 m0,s57,20480\ns_nop 0\nbuffer_load_dwordx4 v4,s[8:11],s31 offen lds\nbuffer_load_dwordx4 v5,s[8:11],s31 offen offset:1024 lds\n" \
"buffer_load_dwordx4 v6,s[8:11],s31 offen offset:2048 lds\nbuffer_load_dwordx4 v7,s[8:11],s31 offen offset:3072 lds\ns_add_u32 m0,s57,24576\ns_nop 0\n" \
"buffer_load_dwordx4 v4,s[8:11],s96 offen lds\nbuffer_load_dwordx4 v5,s[8:11],s96 offen offset:1024 lds\nbuffer_load_dwordx4 v6,s[8:11],s96 offen offset:2048 lds\nbuffer_load_dwordx4 v7,s[8:11],s96 offen offset:3072 lds\n" \
"buffer_load_dwordx4 v[104:107],v1,s[36:39],s7 offen sc1\nbuffer_load_dwordx4 v[108:111],v1,s[36:39],s7 offen offset:1024 sc1\ns_waitcnt vmcnt(1)\nv_xor_b32 v112,s29,v105\n" \
"v_xor_b32 v113,s29,v107\ns_waitcnt vmcnt(0)\nv_xor_b32 v114,s29,v109\nv_xor_b32 v115,s29,v111\n" \
"v_or3_b32 v112,v112,v113,v114\nv_xor_b32 v113,s28,v104\nv_xor_b32 v114,s28,v106\nv_or_b32 v112,v112,v115\n" \
"v_xor_b32 v115,s28,v108\nv_lshl_or_b32 v112,v112,8,v113\nv_xor_b32 v113,s28,v110\nv_or3_b32 v112,v112,v114,v115\n" \
"v_or_b32 v112,v112,v113\nv_cmp_ne_u32 vcc,0,v112\ns_nop 0\ns_cbranch_vccnz .LsrN_%=\n" \
".LbrN_%=:\nv_min_u32 v120,2288,v2\ns_mov_b32 s5,26624\nv_max3_u32 v112,v105,v107,v109\n" \
"v_max_u32_e64 v112,v112,v111\ns_mov_b32 s6,31232\nds_read_b64_tr_b16 a[0:1],v8\nv_max_u32_dpp v112,v112,v112 quad_perm:[1,0,3,2] row_mask:0xf bank_mask:0xf bound_ctrl:1\n" \
"ds_read_b64_tr_b16 a[4:5],v9\nds_read_b64_tr_b16 a[8:9],v10\nv_max_u32_dpp v112,v112,v112 quad_perm:[2,3,0,1] row_mask:0xf bank_mask:0xf bound_ctrl:1\nds_read_b64_tr_b16 a[12:13],v11\n" \
"ds_read_b64_tr_b16 a[16:17],v8 offset:2048\nv_max_u32_dpp v112,v112,v112 row_half_mirror row_mask:0xf bank_mask:0xf bound_ctrl:1\nds_read_b64_tr_b16 a[20:21],v9 offset:2048\nds_read_b64_tr_b16 a[24:25],v10 offset:2048\n" \
"v_max_u32_dpp v112,v112,v112 row_mirror row_mask:0xf bank_mask:0xf bound_ctrl:1\nds_read_b64_tr_b16 a[28:29],v11 offset:2048\nv_readlane_b32 s94,v112,0\nv_readlane_b32 s97,v112,16\n" \
"v_readlane_b32 s95,v112,32\nv_readlane_b32 s100,v112,48\ns_max_u32 s97,s97,s95\ns_max_u32 s94,s94,s100\n" \
"s_max_u32 s94,s94,s97\ns_lshr_b32 s94,s94,24\ns_max_u32 s94,s94,64\ns_min_u32 s94,s94,250\n" \
"s_add_u32 s101,s92,9658368\ns_add_u32 m0,s57,0\nds_read_b64_tr_b16 a[2:3],v8 offset:4096\ns_waitcnt lgkmcnt(1)\n" \
"buffer_load_dwordx4 v4,s[8:11],s101 offen lds\nbuffer_load_dwordx4 v5,s[8:11],s101 offen offset:1024 lds\nbuffer_load_dwordx4 v6,s[8:11],s101 offen offset:2048 lds\nbuffer_load_dwordx4 v7,s[8:11],s101 offen offset:3072 lds\n" \
"ds_read_b64_tr_b16 a[6:7],v9 offset:4096\nds_read_b64_tr_b16 a[10:11],v10 offset:4096\nds_read_b64_tr_b16 a[14:15],v11 offset:4096\nds_read_b64_tr_b16 a[18:19],v8 offset:6144\n" \
"ds_read_b64_tr_b16 a[22:23],v9 offset:6144\nds_read_b64_tr_b16 a[26:27],v10 offset:6144\nds_read_b64_tr_b16 a[30:31],v11 offset:6144\ns_add_u32 s31,s92,294912\n" \
"s_add_u32 m0,s57,4096\nds_read_b64_tr_b16 a[32:33],v8 offset:8192\ns_waitcnt lgkmcnt(1)\nbuffer_load_dwordx4 v4,s[8:11],s31 offen lds\n" \
"buffer_load_dwordx4 v5,s[8:11],s31 offen offset:1024 lds\nbuffer_load_dwordx4 v6,s[8:11],s31 offen offset:2048 lds\nbuffer_load_dwordx4 v7,s[8:11],s31 offen offset:3072 lds\nds_read_b64_tr_b16 a[36:37],v9 offset:8192\n" \
"ds_read_b64_tr_b16 a[40:41],v10 offset:8192\nds_read_b64_tr_b16 a[44:45],v11 offset:8192\nds_read_b64_tr_b16 a[48:49],v8 offset:10240\nds_read_b64_tr_b16 a[52:53],v9 offset:10240\n" \
"ds_read_b64_tr_b16 a[56:57],v10 offset:10240\nds_read_b64_tr_b16 a[60:61],v11 offset:10240\ns_add_u32 s96,s92,9732096\ns_add_u32 m0,s57,8192\n" \
"ds_read_b64_tr_b16 a[34:35],v8 offset:12288\ns_waitcnt lgkmcnt(1)\nbuffer_load_dwordx4 v4,s[8:11],s96 offen lds\nbuffer_load_dwordx4 v5,s[8:11],s96 offen offset:1024 lds\n" \
"buffer_load_dwordx4 v6,s[8:11],s96 offen offset:2048 lds\nbuffer_load_dwordx4 v7,s[8:11],s96 offen offset:3072 lds\nds_read_b64_tr_b16 a[38:39],v9 offset:12288\nds_read_b64_tr_b16 a[42:43],v10 offset:12288\n" \
"ds_read_b64_tr_b16 a[46:47],v11 offset:12288\nds_read_b64_tr_b16 a[50:51],v8 offset:14336\nds_read_b64_tr_b16 a[54:55],v9 offset:14336\nds_read_b64_tr_b16 a[58:59],v10 offset:14336\n" \
"ds_read_b64_tr_b16 a[62:63],v11 offset:14336\ns_add_u32 s97,s92,368640\ns_add_u32 m0,s57,12288\nds_read_b64_tr_b16 a[64:65],v8 offset:16384\n" \
"s_waitcnt lgkmcnt(1)\nbuffer_load_dwordx4 v4,s[8:11],s97 offen lds\nbuffer_load_dwordx4 v5,s[8:11],s97 offen offset:1024 lds\nbuffer_load_dwordx4 v6,s[8:11],s97 offen offset:2048 lds\n" \
"buffer_load_dwordx4 v7,s[8:11],s97 offen offset:3072 lds\nbuffer_load_dwordx4 v[80:83],v120,s[36:39],s5 offen sc1\nbuffer_load_dwordx4 v[84:87],v120,s[36:39],s5 offen offset:2304 sc1\nbuffer_load_dwordx4 v[88:91],v120,s[36:39],s6 offen sc1\n" \
"buffer_load_dwordx4 v[92:95],v120,s[36:39],s6 offen offset:2304 sc1\nds_read_b64_tr_b16 a[68:69],v9 offset:16384\nds_read_b64_tr_b16 a[72:73],v10 offset:16384\nds_read_b64_tr_b16 a[76:77],v11 offset:16384\n" \
"ds_read_b64_tr_b16 a[80:81],v8 offset:18432\nds_read_b64_tr_b16 a[84:85],v9 offset:18432\nds_read_b64_tr_b16 a[88:89],v10 offset:18432\nds_read_b64_tr_b16 a[92:93],v11 offset:18432\n" \
"s_add_u32 s95,s92,9805824\ns_add_u32 m0,s57,16384\nds_read_b64_tr_b16 a[66:67],v8 offset:20480\ns_waitcnt lgkmcnt(1)\n" \
"buffer_load_dwordx4 v4,s[8:11],s95 offen lds\nbuffer_load_dwordx4 v5,s[8:11],s95 offen offset:1024 lds\nbuffer_load_dwordx4 v6,s[8:11],s95 offen offset:2048 lds\nbuffer_load_dwordx4 v7,s[8:11],s95 offen offset:3072 lds\n" \
"ds_read_b64_tr_b16 a[70:71],v9 offset:20480\nds_read_b64_tr_b16 a[74:75],v10 offset:20480\nds_read_b64_tr_b16 a[78:79],v11 offset:20480\nds_read_b64_tr_b16 a[82:83],v8 offset:22528\n" \
"ds_read_b64_tr_b16 a[86:87],v9 offset:22528\nds_read_b64_tr_b16 a[90:91],v10 offset:22528\nds_read_b64_tr_b16 a[94:95],v11 offset:22528\ns_add_u32 s100,s92,442368\n" \
"s_add_u32 m0,s57,20480\nds_read_b64_tr_b16 a[96:97],v8 offset:24576\ns_waitcnt lgkmcnt(1)\nbuffer_load_dwordx4 v4,s[8:11],s100 offen lds\n" \
"buffer_load_dwordx4 v5,s[8:11],s100 offen offset:1024 lds\nbuffer_load_dwordx4 v6,s[8:11],s100 offen offset:2048 lds\nbuffer_load_dwordx4 v7,s[8:11],s100 offen offset:3072 lds\nds_read_b64_tr_b16 a[100:101],v9 offset:24576\n" \
"ds_read_b64_tr_b16 a[104:105],v10 offset:24576\nds_read_b64_tr_b16 a[108:109],v11 offset:24576\nds_read_b64_tr_b16 a[112:113],v8 offset:26624\nds_read_b64_tr_b16 a[116:117],v9 offset:26624\n" \
"ds_read_b64_tr_b16 a[120:121],v10 offset:26624\nds_read_b64_tr_b16 a[124:125],v11 offset:26624\ns_add_u32 s101,s92,9879552\ns_add_u32 m0,s57,24576\n" \
"s_waitcnt vmcnt(24)\nds_read_b64_tr_b16 a[98:99],v8\ns_waitcnt lgkmcnt(1)\nbuffer_load_dwordx4 v4,s[8:11],s101 offen lds\n" \
"buffer_load_dwordx4 v5,s[8:11],s101 offen offset:1024 lds\nbuffer_load_dwordx4 v6,s[8:11],s101 offen offset:2048 lds\nbuffer_load_dwordx4 v7,s[8:11],s101 offen offset:3072 lds\nds_read_b64_tr_b16 a[102:103],v9\n" \
"ds_read_b64_tr_b16 a[106:107],v10\nds_read_b64_tr_b16 a[110:111],v11\nds_read_b64_tr_b16 a[114:115],v8 offset:2048\nds_read_b64_tr_b16 a[118:119],v9 offset:2048\n" \
"ds_read_b64_tr_b16 a[122:123],v10 offset:2048\nds_read_b64_tr_b16 a[126:127],v11 offset:2048\ns_add_u32 s31,s92,516096\ns_add_u32 m0,s57,0\n" \
"s_waitcnt vmcnt(24)\nds_read_b64_tr_b16 a[128:129],v8 offset:4096\ns_waitcnt lgkmcnt(1)\nbuffer_load_dwordx4 v4,s[8:11],s31 offen lds\n" \
"buffer_load_dwordx4 v5,s[8:11],s31 offen offset:1024 lds\nbuffer_load_dwordx4 v6,s[8:11],s31 offen offset:2048 lds\nbuffer_load_dwordx4 v7,s[8:11],s31 offen offset:3072 lds\nds_read_b64_tr_b16 a[132:133],v9 offset:4096\n" \
"ds_read_b64_tr_b16 a[136:137],v10 offset:4096\nds_read_b64_tr_b16 a[140:141],v11 offset:4096\nds_read_b64_tr_b16 a[144:145],v8 offset:6144\nds_read_b64_tr_b16 a[148:149],v9 offset:6144\n" \
"ds_read_b64_tr_b16 a[152:153],v10 offset:6144\nds_read_b64_tr_b16 a[156:157],v11 offset:6144\ns_add_u32 s96,s92,9953280\ns_add_u32 m0,s57,4096\n" \
"s_waitcnt vmcnt(24)\nds_read_b64_tr_b16 a[130:131],v8 offset:8192\ns_waitcnt lgkmcnt(1)\nbuffer_load_dwordx4 v4,s[8:11],s96 offen lds\n" \
"buffer_load_dwordx4 v5,s[8:11],s96 offen offset:1024 lds\nbuffer_load_dwordx4 v6,s[8:11],s96 offen offset:2048 lds\nbuffer_load_dwordx4 v7,s[8:11],s96 offen offset:3072 lds\nds_read_b64_tr_b16 a[134:135],v9 offset:8192\n" \
"ds_read_b64_tr_b16 a[138:139],v10 offset:8192\nds_read_b64_tr_b16 a[142:143],v11 offset:8192\nds_read_b64_tr_b16 a[146:147],v8 offset:10240\nds_read_b64_tr_b16 a[150:151],v9 offset:10240\n" \
"ds_read_b64_tr_b16 a[154:155],v10 offset:10240\nds_read_b64_tr_b16 a[158:159],v11 offset:10240\ns_bfe_u32 s26,s33,0x20001\ns_lshl2_add_u32 s15,s32,s26\n" \
"s_lshl2_add_u32 s15,s15,s3\ns_lshl_b32 s15,s15,14\ns_add_u32 s15,s15,5308416\ns_add_u32 s97,s15,0\n" \
"s_add_u32 m0,s57,8192\ns_waitcnt vmcnt(24)\nds_read_b64_tr_b16 a[160:161],v8 offset:12288\ns_waitcnt lgkmcnt(1)\n" \
"buffer_load_dwordx4 v1,s[36:39],s97 offen sc1 lds\nbuffer_load_dwordx4 v1,s[36:39],s97 offen offset:1024 sc1 lds\nbuffer_load_dwordx4 v1,s[36:39],s97 offen offset:2048 sc1 lds\nbuffer_load_dwordx4 v1,s[36:39],s97 offen offset:3072 sc1 lds\n" \
"ds_read_b64_tr_b16 a[164:165],v9 offset:12288\nds_read_b64_tr_b16 a[168:169],v10 offset:12288\nds_read_b64_tr_b16 a[172:173],v11 offset:12288\nds_read_b64_tr_b16 a[176:177],v8 offset:14336\n" \
"ds_read_b64_tr_b16 a[180:181],v9 offset:14336\nds_read_b64_tr_b16 a[184:185],v10 offset:14336\nds_read_b64_tr_b16 a[188:189],v11 offset:14336\ns_add_u32 s95,s15,4096\n" \
"s_add_u32 m0,s57,12288\ns_waitcnt vmcnt(20)\nds_read_b64_tr_b16 a[162:163],v8 offset:16384\ns_waitcnt lgkmcnt(1)\n" \
"buffer_load_dwordx4 v1,s[36:39],s95 offen sc1 lds\nbuffer_load_dwordx4 v1,s[36:39],s95 offen offset:1024 sc1 lds\nbuffer_load_dwordx4 v1,s[36:39],s95 offen offset:2048 sc1 lds\nbuffer_load_dwordx4 v1,s[36:39],s95 offen offset:3072 sc1 lds\n" \
"ds_read_b64_tr_b16 a[166:167],v9 offset:16384\nds_read_b64_tr_b16 a[170:171],v10 offset:16384\nds_read_b64_tr_b16 a[174:175],v11 offset:16384\nds_read_b64_tr_b16 a[178:179],v8 offset:18432\n" \
"ds_read_b64_tr_b16 a[182:183],v9 offset:18432\nds_read_b64_tr_b16 a[186:187],v10 offset:18432\nds_read_b64_tr_b16 a[190:191],v11 offset:18432\ns_add_u32 s100,s15,8192\n" \
"s_add_u32 m0,s57,16384\ns_waitcnt vmcnt(20)\nds_read_b64_tr_b16 a[192:193],v8 offset:20480\ns_waitcnt lgkmcnt(1)\n" \
"buffer_load_dwordx4 v1,s[36:39],s100 offen sc1 lds\nbuffer_load_dwordx4 v1,s[36:39],s100 offen offset:1024 sc1 lds\nbuffer_load_dwordx4 v1,s[36:39],s100 offen offset:2048 sc1 lds\nbuffer_load_dwordx4 v1,s[36:39],s100 offen offset:3072 sc1 lds\n" \
"ds_read_b64_tr_b16 a[196:197],v9 offset:20480\nds_read_b64_tr_b16 a[200:201],v10 offset:20480\nds_read_b64_tr_b16 a[204:205],v11 offset:20480\nds_read_b64_tr_b16 a[208:209],v8 offset:22528\n" \
"ds_read_b64_tr_b16 a[212:213],v9 offset:22528\nds_read_b64_tr_b16 a[216:217],v10 offset:22528\nds_read_b64_tr_b16 a[220:221],v11 offset:22528\ns_add_u32 s101,s15,12288\n" \
"s_add_u32 m0,s57,20480\ns_waitcnt lgkmcnt(0)\nbuffer_load_dwordx4 v1,s[36:39],s101 offen sc1 lds\nbuffer_load_dwordx4 v1,s[36:39],s101 offen offset:1024 sc1 lds\n" \
"buffer_load_dwordx4 v1,s[36:39],s101 offen offset:2048 sc1 lds\nbuffer_load_dwordx4 v1,s[36:39],s101 offen offset:3072 sc1 lds\nv_cmp_ne_u64 vcc,s[28:29],v[80:81]\nv_cmp_ne_u64 s[24:25],s[28:29],v[84:85]\n" \
"v_cmp_ne_u64 s[26:27],s[28:29],v[88:89]\ns_or_b64 vcc,vcc,s[24:25]\nv_cmp_ne_u64 s[24:25],s[28:29],v[92:93]\ns_or_b64 vcc,vcc,s[26:27]\n" \
"s_or_b64 vcc,vcc,s[24:25]\ns_cbranch_vccnz .LsnN_%=\n.LbnN_%=:\nv_bfe_u32 v112,v0,1,5\n" \
"v_mul_u32_u24 v112,72,v112\nv_lshrrev_b32 v113,6,v0\nv_mul_u32_u24 v114,144,v0\nv_lshl_add_u32 v112,v113,5,v112\n" \
"v_subrev_u32 v114,18368,v114\nv_mov_b32 v115,1048576\nv_cmp_gt_u32 vcc,144,v0\nv_and_b32 v113,1,v0\n" \
"v_lshl_add_u32 v112,v113,4,v112\nv_cndmask_b32 v114,v115,v114,vcc\nv_and_b32 v97,0xffff0000,v82\nv_lshlrev_b32 v96,16,v82\n" \
"s_add_u32 s24,s93,s94\nv_cmp_gt_u32 vcc,128,v0\nv_add_u32 v113,8,v112\nv_mul_f32 v96,0x3ee38e39,v96\n" \
"v_mul_f32 v97,0x3ee38e39,v97\ns_sub_u32 s24,s24,253\nv_cndmask_b32 v112,v114,v112,vcc\nv_and_b32 v99,0xffff0000,v83\n" \
"v_ldexp_f32 v96,v96,s24\nv_ldexp_f32 v97,v97,s24\nv_lshlrev_b32_e64 v98,16,v83\nds_write_b64 v112,v[96:97] offset:28800\n" \
"v_add_u32 v115,72,v114\nv_mul_f32 v98,0x3ee38e39,v98\nv_mul_f32 v99,0x3ee38e39,v99\nv_cndmask_b32_e64 v113,v115,v113,vcc\n" \
"v_ldexp_f32 v98,v98,s24\nv_ldexp_f32 v99,v99,s24\nds_write_b64 v113,v[98:99] offset:28800\nv_and_b32 v101,0xffff0000,v86\n" \
"v_lshlrev_b32 v100,16,v86\nds_write_b64 v112,v[100:101] offset:31104\nv_and_b32 v103,0xffff0000,v87\nv_lshlrev_b32 v102,16,v87\n" \
"s_lshr_b32 s25,s33,3\nds_write_b64 v113,v[102:103] offset:31104\ns_mul_i32 s25,s25,576\nv_and_b32 v105,0xffff0000,v90\n" \
"v_lshlrev_b32 v104,16,v90\ns_sub_u32 s25,163200,s25\nds_write_b64 v112,v[104:105] offset:33408\nv_add_u32 v114,s25,v2\n" \
"v_subrev_u32 v115,163200,v114\nv_and_b32 v107,0xffff0000,v91\nv_lshlrev_b32 v106,16,v91\nv_mov_b32 v116,1048576\n" \
"v_cmp_gt_u32 vcc,576,v115\nds_write_b64 v113,v[106:107] offset:33408\nv_and_b32 v109,0xffff0000,v94\nv_cndmask_b32 v114,v116,v114,vcc\n" \
"v_and_b32 v111,0xffff0000,v95\nv_lshlrev_b32 v108,16,v94\nv_lshlrev_b32 v110,16,v95\nds_write_b128 v114,v[108:111]\n" \
"buffer_load_dwordx4 v[112:115],v3,s[36:39],s16 offen sc1\nbuffer_load_dwordx4 v[116:119],v3,s[36:39],s17 offen sc1\nv_and_b32 v124,48,v0\nv_cmp_gt_u32 vcc,64,v0\n" \
"v_bfe_u32 v127,v0,5,1\nv_and_b32 v13,31,v0\nv_cndmask_b32_e64 v124,v124,v3,vcc\nbuffer_load_dwordx4 v[120:123],v124,s[36:39],s18 offen sc1\n" \
"v_mul_u32_u24 v127,2448,v127\nv_mul_u32_u24 v13,72,v13\ns_mul_i32 s27,s3,4896\nv_add3_u32 v13,v13,v127,s27\n" \
"v_mov_b32 v127,0\ns_waitcnt vmcnt(27)\nds_read_b64_tr_b16 a[194:195],v8 offset:24576\nds_read_b64_tr_b16 a[198:199],v9 offset:24576\n" \
"s_waitcnt lgkmcnt(2)\ns_barrier\nds_read_b64_tr_b16 a[202:203],v10 offset:24576\nds_read_b64_tr_b16 a[206:207],v11 offset:24576\n" \
"ds_read_b64_tr_b16 a[210:211],v8 offset:26624\nds_read_b64_tr_b16 a[214:215],v9 offset:26624\nds_read_b64_tr_b16 a[218:219],v10 offset:26624\nds_read_b64_tr_b16 a[222:223],v11 offset:26624\n" \
"s_waitcnt vmcnt(23)\nds_read_b64_tr_b16 a[224:225],v8\nds_read_b64_tr_b16 a[228:229],v9\nds_read_b64_tr_b16 a[232:233],v10\n" \
"ds_read_b64_tr_b16 a[236:237],v11\nds_read_b64_tr_b16 a[240:241],v8 offset:2048\nds_read_b64_tr_b16 a[244:245],v9 offset:2048\nds_read_b64_tr_b16 a[248:249],v10 offset:2048\n" \
"ds_read_b64_tr_b16 a[252:253],v11 offset:2048\ns_waitcnt vmcnt(19)\nds_read_b64_tr_b16 a[226:227],v8 offset:4096\nds_read_b64_tr_b16 a[230:231],v9 offset:4096\n" \
"ds_read_b64_tr_b16 a[234:235],v10 offset:4096\nds_read_b64_tr_b16 a[238:239],v11 offset:4096\nds_read_b64_tr_b16 a[242:243],v8 offset:6144\nds_read_b64_tr_b16 a[246:247],v9 offset:6144\n" \
"ds_read_b64_tr_b16 a[250:251],v10 offset:6144\nds_read_b64_tr_b16 a[254:255],v11 offset:6144\ns_waitcnt vmcnt(15)\nds_read_b64_tr_b16 v[192:193],v15 offset:8192\n" \
"ds_read_b64_tr_b16 v[196:197],v15 offset:8704\nds_read_b64_tr_b16 v[194:195],v15 offset:9216\nds_read_b64_tr_b16 v[198:199],v15 offset:9728\nds_read_b64_tr_b16 v[200:201],v15 offset:10240\n" \
"ds_read_b64_tr_b16 v[204:205],v15 offset:10752\nds_read_b64_tr_b16 v[202:203],v15 offset:11264\nds_read_b64_tr_b16 v[206:207],v15 offset:11776\ns_waitcnt vmcnt(11)\n" \
"ds_read_b64_tr_b16 v[208:209],v15 offset:12288\nds_read_b64_tr_b16 v[212:213],v15 offset:12800\nds_read_b64_tr_b16 v[210:211],v15 offset:13312\nds_read_b64_tr_b16 v[214:215],v15 offset:13824\n" \
"ds_read_b64_tr_b16 v[216:217],v15 offset:14336\nds_read_b64_tr_b16 v[220:221],v15 offset:14848\nds_read_b64_tr_b16 v[218:219],v15 offset:15360\nds_read_b64_tr_b16 v[222:223],v15 offset:15872\n" \
"s_waitcnt vmcnt(7)\nds_read_b64_tr_b16 v[224:225],v15 offset:16384\nds_read_b64_tr_b16 v[228:229],v15 offset:16896\nds_read_b64_tr_b16 v[226:227],v15 offset:17408\n" \
"ds_read_b64_tr_b16 v[230:231],v15 offset:17920\nds_read_b64_tr_b16 v[232:233],v15 offset:18432\nds_read_b64_tr_b16 v[236:237],v15 offset:18944\nds_read_b64_tr_b16 v[234:235],v15 offset:19456\n" \
"ds_read_b64_tr_b16 v[238:239],v15 offset:19968\ns_waitcnt vmcnt(3)\nds_read_b64_tr_b16 v[240:241],v15 offset:20480\nds_read_b64_tr_b16 v[244:245],v15 offset:20992\n" \
"ds_read_b64_tr_b16 v[242:243],v15 offset:21504\nds_read_b64_tr_b16 v[246:247],v15 offset:22016\nds_read_b64_tr_b16 v[248:249],v15 offset:22528\nds_read_b64_tr_b16 v[252:253],v15 offset:23040\n" \
"ds_read_b64_tr_b16 v[250:251],v15 offset:23552\nds_read_b64_tr_b16 v[254:255],v15 offset:24064\ns_waitcnt vmcnt(1)\nv_and_b32 v125,v112,v116\n" \
"s_waitcnt vmcnt(0)\nv_bitop3_b32 v125,v125,v120,1 bitop3:0x80\nv_cmp_eq_u32 vcc,0,v125\ns_nop 0\n" \
"s_cbranch_vccnz .LsuN_%=\n.LbuN_%=:\nv_mfma_f32_16x16x32_f16 v[80:83],v[32:35],v[112:115],0\nv_and_b32 v3,31,v0\n" \
"v_mul_u32_u24 v3,72,v3\nv_mfma_f32_16x16x32_f16 v[84:87],v[36:39],v[112:115],0\nv_add_u32 v13,9216,v13\nv_mov_b32 v104,-1\n" \
"v_mfma_f32_16x16x32_f16 v[88:91],v[32:35],v[116:119],0\nv_mov_b32 v105,-1\nv_mov_b32 v106,-1\nv_mfma_f32_16x16x32_f16 v[92:95],v[36:39],v[116:119],0\n" \
"v_mov_b32 v107,-1\ns_add_u32 s27,s2,0\nv_mfma_f32_16x16x32_f16 v[96:99],v[32:35],v[120:123],0\ns_lshr_b32 s31,s27,4\n" \
"s_mul_i32 s96,s31,37\ns_lshr_b32 s96,s96,8\ns_lshl2_add_u32 s31,s96,s31\ns_mul_i32 s31,s31,409600\n" \
"s_and_b32 s96,s27,15\ns_lshl_b32 s96,s96,14\ns_add_u32 s31,s31,s96\ns_add_u32 s31,s31,13090816\n" \
"s_cmp_lt_u32 s27,158\ns_cselect_b32 s31,s31,2147483648\nbuffer_store_dwordx4 v[104:107],v2,s[36:39],s31 offen sc1\nv_mfma_f32_16x16x32_f16 v[100:103],v[36:39],v[120:123],0\n" \
"v_cvt_pk_f16_f32 v112,v84,v80\nv_cvt_pk_f16_f32 v113,v85,v81\nv_cvt_pk_f16_f32 v114,v86,v82\nv_cvt_pk_f16_f32 v115,v87,v83\n" \
"ds_write_b128 v57,v[112:115]\nv_cvt_pk_f16_f32 v116,v92,v88\nv_cvt_pk_f16_f32 v117,v93,v89\ns_add_u32 s31,s31,4096\n" \
"buffer_store_dwordx4 v[104:107],v2,s[36:39],s31 offen sc1\nv_cvt_pk_f16_f32 v118,v94,v90\nv_cvt_pk_f16_f32 v119,v95,v91\nds_write_b128 v58,v[116:119]\n" \
"v_cvt_pk_f16_f32 v120,v100,v96\nv_cvt_pk_f16_f32 v121,v101,v97\nv_cvt_pk_f16_f32 v122,v102,v98\nv_cvt_pk_f16_f32 v123,v103,v99\n" \
"ds_write_b128 v60,v[120:123]\ns_add_u32 s31,s31,4096\nbuffer_store_dwordx4 v[104:107],v2,s[36:39],s31 offen sc1\nv_add_u32 v60,31104,v3\n" \
"v_add_u32 v3,28800,v3\nds_read2_b64 v[32:35],v3 offset0:0 offset1:1\nds_read2_b64 v[36:39],v3 offset0:2 offset1:3\ns_add_u32 s31,s31,4096\n" \
"s_add_u32 s27,s2,111\ns_lshr_b32 s97,s27,4\ns_mul_i32 s95,s97,37\ns_lshr_b32 s95,s95,8\n" \
"s_lshl2_add_u32 s97,s95,s97\ns_mul_i32 s97,s97,409600\ns_and_b32 s95,s27,15\ns_lshl_b32 s95,s95,14\n" \
"buffer_store_dwordx4 v[104:107],v2,s[36:39],s31 offen sc1\ns_add_u32 s97,s97,s95\ns_add_u32 s97,s97,13090816\ns_cmp_lt_u32 s27,158\n" \
"s_cselect_b32 s97,s97,2147483648\ns_waitcnt lgkmcnt(2)\ns_barrier\nds_read_b64 v[112:113],v56\n" \
"ds_read_b64 v[116:117],v56 offset:512\nds_read_b64 v[120:121],v56 offset:256\nds_read_b64 v[124:125],v56 offset:768\nds_read_b64 v[114:115],v56 offset:1024\n" \
"ds_read_b64 v[122:123],v56 offset:1280\nds_read_b64 v[56:57],v3 offset:64\nbuffer_store_dwordx4 v[104:107],v2,s[36:39],s97 offen sc1\ns_add_u32 s97,s97,4096\n" \
"v_bfe_u32 v110,v0,1,2\nv_bfe_u32 v111,v0,4,1\nv_xor_b32 v110,v110,v111\nv_and_b32 v111,1,v0\n" \
"v_lshl_add_u32 v110,v110,1,v111\nv_bfe_u32 v111,v0,3,3\nv_lshlrev_b32 v111,12,v111\nv_lshl_add_u32 v4,v110,4,v111\n" \
"v_xor_b32 v5,64,v4\nv_add_u32 v5,31744,v5\nv_add_u32 v6,63488,v4\nbuffer_store_dwordx4 v[104:107],v2,s[36:39],s97 offen sc1\n" \
"s_waitcnt lgkmcnt(3)\nv_swap_b32 v113,v116\ns_add_u32 s97,s97,4096\nv_swap_b32 v121,v124\n" \
"v_add_u32 v7,63488,v5\ns_waitcnt lgkmcnt(0)\nv_mfma_f32_16x16x32_f16 v[80:83],v[112:115],v[40:43],0\nv_mov_b32 v118,v115\n" \
"v_mov_b32 v126,v123\nv_mfma_f32_16x16x32_f16 v[84:87],v[112:115],v[44:47],0\ns_lshr_b32 s26,s32,1\ns_mul_i32 s92,s26,1152\n" \
"v_mfma_f32_16x16x32_f16 v[88:91],v[120:123],v[40:43],0\nbuffer_store_dwordx4 v[104:107],v2,s[36:39],s97 offen sc1\nv_mfma_f32_16x16x32_f16 v[92:95],v[120:123],v[44:47],0\ns_add_u32 s97,s97,4096\n" \
"s_lshr_b32 s26,s3,1\nv_mfma_f32_16x16x32_f16 v[96:99],v[116:119],v[40:43],0\ns_lshl_b32 s26,s26,6\ns_add_u32 s92,s92,s26\n" \
"v_mfma_f32_16x16x32_f16 v[100:103],v[116:119],v[44:47],0\nv_pk_add_f32 v[88:89],v[80:81],v[88:89] neg_lo:[0,1] neg_hi:[0,1]\nv_pk_add_f32 v[90:91],v[82:83],v[90:91] neg_lo:[0,1] neg_hi:[0,1]\nv_pk_add_f32 v[92:93],v[84:85],v[92:93] neg_lo:[0,1] neg_hi:[0,1]\n" \
"buffer_store_dwordx4 v[104:107],v2,s[36:39],s97 offen sc1\nv_pk_add_f32 v[94:95],v[86:87],v[94:95] neg_lo:[0,1] neg_hi:[0,1]\nv_pk_fma_f32 v[80:81],v[80:81],2.0,v[88:89] op_sel_hi:[1,0,1] neg_lo:[0,0,1] neg_hi:[0,0,1]\nv_pk_fma_f32 v[82:83],v[82:83],2.0,v[90:91] op_sel_hi:[1,0,1] neg_lo:[0,0,1] neg_hi:[0,0,1]\n" \
"v_pk_fma_f32 v[84:85],v[84:85],2.0,v[92:93] op_sel_hi:[1,0,1] neg_lo:[0,0,1] neg_hi:[0,0,1]\nv_pk_fma_f32 v[86:87],v[86:87],2.0,v[94:95] op_sel_hi:[1,0,1] neg_lo:[0,0,1] neg_hi:[0,0,1]\nv_cvt_pk_f16_f32 v112,v80,v84\nv_cvt_pk_f16_f32 v113,v81,v85\n" \
"v_cvt_pk_f16_f32 v114,v82,v86\nv_cvt_pk_f16_f32 v115,v83,v87\nv_mfma_f32_16x16x32_f16 v[104:107],v[124:127],v[40:43],0\nv_cvt_pk_f16_f32 v116,v88,v92\n" \
"s_lshr_b32 s100,s33,3\ns_add_u32 s100,s100,0\ns_sub_u32 s101,s100,4\ns_min_u32 s100,s100,s101\n" \
"s_lshl_b32 s101,s100,12\ns_add_u32 s101,s101,s57\ns_add_u32 m0,s101,0\ns_and_b32 s101,s100,1\n" \
"s_lshr_b32 s100,s100,1\ns_lshl_b32 s100,s100,10\ns_add_u32 s100,s100,s101\ns_lshl_b32 s100,s100,7\n" \
"s_lshr_b32 s31,s33,3\ns_add_u32 s31,s31,1\ns_sub_u32 s96,s31,4\ns_min_u32 s31,s31,s96\n" \
"s_lshl_b32 s96,s31,12\ns_add_u32 s96,s96,s57\ns_lshr_b32 s97,s33,3\ns_add_u32 s97,s97,2\n" \
"s_sub_u32 s95,s97,4\ns_min_u32 s97,s97,s95\ns_lshl_b32 s95,s97,12\ns_add_u32 s95,s95,s57\n" \
"v_mfma_f32_16x16x32_f16 v[80:83],v[16:19],v[112:115],0\nv_cvt_pk_f16_f32 v117,v89,v93\nv_cvt_pk_f16_f32 v118,v90,v94\nv_mfma_f32_16x16x32_f16 v[84:87],v[20:23],v[112:115],0\n" \
"v_cvt_pk_f16_f32 v119,v91,v95\nv_pk_add_f32 v[104:105],v[96:97],v[104:105] neg_lo:[0,1] neg_hi:[0,1]\nv_mfma_f32_16x16x32_f16 v[108:111],v[124:127],v[44:47],0\nv_pk_add_f32 v[106:107],v[98:99],v[106:107] neg_lo:[0,1] neg_hi:[0,1]\n" \
"v_pk_fma_f32 v[96:97],v[96:97],2.0,v[104:105] op_sel_hi:[1,0,1] neg_lo:[0,0,1] neg_hi:[0,0,1]\nv_mfma_f32_16x16x32_f16 v[88:91],v[24:27],v[116:119],0\nv_pk_fma_f32 v[98:99],v[98:99],2.0,v[106:107] op_sel_hi:[1,0,1] neg_lo:[0,0,1] neg_hi:[0,0,1]\nds_read2_b64 v[40:43],v3 offset0:4 offset1:5\n" \
"v_mfma_f32_16x16x32_f16 v[92:95],v[28:31],v[116:119],0\nds_write2_b32 v48,v84,v80 offset0:0 offset1:1\nds_write2_b32 v49,v85,v81 offset0:0 offset1:1\nds_write2_b32 v50,v86,v82 offset0:0 offset1:1\n" \
"v_pk_add_f32 v[108:109],v[100:101],v[108:109] neg_lo:[0,1] neg_hi:[0,1]\nv_pk_add_f32 v[110:111],v[102:103],v[110:111] neg_lo:[0,1] neg_hi:[0,1]\nv_pk_fma_f32 v[100:101],v[100:101],2.0,v[108:109] op_sel_hi:[1,0,1] neg_lo:[0,0,1] neg_hi:[0,0,1]\nv_pk_fma_f32 v[102:103],v[102:103],2.0,v[110:111] op_sel_hi:[1,0,1] neg_lo:[0,0,1] neg_hi:[0,0,1]\n" \
"ds_write2_b32 v51,v87,v83 offset0:0 offset1:1\nv_cvt_pk_f16_f32 v120,v96,v100\nv_cvt_pk_f16_f32 v121,v97,v101\nv_cvt_pk_f16_f32 v122,v98,v102\n" \
"v_cvt_pk_f16_f32 v123,v99,v103\nds_write2_b32 v52,v92,v88 offset0:0 offset1:1\nds_write2_b32 v53,v93,v89 offset0:0 offset1:1\nv_mfma_f32_16x16x32_f16 v[96:99],v[16:19],v[120:123],0\n" \
"ds_write2_b32 v54,v94,v90 offset0:0 offset1:1\nv_mfma_f32_16x16x32_f16 v[100:103],v[20:23],v[120:123],0\nds_write2_b32 v55,v95,v91 offset0:0 offset1:1\nv_cvt_pk_f16_f32 v124,v104,v108\n" \
"v_cvt_pk_f16_f32 v125,v105,v109\nv_cvt_pk_f16_f32 v126,v106,v110\nv_cvt_pk_f16_f32 v127,v107,v111\nv_add_u32 v48,2448,v48\n" \
"v_add_u32 v49,2448,v49\nv_mfma_f32_16x16x32_f16 v[104:107],v[24:27],v[124:127],0\nds_write2_b32 v48,v100,v96 offset0:0 offset1:1\nv_mfma_f32_16x16x32_f16 v[108:111],v[28:31],v[124:127],0\n" \
"ds_write2_b32 v49,v101,v97 offset0:0 offset1:1\nv_add_u32 v50,2448,v50\nds_write2_b32 v50,v102,v98 offset0:0 offset1:1\nv_add_u32 v51,2448,v51\n" \
"ds_write2_b32 v51,v103,v99 offset0:0 offset1:1\nv_add_u32 v52,2448,v52\nv_add_u32 v53,2448,v53\nv_add_u32 v54,2448,v54\n" \
"ds_write2_b32 v52,v108,v104 offset0:0 offset1:1\nds_write2_b32 v53,v109,v105 offset0:0 offset1:1\nds_write2_b32 v54,v110,v106 offset0:0 offset1:1\nv_add_u32 v55,2448,v55\n" \
"ds_write2_b32 v55,v111,v107 offset0:0 offset1:1\nds_read2_b64 v[80:83],v13 offset0:0 offset1:1\nds_read2_b64 v[84:87],v13 offset0:2 offset1:3\ns_lshl_b32 s26,s3,8\n" \
"s_add_u32 s92,s92,s26\ns_lshl_b32 s92,s92,4\ns_and_b32 s26,s32,1\ns_lshl3_add_u32 s92,s26,s92\n" \
"s_and_b32 s26,s33,7\ns_add_u32 s92,s92,s26\nds_read2_b64 v[88:91],v13 offset0:4 offset1:5\ns_lshl_b32 s92,s92,8\n" \
"s_add_u32 s100,s100,s92\nbuffer_load_dwordx4 v4,s[40:43],s100 offen lds\nds_read2_b64 v[44:47],v3 offset0:6 offset1:7\nv_and_b32 v48,31,v0\n" \
"ds_read2_b64 v[92:95],v13 offset0:6 offset1:7\nv_mad_u32_u24 v49,v48,56,0\nv_sub_u32 v48,v13,v49\nv_and_b32 v49,31,v0\n" \
"v_mul_u32_u24 v49,68,v49\nv_sub_u32 v49,v13,v49\nbuffer_load_dwordx4 v5,s[40:43],s100 offen offset:1024 lds\nds_read_b64 v[96:97],v13 offset:64\n" \
"s_waitcnt lgkmcnt(5)\nv_pk_fma_f32 v[62:63],v[80:81],v[32:33],v[62:63]\nv_pk_fma_f32 v[64:65],v[82:83],v[34:35],v[64:65]\nv_pk_mul_f32 v[102:103],v[62:63],v[62:63]\n" \
"s_waitcnt lgkmcnt(4)\nv_pk_fma_f32 v[66:67],v[84:85],v[36:37],v[66:67]\nv_pk_add_f32 v[100:101],v[62:63],v[64:65]\nv_pk_fma_f32 v[102:103],v[64:65],v[64:65],v[102:103]\n" \
"v_pk_fma_f32 v[68:69],v[86:87],v[38:39],v[68:69]\nbuffer_load_dwordx4 v6,s[40:43],s100 offen offset:2048 lds\nv_pk_add_f32 v[100:101],v[100:101],v[66:67]\nv_pk_fma_f32 v[102:103],v[66:67],v[66:67],v[102:103]\n" \
"ds_read2_b64 v[112:115],v60 offset0:0 offset1:1\nds_read2_b64 v[116:119],v60 offset0:2 offset1:3\ns_waitcnt lgkmcnt(4)\nv_pk_fma_f32 v[70:71],v[88:89],v[40:41],v[70:71]\n" \
"v_pk_add_f32 v[100:101],v[100:101],v[68:69]\nds_read2_b64 v[120:123],v60 offset0:4 offset1:5\nv_pk_fma_f32 v[102:103],v[68:69],v[68:69],v[102:103]\nbuffer_load_dwordx4 v7,s[40:43],s100 offen offset:3072 lds\n" \
"ds_read2_b64 v[124:127],v60 offset0:6 offset1:7\nv_pk_fma_f32 v[72:73],v[90:91],v[42:43],v[72:73]\nv_pk_add_f32 v[100:101],v[100:101],v[70:71]\nds_read_b64 v[98:99],v60 offset:64\n" \
"v_add_u32 v60,2304,v60\nv_pk_fma_f32 v[102:103],v[70:71],v[70:71],v[102:103]\nds_read2_b64 v[16:19],v60 offset0:0 offset1:1\ns_waitcnt lgkmcnt(6)\n" \
"v_pk_fma_f32 v[74:75],v[92:93],v[44:45],v[74:75]\ns_add_u32 m0,s96,0\ns_and_b32 s96,s31,1\ns_lshr_b32 s31,s31,1\n" \
"s_lshl_b32 s31,s31,10\ns_add_u32 s31,s31,s96\ns_lshl_b32 s31,s31,7\ns_add_u32 s31,s31,s92\n" \
"buffer_load_dwordx4 v4,s[40:43],s31 offen lds\nv_pk_add_f32 v[100:101],v[100:101],v[72:73]\nv_pk_fma_f32 v[102:103],v[72:73],v[72:73],v[102:103]\nv_pk_fma_f32 v[76:77],v[94:95],v[46:47],v[76:77]\n" \
"v_pk_add_f32 v[100:101],v[100:101],v[74:75]\nv_pk_fma_f32 v[102:103],v[74:75],v[74:75],v[102:103]\nds_read2_b64 v[20:23],v60 offset0:2 offset1:3\nv_pk_fma_f32 v[78:79],v[96:97],v[56:57],v[78:79]\n" \
"v_pk_add_f32 v[100:101],v[100:101],v[76:77]\nbuffer_load_dwordx4 v5,s[40:43],s31 offen offset:1024 lds\nv_pk_fma_f32 v[102:103],v[76:77],v[76:77],v[102:103]\nv_pk_add_f32 v[100:101],v[100:101],v[78:79]\n" \
"v_pk_fma_f32 v[102:103],v[78:79],v[78:79],v[102:103]\nds_read2_b64 v[24:27],v60 offset0:4 offset1:5\nv_add_f32 v100,v100,v101\nv_add_f32 v102,v102,v103\n" \
"ds_read2_b64 v[28:31],v60 offset0:6 offset1:7\nds_read_b64 v[58:59],v60 offset:64\nbuffer_load_dwordx4 v6,s[40:43],s31 offen offset:2048 lds\nv_permlane16_swap_b32 v100,v102\n" \
"v_add_f32 v100,v100,v102\ns_lshr_b32 s100,s33,3\ns_add_u32 s100,s100,3\nv_add_f32_dpp v100,v100,v100 quad_perm:[1,0,3,2] row_mask:0xf bank_mask:0xf bound_ctrl:1\n" \
"s_sub_u32 s101,s100,4\ns_min_u32 s100,s100,s101\nv_add_f32_dpp v100,v100,v100 quad_perm:[2,3,0,1] row_mask:0xf bank_mask:0xf bound_ctrl:1\nbuffer_load_dwordx4 v7,s[40:43],s31 offen offset:3072 lds\n" \
"s_add_u32 m0,s95,0\nv_add_f32_dpp v100,v100,v100 row_half_mirror row_mask:0xf bank_mask:0xf bound_ctrl:1\ns_and_b32 s95,s97,1\ns_lshr_b32 s97,s97,1\n" \
"v_add_f32_dpp v100,v100,v100 row_mirror row_mask:0xf bank_mask:0xf bound_ctrl:1\nv_mul_f32 v104,0x3ae38e39,v100\nv_mul_f32 v102,0x3ae38e39,v100\ns_lshl_b32 s97,s97,10\n" \
"s_add_u32 s97,s97,s95\ns_lshl_b32 s97,s97,7\ns_add_u32 s97,s97,s92\nbuffer_load_dwordx4 v4,s[40:43],s97 offen lds\n" \
"v_permlane16_swap_b32 v104,v102\nv_fma_f32 v102,-v104,v104,v102\nv_max_f32 v102,0,v102\nv_add_f32 v102,0x3727c5ac,v102\n" \
"v_rsq_f32 v100,v102\ns_lshl_b32 s101,s100,12\nbuffer_load_dwordx4 v5,s[40:43],s97 offen offset:1024 lds\nv_mul_f32 v101,-v104,v100\n" \
"v_pk_fma_f32 v[80:81],v[62:63],v[100:101],v[100:101] op_sel:[0,0,1] op_sel_hi:[1,0,1]\nv_pk_fma_f32 v[82:83],v[64:65],v[100:101],v[100:101] op_sel:[0,0,1] op_sel_hi:[1,0,1]\nv_pk_fma_f32 v[84:85],v[66:67],v[100:101],v[100:101] op_sel:[0,0,1] op_sel_hi:[1,0,1]\nv_pk_fma_f32 v[86:87],v[68:69],v[100:101],v[100:101] op_sel:[0,0,1] op_sel_hi:[1,0,1]\n" \
"v_pk_fma_f32 v[88:89],v[70:71],v[100:101],v[100:101] op_sel:[0,0,1] op_sel_hi:[1,0,1]\nv_pk_fma_f32 v[90:91],v[72:73],v[100:101],v[100:101] op_sel:[0,0,1] op_sel_hi:[1,0,1]\nv_pk_fma_f32 v[92:93],v[74:75],v[100:101],v[100:101] op_sel:[0,0,1] op_sel_hi:[1,0,1]\nbuffer_load_dwordx4 v6,s[40:43],s97 offen offset:2048 lds\n" \
"v_pk_fma_f32 v[94:95],v[76:77],v[100:101],v[100:101] op_sel:[0,0,1] op_sel_hi:[1,0,1]\nv_pk_fma_f32 v[96:97],v[78:79],v[100:101],v[100:101] op_sel:[0,0,1] op_sel_hi:[1,0,1]\ns_add_u32 s101,s101,s57\ns_waitcnt lgkmcnt(0)\n" \
"v_readlane_b32 s24,v112,0\nv_mov_b32 v60,v61\ns_mov_b32 s25,s24\nv_pk_add_f32 v[112:113],v[112:113],s[24:25] neg_lo:[0,1] neg_hi:[0,1]\n" \
"v_pk_fma_f32 v[80:81],v[80:81],v[16:17],v[112:113]\nbuffer_load_dwordx4 v7,s[40:43],s97 offen offset:3072 lds\nv_pk_mul_f32 v[102:103],v[80:81],v[80:81]\ns_add_u32 m0,s101,0\n" \
"s_and_b32 s101,s100,1\ns_lshr_b32 s100,s100,1\ns_lshl_b32 s100,s100,10\ns_add_u32 s100,s100,s101\n" \
"s_lshl_b32 s100,s100,7\ns_add_u32 s100,s100,s92\nbuffer_load_dwordx4 v4,s[40:43],s100 offen lds\nv_pk_add_f32 v[114:115],v[114:115],s[24:25] neg_lo:[0,1] neg_hi:[0,1]\n" \
"v_pk_fma_f32 v[82:83],v[82:83],v[18:19],v[114:115]\nv_pk_add_f32 v[100:101],v[80:81],v[82:83]\nv_pk_fma_f32 v[102:103],v[82:83],v[82:83],v[102:103]\nv_pk_add_f32 v[116:117],v[116:117],s[24:25] neg_lo:[0,1] neg_hi:[0,1]\n" \
"v_pk_fma_f32 v[84:85],v[84:85],v[20:21],v[116:117]\nv_pk_add_f32 v[100:101],v[100:101],v[84:85]\nbuffer_load_dwordx4 v5,s[40:43],s100 offen offset:1024 lds\nv_pk_fma_f32 v[102:103],v[84:85],v[84:85],v[102:103]\n" \
"v_pk_add_f32 v[118:119],v[118:119],s[24:25] neg_lo:[0,1] neg_hi:[0,1]\nv_pk_fma_f32 v[86:87],v[86:87],v[22:23],v[118:119]\nv_pk_add_f32 v[100:101],v[100:101],v[86:87]\nv_pk_fma_f32 v[102:103],v[86:87],v[86:87],v[102:103]\n" \
"v_pk_add_f32 v[120:121],v[120:121],s[24:25] neg_lo:[0,1] neg_hi:[0,1]\nv_pk_fma_f32 v[88:89],v[88:89],v[24:25],v[120:121]\nbuffer_load_dwordx4 v6,s[40:43],s100 offen offset:2048 lds\nv_pk_add_f32 v[100:101],v[100:101],v[88:89]\n" \
"v_pk_fma_f32 v[102:103],v[88:89],v[88:89],v[102:103]\nv_pk_add_f32 v[122:123],v[122:123],s[24:25] neg_lo:[0,1] neg_hi:[0,1]\nv_pk_fma_f32 v[90:91],v[90:91],v[26:27],v[122:123]\nv_pk_add_f32 v[100:101],v[100:101],v[90:91]\n" \
"v_pk_fma_f32 v[102:103],v[90:91],v[90:91],v[102:103]\nv_pk_add_f32 v[124:125],v[124:125],s[24:25] neg_lo:[0,1] neg_hi:[0,1]\nbuffer_load_dwordx4 v7,s[40:43],s100 offen offset:3072 lds\nv_pk_fma_f32 v[92:93],v[92:93],v[28:29],v[124:125]\n" \
"v_pk_add_f32 v[100:101],v[100:101],v[92:93]\nv_pk_fma_f32 v[102:103],v[92:93],v[92:93],v[102:103]\nv_pk_add_f32 v[126:127],v[126:127],s[24:25] neg_lo:[0,1] neg_hi:[0,1]\nv_pk_fma_f32 v[94:95],v[94:95],v[30:31],v[126:127]\n" \
"v_pk_add_f32 v[100:101],v[100:101],v[94:95]\nv_pk_fma_f32 v[102:103],v[94:95],v[94:95],v[102:103]\nv_pk_add_f32 v[98:99],v[98:99],s[24:25] neg_lo:[0,1] neg_hi:[0,1]\nv_pk_fma_f32 v[96:97],v[96:97],v[58:59],v[98:99]\n" \
"v_pk_add_f32 v[100:101],v[100:101],v[96:97]\nv_pk_fma_f32 v[102:103],v[96:97],v[96:97],v[102:103]\nv_add_f32 v100,v100,v101\nv_add_f32 v102,v102,v103\n" \
"v_pk_add_f32 v[62:63],v[62:63],v[60:61] op_sel_hi:[1,0]\nv_cvt_pk_bf16_f32 v32,v62,v63\nv_permlane16_swap_b32 v100,v102\nv_add_f32 v100,v100,v102\n" \
"v_pk_add_f32 v[64:65],v[64:65],v[60:61] op_sel_hi:[1,0]\nv_cvt_pk_bf16_f32 v33,v64,v65\nv_add_f32_dpp v100,v100,v100 quad_perm:[1,0,3,2] row_mask:0xf bank_mask:0xf bound_ctrl:1\nv_pk_add_f32 v[66:67],v[66:67],v[60:61] op_sel_hi:[1,0]\n" \
"v_cvt_pk_bf16_f32 v34,v66,v67\nv_add_f32_dpp v100,v100,v100 quad_perm:[2,3,0,1] row_mask:0xf bank_mask:0xf bound_ctrl:1\nv_pk_add_f32 v[68:69],v[68:69],v[60:61] op_sel_hi:[1,0]\nv_cvt_pk_bf16_f32 v35,v68,v69\n" \
"v_add_f32_dpp v100,v100,v100 row_half_mirror row_mask:0xf bank_mask:0xf bound_ctrl:1\nv_pk_add_f32 v[70:71],v[70:71],v[60:61] op_sel_hi:[1,0]\nv_cvt_pk_bf16_f32 v36,v70,v71\nv_add_f32_dpp v100,v100,v100 row_mirror row_mask:0xf bank_mask:0xf bound_ctrl:1\n" \
"v_mul_f32 v104,0x3ae38e39,v100\nv_mul_f32 v102,0x3ae38e39,v100\nv_pk_add_f32 v[72:73],v[72:73],v[60:61] op_sel_hi:[1,0]\nv_cvt_pk_bf16_f32 v37,v72,v73\n" \
"v_permlane16_swap_b32 v104,v102\nv_fma_f32 v102,-v104,v104,v102\nv_max_f32 v102,0,v102\nv_add_f32 v102,0x3727c5ac,v102\n" \
"v_rsq_f32_e64 v100,v102\nv_pk_add_f32 v[74:75],v[74:75],v[60:61] op_sel_hi:[1,0]\nv_mul_f32 v101,-v104,v100\nv_pk_fma_f32 v[80:81],v[80:81],v[100:101],v[100:101] op_sel:[0,0,1] op_sel_hi:[1,0,1]\n" \
"v_pk_fma_f32 v[82:83],v[82:83],v[100:101],v[100:101] op_sel:[0,0,1] op_sel_hi:[1,0,1]\nv_pk_fma_f32 v[84:85],v[84:85],v[100:101],v[100:101] op_sel:[0,0,1] op_sel_hi:[1,0,1]\nv_pk_fma_f32 v[86:87],v[86:87],v[100:101],v[100:101] op_sel:[0,0,1] op_sel_hi:[1,0,1]\nv_cvt_pk_bf16_f32 v16,v80,v81\n" \
"v_cvt_pk_bf16_f32 v17,v82,v83\nv_cvt_pk_bf16_f32 v18,v84,v85\nv_cvt_pk_bf16_f32 v19,v86,v87\nds_write_b128 v48,v[16:19]\n" \
"v_pk_fma_f32 v[88:89],v[88:89],v[100:101],v[100:101] op_sel:[0,0,1] op_sel_hi:[1,0,1]\nv_pk_fma_f32 v[90:91],v[90:91],v[100:101],v[100:101] op_sel:[0,0,1] op_sel_hi:[1,0,1]\nv_pk_fma_f32 v[92:93],v[92:93],v[100:101],v[100:101] op_sel:[0,0,1] op_sel_hi:[1,0,1]\nv_pk_fma_f32 v[94:95],v[94:95],v[100:101],v[100:101] op_sel:[0,0,1] op_sel_hi:[1,0,1]\n" \
"v_cvt_pk_bf16_f32 v20,v88,v89\nv_cvt_pk_bf16_f32 v21,v90,v91\nv_cvt_pk_bf16_f32 v22,v92,v93\nv_cvt_pk_bf16_f32 v23,v94,v95\n" \
"ds_write_b128 v48,v[20:23] offset:512\nv_pk_fma_f32 v[96:97],v[96:97],v[100:101],v[100:101] op_sel:[0,0,1] op_sel_hi:[1,0,1]\nv_cvt_pk_bf16_f32 v24,v96,v97\nds_write_b32 v49,v24 offset:1024\n" \
"v_cvt_pk_bf16_f32 v38,v74,v75\nv_pk_add_f32 v[76:77],v[76:77],v[60:61] op_sel_hi:[1,0]\nv_cvt_pk_bf16_f32 v39,v76,v77\nv_pk_add_f32 v[78:79],v[78:79],v[60:61] op_sel_hi:[1,0]\n" \
"v_cvt_pk_bf16_f32 v40,v78,v79\nv_bfe_u32 v50,v0,2,2\nv_mul_u32_u24 v50,4896,v50\nv_bfe_u32 v51,v0,4,2\n" \
"v_lshl_add_u32 v51,v51,2,s3\nv_lshl_add_u32 v50,v51,5,v50\nv_and_b32 v52,3,v0\nv_lshl_add_u32 v50,v52,3,v50\n" \
"v_and_b32 v52,0xf0,v1\nv_lshl_add_u32 v51,v51,16,v52\nv_bfrev_b32 v52,1\nv_cmp_gt_u32 vcc,256,v1\n" \
"v_and_b32 v53,31,v0\nv_mul_u32_u24 v53,12,v53\nv_cndmask_b32 v52,v52,v51,vcc\nv_sub_u32 v53,v12,v53\n" \
"s_lshl_b32 s24,s34,8\ns_add_u32 s24,s24,9764864\ns_waitcnt lgkmcnt(0)\ns_barrier\n" \
"ds_read_b64_tr_b16 v[112:113],v50 offset:9216\nds_read_b64_tr_b16 v[114:115],v50 offset:11664\nds_read_b64_tr_b16 v[116:117],v50 offset:9728\nds_read_b64_tr_b16 v[118:119],v50 offset:12176\n" \
"ds_read_b64_tr_b16 v[120:121],v50 offset:10240\nds_read_b64_tr_b16 v[122:123],v50 offset:12688\ns_add_u32 s25,s24,1048576\ns_add_u32 s26,s24,2097152\n" \
"s_add_u32 s13,s13,7405568\ns_load_dwordx2 s[4:5],s[0:1],56\ns_load_dwordx2 s[16:17],s[0:1],64\ns_waitcnt lgkmcnt(0)\n" \
"buffer_store_dwordx4 v[112:115],v51,s[36:39],s24 offen sc1\nbuffer_store_dwordx4 v[116:119],v51,s[36:39],s25 offen sc1\nbuffer_store_dwordx4 v[120:123],v52,s[36:39],s26 offen sc1\nbuffer_store_dwordx4 v[32:35],v12,s[36:39],s13 offen sc1\n" \
"buffer_store_dwordx4 v[36:39],v12,s[36:39],s13 offen offset:512 sc1\nbuffer_store_dword v40,v53,s[36:39],s13 offen offset:1024 sc1\ns_lshr_b32 s31,s33,3\ns_lshl1_add_u32 s31,s31,0\n" \
"s_sub_u32 s96,s31,7\ns_min_u32 s31,s31,s96\ns_lshl_b32 s96,s31,12\ns_add_u32 s96,s96,s57\n" \
"s_add_u32 m0,s96,0\ns_and_b32 s96,s31,1\ns_lshr_b32 s31,s31,1\ns_lshl_b32 s31,s31,10\n" \
"s_add_u32 s31,s31,s96\ns_lshl_b32 s31,s31,7\ns_add_u32 s31,s31,s92\ns_add_u32 s31,s31,262144\n" \
"s_lshr_b32 s97,s33,3\ns_lshl1_add_u32 s97,s97,1\ns_sub_u32 s95,s97,7\ns_min_u32 s97,s97,s95\n" \
"s_lshl_b32 s95,s97,12\ns_add_u32 s95,s95,s57\ns_lshr_b32 s100,s33,3\ns_lshl1_add_u32 s100,s100,2\n" \
"s_sub_u32 s101,s100,7\ns_min_u32 s100,s100,s101\ns_lshl_b32 s101,s100,12\ns_add_u32 s101,s101,s57\n" \
"v_and_b32 v64,63,v0\ns_and_b32 s25,s33,7\nv_lshrrev_b32 v66,5,v64\nv_and_b32 v67,1,v0\n" \
"s_lshl2_add_u32 s25,s25,s3\ns_and_b32 s26,s33,6\ns_lshr_b32 s27,s3,1\nv_lshl_add_u32 v66,v66,1,v67\n" \
"s_mul_i32 s24,s32,1152\ns_lshl_b32 s25,s25,5\ns_add_u32 s26,s26,s27\ns_mov_b32 s6,9216\n" \
"s_mov_b32 s7,0x20000\nv_lshlrev_b32 v68,3,v66\ns_add_u32 s25,s25,s24\nv_bfe_u32 v72,v0,1,1\n" \
"v_lshrrev_b32 v74,2,v64\ns_lshl4_add_u32 s26,s26,s24\nv_and_b32 v73,12,v0\nv_lshrrev_b32 v71,4,v64\n" \
"v_and_b32 v69,7,v0\nbuffer_load_dwordx2 v[80:81],v68,s[4:7],s25 offen\nv_lshl_add_u32 v72,v72,2,s21\nv_and_b32 v74,12,v74\n" \
"v_add_u32 v71,v71,v73\ns_lshl_b32 s24,s3,4\nv_lshlrev_b32 v70,1,v69\nv_lshl_add_u32 v72,v72,4,v74\n" \
"s_mov_b32 s15,s19\nv_add_u32 v71,s24,v71\nbuffer_load_ushort v82,v70,s[4:7],s26 offen offset:1024\ns_mov_b32 s18,4096\n" \
"s_mov_b32 s19,0x20000\nv_lshlrev_b32 v74,1,v71\ns_lshl_b32 s24,s22,8\nbuffer_load_ushort v83,v74,s[16:19],s24 offen\n" \
"v_add_u32 v72,s3,v72\nbuffer_load_ushort v84,v74,s[16:19],s24 offen offset:128\nv_lshlrev_b32_e64 v75,1,v72\nbuffer_load_ushort v85,v75,s[16:19],s24 offen\n" \
"v_lshl_add_u32 v73,v67,1,v73\nv_mul_u32_u24 v91,1152,v72\nv_lshl_add_u32 v91,v73,1,v91\nv_lshlrev_b32_e64 v73,2,v73\n" \
"v_add_u32 v73,163712,v73\nds_read_b64 v[86:87],v73\ns_mul_i32 s24,s22,147456\ns_mul_i32 s25,s20,288\n" \
"v_bfe_u32 v58,v0,1,1\ns_add_u32 s24,s24,s25\nv_mul_u32_u24 v58,544,v58\nv_bfe_u32 v75,v0,2,1\n" \
"s_lshl_b32 s25,s21,6\nv_lshl_add_u32 v58,v75,8,v58\ns_add_u32 s25,s25,s24\nv_bfe_u32 v75,v0,3,2\n" \
"s_add_u32 s24,s24,256\nv_lshl_add_u32 v58,v75,6,v58\nv_add_u32 v91,s24,v91\nv_mul_u32_u24 v90,1152,v71\n" \
"v_and_b32 v74,3,v0\nv_lshl_add_u32 v58,v66,4,v58\ns_mul_i32 s24,s3,1056\nv_lshl_add_u32 v90,v74,3,v90\n" \
"v_add_u32 v58,s24,v58\nv_add_u32 v90,s25,v90\ns_and_b32 s24,s3,1\nv_mul_u32_u24 v60,288,v75\n" \
"s_lshl_b32 s24,s24,2\ns_lshr_b32 s25,s3,1\nv_lshrrev_b32 v75,5,v64\ns_add_u32 s24,s24,s25\n" \
"v_lshl_add_u32 v60,v75,6,v60\ns_lshl_b32 s24,s24,5\nv_lshl_add_u32 v60,v69,2,v60\ns_add_u32 s24,s24,4224\n" \
"v_add_u32 v60,s24,v60\ns_bfe_u32 s24,s33,0x10001\ns_add_u32 s24,s24,s15\ns_add_u32 s24,s24,17\n" \
"s_lshl2_add_u32 s24,s24,s3\ns_waitcnt lgkmcnt(0)\nv_lshrrev_b32 v86,16,v86\ns_lshl_b32 s24,s24,7\n" \
"v_mov_b32 v92,v4\nv_mov_b32 v93,v5\nv_add_u32 v74,s57,v1\nv_or_b32 v95,v86,v87\n" \
"v_lshl_add_u32 v62,v64,1,s24\nv_bfrev_b32 v76,1\nv_cmp_eq_u32 vcc,0,v64\ns_lshl2_add_u32 s24,s34,s3\n" \
"s_lshl2_add_u32 s24,s24,65536\nv_cndmask_b32 v76,v76,0,vcc\nv_mov_b32 v77,1\ns_barrier\n" \
"v_mov_b32 v57,0xc0135761\nv_add_u32 v59,0x80000,v1\nv_lshl_add_u32 v61,v64,3,s23\nv_mov_b32 v63,0\n" \
"v_bfe_u32 v100,v0,4,2\nv_bfe_u32 v101,v0,2,2\nv_lshl_add_u32 v100,v100,3,v101\nv_bfe_u32 v101,v0,3,2\n" \
"v_lshl_add_u32 v100,v100,2,v101\nv_and_b32 v101,3,v0\nv_lshl_add_u32 v100,v100,2,v101\nv_lshlrev_b32 v100,3,v100\n" \
"v_xor_b32 v8,0,v100\nv_add_u32 v8,s57,v8\nv_mov_b32 v88,v8\nv_xor_b32 v9,32,v100\n" \
"v_add_u32 v9,s57,v9\nv_xor_b32 v10,64,v100\nv_add_u32 v10,s57,v10\nv_xor_b32 v11,96,v100\n" \
"v_add_u32 v11,s57,v11\ns_waitcnt vmcnt(5)\nbuffer_store_dword v77,v76,s[36:39],s24 offen sc1\nds_read_b64_tr_b16 v[128:129],v8\n" \
"ds_read_b64_tr_b16 v[130:131],v8 offset:512\nds_read_b64_tr_b16 v[132:133],v9\nds_read_b64_tr_b16 v[134:135],v9 offset:512\nds_read_b64_tr_b16 v[136:137],v10\n" \
"ds_read_b64_tr_b16 v[138:139],v10 offset:512\nds_read_b64_tr_b16 v[140:141],v11\nds_read_b64_tr_b16 v[142:143],v11 offset:512\nds_read_b64_tr_b16 v[144:145],v8 offset:4096\n" \
"ds_read_b64_tr_b16 v[146:147],v8 offset:4608\nds_read_b64_tr_b16 v[148:149],v9 offset:4096\nds_read_b64_tr_b16 v[150:151],v9 offset:4608\nds_read_b64_tr_b16 v[152:153],v10 offset:4096\n" \
"ds_read_b64_tr_b16 v[154:155],v10 offset:4608\nds_read_b64_tr_b16 v[156:157],v11 offset:4096\nds_read_b64_tr_b16 v[158:159],v11 offset:4608\nds_read_b64_tr_b16 v[160:161],v8 offset:8192\n" \
"ds_read_b64_tr_b16 v[162:163],v8 offset:8704\ns_waitcnt vmcnt(5)\nv_and_b32 v53,0xffff0000,v80\nv_and_b32 v55,0xffff0000,v81\n" \
"v_lshlrev_b32 v52,16,v80\nv_lshlrev_b32 v54,16,v81\nds_read_b64_tr_b16 v[164:165],v9 offset:8192\nds_read_b64_tr_b16 v[166:167],v9 offset:8704\n" \
"ds_read_b64_tr_b16 v[168:169],v10 offset:8192\nds_read_b64_tr_b16 v[170:171],v10 offset:8704\nds_read_b64_tr_b16 v[172:173],v11 offset:8192\ns_waitcnt vmcnt(4)\n" \
"v_lshlrev_b32 v56,16,v82\nds_read_b64_tr_b16 v[174:175],v11 offset:8704\nds_read_b64_tr_b16 v[176:177],v8 offset:12288\nds_read_b64_tr_b16 v[178:179],v8 offset:12800\n" \
"ds_read_b64_tr_b16 v[180:181],v9 offset:12288\nds_read_b64_tr_b16 v[182:183],v9 offset:12800\ns_waitcnt vmcnt(2)\nv_lshl_or_b32 v89,v84,16,v83\n" \
"ds_write_b128 v74,v[88:91] offset:28672\ns_waitcnt vmcnt(1)\nv_lshlrev_b32_e64 v94,16,v85\nds_write_b128 v74,v[92:95] offset:29696\n" \
"ds_read_b64_tr_b16 v[184:185],v10 offset:12288\nds_read_b64_tr_b16 v[186:187],v10 offset:12800\nds_read_b64_tr_b16 v[188:189],v11 offset:12288\nds_read_b64_tr_b16 v[190:191],v11 offset:12800\n" \
"s_waitcnt lgkmcnt(0)\nbuffer_load_dwordx4 v4,s[40:43],s31 offen lds\nbuffer_load_dwordx4 v5,s[40:43],s31 offen offset:1024 lds\nbuffer_load_dwordx4 v6,s[40:43],s31 offen offset:2048 lds\n" \
"buffer_load_dwordx4 v7,s[40:43],s31 offen offset:3072 lds\ns_add_u32 m0,s95,0\ns_and_b32 s95,s97,1\ns_lshr_b32 s97,s97,1\n" \
"s_lshl_b32 s97,s97,10\ns_add_u32 s97,s97,s95\ns_lshl_b32 s97,s97,7\ns_add_u32 s97,s97,s92\n" \
"s_add_u32 s97,s97,262144\nbuffer_load_dwordx4 v4,s[40:43],s97 offen lds\nbuffer_load_dwordx4 v5,s[40:43],s97 offen offset:1024 lds\nbuffer_load_dwordx4 v6,s[40:43],s97 offen offset:2048 lds\n" \
"buffer_load_dwordx4 v7,s[40:43],s97 offen offset:3072 lds\ns_add_u32 m0,s101,0\ns_and_b32 s101,s100,1\ns_lshr_b32 s100,s100,1\n" \
"s_lshl_b32 s100,s100,10\ns_add_u32 s100,s100,s101\ns_lshl_b32 s100,s100,7\ns_add_u32 s100,s100,s92\n" \
"s_add_u32 s100,s100,262144\nbuffer_load_dwordx4 v4,s[40:43],s100 offen lds\nbuffer_load_dwordx4 v5,s[40:43],s100 offen offset:1024 lds\nbuffer_load_dwordx4 v6,s[40:43],s100 offen offset:2048 lds\n" \
"buffer_load_dwordx4 v7,s[40:43],s100 offen offset:3072 lds\ns_lshr_b32 s31,s33,3\ns_lshl1_add_u32 s31,s31,3\ns_sub_u32 s96,s31,7\n" \
"s_min_u32 s31,s31,s96\ns_lshl_b32 s96,s31,12\ns_add_u32 s96,s96,s57\ns_add_u32 m0,s96,0\n" \
"s_and_b32 s96,s31,1\ns_lshr_b32 s31,s31,1\ns_lshl_b32 s31,s31,10\ns_add_u32 s31,s31,s96\n" \
"s_lshl_b32 s31,s31,7\ns_add_u32 s31,s31,s92\ns_add_u32 s31,s31,262144\nbuffer_load_dwordx4 v4,s[40:43],s31 offen lds\n" \
"buffer_load_dwordx4 v5,s[40:43],s31 offen offset:1024 lds\nbuffer_load_dwordx4 v6,s[40:43],s31 offen offset:2048 lds\nbuffer_load_dwordx4 v7,s[40:43],s31 offen offset:3072 lds\ns_lshr_b32 s97,s33,3\n" \
"s_lshl1_add_u32 s97,s97,4\ns_sub_u32 s95,s97,7\ns_min_u32 s97,s97,s95\ns_lshl_b32 s95,s97,12\n" \
"s_add_u32 s95,s95,s57\ns_add_u32 m0,s95,0\ns_and_b32 s95,s97,1\ns_lshr_b32 s97,s97,1\n" \
"s_lshl_b32 s97,s97,10\ns_add_u32 s97,s97,s95\ns_lshl_b32 s97,s97,7\ns_add_u32 s97,s97,s92\n" \
"s_add_u32 s97,s97,262144\nbuffer_load_dwordx4 v4,s[40:43],s97 offen lds\nbuffer_load_dwordx4 v5,s[40:43],s97 offen offset:1024 lds\nbuffer_load_dwordx4 v6,s[40:43],s97 offen offset:2048 lds\n" \
"buffer_load_dwordx4 v7,s[40:43],s97 offen offset:3072 lds\ns_lshr_b32 s100,s33,3\ns_lshl1_add_u32 s100,s100,5\ns_sub_u32 s101,s100,7\n" \
"s_min_u32 s100,s100,s101\ns_lshl_b32 s101,s100,12\ns_add_u32 s101,s101,s57\ns_add_u32 m0,s101,0\n" \
"s_and_b32 s101,s100,1\ns_lshr_b32 s100,s100,1\ns_lshl_b32 s100,s100,10\ns_add_u32 s100,s100,s101\n" \
"s_lshl_b32 s100,s100,7\ns_add_u32 s100,s100,s92\ns_add_u32 s100,s100,262144\nbuffer_load_dwordx4 v4,s[40:43],s100 offen lds\n" \
"buffer_load_dwordx4 v5,s[40:43],s100 offen offset:1024 lds\nbuffer_load_dwordx4 v6,s[40:43],s100 offen offset:2048 lds\nbuffer_load_dwordx4 v7,s[40:43],s100 offen offset:3072 lds\ns_lshr_b32 s31,s33,3\n" \
"s_lshl1_add_u32 s31,s31,6\ns_sub_u32 s96,s31,7\ns_min_u32 s31,s31,s96\ns_lshl_b32 s96,s31,12\n" \
"s_add_u32 s96,s96,s57\ns_add_u32 m0,s96,0\ns_and_b32 s96,s31,1\ns_lshr_b32 s31,s31,1\n" \
"s_lshl_b32 s31,s31,10\ns_add_u32 s31,s31,s96\ns_lshl_b32 s31,s31,7\ns_add_u32 s31,s31,s92\n" \
"s_add_u32 s31,s31,262144\nbuffer_load_dwordx4 v4,s[40:43],s31 offen lds\nbuffer_load_dwordx4 v5,s[40:43],s31 offen offset:1024 lds\nbuffer_load_dwordx4 v6,s[40:43],s31 offen offset:2048 lds\n" \
"buffer_load_dwordx4 v7,s[40:43],s31 offen offset:3072 lds\ns_branch .Lgemm_%=\n.LC_%=:\nv_bfe_u32 v112,v0,3,2\n" \
"v_bfe_u32 v113,v0,5,1\nv_lshl_add_u32 v112,v112,8,v113\ns_lshl_b32 s48,s33,2\nv_lshrrev_b32 v113,3,v0\n" \
"s_add_u32 s48,s48,s3\nv_and_b32 v113,6,v113\nv_and_b32 v114,7,v0\ns_mul_i32 s48,s48,18432\n" \
"s_mul_i32 s49,s32,1152\nv_mul_u32_u24 v112,9216,v112\nv_xor_b32 v113,v113,v114\ns_add_u32 s48,s48,s49\n" \
"v_lshl_add_u32 v4,v113,4,v112\ns_mov_b32 s10,18874368\ns_mov_b32 s11,0x20000\ns_add_u32 s48,s48,1024\n" \
"s_add_u32 m0,s88,20224\ns_add_u32 s50,s48,9437184\nbuffer_load_dwordx4 v4,s[8:11],s48 offen lds\ns_add_u32 m0,s88,24320\n" \
"s_mov_b32 s51,4096\nbuffer_load_dwordx4 v4,s[8:11],s50 offen lds\ns_add_u32 m0,s88,28416\ns_mov_b32 s52,8192\n" \
"buffer_load_dwordx4 v2,s[4:7],0 offen lds\ns_add_u32 m0,s88,32512\ns_mov_b32 s53,14336\nbuffer_load_dwordx4 v2,s[4:7],s51 offen lds\n" \
"buffer_load_dwordx4 v[234:237],v1,s[4:7],s52 offen\nbuffer_load_dwordx4 v[238:241],v1,s[4:7],s52 offen offset:1024\nbuffer_load_dwordx4 v[242:245],v2,s[4:7],s53 offen\ns_mov_b32 s52,18432\n" \
"buffer_load_dwordx4 v[252:255],v2,s[4:7],s52 offen\ns_and_b32 s60,s33,7\ns_mul_i32 s92,s3,589824\ns_mul_i32 s57,s3,30720\n" \
"s_add_u32 s92,s92,s49\ns_lshl_b32 s62,s60,7\nv_add_u32 v5,17408,v4\nv_add_u32 v6,34816,v4\n" \
"v_add_u32 v7,52224,v4\ns_add_u32 s57,s57,38272\ns_add_u32 s92,s92,s62\ns_load_dwordx4 s[44:47],s[0:1],72\n" \
"v_and_b32 v120,0x3c0,v1\ns_lshr_b32 s59,s33,3\nv_bfe_u32 v121,v0,3,2\nv_lshlrev_b32 v120,1,v120\n" \
"s_lshl_b32 s56,s34,8\nv_bfe_u32 v73,v0,4,2\ns_lshl_b32 s59,s59,3\nv_bfe_u32 v113,v0,4,2\n" \
"v_lshl_add_u32 v120,v121,5,v120\nv_and_b32 v122,3,v0\ns_mul_i32 s54,s34,9344\ns_add_u32 s56,s56,10289152\n" \
"s_add_u32 s59,s59,s32\nv_and_b32 v72,15,v0\nv_cvt_f32_u32 v73,v73\nv_cvt_f32_u32 v74,s3\n" \
"v_mul_u32_u24 v113,589824,v113\nv_and_b32 v112,15,v0\nv_lshl_add_u32 v120,v122,3,v120\ns_add_u32 s35,s54,524288\n" \
"s_cmp_eq_u32 s3,2\ns_cselect_b32 s56,s56,2147483648\ns_lshl_b32 s59,s59,3\nv_bfe_u32 v233,v0,3,3\n" \
"v_mul_u32_u24 v233,8192,v233\nv_lshl_add_u32 v233,v1,3,v233\ns_lshl2_add_u32 s100,s33,s3\ns_add_u32 s100,s100,4294967260\n" \
"s_and_b32 s101,s100,31\ns_lshl_b32 s101,s101,3\ns_cmp_lt_u32 s100,256\ns_cselect_b32 s100,s100,0x40000\n" \
"s_lshr_b32 s100,s100,5\ns_add_u32 s100,s100,s101\ns_lshl_b32 s100,s100,6\ns_add_u32 s100,s100,s32\n" \
"s_mul_i32 s100,s100,1152\ns_mov_b32 m0,162176\nv_cvt_f32_u32_e64 v72,v72\nv_fmamk_f32 v73,v73,0x41800000,v74\n" \
"v_lshl_add_u32 v113,v112,4,v113\ns_add_u32 s58,s35,8192\nv_bfe_u32 v119,v0,4,2\nv_xor_b32 v8,0,v120\n" \
"v_xor_b32 v9,32,v120\nv_xor_b32 v10,64,v120\nv_xor_b32 v11,96,v120\ns_add_u32 s59,s59,s60\n" \
"v_mul_f32 v72,0xbb800000,v72\nv_and_b32 v118,15,v0\nv_mul_f32 v73,4.0,v73\nv_and_b32 v116,0x70,v2\n" \
"v_add_u32 v113,s56,v113\nv_add_u32 v114,s58,v2\nv_cmp_gt_u32 vcc,68,v0\nv_lshrrev_b32 v115,3,v0\n" \
"v_cvt_f32_u32 v120,s34\ns_lshl_b32 s59,s59,7\ns_lshl_b32 s61,s34,4\nv_lshrrev_b32 v48,1,v118\n" \
"v_and_b32 v49,1,v119\nv_mul_f32 v73,v73,v72\nv_cndmask_b32 v113,v113,v114,vcc\nv_lshl_add_u32 v115,v115,16,v116\n" \
"v_mul_f32 v50,0x3b800000,v120\ns_add_u32 s87,s59,262144\ns_add_u32 s61,s61,65536\nv_cvt_f32_u32 v48,v48\n" \
"v_cvt_f32_u32 v49,v49\nv_fmamk_f32 v79,v72,0x41800000,v73\nv_bfe_u32 v100,v0,1,2\nv_and_b32 v101,1,v0\n" \
"v_add_u32 v115,s87,v115\nv_mov_b32 v116,s61\nv_cmp_gt_u32 vcc,24,v0\nv_mul_f32 v48,0xbe000000,v48\n" \
"v_fma_f32 v49,v49,2.0,v50\nv_cos_f32 v78,v79\nv_lshl_add_u32 v100,v101,2,v100\nv_cndmask_b32 v115,v116,v115,vcc\n" \
"v_mul_f32 v49,v48,v49\nv_fmamk_f32 v81,v72,0x42000000,v73\nv_fmamk_f32 v83,v72,0x42400000,v73\nv_mul_f32 v101,0x3a000000,v120\n" \
"v_cvt_f32_u32 v100,v100\nv_bfrev_b32 v116,1\nv_cmp_gt_u32 vcc,25,v0\nv_fmamk_f32 v63,v48,0x40800000,v49\n" \
"v_cos_f32 v80,v81\nv_cos_f32 v82,v83\nv_fmamk_f32 v100,v100,0x3e000000,v101\nv_cndmask_b32 v115,v116,v115,vcc\n" \
"v_mov_b32 v16,-1\nv_mov_b32 v17,-1\nv_mov_b32 v18,-1\nv_mov_b32 v19,-1\n" \
"v_lshrrev_b32 v50,1,v119\nv_and_b32 v51,1,v0\nv_cos_f32 v62,v63\nv_cos_f32 v76,v73\n" \
"v_sin_f32 v77,v73\nv_sin_f32 v79,v79\nv_cos_f32 v84,v72\nv_sin_f32 v85,v72\n" \
"v_cvt_f32_u32 v101,v119\nbuffer_store_dwordx4 v[16:19],v2,s[36:39],s35 offen sc1\nv_cmp_eq_u32 vcc,v50,v51\nv_add_f32 v101,v101,v101\n" \
"v_mov_b32 v50,0x3e800000\nv_mul_f32 v101,v101,v100\nv_sin_f32 v81,v81\nv_sin_f32 v83,v83\n" \
"v_cndmask_b32 v52,0,v50,vcc\nv_cos_f32 v56,v49\nv_sin_f32 v57,v49\nv_sin_f32 v63,v63\n" \
"v_cos_f32 v106,v101\nv_sin_f32 v107,v101\nv_cmp_gt_u32 vcc,8,v118\nv_cvt_pk_f16_f32 v128,v76,-v77\n" \
"v_cvt_pk_f16_f32 v132,v77,v76\nv_cvt_pk_f16_f32 v129,v78,-v79\nv_cvt_pk_f16_f32 v133,v79,v78\nv_pk_mul_f32 v[92:93],v[76:77],v[84:85] op_sel:[1,1] op_sel_hi:[0,1] neg_lo:[1,0]\n" \
"v_pk_mul_f32 v[94:95],v[78:79],v[84:85] op_sel:[1,1] op_sel_hi:[0,1] neg_lo:[1,0]\ns_add_u32 s55,s35,4096\nv_cndmask_b32 v102,0,0.5,vcc\nv_lshlrev_b32 v51,31,v0\n" \
"buffer_store_dwordx4 v[16:19],v2,s[36:39],s55 offen sc1\nv_pk_fma_f32 v[76:77],v[76:77],v[84:85],v[92:93] op_sel_hi:[1,0,1]\nv_pk_fma_f32 v[78:79],v[78:79],v[84:85],v[94:95] op_sel_hi:[1,0,1]\nv_xor_b32 v54,v51,v52\n" \
"v_sub_f32_e64 v104,0.5,v102\nv_cvt_pk_f16_f32 v130,v80,-v81\nv_cvt_pk_f16_f32 v134,v81,v80\nv_cvt_pk_f16_f32 v131,v82,-v83\n" \
"v_cvt_pk_f16_f32 v135,v83,v82\nv_pk_mul_f32 v[92:93],v[80:81],v[84:85] op_sel:[1,1] op_sel_hi:[0,1] neg_lo:[1,0]\nv_pk_mul_f32 v[94:95],v[82:83],v[84:85] op_sel:[1,1] op_sel_hi:[0,1] neg_lo:[1,0]\nv_pk_mul_f32 v[58:59],v[56:57],v[52:53] op_sel_hi:[1,0]\n" \
"v_pk_mul_f32 v[60:61],v[56:57],v[54:55] op_sel_hi:[1,0]\nv_pk_mul_f32 v[64:65],v[62:63],v[52:53] op_sel_hi:[1,0]\nv_pk_mul_f32 v[66:67],v[62:63],v[54:55] op_sel_hi:[1,0]\nv_pk_mul_f32 v[108:109],v[106:107],v[102:103] op_sel_hi:[1,0]\n" \
"v_pk_mul_f32 v[110:111],v[106:107],v[104:105] op_sel_hi:[1,0]\nv_pk_fma_f32 v[80:81],v[80:81],v[84:85],v[92:93] op_sel_hi:[1,0,1]\nv_pk_fma_f32 v[82:83],v[82:83],v[84:85],v[94:95] op_sel_hi:[1,0,1]\nv_fmamk_f32 v57,v48,0x3f800000,v49\n" \
"v_fmamk_f32 v63,v48,0x40a00000,v49\nv_fmamk_f32 v107,v100,0x3f800000,v101\nv_cvt_pk_f16_f32 v136,v76,-v77\nv_cvt_pk_f16_f32 v140,v77,v76\n" \
"v_cvt_pk_f16_f32 v137,v78,-v79\nv_cvt_pk_f16_f32 v141,v79,v78\nv_pk_mul_f32 v[92:93],v[76:77],v[84:85] op_sel:[1,1] op_sel_hi:[0,1] neg_lo:[1,0]\nv_pk_mul_f32 v[94:95],v[78:79],v[84:85] op_sel:[1,1] op_sel_hi:[0,1] neg_lo:[1,0]\n" \
"v_cos_f32 v56,v57\nv_cos_f32 v62,v63\nv_cos_f32_e64 v106,v107\nbuffer_store_dwordx4 v[16:19],v113,s[36:39],0 offen sc1\n" \
"v_pk_fma_f32 v[76:77],v[76:77],v[84:85],v[92:93] op_sel_hi:[1,0,1]\nv_pk_fma_f32 v[78:79],v[78:79],v[84:85],v[94:95] op_sel_hi:[1,0,1]\nv_cvt_pk_f16_f32 v138,v80,-v81\nv_cvt_pk_f16_f32 v142,v81,v80\n" \
"v_cvt_pk_f16_f32 v139,v82,-v83\nv_cvt_pk_f16_f32 v143,v83,v82\nv_pk_mul_f32 v[92:93],v[80:81],v[84:85] op_sel:[1,1] op_sel_hi:[0,1] neg_lo:[1,0]\nv_pk_mul_f32 v[94:95],v[82:83],v[84:85] op_sel:[1,1] op_sel_hi:[0,1] neg_lo:[1,0]\n" \
"v_pk_fma_f32 v[80:81],v[80:81],v[84:85],v[92:93] op_sel_hi:[1,0,1]\nv_pk_fma_f32 v[82:83],v[82:83],v[84:85],v[94:95] op_sel_hi:[1,0,1]\nv_sin_f32 v57,v57\nv_sin_f32 v63,v63\n" \
"v_sin_f32_e64 v107,v107\nv_cvt_pk_f16_f32 v144,v76,-v77\nv_cvt_pk_f16_f32 v148,v77,v76\nv_cvt_pk_f16_f32 v145,v78,-v79\n" \
"v_cvt_pk_f16_f32 v149,v79,v78\nv_pk_mul_f32 v[92:93],v[76:77],v[84:85] op_sel:[1,1] op_sel_hi:[0,1] neg_lo:[1,0]\nv_pk_mul_f32 v[94:95],v[78:79],v[84:85] op_sel:[1,1] op_sel_hi:[0,1] neg_lo:[1,0]\nv_mov_b32 v20,0\n" \
"v_mov_b32 v21,0\nv_mov_b32 v22,0\nv_mov_b32 v23,0\nbuffer_store_dwordx4 v[20:23],v115,s[36:39],0 offen sc1\n" \
"v_pk_fma_f32 v[76:77],v[76:77],v[84:85],v[92:93] op_sel_hi:[1,0,1]\nv_pk_fma_f32 v[78:79],v[78:79],v[84:85],v[94:95] op_sel_hi:[1,0,1]\nv_cvt_pk_f16_f32 v40,v58,-v61\nv_cvt_pk_f16_f32 v44,v59,v60\n" \
"v_cvt_pk_f16_f32 v41,v64,-v67\nv_cvt_pk_f16_f32 v45,v65,v66\nv_cvt_pk_f16_f32 v216,v108,-v109\nv_cvt_pk_f16_f32 v220,v109,v108\n" \
"v_cvt_pk_f16_f32 v217,v110,-v111\nv_cvt_pk_f16_f32 v221,-v111,-v110\nv_cvt_pk_f16_f32 v146,v80,-v81\nv_cvt_pk_f16_f32 v150,v81,v80\n" \
"v_cvt_pk_f16_f32 v147,v82,-v83\nv_cvt_pk_f16_f32 v151,v83,v82\nv_pk_mul_f32 v[92:93],v[80:81],v[84:85] op_sel:[1,1] op_sel_hi:[0,1] neg_lo:[1,0]\nv_pk_mul_f32 v[94:95],v[82:83],v[84:85] op_sel:[1,1] op_sel_hi:[0,1] neg_lo:[1,0]\n" \
"buffer_load_dword v233,s[8:11],s100 offen lds\nv_pk_mul_f32 v[58:59],v[56:57],v[52:53] op_sel_hi:[1,0]\nv_pk_mul_f32 v[60:61],v[56:57],v[54:55] op_sel_hi:[1,0]\nv_pk_mul_f32 v[64:65],v[62:63],v[52:53] op_sel_hi:[1,0]\n" \
"v_pk_mul_f32 v[66:67],v[62:63],v[54:55] op_sel_hi:[1,0]\nv_pk_mul_f32 v[108:109],v[106:107],v[102:103] op_sel_hi:[1,0]\nv_pk_mul_f32 v[110:111],v[106:107],v[104:105] op_sel_hi:[1,0]\nv_pk_fma_f32 v[80:81],v[80:81],v[84:85],v[92:93] op_sel_hi:[1,0,1]\n" \
"v_pk_fma_f32 v[82:83],v[82:83],v[84:85],v[94:95] op_sel_hi:[1,0,1]\nv_lshlrev_b32 v3,4,v119\nv_lshrrev_b32 v15,1,v1\nv_lshl_add_u32 v3,v118,6,v3\n" \
"v_cvt_pk_f16_f32 v42,v58,-v61\nv_cvt_pk_f16_f32 v46,v59,v60\nv_cvt_pk_f16_f32 v43,v64,-v67\nv_cvt_pk_f16_f32 v47,v65,v66\n" \
"v_cvt_pk_f16_f32 v218,v108,-v109\nv_cvt_pk_f16_f32 v222,v109,v108\nv_cvt_pk_f16_f32 v219,v110,-v111\nv_cvt_pk_f16_f32 v223,-v111,-v110\n" \
"v_cvt_pk_f16_f32 v152,v76,-v77\nv_cvt_pk_f16_f32 v156,v77,v76\nv_cvt_pk_f16_f32 v153,v78,-v79\nv_cvt_pk_f16_f32 v157,v79,v78\n" \
"v_cvt_pk_f16_f32 v154,v80,-v81\nv_cvt_pk_f16_f32 v158,v81,v80\nv_cvt_pk_f16_f32 v155,v82,-v83\nv_cvt_pk_f16_f32 v159,v83,v82\n" \
"v_add_u32 v15,s57,v15\nv_add_u32 v8,s57,v8\nv_add_u32 v9,s57,v9\nv_add_u32 v10,s57,v10\n" \
"v_add_u32 v11,s57,v11\nv_lshrrev_b32 v77,5,v0\nv_bfe_u32 v113,v0,1,5\nv_and_b32 v115,1,v0\n" \
"s_lshl_b32 s63,s3,1\nv_mul_u32_u24 v77,1160,v77\nv_and_b32 v76,31,v0\nv_and_b32 v114,6,v113\n" \
"v_add_u32 v115,s63,v115\nv_mad_u32_u24 v78,v76,36,v77\nv_xor_b32 v114,v114,v115\nv_lshlrev_b32 v113,7,v113\n" \
"v_lshl_add_u32 v113,v114,4,v113\ns_add_u32 s64,s54,2916352\ns_add_u32 s65,s64,4096\nv_bfrev_b32 v114,1\n" \
"v_cmp_gt_u32 vcc,72,v0\ns_lshl_b32 s67,s32,2\ns_add_u32 s66,s64,8192\nv_cndmask_b32 v114,v114,v2,vcc\n" \
"s_add_u32 s67,s67,s3\ns_lshl_b32 s67,s67,5\ns_add_u32 s67,s67,s33\ns_lshl_b32 s67,s67,11\n" \
"s_add_u32 s67,s67,5308416\ns_lshl2_add_u32 s69,s3,161664\ns_waitcnt vmcnt(20)\nv_readfirstlane_b32 s28,v126\n" \
"v_readfirstlane_b32 s29,v127\ns_mul_i32 s72,s3,2320\nv_mov_b32 v251,s69\nv_cmp_gt_u32 vcc,32,v0\n" \
"s_add_u32 s28,s28,1\ns_addc_u32 s29,s29,0\ns_lshl_b32 s7,s32,11\nv_cndmask_b32 v74,2.0,0,vcc\n" \
"s_mov_b32 s93,128\ns_mov_b32 s73,8192\ns_mov_b32 s74,12288\ns_mov_b32 s75,17408\n" \
"s_mov_b32 s76,21504\ns_mov_b32 s77,16384\ns_mov_b32 s78,25600\ns_add_u32 s7,s7,40960\n" \
"s_mov_b32 s81,0x3d800000\ns_mov_b32 s42,18874368\ns_mov_b32 s43,0x20000\ns_waitcnt lgkmcnt(0)\n" \
"s_mov_b64 s[40:41],s[44:45]\ns_mov_b64 s[44:45],s[46:47]\ns_mov_b32 s46,2359296\ns_mov_b32 s47,0x20000\n" \
"s_waitcnt vmcnt(19)\nv_lshlrev_b32_e64 v48,16,v224\nv_and_b32 v49,0xffff0000,v224\nv_readlane_b32 s70,v48,0\n" \
"v_readlane_b32 s71,v48,32\nv_and_b32 v51,0xffff0000,v225\nv_mov_b32 v72,s70\nv_mov_b32 v73,s71\n" \
"v_lshlrev_b32 v50,16,v225\nv_and_b32 v53,0xffff0000,v226\nv_permlane32_swap_b32 v72,v73\nv_pk_add_f32 v[48:49],v[48:49],v[72:73] op_sel_hi:[1,0] neg_lo:[0,1] neg_hi:[0,1]\n" \
"v_pk_add_f32 v[50:51],v[50:51],v[72:73] op_sel_hi:[1,0] neg_lo:[0,1] neg_hi:[0,1]\nv_pk_mul_f32 v[68:69],v[48:49],v[48:49]\nv_lshlrev_b32_e64 v52,16,v226\nv_and_b32 v55,0xffff0000,v227\n" \
"v_pk_add_f32 v[52:53],v[52:53],v[72:73] op_sel_hi:[1,0] neg_lo:[0,1] neg_hi:[0,1]\nv_pk_add_f32 v[66:67],v[48:49],v[50:51]\nv_pk_fma_f32 v[68:69],v[50:51],v[50:51],v[68:69]\nv_lshlrev_b32 v54,16,v227\n" \
"s_waitcnt vmcnt(18)\nv_and_b32 v57,0xffff0000,v228\nv_pk_add_f32 v[54:55],v[54:55],v[72:73] op_sel_hi:[1,0] neg_lo:[0,1] neg_hi:[0,1]\nv_pk_add_f32 v[66:67],v[66:67],v[52:53]\n" \
"v_pk_fma_f32 v[68:69],v[52:53],v[52:53],v[68:69]\nv_lshlrev_b32_e64 v56,16,v228\nv_and_b32 v59,0xffff0000,v229\nv_pk_add_f32 v[56:57],v[56:57],v[72:73] op_sel_hi:[1,0] neg_lo:[0,1] neg_hi:[0,1]\n" \
"v_pk_add_f32 v[66:67],v[66:67],v[54:55]\nv_pk_fma_f32 v[68:69],v[54:55],v[54:55],v[68:69]\nv_lshlrev_b32_e64 v58,16,v229\nv_and_b32 v61,0xffff0000,v230\n" \
"v_pk_add_f32 v[58:59],v[58:59],v[72:73] op_sel_hi:[1,0] neg_lo:[0,1] neg_hi:[0,1]\nv_pk_add_f32 v[66:67],v[66:67],v[56:57]\nv_pk_fma_f32 v[68:69],v[56:57],v[56:57],v[68:69]\nv_lshlrev_b32_e64 v60,16,v230\n" \
"v_and_b32 v63,0xffff0000,v231\nv_pk_add_f32 v[60:61],v[60:61],v[72:73] op_sel_hi:[1,0] neg_lo:[0,1] neg_hi:[0,1]\nv_pk_add_f32 v[66:67],v[66:67],v[58:59]\nv_pk_fma_f32 v[68:69],v[58:59],v[58:59],v[68:69]\n" \
"v_lshlrev_b32 v62,16,v231\ns_waitcnt vmcnt(17)\nv_and_b32 v65,0xffff0000,v232\nv_pk_add_f32 v[62:63],v[62:63],v[72:73] op_sel_hi:[1,0] neg_lo:[0,1] neg_hi:[0,1]\n" \
"v_pk_add_f32 v[66:67],v[66:67],v[60:61]\nv_pk_fma_f32 v[68:69],v[60:61],v[60:61],v[68:69]\nv_lshlrev_b32_e64 v64,16,v232\nv_pk_add_f32 v[64:65],v[64:65],v[72:73] op_sel_hi:[1,0] neg_lo:[0,1] neg_hi:[0,1]\n" \
"v_pk_add_f32 v[66:67],v[66:67],v[62:63]\nv_pk_fma_f32 v[68:69],v[62:63],v[62:63],v[68:69]\nv_pk_add_f32 v[66:67],v[66:67],v[64:65]\nv_pk_fma_f32 v[68:69],v[64:65],v[64:65],v[68:69]\n" \
"v_add_f32 v66,v66,v67\nv_add_f32 v68,v68,v69\ns_nop 1\nv_permlane16_swap_b32 v66,v68\n" \
"v_add_f32 v66,v66,v68\ns_nop 1\nv_add_f32_dpp v66,v66,v66 quad_perm:[1,0,3,2] row_mask:0xf bank_mask:0xf bound_ctrl:1\ns_nop 1\n" \
"v_add_f32_dpp v66,v66,v66 quad_perm:[2,3,0,1] row_mask:0xf bank_mask:0xf bound_ctrl:1\ns_nop 1\nv_add_f32_dpp v66,v66,v66 row_half_mirror row_mask:0xf bank_mask:0xf bound_ctrl:1\ns_nop 1\n" \
"v_add_f32_dpp v66,v66,v66 row_mirror row_mask:0xf bank_mask:0xf bound_ctrl:1\nv_mul_f32 v70,0x3ae38e39,v66\nv_mul_f32 v68,0x3ae38e39,v66\ns_nop 1\n" \
"v_permlane16_swap_b32 v70,v68\nv_fma_f32 v68,-v70,v70,v68\nv_max_f32 v68,0,v68\nv_add_f32 v68,0x3727c5ac,v68\n" \
"v_rsq_f32 v66,v68\nv_add_f32 v250,v70,v72\nv_mul_f32 v67,-v70,v66\nv_pk_fma_f32 v[48:49],v[48:49],v[66:67],v[66:67] op_sel:[0,0,1] op_sel_hi:[1,0,1]\n" \
"v_pk_fma_f32 v[50:51],v[50:51],v[66:67],v[66:67] op_sel:[0,0,1] op_sel_hi:[1,0,1]\nv_cvt_pk_f16_f32 v80,v48,v49\nv_cvt_pk_f16_f32 v81,v50,v51\nv_pk_fma_f32 v[52:53],v[52:53],v[66:67],v[66:67] op_sel:[0,0,1] op_sel_hi:[1,0,1]\n" \
"v_pk_fma_f32 v[54:55],v[54:55],v[66:67],v[66:67] op_sel:[0,0,1] op_sel_hi:[1,0,1]\nds_write2_b32 v78,v80,v81 offset0:0 offset1:1\nv_cvt_pk_f16_f32 v82,v52,v53\nv_cvt_pk_f16_f32 v83,v54,v55\n" \
"v_pk_fma_f32 v[56:57],v[56:57],v[66:67],v[66:67] op_sel:[0,0,1] op_sel_hi:[1,0,1]\nv_pk_fma_f32 v[58:59],v[58:59],v[66:67],v[66:67] op_sel:[0,0,1] op_sel_hi:[1,0,1]\nds_write2_b32 v78,v82,v83 offset0:2 offset1:3\nv_cvt_pk_f16_f32 v84,v56,v57\n" \
"v_cvt_pk_f16_f32 v85,v58,v59\nv_pk_fma_f32 v[60:61],v[60:61],v[66:67],v[66:67] op_sel:[0,0,1] op_sel_hi:[1,0,1]\nv_pk_fma_f32 v[62:63],v[62:63],v[66:67],v[66:67] op_sel:[0,0,1] op_sel_hi:[1,0,1]\nds_write2_b32 v78,v84,v85 offset0:4 offset1:5\n" \
"v_cvt_pk_f16_f32 v86,v60,v61\nv_cvt_pk_f16_f32 v87,v62,v63\nv_pk_fma_f32 v[64:65],v[64:65],v[66:67],v[66:67] op_sel:[0,0,1] op_sel_hi:[1,0,1]\nds_write2_b32 v78,v86,v87 offset0:6 offset1:7\n" \
"v_cvt_pk_f16_f32 v88,v64,v65\nds_write_b32 v78,v88 offset:32\ns_waitcnt vmcnt(11)\ns_barrier\n" \
"ds_read_u16 v16,v246\nds_read_u16 v17,v246 offset:5216\nds_read_u16 v18,v246 offset:578\nds_read_u16 v19,v246 offset:5794\n" \
"ds_read_u16 v20,v246 offset:1156\nds_read_u16 v21,v246 offset:6372\nds_read_u16 v22,v246 offset:1734\nds_read_u16 v23,v246 offset:6950\n" \
"s_waitcnt vmcnt(8)\nv_lshrrev_b32 v96,1,v234\nds_read_u16 v24,v247\nv_lshrrev_b32 v97,1,v235\n" \
"v_lshrrev_b32 v98,1,v236\ns_waitcnt lgkmcnt(7)\nv_lshl_or_b32 v16,v17,16,v16\nv_lshrrev_b32 v99,1,v237\n" \
"ds_read_u16 v25,v247 offset:5216\ns_waitcnt vmcnt(7)\nv_lshrrev_b32 v100,1,v238\nv_lshrrev_b32 v101,1,v239\n" \
"v_lshrrev_b32 v102,1,v240\nv_lshrrev_b32 v103,1,v241\nds_read_u16 v26,v247 offset:578\nv_add_u32 v96,s72,v96\n" \
"v_add_u32 v97,s72,v97\ns_waitcnt lgkmcnt(7)\nv_lshl_or_b32 v17,v19,16,v18\nv_add_u32 v98,s72,v98\n" \
"ds_read_u16 v27,v247 offset:5794\nv_add_u32 v99,s72,v99\nv_add_u32 v100,s72,v100\nv_add_u32 v101,s72,v101\n" \
"v_add_u32 v102,s72,v102\nds_read_u16 v28,v247 offset:1156\nv_add_u32 v103,s72,v103\nv_pk_maximum3_f16 v124,v16,v17,v16\n" \
"s_waitcnt lgkmcnt(7)\nv_lshl_or_b32 v18,v21,16,v20\nv_pk_minimum3_f16 v125,v16,v17,v16\nds_read_u16 v29,v247 offset:6372\n" \
"ds_read_u16 v30,v247 offset:1734\ns_waitcnt lgkmcnt(7)\nv_lshl_or_b32 v19,v23,16,v22\nbuffer_store_dwordx4 v[16:19],v2,s[36:39],s64 offen sc1\n" \
"ds_read_u16 v31,v247 offset:6950\nds_read_u16 v104,v248\nv_pk_maximum3_f16 v124,v18,v19,v124\nv_pk_minimum3_f16 v125,v18,v19,v125\n" \
"s_waitcnt lgkmcnt(7)\nv_lshl_or_b32 v24,v25,16,v24\nds_read_u16 v105,v248 offset:5216\nds_read_u16 v106,v248 offset:578\n" \
"s_waitcnt lgkmcnt(7)\nv_lshl_or_b32 v25,v27,16,v26\nv_pk_maximum3_f16 v124,v24,v25,v124\nds_read_u16 v107,v248 offset:5794\n" \
"v_pk_minimum3_f16 v125,v24,v25,v125\nds_read_u16 v108,v248 offset:1156\ns_waitcnt lgkmcnt(7)\nv_lshl_or_b32 v26,v29,16,v28\n" \
"ds_read_u16 v109,v248 offset:6372\nds_read_u16 v110,v248 offset:1734\ns_waitcnt lgkmcnt(7)\nv_lshl_or_b32 v27,v31,16,v30\n" \
"buffer_store_dwordx4 v[24:27],v2,s[36:39],s65 offen sc1\nds_read_u16 v111,v248 offset:6950\nds_read_b128 v[116:119],v113 offset:20224\nv_pk_maximum3_f16 v124,v26,v27,v124\n" \
"v_pk_minimum3_f16 v125,v26,v27,v125\nds_read_b128 v[120:123],v113 offset:24320\ns_waitcnt lgkmcnt(8)\nv_lshl_or_b32 v104,v105,16,v104\n" \
"ds_read_b32 v16,v96\nds_read_b32 v24,v96 offset:1160\nds_read_b32 v17,v97\nds_read_b32 v25,v97 offset:1160\n" \
"s_waitcnt lgkmcnt(10)\nv_lshl_or_b32 v105,v107,16,v106\nds_read_b32 v18,v98\nv_pk_maximum3_f16 v124,v104,v105,v124\n" \
"ds_read_b32 v26,v98 offset:1160\nv_pk_minimum3_f16 v125,v104,v105,v125\nds_read_b32 v19,v99\nds_read_b32 v27,v99 offset:1160\n" \
"ds_read_b32 v20,v100\ns_waitcnt lgkmcnt(13)\nv_lshl_or_b32 v106,v109,16,v108\nds_read_b32 v28,v100 offset:1160\n" \
"ds_read_b32 v21,v101\nds_read_b32 v29,v101 offset:1160\ns_waitcnt lgkmcnt(14)\nv_lshl_or_b32 v107,v111,16,v110\n" \
"buffer_store_dwordx4 v[104:107],v114,s[36:39],s66 offen sc1\ns_waitcnt lgkmcnt(12)\nbuffer_store_dwordx4 v[116:119],v1,s[36:39],s67 offen sc1\nbuffer_store_dwordx4 v[120:123],v1,s[36:39],s67 offen offset:1024 sc1\n" \
"ds_read_b32 v22,v102\nds_read_b32 v30,v102 offset:1160\nds_read_b32 v23,v103\nds_read_b32 v31,v103 offset:1160\n" \
"v_pk_maximum3_f16 v124,v106,v107,v124\nv_and_b32 v124,0x7fff7fff,v124\nv_pk_minimum3_f16 v125,v106,v107,v125\nv_and_b32 v125,0x7fff7fff,v125\n" \
"v_pk_max_u16 v124,v124,v125\nv_lshlrev_b32 v125,16,v124\nv_max_u32 v124,v124,v125\ns_nop 1\n" \
"v_max_u32_dpp v124,v124,v124 quad_perm:[1,0,3,2] row_mask:0xf bank_mask:0xf bound_ctrl:1\ns_nop 1\nv_max_u32_dpp v124,v124,v124 quad_perm:[2,3,0,1] row_mask:0xf bank_mask:0xf bound_ctrl:1\ns_nop 1\n" \
"v_max_u32_dpp v124,v124,v124 row_half_mirror row_mask:0xf bank_mask:0xf bound_ctrl:1\ns_nop 1\nv_max_u32_dpp v124,v124,v124 row_mirror row_mask:0xf bank_mask:0xf bound_ctrl:1\ns_nop 0\n" \
"v_readlane_b32 s68,v124,0\nv_readlane_b32 s31,v124,16\nv_readlane_b32 s96,v124,32\nv_readlane_b32 s97,v124,48\n" \
"s_max_u32 s31,s31,s96\ns_max_u32 s68,s68,s97\ns_max_u32 s68,s68,s31\nv_mov_b32 v124,s68\n" \
"ds_write_b32 v251,v124 offset:32\nv_mov_b32 v114,161664\nv_and_b32 v80,15,v0\nv_bfe_u32 v81,v0,4,2\n" \
"v_cvt_f32_u32 v80,v80\nv_cvt_f32_u32 v82,s3\nv_fma_f32 v80,v80,4.0,v82\nv_cvt_f32_u32 v81,v81\n" \
"buffer_load_dwordx4 v[48:51],v2,s[36:39],s73 offen sc1\nv_mul_f32 v82,0xbc800000,v81\nv_cos_f32 v92,v82\nv_sin_f32 v93,v82\n" \
"v_mul_f32 v80,0xbc800000,v80\nv_mul_f32 v81,4.0,v81\nbuffer_load_dwordx4 v[60:63],v2,s[36:39],s75 offen sc1\ns_mov_b32 s64,0x3f7fb10f\n" \
"s_mov_b32 s65,0xbd48fb30\nv_mul_f32 v81,v81,v80\nv_pk_mul_f32 v[100:101],v[92:93],s[64:65] op_sel:[1,1] op_sel_hi:[0,1] neg_lo:[1,0]\nv_fmamk_f32 v87,v80,0x40000000,v81\n" \
"v_pk_fma_f32 v[94:95],v[92:93],s[64:65],v[100:101] op_sel_hi:[1,0,1]\nv_cos_f32_e64 v86,v87\ns_mov_b32 s64,0x3f7fec43\ns_mov_b32 s65,0xbcc90ab0\n" \
"v_fmamk_f32 v89,v80,0x3f800000,v81\nv_fmamk_f32 v91,v80,0x40400000,v81\nv_pk_mul_f32 v[100:101],v[92:93],s[64:65] op_sel:[1,1] op_sel_hi:[0,1] neg_lo:[1,0]\nbuffer_load_dwordx4 v[52:55],v2,s[36:39],s74 offen sc1\n" \
"v_cos_f32 v88,v89\nv_cos_f32 v90,v91\nv_pk_fma_f32 v[96:97],v[92:93],s[64:65],v[100:101] op_sel_hi:[1,0,1]\nv_cos_f32 v84,v81\n" \
"v_sin_f32 v85,v81\nv_sin_f32 v87,v87\ns_mov_b32 s64,0x3f7f4e6d\ns_mov_b32 s65,0xbd96a905\n" \
"v_pk_mul_f32 v[100:101],v[92:93],s[64:65] op_sel:[1,1] op_sel_hi:[0,1] neg_lo:[1,0]\nv_sin_f32 v89,v89\nv_sin_f32_e64 v91,v91\nv_pk_fma_f32 v[98:99],v[92:93],s[64:65],v[100:101] op_sel_hi:[1,0,1]\n" \
"buffer_load_dwordx4 v[64:67],v2,s[36:39],s76 offen sc1\nv_cvt_pk_f16_f32 v160,v84,-v85\nv_cvt_pk_f16_f32 v164,v85,v84\nv_cvt_pk_f16_f32 v161,v86,-v87\n" \
"v_cvt_pk_f16_f32 v165,v87,v86\nv_pk_mul_f32 v[100:101],v[84:85],v[92:93] op_sel:[1,1] op_sel_hi:[0,1] neg_lo:[1,0]\nv_pk_mul_f32 v[102:103],v[86:87],v[94:95] op_sel:[1,1] op_sel_hi:[0,1] neg_lo:[1,0]\nv_pk_fma_f32 v[84:85],v[84:85],v[92:93],v[100:101] op_sel_hi:[1,0,1]\n" \
"v_pk_fma_f32 v[86:87],v[86:87],v[94:95],v[102:103] op_sel_hi:[1,0,1]\nv_cvt_pk_f16_f32 v162,v88,-v89\nv_cvt_pk_f16_f32 v166,v89,v88\nv_cvt_pk_f16_f32 v163,v90,-v91\n" \
"v_cvt_pk_f16_f32 v167,v91,v90\nv_pk_mul_f32 v[100:101],v[88:89],v[96:97] op_sel:[1,1] op_sel_hi:[0,1] neg_lo:[1,0]\nv_pk_mul_f32 v[102:103],v[90:91],v[98:99] op_sel:[1,1] op_sel_hi:[0,1] neg_lo:[1,0]\nv_pk_fma_f32 v[88:89],v[88:89],v[96:97],v[100:101] op_sel_hi:[1,0,1]\n" \
"v_pk_fma_f32 v[90:91],v[90:91],v[98:99],v[102:103] op_sel_hi:[1,0,1]\nbuffer_load_dwordx4 v[56:59],v1,s[36:39],s77 offen sc1\nv_cvt_pk_f16_f32 v168,v84,-v85\nv_cvt_pk_f16_f32 v172,v85,v84\n" \
"v_cvt_pk_f16_f32 v169,v86,-v87\nv_cvt_pk_f16_f32 v173,v87,v86\nv_pk_mul_f32 v[100:101],v[84:85],v[92:93] op_sel:[1,1] op_sel_hi:[0,1] neg_lo:[1,0]\nv_pk_mul_f32 v[102:103],v[86:87],v[94:95] op_sel:[1,1] op_sel_hi:[0,1] neg_lo:[1,0]\n" \
"v_pk_fma_f32 v[84:85],v[84:85],v[92:93],v[100:101] op_sel_hi:[1,0,1]\nv_pk_fma_f32 v[86:87],v[86:87],v[94:95],v[102:103] op_sel_hi:[1,0,1]\nv_cvt_pk_f16_f32 v170,v88,-v89\nv_cvt_pk_f16_f32 v174,v89,v88\n" \
"v_cvt_pk_f16_f32 v171,v90,-v91\nv_cvt_pk_f16_f32 v175,v91,v90\nv_pk_mul_f32 v[100:101],v[88:89],v[96:97] op_sel:[1,1] op_sel_hi:[0,1] neg_lo:[1,0]\nv_pk_mul_f32 v[102:103],v[90:91],v[98:99] op_sel:[1,1] op_sel_hi:[0,1] neg_lo:[1,0]\n" \
"s_waitcnt vmcnt(5) lgkmcnt(0)\ns_barrier\nds_read_b128 v[116:119],v114 offset:32\ns_lshl3_add_u32 s80,s2,4294615040\n" \
"v_pk_fma_f32 v[88:89],v[88:89],v[96:97],v[100:101] op_sel_hi:[1,0,1]\nv_pk_fma_f32 v[90:91],v[90:91],v[98:99],v[102:103] op_sel_hi:[1,0,1]\nbuffer_load_dwordx4 v[68:71],v1,s[36:39],s78 offen sc1\nv_cvt_pk_f16_f32 v176,v84,-v85\n" \
"v_cvt_pk_f16_f32 v180,v85,v84\nv_cvt_pk_f16_f32 v177,v86,-v87\nv_cvt_pk_f16_f32 v181,v87,v86\nv_pk_mul_f32 v[100:101],v[84:85],v[92:93] op_sel:[1,1] op_sel_hi:[0,1] neg_lo:[1,0]\n" \
"v_pk_mul_f32 v[102:103],v[86:87],v[94:95] op_sel:[1,1] op_sel_hi:[0,1] neg_lo:[1,0]\nv_and_b32 v113,15,v0\ns_mov_b32 s79,0xffffff\nv_pk_fma_f32 v[84:85],v[84:85],v[92:93],v[100:101] op_sel_hi:[1,0,1]\n" \
"v_pk_fma_f32 v[86:87],v[86:87],v[94:95],v[102:103] op_sel_hi:[1,0,1]\nv_bfrev_b32 v115,1\ns_add_u32 s70,s35,s88\nv_cvt_pk_f16_f32 v178,v88,-v89\n" \
"v_cvt_pk_f16_f32 v182,v89,v88\nv_cvt_pk_f16_f32 v179,v90,-v91\nv_cvt_pk_f16_f32 v183,v91,v90\nv_pk_mul_f32 v[100:101],v[88:89],v[96:97] op_sel:[1,1] op_sel_hi:[0,1] neg_lo:[1,0]\n" \
"s_waitcnt lgkmcnt(0)\nv_max3_u32 v116,v116,v117,v118\nv_max_u32 v116,v116,v119\nv_and_b32 v119,0xf8,v0\n" \
"v_cmp_ne_u32 vcc,192,v119\nv_lshlrev_b32 v118,11,v0\nv_add_u32 v118,s80,v118\nv_bfrev_b32 v119,1\n" \
"v_cndmask_b32 v118,v118,v119,vcc\nv_lshlrev_b32 v116,1,v116\nv_mov_b32 v117,s29\nv_cmp_eq_u32 vcc,0,v113\n" \
"v_bfi_b32 v117,s79,v117,v116\nv_pk_mul_f32 v[102:103],v[90:91],v[98:99] op_sel:[1,1] op_sel_hi:[0,1] neg_lo:[1,0]\nv_cndmask_b32 v115,v115,v3,vcc\ns_add_u32 s72,s70,8192\n" \
"v_mov_b32 v116,s28\nv_pk_fma_f32 v[88:89],v[88:89],v[96:97],v[100:101] op_sel_hi:[1,0,1]\nv_pk_fma_f32 v[90:91],v[90:91],v[98:99],v[102:103] op_sel_hi:[1,0,1]\nv_cmp_gt_u32 vcc,64,v0\n" \
"s_cmp_lt_u32 s3,2\ns_cselect_b32 s72,s72,2147483648\nbuffer_store_dwordx2 v[116:117],v118,s[36:39],0 offen sc1\nv_add_u32 v112,28416,v1\n" \
"v_cvt_pk_f16_f32 v184,v84,-v85\nv_cvt_pk_f16_f32 v188,v85,v84\nv_cvt_pk_f16_f32 v185,v86,-v87\nv_cvt_pk_f16_f32 v189,v87,v86\n" \
"v_cvt_pk_f16_f32 v186,v88,-v89\nv_cvt_pk_f16_f32 v190,v89,v88\nv_cvt_pk_f16_f32 v187,v90,-v91\nv_cvt_pk_f16_f32 v191,v91,v90\n" \
"v_cndmask_b32_e64 v115,v115,v3,vcc\ns_add_u32 s71,s70,4096\nds_read_b128 v[192:195],v112\nds_read_b128 v[196:199],v112 offset:1024\n" \
"ds_read_b128 v[200:203],v112 offset:2048\nds_read_b128 v[204:207],v112 offset:3072\nds_read_b128 v[208:211],v112 offset:4096\ns_lshl1_add_u32 s95,s33,s3\n" \
"s_add_u32 s95,s95,74\ns_cmp_lt_u32 s3,2\ns_cselect_b32 s95,s95,256\nds_read_b128 v[212:215],v112 offset:5120\n" \
"s_and_b32 s100,s95,31\ns_lshl_b32 s100,s100,3\nds_read_b128 v[32:35],v112 offset:6144\ns_cmp_lt_u32 s95,256\n" \
"s_cselect_b32 s95,s95,0x40000\nds_read_b128 v[36:39],v112 offset:7168\ns_lshr_b32 s95,s95,5\ns_add_u32 s95,s95,s100\n" \
"s_lshl_b32 s95,s95,6\ns_add_u32 s95,s95,s32\ns_mul_i32 s95,s95,1152\ns_mov_b32 m0,162176\n" \
"s_lshl_b32 s101,s33,7\nbuffer_load_dword v233,s[8:11],s95 offen lds\ns_sub_u32 s101,s101,1280\nv_add_u32 v111,s101,v0\n" \
"v_mul_u32_u24 v110,52429,v111\nv_lshrrev_b32 v110,18,v110\nv_lshlrev_b32 v111,7,v111\ns_mov_b32 s101,6272\n" \
"v_mad_u32_u24 v111,v110,s101,v111\ns_mul_i32 s31,s32,576\ns_add_u32 s31,s31,2304\ns_andn2_b32 s31,s31,127\n" \
"s_cmp_gt_u32 s3,1\ns_cselect_b32 s31,s31,2147483648\ns_mov_b32 s26,3981312\ns_mov_b32 s27,0x20000\n" \
"s_mov_b32 m0,162176\ns_nop 0\nbuffer_load_dword v111,s[24:27],s31 offen lds\ns_waitcnt vmcnt(8)\n" \
"v_cmp_ne_u64 vcc,s[28:29],v[48:49]\ns_waitcnt vmcnt(6)\nv_cmp_ne_u64 s[84:85],s[28:29],v[52:53]\ns_waitcnt vmcnt(4)\n" \
"v_cmp_ne_u64 s[82:83],s[28:29],v[56:57]\ns_or_b64 vcc,vcc,s[84:85]\nv_cmp_ne_u64 s[84:85],s[28:29],v[60:61]\ns_or_b64 vcc,vcc,s[82:83]\n" \
"v_cmp_ne_u64 s[82:83],s[28:29],v[64:65]\ns_or_b64 vcc,vcc,s[84:85]\ns_waitcnt vmcnt(3)\nv_cmp_ne_u64 s[84:85],s[28:29],v[68:69]\n" \
"s_or_b64 vcc,vcc,s[82:83]\ns_or_b64 vcc,vcc,s[84:85]\ns_cbranch_vccnz .LsmC_%=\n.LbmC_%=:\n" \
"v_pk_add_f32 v[50:51],v[50:51],v[62:63]\nv_pk_add_f32 v[54:55],v[54:55],v[66:67]\nv_pk_mul_f32 v[62:63],v[50:51],2.0 op_sel_hi:[1,0]\nv_pk_add_f32 v[58:59],v[58:59],v[70:71]\n" \
"v_max3_f32 v112,|v62|,|v63|,0\nv_pk_fma_f32 v[66:67],v[54:55],2.0,v[74:75] op_sel_hi:[1,0,0]\nv_max3_f32 v112,|v66|,|v67|,v112\nv_pk_fma_f32 v[70:71],v[58:59],2.0,2.0 op_sel_hi:[1,0,0]\n" \
"v_max3_f32 v112,|v70|,|v71|,v112\nv_cmp_lt_f32 s[82:83],s81,v112\nv_cmp_ngt_f32 vcc,0x41000000,v112\ns_cmp_lg_u64 s[82:83],0\n" \
"s_cselect_b32 s82,2,0\nv_cvt_pk_f16_f32 v62,v62,v63\ns_cmp_lg_u64 vcc,0\ns_addc_u32 s82,s82,0\n" \
"ds_write_b32 v252,v62 offset:62848\nv_cvt_pk_f16_f32 v66,v66,v67\nds_write_b32 v253,v66 offset:62848\nv_cvt_pk_f16_f32 v70,v70,v71\n" \
"v_mov_b32 v113,s82\nds_write_b32 v254,v70 offset:62848\nds_write_b32 v251,v113\ns_waitcnt lgkmcnt(0)\n" \
"s_barrier\nds_read_b128 v[124:127],v114\nds_read_b64 v[96:97],v234 offset:62848\nds_read_b64 v[98:99],v235 offset:62848\n" \
"ds_read_b64 v[100:101],v236 offset:62848\nds_read_b64 v[102:103],v237 offset:62848\nds_read_b64 v[104:105],v238 offset:62848\nds_read_b64 v[106:107],v239 offset:62848\n" \
"ds_read_b64 v[108:109],v240 offset:62848\nds_read_b64 v[110:111],v241 offset:62848\ns_waitcnt lgkmcnt(7)\nv_or3_b32 v124,v124,v125,v126\n" \
"v_or_b32 v124,v124,v127\nv_cmp_ne_u32 vcc,2,v124\ns_nop 0\ns_cbranch_vccnz .LsxC_%=\n" \
".LbxC_%=:\nv_pk_fma_f16 v80,v16,v96,v97\ns_waitcnt lgkmcnt(4)\nv_pk_fma_f16 v81,v17,v98,v99\n" \
"v_pk_fma_f16 v82,v18,v100,v101\nv_pk_fma_f16 v83,v19,v102,v103\nv_pk_fma_f16 v88,v24,v96,v97\ns_waitcnt lgkmcnt(2)\n" \
"v_pk_fma_f16 v84,v20,v104,v105\nv_pk_fma_f16 v85,v21,v106,v107\nv_pk_fma_f16 v89,v25,v98,v99\ns_waitcnt lgkmcnt(0)\n" \
"v_pk_fma_f16 v86,v22,v108,v109\nv_pk_fma_f16 v90,v26,v100,v101\nv_pk_fma_f16 v87,v23,v110,v111\nv_pk_fma_f16 v91,v27,v102,v103\n" \
"v_pk_fma_f16 v92,v28,v104,v105\nv_pk_fma_f16 v93,v29,v106,v107\nv_pk_fma_f16 v94,v30,v108,v109\ns_add_u32 s96,s92,0\n" \
"s_add_u32 m0,s57,0\ns_add_u32 s97,s92,9437184\nbuffer_load_dwordx4 v4,s[8:11],s96 offen lds\nv_pk_fma_f16 v95,v31,v110,v111\n" \
"v_mfma_f32_16x16x32_f16 v[48:51],v[80:83],v[192:195],0\ns_add_u32 s95,s92,73728\ns_add_u32 s100,s92,9510912\nv_mfma_f32_16x16x32_f16 v[52:55],v[80:83],v[196:199],0\n" \
"s_add_u32 s101,s92,147456\ns_mul_i32 s76,s32,18\nbuffer_load_dwordx4 v5,s[8:11],s96 offen offset:1024 lds\nv_mfma_f32_16x16x32_f16 v[56:59],v[84:87],v[32:35],0\n" \
"s_add_u32 s76,s76,s33\ns_sub_u32 s76,s76,14\nv_mfma_f32_16x16x32_f16 v[60:63],v[84:87],v[36:39],0\ns_cmp_lt_u32 s33,14\n" \
"s_cselect_b32 s76,144,s76\nv_mfma_f32_16x16x32_f16 v[64:67],v[88:91],v[192:195],0\nbuffer_load_dwordx4 v6,s[8:11],s96 offen offset:2048 lds\nv_mfma_f32_16x16x32_f16 v[68:71],v[88:91],v[196:199],0\n" \
"v_pk_add_f32 v[56:57],v[48:49],v[56:57] neg_lo:[0,1] neg_hi:[0,1]\nv_sub_f32 v58,v50,v58\nv_mfma_f32_16x16x32_f16 v[72:75],v[92:95],v[32:35],0\nv_pk_add_f32 v[60:61],v[52:53],v[60:61] neg_lo:[0,1] neg_hi:[0,1]\n" \
"v_sub_f32 v62,v54,v62\nv_mfma_f32_16x16x32_f16 v[76:79],v[92:95],v[36:39],0\nbuffer_load_dwordx4 v7,s[8:11],s96 offen offset:3072 lds\nv_pk_fma_f32 v[48:49],v[48:49],2.0,v[56:57] op_sel_hi:[1,0,1] neg_lo:[0,0,1] neg_hi:[0,0,1]\n" \
"v_fma_f32 v50,v50,2.0,-v58\nv_pk_fma_f32 v[52:53],v[52:53],2.0,v[60:61] op_sel_hi:[1,0,1] neg_lo:[0,0,1] neg_hi:[0,0,1]\nv_fma_f32 v54,v54,2.0,-v62\nv_pk_add_f32 v[72:73],v[64:65],v[72:73] neg_lo:[0,1] neg_hi:[0,1]\n" \
"v_sub_f32 v74,v66,v74\nv_pk_fma_f32 v[64:65],v[64:65],2.0,v[72:73] op_sel_hi:[1,0,1] neg_lo:[0,0,1] neg_hi:[0,0,1]\nv_pk_add_f32 v[76:77],v[68:69],v[76:77] neg_lo:[0,1] neg_hi:[0,1]\ns_add_u32 m0,s57,4096\n" \
"v_sub_f32 v78,v70,v78\nbuffer_load_dwordx4 v4,s[8:11],s97 offen lds\nv_fma_f32 v66,v66,2.0,-v74\nv_pk_fma_f32 v[68:69],v[68:69],2.0,v[76:77] op_sel_hi:[1,0,1] neg_lo:[0,0,1] neg_hi:[0,0,1]\n" \
"v_fma_f32 v70,v70,2.0,-v78\nv_cvt_pk_f16_f32 v16,v48,v52\nv_cvt_pk_f16_f32 v17,v49,v53\nv_cvt_pk_f16_f32 v18,v50,v54\n" \
"v_cvt_pk_f16_f32 v24,v64,v68\nbuffer_load_dwordx4 v5,s[8:11],s97 offen offset:1024 lds\nv_cvt_pk_f16_f32 v25,v65,v69\nv_cvt_pk_f16_f32 v26,v66,v70\n" \
"v_mfma_f32_16x16x32_f16 v[48:51],v[200:203],v[16:19],0\nv_cvt_pk_f16_f32 v20,v56,v60\nv_cvt_pk_f16_f32 v21,v57,v61\nv_mfma_f32_16x16x32_f16 v[52:55],v[204:207],v[16:19],0\n" \
"buffer_load_dwordx4 v6,s[8:11],s97 offen offset:2048 lds\nv_mfma_f32_16x16x32_f16 v[64:67],v[200:203],v[24:27],0\nv_cvt_pk_f16_f32 v22,v58,v62\nv_cvt_pk_f16_f32 v28,v72,v76\n" \
"v_mfma_f32_16x16x32_f16 v[68:71],v[204:207],v[24:27],0\nv_cvt_pk_f16_f32 v29,v73,v77\nv_cvt_pk_f16_f32 v30,v74,v78\nbuffer_load_dwordx4 v7,s[8:11],s97 offen offset:3072 lds\n" \
"v_mfma_f32_16x16x32_f16 v[56:59],v[200:203],v[20:23],0\nv_cvt_pk_f16_f32 v80,v48,v52\nv_cvt_pk_f16_f32 v82,v49,v53\nv_mfma_f32_16x16x32_f16 v[60:63],v[204:207],v[20:23],0\n" \
"v_cvt_pk_f16_f32 v84,v50,v54\nv_cvt_pk_f16_f32 v81,v64,v68\ns_add_u32 m0,s57,8192\nv_mfma_f32_16x16x32_f16 v[72:75],v[200:203],v[28:31],0\n" \
"buffer_load_dwordx4 v4,s[8:11],s95 offen lds\nv_mfma_f32_16x16x32_f16 v[76:79],v[204:207],v[28:31],0\nds_write_b64 v255,v[80:81]\nv_cvt_pk_f16_f32 v83,v65,v69\n" \
"ds_write_b64 v255,v[82:83] offset:512\nv_cvt_pk_f16_f32 v85,v66,v70\nbuffer_load_dwordx4 v5,s[8:11],s95 offen offset:1024 lds\nds_write_b64 v255,v[84:85] offset:1024\n" \
"v_cvt_pk_f16_f32 v86,v56,v60\nv_cvt_pk_f16_f32 v88,v57,v61\nv_cvt_pk_f16_f32 v87,v72,v76\nds_write_b64 v255,v[86:87] offset:256\n" \
"buffer_load_dwordx4 v6,s[8:11],s95 offen offset:2048 lds\nv_cvt_pk_f16_f32 v89,v73,v77\nds_write_b64 v255,v[88:89] offset:768\nv_cvt_pk_f16_f32 v90,v58,v62\n" \
"v_cvt_pk_f16_f32 v91,v74,v78\nds_write_b64 v255,v[90:91] offset:1280\nbuffer_load_dwordx4 v7,s[8:11],s95 offen offset:3072 lds\ns_add_u32 m0,s57,12288\n" \
"v_lshrrev_b32 v124,2,v0\nv_mul_u32_u24 v124,9344,v124\nv_and_b32 v125,48,v2\nv_add_u32 v124,v124,v125\n" \
"v_xor_b32 v125,32,v124\ns_lshl_b32 s77,s76,6\ns_add_u32 s66,s77,2916352\nbuffer_load_dwordx4 v4,s[8:11],s100 offen lds\n" \
"s_waitcnt lgkmcnt(0)\ns_barrier\nds_read_b128 v[16:19],v243\nds_read_b128 v[20:23],v244\n" \
"ds_read_b128 v[24:27],v245\ns_add_u32 s67,s77,3514368\ns_add_u32 s68,s77,4112384\ns_add_u32 s69,s77,4710400\n" \
"buffer_load_dwordx4 v5,s[8:11],s100 offen offset:1024 lds\nbuffer_load_dwordx4 v6,s[8:11],s100 offen offset:2048 lds\ns_waitcnt lgkmcnt(2)\nv_mfma_f32_16x16x32_f16 v[48:51],v[40:43],v[16:19],0\n" \
"v_mfma_f32_16x16x32_f16 v[52:55],v[44:47],v[16:19],0\nbuffer_load_dwordx4 v7,s[8:11],s100 offen offset:3072 lds\nbuffer_load_dwordx4 v[104:107],v1,s[36:39],s7 offen sc1\nbuffer_load_dwordx4 v[108:111],v1,s[36:39],s7 offen offset:1024 sc1\n" \
"s_add_u32 m0,s57,16384\ns_waitcnt lgkmcnt(0)\nv_mfma_f32_16x16x32_f16 v[56:59],v[40:43],v[20:23],0\nv_mfma_f32_16x16x32_f16 v[60:63],v[44:47],v[20:23],0\n" \
"v_mfma_f32_16x16x32_f16 v[64:67],v[40:43],v[24:27],0\nv_cvt_pk_f16_f32 v80,v48,v52\nv_mfma_f32_16x16x32_f16 v[68:71],v[44:47],v[24:27],0\nbuffer_load_dwordx4 v4,s[8:11],s101 offen lds\n" \
"v_cvt_pk_f16_f32 v81,v49,v53\nv_cvt_pk_f16_f32 v82,v50,v54\nv_cvt_pk_f16_f32 v83,v51,v55\nv_and_b32 v80,-2,v80\n" \
"v_cvt_pk_f16_f32 v84,v56,v60\nv_cvt_pk_f16_f32 v85,v57,v61\nv_cvt_pk_f16_f32 v86,v58,v62\nv_cvt_pk_f16_f32 v88,v64,v68\n" \
"buffer_load_dwordx4 v5,s[8:11],s101 offen offset:1024 lds\nv_cvt_pk_f16_f32 v87,v59,v63\nv_cvt_pk_f16_f32 v89,v65,v69\nv_cvt_pk_f16_f32 v90,v66,v70\n" \
"v_cvt_pk_f16_f32 v91,v67,v71\nv_and_b32 v84,-2,v84\nv_and_b32 v88,-2,v88\nbuffer_load_dwordx4 v6,s[8:11],s101 offen offset:2048 lds\n" \
"s_waitcnt vmcnt(4)\nv_xor_b32 v116,s29,v105\nv_xor_b32 v117,s29,v107\ns_waitcnt vmcnt(3)\n" \
"v_xor_b32 v118,s29,v109\nv_or3_b32 v116,v116,v117,v118\nbuffer_load_dwordx4 v7,s[8:11],s101 offen offset:3072 lds\nv_xor_b32 v119,s29,v111\n" \
"v_xor_b32 v117,s28,v104\nv_or_b32 v116,v116,v119\nv_lshl_or_b32 v116,v116,8,v117\nv_xor_b32 v118,s28,v106\n" \
"v_xor_b32 v119,s28,v108\nv_or3_b32 v116,v116,v118,v119\nv_xor_b32 v117,s28,v110\nv_or_b32 v116,v116,v117\n" \
"v_cmp_ne_u32 vcc,0,v116\ns_nop 0\ns_cbranch_vccnz .Lsw_%=\nbuffer_store_dwordx4 v[80:83],v3,s[36:39],s70 offen sc1\n" \
"buffer_store_dwordx4 v[84:87],v3,s[36:39],s71 offen sc1\nbuffer_store_dwordx4 v[88:91],v115,s[36:39],s72 offen sc1\ns_add_u32 m0,s88,16384\ns_nop 0\n" \
"buffer_load_dwordx4 v124,s[36:39],s66 offen sc1 lds\ns_add_u32 m0,s88,20480\ns_nop 0\nbuffer_load_dwordx4 v125,s[36:39],s67 offen sc1 lds\n" \
"s_add_u32 m0,s88,24576\ns_nop 0\nbuffer_load_dwordx4 v124,s[36:39],s68 offen sc1 lds\ns_add_u32 m0,s88,28672\n" \
"s_nop 0\nbuffer_load_dwordx4 v125,s[36:39],s69 offen sc1 lds\n.Lbw_%=:\ns_cmp_lt_u32 s3,2\n" \
"s_cselect_b32 s18,s72,s70\ns_mov_b32 s12,s88\ns_mov_b32 s13,s89\ns_mov_b32 s14,s87\n" \
"s_mov_b32 s16,s70\ns_mov_b32 s17,s71\nv_bfe_u32 v86,v0,4,2\nv_and_b32 v88,1,v86\n" \
"v_bfe_u32 v85,v0,3,1\nv_and_b32 v84,7,v0\nv_lshlrev_b32 v88,3,v88\nv_lshl_add_u32 v87,v86,6,v85\n" \
"s_lshl_b32 s84,s3,2\nv_xor_b32 v89,v84,v88\nv_add_u32 v87,s84,v87\nv_lshlrev_b32_e64 v89,2,v89\n" \
"v_lshl_add_u32 v80,v87,6,v89\nv_and_b32 v87,0xf0,v1\nv_add_u32 v80,16384,v80\nv_lshl_add_u32 v246,v86,12,v87\n" \
"v_sub_u32 v82,46076,v80\nv_lshl_add_u32 v87,v84,1,v85\nv_xor_b32 v82,32,v82\nv_lshlrev_b32 v87,4,v87\n" \
"v_add_u32 v81,128,v80\nv_subrev_u32 v83,128,v82\nv_lshl_add_u32 v87,v85,10,v87\nv_lshl_add_u32 v87,v86,8,v87\n" \
"s_lshl_b32 s86,s3,12\nv_add_u32 v252,s86,v87\nv_lshlrev_b32 v87,3,v85\nv_lshl_add_u32 v87,v84,4,v87\n" \
"s_lshr_b32 s84,s76,5\nv_lshl_add_u32 v87,v86,12,v87\ns_lshl_b32 s85,s3,8\ns_add_u32 s84,s84,1\n" \
"v_add_u32 v246,s85,v246\nv_add_u32 v248,s85,v87\ns_cmp_lt_u32 s76,128\ns_cselect_b32 s84,s84,0\n" \
"v_max3_u32 v126,v105,v107,v109\ns_and_b32 s85,s76,31\nv_max_u32 v126,v126,v111\ns_lshl_b32 s84,s84,6\n" \
"s_mul_i32 s85,s85,225\nv_max_u32_dpp v126,v126,v126 quad_perm:[1,0,3,2] row_mask:0xf bank_mask:0xf bound_ctrl:1\ns_add_u32 s84,s84,s85\ns_mul_i32 s85,s84,58255\n" \
"v_max_u32_dpp v126,v126,v126 quad_perm:[2,3,0,1] row_mask:0xf bank_mask:0xf bound_ctrl:1\ns_lshr_b32 s85,s85,24\ns_mul_i32 s85,s85,288\nv_max_u32_dpp v126,v126,v126 row_half_mirror row_mask:0xf bank_mask:0xf bound_ctrl:1\n" \
"s_sub_u32 s84,s84,s85\nv_cvt_f32_u32 v92,s84\nv_max_u32_dpp v126,v126,v126 row_mirror row_mask:0xf bank_mask:0xf bound_ctrl:1\nv_mul_f32 v92,0x3ae38e39,v92\n" \
"v_readlane_b32 s94,v126,0\nv_readlane_b32 s31,v126,16\nv_readlane_b32 s96,v126,32\nv_readlane_b32 s97,v126,48\n" \
"s_max_u32 s31,s31,s96\ns_max_u32 s94,s94,s97\ns_max_u32 s94,s94,s31\nv_cos_f32 v90,v92\n" \
"s_lshr_b32 s94,s94,24\nv_bfe_u32 v87,v0,1,3\nv_and_b32 v88,1,v0\ns_max_u32 s94,s94,64\n" \
"v_lshl_add_u32 v87,v88,3,v87\ns_min_u32 s94,s94,250\nv_lshl_add_u32 v87,v86,4,v87\ns_sub_u32 s85,251,s94\n" \
"v_sin_f32 v92,v92\nv_lshlrev_b32 v87,4,v87\ns_cmp_eq_u32 s76,128\ns_subb_u32 s85,s85,0\n" \
"v_bfe_u32 v120,v0,4,2\nv_add_u32 v254,s86,v87\ns_mul_i32 s80,s3,37376\nv_mul_u32_u24 v120,598016,v120\n" \
"v_and_b32 v121,15,v0\nv_and_b32 v87,15,v0\ns_lshl_b32 s85,s85,23\ns_add_u32 s80,s80,s77\n" \
"v_lshl_add_u32 v120,v121,2,v120\nv_mul_u32_u24 v87,149504,v87\nv_fma_f32 v94,v92,s85,s85\ns_mov_b32 s79,0x34003400\n" \
"s_add_u32 s80,s80,524288\nv_add_u32 v121,149504,v120\nv_add_u32 v122,299008,v120\nv_add_u32 v123,448512,v120\n" \
"v_lshl_add_u32 v255,v86,4,v87\nv_pk_mul_f16 v112,v192,s79\nv_pk_mul_f16 v113,v193,s79\nv_pk_mul_f16 v114,v194,s79\n" \
"v_pk_mul_f16 v115,v195,s79\nv_pk_mul_f16 v116,v196,s79\nv_pk_mul_f16 v117,v197,s79\nv_pk_mul_f16 v118,v198,s79\n" \
"v_pk_mul_f16 v119,v199,s79\nv_fma_f32 v92,-v92,s85,s85\ns_add_u32 s81,s80,9344\ns_add_u32 s82,s80,18688\n" \
"s_add_u32 s83,s80,28032\nv_xor_b32 v247,16,v246\nv_xor_b32 v253,16,v252\nv_mul_f32 v90,s85,v90\n" \
"ds_read_b64_tr_b16 a[0:1],v8\nds_read_b64_tr_b16 a[4:5],v9\nds_read_b64_tr_b16 a[8:9],v10\nds_read_b64_tr_b16 a[12:13],v11\n" \
"ds_read_b64_tr_b16 a[16:17],v8 offset:2048\nds_read_b64_tr_b16 a[20:21],v9 offset:2048\nds_read_b64_tr_b16 a[24:25],v10 offset:2048\nds_read_b64_tr_b16 a[28:29],v11 offset:2048\n" \
"ds_read_b64_tr_b16 a[2:3],v8 offset:4096\nds_read_b64_tr_b16 a[6:7],v9 offset:4096\nds_read_b64_tr_b16 a[10:11],v10 offset:4096\nds_read_b64_tr_b16 a[14:15],v11 offset:4096\n" \
"ds_read_b64_tr_b16 a[18:19],v8 offset:6144\nds_read_b64_tr_b16 a[22:23],v9 offset:6144\nds_read_b64_tr_b16 a[26:27],v10 offset:6144\nds_read_b64_tr_b16 a[30:31],v11 offset:6144\n" \
"ds_read_b64_tr_b16 a[32:33],v8 offset:8192\nds_read_b64_tr_b16 a[36:37],v9 offset:8192\nds_read_b64_tr_b16 a[40:41],v10 offset:8192\nds_read_b64_tr_b16 a[44:45],v11 offset:8192\n" \
"ds_read_b64_tr_b16 a[48:49],v8 offset:10240\nds_read_b64_tr_b16 a[52:53],v9 offset:10240\nds_read_b64_tr_b16 a[56:57],v10 offset:10240\nds_read_b64_tr_b16 a[60:61],v11 offset:10240\n" \
"ds_read_b64_tr_b16 a[34:35],v8 offset:12288\nds_read_b64_tr_b16 a[38:39],v9 offset:12288\nds_read_b64_tr_b16 a[42:43],v10 offset:12288\nds_read_b64_tr_b16 a[46:47],v11 offset:12288\n" \
"ds_read_b64_tr_b16 a[50:51],v8 offset:14336\nds_read_b64_tr_b16 a[54:55],v9 offset:14336\nds_read_b64_tr_b16 a[58:59],v10 offset:14336\nds_read_b64_tr_b16 a[62:63],v11 offset:14336\n" \
"s_waitcnt vmcnt(0)\ns_barrier\nds_read2st64_b32 v[64:65],v80 offset0:0 offset1:4\nds_read2st64_b32 v[72:73],v82 offset0:12 offset1:8\n" \
"ds_read2st64_b32 v[66:67],v80 offset0:8 offset1:12\nds_read2st64_b32 v[74:75],v82 offset0:4 offset1:0\nds_read2st64_b32 v[68:69],v81 offset0:0 offset1:4\nds_read2st64_b32 v[76:77],v83 offset0:12 offset1:8\n" \
"ds_read2st64_b32 v[70:71],v81 offset0:8 offset1:12\nds_read2st64_b32 v[78:79],v83 offset0:4 offset1:0\ns_waitcnt lgkmcnt(7)\nv_and_b32 v20,0xffff0000,v64\n" \
"v_and_b32 v21,0xffff0000,v65\nv_lshlrev_b32 v16,16,v64\nv_lshlrev_b32 v17,16,v65\ns_waitcnt lgkmcnt(6)\n" \
"v_and_b32 v36,0xffff0000,v72\nv_and_b32 v37,0xffff0000,v73\nv_lshlrev_b32 v32,16,v72\ns_waitcnt lgkmcnt(5)\n" \
"v_and_b32 v22,0xffff0000,v66\nv_and_b32 v23,0xffff0000,v67\nv_lshlrev_b32 v33,16,v73\nv_lshlrev_b32 v18,16,v66\n" \
"s_waitcnt lgkmcnt(4)\nv_and_b32 v38,0xffff0000,v74\nv_and_b32 v39,0xffff0000,v75\nv_lshlrev_b32 v34,16,v74\n" \
"s_waitcnt lgkmcnt(3)\nv_and_b32 v44,0xffff0000,v68\nv_and_b32 v45,0xffff0000,v69\nv_lshlrev_b32 v19,16,v67\n" \
"s_waitcnt lgkmcnt(2)\nv_and_b32 v60,0xffff0000,v76\nv_and_b32 v61,0xffff0000,v77\nv_lshlrev_b32 v35,16,v75\n" \
"v_lshlrev_b32 v40,16,v68\ns_waitcnt lgkmcnt(1)\nv_and_b32 v46,0xffff0000,v70\nv_and_b32 v47,0xffff0000,v71\n" \
"v_lshlrev_b32 v56,16,v76\ns_waitcnt lgkmcnt(0)\nv_and_b32 v62,0xffff0000,v78\nv_and_b32 v63,0xffff0000,v79\n" \
"v_lshlrev_b32 v41,16,v69\nv_lshlrev_b32 v57,16,v77\nv_lshlrev_b32 v42,16,v70\nv_lshlrev_b32 v58,16,v78\n" \
"v_lshlrev_b32 v43,16,v71\nv_lshlrev_b32 v59,16,v79\ns_cmp_eq_u32 s76,128\ns_cbranch_scc1 .Lsz_%=\n" \
".Lbz_%=:\nv_pk_mul_f32 v[24:25],v[16:17],v[94:95] op_sel_hi:[1,0]\nv_pk_mul_f32 v[28:29],v[20:21],v[94:95] op_sel_hi:[1,0]\nv_pk_add_f32 v[64:65],v[20:21],v[36:37]\n" \
"v_pk_add_f32 v[66:67],v[16:17],v[32:33] neg_lo:[0,1] neg_hi:[0,1]\nv_pk_mul_f32 v[26:27],v[18:19],v[94:95] op_sel_hi:[1,0]\nv_pk_mul_f32 v[30:31],v[22:23],v[94:95] op_sel_hi:[1,0]\nv_pk_add_f32 v[68:69],v[22:23],v[38:39]\n" \
"v_pk_add_f32 v[70:71],v[18:19],v[34:35] neg_lo:[0,1] neg_hi:[0,1]\nv_pk_mul_f32 v[48:49],v[40:41],v[94:95] op_sel_hi:[1,0]\nv_pk_mul_f32 v[52:53],v[44:45],v[94:95] op_sel_hi:[1,0]\nv_pk_add_f32 v[72:73],v[44:45],v[60:61]\n" \
"v_pk_add_f32 v[74:75],v[40:41],v[56:57] neg_lo:[0,1] neg_hi:[0,1]\nv_pk_mul_f32 v[50:51],v[42:43],v[94:95] op_sel_hi:[1,0]\nv_pk_mul_f32 v[54:55],v[46:47],v[94:95] op_sel_hi:[1,0]\nv_pk_add_f32 v[76:77],v[46:47],v[62:63]\n" \
"v_pk_add_f32 v[78:79],v[42:43],v[58:59] neg_lo:[0,1] neg_hi:[0,1]\nv_pk_mul_f32 v[16:17],v[16:17],v[92:93] op_sel_hi:[1,0]\nv_pk_mul_f32 v[20:21],v[20:21],v[92:93] op_sel_hi:[1,0]\nv_pk_mul_f32 v[18:19],v[18:19],v[92:93] op_sel_hi:[1,0]\n" \
"v_pk_mul_f32 v[22:23],v[22:23],v[92:93] op_sel_hi:[1,0]\nbuffer_load_dword v96,v120,s[36:39],s80 offen sc1\nbuffer_load_dword v97,v121,s[36:39],s80 offen sc1\nbuffer_load_dword v98,v122,s[36:39],s80 offen sc1\n" \
"buffer_load_dword v99,v123,s[36:39],s80 offen sc1\nbuffer_load_dword v100,v120,s[36:39],s81 offen sc1\nbuffer_load_dword v101,v121,s[36:39],s81 offen sc1\nbuffer_load_dword v102,v122,s[36:39],s81 offen sc1\n" \
"buffer_load_dword v103,v123,s[36:39],s81 offen sc1\nbuffer_load_dword v104,v120,s[36:39],s82 offen sc1\nbuffer_load_dword v105,v121,s[36:39],s82 offen sc1\nbuffer_load_dword v106,v122,s[36:39],s82 offen sc1\n" \
"buffer_load_dword v107,v123,s[36:39],s82 offen sc1\nbuffer_load_dword v108,v120,s[36:39],s83 offen sc1\nbuffer_load_dword v109,v121,s[36:39],s83 offen sc1\nbuffer_load_dword v110,v122,s[36:39],s83 offen sc1\n" \
"buffer_load_dword v111,v123,s[36:39],s83 offen sc1\nv_pk_mul_f32 v[40:41],v[40:41],v[92:93] op_sel_hi:[1,0]\nv_pk_mul_f32 v[44:45],v[44:45],v[92:93] op_sel_hi:[1,0]\nv_pk_mul_f32 v[42:43],v[42:43],v[92:93] op_sel_hi:[1,0]\n" \
"v_pk_mul_f32 v[46:47],v[46:47],v[92:93] op_sel_hi:[1,0]\nv_pk_fma_f32 v[24:25],v[32:33],v[92:93],v[24:25] op_sel_hi:[1,0,1]\nv_pk_fma_f32 v[28:29],v[36:37],v[92:93],v[28:29] op_sel_hi:[1,0,1] neg_lo:[1,0,0] neg_hi:[1,0,0]\nv_pk_fma_f32 v[16:17],v[32:33],v[94:95],v[16:17] op_sel_hi:[1,0,1]\n" \
"v_pk_fma_f32 v[20:21],v[36:37],v[94:95],v[20:21] op_sel_hi:[1,0,1] neg_lo:[1,0,0] neg_hi:[1,0,0]\nv_pk_fma_f32 v[26:27],v[34:35],v[92:93],v[26:27] op_sel_hi:[1,0,1]\nv_pk_fma_f32 v[30:31],v[38:39],v[92:93],v[30:31] op_sel_hi:[1,0,1] neg_lo:[1,0,0] neg_hi:[1,0,0]\nv_pk_fma_f32 v[18:19],v[34:35],v[94:95],v[18:19] op_sel_hi:[1,0,1]\n" \
"v_pk_fma_f32 v[22:23],v[38:39],v[94:95],v[22:23] op_sel_hi:[1,0,1] neg_lo:[1,0,0] neg_hi:[1,0,0]\nv_pk_fma_f32 v[48:49],v[56:57],v[92:93],v[48:49] op_sel_hi:[1,0,1]\nv_pk_fma_f32 v[52:53],v[60:61],v[92:93],v[52:53] op_sel_hi:[1,0,1] neg_lo:[1,0,0] neg_hi:[1,0,0]\nv_pk_fma_f32 v[40:41],v[56:57],v[94:95],v[40:41] op_sel_hi:[1,0,1]\n" \
"v_pk_fma_f32 v[44:45],v[60:61],v[94:95],v[44:45] op_sel_hi:[1,0,1] neg_lo:[1,0,0] neg_hi:[1,0,0]\nv_pk_fma_f32 v[50:51],v[58:59],v[92:93],v[50:51] op_sel_hi:[1,0,1]\nv_pk_fma_f32 v[54:55],v[62:63],v[92:93],v[54:55] op_sel_hi:[1,0,1] neg_lo:[1,0,0] neg_hi:[1,0,0]\nv_pk_fma_f32 v[42:43],v[58:59],v[94:95],v[42:43] op_sel_hi:[1,0,1]\n" \
"v_pk_fma_f32 v[46:47],v[62:63],v[94:95],v[46:47] op_sel_hi:[1,0,1] neg_lo:[1,0,0] neg_hi:[1,0,0]\nv_pk_mul_f32 v[32:33],v[64:65],v[90:91] op_sel_hi:[1,0] neg_lo:[1,0] neg_hi:[1,0]\nv_pk_mul_f32 v[36:37],v[66:67],v[90:91] op_sel_hi:[1,0]\nv_pk_mul_f32 v[34:35],v[68:69],v[90:91] op_sel_hi:[1,0] neg_lo:[1,0] neg_hi:[1,0]\n" \
"v_pk_mul_f32 v[38:39],v[70:71],v[90:91] op_sel_hi:[1,0]\nv_pk_mul_f32 v[56:57],v[72:73],v[90:91] op_sel_hi:[1,0] neg_lo:[1,0] neg_hi:[1,0]\nv_pk_mul_f32 v[60:61],v[74:75],v[90:91] op_sel_hi:[1,0]\nv_pk_mul_f32 v[58:59],v[76:77],v[90:91] op_sel_hi:[1,0] neg_lo:[1,0] neg_hi:[1,0]\n" \
"v_pk_mul_f32 v[62:63],v[78:79],v[90:91] op_sel_hi:[1,0]\ns_mov_b32 s78,0x11111111\ns_mov_b32 s79,0x11111111\nds_read_b64_tr_b16 a[64:65],v8 offset:16384\n" \
"ds_read_b64_tr_b16 a[68:69],v9 offset:16384\nds_read_b64_tr_b16 a[72:73],v10 offset:16384\nds_read_b64_tr_b16 a[76:77],v11 offset:16384\nds_read_b64_tr_b16 a[80:81],v8 offset:18432\n" \
"ds_read_b64_tr_b16 a[84:85],v9 offset:18432\nds_read_b64_tr_b16 a[88:89],v10 offset:18432\nds_read_b64_tr_b16 a[92:93],v11 offset:18432\ns_waitcnt vmcnt(12)\n" \
"v_mfma_f32_16x16x32_f16 v[64:67],v[128:131],v[96:99],0\nv_or3_b32 v124,v96,v97,v98\ns_waitcnt vmcnt(11)\nv_or3_b32 v124,v124,v99,v100\n" \
"v_mfma_f32_16x16x32_f16 v[68:71],v[132:135],v[96:99],0\ns_waitcnt vmcnt(9)\nv_or3_b32 v124,v124,v101,v102\ns_waitcnt vmcnt(8)\n" \
"v_mfma_f32_16x16x32_f16 v[72:75],v[136:139],v[100:103],0\ns_waitcnt vmcnt(7)\nv_or3_b32 v124,v124,v103,v104\nv_mfma_f32_16x16x32_f16 v[76:79],v[140:143],v[100:103],0\n" \
"s_waitcnt vmcnt(5)\nv_or3_b32 v124,v124,v105,v106\ns_waitcnt vmcnt(4)\nv_mfma_f32_16x16x32_f16 v[80:83],v[144:147],v[104:107],0\n" \
"s_waitcnt vmcnt(3)\nv_or3_b32 v124,v124,v107,v108\nv_mfma_f32_16x16x32_f16 v[84:87],v[148:151],v[104:107],0\ns_waitcnt vmcnt(1)\n" \
"v_or3_b32 v124,v124,v109,v110\ns_waitcnt vmcnt(0)\nv_mfma_f32_16x16x32_f16 v[88:91],v[152:155],v[108:111],0\nv_or_b32 v124,v124,v111\n" \
"v_mfma_f32_16x16x32_f16 v[92:95],v[156:159],v[108:111],0\nv_and_b32 v124,1,v124\nv_cmp_ne_u32 vcc,0,v124\ns_nop 0\n" \
"s_and_b64 vcc,vcc,s[78:79]\ns_cbranch_vccnz .Lst_%=\n.Lbt_%=:\nv_cvt_pk_f16_f32 v96,v64,v68\n" \
"v_cvt_pk_f16_f32 v97,v72,v76\nv_cvt_pk_f16_f32 v98,v80,v84\nv_cvt_pk_f16_f32 v100,v65,v69\nv_cvt_pk_f16_f32 v99,v88,v92\n" \
"ds_write_b128 v246,v[96:99]\nv_cvt_pk_f16_f32 v101,v73,v77\nv_cvt_pk_f16_f32 v102,v81,v85\nv_cvt_pk_f16_f32 v103,v89,v93\n" \
"ds_write_b128 v247,v[100:103] offset:1024\ns_add_u32 s95,s92,9584640\ns_add_u32 m0,s57,20480\ns_add_u32 s100,s92,221184\n" \
"buffer_load_dwordx4 v4,s[8:11],s95 offen lds\nv_cvt_pk_f16_f32 v104,v66,v70\nv_cvt_pk_f16_f32 v105,v74,v78\nv_cvt_pk_f16_f32 v106,v82,v86\n" \
"v_cvt_pk_f16_f32 v107,v90,v94\nds_write_b128 v246,v[104:107] offset:2048\nbuffer_load_dwordx4 v5,s[8:11],s95 offen offset:1024 lds\nv_cvt_pk_f16_f32 v108,v67,v71\n" \
"v_cvt_pk_f16_f32 v109,v75,v79\nv_cvt_pk_f16_f32 v110,v83,v87\nv_cvt_pk_f16_f32 v111,v91,v95\nds_write_b128 v247,v[108:111] offset:3072\n" \
"buffer_load_dwordx4 v6,s[8:11],s95 offen offset:2048 lds\ns_add_u32 s101,s92,9658368\ns_add_u32 s31,s92,294912\ns_add_u32 s96,s92,9732096\n" \
"s_add_u32 s97,s92,368640\ns_mov_b32 s5,26624\ns_mov_b32 s6,31232\nbuffer_load_dwordx4 v7,s[8:11],s95 offen offset:3072 lds\n" \
"s_add_u32 m0,s57,24576\ns_add_u32 s95,s92,9805824\ns_waitcnt lgkmcnt(0)\ns_barrier\n" \
"ds_read_b128 v[96:99],v252\nds_read_b128 v[100:103],v253\nds_read_b128 v[104:107],v252 offset:2048\nds_read_b128 v[108:111],v253 offset:2048\n" \
"buffer_load_dwordx4 v4,s[8:11],s100 offen lds\nbuffer_load_dwordx4 v5,s[8:11],s100 offen offset:1024 lds\ns_waitcnt lgkmcnt(3)\nv_mfma_f32_16x16x32_f16 v[64:67],v[112:115],v[96:99],0\n" \
"v_mfma_f32_16x16x32_f16 v[68:71],v[116:119],v[96:99],0\nbuffer_load_dwordx4 v6,s[8:11],s100 offen offset:2048 lds\ns_waitcnt lgkmcnt(0)\nv_mfma_f32_16x16x32_f16 v[72:75],v[112:115],v[100:103],0\n" \
"v_mfma_f32_16x16x32_f16 v[76:79],v[116:119],v[100:103],0\nv_mfma_f32_16x16x32_f16 v[80:83],v[112:115],v[104:107],0\nbuffer_load_dwordx4 v7,s[8:11],s100 offen offset:3072 lds\nv_mfma_f32_16x16x32_f16 v[84:87],v[116:119],v[104:107],0\n" \
"s_add_u32 m0,s57,0\nv_pk_mul_f32 v[122:123],v[16:17],v[64:65]\nv_pk_mul_f32 v[120:121],v[16:17],v[68:69]\nv_pk_mul_f32 v[124:125],v[18:19],v[70:71]\n" \
"v_pk_mul_f32 v[126:127],v[18:19],v[66:67]\nv_pk_fma_f32 v[120:121],v[20:21],v[64:65],v[120:121]\nbuffer_load_dwordx4 v4,s[8:11],s101 offen lds\nv_pk_fma_f32 v[122:123],v[20:21],v[68:69],v[122:123] neg_lo:[1,0,0] neg_hi:[1,0,0]\n" \
"v_pk_fma_f32 v[124:125],v[22:23],v[66:67],v[124:125]\nv_pk_fma_f32 v[126:127],v[22:23],v[70:71],v[126:127] neg_lo:[1,0,0] neg_hi:[1,0,0]\nv_pk_fma_f32 v[120:121],v[32:33],v[76:77],v[120:121]\nv_pk_fma_f32 v[122:123],v[32:33],v[72:73],v[122:123]\n" \
"v_pk_fma_f32 v[124:125],v[34:35],v[78:79],v[124:125]\nv_pk_fma_f32 v[126:127],v[34:35],v[74:75],v[126:127]\nbuffer_load_dwordx4 v5,s[8:11],s101 offen offset:1024 lds\nv_pk_fma_f32 v[120:121],v[36:37],v[72:73],v[120:121]\n" \
"v_pk_fma_f32 v[122:123],v[36:37],v[76:77],v[122:123] neg_lo:[1,0,0] neg_hi:[1,0,0]\nv_pk_fma_f32 v[124:125],v[38:39],v[74:75],v[124:125]\nv_pk_fma_f32 v[126:127],v[38:39],v[78:79],v[126:127] neg_lo:[1,0,0] neg_hi:[1,0,0]\nv_mfma_f32_16x16x32_f16 v[88:91],v[112:115],v[108:111],0\n" \
"v_cvt_pk_f16_f32 v96,v120,v122\nv_cvt_pk_f16_f32 v97,v121,v123\nbuffer_load_dwordx4 v6,s[8:11],s101 offen offset:2048 lds\nv_mfma_f32_16x16x32_f16 v[92:95],v[116:119],v[108:111],0\n" \
"v_cvt_pk_f16_f32 v98,v124,v126\nv_cvt_pk_f16_f32 v99,v125,v127\nv_pk_mul_f32 v[120:121],v[24:25],v[76:77]\nv_pk_mul_f32 v[122:123],v[24:25],v[72:73]\n" \
"v_pk_mul_f32 v[124:125],v[26:27],v[78:79]\nv_pk_mul_f32 v[126:127],v[26:27],v[74:75]\nbuffer_load_dwordx4 v7,s[8:11],s101 offen offset:3072 lds\nv_pk_fma_f32 v[120:121],v[28:29],v[72:73],v[120:121]\n" \
"v_pk_fma_f32 v[122:123],v[28:29],v[76:77],v[122:123] neg_lo:[1,0,0] neg_hi:[1,0,0]\nv_pk_fma_f32 v[124:125],v[30:31],v[74:75],v[124:125]\nv_pk_fma_f32 v[126:127],v[30:31],v[78:79],v[126:127] neg_lo:[1,0,0] neg_hi:[1,0,0]\nv_pk_fma_f32 v[120:121],v[32:33],v[68:69],v[120:121] neg_lo:[1,0,0] neg_hi:[1,0,0]\n" \
"v_pk_fma_f32 v[122:123],v[32:33],v[64:65],v[122:123] neg_lo:[1,0,0] neg_hi:[1,0,0]\nv_pk_fma_f32 v[124:125],v[34:35],v[70:71],v[124:125] neg_lo:[1,0,0] neg_hi:[1,0,0]\ns_add_u32 m0,s57,4096\ns_add_u32 s100,s92,442368\n" \
"buffer_load_dwordx4 v4,s[8:11],s31 offen lds\nv_pk_fma_f32 v[126:127],v[34:35],v[66:67],v[126:127] neg_lo:[1,0,0] neg_hi:[1,0,0]\nv_pk_fma_f32 v[120:121],v[36:37],v[64:65],v[120:121] neg_lo:[1,0,0] neg_hi:[1,0,0]\nv_pk_fma_f32 v[122:123],v[36:37],v[68:69],v[122:123]\n" \
"v_pk_fma_f32 v[124:125],v[38:39],v[66:67],v[124:125] neg_lo:[1,0,0] neg_hi:[1,0,0]\nv_pk_fma_f32 v[126:127],v[38:39],v[70:71],v[126:127]\nv_cvt_pk_f16_f32 v100,v120,v122\nv_cvt_pk_f16_f32 v101,v121,v123\n" \
"buffer_load_dwordx4 v5,s[8:11],s31 offen offset:1024 lds\nv_cvt_pk_f16_f32 v102,v124,v126\nv_cvt_pk_f16_f32 v103,v125,v127\nv_pk_mul_f32 v[120:121],v[40:41],v[84:85]\n" \
"v_pk_mul_f32 v[122:123],v[40:41],v[80:81]\nv_pk_mul_f32 v[124:125],v[42:43],v[86:87]\nv_pk_mul_f32 v[126:127],v[42:43],v[82:83]\nv_pk_fma_f32 v[120:121],v[44:45],v[80:81],v[120:121]\n" \
"buffer_load_dwordx4 v6,s[8:11],s31 offen offset:2048 lds\nv_pk_fma_f32 v[122:123],v[44:45],v[84:85],v[122:123] neg_lo:[1,0,0] neg_hi:[1,0,0]\nv_pk_fma_f32 v[124:125],v[46:47],v[82:83],v[124:125]\nv_pk_fma_f32 v[126:127],v[46:47],v[86:87],v[126:127] neg_lo:[1,0,0] neg_hi:[1,0,0]\n" \
"v_pk_fma_f32 v[120:121],v[56:57],v[92:93],v[120:121]\nv_pk_fma_f32 v[122:123],v[56:57],v[88:89],v[122:123]\nv_pk_fma_f32 v[124:125],v[58:59],v[94:95],v[124:125]\nv_pk_fma_f32 v[126:127],v[58:59],v[90:91],v[126:127]\n" \
"buffer_load_dwordx4 v7,s[8:11],s31 offen offset:3072 lds\nv_pk_fma_f32 v[120:121],v[60:61],v[88:89],v[120:121]\nv_pk_fma_f32 v[122:123],v[60:61],v[92:93],v[122:123] neg_lo:[1,0,0] neg_hi:[1,0,0]\nv_pk_fma_f32 v[124:125],v[62:63],v[90:91],v[124:125]\n" \
"v_pk_fma_f32 v[126:127],v[62:63],v[94:95],v[126:127] neg_lo:[1,0,0] neg_hi:[1,0,0]\nv_cvt_pk_f16_f32 v104,v120,v122\nv_cvt_pk_f16_f32 v105,v121,v123\nv_cvt_pk_f16_f32 v106,v124,v126\n" \
"s_add_u32 m0,s57,8192\ns_add_u32 s101,s92,9879552\nbuffer_load_dwordx4 v4,s[8:11],s96 offen lds\nv_cvt_pk_f16_f32 v107,v125,v127\n" \
"v_pk_mul_f32 v[120:121],v[48:49],v[92:93]\nv_pk_mul_f32 v[122:123],v[48:49],v[88:89]\nv_pk_mul_f32 v[124:125],v[50:51],v[94:95]\nv_pk_mul_f32 v[126:127],v[50:51],v[90:91]\n" \
"v_pk_fma_f32 v[120:121],v[52:53],v[88:89],v[120:121]\nv_pk_fma_f32 v[122:123],v[52:53],v[92:93],v[122:123] neg_lo:[1,0,0] neg_hi:[1,0,0]\nbuffer_load_dwordx4 v5,s[8:11],s96 offen offset:1024 lds\nv_pk_fma_f32 v[124:125],v[54:55],v[90:91],v[124:125]\n" \
"v_pk_fma_f32 v[126:127],v[54:55],v[94:95],v[126:127] neg_lo:[1,0,0] neg_hi:[1,0,0]\nv_pk_fma_f32 v[120:121],v[56:57],v[84:85],v[120:121] neg_lo:[1,0,0] neg_hi:[1,0,0]\nv_pk_fma_f32 v[122:123],v[56:57],v[80:81],v[122:123] neg_lo:[1,0,0] neg_hi:[1,0,0]\nv_pk_fma_f32 v[124:125],v[58:59],v[86:87],v[124:125] neg_lo:[1,0,0] neg_hi:[1,0,0]\n" \
"v_pk_fma_f32 v[126:127],v[58:59],v[82:83],v[126:127] neg_lo:[1,0,0] neg_hi:[1,0,0]\nv_pk_fma_f32 v[120:121],v[60:61],v[80:81],v[120:121] neg_lo:[1,0,0] neg_hi:[1,0,0]\nbuffer_load_dwordx4 v6,s[8:11],s96 offen offset:2048 lds\nv_pk_fma_f32 v[122:123],v[60:61],v[84:85],v[122:123]\n" \
"v_pk_fma_f32 v[124:125],v[62:63],v[82:83],v[124:125] neg_lo:[1,0,0] neg_hi:[1,0,0]\nv_pk_fma_f32 v[126:127],v[62:63],v[86:87],v[126:127]\nv_mfma_f32_16x16x32_f16 v[64:67],v[112:115],v[96:99],0\nv_cvt_pk_f16_f32 v108,v120,v122\n" \
"v_cvt_pk_f16_f32 v109,v121,v123\nv_mfma_f32_16x16x32_f16 v[68:71],v[116:119],v[96:99],0\nbuffer_load_dwordx4 v7,s[8:11],s96 offen offset:3072 lds\nv_mfma_f32_16x16x32_f16 v[80:83],v[112:115],v[104:107],0\n" \
"v_cvt_pk_f16_f32 v110,v124,v126\nv_cvt_pk_f16_f32 v111,v125,v127\nv_mfma_f32_16x16x32_f16 v[84:87],v[116:119],v[104:107],0\ns_add_u32 m0,s57,12288\n" \
"s_waitcnt vmcnt(16)\nds_read_b64_tr_b16 a[66:67],v8 offset:20480\nv_mfma_f32_16x16x32_f16 v[72:75],v[112:115],v[100:103],0\nbuffer_load_dwordx4 v4,s[8:11],s97 offen lds\n" \
"v_mfma_f32_16x16x32_f16 v[76:79],v[116:119],v[100:103],0\nv_cvt_pk_f16_f32 v96,v64,v68\nv_cvt_pk_f16_f32 v98,v65,v69\nv_mfma_f32_16x16x32_f16 v[88:91],v[112:115],v[108:111],0\n" \
"v_cvt_pk_f16_f32 v97,v80,v84\nds_write_b64 v248,v[96:97] offset:16384\nbuffer_load_dwordx4 v5,s[8:11],s97 offen offset:1024 lds\nv_mfma_f32_16x16x32_f16 v[92:95],v[116:119],v[108:111],0\n" \
"v_cvt_pk_f16_f32 v99,v81,v85\nds_write_b64 v248,v[98:99] offset:17408\nv_cvt_pk_f16_f32 v100,v66,v70\nv_cvt_pk_f16_f32 v101,v82,v86\n" \
"ds_write_b64 v248,v[100:101] offset:18432\nbuffer_load_dwordx4 v6,s[8:11],s97 offen offset:2048 lds\nv_cvt_pk_f16_f32 v102,v67,v71\nv_cvt_pk_f16_f32 v103,v83,v87\n" \
"ds_write_b64 v248,v[102:103] offset:19456\nv_cvt_pk_f16_f32 v104,v72,v76\nv_cvt_pk_f16_f32 v105,v88,v92\nds_write_b64 v248,v[104:105] offset:16512\n" \
"buffer_load_dwordx4 v7,s[8:11],s97 offen offset:3072 lds\nv_cvt_pk_f16_f32 v106,v73,v77\nv_cvt_pk_f16_f32 v107,v89,v93\nds_write_b64 v248,v[106:107] offset:17536\n" \
"v_cvt_pk_f16_f32 v108,v74,v78\nv_cvt_pk_f16_f32 v109,v90,v94\nds_write_b64 v248,v[108:109] offset:18560\ns_add_u32 m0,s57,16384\n" \
"v_cvt_pk_f16_f32 v110,v75,v79\nbuffer_load_dwordx4 v4,s[8:11],s95 offen lds\nv_cvt_pk_f16_f32 v111,v91,v95\nds_write_b64 v248,v[110:111] offset:19584\n" \
"ds_read_b64_tr_b16 a[70:71],v9 offset:20480\nds_read_b64_tr_b16 a[74:75],v10 offset:20480\nds_read_b64_tr_b16 a[78:79],v11 offset:20480\nds_read_b64_tr_b16 a[82:83],v8 offset:22528\n" \
"ds_read_b64_tr_b16 a[86:87],v9 offset:22528\nbuffer_load_dwordx4 v5,s[8:11],s95 offen offset:1024 lds\nds_read_b64_tr_b16 a[90:91],v10 offset:22528\nds_read_b64_tr_b16 a[94:95],v11 offset:22528\n" \
"s_waitcnt vmcnt(18)\nds_read_b64_tr_b16 a[96:97],v8 offset:24576\nds_read_b64_tr_b16 a[100:101],v9 offset:24576\nds_read_b64_tr_b16 a[104:105],v10 offset:24576\n" \
"ds_read_b64_tr_b16 a[108:109],v11 offset:24576\nds_read_b64_tr_b16 a[112:113],v8 offset:26624\nds_read_b64_tr_b16 a[116:117],v9 offset:26624\nds_read_b64_tr_b16 a[120:121],v10 offset:26624\n" \
"buffer_load_dwordx4 v6,s[8:11],s95 offen offset:2048 lds\ns_waitcnt lgkmcnt(9)\ns_barrier\nds_read_b128 v[96:99],v254 offset:16384\n" \
"ds_read_b128 v[100:103],v254 offset:17408\nds_read_b128 v[104:107],v254 offset:18432\nds_read_b128 v[108:111],v254 offset:19456\nds_read_b64_tr_b16 a[124:125],v11 offset:26624\n" \
"v_min_u32 v32,2288,v2\nbuffer_load_dwordx4 v7,s[8:11],s95 offen offset:3072 lds\ns_add_u32 m0,s57,20480\ns_waitcnt lgkmcnt(5)\n" \
"buffer_load_dwordx4 v4,s[8:11],s100 offen lds\ns_waitcnt lgkmcnt(4)\nv_mfma_f32_16x16x32_f16 v[64:67],v[96:99],v[160:163],0\nv_mfma_f32_16x16x32_f16 v[68:71],v[96:99],v[164:167],0\n" \
"s_waitcnt lgkmcnt(2)\nv_mfma_f32_16x16x32_f16 v[72:75],v[100:103],v[168:171],0\nbuffer_load_dwordx4 v5,s[8:11],s100 offen offset:1024 lds\nv_mfma_f32_16x16x32_f16 v[76:79],v[100:103],v[172:175],0\n" \
"v_mfma_f32_16x16x32_f16 v[80:83],v[104:107],v[176:179],0\nv_mfma_f32_16x16x32_f16 v[84:87],v[104:107],v[180:183],0\nbuffer_load_dwordx4 v6,s[8:11],s100 offen offset:2048 lds\ns_waitcnt lgkmcnt(0)\n" \
"v_mfma_f32_16x16x32_f16 v[88:91],v[108:111],v[184:187],0\nv_cvt_pk_f16_f32 v96,v68,v64\nv_cvt_pk_f16_f32 v97,v69,v65\nv_cvt_pk_f16_f32 v98,v70,v66\n" \
"v_cvt_pk_f16_f32 v99,v71,v67\nv_or_b32_e64 v96,1,v96\nbuffer_load_dwordx4 v7,s[8:11],s100 offen offset:3072 lds\nv_mfma_f32_16x16x32_f16 v[92:95],v[108:111],v[188:191],0\n" \
"v_cvt_pk_f16_f32 v100,v76,v72\nv_cvt_pk_f16_f32 v101,v77,v73\nv_cvt_pk_f16_f32 v102,v78,v74\nv_cvt_pk_f16_f32 v103,v79,v75\n" \
"v_or_b32_e64 v100,1,v100\nv_cvt_pk_f16_f32 v104,v84,v80\ns_add_u32 m0,s57,24576\nv_cvt_pk_f16_f32 v105,v85,v81\n" \
"buffer_load_dwordx4 v4,s[8:11],s101 offen lds\nv_cvt_pk_f16_f32 v106,v86,v82\nv_cvt_pk_f16_f32 v107,v87,v83\nv_or_b32_e64 v104,1,v104\n" \
"v_cvt_pk_f16_f32 v108,v92,v88\nv_cvt_pk_f16_f32 v109,v93,v89\nv_cvt_pk_f16_f32 v110,v94,v90\nv_cvt_pk_f16_f32 v111,v95,v91\n" \
"v_or_b32_e64 v108,1,v108\nbuffer_load_dwordx4 v5,s[8:11],s101 offen offset:1024 lds\nbuffer_load_dwordx4 v6,s[8:11],s101 offen offset:2048 lds\nbuffer_load_dwordx4 v7,s[8:11],s101 offen offset:3072 lds\n" \
"buffer_load_dwordx4 v[16:19],v32,s[36:39],s5 offen sc1\nbuffer_load_dwordx4 v[20:23],v32,s[36:39],s5 offen offset:2304 sc1\nbuffer_load_dwordx4 v[24:27],v32,s[36:39],s6 offen sc1\nbuffer_load_dwordx4 v[28:31],v32,s[36:39],s6 offen offset:2304 sc1\n" \
"buffer_store_dwordx4 v[96:99],v255,s[36:39],s80 offen sc1\nbuffer_store_dwordx4 v[100:103],v255,s[36:39],s81 offen sc1\nbuffer_store_dwordx4 v[104:107],v255,s[36:39],s82 offen sc1\nbuffer_store_dwordx4 v[108:111],v255,s[36:39],s83 offen sc1\n" \
"s_barrier\ns_waitcnt vmcnt(32)\nds_read_b64_tr_b16 a[98:99],v8\nds_read_b64_tr_b16 a[102:103],v9\n" \
"ds_read_b64_tr_b16 a[106:107],v10\nds_read_b64_tr_b16 a[110:111],v11\nds_read_b64_tr_b16 a[114:115],v8 offset:2048\nds_read_b64_tr_b16 a[118:119],v9 offset:2048\n" \
"ds_read_b64_tr_b16 a[122:123],v10 offset:2048\nds_read_b64_tr_b16 a[126:127],v11 offset:2048\ns_waitcnt vmcnt(28)\nds_read_b64_tr_b16 a[128:129],v8 offset:4096\n" \
"ds_read_b64_tr_b16 a[132:133],v9 offset:4096\nds_read_b64_tr_b16 a[136:137],v10 offset:4096\nds_read_b64_tr_b16 a[140:141],v11 offset:4096\nds_read_b64_tr_b16 a[144:145],v8 offset:6144\n" \
"ds_read_b64_tr_b16 a[148:149],v9 offset:6144\nds_read_b64_tr_b16 a[152:153],v10 offset:6144\nds_read_b64_tr_b16 a[156:157],v11 offset:6144\ns_waitcnt vmcnt(24)\n" \
"ds_read_b64_tr_b16 a[130:131],v8 offset:8192\nds_read_b64_tr_b16 a[134:135],v9 offset:8192\nds_read_b64_tr_b16 a[138:139],v10 offset:8192\nds_read_b64_tr_b16 a[142:143],v11 offset:8192\n" \
"ds_read_b64_tr_b16 a[146:147],v8 offset:10240\nds_read_b64_tr_b16 a[150:151],v9 offset:10240\nds_read_b64_tr_b16 a[154:155],v10 offset:10240\nds_read_b64_tr_b16 a[158:159],v11 offset:10240\n" \
"s_waitcnt vmcnt(7)\nv_cmp_ne_u64 vcc,s[28:29],v[16:17]\ns_waitcnt vmcnt(6)\nv_cmp_ne_u64 s[24:25],s[28:29],v[20:21]\n" \
"s_waitcnt vmcnt(5)\nv_cmp_ne_u64 s[26:27],s[28:29],v[24:25]\ns_or_b64 vcc,vcc,s[24:25]\ns_waitcnt vmcnt(4)\n" \
"v_cmp_ne_u64 s[24:25],s[28:29],v[28:29]\ns_or_b64 vcc,vcc,s[26:27]\ns_or_b64 vcc,vcc,s[24:25]\ns_cbranch_vccnz .LsnC_%=\n" \
".LbnC_%=:\nv_bfe_u32 v112,v0,1,5\nv_mul_u32_u24 v112,72,v112\nv_lshrrev_b32 v113,6,v0\n" \
"v_mul_u32_u24 v114,144,v0\nv_lshl_add_u32 v112,v113,5,v112\nv_subrev_u32 v114,18368,v114\nv_mov_b32 v115,1048576\n" \
"v_cmp_gt_u32 vcc,144,v0\nv_and_b32 v113,1,v0\nv_lshl_add_u32 v112,v113,4,v112\nv_cndmask_b32 v114,v115,v114,vcc\n" \
"v_and_b32 v97,0xffff0000,v18\nv_lshlrev_b32 v96,16,v18\ns_add_u32 s24,s93,s94\nv_cmp_gt_u32 vcc,128,v0\n" \
"v_add_u32 v113,8,v112\nv_mul_f32 v96,0x3ee38e39,v96\nv_mul_f32 v97,0x3ee38e39,v97\ns_sub_u32 s24,s24,253\n" \
"v_cndmask_b32 v112,v114,v112,vcc\nv_and_b32 v99,0xffff0000,v19\nv_ldexp_f32 v96,v96,s24\nv_ldexp_f32 v97,v97,s24\n" \
"v_lshlrev_b32_e64 v98,16,v19\nds_write_b64 v112,v[96:97] offset:28800\nv_add_u32 v115,72,v114\nv_mul_f32 v98,0x3ee38e39,v98\n" \
"v_mul_f32 v99,0x3ee38e39,v99\nv_cndmask_b32_e64 v113,v115,v113,vcc\nv_ldexp_f32 v98,v98,s24\nv_ldexp_f32 v99,v99,s24\n" \
"ds_write_b64 v113,v[98:99] offset:28800\nv_and_b32 v101,0xffff0000,v22\nv_lshlrev_b32 v100,16,v22\nds_write_b64 v112,v[100:101] offset:31104\n" \
"v_and_b32 v103,0xffff0000,v23\nv_lshlrev_b32 v102,16,v23\ns_lshr_b32 s25,s33,3\nds_write_b64 v113,v[102:103] offset:31104\n" \
"s_mul_i32 s25,s25,576\nv_and_b32 v105,0xffff0000,v26\nv_lshlrev_b32 v104,16,v26\ns_sub_u32 s25,163200,s25\n" \
"ds_write_b64 v112,v[104:105] offset:33408\nv_add_u32 v114,s25,v2\nv_subrev_u32 v115,163200,v114\nv_and_b32 v107,0xffff0000,v27\n" \
"v_lshlrev_b32 v106,16,v27\nv_mov_b32 v116,1048576\nv_cmp_gt_u32 vcc,576,v115\nds_write_b64 v113,v[106:107] offset:33408\n" \
"v_and_b32 v109,0xffff0000,v30\nv_cndmask_b32 v114,v116,v114,vcc\nv_and_b32 v111,0xffff0000,v31\nv_lshlrev_b32 v108,16,v30\n" \
"v_lshlrev_b32 v110,16,v31\nds_write_b128 v114,v[108:111]\nv_and_b32 v112,3,v0\ns_mul_i32 s27,s3,4896\n" \
"v_and_b32 v113,12,v0\nv_lshlrev_b32 v112,2,v112\ns_add_u32 s27,s27,9216\nv_max_u32 v112,v112,v113\n" \
"v_mov_b32 v114,1048576\nv_mov_b32 v113,s27\nv_cmp_gt_u32 vcc,12,v112\nv_and_b32 v124,48,v0\n" \
"s_and_b32 s22,s33,7\nv_cndmask_b32 v112,v114,v113,vcc\nv_add_u32 v48,v234,v112\nv_add_u32 v49,v235,v112\n" \
"v_add_u32 v50,v236,v112\nv_add_u32 v51,v237,v112\nv_add_u32 v52,v238,v112\nv_add_u32 v53,v239,v112\n" \
"v_add_u32 v54,v240,v112\nv_add_u32 v55,v241,v112\nv_cmp_gt_u32 vcc,64,v0\ns_and_b32 s24,s32,1\n" \
"s_lshl3_add_u32 s22,s24,s22\nv_cndmask_b32 v124,v124,v3,vcc\ns_bfe_u32 s24,s33,0x10002\ns_lshl1_add_u32 s24,s32,s24\n" \
"s_waitcnt lgkmcnt(0)\ns_barrier\ns_mul_i32 s19,s24,18\ns_and_b32 s24,s33,3\n" \
"s_lshl2_add_u32 s23,s24,s19\ns_lshl_b32 s24,s3,5\ns_lshr_b32 s20,s33,3\ns_lshr_b32 s21,s32,1\n" \
"s_add_u32 s54,s14,s24\ns_lshl2_add_u32 s24,s20,s21\ns_add_u32 s23,s23,s3\ns_lshl2_add_u32 s24,s24,s3\n" \
"s_add_u32 s23,s23,1\ns_lshl_b32 s24,s24,9\ns_lshl_b32 s23,s23,9\ns_add_u32 s24,s24,262144\n" \
"s_sub_u32 s59,s24,s23\ns_lshr_b32 s25,s3,1\ns_mul_i32 s24,s21,1152\ns_mul_i32 s25,s25,576\n" \
"s_add_u32 s24,s24,s25\ns_lshl4_add_u32 s24,s24,s22\ns_and_b32 s25,s3,1\ns_lshl_b32 s24,s24,8\n" \
"s_lshl_b32 s65,s25,17\ns_add_u32 s65,s65,s24\ns_lshl_b32 s25,s25,20\ns_add_u32 s24,s24,s25\n" \
"s_add_u32 s60,s24,655488\ns_add_u32 s61,s24,786432\ns_add_u32 s62,s24,786560\ns_add_u32 s63,s24,917504\n" \
"s_add_u32 s64,s24,917632\ns_mul_i32 s52,s20,4505600\ns_lshl2_add_u32 s24,s21,s3\ns_add_u32 s52,s52,12123648\n" \
"s_mul_i32 s24,s24,9216\ns_add_u32 s24,s24,s52\ns_add_u32 s68,s24,819712\ns_lshl_b32 s24,s22,14\n" \
"s_add_u32 s24,s24,s52\ns_lshl2_add_u32 s25,s21,s3\ns_add_u32 s24,s24,967168\ns_lshl_b32 s25,s25,8\n" \
"s_add_u32 s79,s25,s24\ns_add_u32 s25,s21,4\ns_and_b32 s25,s25,3\ns_add_u32 s24,s24,s12\n" \
"s_lshl_b32 s25,s25,12\ns_add_u32 s25,s25,s24\ns_sub_u32 s74,s25,819200\ns_add_u32 s25,s21,3\n" \
"s_and_b32 s25,s25,3\ns_lshl_b32 s25,s25,12\ns_add_u32 s25,s25,s24\ns_sub_u32 s71,s25,2048000\n" \
"s_sub_u32 s76,s25,1638400\ns_add_u32 s25,s21,2\ns_and_b32 s25,s25,3\ns_lshl_b32 s25,s25,12\n" \
"s_add_u32 s25,s25,s24\ns_sub_u32 s72,s25,1638400\ns_sub_u32 s77,s25,1638400\nbuffer_load_dwordx4 v[112:115],v3,s[36:39],s16 offen sc1\n" \
"buffer_load_dwordx4 v[116:119],v3,s[36:39],s17 offen sc1\nbuffer_load_dwordx4 v[120:123],v124,s[36:39],s18 offen sc1\ns_add_u32 s25,s21,1\ns_and_b32 s25,s25,3\n" \
"s_lshl_b32 s25,s25,12\ns_add_u32 s25,s25,s24\ns_sub_u32 s73,s25,1638400\ns_sub_u32 s78,s25,1228800\n" \
"s_lshl2_add_u32 s26,s33,s3\ns_and_b32 s24,s26,15\ns_and_b32 s25,s26,8\ns_lshl2_add_u32 s24,s24,192\n" \
"s_mul_i32 s25,s25,28\ns_add_u32 s25,s25,s24\ns_sub_u32 s25,s25,32\ns_bitcmp1_b32 s26,4\n" \
"s_cselect_b32 s24,s25,s24\ns_and_b32 s25,s26,31\ns_lshl2_add_u32 s25,s25,448\ns_bitcmp1_b32 s26,5\n" \
"s_cselect_b32 s24,s25,s24\ns_lshr_b32 s25,s26,6\ns_lshl1_add_u32 s25,s21,s25\ns_mul_i32 s25,s25,576\n" \
"s_add_u32 s24,s24,s25\ns_and_b32 s25,s32,1\ns_lshl1_add_u32 s24,s24,s25\ns_lshl_b32 s24,s24,11\n" \
"s_bfe_u32 s25,s33,131072\ns_add_u32 s24,s24,s25\nv_bfe_u32 v127,v0,5,1\nv_and_b32 v13,31,v0\n" \
"s_mul_i32 s48,s20,589824\ns_add_u32 s89,s24,1\nv_mul_u32_u24 v127,2448,v127\nv_mul_u32_u24 v13,72,v13\n" \
"s_mul_i32 s27,s3,4896\ns_lshl4_add_u32 s48,s12,s48\ns_lshl_b32 s86,s21,11\ns_lshl2_add_u32 s24,s2,s3\n" \
"v_and_b32 v63,0xffff0000,v224\nv_and_b32 v65,0xffff0000,v225\nv_and_b32 v67,0xffff0000,v226\nv_and_b32 v69,0xffff0000,v227\n" \
"v_and_b32 v71,0xffff0000,v228\nv_and_b32 v73,0xffff0000,v229\nv_and_b32 v75,0xffff0000,v230\nv_and_b32 v77,0xffff0000,v231\n" \
"v_and_b32 v79,0xffff0000,v232\nv_lshlrev_b32 v62,16,v224\nv_lshlrev_b32 v64,16,v225\nv_lshlrev_b32 v66,16,v226\n" \
"v_lshlrev_b32 v68,16,v227\nv_lshlrev_b32 v70,16,v228\nv_lshlrev_b32 v72,16,v229\nv_lshlrev_b32 v74,16,v230\n" \
"v_lshlrev_b32 v76,16,v231\nv_lshlrev_b32 v78,16,v232\nv_add3_u32 v13,v13,v127,s27\ns_add_u32 s48,s48,9764864\n" \
"s_add_u32 s65,s65,2097152\ns_lshl_b32 s67,s21,7\ns_lshl_b32 s85,s21,17\ns_sub_u32 s86,s14,s86\n" \
"s_lshl_b32 s24,s24,8\nv_pk_add_f32 v[62:63],v[62:63],v[250:251] op_sel_hi:[1,0] neg_lo:[0,1] neg_hi:[0,1]\nv_pk_add_f32 v[64:65],v[64:65],v[250:251] op_sel_hi:[1,0] neg_lo:[0,1] neg_hi:[0,1]\nv_pk_add_f32 v[66:67],v[66:67],v[250:251] op_sel_hi:[1,0] neg_lo:[0,1] neg_hi:[0,1]\n" \
"v_pk_add_f32 v[68:69],v[68:69],v[250:251] op_sel_hi:[1,0] neg_lo:[0,1] neg_hi:[0,1]\nv_pk_add_f32 v[70:71],v[70:71],v[250:251] op_sel_hi:[1,0] neg_lo:[0,1] neg_hi:[0,1]\nv_pk_add_f32 v[72:73],v[72:73],v[250:251] op_sel_hi:[1,0] neg_lo:[0,1] neg_hi:[0,1]\nv_pk_add_f32 v[74:75],v[74:75],v[250:251] op_sel_hi:[1,0] neg_lo:[0,1] neg_hi:[0,1]\n" \
"v_pk_add_f32 v[76:77],v[76:77],v[250:251] op_sel_hi:[1,0] neg_lo:[0,1] neg_hi:[0,1]\nv_pk_add_f32 v[78:79],v[78:79],v[250:251] op_sel_hi:[1,0] neg_lo:[0,1] neg_hi:[0,1]\nv_mov_b64 v[16:17],v[192:193]\nv_mov_b64 v[18:19],v[194:195]\n" \
"v_mov_b64 v[20:21],v[196:197]\nv_mov_b64 v[22:23],v[198:199]\nv_mov_b64 v[24:25],v[208:209]\nv_mov_b64 v[26:27],v[210:211]\n" \
"v_mov_b64 v[28:29],v[212:213]\nv_mov_b64 v[30:31],v[214:215]\nv_mov_b64 v[32:33],v[216:217]\nv_mov_b64 v[34:35],v[218:219]\n" \
"v_mov_b64 v[36:37],v[220:221]\nv_mov_b64 v[38:39],v[222:223]\nv_mov_b64 v[40:41],v[200:201]\nv_mov_b64 v[42:43],v[202:203]\n" \
"v_mov_b64 v[44:45],v[204:205]\nv_mov_b64 v[46:47],v[206:207]\nv_mov_b64 v[56:57],v[242:243]\nv_mov_b64 v[58:59],v[244:245]\n" \
"v_mov_b32 v60,v249\nv_mov_b32 v61,v250\nds_read_b64_tr_b16 a[160:161],v8 offset:12288\nds_read_b64_tr_b16 a[164:165],v9 offset:12288\n" \
"ds_read_b64_tr_b16 a[168:169],v10 offset:12288\nds_read_b64_tr_b16 a[172:173],v11 offset:12288\nds_read_b64_tr_b16 a[176:177],v8 offset:14336\nds_read_b64_tr_b16 a[180:181],v9 offset:14336\n" \
"ds_read_b64_tr_b16 a[184:185],v10 offset:14336\nds_read_b64_tr_b16 a[188:189],v11 offset:14336\nds_read_b64_tr_b16 a[162:163],v8 offset:16384\nds_read_b64_tr_b16 a[166:167],v9 offset:16384\n" \
"ds_read_b64_tr_b16 a[170:171],v10 offset:16384\nds_read_b64_tr_b16 a[174:175],v11 offset:16384\nds_read_b64_tr_b16 a[178:179],v8 offset:18432\nv_mov_b32_e64 v127,0\n" \
"ds_read_b64_tr_b16 a[182:183],v9 offset:18432\nds_read_b64_tr_b16 a[186:187],v10 offset:18432\ns_add_u32 s49,s48,4096\ns_add_u32 s50,s48,8192\n" \
"s_add_u32 s51,s48,12288\ns_add_u32 s53,s52,3276800\ns_add_u32 s83,s52,2048000\ns_mov_b32 s55,0xbdd2d3e7\n" \
"s_mul_i32 s56,s3,9568\ns_lshl_b32 s58,s3,13\ns_add_u32 s75,s59,196608\ns_add_u32 s66,s65,128\n" \
"ds_read_b64_tr_b16 a[190:191],v11 offset:18432\nds_read_b64_tr_b16 a[192:193],v8 offset:20480\ns_add_u32 s67,s67,163200\nds_read_b64_tr_b16 a[196:197],v9 offset:20480\n" \
"s_add_u32 s69,s68,3072\nds_read_b64_tr_b16 a[200:201],v10 offset:20480\ns_add_u32 s70,s68,6144\nds_read_b64_tr_b16 a[204:205],v11 offset:20480\n" \
"s_add_u32 s80,s79,4096\nds_read_b64_tr_b16 a[208:209],v8 offset:22528\ns_add_u32 s81,s79,8192\nds_read_b64_tr_b16 a[212:213],v9 offset:22528\n" \
"s_add_u32 s82,s79,12288\nds_read_b64_tr_b16 a[216:217],v10 offset:22528\nds_read_b64_tr_b16 a[220:221],v11 offset:22528\ns_and_b32 s84,s33,1\n" \
"ds_read_b64_tr_b16 a[194:195],v8 offset:24576\ns_add_u32 s85,s85,65536\nds_read_b64_tr_b16 a[198:199],v9 offset:24576\ns_add_u32 s87,s12,65536\n" \
"ds_read_b64_tr_b16 a[202:203],v10 offset:24576\ns_add_u32 s88,s74,819200\nds_read_b64_tr_b16 a[206:207],v11 offset:24576\ns_add_u32 s86,s86,0x10000\n" \
"ds_read_b64_tr_b16 a[210:211],v8 offset:26624\ns_mov_b32 s90,7405568\nds_read_b64_tr_b16 a[214:215],v9 offset:26624\ns_add_u32 s91,s24,69206016\n" \
"ds_read_b64_tr_b16 a[218:219],v10 offset:26624\nds_read_b64_tr_b16 a[222:223],v11 offset:26624\ns_sub_u32 s98,s57,s12\ns_mov_b32 s99,s12\n" \
"s_waitcnt vmcnt(1)\nv_and_b32 v125,v112,v116\ns_waitcnt vmcnt(0)\nv_bitop3_b32 v125,v125,v120,1 bitop3:0x80\n" \
"v_cmp_eq_u32 vcc,0,v125\ns_nop 0\ns_cbranch_vccnz .LsuC_%=\n.LbuC_%=:\n" \
"v_mfma_f32_16x16x32_f16 v[80:83],v[32:35],v[112:115],0\nv_and_b32 v3,31,v0\nv_mul_u32_u24 v3,72,v3\nv_mfma_f32_16x16x32_f16 v[84:87],v[36:39],v[112:115],0\n" \
"v_add_u32 v13,9216,v13\nv_mov_b32 v104,-1\nv_mfma_f32_16x16x32_f16 v[88:91],v[32:35],v[116:119],0\nv_mov_b32 v105,-1\n" \
"v_mov_b32 v106,-1\nv_mfma_f32_16x16x32_f16 v[92:95],v[36:39],v[116:119],0\nv_mov_b32 v107,-1\ns_add_u32 s27,s2,192\n" \
"v_mfma_f32_16x16x32_f16 v[96:99],v[32:35],v[120:123],0\ns_lshr_b32 s31,s27,4\ns_mul_i32 s96,s31,37\ns_lshr_b32 s96,s96,8\n" \
"s_lshl2_add_u32 s31,s96,s31\ns_mul_i32 s31,s31,409600\ns_and_b32 s96,s27,15\ns_lshl_b32 s96,s96,14\n" \
"s_add_u32 s31,s31,s96\ns_add_u32 s31,s31,13090816\nbuffer_store_dwordx4 v[104:107],v2,s[36:39],s31 offen sc1\nv_mfma_f32_16x16x32_f16 v[100:103],v[36:39],v[120:123],0\n" \
"v_cvt_pk_f16_f32 v112,v84,v80\nv_cvt_pk_f16_f32 v113,v85,v81\nv_cvt_pk_f16_f32 v114,v86,v82\nv_cvt_pk_f16_f32 v115,v87,v83\n" \
"ds_write_b128 v57,v[112:115]\nv_cvt_pk_f16_f32 v116,v92,v88\nv_cvt_pk_f16_f32 v117,v93,v89\ns_add_u32 s31,s31,4096\n" \
"buffer_store_dwordx4 v[104:107],v2,s[36:39],s31 offen sc1\nv_cvt_pk_f16_f32 v118,v94,v90\nv_cvt_pk_f16_f32 v119,v95,v91\nds_write_b128 v58,v[116:119]\n" \
"v_cvt_pk_f16_f32 v120,v100,v96\nv_cvt_pk_f16_f32 v121,v101,v97\nv_cvt_pk_f16_f32 v122,v102,v98\nv_cvt_pk_f16_f32 v123,v103,v99\n" \
"ds_write_b128 v60,v[120:123]\ns_add_u32 s31,s31,4096\nbuffer_store_dwordx4 v[104:107],v2,s[36:39],s31 offen sc1\nv_add_u32 v60,31104,v3\n" \
"v_add_u32 v3,28800,v3\nds_read2_b64 v[32:35],v3 offset0:0 offset1:1\nds_read2_b64 v[36:39],v3 offset0:2 offset1:3\ns_add_u32 s31,s31,4096\n" \
"s_add_u32 s27,s2,47\ns_lshr_b32 s97,s27,4\ns_mul_i32 s95,s97,37\ns_lshr_b32 s95,s95,8\n" \
"s_lshl2_add_u32 s97,s95,s97\ns_mul_i32 s97,s97,409600\ns_and_b32 s95,s27,15\ns_lshl_b32 s95,s95,14\n" \
"buffer_store_dwordx4 v[104:107],v2,s[36:39],s31 offen sc1\ns_add_u32 s97,s97,s95\ns_add_u32 s97,s97,13090816\ns_add_u32 s100,s92,516096\n" \
"s_waitcnt lgkmcnt(2)\ns_barrier\nds_read_b64 v[112:113],v56\nds_read_b64 v[116:117],v56 offset:512\n" \
"ds_read_b64 v[120:121],v56 offset:256\nds_read_b64 v[124:125],v56 offset:768\nds_read_b64 v[114:115],v56 offset:1024\nds_read_b64 v[122:123],v56 offset:1280\n" \
"ds_read_b64 v[56:57],v3 offset:64\ns_add_u32 m0,s57,0\nbuffer_store_dwordx4 v[104:107],v2,s[36:39],s97 offen sc1\ns_add_u32 s97,s97,4096\n" \
"s_add_u32 s101,s92,9953280\ns_bfe_u32 s26,s33,0x20001\ns_lshl2_add_u32 s15,s32,s26\ns_lshl2_add_u32 s15,s15,s3\n" \
"s_lshl_b32 s15,s15,14\ns_add_u32 s15,s15,5308416\ns_add_u32 s31,s15,0\ns_add_u32 s96,s15,4096\n" \
"s_add_u32 s95,s15,12288\nv_bfe_u32 v110,v0,1,2\nv_bfe_u32 v111,v0,4,1\nv_xor_b32 v110,v110,v111\n" \
"buffer_store_dwordx4 v[104:107],v2,s[36:39],s97 offen sc1\ns_waitcnt lgkmcnt(2)\nv_swap_b32 v113,v116\nv_swap_b32 v121,v124\n" \
"v_mov_b32 v118,v115\nv_mfma_f32_16x16x32_f16 v[80:83],v[112:115],v[40:43],0\ns_waitcnt lgkmcnt(0)\nv_mov_b32 v126,v123\n" \
"s_add_u32 s97,s97,4096\nv_mfma_f32_16x16x32_f16 v[84:87],v[112:115],v[44:47],0\nv_and_b32_e64 v111,1,v0\nv_lshl_add_u32 v110,v110,1,v111\n" \
"v_mfma_f32_16x16x32_f16 v[88:91],v[120:123],v[40:43],0\nv_bfe_u32 v111,v0,3,3\nbuffer_store_dwordx4 v[104:107],v2,s[36:39],s97 offen sc1\nv_mfma_f32_16x16x32_f16 v[92:95],v[120:123],v[44:47],0\n" \
"s_add_u32 s97,s97,4096\nv_lshlrev_b32 v111,12,v111\nv_mfma_f32_16x16x32_f16 v[96:99],v[116:119],v[40:43],0\ns_lshr_b32 s26,s32,1\n" \
"s_mul_i32 s92,s26,1152\nv_pk_add_f32 v[88:89],v[80:81],v[88:89] neg_lo:[0,1] neg_hi:[0,1]\nv_pk_add_f32 v[90:91],v[82:83],v[90:91] neg_lo:[0,1] neg_hi:[0,1]\nv_pk_fma_f32 v[80:81],v[80:81],2.0,v[88:89] op_sel_hi:[1,0,1] neg_lo:[0,0,1] neg_hi:[0,0,1]\n" \
"v_pk_add_f32 v[92:93],v[84:85],v[92:93] neg_lo:[0,1] neg_hi:[0,1]\nv_pk_add_f32 v[94:95],v[86:87],v[94:95] neg_lo:[0,1] neg_hi:[0,1]\nbuffer_store_dwordx4 v[104:107],v2,s[36:39],s97 offen sc1\nv_pk_fma_f32 v[82:83],v[82:83],2.0,v[90:91] op_sel_hi:[1,0,1] neg_lo:[0,0,1] neg_hi:[0,0,1]\n" \
"v_pk_fma_f32 v[84:85],v[84:85],2.0,v[92:93] op_sel_hi:[1,0,1] neg_lo:[0,0,1] neg_hi:[0,0,1]\nv_pk_fma_f32 v[86:87],v[86:87],2.0,v[94:95] op_sel_hi:[1,0,1] neg_lo:[0,0,1] neg_hi:[0,0,1]\nv_cvt_pk_f16_f32 v112,v80,v84\nv_cvt_pk_f16_f32 v113,v81,v85\n" \
"v_cvt_pk_f16_f32 v114,v82,v86\nv_cvt_pk_f16_f32 v115,v83,v87\nv_mfma_f32_16x16x32_f16 v[100:103],v[116:119],v[44:47],0\nv_cvt_pk_f16_f32 v116,v88,v92\n" \
"v_cvt_pk_f16_f32 v117,v89,v93\nv_mfma_f32_16x16x32_f16 v[80:83],v[16:19],v[112:115],0\nbuffer_load_dwordx4 v4,s[8:11],s100 offen lds\nv_mfma_f32_16x16x32_f16 v[84:87],v[20:23],v[112:115],0\n" \
"v_cvt_pk_f16_f32 v118,v90,v94\nv_cvt_pk_f16_f32 v119,v91,v95\nv_mfma_f32_16x16x32_f16 v[104:107],v[124:127],v[40:43],0\nds_read2_b64 v[40:43],v3 offset0:4 offset1:5\n" \
"ds_read2_b64 v[112:115],v60 offset0:0 offset1:1\nv_mfma_f32_16x16x32_f16 v[88:91],v[24:27],v[116:119],0\nbuffer_load_dwordx4 v5,s[8:11],s100 offen offset:1024 lds\nv_mfma_f32_16x16x32_f16 v[92:95],v[28:31],v[116:119],0\n" \
"ds_write2_b32 v48,v84,v80 offset0:0 offset1:1\nds_write2_b32 v49,v85,v81 offset0:0 offset1:1\nds_write2_b32 v50,v86,v82 offset0:0 offset1:1\nbuffer_load_dwordx4 v6,s[8:11],s100 offen offset:2048 lds\n" \
"v_pk_add_f32 v[104:105],v[96:97],v[104:105] neg_lo:[0,1] neg_hi:[0,1]\nv_pk_add_f32 v[106:107],v[98:99],v[106:107] neg_lo:[0,1] neg_hi:[0,1]\nv_pk_fma_f32 v[96:97],v[96:97],2.0,v[104:105] op_sel_hi:[1,0,1] neg_lo:[0,0,1] neg_hi:[0,0,1]\nv_pk_fma_f32 v[98:99],v[98:99],2.0,v[106:107] op_sel_hi:[1,0,1] neg_lo:[0,0,1] neg_hi:[0,0,1]\n" \
"ds_write2_b32 v51,v87,v83 offset0:0 offset1:1\nds_write2_b32 v52,v92,v88 offset0:0 offset1:1\nbuffer_load_dwordx4 v7,s[8:11],s100 offen offset:3072 lds\nds_write2_b32 v53,v93,v89 offset0:0 offset1:1\n" \
"ds_write2_b32 v54,v94,v90 offset0:0 offset1:1\nds_write2_b32 v55,v95,v91 offset0:0 offset1:1\nv_add_u32 v48,2448,v48\ns_add_u32 m0,s57,4096\n" \
"s_add_u32 s97,s15,8192\nbuffer_load_dwordx4 v4,s[8:11],s101 offen lds\nv_add_u32 v49,2448,v49\nv_add_u32 v50,2448,v50\n" \
"v_add_u32 v51,2448,v51\nv_add_u32 v52,2448,v52\nv_add_u32 v53,2448,v53\nv_add_u32 v54,2448,v54\n" \
"v_add_u32 v55,2448,v55\nbuffer_load_dwordx4 v5,s[8:11],s101 offen offset:1024 lds\nds_read2_b64 v[116:119],v60 offset0:2 offset1:3\nv_lshl_add_u32 v4,v110,4,v111\n" \
"v_mfma_f32_16x16x32_f16 v[108:111],v[124:127],v[44:47],0\nds_read2_b64 v[44:47],v3 offset0:6 offset1:7\nv_xor_b32 v5,64,v4\nv_add_u32 v5,31744,v5\n" \
"s_lshr_b32 s26,s3,1\nbuffer_load_dwordx4 v6,s[8:11],s101 offen offset:2048 lds\nv_add_u32 v6,63488,v4\ns_lshl_b32 s26,s26,6\n" \
"s_add_u32 s92,s92,s26\nv_pk_add_f32 v[108:109],v[100:101],v[108:109] neg_lo:[0,1] neg_hi:[0,1]\nv_pk_add_f32 v[110:111],v[102:103],v[110:111] neg_lo:[0,1] neg_hi:[0,1]\nv_pk_fma_f32 v[100:101],v[100:101],2.0,v[108:109] op_sel_hi:[1,0,1] neg_lo:[0,0,1] neg_hi:[0,0,1]\n" \
"v_pk_fma_f32 v[102:103],v[102:103],2.0,v[110:111] op_sel_hi:[1,0,1] neg_lo:[0,0,1] neg_hi:[0,0,1]\nv_cvt_pk_f16_f32 v120,v96,v100\nbuffer_load_dwordx4 v7,s[8:11],s101 offen offset:3072 lds\nv_cvt_pk_f16_f32 v121,v97,v101\n" \
"v_cvt_pk_f16_f32 v122,v98,v102\nv_cvt_pk_f16_f32 v123,v99,v103\nv_cvt_pk_f16_f32 v124,v104,v108\nv_cvt_pk_f16_f32 v125,v105,v109\n" \
"v_mfma_f32_16x16x32_f16 v[96:99],v[16:19],v[120:123],0\nv_cvt_pk_f16_f32 v126,v106,v110\ns_add_u32 m0,s57,8192\ns_lshr_b32 s100,s33,3\n" \
"buffer_load_dwordx4 v1,s[36:39],s31 offen sc1 lds\nv_mfma_f32_16x16x32_f16 v[100:103],v[20:23],v[120:123],0\nv_cvt_pk_f16_f32 v127,v107,v111\nds_read2_b64 v[120:123],v60 offset0:4 offset1:5\n" \
"v_add_u32 v7,63488,v5\nv_mfma_f32_16x16x32_f16 v[104:107],v[24:27],v[124:127],0\ns_lshl_b32 s26,s3,8\ns_add_u32 s92,s92,s26\n" \
"buffer_load_dwordx4 v1,s[36:39],s31 offen offset:1024 sc1 lds\nv_mfma_f32_16x16x32_f16 v[108:111],v[28:31],v[124:127],0\nds_write2_b32 v48,v100,v96 offset0:0 offset1:1\nds_write2_b32 v49,v101,v97 offset0:0 offset1:1\n" \
"ds_write2_b32 v50,v102,v98 offset0:0 offset1:1\nbuffer_load_dwordx4 v1,s[36:39],s31 offen offset:2048 sc1 lds\nds_write2_b32 v51,v103,v99 offset0:0 offset1:1\nds_read2_b64 v[124:127],v60 offset0:6 offset1:7\n" \
"ds_read_b64 v[98:99],v60 offset:64\nv_add_u32 v60,2304,v60\nds_write2_b32 v52,v108,v104 offset0:0 offset1:1\nds_write2_b32 v53,v109,v105 offset0:0 offset1:1\n" \
"buffer_load_dwordx4 v1,s[36:39],s31 offen offset:3072 sc1 lds\nds_write2_b32 v54,v110,v106 offset0:0 offset1:1\nds_write2_b32 v55,v111,v107 offset0:0 offset1:1\nds_read2_b64 v[80:83],v13 offset0:0 offset1:1\n" \
"ds_read2_b64 v[84:87],v13 offset0:2 offset1:3\ns_add_u32 m0,s57,12288\ns_lshl_b32 s92,s92,4\nbuffer_load_dwordx4 v1,s[36:39],s96 offen sc1 lds\n" \
"ds_read2_b64 v[88:91],v13 offset0:4 offset1:5\ns_and_b32 s26,s32,1\nds_read2_b64 v[92:95],v13 offset0:6 offset1:7\ns_lshl3_add_u32 s92,s26,s92\n" \
"s_and_b32 s26,s33,7\ns_add_u32 s92,s92,s26\ns_lshl_b32 s92,s92,8\ns_add_u32 s100,s100,0\n" \
"s_sub_u32 s101,s100,4\nbuffer_load_dwordx4 v1,s[36:39],s96 offen offset:1024 sc1 lds\nds_read_b64 v[96:97],v13 offset:64\nds_read2_b64 v[16:19],v60 offset0:0 offset1:1\n" \
"ds_read2_b64 v[20:23],v60 offset0:2 offset1:3\ns_waitcnt lgkmcnt(6)\nv_pk_fma_f32 v[62:63],v[80:81],v[32:33],v[62:63]\nv_pk_fma_f32 v[64:65],v[82:83],v[34:35],v[64:65]\n" \
"v_pk_mul_f32 v[102:103],v[62:63],v[62:63]\nv_pk_add_f32 v[100:101],v[62:63],v[64:65]\nv_pk_fma_f32 v[102:103],v[64:65],v[64:65],v[102:103]\nbuffer_load_dwordx4 v1,s[36:39],s96 offen offset:2048 sc1 lds\n" \
"s_waitcnt lgkmcnt(4)\nv_pk_fma_f32 v[66:67],v[84:85],v[36:37],v[66:67]\nv_pk_fma_f32 v[68:69],v[86:87],v[38:39],v[68:69]\nv_pk_add_f32 v[100:101],v[100:101],v[66:67]\n" \
"v_pk_fma_f32 v[102:103],v[66:67],v[66:67],v[102:103]\nv_pk_fma_f32 v[70:71],v[88:89],v[40:41],v[70:71]\nv_pk_add_f32 v[100:101],v[100:101],v[68:69]\nv_pk_fma_f32 v[102:103],v[68:69],v[68:69],v[102:103]\n" \
"buffer_load_dwordx4 v1,s[36:39],s96 offen offset:3072 sc1 lds\nv_pk_fma_f32 v[72:73],v[90:91],v[42:43],v[72:73]\nv_pk_add_f32 v[100:101],v[100:101],v[70:71]\nv_pk_fma_f32 v[102:103],v[70:71],v[70:71],v[102:103]\n" \
"s_waitcnt lgkmcnt(1)\nv_pk_fma_f32 v[74:75],v[92:93],v[44:45],v[74:75]\nv_pk_add_f32 v[100:101],v[100:101],v[72:73]\nv_pk_fma_f32 v[102:103],v[72:73],v[72:73],v[102:103]\n" \
"v_pk_fma_f32 v[76:77],v[94:95],v[46:47],v[76:77]\ns_add_u32 m0,s57,16384\ns_min_u32 s100,s100,s101\nbuffer_load_dwordx4 v1,s[36:39],s97 offen sc1 lds\n" \
"v_pk_add_f32 v[100:101],v[100:101],v[74:75]\nv_pk_fma_f32 v[102:103],v[74:75],v[74:75],v[102:103]\nv_pk_fma_f32 v[78:79],v[96:97],v[56:57],v[78:79]\nv_pk_add_f32 v[100:101],v[100:101],v[76:77]\n" \
"v_pk_fma_f32 v[102:103],v[76:77],v[76:77],v[102:103]\nv_pk_add_f32 v[100:101],v[100:101],v[78:79]\nv_pk_fma_f32 v[102:103],v[78:79],v[78:79],v[102:103]\nbuffer_load_dwordx4 v1,s[36:39],s97 offen offset:1024 sc1 lds\n" \
"ds_read2_b64 v[24:27],v60 offset0:4 offset1:5\nv_add_f32 v100,v100,v101\nv_add_f32 v102,v102,v103\nds_read2_b64 v[28:31],v60 offset0:6 offset1:7\n" \
"ds_read_b64 v[58:59],v60 offset:64\nv_permlane16_swap_b32 v100,v102\nv_add_f32 v100,v100,v102\ns_waitcnt vmcnt(14)\n" \
"ds_read_b64_tr_b16 a[224:225],v8\nbuffer_load_dwordx4 v1,s[36:39],s97 offen offset:2048 sc1 lds\nv_add_f32_dpp v100,v100,v100 quad_perm:[1,0,3,2] row_mask:0xf bank_mask:0xf bound_ctrl:1\nds_read_b64_tr_b16 a[228:229],v9\n" \
"ds_read_b64_tr_b16 a[232:233],v10\nv_add_f32_dpp v100,v100,v100 quad_perm:[2,3,0,1] row_mask:0xf bank_mask:0xf bound_ctrl:1\nds_read_b64_tr_b16 a[236:237],v11\nds_read_b64_tr_b16 a[240:241],v8 offset:2048\n" \
"v_add_f32_dpp v100,v100,v100 row_half_mirror row_mask:0xf bank_mask:0xf bound_ctrl:1\nds_read_b64_tr_b16 a[244:245],v9 offset:2048\nbuffer_load_dwordx4 v1,s[36:39],s97 offen offset:3072 sc1 lds\nv_add_f32_dpp v100,v100,v100 row_mirror row_mask:0xf bank_mask:0xf bound_ctrl:1\n" \
"v_mul_f32 v104,0x3ae38e39,v100\nv_mul_f32 v102,0x3ae38e39,v100\nds_read_b64_tr_b16 a[248:249],v10 offset:2048\nds_read_b64_tr_b16 a[252:253],v11 offset:2048\n" \
"v_permlane16_swap_b32 v104,v102\nv_fma_f32 v102,-v104,v104,v102\ns_add_u32 m0,s57,20480\ns_lshl_b32 s101,s100,12\n" \
"buffer_load_dwordx4 v1,s[36:39],s95 offen sc1 lds\nv_max_f32 v102,0,v102\nv_add_f32 v102,0x3727c5ac,v102\nv_rsq_f32 v100,v102\n" \
"s_waitcnt vmcnt(13)\nds_read_b64_tr_b16 a[226:227],v8 offset:4096\nv_mul_f32 v101,-v104,v100\nv_pk_fma_f32 v[80:81],v[62:63],v[100:101],v[100:101] op_sel:[0,0,1] op_sel_hi:[1,0,1]\n" \
"v_pk_fma_f32 v[82:83],v[64:65],v[100:101],v[100:101] op_sel:[0,0,1] op_sel_hi:[1,0,1]\nbuffer_load_dwordx4 v1,s[36:39],s95 offen offset:1024 sc1 lds\nv_pk_fma_f32 v[84:85],v[66:67],v[100:101],v[100:101] op_sel:[0,0,1] op_sel_hi:[1,0,1]\nv_pk_fma_f32 v[86:87],v[68:69],v[100:101],v[100:101] op_sel:[0,0,1] op_sel_hi:[1,0,1]\n" \
"v_pk_fma_f32 v[88:89],v[70:71],v[100:101],v[100:101] op_sel:[0,0,1] op_sel_hi:[1,0,1]\nv_pk_fma_f32 v[90:91],v[72:73],v[100:101],v[100:101] op_sel:[0,0,1] op_sel_hi:[1,0,1]\nv_pk_fma_f32 v[92:93],v[74:75],v[100:101],v[100:101] op_sel:[0,0,1] op_sel_hi:[1,0,1]\nv_pk_fma_f32 v[94:95],v[76:77],v[100:101],v[100:101] op_sel:[0,0,1] op_sel_hi:[1,0,1]\n" \
"v_pk_fma_f32 v[96:97],v[78:79],v[100:101],v[100:101] op_sel:[0,0,1] op_sel_hi:[1,0,1]\nbuffer_load_dwordx4 v1,s[36:39],s95 offen offset:2048 sc1 lds\nds_read_b64_tr_b16 a[230:231],v9 offset:4096\nds_read_b64_tr_b16 a[234:235],v10 offset:4096\n" \
"ds_read_b64_tr_b16 a[238:239],v11 offset:4096\nds_read_b64_tr_b16 a[242:243],v8 offset:6144\nds_read_b64_tr_b16 a[246:247],v9 offset:6144\nds_read_b64_tr_b16 a[250:251],v10 offset:6144\n" \
"ds_read_b64_tr_b16 a[254:255],v11 offset:6144\ns_waitcnt vmcnt(11)\nds_read_b64_tr_b16 v[192:193],v15 offset:8192\nds_read_b64_tr_b16 v[196:197],v15 offset:8704\n" \
"buffer_load_dwordx4 v1,s[36:39],s95 offen offset:3072 sc1 lds\nds_read_b64_tr_b16 v[194:195],v15 offset:9216\nds_read_b64_tr_b16 v[198:199],v15 offset:9728\nds_read_b64_tr_b16 v[200:201],v15 offset:10240\n" \
"ds_read_b64_tr_b16 v[204:205],v15 offset:10752\nds_read_b64_tr_b16 v[202:203],v15 offset:11264\nds_read_b64_tr_b16 v[206:207],v15 offset:11776\ns_add_u32 s101,s101,s57\n" \
"s_add_u32 m0,s101,0\ns_and_b32 s101,s100,1\ns_lshr_b32 s100,s100,1\ns_lshl_b32 s100,s100,10\n" \
"s_add_u32 s100,s100,s101\ns_lshl_b32 s100,s100,7\ns_add_u32 s100,s100,s92\ns_lshr_b32 s31,s33,3\n" \
"s_add_u32 s31,s31,1\ns_sub_u32 s96,s31,4\ns_min_u32 s31,s31,s96\ns_lshl_b32 s96,s31,12\n" \
"s_add_u32 s96,s96,s57\ns_lshr_b32 s97,s33,3\ns_add_u32 s97,s97,2\ns_sub_u32 s95,s97,4\n" \
"s_min_u32 s97,s97,s95\ns_lshl_b32 s95,s97,12\ns_add_u32 s95,s95,s57\nv_readlane_b32 s24,v112,0\n" \
"v_mov_b32 v60,v61\ns_mov_b32 s25,s24\nv_pk_add_f32 v[112:113],v[112:113],s[24:25] neg_lo:[0,1] neg_hi:[0,1]\nv_pk_fma_f32 v[80:81],v[80:81],v[16:17],v[112:113]\n" \
"v_pk_mul_f32 v[102:103],v[80:81],v[80:81]\nv_pk_add_f32 v[114:115],v[114:115],s[24:25] neg_lo:[0,1] neg_hi:[0,1]\nv_pk_fma_f32 v[82:83],v[82:83],v[18:19],v[114:115]\nv_pk_add_f32 v[100:101],v[80:81],v[82:83]\n" \
"v_pk_fma_f32 v[102:103],v[82:83],v[82:83],v[102:103]\nv_pk_add_f32 v[116:117],v[116:117],s[24:25] neg_lo:[0,1] neg_hi:[0,1]\ns_waitcnt lgkmcnt(0)\nv_pk_fma_f32 v[84:85],v[84:85],v[20:21],v[116:117]\n" \
"v_pk_add_f32 v[100:101],v[100:101],v[84:85]\nv_pk_fma_f32 v[102:103],v[84:85],v[84:85],v[102:103]\nv_pk_add_f32 v[118:119],v[118:119],s[24:25] neg_lo:[0,1] neg_hi:[0,1]\nv_pk_fma_f32 v[86:87],v[86:87],v[22:23],v[118:119]\n" \
"v_pk_add_f32 v[100:101],v[100:101],v[86:87]\nv_pk_fma_f32 v[102:103],v[86:87],v[86:87],v[102:103]\nv_pk_add_f32 v[120:121],v[120:121],s[24:25] neg_lo:[0,1] neg_hi:[0,1]\nv_pk_fma_f32 v[88:89],v[88:89],v[24:25],v[120:121]\n" \
"v_pk_add_f32 v[100:101],v[100:101],v[88:89]\nv_pk_fma_f32 v[102:103],v[88:89],v[88:89],v[102:103]\nv_pk_add_f32 v[122:123],v[122:123],s[24:25] neg_lo:[0,1] neg_hi:[0,1]\nv_pk_fma_f32 v[90:91],v[90:91],v[26:27],v[122:123]\n" \
"v_pk_add_f32 v[100:101],v[100:101],v[90:91]\nv_pk_fma_f32 v[102:103],v[90:91],v[90:91],v[102:103]\ns_waitcnt vmcnt(8)\nds_read_b64_tr_b16 v[208:209],v15 offset:12288\n" \
"ds_read_b64_tr_b16 v[212:213],v15 offset:12800\nds_read_b64_tr_b16 v[210:211],v15 offset:13312\nds_read_b64_tr_b16 v[214:215],v15 offset:13824\nds_read_b64_tr_b16 v[216:217],v15 offset:14336\n" \
"ds_read_b64_tr_b16 v[220:221],v15 offset:14848\nds_read_b64_tr_b16 v[218:219],v15 offset:15360\nds_read_b64_tr_b16 v[222:223],v15 offset:15872\ns_waitcnt lgkmcnt(0)\n" \
"buffer_load_dwordx4 v4,s[40:43],s100 offen lds\nv_pk_add_f32 v[124:125],v[124:125],s[24:25] neg_lo:[0,1] neg_hi:[0,1]\nv_pk_fma_f32 v[92:93],v[92:93],v[28:29],v[124:125]\nv_pk_add_f32 v[100:101],v[100:101],v[92:93]\n" \
"v_pk_fma_f32 v[102:103],v[92:93],v[92:93],v[102:103]\nv_pk_add_f32 v[126:127],v[126:127],s[24:25] neg_lo:[0,1] neg_hi:[0,1]\nv_pk_fma_f32 v[94:95],v[94:95],v[30:31],v[126:127]\nv_pk_add_f32 v[100:101],v[100:101],v[94:95]\n" \
"buffer_load_dwordx4 v5,s[40:43],s100 offen offset:1024 lds\nv_pk_fma_f32 v[102:103],v[94:95],v[94:95],v[102:103]\nv_pk_add_f32 v[98:99],v[98:99],s[24:25] neg_lo:[0,1] neg_hi:[0,1]\nv_pk_fma_f32 v[96:97],v[96:97],v[58:59],v[98:99]\n" \
"v_pk_add_f32 v[100:101],v[100:101],v[96:97]\nv_pk_fma_f32 v[102:103],v[96:97],v[96:97],v[102:103]\nv_add_f32 v100,v100,v101\nv_add_f32 v102,v102,v103\n" \
"v_pk_add_f32 v[62:63],v[62:63],v[60:61] op_sel_hi:[1,0]\nbuffer_load_dwordx4 v6,s[40:43],s100 offen offset:2048 lds\nv_permlane16_swap_b32 v100,v102\nv_add_f32_e64 v100,v100,v102\n" \
"v_cvt_pk_bf16_f32 v32,v62,v63\nv_pk_add_f32 v[64:65],v[64:65],v[60:61] op_sel_hi:[1,0]\nv_add_f32_dpp v100,v100,v100 quad_perm:[1,0,3,2] row_mask:0xf bank_mask:0xf bound_ctrl:1\nv_cvt_pk_bf16_f32 v33,v64,v65\n" \
"v_pk_add_f32 v[66:67],v[66:67],v[60:61] op_sel_hi:[1,0]\nbuffer_load_dwordx4 v7,s[40:43],s100 offen offset:3072 lds\nv_add_f32_dpp v100,v100,v100 quad_perm:[2,3,0,1] row_mask:0xf bank_mask:0xf bound_ctrl:1\ns_add_u32 m0,s96,0\n" \
"s_and_b32 s96,s31,1\nv_add_f32_dpp v100,v100,v100 row_half_mirror row_mask:0xf bank_mask:0xf bound_ctrl:1\ns_lshr_b32 s31,s31,1\ns_lshl_b32 s31,s31,10\n" \
"v_add_f32_dpp v100,v100,v100 row_mirror row_mask:0xf bank_mask:0xf bound_ctrl:1\nv_mul_f32 v104,0x3ae38e39,v100\ns_add_u32 s31,s31,s96\ns_lshl_b32 s31,s31,7\n" \
"s_add_u32 s31,s31,s92\nbuffer_load_dwordx4 v4,s[40:43],s31 offen lds\nv_mul_f32 v102,0x3ae38e39,v100\ns_lshr_b32 s100,s33,3\n" \
"s_add_u32 s100,s100,3\nv_permlane16_swap_b32 v104,v102\nv_fma_f32 v102,-v104,v104,v102\nv_max_f32 v102,0,v102\n" \
"v_add_f32 v102,0x3727c5ac,v102\nbuffer_load_dwordx4 v5,s[40:43],s31 offen offset:1024 lds\nv_rsq_f32 v100,v102\ns_sub_u32 s101,s100,4\n" \
"v_mul_f32 v101,-v104,v100\nv_pk_fma_f32 v[80:81],v[80:81],v[100:101],v[100:101] op_sel:[0,0,1] op_sel_hi:[1,0,1]\nv_pk_fma_f32 v[82:83],v[82:83],v[100:101],v[100:101] op_sel:[0,0,1] op_sel_hi:[1,0,1]\nv_pk_fma_f32 v[84:85],v[84:85],v[100:101],v[100:101] op_sel:[0,0,1] op_sel_hi:[1,0,1]\n" \
"v_pk_fma_f32 v[86:87],v[86:87],v[100:101],v[100:101] op_sel:[0,0,1] op_sel_hi:[1,0,1]\nbuffer_load_dwordx4 v6,s[40:43],s31 offen offset:2048 lds\nv_cvt_pk_bf16_f32 v16,v80,v81\nv_cvt_pk_bf16_f32 v17,v82,v83\n" \
"v_cvt_pk_bf16_f32 v18,v84,v85\nv_cvt_pk_bf16_f32 v19,v86,v87\nv_pk_fma_f32 v[88:89],v[88:89],v[100:101],v[100:101] op_sel:[0,0,1] op_sel_hi:[1,0,1]\nv_pk_fma_f32 v[90:91],v[90:91],v[100:101],v[100:101] op_sel:[0,0,1] op_sel_hi:[1,0,1]\n" \
"v_pk_fma_f32 v[92:93],v[92:93],v[100:101],v[100:101] op_sel:[0,0,1] op_sel_hi:[1,0,1]\nbuffer_load_dwordx4 v7,s[40:43],s31 offen offset:3072 lds\nv_pk_fma_f32 v[94:95],v[94:95],v[100:101],v[100:101] op_sel:[0,0,1] op_sel_hi:[1,0,1]\nv_cvt_pk_bf16_f32 v20,v88,v89\n" \
"v_cvt_pk_bf16_f32 v21,v90,v91\nv_cvt_pk_bf16_f32 v22,v92,v93\nv_cvt_pk_bf16_f32 v23,v94,v95\nv_pk_fma_f32 v[96:97],v[96:97],v[100:101],v[100:101] op_sel:[0,0,1] op_sel_hi:[1,0,1]\n" \
"v_cvt_pk_bf16_f32 v24,v96,v97\ns_add_u32 m0,s95,0\ns_and_b32 s95,s97,1\ns_lshr_b32 s97,s97,1\n" \
"s_lshl_b32 s97,s97,10\ns_add_u32 s97,s97,s95\ns_lshl_b32 s97,s97,7\ns_add_u32 s97,s97,s92\n" \
"buffer_load_dwordx4 v4,s[40:43],s97 offen lds\ns_min_u32 s100,s100,s101\ns_lshl_b32 s101,s100,12\ns_add_u32 s101,s101,s57\n" \
"v_cvt_pk_bf16_f32 v34,v66,v67\nv_pk_add_f32 v[68:69],v[68:69],v[60:61] op_sel_hi:[1,0]\nv_cvt_pk_bf16_f32 v35,v68,v69\nv_pk_add_f32 v[70:71],v[70:71],v[60:61] op_sel_hi:[1,0]\n" \
"v_cvt_pk_bf16_f32 v36,v70,v71\nbuffer_load_dwordx4 v5,s[40:43],s97 offen offset:1024 lds\nv_pk_add_f32 v[72:73],v[72:73],v[60:61] op_sel_hi:[1,0]\nv_cvt_pk_bf16_f32 v37,v72,v73\n" \
"v_pk_add_f32 v[74:75],v[74:75],v[60:61] op_sel_hi:[1,0]\nv_cvt_pk_bf16_f32 v38,v74,v75\nv_pk_add_f32 v[76:77],v[76:77],v[60:61] op_sel_hi:[1,0]\nv_cvt_pk_bf16_f32 v39,v76,v77\n" \
"v_pk_add_f32 v[78:79],v[78:79],v[60:61] op_sel_hi:[1,0]\nbuffer_load_dwordx4 v6,s[40:43],s97 offen offset:2048 lds\nv_cvt_pk_bf16_f32 v40,v78,v79\nv_and_b32 v48,31,v0\n" \
"v_mad_u32_u24 v49,v48,56,0\nv_sub_u32 v48,v13,v49\nds_write_b128 v48,v[16:19]\nbuffer_load_dwordx4 v7,s[40:43],s97 offen offset:3072 lds\n" \
"ds_write_b128 v48,v[20:23] offset:512\ns_add_u32 m0,s101,0\ns_and_b32 s101,s100,1\ns_lshr_b32 s100,s100,1\n" \
"s_lshl_b32 s100,s100,10\ns_add_u32 s100,s100,s101\ns_lshl_b32 s100,s100,7\ns_add_u32 s100,s100,s92\n" \
"buffer_load_dwordx4 v4,s[40:43],s100 offen lds\nv_and_b32 v49,31,v0\nv_mul_u32_u24 v49,68,v49\nv_sub_u32 v49,v13,v49\n" \
"ds_write_b32 v49,v24 offset:1024\nv_bfe_u32 v50,v0,2,2\nv_mul_u32_u24 v50,4896,v50\nv_bfe_u32 v51,v0,4,2\n" \
"v_lshl_add_u32 v51,v51,2,s3\nbuffer_load_dwordx4 v5,s[40:43],s100 offen offset:1024 lds\nv_lshl_add_u32 v50,v51,5,v50\nv_and_b32 v52,3,v0\n" \
"v_lshl_add_u32 v50,v52,3,v50\nv_and_b32 v52,0xf0,v1\nv_lshl_add_u32 v51,v51,16,v52\nv_bfrev_b32 v52,1\n" \
"v_cmp_gt_u32 vcc,256,v1\nv_and_b32 v53,31,v0\nbuffer_load_dwordx4 v6,s[40:43],s100 offen offset:2048 lds\ns_waitcnt lgkmcnt(0)\n" \
"s_barrier\nds_read_b64_tr_b16 v[112:113],v50 offset:9216\nds_read_b64_tr_b16 v[114:115],v50 offset:11664\nds_read_b64_tr_b16 v[116:117],v50 offset:9728\n" \
"ds_read_b64_tr_b16 v[118:119],v50 offset:12176\nds_read_b64_tr_b16 v[120:121],v50 offset:10240\nds_read_b64_tr_b16 v[122:123],v50 offset:12688\nbuffer_load_dwordx4 v7,s[40:43],s100 offen offset:3072 lds\n" \
"v_cndmask_b32 v52,v52,v51,vcc\nv_mul_u32_u24 v53,12,v53\nv_sub_u32 v53,v12,v53\ns_lshl_b32 s24,s34,8\n" \
"s_add_u32 s24,s24,9764864\ns_add_u32 s25,s24,1048576\ns_add_u32 s26,s24,2097152\ns_add_u32 s13,s13,7405568\n" \
"s_load_dwordx2 s[4:5],s[0:1],56\ns_load_dwordx2 s[16:17],s[0:1],64\ns_waitcnt lgkmcnt(0)\nbuffer_store_dwordx4 v[112:115],v51,s[36:39],s24 offen sc1\n" \
"buffer_store_dwordx4 v[116:119],v51,s[36:39],s25 offen sc1\nbuffer_store_dwordx4 v[120:123],v52,s[36:39],s26 offen sc1\nbuffer_store_dwordx4 v[32:35],v12,s[36:39],s13 offen sc1\nbuffer_store_dwordx4 v[36:39],v12,s[36:39],s13 offen offset:512 sc1\n" \
"buffer_store_dword v40,v53,s[36:39],s13 offen offset:1024 sc1\ns_lshr_b32 s31,s33,3\ns_lshl1_add_u32 s31,s31,0\ns_sub_u32 s96,s31,7\n" \
"s_min_u32 s31,s31,s96\ns_lshl_b32 s96,s31,12\ns_add_u32 s96,s96,s57\ns_add_u32 m0,s96,0\n" \
"s_and_b32 s96,s31,1\ns_lshr_b32 s31,s31,1\ns_lshl_b32 s31,s31,10\ns_add_u32 s31,s31,s96\n" \
"s_lshl_b32 s31,s31,7\ns_add_u32 s31,s31,s92\ns_add_u32 s31,s31,262144\ns_lshr_b32 s97,s33,3\n" \
"s_lshl1_add_u32 s97,s97,1\ns_sub_u32 s95,s97,7\ns_min_u32 s97,s97,s95\ns_lshl_b32 s95,s97,12\n" \
"s_add_u32 s95,s95,s57\ns_lshr_b32 s100,s33,3\ns_lshl1_add_u32 s100,s100,2\ns_sub_u32 s101,s100,7\n" \
"s_min_u32 s100,s100,s101\ns_lshl_b32 s101,s100,12\ns_add_u32 s101,s101,s57\nv_and_b32 v64,63,v0\n" \
"s_and_b32 s25,s33,7\nv_lshrrev_b32 v66,5,v64\nv_and_b32 v67,1,v0\ns_lshl2_add_u32 s25,s25,s3\n" \
"s_and_b32 s26,s33,6\ns_lshr_b32 s27,s3,1\nv_lshl_add_u32 v66,v66,1,v67\ns_mul_i32 s24,s32,1152\n" \
"s_lshl_b32 s25,s25,5\ns_add_u32 s26,s26,s27\ns_mov_b32 s6,9216\ns_mov_b32 s7,0x20000\n" \
"v_lshlrev_b32 v68,3,v66\ns_add_u32 s25,s25,s24\nv_bfe_u32 v72,v0,1,1\nv_lshrrev_b32 v74,2,v64\n" \
"s_lshl4_add_u32 s26,s26,s24\nv_and_b32 v73,12,v0\nv_lshrrev_b32 v71,4,v64\nv_and_b32 v69,7,v0\n" \
"buffer_load_dwordx2 v[80:81],v68,s[4:7],s25 offen\nv_lshl_add_u32 v72,v72,2,s21\nv_and_b32 v74,12,v74\nv_add_u32 v71,v71,v73\n" \
"s_lshl_b32 s24,s3,4\nv_lshlrev_b32 v70,1,v69\nv_lshl_add_u32 v72,v72,4,v74\ns_mov_b32 s15,s19\n" \
"v_add_u32 v71,s24,v71\nbuffer_load_ushort v82,v70,s[4:7],s26 offen offset:1024\ns_mov_b32 s18,4096\ns_mov_b32 s19,0x20000\n" \
"v_lshlrev_b32 v74,1,v71\ns_lshl_b32 s24,s22,8\nbuffer_load_ushort v83,v74,s[16:19],s24 offen\nv_add_u32 v72,s3,v72\n" \
"buffer_load_ushort v84,v74,s[16:19],s24 offen offset:128\nv_lshlrev_b32_e64 v75,1,v72\nbuffer_load_ushort v85,v75,s[16:19],s24 offen\nv_lshl_add_u32 v73,v67,1,v73\n" \
"v_mul_u32_u24 v91,1152,v72\nv_lshl_add_u32 v91,v73,1,v91\nv_lshlrev_b32_e64 v73,2,v73\nv_add_u32 v73,163712,v73\n" \
"ds_read_b64 v[86:87],v73\ns_mul_i32 s24,s22,147456\ns_mul_i32 s25,s20,288\nv_bfe_u32 v58,v0,1,1\n" \
"s_add_u32 s24,s24,s25\nv_mul_u32_u24 v58,544,v58\nv_bfe_u32 v75,v0,2,1\ns_lshl_b32 s25,s21,6\n" \
"v_lshl_add_u32 v58,v75,8,v58\ns_add_u32 s25,s25,s24\nv_bfe_u32 v75,v0,3,2\ns_add_u32 s24,s24,256\n" \
"v_lshl_add_u32 v58,v75,6,v58\nv_add_u32 v91,s24,v91\nv_mul_u32_u24 v90,1152,v71\nv_and_b32 v74,3,v0\n" \
"v_lshl_add_u32 v58,v66,4,v58\ns_mul_i32 s24,s3,1056\nv_lshl_add_u32 v90,v74,3,v90\nv_add_u32 v58,s24,v58\n" \
"v_add_u32 v90,s25,v90\ns_and_b32 s24,s3,1\nv_mul_u32_u24 v60,288,v75\ns_lshl_b32 s24,s24,2\n" \
"s_lshr_b32 s25,s3,1\nv_lshrrev_b32 v75,5,v64\ns_add_u32 s24,s24,s25\nv_lshl_add_u32 v60,v75,6,v60\n" \
"s_lshl_b32 s24,s24,5\nv_lshl_add_u32 v60,v69,2,v60\ns_add_u32 s24,s24,4224\nv_add_u32 v60,s24,v60\n" \
"s_bfe_u32 s24,s33,0x10001\ns_add_u32 s24,s24,s15\ns_add_u32 s24,s24,17\ns_lshl2_add_u32 s24,s24,s3\n" \
"s_waitcnt lgkmcnt(0)\nv_lshrrev_b32 v86,16,v86\ns_lshl_b32 s24,s24,7\nv_mov_b32 v92,v4\n" \
"v_mov_b32 v93,v5\nv_add_u32 v74,s57,v1\nv_or_b32 v95,v86,v87\nv_lshl_add_u32 v62,v64,1,s24\n" \
"v_bfrev_b32 v76,1\nv_cmp_eq_u32 vcc,0,v64\ns_lshl2_add_u32 s24,s34,s3\ns_lshl2_add_u32 s24,s24,65536\n" \
"v_cndmask_b32 v76,v76,0,vcc\nv_mov_b32 v77,1\ns_barrier\nv_mov_b32 v57,0xc0135761\n" \
"v_add_u32 v59,0x80000,v1\nv_lshl_add_u32 v61,v64,3,s23\nv_mov_b32 v63,0\ns_waitcnt vmcnt(31)\n" \
"ds_read_b64_tr_b16 v[224:225],v15 offset:16384\nds_read_b64_tr_b16 v[228:229],v15 offset:16896\nds_read_b64_tr_b16 v[226:227],v15 offset:17408\nds_read_b64_tr_b16 v[230:231],v15 offset:17920\n" \
"ds_read_b64_tr_b16 v[232:233],v15 offset:18432\nds_read_b64_tr_b16 v[236:237],v15 offset:18944\nds_read_b64_tr_b16 v[234:235],v15 offset:19456\nds_read_b64_tr_b16 v[238:239],v15 offset:19968\n" \
"s_waitcnt vmcnt(27)\nds_read_b64_tr_b16 v[240:241],v15 offset:20480\nds_read_b64_tr_b16 v[244:245],v15 offset:20992\nds_read_b64_tr_b16 v[242:243],v15 offset:21504\n" \
"ds_read_b64_tr_b16 v[246:247],v15 offset:22016\nds_read_b64_tr_b16 v[248:249],v15 offset:22528\nv_bfe_u32 v100,v0,4,2\nds_read_b64_tr_b16 v[252:253],v15 offset:23040\n" \
"ds_read_b64_tr_b16 v[250:251],v15 offset:23552\nv_bfe_u32 v101,v0,2,2\nv_lshl_add_u32 v100,v100,3,v101\nv_bfe_u32 v101,v0,3,2\n" \
"v_lshl_add_u32 v100,v100,2,v101\nv_and_b32 v101,3,v0\nv_lshl_add_u32 v100,v100,2,v101\nv_lshlrev_b32 v100,3,v100\n" \
"v_xor_b32 v8,0,v100\nv_add_u32 v8,s57,v8\nv_mov_b32 v88,v8\ns_waitcnt vmcnt(5)\n" \
"buffer_store_dword v77,v76,s[36:39],s24 offen sc1\nds_read_b64_tr_b16 v[254:255],v15 offset:24064\nv_xor_b32 v9,32,v100\nv_add_u32 v9,s57,v9\n" \
"v_xor_b32 v10,64,v100\nv_add_u32 v10,s57,v10\nv_xor_b32 v11,96,v100\nv_add_u32 v11,s57,v11\n" \
"s_waitcnt vmcnt(5)\nv_and_b32 v53,0xffff0000,v80\nv_and_b32 v55,0xffff0000,v81\nv_lshlrev_b32 v52,16,v80\n" \
"v_lshlrev_b32 v54,16,v81\ns_waitcnt vmcnt(4)\nv_lshlrev_b32 v56,16,v82\ns_waitcnt vmcnt(2)\n" \
"v_lshl_or_b32 v89,v84,16,v83\nds_write_b128 v74,v[88:91] offset:28672\ns_waitcnt vmcnt(1)\nv_lshlrev_b32_e64 v94,16,v85\n" \
"ds_write_b128 v74,v[92:95] offset:29696\nds_read_b64_tr_b16 v[128:129],v8\nds_read_b64_tr_b16 v[130:131],v8 offset:512\nds_read_b64_tr_b16 v[132:133],v9\n" \
"ds_read_b64_tr_b16 v[134:135],v9 offset:512\nds_read_b64_tr_b16 v[136:137],v10\nds_read_b64_tr_b16 v[138:139],v10 offset:512\nds_read_b64_tr_b16 v[140:141],v11\n" \
"ds_read_b64_tr_b16 v[142:143],v11 offset:512\nds_read_b64_tr_b16 v[144:145],v8 offset:4096\nds_read_b64_tr_b16 v[146:147],v8 offset:4608\nds_read_b64_tr_b16 v[148:149],v9 offset:4096\n" \
"ds_read_b64_tr_b16 v[150:151],v9 offset:4608\nds_read_b64_tr_b16 v[152:153],v10 offset:4096\nds_read_b64_tr_b16 v[154:155],v10 offset:4608\nds_read_b64_tr_b16 v[156:157],v11 offset:4096\n" \
"ds_read_b64_tr_b16 v[158:159],v11 offset:4608\nds_read_b64_tr_b16 v[160:161],v8 offset:8192\nds_read_b64_tr_b16 v[162:163],v8 offset:8704\nds_read_b64_tr_b16 v[164:165],v9 offset:8192\n" \
"ds_read_b64_tr_b16 v[166:167],v9 offset:8704\nds_read_b64_tr_b16 v[168:169],v10 offset:8192\nds_read_b64_tr_b16 v[170:171],v10 offset:8704\nds_read_b64_tr_b16 v[172:173],v11 offset:8192\n" \
"ds_read_b64_tr_b16 v[174:175],v11 offset:8704\nds_read_b64_tr_b16 v[176:177],v8 offset:12288\nds_read_b64_tr_b16 v[178:179],v8 offset:12800\nds_read_b64_tr_b16 v[180:181],v9 offset:12288\n" \
"ds_read_b64_tr_b16 v[182:183],v9 offset:12800\nds_read_b64_tr_b16 v[184:185],v10 offset:12288\nds_read_b64_tr_b16 v[186:187],v10 offset:12800\nds_read_b64_tr_b16 v[188:189],v11 offset:12288\n" \
"ds_read_b64_tr_b16 v[190:191],v11 offset:12800\ns_waitcnt lgkmcnt(0)\nbuffer_load_dwordx4 v4,s[40:43],s31 offen lds\nbuffer_load_dwordx4 v5,s[40:43],s31 offen offset:1024 lds\n" \
"buffer_load_dwordx4 v6,s[40:43],s31 offen offset:2048 lds\nbuffer_load_dwordx4 v7,s[40:43],s31 offen offset:3072 lds\ns_add_u32 m0,s95,0\ns_and_b32 s95,s97,1\n" \
"s_lshr_b32 s97,s97,1\ns_lshl_b32 s97,s97,10\ns_add_u32 s97,s97,s95\ns_lshl_b32 s97,s97,7\n" \
"s_add_u32 s97,s97,s92\ns_add_u32 s97,s97,262144\nbuffer_load_dwordx4 v4,s[40:43],s97 offen lds\nbuffer_load_dwordx4 v5,s[40:43],s97 offen offset:1024 lds\n" \
"buffer_load_dwordx4 v6,s[40:43],s97 offen offset:2048 lds\nbuffer_load_dwordx4 v7,s[40:43],s97 offen offset:3072 lds\ns_add_u32 m0,s101,0\ns_and_b32 s101,s100,1\n" \
"s_lshr_b32 s100,s100,1\ns_lshl_b32 s100,s100,10\ns_add_u32 s100,s100,s101\ns_lshl_b32 s100,s100,7\n" \
"s_add_u32 s100,s100,s92\ns_add_u32 s100,s100,262144\nbuffer_load_dwordx4 v4,s[40:43],s100 offen lds\nbuffer_load_dwordx4 v5,s[40:43],s100 offen offset:1024 lds\n" \
"buffer_load_dwordx4 v6,s[40:43],s100 offen offset:2048 lds\nbuffer_load_dwordx4 v7,s[40:43],s100 offen offset:3072 lds\ns_lshr_b32 s31,s33,3\ns_lshl1_add_u32 s31,s31,3\n" \
"s_sub_u32 s96,s31,7\ns_min_u32 s31,s31,s96\ns_lshl_b32 s96,s31,12\ns_add_u32 s96,s96,s57\n" \
"s_add_u32 m0,s96,0\ns_and_b32 s96,s31,1\ns_lshr_b32 s31,s31,1\ns_lshl_b32 s31,s31,10\n" \
"s_add_u32 s31,s31,s96\ns_lshl_b32 s31,s31,7\ns_add_u32 s31,s31,s92\ns_add_u32 s31,s31,262144\n" \
"buffer_load_dwordx4 v4,s[40:43],s31 offen lds\nbuffer_load_dwordx4 v5,s[40:43],s31 offen offset:1024 lds\nbuffer_load_dwordx4 v6,s[40:43],s31 offen offset:2048 lds\nbuffer_load_dwordx4 v7,s[40:43],s31 offen offset:3072 lds\n" \
"s_lshr_b32 s97,s33,3\ns_lshl1_add_u32 s97,s97,4\ns_sub_u32 s95,s97,7\ns_min_u32 s97,s97,s95\n" \
"s_lshl_b32 s95,s97,12\ns_add_u32 s95,s95,s57\ns_add_u32 m0,s95,0\ns_and_b32 s95,s97,1\n" \
"s_lshr_b32 s97,s97,1\ns_lshl_b32 s97,s97,10\ns_add_u32 s97,s97,s95\ns_lshl_b32 s97,s97,7\n" \
"s_add_u32 s97,s97,s92\ns_add_u32 s97,s97,262144\nbuffer_load_dwordx4 v4,s[40:43],s97 offen lds\nbuffer_load_dwordx4 v5,s[40:43],s97 offen offset:1024 lds\n" \
"buffer_load_dwordx4 v6,s[40:43],s97 offen offset:2048 lds\nbuffer_load_dwordx4 v7,s[40:43],s97 offen offset:3072 lds\ns_lshr_b32 s100,s33,3\ns_lshl1_add_u32 s100,s100,5\n" \
"s_sub_u32 s101,s100,7\ns_min_u32 s100,s100,s101\ns_lshl_b32 s101,s100,12\ns_add_u32 s101,s101,s57\n" \
"s_add_u32 m0,s101,0\ns_and_b32 s101,s100,1\ns_lshr_b32 s100,s100,1\ns_lshl_b32 s100,s100,10\n" \
"s_add_u32 s100,s100,s101\ns_lshl_b32 s100,s100,7\ns_add_u32 s100,s100,s92\ns_add_u32 s100,s100,262144\n" \
"buffer_load_dwordx4 v4,s[40:43],s100 offen lds\nbuffer_load_dwordx4 v5,s[40:43],s100 offen offset:1024 lds\nbuffer_load_dwordx4 v6,s[40:43],s100 offen offset:2048 lds\nbuffer_load_dwordx4 v7,s[40:43],s100 offen offset:3072 lds\n" \
"s_lshr_b32 s31,s33,3\ns_lshl1_add_u32 s31,s31,6\ns_sub_u32 s96,s31,7\ns_min_u32 s31,s31,s96\n" \
"s_lshl_b32 s96,s31,12\ns_add_u32 s96,s96,s57\ns_add_u32 m0,s96,0\ns_and_b32 s96,s31,1\n" \
"s_lshr_b32 s31,s31,1\ns_lshl_b32 s31,s31,10\ns_add_u32 s31,s31,s96\ns_lshl_b32 s31,s31,7\n" \
"s_add_u32 s31,s31,s92\ns_add_u32 s31,s31,262144\nbuffer_load_dwordx4 v4,s[40:43],s31 offen lds\nbuffer_load_dwordx4 v5,s[40:43],s31 offen offset:1024 lds\n" \
"buffer_load_dwordx4 v6,s[40:43],s31 offen offset:2048 lds\nbuffer_load_dwordx4 v7,s[40:43],s31 offen offset:3072 lds\n.Lgemm_%=:\ns_add_u32 s4,s36,s48\n" \
"s_addc_u32 s5,s37,0\ns_add_u32 s6,s36,s49\ns_addc_u32 s7,s37,0\ns_add_u32 s8,s36,s50\n" \
"s_addc_u32 s9,s37,0\ns_add_u32 s10,s36,s51\ns_addc_u32 s11,s37,0\ns_add_u32 s12,s36,s68\n" \
"s_addc_u32 s13,s37,0\ns_add_u32 s14,s36,s69\ns_addc_u32 s15,s37,0\ns_add_u32 s16,s36,s70\n" \
"s_addc_u32 s17,s37,0\ns_memrealtime s[94:95]\nglobal_load_dwordx4 v[64:67],v59,s[4:5] sc1\nglobal_load_dwordx4 v[68:71],v59,s[4:5] offset:1024 sc1\n" \
"global_load_dwordx4 v[72:75],v59,s[4:5] offset:2048 sc1\nglobal_load_dwordx4 v[76:79],v59,s[4:5] offset:3072 sc1\nglobal_load_dwordx4 v[80:83],v59,s[6:7] sc1\nglobal_load_dwordx4 v[84:87],v59,s[6:7] offset:1024 sc1\n" \
"global_load_dwordx4 v[88:91],v59,s[6:7] offset:2048 sc1\nglobal_load_dwordx4 v[92:95],v59,s[6:7] offset:3072 sc1\nglobal_load_dwordx4 v[96:99],v59,s[8:9] sc1\nglobal_load_dwordx4 v[100:103],v59,s[8:9] offset:1024 sc1\n" \
"global_load_dwordx4 v[104:107],v59,s[8:9] offset:2048 sc1\nglobal_load_dwordx4 v[108:111],v59,s[8:9] offset:3072 sc1\nglobal_load_dwordx4 v[112:115],v59,s[10:11] sc1\nglobal_load_dwordx4 v[116:119],v59,s[10:11] offset:1024 sc1\n" \
"global_load_dwordx4 v[120:123],v59,s[10:11] offset:2048 sc1\nglobal_load_dwordx4 v[124:127],v59,s[10:11] offset:3072 sc1\ns_branch .Lz0_%=\n.Lzr_%=:\n" \
"v_and_b32 v0,0x3ff,v59\nv_lshlrev_b32 v0,0,v0\nv_bfrev_b32 v1,1\ns_waitcnt lgkmcnt(0)\n" \
".Lh_%=:\nbuffer_load_dwordx4 v[4:7],v0,s[36:39],s87 offen sc1\ns_memrealtime s[96:97]\ns_waitcnt vmcnt(0) lgkmcnt(0)\n" \
"v_bitop3_b32 v4,v4,v5,v6 bitop3:0x80\nv_and_b32 v4,v4,v7\nv_cmp_ne_u32 vcc,0,v4\nv_max3_u32 v2,v64,v68,v72\n" \
"v_max3_u32 v2,v2,v76,v80\nv_max3_u32 v2,v2,v84,v88\nv_max3_u32 v2,v2,v92,v96\nv_max3_u32 v2,v2,v100,v104\n" \
"v_max3_u32 v2,v2,v108,v112\nv_max3_u32 v2,v2,v116,v120\nv_max_u32 v2,v2,v124\nv_cndmask_b32 v0,v0,v1,vcc\n" \
"v_cmp_eq_u32 vcc,-1,v2\nv_cmp_ne_u32 s[100:101],v0,v1\ns_sub_u32 s96,s96,s94\ns_cbranch_vccz .Lj_%=\n" \
"v_cmpx_eq_u32 vcc,-1,v64\nglobal_load_dwordx4 v[64:67],v59,s[4:5] sc1\ns_mov_b64 exec,-1\nv_cmpx_eq_u32 vcc,-1,v68\n" \
"global_load_dwordx4 v[68:71],v59,s[4:5] offset:1024 sc1\ns_mov_b64 exec,-1\nv_cmpx_eq_u32 vcc,-1,v72\nglobal_load_dwordx4 v[72:75],v59,s[4:5] offset:2048 sc1\n" \
"s_mov_b64 exec,-1\nv_cmpx_eq_u32 vcc,-1,v76\nglobal_load_dwordx4 v[76:79],v59,s[4:5] offset:3072 sc1\ns_mov_b64 exec,-1\n" \
"v_cmpx_eq_u32 vcc,-1,v80\nglobal_load_dwordx4 v[80:83],v59,s[6:7] sc1\ns_mov_b64 exec,-1\nv_cmpx_eq_u32 vcc,-1,v84\n" \
"global_load_dwordx4 v[84:87],v59,s[6:7] offset:1024 sc1\ns_mov_b64 exec,-1\nv_cmpx_eq_u32 vcc,-1,v88\nglobal_load_dwordx4 v[88:91],v59,s[6:7] offset:2048 sc1\n" \
"s_mov_b64 exec,-1\nv_cmpx_eq_u32 vcc,-1,v92\nglobal_load_dwordx4 v[92:95],v59,s[6:7] offset:3072 sc1\ns_mov_b64 exec,-1\n" \
"v_cmpx_eq_u32 vcc,-1,v96\nglobal_load_dwordx4 v[96:99],v59,s[8:9] sc1\ns_mov_b64 exec,-1\nv_cmpx_eq_u32 vcc,-1,v100\n" \
"global_load_dwordx4 v[100:103],v59,s[8:9] offset:1024 sc1\ns_mov_b64 exec,-1\nv_cmpx_eq_u32 vcc,-1,v104\nglobal_load_dwordx4 v[104:107],v59,s[8:9] offset:2048 sc1\n" \
"s_mov_b64 exec,-1\nv_cmpx_eq_u32 vcc,-1,v108\nglobal_load_dwordx4 v[108:111],v59,s[8:9] offset:3072 sc1\ns_mov_b64 exec,-1\n" \
"v_cmpx_eq_u32 vcc,-1,v112\nglobal_load_dwordx4 v[112:115],v59,s[10:11] sc1\ns_mov_b64 exec,-1\nv_cmpx_eq_u32 vcc,-1,v116\n" \
"global_load_dwordx4 v[116:119],v59,s[10:11] offset:1024 sc1\ns_mov_b64 exec,-1\nv_cmpx_eq_u32 vcc,-1,v120\nglobal_load_dwordx4 v[120:123],v59,s[10:11] offset:2048 sc1\n" \
"s_mov_b64 exec,-1\nv_cmpx_eq_u32 vcc,-1,v124\nglobal_load_dwordx4 v[124:127],v59,s[10:11] offset:3072 sc1\ns_mov_b64 exec,-1\n" \
".Lj_%=:\ns_cmp_gt_u32 s96,0x4c4b40\ns_cbranch_scc1 .Lg_%=\ns_cmp_lg_u64 s[100:101],0\n" \
"s_cbranch_scc1 .Lh_%=\n.Lg_%=:\ns_waitcnt vmcnt(0)\ns_mov_b32 s33,s86\n" \
"s_nop 0\ns_mov_b32 m0,s56\ns_and_b32 s95,s89,7\ns_andn2_b32 s101,s89,7\n" \
"s_mul_i32 s95,s95,409600\ns_add_u32 s95,s95,s52\ns_mov_b32 s86,0x11111111\ns_mov_b32 s87,0x11111111\n" \
"v_add_u32 v59,s85,v59\ns_add_u32 s100,s90,73728\ns_mov_b32 s35,73728\ns_mov_b32 s34,s91\n" \
"s_waitcnt lgkmcnt(0)\ns_cmp_eq_u32 s84,0\ns_cbranch_scc0 .Ls_%=\n.Lf_%=:\n" \
"s_waitcnt vmcnt(15)\nv_mfma_f32_16x16x32_bf16 v[0:3],v[64:67],a[0:3],0\nds_read_b32 v44,v60\ns_waitcnt vmcnt(14)\n" \
"v_mfma_f32_16x16x32_bf16 v[0:3],v[68:71],a[16:19],v[0:3]\nds_read_b32 v45,v60 offset:9568\nv_add_f32 v40,1.0,v40\nv_mfma_f32_16x16x32_bf16 v[4:7],v[64:67],a[4:7],0\n" \
"ds_read_b32 v46,v60 offset:19136\nv_add_u32 v59,0x10000,v59\nv_mfma_f32_16x16x32_bf16 v[4:7],v[68:71],a[20:23],v[4:7]\nds_read_b32 v47,v60 offset:28704\n" \
"v_and_b32 v59,0x7ffff,v59\nv_mfma_f32_16x16x32_bf16 v[8:11],v[64:67],a[8:11],0\nv_add_f32 v41,1.0,v41\nv_add_f32 v42,1.0,v42\n" \
"v_mfma_f32_16x16x32_bf16 v[8:11],v[68:71],a[24:27],v[8:11]\nv_add_f32 v43,1.0,v43\nds_write_addtid_b32 v20 offset:5344\nv_mfma_f32_16x16x32_bf16 v[12:15],v[64:67],a[12:15],0\n" \
"v_rcp_f32 v40,v40\nv_mfma_f32_16x16x32_bf16 v[12:15],v[68:71],a[28:31],v[12:15]\nv_rcp_f32 v41,v41\nv_mfma_f32_16x16x32_bf16 v[16:19],v[64:67],v[192:195],0\n" \
"global_load_dwordx4 v[64:67],v59,s[4:5] sc1\nds_write_addtid_b32 v21 offset:5888\nv_mfma_f32_16x16x32_bf16 v[16:19],v[68:71],v[196:199],v[16:19]\nds_write_addtid_b32 v22 offset:5600\n" \
"s_waitcnt vmcnt(14)\nv_mfma_f32_16x16x32_bf16 v[0:3],v[72:75],a[32:35],v[0:3]\nds_write_addtid_b32 v23 offset:6144\ns_waitcnt vmcnt(13)\n" \
"v_mfma_f32_16x16x32_bf16 v[0:3],v[76:79],a[48:51],v[0:3]\nv_rcp_f32 v42,v42\nv_mfma_f32_16x16x32_bf16 v[4:7],v[72:75],a[36:39],v[4:7]\nv_rcp_f32 v43,v43\n" \
"v_mfma_f32_16x16x32_bf16 v[4:7],v[76:79],a[52:55],v[4:7]\nglobal_load_dwordx4 v[68:71],v59,s[4:5] offset:1024 sc1\nv_mul_f32 v36,v36,v40\nv_mfma_f32_16x16x32_bf16 v[8:11],v[72:75],a[40:43],v[8:11]\n" \
"v_mul_f32 v37,v37,v41\nv_mul_f32 v38,v38,v42\nv_mfma_f32_16x16x32_bf16 v[8:11],v[76:79],a[56:59],v[8:11]\nv_mul_f32 v39,v39,v43\n" \
"v_cvt_pk_bf16_f32 v40,v36,v37\nv_mfma_f32_16x16x32_bf16 v[12:15],v[72:75],a[44:47],v[12:15]\nds_write_addtid_b32 v24 offset:6400\nv_cvt_pk_bf16_f32 v41,v38,v39\n" \
"v_mfma_f32_16x16x32_bf16 v[12:15],v[76:79],a[60:63],v[12:15]\nds_write_addtid_b32 v25 offset:6944\nbuffer_store_dwordx2 v[40:41],v61,s[36:39],s52 offen sc1\nv_mfma_f32_16x16x32_bf16 v[16:19],v[72:75],v[200:203],v[16:19]\n" \
"global_load_dwordx4 v[72:75],v59,s[4:5] offset:2048 sc1\nds_write_addtid_b32 v26 offset:6656\nv_mfma_f32_16x16x32_bf16 v[16:19],v[76:79],v[204:207],v[16:19]\nds_write_addtid_b32 v27 offset:7200\n" \
"s_waitcnt vmcnt(15)\nv_mfma_f32_16x16x32_bf16 v[0:3],v[80:83],a[64:67],v[0:3]\nds_write_addtid_b32 v28 offset:7456\ns_waitcnt vmcnt(14)\n" \
"v_mfma_f32_16x16x32_bf16 v[0:3],v[84:87],a[80:83],v[0:3]\nds_write_addtid_b32 v29 offset:8000\ns_waitcnt lgkmcnt(10)\nv_mfma_f32_16x16x32_bf16 v[4:7],v[80:83],a[68:71],v[4:7]\n" \
"ds_write_addtid_b32 v30 offset:7712\nv_add_f32 v44,v44,v45\nv_mfma_f32_16x16x32_bf16 v[4:7],v[84:87],a[84:87],v[4:7]\nglobal_load_dwordx4 v[76:79],v59,s[4:5] offset:3072 sc1\n" \
"ds_write_addtid_b32 v31 offset:8256\nv_mfma_f32_16x16x32_bf16 v[8:11],v[80:83],a[72:75],v[8:11]\nds_write_addtid_b32 v32 offset:8512\nv_add_f32 v46,v46,v47\n" \
"v_mfma_f32_16x16x32_bf16 v[8:11],v[84:87],a[88:91],v[8:11]\nds_write_addtid_b32 v33 offset:9056\nv_add_f32 v44,v44,v56\nv_mfma_f32_16x16x32_bf16 v[12:15],v[80:83],a[76:79],v[12:15]\n" \
"ds_write_addtid_b32 v34 offset:8768\nv_add_f32 v44,v44,v46\nv_mfma_f32_16x16x32_bf16 v[12:15],v[84:87],a[92:95],v[12:15]\nds_write_addtid_b32 v35 offset:9312\n" \
"v_mul_f32 v45,v44,v44\nv_mfma_f32_16x16x32_bf16 v[16:19],v[80:83],v[208:211],v[16:19]\nglobal_load_dwordx4 v[80:83],v59,s[6:7] sc1\nv_fma_f32 v45,v45,s55,v57\n" \
"v_mfma_f32_16x16x32_bf16 v[16:19],v[84:87],v[212:215],v[16:19]\nv_mul_f32 v45,v44,v45\ns_waitcnt vmcnt(15)\nv_mfma_f32_16x16x32_bf16 v[0:3],v[88:91],a[96:99],v[0:3]\n" \
"s_waitcnt vmcnt(14)\nv_mfma_f32_16x16x32_bf16 v[0:3],v[92:95],a[112:115],v[0:3]\nv_exp_f32 v45,v45\nv_mfma_f32_16x16x32_bf16 v[4:7],v[88:91],a[100:103],v[4:7]\n" \
"v_add_f32 v45,1.0,v45\nv_mfma_f32_16x16x32_bf16 v[4:7],v[92:95],a[116:119],v[4:7]\nglobal_load_dwordx4 v[84:87],v59,s[6:7] offset:1024 sc1\nv_mfma_f32_16x16x32_bf16 v[8:11],v[88:91],a[104:107],v[8:11]\n" \
"s_waitcnt lgkmcnt(0)\ns_barrier\nv_mfma_f32_16x16x32_bf16 v[8:11],v[92:95],a[120:123],v[8:11]\nv_rcp_f32 v45,v45\n" \
"v_mfma_f32_16x16x32_bf16 v[12:15],v[88:91],a[108:111],v[12:15]\nv_mul_f32 v44,v44,v45\nv_cvt_pk_bf16_f32 v45,v44,v44\nv_mfma_f32_16x16x32_bf16 v[12:15],v[92:95],a[124:127],v[12:15]\n" \
"buffer_store_short v45,v62,s[36:39],s52 offen sc1\nds_read_b128 v[36:39],v58 offset:5344\nv_mfma_f32_16x16x32_bf16 v[16:19],v[88:91],v[216:219],v[16:19]\nglobal_load_dwordx4 v[88:91],v59,s[6:7] offset:2048 sc1\n" \
"ds_read_b128 v[40:43],v58 offset:14912\nv_mfma_f32_16x16x32_bf16 v[16:19],v[92:95],v[220:223],v[16:19]\nds_read_b128 v[44:47],v58 offset:24480\ns_waitcnt vmcnt(16)\n" \
"v_mfma_f32_16x16x32_bf16 v[0:3],v[96:99],a[128:131],v[0:3]\nds_read_b128 v[48:51],v58 offset:34048\ns_waitcnt vmcnt(15)\nv_mfma_f32_16x16x32_bf16 v[0:3],v[100:103],a[144:147],v[0:3]\n" \
"v_mfma_f32_16x16x32_bf16 v[4:7],v[96:99],a[132:135],v[4:7]\nv_mfma_f32_16x16x32_bf16 v[4:7],v[100:103],a[148:151],v[4:7]\nglobal_load_dwordx4 v[92:95],v59,s[6:7] offset:3072 sc1\nv_mfma_f32_16x16x32_bf16 v[8:11],v[96:99],a[136:139],v[8:11]\n" \
"v_mfma_f32_16x16x32_bf16 v[8:11],v[100:103],a[152:155],v[8:11]\ns_cmp_eq_u32 s52,s83\ns_cbranch_scc1 .Lxq5_%=\n.Lyq5_%=:\n" \
"v_mfma_f32_16x16x32_bf16 v[12:15],v[96:99],a[140:143],v[12:15]\ns_waitcnt lgkmcnt(2)\nv_add_f32 v36,v36,v40\nv_mfma_f32_16x16x32_bf16 v[12:15],v[100:103],a[156:159],v[12:15]\n" \
"v_add_f32 v37,v37,v41\nv_add_f32 v38,v38,v42\nv_mfma_f32_16x16x32_bf16 v[16:19],v[96:99],v[224:227],v[16:19]\nglobal_load_dwordx4 v[96:99],v59,s[8:9] sc1\n" \
"v_add_f32 v39,v39,v43\nv_mfma_f32_16x16x32_bf16 v[16:19],v[100:103],v[228:231],v[16:19]\nv_add_f32 v36,v36,v52\ns_waitcnt vmcnt(16)\n" \
"v_mfma_f32_16x16x32_bf16 v[0:3],v[104:107],a[160:163],v[0:3]\nv_add_f32 v37,v37,v53\ns_waitcnt vmcnt(15)\nv_mfma_f32_16x16x32_bf16 v[0:3],v[108:111],a[176:179],v[0:3]\n" \
"v_add_f32 v38,v38,v54\nv_add_f32 v39,v39,v55\nv_mfma_f32_16x16x32_bf16 v[4:7],v[104:107],a[164:167],v[4:7]\ns_waitcnt lgkmcnt(0)\n" \
"v_add_f32 v44,v44,v48\nv_mfma_f32_16x16x32_bf16 v[4:7],v[108:111],a[180:183],v[4:7]\nglobal_load_dwordx4 v[100:103],v59,s[8:9] offset:1024 sc1\nv_add_f32 v45,v45,v49\n" \
"v_mfma_f32_16x16x32_bf16 v[8:11],v[104:107],a[168:171],v[8:11]\nv_add_f32 v46,v46,v50\nv_add_f32 v47,v47,v51\nv_mfma_f32_16x16x32_bf16 v[8:11],v[108:111],a[184:187],v[8:11]\n" \
"v_add_f32 v36,v36,v44\nv_add_f32 v37,v37,v45\nv_mfma_f32_16x16x32_bf16 v[12:15],v[104:107],a[172:175],v[12:15]\nv_add_f32 v38,v38,v46\n" \
"v_add_f32 v39,v39,v47\nv_mfma_f32_16x16x32_bf16 v[12:15],v[108:111],a[188:191],v[12:15]\nv_mul_f32 v40,v36,v36\nv_mul_f32 v41,v37,v37\n" \
"v_mfma_f32_16x16x32_bf16 v[16:19],v[104:107],v[232:235],v[16:19]\nglobal_load_dwordx4 v[104:107],v59,s[8:9] offset:2048 sc1\nv_mul_f32 v42,v38,v38\nv_mfma_f32_16x16x32_bf16 v[16:19],v[108:111],v[236:239],v[16:19]\n" \
"v_mul_f32 v43,v39,v39\ns_waitcnt vmcnt(16)\nv_mfma_f32_16x16x32_bf16 v[0:3],v[112:115],a[192:195],v[0:3]\nv_fma_f32 v40,v40,s55,v57\n" \
"s_waitcnt vmcnt(15)\nv_mfma_f32_16x16x32_bf16 v[0:3],v[116:119],a[208:211],v[0:3]\nv_fma_f32 v41,v41,s55,v57\nv_fma_f32 v42,v42,s55,v57\n" \
"v_mfma_f32_16x16x32_bf16 v[4:7],v[112:115],a[196:199],v[4:7]\nv_fma_f32 v43,v43,s55,v57\nv_mul_f32 v40,v36,v40\nv_mfma_f32_16x16x32_bf16 v[4:7],v[116:119],a[212:215],v[4:7]\n" \
"global_load_dwordx4 v[108:111],v59,s[8:9] offset:3072 sc1\nv_mul_f32 v41,v37,v41\nv_mfma_f32_16x16x32_bf16 v[8:11],v[112:115],a[200:203],v[8:11]\nv_mul_f32 v42,v38,v42\n" \
"v_mul_f32 v43,v39,v43\nv_mfma_f32_16x16x32_bf16 v[8:11],v[116:119],a[216:219],v[8:11]\nv_exp_f32 v40,v40\nv_mfma_f32_16x16x32_bf16 v[12:15],v[112:115],a[204:207],v[12:15]\n" \
"v_exp_f32 v41,v41\nv_mfma_f32_16x16x32_bf16 v[12:15],v[116:119],a[220:223],v[12:15]\nv_exp_f32 v42,v42\nv_mfma_f32_16x16x32_bf16 v[16:19],v[112:115],v[240:243],v[16:19]\n" \
"global_load_dwordx4 v[112:115],v59,s[10:11] sc1\nv_mfma_f32_16x16x32_bf16 v[16:19],v[116:119],v[244:247],v[16:19]\ns_waitcnt vmcnt(16)\nv_mfma_f32_16x16x32_bf16 v[0:3],v[120:123],a[224:227],v[0:3]\n" \
"s_waitcnt vmcnt(15)\nv_mfma_f32_16x16x32_bf16 v[0:3],v[124:127],a[240:243],v[0:3]\nv_exp_f32 v43,v43\nv_mfma_f32_16x16x32_bf16 v[4:7],v[120:123],a[228:231],v[4:7]\n" \
"v_add_f32 v40,1.0,v40\nv_add_f32 v41,1.0,v41\nv_mfma_f32_16x16x32_bf16 v[4:7],v[124:127],a[244:247],v[4:7]\nglobal_load_dwordx4 v[116:119],v59,s[10:11] offset:1024 sc1\n" \
"v_add_f32 v42,1.0,v42\nv_mfma_f32_16x16x32_bf16 v[8:11],v[120:123],a[232:235],v[8:11]\nv_add_f32 v43,1.0,v43\nv_mfma_f32_16x16x32_bf16 v[8:11],v[124:127],a[248:251],v[8:11]\n" \
"v_rcp_f32 v40,v40\nv_mfma_f32_16x16x32_bf16 v[12:15],v[120:123],a[236:239],v[12:15]\ns_cmp_eq_u32 s52,s95\ns_cbranch_scc1 .Lxt5_%=\n" \
".Lyt5_%=:\nv_mfma_f32_16x16x32_bf16 v[12:15],v[124:127],a[252:255],v[12:15]\nv_rcp_f32 v41,v41\nv_mfma_f32_16x16x32_bf16 v[16:19],v[120:123],v[248:251],v[16:19]\n" \
"global_load_dwordx4 v[120:123],v59,s[10:11] offset:2048 sc1\nv_rcp_f32 v42,v42\nv_mfma_f32_16x16x32_bf16 v[16:19],v[124:127],v[252:255],v[16:19]\nglobal_load_dwordx4 v[124:127],v59,s[10:11] offset:3072 sc1\n" \
"v_rcp_f32 v43,v43\ns_add_u32 s52,s52,409600\ns_cmp_eq_u32 s52,s53\ns_cbranch_scc1 .Lls_%=\n" \
".Ls_%=:\ns_waitcnt vmcnt(17)\nv_mfma_f32_16x16x32_bf16 v[20:23],v[64:67],a[0:3],0\nv_mul_f32 v36,v36,v40\n" \
"s_waitcnt vmcnt(16)\nv_mfma_f32_16x16x32_bf16 v[20:23],v[68:71],a[16:19],v[20:23]\nv_add_u32 v59,0x10000,v59\nv_mul_f32 v37,v37,v41\n" \
"v_mfma_f32_16x16x32_bf16 v[24:27],v[64:67],a[4:7],0\nv_and_b32 v59,0x7ffff,v59\nv_mul_f32 v38,v38,v42\nv_mfma_f32_16x16x32_bf16 v[24:27],v[68:71],a[20:23],v[24:27]\n" \
"v_mul_f32 v39,v39,v43\nv_cvt_pk_bf16_f32 v40,v36,v37\nv_mfma_f32_16x16x32_bf16 v[28:31],v[64:67],a[8:11],0\nds_write_addtid_b32 v0\n" \
"v_cvt_pk_bf16_f32 v41,v38,v39\nv_mfma_f32_16x16x32_bf16 v[28:31],v[68:71],a[24:27],v[28:31]\nds_write_addtid_b32 v1 offset:544\nbuffer_store_dwordx2 v[40:41],v61,s[36:39],s52 offen sc1\n" \
"v_mfma_f32_16x16x32_bf16 v[32:35],v[64:67],a[12:15],0\nglobal_load_dwordx4 v[64:67],v59,s[4:5] sc1\nds_write_addtid_b32 v2 offset:256\nv_mfma_f32_16x16x32_bf16 v[32:35],v[68:71],a[28:31],v[32:35]\n" \
"ds_write_addtid_b32 v3 offset:800\ns_waitcnt vmcnt(16)\nv_mfma_f32_16x16x32_bf16 v[20:23],v[72:75],a[32:35],v[20:23]\nds_write_addtid_b32 v4 offset:1056\n" \
"s_waitcnt vmcnt(15)\nv_mfma_f32_16x16x32_bf16 v[20:23],v[76:79],a[48:51],v[20:23]\nds_write_addtid_b32 v5 offset:1600\nv_mfma_f32_16x16x32_bf16 v[24:27],v[72:75],a[36:39],v[24:27]\n" \
"global_load_dwordx4 v[68:71],v59,s[4:5] offset:1024 sc1\nds_write_addtid_b32 v6 offset:1312\nv_mfma_f32_16x16x32_bf16 v[24:27],v[76:79],a[52:55],v[24:27]\nds_write_addtid_b32 v7 offset:1856\n" \
"v_mfma_f32_16x16x32_bf16 v[28:31],v[72:75],a[40:43],v[28:31]\nds_write_addtid_b32 v8 offset:2112\nv_mfma_f32_16x16x32_bf16 v[28:31],v[76:79],a[56:59],v[28:31]\nds_write_addtid_b32 v9 offset:2656\n" \
"v_mfma_f32_16x16x32_bf16 v[32:35],v[72:75],a[44:47],v[32:35]\nglobal_load_dwordx4 v[72:75],v59,s[4:5] offset:2048 sc1\nds_write_addtid_b32 v10 offset:2368\nv_mfma_f32_16x16x32_bf16 v[32:35],v[76:79],a[60:63],v[32:35]\n" \
"ds_write_addtid_b32 v11 offset:2912\ns_waitcnt vmcnt(16)\nv_mfma_f32_16x16x32_bf16 v[20:23],v[80:83],a[64:67],v[20:23]\nds_write_addtid_b32 v12 offset:3168\n" \
"s_waitcnt vmcnt(15)\nv_mfma_f32_16x16x32_bf16 v[20:23],v[84:87],a[80:83],v[20:23]\nds_write_addtid_b32 v13 offset:3712\nv_mfma_f32_16x16x32_bf16 v[24:27],v[80:83],a[68:71],v[24:27]\n" \
"global_load_dwordx4 v[76:79],v59,s[4:5] offset:3072 sc1\nds_write_addtid_b32 v14 offset:3424\nv_mfma_f32_16x16x32_bf16 v[24:27],v[84:87],a[84:87],v[24:27]\nds_write_addtid_b32 v15 offset:3968\n" \
"v_mfma_f32_16x16x32_bf16 v[28:31],v[80:83],a[72:75],v[28:31]\nds_write_addtid_b32 v16 offset:4224\nv_mfma_f32_16x16x32_bf16 v[28:31],v[84:87],a[88:91],v[28:31]\nds_write_addtid_b32 v17 offset:4512\n" \
"v_mfma_f32_16x16x32_bf16 v[32:35],v[80:83],a[76:79],v[32:35]\nglobal_load_dwordx4 v[80:83],v59,s[6:7] sc1\nds_write_addtid_b32 v18 offset:4800\nv_mfma_f32_16x16x32_bf16 v[32:35],v[84:87],a[92:95],v[32:35]\n" \
"ds_write_addtid_b32 v19 offset:5088\ns_waitcnt vmcnt(15)\nv_mfma_f32_16x16x32_bf16 v[20:23],v[88:91],a[96:99],v[20:23]\ns_waitcnt vmcnt(14)\n" \
"v_mfma_f32_16x16x32_bf16 v[20:23],v[92:95],a[112:115],v[20:23]\nv_mfma_f32_16x16x32_bf16 v[24:27],v[88:91],a[100:103],v[24:27]\nglobal_load_dwordx4 v[84:87],v59,s[6:7] offset:1024 sc1\nv_mfma_f32_16x16x32_bf16 v[24:27],v[92:95],a[116:119],v[24:27]\n" \
"v_mfma_f32_16x16x32_bf16 v[28:31],v[88:91],a[104:107],v[28:31]\nv_mfma_f32_16x16x32_bf16 v[28:31],v[92:95],a[120:123],v[28:31]\ns_waitcnt lgkmcnt(0)\ns_barrier\n" \
"v_mfma_f32_16x16x32_bf16 v[32:35],v[88:91],a[108:111],v[32:35]\nglobal_load_dwordx4 v[88:91],v59,s[6:7] offset:2048 sc1\nds_read_b128 v[36:39],v58\nv_mfma_f32_16x16x32_bf16 v[32:35],v[92:95],a[124:127],v[32:35]\n" \
"ds_read_b128 v[40:43],v58 offset:9568\ns_waitcnt vmcnt(15)\nv_mfma_f32_16x16x32_bf16 v[20:23],v[96:99],a[128:131],v[20:23]\nds_read_b128 v[44:47],v58 offset:19136\n" \
"s_waitcnt vmcnt(14)\nv_mfma_f32_16x16x32_bf16 v[20:23],v[100:103],a[144:147],v[20:23]\nds_read_b128 v[48:51],v58 offset:28704\nv_mfma_f32_16x16x32_bf16 v[24:27],v[96:99],a[132:135],v[24:27]\n" \
"global_load_dwordx4 v[92:95],v59,s[6:7] offset:3072 sc1\nv_mfma_f32_16x16x32_bf16 v[24:27],v[100:103],a[148:151],v[24:27]\nv_mfma_f32_16x16x32_bf16 v[28:31],v[96:99],a[136:139],v[28:31]\ns_cmp_eq_u32 s52,s83\n" \
"s_cbranch_scc1 .Lxq4_%=\n.Lyq4_%=:\nv_mfma_f32_16x16x32_bf16 v[28:31],v[100:103],a[152:155],v[28:31]\nv_mfma_f32_16x16x32_bf16 v[32:35],v[96:99],a[140:143],v[32:35]\n" \
"global_load_dwordx4 v[96:99],v59,s[8:9] sc1\nv_mfma_f32_16x16x32_bf16 v[32:35],v[100:103],a[156:159],v[32:35]\ns_waitcnt lgkmcnt(2)\ns_waitcnt vmcnt(15)\n" \
"v_mfma_f32_16x16x32_bf16 v[20:23],v[104:107],a[160:163],v[20:23]\nv_add_f32 v36,v36,v40\ns_waitcnt vmcnt(14)\nv_mfma_f32_16x16x32_bf16 v[20:23],v[108:111],a[176:179],v[20:23]\n" \
"v_add_f32 v37,v37,v41\nv_add_f32 v38,v38,v42\nv_mfma_f32_16x16x32_bf16 v[24:27],v[104:107],a[164:167],v[24:27]\nglobal_load_dwordx4 v[100:103],v59,s[8:9] offset:1024 sc1\n" \
"v_add_f32 v39,v39,v43\nv_mfma_f32_16x16x32_bf16 v[24:27],v[108:111],a[180:183],v[24:27]\nv_add_f32 v36,v36,v52\nv_add_f32 v37,v37,v53\n" \
"v_mfma_f32_16x16x32_bf16 v[28:31],v[104:107],a[168:171],v[28:31]\nv_add_f32 v38,v38,v54\nv_add_f32 v39,v39,v55\nv_mfma_f32_16x16x32_bf16 v[28:31],v[108:111],a[184:187],v[28:31]\n" \
"s_waitcnt lgkmcnt(0)\nv_add_f32 v44,v44,v48\nv_mfma_f32_16x16x32_bf16 v[32:35],v[104:107],a[172:175],v[32:35]\nglobal_load_dwordx4 v[104:107],v59,s[8:9] offset:2048 sc1\n" \
"v_add_f32 v45,v45,v49\nv_mfma_f32_16x16x32_bf16 v[32:35],v[108:111],a[188:191],v[32:35]\nv_add_f32 v46,v46,v50\ns_waitcnt vmcnt(15)\n" \
"v_mfma_f32_16x16x32_bf16 v[20:23],v[112:115],a[192:195],v[20:23]\nv_add_f32 v47,v47,v51\ns_waitcnt vmcnt(14)\nv_mfma_f32_16x16x32_bf16 v[20:23],v[116:119],a[208:211],v[20:23]\n" \
"v_add_f32 v36,v36,v44\nv_add_f32 v37,v37,v45\nv_mfma_f32_16x16x32_bf16 v[24:27],v[112:115],a[196:199],v[24:27]\nglobal_load_dwordx4 v[108:111],v59,s[8:9] offset:3072 sc1\n" \
"v_add_f32 v38,v38,v46\nv_mfma_f32_16x16x32_bf16 v[24:27],v[116:119],a[212:215],v[24:27]\nv_add_f32 v39,v39,v47\nv_mul_f32 v40,v36,v36\n" \
"v_mfma_f32_16x16x32_bf16 v[28:31],v[112:115],a[200:203],v[28:31]\nv_mul_f32 v41,v37,v37\nv_mul_f32 v42,v38,v38\nv_mfma_f32_16x16x32_bf16 v[28:31],v[116:119],a[216:219],v[28:31]\n" \
"v_mul_f32 v43,v39,v39\nv_fma_f32 v40,v40,s55,v57\nv_mfma_f32_16x16x32_bf16 v[32:35],v[112:115],a[204:207],v[32:35]\nglobal_load_dwordx4 v[112:115],v59,s[10:11] sc1\n" \
"v_fma_f32 v41,v41,s55,v57\nv_mfma_f32_16x16x32_bf16 v[32:35],v[116:119],a[220:223],v[32:35]\nv_fma_f32 v42,v42,s55,v57\ns_waitcnt vmcnt(15)\n" \
"v_mfma_f32_16x16x32_bf16 v[20:23],v[120:123],a[224:227],v[20:23]\nv_fma_f32 v43,v43,s55,v57\ns_waitcnt vmcnt(14)\nv_mfma_f32_16x16x32_bf16 v[20:23],v[124:127],a[240:243],v[20:23]\n" \
"v_mul_f32 v40,v36,v40\nv_mul_f32 v41,v37,v41\nv_mfma_f32_16x16x32_bf16 v[24:27],v[120:123],a[228:231],v[24:27]\nglobal_load_dwordx4 v[116:119],v59,s[10:11] offset:1024 sc1\n" \
"v_mul_f32 v42,v38,v42\nv_mfma_f32_16x16x32_bf16 v[24:27],v[124:127],a[244:247],v[24:27]\nv_mul_f32 v43,v39,v43\nv_mfma_f32_16x16x32_bf16 v[28:31],v[120:123],a[232:235],v[28:31]\n" \
"s_cmp_eq_u32 s52,s95\ns_cbranch_scc1 .Lxt4_%=\n.Lyt4_%=:\nv_mfma_f32_16x16x32_bf16 v[28:31],v[124:127],a[248:251],v[28:31]\n" \
"v_exp_f32 v40,v40\nv_mfma_f32_16x16x32_bf16 v[32:35],v[120:123],a[236:239],v[32:35]\nglobal_load_dwordx4 v[120:123],v59,s[10:11] offset:2048 sc1\nv_exp_f32 v41,v41\n" \
"v_mfma_f32_16x16x32_bf16 v[32:35],v[124:127],a[252:255],v[32:35]\nglobal_load_dwordx4 v[124:127],v59,s[10:11] offset:3072 sc1\nv_exp_f32 v42,v42\nv_exp_f32 v43,v43\n" \
"s_add_u32 s52,s52,409600\ns_cmp_eq_u32 s52,s53\ns_cbranch_scc0 .Lf_%=\nv_mfma_f32_16x16x32_bf16 v[0:3],v[64:67],a[0:3],0\n" \
"ds_read_b32 v44,v60\ns_waitcnt vmcnt(16)\nv_mfma_f32_16x16x32_bf16 v[0:3],v[68:71],a[16:19],v[0:3]\nds_read_b32 v45,v60 offset:9568\n" \
"v_and_b32 v59,0x3ff,v59\nv_mfma_f32_16x16x32_bf16 v[4:7],v[64:67],a[4:7],0\nds_read_b32 v46,v60 offset:19136\nds_write_addtid_b32 v20 offset:5344\n" \
"v_mfma_f32_16x16x32_bf16 v[4:7],v[68:71],a[20:23],v[4:7]\nds_read_b32 v47,v60 offset:28704\nds_write_addtid_b32 v21 offset:5888\nv_mfma_f32_16x16x32_bf16 v[8:11],v[64:67],a[8:11],0\n" \
"s_add_u32 s4,s12,409600\ns_addc_u32 s5,s13,0\nv_mfma_f32_16x16x32_bf16 v[8:11],v[68:71],a[24:27],v[8:11]\ns_add_u32 s6,s14,409600\n" \
"s_addc_u32 s7,s15,0\nv_mfma_f32_16x16x32_bf16 v[12:15],v[64:67],a[12:15],0\ns_add_u32 s8,s16,409600\ns_addc_u32 s9,s17,0\n" \
"v_mfma_f32_16x16x32_bf16 v[12:15],v[68:71],a[28:31],v[12:15]\nds_write_addtid_b32 v22 offset:5600\nds_write_addtid_b32 v23 offset:6144\nv_mfma_f32_16x16x32_bf16 v[16:19],v[64:67],v[192:195],0\n" \
"ds_write_addtid_b32 v24 offset:6400\nds_write_addtid_b32 v25 offset:6944\nv_mfma_f32_16x16x32_bf16 v[16:19],v[68:71],v[196:199],v[16:19]\nds_write_addtid_b32 v26 offset:6656\n" \
"s_waitcnt vmcnt(15)\nv_mfma_f32_16x16x32_bf16 v[0:3],v[72:75],a[32:35],v[0:3]\nv_add_u32 v71,s57,v59\ns_waitcnt vmcnt(14)\n" \
"v_mfma_f32_16x16x32_bf16 v[0:3],v[76:79],a[48:51],v[0:3]\ns_waitcnt vmcnt(8)\nv_cmp_eq_u32 vcc,0,v63\nv_mfma_f32_16x16x32_bf16 v[4:7],v[72:75],a[36:39],v[4:7]\n" \
"ds_read_b32 v64,v71 offset:28672\nds_read_b64 v[68:69],v71 offset:29696\nv_mfma_f32_16x16x32_bf16 v[4:7],v[76:79],a[52:55],v[4:7]\ns_and_b64 vcc,vcc,s[86:87]\n" \
"s_cbranch_scc1 .LsaF_%=\n.LbaF_%=:\nv_mfma_f32_16x16x32_bf16 v[8:11],v[72:75],a[40:43],v[8:11]\nds_write_addtid_b32 v27 offset:7200\n" \
"ds_write_addtid_b32 v28 offset:7456\nv_mfma_f32_16x16x32_bf16 v[8:11],v[76:79],a[56:59],v[8:11]\nds_write_addtid_b32 v29 offset:8000\nds_write_addtid_b32 v30 offset:7712\n" \
"v_mfma_f32_16x16x32_bf16 v[12:15],v[72:75],a[44:47],v[12:15]\ns_waitcnt lgkmcnt(4)\nv_xor_b32 v65,32,v64\nv_mfma_f32_16x16x32_bf16 v[12:15],v[76:79],a[60:63],v[12:15]\n" \
"v_xor_b32 v66,64,v64\nv_xor_b32 v67,96,v64\nv_mfma_f32_16x16x32_bf16 v[16:19],v[72:75],v[200:203],v[16:19]\nv_add_u32 v70,0xf800,v68\n" \
"v_add_u32 v71,0xf800,v69\nv_mfma_f32_16x16x32_bf16 v[16:19],v[76:79],v[204:207],v[16:19]\nds_write_addtid_b32 v31 offset:8256\nv_mfma_f32_16x16x32_bf16 v[0:3],v[80:83],a[64:67],v[0:3]\n" \
"global_load_dwordx4 v[72:75],v59,s[12:13] sc1\nv_mfma_f32_16x16x32_bf16 v[0:3],v[84:87],a[80:83],v[0:3]\nds_write_addtid_b32 v32 offset:8512\nds_write_addtid_b32 v33 offset:9056\n" \
"v_mfma_f32_16x16x32_bf16 v[4:7],v[80:83],a[68:71],v[4:7]\nglobal_load_dwordx4 v[76:79],v59,s[12:13] offset:1024 sc1\nds_write_addtid_b32 v34 offset:8768\nv_mfma_f32_16x16x32_bf16 v[4:7],v[84:87],a[84:87],v[4:7]\n" \
"ds_write_addtid_b32 v35 offset:9312\nds_read_b64_tr_b16 a[0:1],v64\nv_mfma_f32_16x16x32_bf16 v[8:11],v[80:83],a[72:75],v[8:11]\nds_read_b64_tr_b16 a[2:3],v64 offset:512\n" \
"ds_read_b64_tr_b16 a[4:5],v65\nv_mfma_f32_16x16x32_bf16 v[8:11],v[84:87],a[88:91],v[8:11]\nds_read_b64_tr_b16 a[6:7],v65 offset:512\nds_read_b64_tr_b16 a[8:9],v66\n" \
"v_mfma_f32_16x16x32_bf16 v[12:15],v[80:83],a[76:79],v[12:15]\nds_read_b64_tr_b16 a[10:11],v66 offset:512\nds_read_b64_tr_b16 a[12:13],v67\nv_mfma_f32_16x16x32_bf16 v[12:15],v[84:87],a[92:95],v[12:15]\n" \
"ds_read_b64_tr_b16 a[14:15],v67 offset:512\nds_read_b64_tr_b16 a[16:17],v64 offset:4096\nv_mfma_f32_16x16x32_bf16 v[16:19],v[80:83],v[208:211],v[16:19]\nds_read_b64_tr_b16 a[18:19],v64 offset:4608\n" \
"ds_read_b64_tr_b16 a[20:21],v65 offset:4096\nv_mfma_f32_16x16x32_bf16 v[16:19],v[84:87],v[212:215],v[16:19]\nds_read_b64_tr_b16 a[22:23],v65 offset:4608\ns_waitcnt vmcnt(13)\n" \
"v_mfma_f32_16x16x32_bf16 v[0:3],v[88:91],a[96:99],v[0:3]\nds_read_b64_tr_b16 a[24:25],v66 offset:4096\ns_waitcnt vmcnt(12)\nv_mfma_f32_16x16x32_bf16 v[0:3],v[92:95],a[112:115],v[0:3]\n" \
"s_waitcnt lgkmcnt(5)\ns_add_u32 m0,s57,0\nv_mfma_f32_16x16x32_bf16 v[4:7],v[88:91],a[100:103],v[4:7]\nbuffer_load_dwordx4 v68,s[40:43],s60 offen lds\n" \
"ds_read_b64_tr_b16 a[26:27],v66 offset:4608\nv_mfma_f32_16x16x32_bf16 v[4:7],v[92:95],a[116:119],v[4:7]\ns_waitcnt lgkmcnt(0)\ns_barrier\n" \
"v_mfma_f32_16x16x32_bf16 v[8:11],v[88:91],a[104:107],v[8:11]\nbuffer_load_dwordx4 v69,s[40:43],s60 offen offset:1024 lds\nds_read_b64_tr_b16 a[28:29],v67 offset:4096\nv_mfma_f32_16x16x32_bf16 v[8:11],v[92:95],a[120:123],v[8:11]\n" \
"ds_read_b64_tr_b16 a[30:31],v67 offset:4608\nds_read_b64_tr_b16 a[32:33],v64 offset:8192\nv_mfma_f32_16x16x32_bf16 v[12:15],v[88:91],a[108:111],v[12:15]\nbuffer_load_dwordx4 v70,s[40:43],s60 offen offset:2048 lds\n" \
"ds_read_b64_tr_b16 a[34:35],v64 offset:8704\nv_mfma_f32_16x16x32_bf16 v[12:15],v[92:95],a[124:127],v[12:15]\nds_read_b64_tr_b16 a[36:37],v65 offset:8192\nds_read_b64_tr_b16 a[38:39],v65 offset:8704\n" \
"v_mfma_f32_16x16x32_bf16 v[16:19],v[88:91],v[216:219],v[16:19]\nbuffer_load_dwordx4 v71,s[40:43],s60 offen offset:3072 lds\nds_read_b64_tr_b16 a[40:41],v66 offset:8192\nv_mfma_f32_16x16x32_bf16 v[16:19],v[92:95],v[220:223],v[16:19]\n" \
"ds_read_b64_tr_b16 a[42:43],v66 offset:8704\ns_waitcnt vmcnt(13)\nv_mfma_f32_16x16x32_bf16 v[0:3],v[96:99],a[128:131],v[0:3]\nglobal_load_dwordx4 v[80:83],v59,s[12:13] offset:2048 sc1\n" \
"s_waitcnt vmcnt(13)\nv_mfma_f32_16x16x32_bf16 v[0:3],v[100:103],a[144:147],v[0:3]\ns_waitcnt lgkmcnt(6)\ns_add_u32 m0,s57,4096\n" \
"v_mfma_f32_16x16x32_bf16 v[4:7],v[96:99],a[132:135],v[4:7]\nbuffer_load_dwordx4 v68,s[40:43],s61 offen lds\nds_read_b64_tr_b16 a[44:45],v67 offset:8192\nv_mfma_f32_16x16x32_bf16 v[4:7],v[100:103],a[148:151],v[4:7]\n" \
"ds_read_b64_tr_b16 a[46:47],v67 offset:8704\nds_read_b64_tr_b16 a[48:49],v64 offset:12288\nv_mfma_f32_16x16x32_bf16 v[8:11],v[96:99],a[136:139],v[8:11]\nbuffer_load_dwordx4 v69,s[40:43],s61 offen offset:1024 lds\n" \
"ds_read_b64_tr_b16 a[50:51],v64 offset:12800\nv_mfma_f32_16x16x32_bf16 v[8:11],v[100:103],a[152:155],v[8:11]\nds_read_b64_tr_b16 a[52:53],v65 offset:12288\nds_read_b64_tr_b16 a[54:55],v65 offset:12800\n" \
"v_mfma_f32_16x16x32_bf16 v[12:15],v[96:99],a[140:143],v[12:15]\nbuffer_load_dwordx4 v70,s[40:43],s61 offen offset:2048 lds\nds_read_b64_tr_b16 a[56:57],v66 offset:12288\nv_mfma_f32_16x16x32_bf16 v[12:15],v[100:103],a[156:159],v[12:15]\n" \
"ds_read_b64_tr_b16 a[58:59],v66 offset:12800\nds_read_b64_tr_b16 a[60:61],v67 offset:12288\nv_mfma_f32_16x16x32_bf16 v[16:19],v[96:99],v[224:227],v[16:19]\nbuffer_load_dwordx4 v71,s[40:43],s61 offen offset:3072 lds\n" \
"ds_read_b64_tr_b16 a[62:63],v67 offset:12800\nv_mfma_f32_16x16x32_bf16 v[16:19],v[100:103],v[228:231],v[16:19]\nds_read_b64_tr_b16 a[64:65],v64 offset:16384\ns_waitcnt vmcnt(16)\n" \
"v_mfma_f32_16x16x32_bf16 v[0:3],v[104:107],a[160:163],v[0:3]\nglobal_load_dwordx4 v[84:87],v59,s[14:15] sc1\ns_waitcnt vmcnt(16)\nv_mfma_f32_16x16x32_bf16 v[0:3],v[108:111],a[176:179],v[0:3]\n" \
"s_waitcnt lgkmcnt(9)\ns_add_u32 m0,s57,8192\nv_mfma_f32_16x16x32_bf16 v[4:7],v[104:107],a[164:167],v[4:7]\nbuffer_load_dwordx4 v68,s[40:43],s62 offen lds\n" \
"ds_read_b64_tr_b16 a[66:67],v64 offset:16896\nv_mfma_f32_16x16x32_bf16 v[4:7],v[108:111],a[180:183],v[4:7]\nds_read_b64_tr_b16 a[68:69],v65 offset:16384\nds_read_b64_tr_b16 a[70:71],v65 offset:16896\n" \
"v_mfma_f32_16x16x32_bf16 v[8:11],v[104:107],a[168:171],v[8:11]\nbuffer_load_dwordx4 v69,s[40:43],s62 offen offset:1024 lds\nds_read_b64_tr_b16 a[72:73],v66 offset:16384\nv_mfma_f32_16x16x32_bf16 v[8:11],v[108:111],a[184:187],v[8:11]\n" \
"ds_read_b64_tr_b16 a[74:75],v66 offset:16896\nds_read_b64_tr_b16 a[76:77],v67 offset:16384\nv_mfma_f32_16x16x32_bf16 v[12:15],v[104:107],a[172:175],v[12:15]\nbuffer_load_dwordx4 v70,s[40:43],s62 offen offset:2048 lds\n" \
"ds_read_b64_tr_b16 a[78:79],v67 offset:16896\nv_mfma_f32_16x16x32_bf16 v[12:15],v[108:111],a[188:191],v[12:15]\nds_read_b64_tr_b16 a[80:81],v64 offset:20480\nds_read_b64_tr_b16 a[82:83],v64 offset:20992\n" \
"v_mfma_f32_16x16x32_bf16 v[16:19],v[104:107],v[232:235],v[16:19]\nbuffer_load_dwordx4 v71,s[40:43],s62 offen offset:3072 lds\nds_read_b64_tr_b16 a[84:85],v65 offset:20480\nv_mfma_f32_16x16x32_bf16 v[16:19],v[108:111],v[236:239],v[16:19]\n" \
"ds_read_b64_tr_b16 a[86:87],v65 offset:20992\ns_waitcnt vmcnt(19)\nv_mfma_f32_16x16x32_bf16 v[0:3],v[112:115],a[192:195],v[0:3]\nglobal_load_dwordx4 v[88:91],v59,s[14:15] offset:1024 sc1\n" \
"s_waitcnt vmcnt(19)\nv_mfma_f32_16x16x32_bf16 v[0:3],v[116:119],a[208:211],v[0:3]\ns_waitcnt lgkmcnt(12)\ns_add_u32 m0,s57,12288\n" \
"v_mfma_f32_16x16x32_bf16 v[4:7],v[112:115],a[196:199],v[4:7]\nbuffer_load_dwordx4 v68,s[40:43],s63 offen lds\nds_read_b64_tr_b16 a[88:89],v66 offset:20480\nv_mfma_f32_16x16x32_bf16 v[4:7],v[116:119],a[212:215],v[4:7]\n" \
"ds_read_b64_tr_b16 a[90:91],v66 offset:20992\nds_read_b64_tr_b16 a[92:93],v67 offset:20480\nv_mfma_f32_16x16x32_bf16 v[8:11],v[112:115],a[200:203],v[8:11]\nbuffer_load_dwordx4 v69,s[40:43],s63 offen offset:1024 lds\n" \
"ds_read_b64_tr_b16 a[94:95],v67 offset:20992\nv_mfma_f32_16x16x32_bf16 v[8:11],v[116:119],a[216:219],v[8:11]\nds_read_b64_tr_b16 a[96:97],v64 offset:24576\nds_read_b64_tr_b16 a[98:99],v64 offset:25088\n" \
"v_mfma_f32_16x16x32_bf16 v[12:15],v[112:115],a[204:207],v[12:15]\nbuffer_load_dwordx4 v70,s[40:43],s63 offen offset:2048 lds\nds_read_b64_tr_b16 a[100:101],v65 offset:24576\nv_mfma_f32_16x16x32_bf16 v[12:15],v[116:119],a[220:223],v[12:15]\n" \
"ds_read_b64_tr_b16 a[102:103],v65 offset:25088\nds_read_b64_tr_b16 a[104:105],v66 offset:24576\nv_mfma_f32_16x16x32_bf16 v[16:19],v[112:115],v[240:243],v[16:19]\nbuffer_load_dwordx4 v71,s[40:43],s63 offen offset:3072 lds\n" \
"ds_read_b64_tr_b16 a[106:107],v66 offset:25088\nv_mfma_f32_16x16x32_bf16 v[16:19],v[116:119],v[244:247],v[16:19]\nds_read_b64_tr_b16 a[108:109],v67 offset:24576\ns_waitcnt vmcnt(22)\n" \
"v_mfma_f32_16x16x32_bf16 v[0:3],v[120:123],a[224:227],v[0:3]\nglobal_load_dwordx4 v[92:95],v59,s[14:15] offset:2048 sc1\ns_waitcnt vmcnt(22)\nv_mfma_f32_16x16x32_bf16 v[0:3],v[124:127],a[240:243],v[0:3]\n" \
"s_add_u32 m0,s57,16384\nv_mfma_f32_16x16x32_bf16 v[4:7],v[120:123],a[228:231],v[4:7]\nbuffer_load_dwordx4 v68,s[40:43],s64 offen lds\nds_read_b64_tr_b16 a[110:111],v67 offset:25088\n" \
"v_mfma_f32_16x16x32_bf16 v[4:7],v[124:127],a[244:247],v[4:7]\nv_add_f32 v40,1.0,v40\nv_add_f32 v41,1.0,v41\nv_mfma_f32_16x16x32_bf16 v[8:11],v[120:123],a[232:235],v[8:11]\n" \
"buffer_load_dwordx4 v69,s[40:43],s64 offen offset:1024 lds\nv_add_f32 v42,1.0,v42\nv_mfma_f32_16x16x32_bf16 v[8:11],v[124:127],a[248:251],v[8:11]\nv_add_f32 v43,1.0,v43\n" \
"s_waitcnt lgkmcnt(15)\nv_mfma_f32_16x16x32_bf16 v[12:15],v[120:123],a[236:239],v[12:15]\nbuffer_load_dwordx4 v70,s[40:43],s64 offen offset:2048 lds\nv_add_f32 v44,v44,v45\n" \
"v_mfma_f32_16x16x32_bf16 v[12:15],v[124:127],a[252:255],v[12:15]\nv_rcp_f32 v40,v40\nv_mfma_f32_16x16x32_bf16 v[16:19],v[120:123],v[248:251],v[16:19]\nbuffer_load_dwordx4 v71,s[40:43],s64 offen offset:3072 lds\n" \
"v_add_f32 v46,v46,v47\nv_mfma_f32_16x16x32_bf16 v[16:19],v[124:127],v[252:255],v[16:19]\ns_mov_b32 m0,s56\ns_waitcnt vmcnt(25)\n" \
"v_mfma_f32_16x16x32_bf16 v[208:211],v[128:131],v[72:75],0\nglobal_load_dwordx4 v[108:111],v59,s[4:5] sc1\ns_waitcnt vmcnt(25)\nv_mfma_f32_16x16x32_bf16 v[208:211],v[160:163],v[76:79],v[208:211]\n" \
"ds_write_addtid_b32 v0\nds_write_addtid_b32 v1 offset:544\nv_mfma_f32_16x16x32_bf16 v[212:215],v[132:135],v[72:75],0\nglobal_load_dwordx4 v[112:115],v59,s[4:5] offset:1024 sc1\n" \
"ds_write_addtid_b32 v2 offset:256\nv_mfma_f32_16x16x32_bf16 v[212:215],v[164:167],v[76:79],v[212:215]\nds_write_addtid_b32 v3 offset:800\nds_write_addtid_b32 v4 offset:1056\n" \
"v_mfma_f32_16x16x32_bf16 v[216:219],v[136:139],v[72:75],0\nglobal_load_dwordx4 v[116:119],v59,s[4:5] offset:2048 sc1\nds_write_addtid_b32 v5 offset:1600\nv_mfma_f32_16x16x32_bf16 v[216:219],v[168:171],v[76:79],v[216:219]\n" \
"ds_write_addtid_b32 v6 offset:1312\nds_write_addtid_b32 v7 offset:1856\nv_mfma_f32_16x16x32_bf16 v[220:223],v[140:143],v[72:75],0\nds_write_addtid_b32 v8 offset:2112\n" \
"ds_write_addtid_b32 v9 offset:2656\nv_mfma_f32_16x16x32_bf16 v[220:223],v[172:175],v[76:79],v[220:223]\nds_write_addtid_b32 v10 offset:2368\nds_write_addtid_b32 v11 offset:2912\n" \
"v_mfma_f32_16x16x32_bf16 v[224:227],v[144:147],v[72:75],0\nds_write_addtid_b32 v12 offset:3168\nds_write_addtid_b32 v13 offset:3712\nv_mfma_f32_16x16x32_bf16 v[224:227],v[176:179],v[76:79],v[224:227]\n" \
"ds_write_addtid_b32 v14 offset:3424\nds_write_addtid_b32 v15 offset:3968\nv_mfma_f32_16x16x32_bf16 v[228:231],v[148:151],v[72:75],0\nds_write_addtid_b32 v16 offset:4224\n" \
"ds_write_addtid_b32 v17 offset:4512\nv_mfma_f32_16x16x32_bf16 v[228:231],v[180:183],v[76:79],v[228:231]\nds_write_addtid_b32 v18 offset:4800\nds_write_addtid_b32 v19 offset:5088\n" \
"v_mfma_f32_16x16x32_bf16 v[232:235],v[152:155],v[72:75],0\ns_waitcnt lgkmcnt(15)\ns_add_u32 m0,s57,20480\nv_mfma_f32_16x16x32_bf16 v[232:235],v[184:187],v[76:79],v[232:235]\n" \
"buffer_load_dwordx4 v68,s[40:43],s65 offen lds\ns_waitcnt vmcnt(24)\nv_mfma_f32_16x16x32_bf16 v[236:239],v[156:159],v[72:75],0\nds_read_b64_tr_b16 a[112:113],v64\n" \
"ds_read_b64_tr_b16 a[114:115],v64 offset:512\nv_mfma_f32_16x16x32_bf16 v[236:239],v[188:191],v[76:79],v[236:239]\ns_waitcnt lgkmcnt(15)\ns_waitcnt vmcnt(23)\n" \
"v_mfma_f32_16x16x32_bf16 v[208:211],a[0:3],v[80:83],v[208:211]\ns_waitcnt vmcnt(18)\nv_mfma_f32_16x16x32_bf16 v[208:211],a[32:35],v[84:87],v[208:211]\nbuffer_load_dwordx4 v69,s[40:43],s65 offen offset:1024 lds\n" \
"ds_read_b64_tr_b16 a[116:117],v65\nv_mfma_f32_16x16x32_bf16 v[212:215],a[4:7],v[80:83],v[212:215]\ns_waitcnt lgkmcnt(0)\ns_barrier\n" \
"v_mfma_f32_16x16x32_bf16 v[212:215],a[36:39],v[84:87],v[212:215]\nbuffer_load_dwordx4 v70,s[40:43],s65 offen offset:2048 lds\nds_read_b64_tr_b16 a[118:119],v65 offset:512\nv_mfma_f32_16x16x32_bf16 v[216:219],a[8:11],v[80:83],v[216:219]\n" \
"ds_read_b64_tr_b16 a[120:121],v66\nds_read_b64_tr_b16 a[122:123],v66 offset:512\nv_mfma_f32_16x16x32_bf16 v[216:219],a[40:43],v[84:87],v[216:219]\nbuffer_load_dwordx4 v71,s[40:43],s65 offen offset:3072 lds\n" \
"ds_read_b64_tr_b16 a[124:125],v67\nv_mfma_f32_16x16x32_bf16 v[220:223],a[12:15],v[80:83],v[220:223]\nds_read_b64_tr_b16 a[126:127],v67 offset:512\nv_mfma_f32_16x16x32_bf16 v[220:223],a[44:47],v[84:87],v[220:223]\n" \
"global_load_dwordx4 v[120:123],v59,s[6:7] sc1\nv_mfma_f32_16x16x32_bf16 v[224:227],a[16:19],v[80:83],v[224:227]\nds_read_b64_tr_b16 a[128:129],v64 offset:4096\nv_mfma_f32_16x16x32_bf16 v[224:227],a[48:51],v[84:87],v[224:227]\n" \
"s_add_u32 m0,s57,24576\nv_mfma_f32_16x16x32_bf16 v[228:231],a[20:23],v[80:83],v[228:231]\nbuffer_load_dwordx4 v68,s[40:43],s66 offen lds\nds_read_b64_tr_b16 a[130:131],v64 offset:4608\n" \
"v_mfma_f32_16x16x32_bf16 v[228:231],a[52:55],v[84:87],v[228:231]\nds_read_b64_tr_b16 a[132:133],v65 offset:4096\nds_read_b64_tr_b16 a[134:135],v65 offset:4608\nv_mfma_f32_16x16x32_bf16 v[232:235],a[24:27],v[80:83],v[232:235]\n" \
"buffer_load_dwordx4 v69,s[40:43],s66 offen offset:1024 lds\nds_read_b64_tr_b16 a[136:137],v66 offset:4096\nv_mfma_f32_16x16x32_bf16 v[232:235],a[56:59],v[84:87],v[232:235]\nds_read_b64_tr_b16 a[138:139],v66 offset:4608\n" \
"ds_read_b64_tr_b16 a[140:141],v67 offset:4096\nv_mfma_f32_16x16x32_bf16 v[236:239],a[28:31],v[80:83],v[236:239]\nbuffer_load_dwordx4 v70,s[40:43],s66 offen offset:2048 lds\nds_read_b64_tr_b16 a[142:143],v67 offset:4608\n" \
"v_mfma_f32_16x16x32_bf16 v[236:239],a[60:63],v[84:87],v[236:239]\ns_waitcnt vmcnt(20)\nv_mfma_f32_16x16x32_bf16 v[208:211],a[64:67],v[88:91],v[208:211]\ns_waitcnt vmcnt(15)\n" \
"v_mfma_f32_16x16x32_bf16 v[208:211],a[96:99],v[92:95],v[208:211]\nbuffer_load_dwordx4 v71,s[40:43],s66 offen offset:3072 lds\nv_mfma_f32_16x16x32_bf16 v[212:215],a[68:71],v[88:91],v[212:215]\nds_read_b64_tr_b16 a[144:145],v64 offset:8192\n" \
"ds_read_b64_tr_b16 a[146:147],v64 offset:8704\nv_mfma_f32_16x16x32_bf16 v[212:215],a[100:103],v[92:95],v[212:215]\nglobal_load_dwordx4 v[124:127],v59,s[6:7] offset:1024 sc1\nds_read_b64_tr_b16 a[148:149],v65 offset:8192\n" \
"v_mfma_f32_16x16x32_bf16 v[216:219],a[72:75],v[88:91],v[216:219]\nds_read_b64_tr_b16 a[150:151],v65 offset:8704\nds_read_b64_tr_b16 a[152:153],v66 offset:8192\nv_mfma_f32_16x16x32_bf16 v[216:219],a[104:107],v[92:95],v[216:219]\n" \
"global_load_dwordx4 v[192:195],v59,s[6:7] offset:2048 sc1\nds_read_b64_tr_b16 a[154:155],v66 offset:8704\nv_mfma_f32_16x16x32_bf16 v[220:223],a[76:79],v[88:91],v[220:223]\nds_read_b64_tr_b16 a[156:157],v67 offset:8192\n" \
"ds_read_b64_tr_b16 a[158:159],v67 offset:8704\nv_mfma_f32_16x16x32_bf16 v[220:223],a[108:111],v[92:95],v[220:223]\nglobal_load_dwordx4 v[96:99],v59,s[16:17] sc1\ns_waitcnt lgkmcnt(15)\n" \
"v_mfma_f32_16x16x32_bf16 v[224:227],a[80:83],v[88:91],v[224:227]\nds_read_b64_tr_b16 a[160:161],v64 offset:12288\nv_mfma_f32_16x16x32_bf16 v[228:231],a[84:87],v[88:91],v[228:231]\nglobal_load_dwordx4 v[100:103],v59,s[16:17] offset:1024 sc1\n" \
"ds_read_b64_tr_b16 a[162:163],v64 offset:12800\nv_mfma_f32_16x16x32_bf16 v[232:235],a[88:91],v[88:91],v[232:235]\nds_read_b64_tr_b16 a[164:165],v65 offset:12288\nds_read_b64_tr_b16 a[166:167],v65 offset:12800\n" \
"v_mfma_f32_16x16x32_bf16 v[236:239],a[92:95],v[88:91],v[236:239]\nglobal_load_dwordx4 v[104:107],v59,s[16:17] offset:2048 sc1\ns_waitcnt vmcnt(16)\nv_mfma_f32_16x16x32_bf16 v[0:3],v[128:131],v[108:111],0\n" \
"ds_read_b64_tr_b16 a[168:169],v66 offset:12288\ns_waitcnt vmcnt(15)\nv_mfma_f32_16x16x32_bf16 v[0:3],v[160:163],v[112:115],v[0:3]\nglobal_load_dwordx4 v[196:199],v59,s[8:9] sc1\n" \
"ds_read_b64_tr_b16 a[170:171],v66 offset:12800\nv_mfma_f32_16x16x32_bf16 v[4:7],v[132:135],v[108:111],0\nds_read_b64_tr_b16 a[172:173],v67 offset:12288\nds_read_b64_tr_b16 a[174:175],v67 offset:12800\n" \
"v_mfma_f32_16x16x32_bf16 v[4:7],v[164:167],v[112:115],v[4:7]\nglobal_load_dwordx4 v[200:203],v59,s[8:9] offset:1024 sc1\nds_read_b32 v252,v60\nv_mfma_f32_16x16x32_bf16 v[8:11],v[136:139],v[108:111],0\n" \
"s_waitcnt vmcnt(19)\nds_read_b64_tr_b16 a[176:177],v64 offset:16384\nv_mfma_f32_16x16x32_bf16 v[8:11],v[168:171],v[112:115],v[8:11]\nglobal_load_dwordx4 v[204:207],v59,s[8:9] offset:2048 sc1\n" \
"ds_read_b64_tr_b16 a[178:179],v64 offset:16896\nv_mfma_f32_16x16x32_bf16 v[12:15],v[140:143],v[108:111],0\ns_add_u32 s59,s59,0x10000\nbuffer_load_dword v63,v61,s[36:39],s59 offen sc1\n" \
"v_mfma_f32_16x16x32_bf16 v[12:15],v[172:175],v[112:115],v[12:15]\nv_add_u32 v59,819200,v59\nds_read_b64_tr_b16 a[180:181],v65 offset:16384\nv_mfma_f32_16x16x32_bf16 v[16:19],v[144:147],v[108:111],0\n" \
"ds_read_b64_tr_b16 a[182:183],v65 offset:16896\nds_read_b64_tr_b16 a[184:185],v66 offset:16384\nv_mfma_f32_16x16x32_bf16 v[16:19],v[176:179],v[112:115],v[16:19]\nds_read_b64_tr_b16 a[186:187],v66 offset:16896\n" \
"ds_read_b64_tr_b16 a[188:189],v67 offset:16384\nv_mfma_f32_16x16x32_bf16 v[20:23],v[148:151],v[108:111],0\nds_read_b64_tr_b16 a[190:191],v67 offset:16896\nds_read_b32 v253,v60 offset:9568\n" \
"v_mfma_f32_16x16x32_bf16 v[20:23],v[180:183],v[112:115],v[20:23]\nds_read_b32 v254,v60 offset:19136\nds_read_b32 v255,v60 offset:28704\nv_mfma_f32_16x16x32_bf16 v[24:27],v[152:155],v[108:111],0\n" \
"v_rcp_f32 v41,v41\nv_mfma_f32_16x16x32_bf16 v[24:27],v[184:187],v[112:115],v[24:27]\nv_add_f32 v44,v44,v56\nv_add_f32 v44,v44,v46\n" \
"v_mfma_f32_16x16x32_bf16 v[28:31],v[156:159],v[108:111],0\nv_rcp_f32 v42,v42\nv_mfma_f32_16x16x32_bf16 v[28:31],v[188:191],v[112:115],v[28:31]\nv_mul_f32 v45,v44,v44\n" \
"s_waitcnt vmcnt(18)\nv_mfma_f32_16x16x32_bf16 v[0:3],a[0:3],v[116:119],v[0:3]\nv_fma_f32 v45,v45,s55,v57\ns_waitcnt vmcnt(13)\n" \
"v_mfma_f32_16x16x32_bf16 v[0:3],a[32:35],v[120:123],v[0:3]\ns_waitcnt lgkmcnt(0)\nv_add_f32 v252,v252,v253\nv_mfma_f32_16x16x32_bf16 v[4:7],a[4:7],v[116:119],v[4:7]\n" \
"v_add_f32 v254,v254,v255\nv_add_f32 v252,v252,v56\nv_mfma_f32_16x16x32_bf16 v[4:7],a[36:39],v[120:123],v[4:7]\nv_add_f32 v252,v252,v254\n" \
"v_mul_f32 v253,v252,v252\nv_mfma_f32_16x16x32_bf16 v[8:11],a[8:11],v[116:119],v[8:11]\nv_fma_f32 v253,v253,s55,v57\nv_mul_f32 v45,v44,v45\n" \
"v_mfma_f32_16x16x32_bf16 v[8:11],a[40:43],v[120:123],v[8:11]\nv_rcp_f32 v43,v43\nv_mfma_f32_16x16x32_bf16 v[12:15],a[12:15],v[116:119],v[12:15]\nv_mul_f32 v253,v252,v253\n" \
"v_mul_f32 v36,v36,v40\nv_mfma_f32_16x16x32_bf16 v[12:15],a[44:47],v[120:123],v[12:15]\nv_exp_f32 v45,v45\nv_mfma_f32_16x16x32_bf16 v[16:19],a[16:19],v[116:119],v[16:19]\n" \
"v_exp_f32 v253,v253\nv_mfma_f32_16x16x32_bf16 v[16:19],a[48:51],v[120:123],v[16:19]\nv_mul_f32 v37,v37,v41\nv_add_f32 v45,1.0,v45\n" \
"v_mfma_f32_16x16x32_bf16 v[20:23],a[20:23],v[116:119],v[20:23]\nv_add_f32 v253,1.0,v253\nv_mul_f32 v38,v38,v42\nv_mfma_f32_16x16x32_bf16 v[20:23],a[52:55],v[120:123],v[20:23]\n" \
"ds_read_b64_tr_b16 a[192:193],v64 offset:20480\nv_mfma_f32_16x16x32_bf16 v[24:27],a[24:27],v[116:119],v[24:27]\nds_read_b64_tr_b16 a[194:195],v64 offset:20992\nds_read_b64_tr_b16 a[196:197],v65 offset:20480\n" \
"v_mfma_f32_16x16x32_bf16 v[24:27],a[56:59],v[120:123],v[24:27]\nds_read_b64_tr_b16 a[198:199],v65 offset:20992\nds_read_b64_tr_b16 a[200:201],v66 offset:20480\nv_mfma_f32_16x16x32_bf16 v[28:31],a[28:31],v[116:119],v[28:31]\n" \
"ds_read_b64_tr_b16 a[202:203],v66 offset:20992\nds_read_b64_tr_b16 a[204:205],v67 offset:20480\nv_mfma_f32_16x16x32_bf16 v[28:31],a[60:63],v[120:123],v[28:31]\nds_read_b64_tr_b16 a[206:207],v67 offset:20992\n" \
"s_waitcnt vmcnt(8)\nv_mfma_f32_16x16x32_bf16 v[0:3],a[64:67],v[124:127],v[0:3]\nv_mul_f32 v39,v39,v43\ns_waitcnt vmcnt(7)\n" \
"v_mfma_f32_16x16x32_bf16 v[0:3],a[96:99],v[192:195],v[0:3]\nv_rcp_f32 v45,v45\nv_mfma_f32_16x16x32_bf16 v[4:7],a[68:71],v[124:127],v[4:7]\nv_rcp_f32 v253,v253\n" \
"v_mfma_f32_16x16x32_bf16 v[4:7],a[100:103],v[192:195],v[4:7]\nv_cvt_pk_bf16_f32 v40,v36,v37\nv_mul_f32 v44,v44,v45\nv_mfma_f32_16x16x32_bf16 v[8:11],a[72:75],v[124:127],v[8:11]\n" \
"v_mul_f32 v252,v252,v253\nv_cvt_pk_bf16_f32 v41,v38,v39\nv_mfma_f32_16x16x32_bf16 v[8:11],a[104:107],v[192:195],v[8:11]\nv_cvt_pk_bf16_f32 v45,v44,v44\n" \
"v_cvt_pk_bf16_f32 v253,v252,v252\nv_mfma_f32_16x16x32_bf16 v[12:15],a[76:79],v[124:127],v[12:15]\nbuffer_store_dwordx2 v[40:41],v61,s[36:39],s52 offen sc1\nbuffer_store_short v45,v62,s[36:39],s52 offen sc1\n" \
"ds_read_b128 v[36:39],v58 offset:5344\nds_read_b128 v[40:43],v58 offset:14912\nds_read_b128 v[44:47],v58 offset:24480\nds_read_b128 v[48:51],v58 offset:34048\n" \
"s_waitcnt lgkmcnt(2)\nv_add_f32 v36,v36,v40\nv_add_f32 v37,v37,v41\nv_add_f32 v38,v38,v42\n" \
"v_add_f32 v39,v39,v43\nv_add_f32 v36,v36,v52\nv_add_f32 v37,v37,v53\nv_add_f32 v38,v38,v54\n" \
"v_add_f32 v39,v39,v55\ns_waitcnt lgkmcnt(0)\nv_add_f32 v44,v44,v48\nv_add_f32 v45,v45,v49\n" \
"v_add_f32 v46,v46,v50\nv_add_f32 v47,v47,v51\nv_add_f32 v36,v36,v44\nv_add_f32 v37,v37,v45\n" \
"v_add_f32 v38,v38,v46\nv_add_f32 v39,v39,v47\nv_mul_f32 v40,v36,v36\nv_mul_f32 v41,v37,v37\n" \
"v_mul_f32 v42,v38,v38\nv_mul_f32 v43,v39,v39\nv_fma_f32 v40,v40,s55,v57\nv_fma_f32 v41,v41,s55,v57\n" \
"v_fma_f32 v42,v42,s55,v57\nv_fma_f32 v43,v43,s55,v57\nv_mul_f32 v40,v36,v40\nv_mul_f32 v41,v37,v41\n" \
"v_mul_f32 v42,v38,v42\nv_mul_f32 v43,v39,v43\nv_exp_f32 v40,v40\nv_exp_f32 v41,v41\n" \
"v_exp_f32 v42,v42\nv_exp_f32 v43,v43\nv_add_f32 v40,1.0,v40\nv_add_f32 v41,1.0,v41\n" \
"v_add_f32 v42,1.0,v42\nv_add_f32 v43,1.0,v43\nv_rcp_f32 v40,v40\nv_rcp_f32 v41,v41\n" \
"v_rcp_f32 v42,v42\nv_rcp_f32 v43,v43\nv_mul_f32 v36,v36,v40\nv_mul_f32 v37,v37,v41\n" \
"v_mul_f32 v38,v38,v42\nv_mul_f32 v39,v39,v43\nv_cvt_pk_bf16_f32 v40,v36,v37\nv_cvt_pk_bf16_f32 v41,v38,v39\n" \
"v_mfma_f32_16x16x32_bf16 v[12:15],a[108:111],v[192:195],v[12:15]\ns_add_u32 s52,s52,409600\nbuffer_store_dwordx2 v[40:41],v61,s[36:39],s52 offen sc1\nds_read_b128 v[36:39],v58\n" \
"v_mfma_f32_16x16x32_bf16 v[16:19],a[80:83],v[124:127],v[16:19]\nds_read_b128 v[40:43],v58 offset:9568\nds_read_b128 v[44:47],v58 offset:19136\nv_mfma_f32_16x16x32_bf16 v[20:23],a[84:87],v[124:127],v[20:23]\n" \
"ds_read_b128 v[48:51],v58 offset:28704\ns_add_u32 s52,s52,409600\nv_mfma_f32_16x16x32_bf16 v[24:27],a[88:91],v[124:127],v[24:27]\nds_read_b64_tr_b16 a[208:209],v64 offset:24576\n" \
"v_mfma_f32_16x16x32_bf16 v[28:31],a[92:95],v[124:127],v[28:31]\nds_read_b64_tr_b16 a[210:211],v64 offset:25088\nv_mfma_f32_16x16x32_bf16 v[224:227],a[112:115],v[92:95],v[224:227]\ns_waitcnt vmcnt(9)\n" \
"v_mfma_f32_16x16x32_bf16 v[224:227],a[144:147],v[96:99],v[224:227]\nds_read_b64_tr_b16 a[212:213],v65 offset:24576\nds_read_b64_tr_b16 a[214:215],v65 offset:25088\nv_mfma_f32_16x16x32_bf16 v[228:231],a[116:119],v[92:95],v[228:231]\n" \
"ds_read_b64_tr_b16 a[216:217],v66 offset:24576\nds_read_b64_tr_b16 a[218:219],v66 offset:25088\nv_mfma_f32_16x16x32_bf16 v[228:231],a[148:151],v[96:99],v[228:231]\nds_read_b64_tr_b16 a[220:221],v67 offset:24576\n" \
"ds_read_b64_tr_b16 a[222:223],v67 offset:25088\nv_mfma_f32_16x16x32_bf16 v[232:235],a[120:123],v[92:95],v[232:235]\ns_waitcnt lgkmcnt(10)\nv_add_f32 v36,v36,v40\n" \
"v_add_f32 v37,v37,v41\nv_mfma_f32_16x16x32_bf16 v[232:235],a[152:155],v[96:99],v[232:235]\nv_add_f32 v38,v38,v42\nv_add_f32 v39,v39,v43\n" \
"v_add_f32 v36,v36,v52\nv_mfma_f32_16x16x32_bf16 v[236:239],a[124:127],v[92:95],v[236:239]\nv_add_f32 v37,v37,v53\nv_add_f32 v38,v38,v54\n" \
"v_add_f32 v39,v39,v55\nv_mfma_f32_16x16x32_bf16 v[236:239],a[156:159],v[96:99],v[236:239]\ns_waitcnt lgkmcnt(8)\nv_add_f32 v44,v44,v48\n" \
"v_mfma_f32_16x16x32_bf16 v[208:211],a[128:131],v[96:99],v[208:211]\nv_add_f32 v45,v45,v49\ns_waitcnt vmcnt(8)\nv_mfma_f32_16x16x32_bf16 v[208:211],a[160:163],v[100:103],v[208:211]\n" \
"v_add_f32 v46,v46,v50\nv_add_f32 v47,v47,v51\nv_add_f32 v36,v36,v44\nv_mfma_f32_16x16x32_bf16 v[212:215],a[132:135],v[96:99],v[212:215]\n" \
"v_add_f32 v37,v37,v45\nv_add_f32 v38,v38,v46\nv_add_f32 v39,v39,v47\nv_mfma_f32_16x16x32_bf16 v[212:215],a[164:167],v[100:103],v[212:215]\n" \
"v_mul_f32 v40,v36,v36\nv_mul_f32 v41,v37,v37\nv_mul_f32 v42,v38,v38\nv_mfma_f32_16x16x32_bf16 v[216:219],a[136:139],v[96:99],v[216:219]\n" \
"v_mul_f32 v43,v39,v39\nv_fma_f32 v40,v40,s55,v57\nv_fma_f32 v41,v41,s55,v57\nv_mfma_f32_16x16x32_bf16 v[216:219],a[168:171],v[100:103],v[216:219]\n" \
"v_fma_f32 v42,v42,s55,v57\nv_fma_f32 v43,v43,s55,v57\nv_mul_f32 v40,v36,v40\nv_mfma_f32_16x16x32_bf16 v[220:223],a[140:143],v[96:99],v[220:223]\n" \
"v_mul_f32 v41,v37,v41\nv_mul_f32 v42,v38,v42\nv_mul_f32 v43,v39,v43\nv_mfma_f32_16x16x32_bf16 v[220:223],a[172:175],v[100:103],v[220:223]\n" \
"v_exp_f32 v40,v40\nv_mfma_f32_16x16x32_bf16 v[224:227],a[176:179],v[100:103],v[224:227]\ns_waitcnt lgkmcnt(0)\ns_waitcnt vmcnt(7)\n" \
"v_mfma_f32_16x16x32_bf16 v[224:227],a[208:211],v[104:107],v[224:227]\nv_exp_f32 v41,v41\nv_mfma_f32_16x16x32_bf16 v[228:231],a[180:183],v[100:103],v[228:231]\nv_exp_f32 v42,v42\n" \
"v_mfma_f32_16x16x32_bf16 v[228:231],a[212:215],v[104:107],v[228:231]\nv_exp_f32 v43,v43\nv_add_f32 v40,1.0,v40\nv_mfma_f32_16x16x32_bf16 v[232:235],a[184:187],v[100:103],v[232:235]\n" \
"v_add_f32 v41,1.0,v41\nv_add_f32 v42,1.0,v42\nv_add_f32 v43,1.0,v43\nv_mfma_f32_16x16x32_bf16 v[232:235],a[216:219],v[104:107],v[232:235]\n" \
"v_rcp_f32 v40,v40\nv_mfma_f32_16x16x32_bf16 v[236:239],a[188:191],v[100:103],v[236:239]\nv_rcp_f32 v41,v41\nv_rcp_f32 v42,v42\n" \
"v_rcp_f32 v43,v43\nv_mul_f32 v36,v36,v40\nv_mul_f32 v37,v37,v41\nv_mul_f32 v38,v38,v42\n" \
"v_mul_f32 v39,v39,v43\nv_cvt_pk_bf16_f32 v40,v36,v37\nv_cvt_pk_bf16_f32 v41,v38,v39\nv_mfma_f32_16x16x32_bf16 v[236:239],a[220:223],v[104:107],v[236:239]\n" \
"buffer_store_short v253,v62,s[36:39],s52 offen sc1\nv_mfma_f32_16x16x32_bf16 v[208:211],a[192:195],v[104:107],v[208:211]\nv_and_b32 v58,0x3ff,v59\nv_add_u32 v58,s99,v58\n" \
"v_mfma_f32_16x16x32_bf16 v[212:215],a[196:199],v[104:107],v[212:215]\nv_add_u32 v46,s98,v58\nds_read_b64 v[56:57],v46 offset:28680\nv_mfma_f32_16x16x32_bf16 v[216:219],a[200:203],v[104:107],v[216:219]\n" \
"buffer_store_dwordx2 v[40:41],v61,s[36:39],s52 offen sc1\nglobal_load_dwordx4 v[72:75],v59,s[12:13] sc1\nv_mfma_f32_16x16x32_bf16 v[220:223],a[204:207],v[104:107],v[220:223]\nv_mfma_f32_16x16x32_bf16 v[16:19],a[112:115],v[192:195],v[16:19]\n" \
"global_load_dwordx4 v[76:79],v59,s[12:13] offset:1024 sc1\ns_waitcnt vmcnt(10)\nv_mfma_f32_16x16x32_bf16 v[16:19],a[144:147],v[196:199],v[16:19]\ns_waitcnt lgkmcnt(0)\n" \
"s_barrier\ns_mov_b32 m0,s58\nv_mfma_f32_16x16x32_bf16 v[20:23],a[116:119],v[192:195],v[20:23]\nglobal_load_dwordx4 v[80:83],v59,s[12:13] offset:2048 sc1\n" \
"ds_write_addtid_b32 v208\nv_mfma_f32_16x16x32_bf16 v[20:23],a[148:151],v[196:199],v[20:23]\nds_write_addtid_b32 v209 offset:256\nds_write_addtid_b32 v210 offset:512\n" \
"v_mfma_f32_16x16x32_bf16 v[24:27],a[120:123],v[192:195],v[24:27]\nglobal_load_dwordx4 v[84:87],v59,s[14:15] sc1\nds_write_addtid_b32 v211 offset:768\nv_mfma_f32_16x16x32_bf16 v[24:27],a[152:155],v[196:199],v[24:27]\n" \
"ds_write_addtid_b32 v212 offset:1024\nds_write_addtid_b32 v213 offset:1280\nv_mfma_f32_16x16x32_bf16 v[28:31],a[124:127],v[192:195],v[28:31]\nglobal_load_dwordx4 v[88:91],v59,s[14:15] offset:1024 sc1\n" \
"ds_write_addtid_b32 v214 offset:1536\nv_mfma_f32_16x16x32_bf16 v[28:31],a[156:159],v[196:199],v[28:31]\nds_write_addtid_b32 v215 offset:1792\nds_write_addtid_b32 v216 offset:2048\n" \
"v_mfma_f32_16x16x32_bf16 v[0:3],a[128:131],v[196:199],v[0:3]\nglobal_load_dwordx4 v[92:95],v59,s[14:15] offset:2048 sc1\ns_waitcnt vmcnt(13)\nv_mfma_f32_16x16x32_bf16 v[0:3],a[160:163],v[200:203],v[0:3]\n" \
"ds_write_addtid_b32 v217 offset:2304\nds_write_addtid_b32 v218 offset:2560\nv_mfma_f32_16x16x32_bf16 v[4:7],a[132:135],v[196:199],v[4:7]\nglobal_load_dwordx4 v[96:99],v59,s[16:17] sc1\n" \
"ds_write_addtid_b32 v219 offset:2816\nv_mfma_f32_16x16x32_bf16 v[4:7],a[164:167],v[200:203],v[4:7]\nds_write_addtid_b32 v220 offset:3072\nds_write_addtid_b32 v221 offset:3328\n" \
"v_mfma_f32_16x16x32_bf16 v[8:11],a[136:139],v[196:199],v[8:11]\nglobal_load_dwordx4 v[100:103],v59,s[16:17] offset:1024 sc1\nds_write_addtid_b32 v222 offset:3584\nv_mfma_f32_16x16x32_bf16 v[8:11],a[168:171],v[200:203],v[8:11]\n" \
"ds_write_addtid_b32 v223 offset:3840\nds_write_addtid_b32 v224 offset:4096\nv_mfma_f32_16x16x32_bf16 v[12:15],a[140:143],v[196:199],v[12:15]\nglobal_load_dwordx4 v[104:107],v59,s[16:17] offset:2048 sc1\n" \
"ds_write_addtid_b32 v225 offset:4352\nv_mfma_f32_16x16x32_bf16 v[12:15],a[172:175],v[200:203],v[12:15]\nds_write_addtid_b32 v226 offset:4608\nds_write_addtid_b32 v227 offset:4864\n" \
"v_mfma_f32_16x16x32_bf16 v[16:19],a[176:179],v[200:203],v[16:19]\nds_write_addtid_b32 v228 offset:5120\ns_waitcnt vmcnt(15)\nv_mfma_f32_16x16x32_bf16 v[16:19],a[208:211],v[204:207],v[16:19]\n" \
"ds_write_addtid_b32 v229 offset:5376\nds_write_addtid_b32 v230 offset:5632\nv_mfma_f32_16x16x32_bf16 v[20:23],a[180:183],v[200:203],v[20:23]\nds_write_addtid_b32 v231 offset:5888\n" \
"ds_write_addtid_b32 v232 offset:6144\nv_mfma_f32_16x16x32_bf16 v[20:23],a[212:215],v[204:207],v[20:23]\nds_write_addtid_b32 v233 offset:6400\nds_write_addtid_b32 v234 offset:6656\n" \
"v_mfma_f32_16x16x32_bf16 v[24:27],a[184:187],v[200:203],v[24:27]\nds_write_addtid_b32 v235 offset:6912\nds_write_addtid_b32 v236 offset:7168\nv_mfma_f32_16x16x32_bf16 v[24:27],a[216:219],v[204:207],v[24:27]\n" \
"ds_write_addtid_b32 v237 offset:7424\nds_write_addtid_b32 v238 offset:7680\nv_mfma_f32_16x16x32_bf16 v[28:31],a[188:191],v[200:203],v[28:31]\nds_write_addtid_b32 v239 offset:7936\n" \
"v_mfma_f32_16x16x32_bf16 v[28:31],a[220:223],v[204:207],v[28:31]\nv_mfma_f32_16x16x32_bf16 v[0:3],a[192:195],v[204:207],v[0:3]\nv_mfma_f32_16x16x32_bf16 v[4:7],a[196:199],v[204:207],v[4:7]\nv_mfma_f32_16x16x32_bf16 v[8:11],a[200:203],v[204:207],v[8:11]\n" \
"v_mfma_f32_16x16x32_bf16 v[12:15],a[204:207],v[204:207],v[12:15]\ns_waitcnt vmcnt(7)\nv_mfma_f32_16x16x32_bf16 v[208:211],v[128:131],v[72:75],0\ns_mov_b64 exec,1\n" \
"buffer_store_dword v61,off,s[36:39],s54 sc1\ns_mov_b64 exec,-1\nv_mfma_f32_16x16x32_bf16 v[208:211],v[160:163],v[76:79],v[208:211]\nv_cmp_eq_u32 vcc,0,v63\n" \
"v_mfma_f32_16x16x32_bf16 v[212:215],v[132:135],v[72:75],0\ns_waitcnt lgkmcnt(0)\ns_barrier\nv_mfma_f32_16x16x32_bf16 v[212:215],v[164:167],v[76:79],v[212:215]\n" \
"s_and_b64 vcc,vcc,s[86:87]\ns_cbranch_scc1 .LsbF_%=\n.LbbF_%=:\nv_mfma_f32_16x16x32_bf16 v[216:219],v[136:139],v[72:75],0\n" \
"global_load_dwordx4 v[108:111],v59,s[4:5] sc1\nds_write_addtid_b32 v0 offset:32768\nv_mfma_f32_16x16x32_bf16 v[216:219],v[168:171],v[76:79],v[216:219]\nds_write_addtid_b32 v1 offset:33024\n" \
"ds_write_addtid_b32 v2 offset:33280\nv_mfma_f32_16x16x32_bf16 v[220:223],v[140:143],v[72:75],0\nglobal_load_dwordx4 v[112:115],v59,s[4:5] offset:1024 sc1\nds_write_addtid_b32 v3 offset:33536\n" \
"v_mfma_f32_16x16x32_bf16 v[220:223],v[172:175],v[76:79],v[220:223]\nds_write_addtid_b32 v4 offset:33792\nds_write_addtid_b32 v5 offset:34048\nv_mfma_f32_16x16x32_bf16 v[224:227],v[144:147],v[72:75],0\n" \
"global_load_dwordx4 v[116:119],v59,s[4:5] offset:2048 sc1\nds_write_addtid_b32 v6 offset:34304\nv_mfma_f32_16x16x32_bf16 v[224:227],v[176:179],v[76:79],v[224:227]\nds_write_addtid_b32 v7 offset:34560\n" \
"ds_write_addtid_b32 v8 offset:34816\nv_mfma_f32_16x16x32_bf16 v[228:231],v[148:151],v[72:75],0\nglobal_load_dwordx4 v[120:123],v59,s[6:7] sc1\nds_write_addtid_b32 v9 offset:35072\n" \
"v_mfma_f32_16x16x32_bf16 v[228:231],v[180:183],v[76:79],v[228:231]\nds_write_addtid_b32 v10 offset:35328\nds_write_addtid_b32 v11 offset:35584\nv_mfma_f32_16x16x32_bf16 v[232:235],v[152:155],v[72:75],0\n" \
"global_load_dwordx4 v[124:127],v59,s[6:7] offset:1024 sc1\nds_write_addtid_b32 v12 offset:35840\nv_mfma_f32_16x16x32_bf16 v[232:235],v[184:187],v[76:79],v[232:235]\nds_write_addtid_b32 v13 offset:36096\n" \
"ds_write_addtid_b32 v14 offset:36352\nv_mfma_f32_16x16x32_bf16 v[236:239],v[156:159],v[72:75],0\nglobal_load_dwordx4 v[192:195],v59,s[6:7] offset:2048 sc1\nds_write_addtid_b32 v15 offset:36608\n" \
"v_mfma_f32_16x16x32_bf16 v[236:239],v[188:191],v[76:79],v[236:239]\nds_write_addtid_b32 v16 offset:36864\ns_waitcnt vmcnt(12)\nv_mfma_f32_16x16x32_bf16 v[208:211],a[0:3],v[80:83],v[208:211]\n" \
"global_load_dwordx4 v[196:199],v59,s[8:9] sc1\nds_write_addtid_b32 v17 offset:37120\nv_mfma_f32_16x16x32_bf16 v[208:211],a[32:35],v[84:87],v[208:211]\nds_write_addtid_b32 v18 offset:37376\n" \
"ds_write_addtid_b32 v19 offset:37632\nv_mfma_f32_16x16x32_bf16 v[212:215],a[4:7],v[80:83],v[212:215]\nglobal_load_dwordx4 v[200:203],v59,s[8:9] offset:1024 sc1\nds_write_addtid_b32 v20 offset:37888\n" \
"v_mfma_f32_16x16x32_bf16 v[212:215],a[36:39],v[84:87],v[212:215]\nds_write_addtid_b32 v21 offset:38144\nds_write_addtid_b32 v22 offset:38400\nv_mfma_f32_16x16x32_bf16 v[216:219],a[8:11],v[80:83],v[216:219]\n" \
"global_load_dwordx4 v[204:207],v59,s[8:9] offset:2048 sc1\nds_write_addtid_b32 v23 offset:38656\nv_mfma_f32_16x16x32_bf16 v[216:219],a[40:43],v[84:87],v[216:219]\nds_write_addtid_b32 v24 offset:38912\n" \
"ds_write_addtid_b32 v25 offset:39168\nv_mfma_f32_16x16x32_bf16 v[220:223],a[12:15],v[80:83],v[220:223]\nds_write_addtid_b32 v26 offset:39424\nds_write_addtid_b32 v27 offset:39680\n" \
"v_mfma_f32_16x16x32_bf16 v[220:223],a[44:47],v[84:87],v[220:223]\nds_write_addtid_b32 v28 offset:39936\nds_write_addtid_b32 v29 offset:40192\nv_mfma_f32_16x16x32_bf16 v[224:227],a[16:19],v[80:83],v[224:227]\n" \
"ds_write_addtid_b32 v30 offset:40448\nds_write_addtid_b32 v31 offset:40704\nv_mfma_f32_16x16x32_bf16 v[224:227],a[48:51],v[84:87],v[224:227]\nds_read_b128 v[32:35],v58\n" \
"ds_read_b128 v[36:39],v58 offset:8192\nv_mfma_f32_16x16x32_bf16 v[228:231],a[20:23],v[80:83],v[228:231]\nds_read_b128 v[40:43],v58 offset:16384\nds_read_b128 v[44:47],v58 offset:24576\n" \
"v_mfma_f32_16x16x32_bf16 v[228:231],a[52:55],v[84:87],v[228:231]\nds_read_b128 v[48:51],v58 offset:4096\nds_read_b128 v[240:243],v58 offset:12288\nv_mfma_f32_16x16x32_bf16 v[232:235],a[24:27],v[80:83],v[232:235]\n" \
"ds_read_b128 v[244:247],v58 offset:20480\nds_read_b128 v[248:251],v58 offset:28672\nv_mfma_f32_16x16x32_bf16 v[232:235],a[56:59],v[84:87],v[232:235]\nv_mfma_f32_16x16x32_bf16 v[236:239],a[28:31],v[80:83],v[236:239]\n" \
"s_waitcnt lgkmcnt(0)\ns_barrier\nv_mfma_f32_16x16x32_bf16 v[236:239],a[60:63],v[84:87],v[236:239]\ns_waitcnt vmcnt(13)\n" \
"v_mfma_f32_16x16x32_bf16 v[208:211],a[64:67],v[88:91],v[208:211]\nv_mfma_f32_16x16x32_bf16 v[208:211],a[96:99],v[92:95],v[208:211]\nv_mfma_f32_16x16x32_bf16 v[212:215],a[68:71],v[88:91],v[212:215]\nv_add_f32 v32,v32,v36\n" \
"v_mfma_f32_16x16x32_bf16 v[212:215],a[100:103],v[92:95],v[212:215]\nv_add_f32 v33,v33,v37\nv_add_f32 v34,v34,v38\nv_mfma_f32_16x16x32_bf16 v[216:219],a[72:75],v[88:91],v[216:219]\n" \
"v_add_f32 v35,v35,v39\nv_add_f32 v40,v40,v44\nv_mfma_f32_16x16x32_bf16 v[216:219],a[104:107],v[92:95],v[216:219]\nv_add_f32 v41,v41,v45\n" \
"v_add_f32 v42,v42,v46\nv_mfma_f32_16x16x32_bf16 v[220:223],a[76:79],v[88:91],v[220:223]\nv_add_f32 v43,v43,v47\nv_add_f32 v32,v32,v40\n" \
"v_mfma_f32_16x16x32_bf16 v[220:223],a[108:111],v[92:95],v[220:223]\nv_add_f32 v33,v33,v41\nv_add_f32 v34,v34,v42\nv_mfma_f32_16x16x32_bf16 v[224:227],a[80:83],v[88:91],v[224:227]\n" \
"v_add_f32 v35,v35,v43\nv_cvt_pk_bf16_f32 v36,v32,v33\nv_mfma_f32_16x16x32_bf16 v[224:227],a[112:115],v[92:95],v[224:227]\nv_cvt_pk_bf16_f32 v37,v34,v35\n" \
"v_mfma_f32_16x16x32_bf16 v[228:231],a[84:87],v[88:91],v[228:231]\nv_add_f32 v48,v48,v240\nv_add_f32 v49,v49,v241\nv_mfma_f32_16x16x32_bf16 v[228:231],a[116:119],v[92:95],v[228:231]\n" \
"v_add_f32 v50,v50,v242\nv_add_f32 v51,v51,v243\nv_mfma_f32_16x16x32_bf16 v[232:235],a[88:91],v[88:91],v[232:235]\nv_add_f32 v244,v244,v248\n" \
"v_add_f32 v245,v245,v249\nv_mfma_f32_16x16x32_bf16 v[232:235],a[120:123],v[92:95],v[232:235]\nv_add_f32 v246,v246,v250\nv_add_f32 v247,v247,v251\n" \
"v_mfma_f32_16x16x32_bf16 v[236:239],a[92:95],v[88:91],v[236:239]\nv_add_f32 v48,v48,v244\nv_add_f32 v49,v49,v245\nv_mfma_f32_16x16x32_bf16 v[236:239],a[124:127],v[92:95],v[236:239]\n" \
"v_add_f32 v50,v50,v246\ns_waitcnt vmcnt(11)\nv_mfma_f32_16x16x32_bf16 v[208:211],a[128:131],v[96:99],v[208:211]\nv_add_f32 v51,v51,v247\n" \
"v_cvt_pk_bf16_f32 v38,v48,v49\nv_mfma_f32_16x16x32_bf16 v[208:211],a[160:163],v[100:103],v[208:211]\nv_cvt_pk_bf16_f32 v39,v50,v51\nbuffer_store_dwordx4 v[36:39],v59,s[36:39],s74 offen sc1\n" \
"ds_read_b128 v[32:35],v58 offset:32768\nv_mfma_f32_16x16x32_bf16 v[212:215],a[132:135],v[96:99],v[212:215]\nds_read_b128 v[36:39],v58 offset:40960\nds_read_b128 v[40:43],v58 offset:49152\n" \
"v_mfma_f32_16x16x32_bf16 v[212:215],a[164:167],v[100:103],v[212:215]\nds_read_b128 v[44:47],v58 offset:57344\nds_read_b128 v[48:51],v58 offset:36864\nv_mfma_f32_16x16x32_bf16 v[216:219],a[136:139],v[96:99],v[216:219]\n" \
"ds_read_b128 v[240:243],v58 offset:45056\nds_read_b128 v[244:247],v58 offset:53248\nv_mfma_f32_16x16x32_bf16 v[216:219],a[168:171],v[100:103],v[216:219]\nds_read_b128 v[248:251],v58 offset:61440\n" \
"v_mfma_f32_16x16x32_bf16 v[220:223],a[140:143],v[96:99],v[220:223]\nv_mfma_f32_16x16x32_bf16 v[220:223],a[172:175],v[100:103],v[220:223]\nv_mfma_f32_16x16x32_bf16 v[224:227],a[144:147],v[96:99],v[224:227]\nv_mfma_f32_16x16x32_bf16 v[224:227],a[176:179],v[100:103],v[224:227]\n" \
"v_mfma_f32_16x16x32_bf16 v[228:231],a[148:151],v[96:99],v[228:231]\nv_mfma_f32_16x16x32_bf16 v[228:231],a[180:183],v[100:103],v[228:231]\ns_waitcnt lgkmcnt(4)\nv_add_f32 v32,v32,v36\n" \
"v_add_f32 v33,v33,v37\nv_mfma_f32_16x16x32_bf16 v[232:235],a[152:155],v[96:99],v[232:235]\nv_add_f32 v34,v34,v38\nv_add_f32 v35,v35,v39\n" \
"v_add_f32 v40,v40,v44\nv_mfma_f32_16x16x32_bf16 v[232:235],a[184:187],v[100:103],v[232:235]\nv_add_f32 v41,v41,v45\nv_add_f32 v42,v42,v46\n" \
"v_add_f32 v43,v43,v47\nv_mfma_f32_16x16x32_bf16 v[236:239],a[156:159],v[96:99],v[236:239]\nv_add_f32 v32,v32,v40\nv_add_f32 v33,v33,v41\n" \
"v_add_f32 v34,v34,v42\nv_mfma_f32_16x16x32_bf16 v[236:239],a[188:191],v[100:103],v[236:239]\nv_add_f32 v35,v35,v43\nv_cvt_pk_bf16_f32 v36,v32,v33\n" \
"s_waitcnt vmcnt(11)\nv_mfma_f32_16x16x32_bf16 v[208:211],a[192:195],v[104:107],v[208:211]\nv_cvt_pk_bf16_f32 v37,v34,v35\ns_waitcnt lgkmcnt(0)\n" \
"v_add_f32 v48,v48,v240\nv_mfma_f32_16x16x32_bf16 v[212:215],a[196:199],v[104:107],v[212:215]\nv_add_f32 v49,v49,v241\nv_add_f32 v50,v50,v242\n" \
"v_add_f32 v51,v51,v243\nv_mfma_f32_16x16x32_bf16 v[216:219],a[200:203],v[104:107],v[216:219]\nv_add_f32 v244,v244,v248\nv_add_f32 v245,v245,v249\n" \
"v_mfma_f32_16x16x32_bf16 v[220:223],a[204:207],v[104:107],v[220:223]\nv_add_f32 v246,v246,v250\nv_add_f32 v247,v247,v251\nv_mfma_f32_16x16x32_bf16 v[224:227],a[208:211],v[104:107],v[224:227]\n" \
"v_add_f32 v48,v48,v244\nv_add_f32 v49,v49,v245\nv_mfma_f32_16x16x32_bf16 v[228:231],a[212:215],v[104:107],v[228:231]\nv_add_f32 v50,v50,v246\n" \
"v_add_f32 v51,v51,v247\nv_mfma_f32_16x16x32_bf16 v[232:235],a[216:219],v[104:107],v[232:235]\nv_cvt_pk_bf16_f32 v38,v48,v49\nv_cvt_pk_bf16_f32 v39,v50,v51\n" \
"v_mfma_f32_16x16x32_bf16 v[236:239],a[220:223],v[104:107],v[236:239]\nv_add_u32 v59,409600,v59\nbuffer_store_dwordx4 v[36:39],v59,s[36:39],s74 offen sc1\ns_branch .Le_%=\n" \
".Lzl_%=:\ns_branch .Lzr_%=\n.Lls_%=:\nv_mfma_f32_16x16x32_bf16 v[20:23],v[64:67],a[0:3],0\n" \
"v_and_b32 v59,0x3ff,v59\nv_mfma_f32_16x16x32_bf16 v[20:23],v[68:71],a[16:19],v[20:23]\ns_add_u32 s4,s12,409600\ns_addc_u32 s5,s13,0\n" \
"v_mfma_f32_16x16x32_bf16 v[24:27],v[64:67],a[4:7],0\ns_add_u32 s6,s14,409600\ns_addc_u32 s7,s15,0\nv_mfma_f32_16x16x32_bf16 v[24:27],v[68:71],a[20:23],v[24:27]\n" \
"s_add_u32 s8,s16,409600\ns_addc_u32 s9,s17,0\nv_mfma_f32_16x16x32_bf16 v[28:31],v[64:67],a[8:11],0\nds_write_addtid_b32 v0\n" \
"ds_write_addtid_b32 v1 offset:544\nv_mfma_f32_16x16x32_bf16 v[28:31],v[68:71],a[24:27],v[28:31]\nds_write_addtid_b32 v2 offset:256\nds_write_addtid_b32 v3 offset:800\n" \
"v_mfma_f32_16x16x32_bf16 v[32:35],v[64:67],a[12:15],0\nds_write_addtid_b32 v4 offset:1056\nds_write_addtid_b32 v5 offset:1600\nv_mfma_f32_16x16x32_bf16 v[32:35],v[68:71],a[28:31],v[32:35]\n" \
"ds_write_addtid_b32 v6 offset:1312\ns_waitcnt vmcnt(16)\nv_mfma_f32_16x16x32_bf16 v[20:23],v[72:75],a[32:35],v[20:23]\nv_add_u32 v71,s57,v59\n" \
"s_waitcnt vmcnt(15)\nv_mfma_f32_16x16x32_bf16 v[20:23],v[76:79],a[48:51],v[20:23]\ns_waitcnt vmcnt(8)\nv_cmp_eq_u32 vcc,0,v63\n" \
"v_mfma_f32_16x16x32_bf16 v[24:27],v[72:75],a[36:39],v[24:27]\nds_read_b32 v64,v71 offset:28672\nds_read_b64 v[68:69],v71 offset:29696\nv_mfma_f32_16x16x32_bf16 v[24:27],v[76:79],a[52:55],v[24:27]\n" \
"s_and_b64 vcc,vcc,s[86:87]\ns_cbranch_scc1 .LsaS_%=\n.LbaS_%=:\nv_mfma_f32_16x16x32_bf16 v[28:31],v[72:75],a[40:43],v[28:31]\n" \
"ds_write_addtid_b32 v7 offset:1856\nds_write_addtid_b32 v8 offset:2112\nv_mfma_f32_16x16x32_bf16 v[28:31],v[76:79],a[56:59],v[28:31]\nds_write_addtid_b32 v9 offset:2656\n" \
"ds_write_addtid_b32 v10 offset:2368\nv_mfma_f32_16x16x32_bf16 v[32:35],v[72:75],a[44:47],v[32:35]\ns_waitcnt lgkmcnt(4)\nv_xor_b32 v65,32,v64\n" \
"v_mfma_f32_16x16x32_bf16 v[32:35],v[76:79],a[60:63],v[32:35]\nv_xor_b32 v66,64,v64\nv_mfma_f32_16x16x32_bf16 v[20:23],v[80:83],a[64:67],v[20:23]\nglobal_load_dwordx4 v[72:75],v59,s[12:13] sc1\n" \
"v_mfma_f32_16x16x32_bf16 v[20:23],v[84:87],a[80:83],v[20:23]\nv_xor_b32 v67,96,v64\nv_add_u32 v70,0xf800,v68\nv_mfma_f32_16x16x32_bf16 v[24:27],v[80:83],a[68:71],v[24:27]\n" \
"global_load_dwordx4 v[76:79],v59,s[12:13] offset:1024 sc1\nv_add_u32 v71,0xf800,v69\nv_mfma_f32_16x16x32_bf16 v[24:27],v[84:87],a[84:87],v[24:27]\nds_write_addtid_b32 v11 offset:2912\n" \
"ds_write_addtid_b32 v12 offset:3168\nv_mfma_f32_16x16x32_bf16 v[28:31],v[80:83],a[72:75],v[28:31]\nds_write_addtid_b32 v13 offset:3712\nds_write_addtid_b32 v14 offset:3424\n" \
"v_mfma_f32_16x16x32_bf16 v[28:31],v[84:87],a[88:91],v[28:31]\nds_write_addtid_b32 v15 offset:3968\nds_write_addtid_b32 v16 offset:4224\nv_mfma_f32_16x16x32_bf16 v[32:35],v[80:83],a[76:79],v[32:35]\n" \
"ds_write_addtid_b32 v17 offset:4512\nds_write_addtid_b32 v18 offset:4800\nv_mfma_f32_16x16x32_bf16 v[32:35],v[84:87],a[92:95],v[32:35]\nds_write_addtid_b32 v19 offset:5088\n" \
"v_mfma_f32_16x16x32_bf16 v[20:23],v[88:91],a[96:99],v[20:23]\nglobal_load_dwordx4 v[80:83],v59,s[12:13] offset:2048 sc1\nv_mfma_f32_16x16x32_bf16 v[20:23],v[92:95],a[112:115],v[20:23]\nds_read_b64_tr_b16 a[0:1],v64\n" \
"ds_read_b64_tr_b16 a[2:3],v64 offset:512\nv_mfma_f32_16x16x32_bf16 v[24:27],v[88:91],a[100:103],v[24:27]\nds_read_b64_tr_b16 a[4:5],v65\nds_read_b64_tr_b16 a[6:7],v65 offset:512\n" \
"v_mfma_f32_16x16x32_bf16 v[24:27],v[92:95],a[116:119],v[24:27]\nds_read_b64_tr_b16 a[8:9],v66\nds_read_b64_tr_b16 a[10:11],v66 offset:512\nv_mfma_f32_16x16x32_bf16 v[28:31],v[88:91],a[104:107],v[28:31]\n" \
"ds_read_b64_tr_b16 a[12:13],v67\nds_read_b64_tr_b16 a[14:15],v67 offset:512\nv_mfma_f32_16x16x32_bf16 v[28:31],v[92:95],a[120:123],v[28:31]\ns_waitcnt lgkmcnt(0)\n" \
"s_barrier\nv_mfma_f32_16x16x32_bf16 v[32:35],v[88:91],a[108:111],v[32:35]\nds_read_b64_tr_b16 a[16:17],v64 offset:4096\nds_read_b64_tr_b16 a[18:19],v64 offset:4608\n" \
"v_mfma_f32_16x16x32_bf16 v[32:35],v[92:95],a[124:127],v[32:35]\nds_read_b64_tr_b16 a[20:21],v65 offset:4096\ns_waitcnt vmcnt(10)\nv_mfma_f32_16x16x32_bf16 v[20:23],v[96:99],a[128:131],v[20:23]\n" \
"ds_read_b64_tr_b16 a[22:23],v65 offset:4608\ns_waitcnt vmcnt(9)\nv_mfma_f32_16x16x32_bf16 v[20:23],v[100:103],a[144:147],v[20:23]\ns_add_u32 m0,s57,0\n" \
"v_mfma_f32_16x16x32_bf16 v[24:27],v[96:99],a[132:135],v[24:27]\nbuffer_load_dwordx4 v68,s[40:43],s60 offen lds\nds_read_b64_tr_b16 a[24:25],v66 offset:4096\nv_mfma_f32_16x16x32_bf16 v[24:27],v[100:103],a[148:151],v[24:27]\n" \
"ds_read_b64_tr_b16 a[26:27],v66 offset:4608\nds_read_b64_tr_b16 a[28:29],v67 offset:4096\nv_mfma_f32_16x16x32_bf16 v[28:31],v[96:99],a[136:139],v[28:31]\nbuffer_load_dwordx4 v69,s[40:43],s60 offen offset:1024 lds\n" \
"ds_read_b64_tr_b16 a[30:31],v67 offset:4608\nv_mfma_f32_16x16x32_bf16 v[28:31],v[100:103],a[152:155],v[28:31]\nds_read_b64_tr_b16 a[32:33],v64 offset:8192\nds_read_b64_tr_b16 a[34:35],v64 offset:8704\n" \
"v_mfma_f32_16x16x32_bf16 v[32:35],v[96:99],a[140:143],v[32:35]\nbuffer_load_dwordx4 v70,s[40:43],s60 offen offset:2048 lds\nds_read_b64_tr_b16 a[36:37],v65 offset:8192\nv_mfma_f32_16x16x32_bf16 v[32:35],v[100:103],a[156:159],v[32:35]\n" \
"ds_read_b64_tr_b16 a[38:39],v65 offset:8704\ns_waitcnt vmcnt(11)\nv_mfma_f32_16x16x32_bf16 v[20:23],v[104:107],a[160:163],v[20:23]\nbuffer_load_dwordx4 v71,s[40:43],s60 offen offset:3072 lds\n" \
"s_waitcnt vmcnt(11)\nv_mfma_f32_16x16x32_bf16 v[20:23],v[108:111],a[176:179],v[20:23]\nds_read_b64_tr_b16 a[40:41],v66 offset:8192\nds_read_b64_tr_b16 a[42:43],v66 offset:8704\n" \
"v_mfma_f32_16x16x32_bf16 v[24:27],v[104:107],a[164:167],v[24:27]\nglobal_load_dwordx4 v[84:87],v59,s[14:15] sc1\nds_read_b64_tr_b16 a[44:45],v67 offset:8192\nv_mfma_f32_16x16x32_bf16 v[24:27],v[108:111],a[180:183],v[24:27]\n" \
"s_waitcnt lgkmcnt(7)\ns_add_u32 m0,s57,4096\nv_mfma_f32_16x16x32_bf16 v[28:31],v[104:107],a[168:171],v[28:31]\nbuffer_load_dwordx4 v68,s[40:43],s61 offen lds\n" \
"ds_read_b64_tr_b16 a[46:47],v67 offset:8704\nv_mfma_f32_16x16x32_bf16 v[28:31],v[108:111],a[184:187],v[28:31]\nds_read_b64_tr_b16 a[48:49],v64 offset:12288\nds_read_b64_tr_b16 a[50:51],v64 offset:12800\n" \
"v_mfma_f32_16x16x32_bf16 v[32:35],v[104:107],a[172:175],v[32:35]\nbuffer_load_dwordx4 v69,s[40:43],s61 offen offset:1024 lds\nds_read_b64_tr_b16 a[52:53],v65 offset:12288\nv_mfma_f32_16x16x32_bf16 v[32:35],v[108:111],a[188:191],v[32:35]\n" \
"ds_read_b64_tr_b16 a[54:55],v65 offset:12800\ns_waitcnt vmcnt(13)\nv_mfma_f32_16x16x32_bf16 v[20:23],v[112:115],a[192:195],v[20:23]\nbuffer_load_dwordx4 v70,s[40:43],s61 offen offset:2048 lds\n" \
"s_waitcnt vmcnt(13)\nv_mfma_f32_16x16x32_bf16 v[20:23],v[116:119],a[208:211],v[20:23]\nds_read_b64_tr_b16 a[56:57],v66 offset:12288\nds_read_b64_tr_b16 a[58:59],v66 offset:12800\n" \
"v_mfma_f32_16x16x32_bf16 v[24:27],v[112:115],a[196:199],v[24:27]\nbuffer_load_dwordx4 v71,s[40:43],s61 offen offset:3072 lds\nds_read_b64_tr_b16 a[60:61],v67 offset:12288\nv_mfma_f32_16x16x32_bf16 v[24:27],v[116:119],a[212:215],v[24:27]\n" \
"ds_read_b64_tr_b16 a[62:63],v67 offset:12800\nds_read_b64_tr_b16 a[64:65],v64 offset:16384\nv_mfma_f32_16x16x32_bf16 v[28:31],v[112:115],a[200:203],v[28:31]\nglobal_load_dwordx4 v[88:91],v59,s[14:15] offset:1024 sc1\n" \
"ds_read_b64_tr_b16 a[66:67],v64 offset:16896\nv_mfma_f32_16x16x32_bf16 v[28:31],v[116:119],a[216:219],v[28:31]\ns_waitcnt lgkmcnt(10)\ns_add_u32 m0,s57,8192\n" \
"v_mfma_f32_16x16x32_bf16 v[32:35],v[112:115],a[204:207],v[32:35]\nbuffer_load_dwordx4 v68,s[40:43],s62 offen lds\nds_read_b64_tr_b16 a[68:69],v65 offset:16384\nv_mfma_f32_16x16x32_bf16 v[32:35],v[116:119],a[220:223],v[32:35]\n" \
"ds_read_b64_tr_b16 a[70:71],v65 offset:16896\ns_waitcnt vmcnt(15)\nv_mfma_f32_16x16x32_bf16 v[20:23],v[120:123],a[224:227],v[20:23]\nbuffer_load_dwordx4 v69,s[40:43],s62 offen offset:1024 lds\n" \
"s_waitcnt vmcnt(15)\nv_mfma_f32_16x16x32_bf16 v[20:23],v[124:127],a[240:243],v[20:23]\nds_read_b64_tr_b16 a[72:73],v66 offset:16384\nds_read_b64_tr_b16 a[74:75],v66 offset:16896\n" \
"v_mfma_f32_16x16x32_bf16 v[24:27],v[120:123],a[228:231],v[24:27]\nbuffer_load_dwordx4 v70,s[40:43],s62 offen offset:2048 lds\nds_read_b64_tr_b16 a[76:77],v67 offset:16384\nv_mfma_f32_16x16x32_bf16 v[24:27],v[124:127],a[244:247],v[24:27]\n" \
"ds_read_b64_tr_b16 a[78:79],v67 offset:16896\nds_read_b64_tr_b16 a[80:81],v64 offset:20480\nv_mfma_f32_16x16x32_bf16 v[28:31],v[120:123],a[232:235],v[28:31]\nbuffer_load_dwordx4 v71,s[40:43],s62 offen offset:3072 lds\n" \
"ds_read_b64_tr_b16 a[82:83],v64 offset:20992\nv_mfma_f32_16x16x32_bf16 v[28:31],v[124:127],a[248:251],v[28:31]\nds_read_b64_tr_b16 a[84:85],v65 offset:20480\nds_read_b64_tr_b16 a[86:87],v65 offset:20992\n" \
"v_mfma_f32_16x16x32_bf16 v[32:35],v[120:123],a[236:239],v[32:35]\nglobal_load_dwordx4 v[92:95],v59,s[14:15] offset:2048 sc1\nds_read_b64_tr_b16 a[88:89],v66 offset:20480\nv_mfma_f32_16x16x32_bf16 v[32:35],v[124:127],a[252:255],v[32:35]\n" \
"ds_read_b64_tr_b16 a[90:91],v66 offset:20992\ns_waitcnt vmcnt(17)\nv_mfma_f32_16x16x32_bf16 v[208:211],v[128:131],v[72:75],0\nglobal_load_dwordx4 v[108:111],v59,s[4:5] sc1\n" \
"s_waitcnt vmcnt(17)\nv_mfma_f32_16x16x32_bf16 v[208:211],v[160:163],v[76:79],v[208:211]\ns_waitcnt lgkmcnt(14)\ns_add_u32 m0,s57,12288\n" \
"v_mfma_f32_16x16x32_bf16 v[212:215],v[132:135],v[72:75],0\nbuffer_load_dwordx4 v68,s[40:43],s63 offen lds\nds_read_b64_tr_b16 a[92:93],v67 offset:20480\nv_mfma_f32_16x16x32_bf16 v[212:215],v[164:167],v[76:79],v[212:215]\n" \
"ds_read_b64_tr_b16 a[94:95],v67 offset:20992\nds_read_b64_tr_b16 a[96:97],v64 offset:24576\nv_mfma_f32_16x16x32_bf16 v[216:219],v[136:139],v[72:75],0\nbuffer_load_dwordx4 v69,s[40:43],s63 offen offset:1024 lds\n" \
"ds_read_b64_tr_b16 a[98:99],v64 offset:25088\nv_mfma_f32_16x16x32_bf16 v[216:219],v[168:171],v[76:79],v[216:219]\nds_read_b64_tr_b16 a[100:101],v65 offset:24576\nds_read_b64_tr_b16 a[102:103],v65 offset:25088\n" \
"v_mfma_f32_16x16x32_bf16 v[220:223],v[140:143],v[72:75],0\nbuffer_load_dwordx4 v70,s[40:43],s63 offen offset:2048 lds\nds_read_b64_tr_b16 a[104:105],v66 offset:24576\nv_mfma_f32_16x16x32_bf16 v[220:223],v[172:175],v[76:79],v[220:223]\n" \
"ds_read_b64_tr_b16 a[106:107],v66 offset:25088\nds_read_b64_tr_b16 a[108:109],v67 offset:24576\nv_mfma_f32_16x16x32_bf16 v[224:227],v[144:147],v[72:75],0\nbuffer_load_dwordx4 v71,s[40:43],s63 offen offset:3072 lds\n" \
"s_mov_b32 m0,s56\nv_mfma_f32_16x16x32_bf16 v[224:227],v[176:179],v[76:79],v[224:227]\nds_write_addtid_b32 v20 offset:5344\nds_write_addtid_b32 v21 offset:5888\n" \
"v_mfma_f32_16x16x32_bf16 v[228:231],v[148:151],v[72:75],0\nglobal_load_dwordx4 v[112:115],v59,s[4:5] offset:1024 sc1\nds_write_addtid_b32 v22 offset:5600\nv_mfma_f32_16x16x32_bf16 v[228:231],v[180:183],v[76:79],v[228:231]\n" \
"ds_write_addtid_b32 v23 offset:6144\nds_write_addtid_b32 v24 offset:6400\nv_mfma_f32_16x16x32_bf16 v[232:235],v[152:155],v[72:75],0\nglobal_load_dwordx4 v[116:119],v59,s[4:5] offset:2048 sc1\n" \
"ds_write_addtid_b32 v25 offset:6944\nv_mfma_f32_16x16x32_bf16 v[232:235],v[184:187],v[76:79],v[232:235]\nds_write_addtid_b32 v26 offset:6656\nds_write_addtid_b32 v27 offset:7200\n" \
"v_mfma_f32_16x16x32_bf16 v[236:239],v[156:159],v[72:75],0\nds_write_addtid_b32 v28 offset:7456\nds_write_addtid_b32 v29 offset:8000\nv_mfma_f32_16x16x32_bf16 v[236:239],v[188:191],v[76:79],v[236:239]\n" \
"s_waitcnt lgkmcnt(15)\ns_waitcnt vmcnt(22)\nv_mfma_f32_16x16x32_bf16 v[208:211],a[0:3],v[80:83],v[208:211]\ns_waitcnt vmcnt(17)\n" \
"v_mfma_f32_16x16x32_bf16 v[208:211],a[32:35],v[84:87],v[208:211]\nds_write_addtid_b32 v30 offset:7712\nds_write_addtid_b32 v31 offset:8256\nv_mfma_f32_16x16x32_bf16 v[212:215],a[4:7],v[80:83],v[212:215]\n" \
"ds_write_addtid_b32 v32 offset:8512\nds_write_addtid_b32 v33 offset:9056\nv_mfma_f32_16x16x32_bf16 v[212:215],a[36:39],v[84:87],v[212:215]\nds_write_addtid_b32 v34 offset:8768\n" \
"ds_write_addtid_b32 v35 offset:9312\nv_mfma_f32_16x16x32_bf16 v[216:219],a[8:11],v[80:83],v[216:219]\ns_waitcnt lgkmcnt(15)\ns_add_u32 m0,s57,16384\n" \
"v_mfma_f32_16x16x32_bf16 v[216:219],a[40:43],v[84:87],v[216:219]\nbuffer_load_dwordx4 v68,s[40:43],s64 offen lds\nds_read_b64_tr_b16 a[110:111],v67 offset:25088\nv_mfma_f32_16x16x32_bf16 v[220:223],a[12:15],v[80:83],v[220:223]\n" \
"v_mul_f32 v36,v36,v40\nv_mul_f32 v37,v37,v41\nv_mfma_f32_16x16x32_bf16 v[220:223],a[44:47],v[84:87],v[220:223]\nbuffer_load_dwordx4 v69,s[40:43],s64 offen offset:1024 lds\n" \
"s_waitcnt lgkmcnt(15)\nv_mfma_f32_16x16x32_bf16 v[224:227],a[16:19],v[80:83],v[224:227]\nv_mul_f32 v38,v38,v42\nv_mfma_f32_16x16x32_bf16 v[224:227],a[48:51],v[84:87],v[224:227]\n" \
"buffer_load_dwordx4 v70,s[40:43],s64 offen offset:2048 lds\nv_mul_f32 v39,v39,v43\nv_mfma_f32_16x16x32_bf16 v[228:231],a[20:23],v[80:83],v[228:231]\ns_waitcnt lgkmcnt(0)\n" \
"s_barrier\nv_mfma_f32_16x16x32_bf16 v[228:231],a[52:55],v[84:87],v[228:231]\nbuffer_load_dwordx4 v71,s[40:43],s64 offen offset:3072 lds\nv_mfma_f32_16x16x32_bf16 v[232:235],a[24:27],v[80:83],v[232:235]\n" \
"ds_read_b64_tr_b16 a[112:113],v64\nds_read_b64_tr_b16 a[114:115],v64 offset:512\nv_mfma_f32_16x16x32_bf16 v[232:235],a[56:59],v[84:87],v[232:235]\nglobal_load_dwordx4 v[120:123],v59,s[6:7] sc1\n" \
"ds_read_b64_tr_b16 a[116:117],v65\nv_mfma_f32_16x16x32_bf16 v[236:239],a[28:31],v[80:83],v[236:239]\ns_add_u32 m0,s57,20480\nv_mfma_f32_16x16x32_bf16 v[236:239],a[60:63],v[84:87],v[236:239]\n" \
"s_waitcnt vmcnt(17)\nv_mfma_f32_16x16x32_bf16 v[208:211],a[64:67],v[88:91],v[208:211]\ns_waitcnt vmcnt(12)\nv_mfma_f32_16x16x32_bf16 v[208:211],a[96:99],v[92:95],v[208:211]\n" \
"buffer_load_dwordx4 v68,s[40:43],s65 offen lds\nds_read_b64_tr_b16 a[118:119],v65 offset:512\nv_mfma_f32_16x16x32_bf16 v[212:215],a[68:71],v[88:91],v[212:215]\nds_read_b64_tr_b16 a[120:121],v66\n" \
"ds_read_b64_tr_b16 a[122:123],v66 offset:512\nv_mfma_f32_16x16x32_bf16 v[212:215],a[100:103],v[92:95],v[212:215]\nbuffer_load_dwordx4 v69,s[40:43],s65 offen offset:1024 lds\nds_read_b64_tr_b16 a[124:125],v67\n" \
"v_mfma_f32_16x16x32_bf16 v[216:219],a[72:75],v[88:91],v[216:219]\nds_read_b64_tr_b16 a[126:127],v67 offset:512\nv_mfma_f32_16x16x32_bf16 v[216:219],a[104:107],v[92:95],v[216:219]\nbuffer_load_dwordx4 v70,s[40:43],s65 offen offset:2048 lds\n" \
"ds_read_b64_tr_b16 a[128:129],v64 offset:4096\nv_mfma_f32_16x16x32_bf16 v[220:223],a[76:79],v[88:91],v[220:223]\nds_read_b64_tr_b16 a[130:131],v64 offset:4608\nds_read_b64_tr_b16 a[132:133],v65 offset:4096\n" \
"v_mfma_f32_16x16x32_bf16 v[220:223],a[108:111],v[92:95],v[220:223]\nbuffer_load_dwordx4 v71,s[40:43],s65 offen offset:3072 lds\nv_mfma_f32_16x16x32_bf16 v[224:227],a[80:83],v[88:91],v[224:227]\nds_read_b64_tr_b16 a[134:135],v65 offset:4608\n" \
"ds_read_b64_tr_b16 a[136:137],v66 offset:4096\nv_mfma_f32_16x16x32_bf16 v[228:231],a[84:87],v[88:91],v[228:231]\nglobal_load_dwordx4 v[124:127],v59,s[6:7] offset:1024 sc1\nds_read_b64_tr_b16 a[138:139],v66 offset:4608\n" \
"v_mfma_f32_16x16x32_bf16 v[232:235],a[88:91],v[88:91],v[232:235]\ns_add_u32 m0,s57,24576\nv_mfma_f32_16x16x32_bf16 v[236:239],a[92:95],v[88:91],v[236:239]\nbuffer_load_dwordx4 v68,s[40:43],s66 offen lds\n" \
"s_waitcnt vmcnt(17)\nv_mfma_f32_16x16x32_bf16 v[0:3],v[128:131],v[108:111],0\nds_read_b64_tr_b16 a[140:141],v67 offset:4096\ns_waitcnt vmcnt(12)\n" \
"v_mfma_f32_16x16x32_bf16 v[0:3],v[160:163],v[112:115],v[0:3]\nbuffer_load_dwordx4 v69,s[40:43],s66 offen offset:1024 lds\nds_read_b64_tr_b16 a[142:143],v67 offset:4608\nv_mfma_f32_16x16x32_bf16 v[4:7],v[132:135],v[108:111],0\n" \
"s_waitcnt vmcnt(20)\nds_read_b64_tr_b16 a[144:145],v64 offset:8192\nv_mfma_f32_16x16x32_bf16 v[4:7],v[164:167],v[112:115],v[4:7]\nbuffer_load_dwordx4 v70,s[40:43],s66 offen offset:2048 lds\n" \
"ds_read_b64_tr_b16 a[146:147],v64 offset:8704\nv_mfma_f32_16x16x32_bf16 v[8:11],v[136:139],v[108:111],0\nds_read_b64_tr_b16 a[148:149],v65 offset:8192\nds_read_b64_tr_b16 a[150:151],v65 offset:8704\n" \
"v_mfma_f32_16x16x32_bf16 v[8:11],v[168:171],v[112:115],v[8:11]\nbuffer_load_dwordx4 v71,s[40:43],s66 offen offset:3072 lds\nds_read_b64_tr_b16 a[152:153],v66 offset:8192\nv_mfma_f32_16x16x32_bf16 v[12:15],v[140:143],v[108:111],0\n" \
"ds_read_b64_tr_b16 a[154:155],v66 offset:8704\nds_read_b64_tr_b16 a[156:157],v67 offset:8192\nv_mfma_f32_16x16x32_bf16 v[12:15],v[172:175],v[112:115],v[12:15]\nglobal_load_dwordx4 v[192:195],v59,s[6:7] offset:2048 sc1\n" \
"ds_read_b64_tr_b16 a[158:159],v67 offset:8704\nv_mfma_f32_16x16x32_bf16 v[16:19],v[144:147],v[108:111],0\nv_cvt_pk_bf16_f32 v40,v36,v37\nv_cvt_pk_bf16_f32 v41,v38,v39\n" \
"v_mfma_f32_16x16x32_bf16 v[16:19],v[176:179],v[112:115],v[16:19]\nglobal_load_dwordx4 v[96:99],v59,s[16:17] sc1\nbuffer_store_dwordx2 v[40:41],v61,s[36:39],s52 offen sc1\nds_read_b128 v[36:39],v58\n" \
"ds_read_b128 v[40:43],v58 offset:9568\nds_read_b128 v[44:47],v58 offset:19136\nds_read_b128 v[48:51],v58 offset:28704\ns_waitcnt lgkmcnt(2)\n" \
"v_add_f32 v36,v36,v40\nv_add_f32 v37,v37,v41\nv_add_f32 v38,v38,v42\nv_add_f32 v39,v39,v43\n" \
"v_add_f32 v36,v36,v52\nv_add_f32 v37,v37,v53\nv_add_f32 v38,v38,v54\nv_add_f32 v39,v39,v55\n" \
"s_waitcnt lgkmcnt(0)\nv_add_f32 v44,v44,v48\nv_add_f32 v45,v45,v49\nv_add_f32 v46,v46,v50\n" \
"v_add_f32 v47,v47,v51\nv_add_f32 v36,v36,v44\nv_add_f32 v37,v37,v45\nv_add_f32 v38,v38,v46\n" \
"v_add_f32 v39,v39,v47\nv_mul_f32 v40,v36,v36\nv_mul_f32 v41,v37,v37\nv_mul_f32 v42,v38,v38\n" \
"v_mul_f32 v43,v39,v39\nv_fma_f32 v40,v40,s55,v57\nv_fma_f32 v41,v41,s55,v57\nv_fma_f32 v42,v42,s55,v57\n" \
"v_fma_f32 v43,v43,s55,v57\nv_mul_f32 v40,v36,v40\nv_mul_f32 v41,v37,v41\nv_mul_f32 v42,v38,v42\n" \
"v_mul_f32 v43,v39,v43\nv_exp_f32 v40,v40\nv_exp_f32 v41,v41\nv_exp_f32 v42,v42\n" \
"v_exp_f32 v43,v43\nv_add_f32 v40,1.0,v40\nv_add_f32 v41,1.0,v41\nv_add_f32 v42,1.0,v42\n" \
"v_add_f32 v43,1.0,v43\nv_rcp_f32 v40,v40\nv_rcp_f32 v41,v41\nv_rcp_f32 v42,v42\n" \
"v_rcp_f32 v43,v43\nv_mul_f32 v36,v36,v40\nv_mul_f32 v37,v37,v41\nv_mul_f32 v38,v38,v42\n" \
"v_mul_f32 v39,v39,v43\nv_cvt_pk_bf16_f32 v40,v36,v37\nv_cvt_pk_bf16_f32 v41,v38,v39\nv_mfma_f32_16x16x32_bf16 v[20:23],v[148:151],v[108:111],0\n" \
"s_add_u32 s52,s52,409600\nbuffer_store_dwordx2 v[40:41],v61,s[36:39],s52 offen sc1\nv_mfma_f32_16x16x32_bf16 v[20:23],v[180:183],v[112:115],v[20:23]\nglobal_load_dwordx4 v[100:103],v59,s[16:17] offset:1024 sc1\n" \
"ds_read_b32 v48,v60\nv_mfma_f32_16x16x32_bf16 v[24:27],v[152:155],v[108:111],0\nds_read_b32 v49,v60 offset:9568\nds_read_b32 v50,v60 offset:19136\n" \
"v_mfma_f32_16x16x32_bf16 v[24:27],v[184:187],v[112:115],v[24:27]\nglobal_load_dwordx4 v[104:107],v59,s[16:17] offset:2048 sc1\nds_read_b32 v51,v60 offset:28704\nv_mfma_f32_16x16x32_bf16 v[28:31],v[156:159],v[108:111],0\n" \
"ds_read_b64_tr_b16 a[160:161],v64 offset:12288\nv_mfma_f32_16x16x32_bf16 v[28:31],v[188:191],v[112:115],v[28:31]\nglobal_load_dwordx4 v[196:199],v59,s[8:9] sc1\ns_waitcnt vmcnt(21)\n" \
"v_mfma_f32_16x16x32_bf16 v[0:3],a[0:3],v[116:119],v[0:3]\nds_read_b64_tr_b16 a[162:163],v64 offset:12800\ns_waitcnt vmcnt(16)\nv_mfma_f32_16x16x32_bf16 v[0:3],a[32:35],v[120:123],v[0:3]\n" \
"global_load_dwordx4 v[200:203],v59,s[8:9] offset:1024 sc1\nds_read_b64_tr_b16 a[164:165],v65 offset:12288\nv_mfma_f32_16x16x32_bf16 v[4:7],a[4:7],v[116:119],v[4:7]\nds_read_b64_tr_b16 a[166:167],v65 offset:12800\n" \
"ds_read_b64_tr_b16 a[168:169],v66 offset:12288\nv_mfma_f32_16x16x32_bf16 v[4:7],a[36:39],v[120:123],v[4:7]\nglobal_load_dwordx4 v[204:207],v59,s[8:9] offset:2048 sc1\nds_read_b64_tr_b16 a[170:171],v66 offset:12800\n" \
"v_mfma_f32_16x16x32_bf16 v[8:11],a[8:11],v[116:119],v[8:11]\ns_add_u32 s59,s59,0x10000\nbuffer_load_dword v63,v61,s[36:39],s59 offen sc1\nv_mfma_f32_16x16x32_bf16 v[8:11],a[40:43],v[120:123],v[8:11]\n" \
"v_add_u32 v59,819200,v59\nds_read_b64_tr_b16 a[172:173],v67 offset:12288\nv_mfma_f32_16x16x32_bf16 v[12:15],a[12:15],v[116:119],v[12:15]\nds_read_b64_tr_b16 a[174:175],v67 offset:12800\n" \
"s_waitcnt lgkmcnt(8)\nv_mfma_f32_16x16x32_bf16 v[12:15],a[44:47],v[120:123],v[12:15]\nv_add_f32 v48,v48,v49\nv_add_f32 v50,v50,v51\n" \
"v_mfma_f32_16x16x32_bf16 v[16:19],a[16:19],v[116:119],v[16:19]\nv_add_f32 v48,v48,v56\nv_add_f32 v48,v48,v50\nv_mfma_f32_16x16x32_bf16 v[16:19],a[48:51],v[120:123],v[16:19]\n" \
"v_mul_f32 v49,v48,v48\nv_fma_f32 v49,v49,s55,v57\nv_mfma_f32_16x16x32_bf16 v[20:23],a[20:23],v[116:119],v[20:23]\nv_mul_f32 v49,v48,v49\n" \
"v_mfma_f32_16x16x32_bf16 v[20:23],a[52:55],v[120:123],v[20:23]\nv_exp_f32 v49,v49\nv_mfma_f32_16x16x32_bf16 v[24:27],a[24:27],v[116:119],v[24:27]\nv_add_f32 v49,1.0,v49\n" \
"v_mfma_f32_16x16x32_bf16 v[24:27],a[56:59],v[120:123],v[24:27]\nv_rcp_f32 v49,v49\nv_mfma_f32_16x16x32_bf16 v[28:31],a[28:31],v[116:119],v[28:31]\nv_mul_f32 v48,v48,v49\n" \
"v_cvt_pk_bf16_f32 v49,v48,v48\nv_mfma_f32_16x16x32_bf16 v[28:31],a[60:63],v[120:123],v[28:31]\nbuffer_store_short v49,v62,s[36:39],s52 offen sc1\ns_waitcnt vmcnt(15)\n" \
"v_mfma_f32_16x16x32_bf16 v[0:3],a[64:67],v[124:127],v[0:3]\nds_read_b128 v[36:39],v58 offset:5344\ns_waitcnt vmcnt(10)\nv_mfma_f32_16x16x32_bf16 v[0:3],a[96:99],v[192:195],v[0:3]\n" \
"ds_read_b128 v[40:43],v58 offset:14912\nds_read_b128 v[44:47],v58 offset:24480\nv_mfma_f32_16x16x32_bf16 v[4:7],a[68:71],v[124:127],v[4:7]\nds_read_b128 v[48:51],v58 offset:34048\n" \
"s_add_u32 s52,s52,409600\nv_mfma_f32_16x16x32_bf16 v[4:7],a[100:103],v[192:195],v[4:7]\nds_read_b64_tr_b16 a[176:177],v64 offset:16384\nv_mfma_f32_16x16x32_bf16 v[8:11],a[72:75],v[124:127],v[8:11]\n" \
"ds_read_b64_tr_b16 a[178:179],v64 offset:16896\nds_read_b64_tr_b16 a[180:181],v65 offset:16384\nv_mfma_f32_16x16x32_bf16 v[8:11],a[104:107],v[192:195],v[8:11]\nds_read_b64_tr_b16 a[182:183],v65 offset:16896\n" \
"ds_read_b64_tr_b16 a[184:185],v66 offset:16384\nv_mfma_f32_16x16x32_bf16 v[12:15],a[76:79],v[124:127],v[12:15]\nds_read_b64_tr_b16 a[186:187],v66 offset:16896\nds_read_b64_tr_b16 a[188:189],v67 offset:16384\n" \
"v_mfma_f32_16x16x32_bf16 v[12:15],a[108:111],v[192:195],v[12:15]\nds_read_b64_tr_b16 a[190:191],v67 offset:16896\nv_mfma_f32_16x16x32_bf16 v[16:19],a[80:83],v[124:127],v[16:19]\nv_mfma_f32_16x16x32_bf16 v[20:23],a[84:87],v[124:127],v[20:23]\n" \
"s_waitcnt lgkmcnt(10)\nv_add_f32 v36,v36,v40\nv_add_f32 v37,v37,v41\nv_mfma_f32_16x16x32_bf16 v[24:27],a[88:91],v[124:127],v[24:27]\n" \
"v_add_f32 v38,v38,v42\nv_add_f32 v39,v39,v43\nv_add_f32 v36,v36,v52\nv_mfma_f32_16x16x32_bf16 v[28:31],a[92:95],v[124:127],v[28:31]\n" \
"v_add_f32 v37,v37,v53\nv_add_f32 v38,v38,v54\nv_mfma_f32_16x16x32_bf16 v[224:227],a[112:115],v[92:95],v[224:227]\nv_add_f32 v39,v39,v55\n" \
"s_waitcnt vmcnt(9)\nv_mfma_f32_16x16x32_bf16 v[224:227],a[144:147],v[96:99],v[224:227]\nds_read_b64_tr_b16 a[192:193],v64 offset:20480\ns_waitcnt lgkmcnt(9)\n" \
"v_mfma_f32_16x16x32_bf16 v[228:231],a[116:119],v[92:95],v[228:231]\nds_read_b64_tr_b16 a[194:195],v64 offset:20992\nds_read_b64_tr_b16 a[196:197],v65 offset:20480\nv_add_f32 v44,v44,v48\n" \
"v_mfma_f32_16x16x32_bf16 v[228:231],a[148:151],v[96:99],v[228:231]\nds_read_b64_tr_b16 a[198:199],v65 offset:20992\nds_read_b64_tr_b16 a[200:201],v66 offset:20480\nv_add_f32 v45,v45,v49\n" \
"v_mfma_f32_16x16x32_bf16 v[232:235],a[120:123],v[92:95],v[232:235]\nds_read_b64_tr_b16 a[202:203],v66 offset:20992\nds_read_b64_tr_b16 a[204:205],v67 offset:20480\nv_add_f32 v46,v46,v50\n" \
"v_mfma_f32_16x16x32_bf16 v[232:235],a[152:155],v[96:99],v[232:235]\nds_read_b64_tr_b16 a[206:207],v67 offset:20992\nv_add_f32 v47,v47,v51\nv_add_f32 v36,v36,v44\n" \
"v_mfma_f32_16x16x32_bf16 v[236:239],a[124:127],v[92:95],v[236:239]\nv_add_f32 v37,v37,v45\nv_add_f32 v38,v38,v46\nv_add_f32 v39,v39,v47\n" \
"v_mfma_f32_16x16x32_bf16 v[236:239],a[156:159],v[96:99],v[236:239]\nv_mul_f32 v40,v36,v36\nv_mul_f32 v41,v37,v37\ns_waitcnt lgkmcnt(15)\n" \
"v_mfma_f32_16x16x32_bf16 v[208:211],a[128:131],v[96:99],v[208:211]\nv_mul_f32 v42,v38,v38\ns_waitcnt vmcnt(6)\nv_mfma_f32_16x16x32_bf16 v[208:211],a[160:163],v[100:103],v[208:211]\n" \
"ds_read_b64_tr_b16 a[208:209],v64 offset:24576\nds_read_b64_tr_b16 a[210:211],v64 offset:25088\nv_mfma_f32_16x16x32_bf16 v[212:215],a[132:135],v[96:99],v[212:215]\nds_read_b64_tr_b16 a[212:213],v65 offset:24576\n" \
"ds_read_b64_tr_b16 a[214:215],v65 offset:25088\nv_mul_f32 v43,v39,v39\nv_mfma_f32_16x16x32_bf16 v[212:215],a[164:167],v[100:103],v[212:215]\nds_read_b64_tr_b16 a[216:217],v66 offset:24576\n" \
"ds_read_b64_tr_b16 a[218:219],v66 offset:25088\nv_fma_f32 v40,v40,s55,v57\nv_mfma_f32_16x16x32_bf16 v[216:219],a[136:139],v[96:99],v[216:219]\nds_read_b64_tr_b16 a[220:221],v67 offset:24576\n" \
"ds_read_b64_tr_b16 a[222:223],v67 offset:25088\nv_fma_f32 v41,v41,s55,v57\nv_mfma_f32_16x16x32_bf16 v[216:219],a[168:171],v[100:103],v[216:219]\nv_fma_f32 v42,v42,s55,v57\n" \
"v_fma_f32 v43,v43,s55,v57\nv_mul_f32 v40,v36,v40\nv_mfma_f32_16x16x32_bf16 v[220:223],a[140:143],v[96:99],v[220:223]\nv_mul_f32 v41,v37,v41\n" \
"v_mul_f32 v42,v38,v42\nv_mul_f32 v43,v39,v43\nv_mfma_f32_16x16x32_bf16 v[220:223],a[172:175],v[100:103],v[220:223]\nv_exp_f32 v40,v40\n" \
"s_waitcnt lgkmcnt(15)\nv_mfma_f32_16x16x32_bf16 v[224:227],a[176:179],v[100:103],v[224:227]\ns_waitcnt lgkmcnt(0)\ns_waitcnt vmcnt(5)\n" \
"v_mfma_f32_16x16x32_bf16 v[224:227],a[208:211],v[104:107],v[224:227]\nv_exp_f32 v41,v41\nv_mfma_f32_16x16x32_bf16 v[228:231],a[180:183],v[100:103],v[228:231]\nv_exp_f32 v42,v42\n" \
"v_mfma_f32_16x16x32_bf16 v[228:231],a[212:215],v[104:107],v[228:231]\nv_exp_f32 v43,v43\nv_add_f32 v40,1.0,v40\nv_mfma_f32_16x16x32_bf16 v[232:235],a[184:187],v[100:103],v[232:235]\n" \
"v_add_f32 v41,1.0,v41\nv_add_f32 v42,1.0,v42\nv_add_f32 v43,1.0,v43\nv_mfma_f32_16x16x32_bf16 v[232:235],a[216:219],v[104:107],v[232:235]\n" \
"v_rcp_f32 v40,v40\nv_mfma_f32_16x16x32_bf16 v[236:239],a[188:191],v[100:103],v[236:239]\nv_rcp_f32 v41,v41\nv_rcp_f32 v42,v42\n" \
"v_rcp_f32 v43,v43\nv_mul_f32 v36,v36,v40\nv_mul_f32 v37,v37,v41\nv_mul_f32 v38,v38,v42\n" \
"v_mul_f32 v39,v39,v43\nv_cvt_pk_bf16_f32 v40,v36,v37\nv_cvt_pk_bf16_f32 v41,v38,v39\nv_mfma_f32_16x16x32_bf16 v[236:239],a[220:223],v[104:107],v[236:239]\n" \
"v_and_b32 v58,0x3ff,v59\nv_mfma_f32_16x16x32_bf16 v[208:211],a[192:195],v[104:107],v[208:211]\nv_add_u32 v58,s99,v58\nv_add_u32 v46,s98,v58\n" \
"v_mfma_f32_16x16x32_bf16 v[212:215],a[196:199],v[104:107],v[212:215]\nds_read_b64 v[56:57],v46 offset:28680\nbuffer_store_dwordx2 v[40:41],v61,s[36:39],s52 offen sc1\nv_mfma_f32_16x16x32_bf16 v[216:219],a[200:203],v[104:107],v[216:219]\n" \
"global_load_dwordx4 v[72:75],v59,s[12:13] sc1\nv_mfma_f32_16x16x32_bf16 v[220:223],a[204:207],v[104:107],v[220:223]\nv_mfma_f32_16x16x32_bf16 v[16:19],a[112:115],v[192:195],v[16:19]\nglobal_load_dwordx4 v[76:79],v59,s[12:13] offset:1024 sc1\n" \
"s_waitcnt vmcnt(7)\nv_mfma_f32_16x16x32_bf16 v[16:19],a[144:147],v[196:199],v[16:19]\ns_waitcnt lgkmcnt(0)\ns_barrier\n" \
"s_mov_b32 m0,s58\nv_mfma_f32_16x16x32_bf16 v[20:23],a[116:119],v[192:195],v[20:23]\nglobal_load_dwordx4 v[80:83],v59,s[12:13] offset:2048 sc1\nds_write_addtid_b32 v208\n" \
"v_mfma_f32_16x16x32_bf16 v[20:23],a[148:151],v[196:199],v[20:23]\nds_write_addtid_b32 v209 offset:256\nds_write_addtid_b32 v210 offset:512\nv_mfma_f32_16x16x32_bf16 v[24:27],a[120:123],v[192:195],v[24:27]\n" \
"global_load_dwordx4 v[84:87],v59,s[14:15] sc1\nds_write_addtid_b32 v211 offset:768\nv_mfma_f32_16x16x32_bf16 v[24:27],a[152:155],v[196:199],v[24:27]\nds_write_addtid_b32 v212 offset:1024\n" \
"ds_write_addtid_b32 v213 offset:1280\nv_mfma_f32_16x16x32_bf16 v[28:31],a[124:127],v[192:195],v[28:31]\nglobal_load_dwordx4 v[88:91],v59,s[14:15] offset:1024 sc1\nds_write_addtid_b32 v214 offset:1536\n" \
"v_mfma_f32_16x16x32_bf16 v[28:31],a[156:159],v[196:199],v[28:31]\nds_write_addtid_b32 v215 offset:1792\nds_write_addtid_b32 v216 offset:2048\nv_mfma_f32_16x16x32_bf16 v[0:3],a[128:131],v[196:199],v[0:3]\n" \
"global_load_dwordx4 v[92:95],v59,s[14:15] offset:2048 sc1\ns_waitcnt vmcnt(10)\nv_mfma_f32_16x16x32_bf16 v[0:3],a[160:163],v[200:203],v[0:3]\nds_write_addtid_b32 v217 offset:2304\n" \
"ds_write_addtid_b32 v218 offset:2560\nv_mfma_f32_16x16x32_bf16 v[4:7],a[132:135],v[196:199],v[4:7]\nglobal_load_dwordx4 v[96:99],v59,s[16:17] sc1\nds_write_addtid_b32 v219 offset:2816\n" \
"v_mfma_f32_16x16x32_bf16 v[4:7],a[164:167],v[200:203],v[4:7]\nds_write_addtid_b32 v220 offset:3072\nds_write_addtid_b32 v221 offset:3328\nv_mfma_f32_16x16x32_bf16 v[8:11],a[136:139],v[196:199],v[8:11]\n" \
"global_load_dwordx4 v[100:103],v59,s[16:17] offset:1024 sc1\nds_write_addtid_b32 v222 offset:3584\nv_mfma_f32_16x16x32_bf16 v[8:11],a[168:171],v[200:203],v[8:11]\nds_write_addtid_b32 v223 offset:3840\n" \
"ds_write_addtid_b32 v224 offset:4096\nv_mfma_f32_16x16x32_bf16 v[12:15],a[140:143],v[196:199],v[12:15]\nglobal_load_dwordx4 v[104:107],v59,s[16:17] offset:2048 sc1\nds_write_addtid_b32 v225 offset:4352\n" \
"v_mfma_f32_16x16x32_bf16 v[12:15],a[172:175],v[200:203],v[12:15]\nds_write_addtid_b32 v226 offset:4608\nds_write_addtid_b32 v227 offset:4864\nv_mfma_f32_16x16x32_bf16 v[16:19],a[176:179],v[200:203],v[16:19]\n" \
"ds_write_addtid_b32 v228 offset:5120\ns_waitcnt vmcnt(12)\nv_mfma_f32_16x16x32_bf16 v[16:19],a[208:211],v[204:207],v[16:19]\nds_write_addtid_b32 v229 offset:5376\n" \
"ds_write_addtid_b32 v230 offset:5632\nv_mfma_f32_16x16x32_bf16 v[20:23],a[180:183],v[200:203],v[20:23]\nds_write_addtid_b32 v231 offset:5888\nds_write_addtid_b32 v232 offset:6144\n" \
"v_mfma_f32_16x16x32_bf16 v[20:23],a[212:215],v[204:207],v[20:23]\nds_write_addtid_b32 v233 offset:6400\nds_write_addtid_b32 v234 offset:6656\nv_mfma_f32_16x16x32_bf16 v[24:27],a[184:187],v[200:203],v[24:27]\n" \
"ds_write_addtid_b32 v235 offset:6912\nds_write_addtid_b32 v236 offset:7168\nv_mfma_f32_16x16x32_bf16 v[24:27],a[216:219],v[204:207],v[24:27]\nds_write_addtid_b32 v237 offset:7424\n" \
"ds_write_addtid_b32 v238 offset:7680\nv_mfma_f32_16x16x32_bf16 v[28:31],a[188:191],v[200:203],v[28:31]\nds_write_addtid_b32 v239 offset:7936\nv_mfma_f32_16x16x32_bf16 v[28:31],a[220:223],v[204:207],v[28:31]\n" \
"v_mfma_f32_16x16x32_bf16 v[0:3],a[192:195],v[204:207],v[0:3]\nv_mfma_f32_16x16x32_bf16 v[4:7],a[196:199],v[204:207],v[4:7]\nv_mfma_f32_16x16x32_bf16 v[8:11],a[200:203],v[204:207],v[8:11]\nv_mfma_f32_16x16x32_bf16 v[12:15],a[204:207],v[204:207],v[12:15]\n" \
"s_waitcnt vmcnt(7)\nv_mfma_f32_16x16x32_bf16 v[208:211],v[128:131],v[72:75],0\ns_mov_b64 exec,1\nbuffer_store_dword v61,off,s[36:39],s54 sc1\n" \
"s_mov_b64 exec,-1\nv_mfma_f32_16x16x32_bf16 v[208:211],v[160:163],v[76:79],v[208:211]\nv_cmp_eq_u32 vcc,0,v63\nv_mfma_f32_16x16x32_bf16 v[212:215],v[132:135],v[72:75],0\n" \
"s_waitcnt lgkmcnt(0)\ns_barrier\nv_mfma_f32_16x16x32_bf16 v[212:215],v[164:167],v[76:79],v[212:215]\ns_and_b64 vcc,vcc,s[86:87]\n" \
"s_cbranch_scc1 .LsbS_%=\n.LbbS_%=:\nv_mfma_f32_16x16x32_bf16 v[216:219],v[136:139],v[72:75],0\nglobal_load_dwordx4 v[108:111],v59,s[4:5] sc1\n" \
"ds_write_addtid_b32 v0 offset:32768\nv_mfma_f32_16x16x32_bf16 v[216:219],v[168:171],v[76:79],v[216:219]\nds_write_addtid_b32 v1 offset:33024\nds_write_addtid_b32 v2 offset:33280\n" \
"v_mfma_f32_16x16x32_bf16 v[220:223],v[140:143],v[72:75],0\nglobal_load_dwordx4 v[112:115],v59,s[4:5] offset:1024 sc1\nds_write_addtid_b32 v3 offset:33536\nv_mfma_f32_16x16x32_bf16 v[220:223],v[172:175],v[76:79],v[220:223]\n" \
"ds_write_addtid_b32 v4 offset:33792\nds_write_addtid_b32 v5 offset:34048\nv_mfma_f32_16x16x32_bf16 v[224:227],v[144:147],v[72:75],0\nglobal_load_dwordx4 v[116:119],v59,s[4:5] offset:2048 sc1\n" \
"ds_write_addtid_b32 v6 offset:34304\nv_mfma_f32_16x16x32_bf16 v[224:227],v[176:179],v[76:79],v[224:227]\nds_write_addtid_b32 v7 offset:34560\nds_write_addtid_b32 v8 offset:34816\n" \
"v_mfma_f32_16x16x32_bf16 v[228:231],v[148:151],v[72:75],0\nglobal_load_dwordx4 v[120:123],v59,s[6:7] sc1\nds_write_addtid_b32 v9 offset:35072\nv_mfma_f32_16x16x32_bf16 v[228:231],v[180:183],v[76:79],v[228:231]\n" \
"ds_write_addtid_b32 v10 offset:35328\nds_write_addtid_b32 v11 offset:35584\nv_mfma_f32_16x16x32_bf16 v[232:235],v[152:155],v[72:75],0\nglobal_load_dwordx4 v[124:127],v59,s[6:7] offset:1024 sc1\n" \
"ds_write_addtid_b32 v12 offset:35840\nv_mfma_f32_16x16x32_bf16 v[232:235],v[184:187],v[76:79],v[232:235]\nds_write_addtid_b32 v13 offset:36096\nds_write_addtid_b32 v14 offset:36352\n" \
"v_mfma_f32_16x16x32_bf16 v[236:239],v[156:159],v[72:75],0\nglobal_load_dwordx4 v[192:195],v59,s[6:7] offset:2048 sc1\nds_write_addtid_b32 v15 offset:36608\nv_mfma_f32_16x16x32_bf16 v[236:239],v[188:191],v[76:79],v[236:239]\n" \
"ds_write_addtid_b32 v16 offset:36864\ns_waitcnt vmcnt(12)\nv_mfma_f32_16x16x32_bf16 v[208:211],a[0:3],v[80:83],v[208:211]\nglobal_load_dwordx4 v[196:199],v59,s[8:9] sc1\n" \
"ds_write_addtid_b32 v17 offset:37120\nv_mfma_f32_16x16x32_bf16 v[208:211],a[32:35],v[84:87],v[208:211]\nds_write_addtid_b32 v18 offset:37376\nds_write_addtid_b32 v19 offset:37632\n" \
"v_mfma_f32_16x16x32_bf16 v[212:215],a[4:7],v[80:83],v[212:215]\nglobal_load_dwordx4 v[200:203],v59,s[8:9] offset:1024 sc1\nds_write_addtid_b32 v20 offset:37888\nv_mfma_f32_16x16x32_bf16 v[212:215],a[36:39],v[84:87],v[212:215]\n" \
"ds_write_addtid_b32 v21 offset:38144\nds_write_addtid_b32 v22 offset:38400\nv_mfma_f32_16x16x32_bf16 v[216:219],a[8:11],v[80:83],v[216:219]\nglobal_load_dwordx4 v[204:207],v59,s[8:9] offset:2048 sc1\n" \
"ds_write_addtid_b32 v23 offset:38656\nv_mfma_f32_16x16x32_bf16 v[216:219],a[40:43],v[84:87],v[216:219]\nds_write_addtid_b32 v24 offset:38912\nds_write_addtid_b32 v25 offset:39168\n" \
"v_mfma_f32_16x16x32_bf16 v[220:223],a[12:15],v[80:83],v[220:223]\nds_write_addtid_b32 v26 offset:39424\nds_write_addtid_b32 v27 offset:39680\nv_mfma_f32_16x16x32_bf16 v[220:223],a[44:47],v[84:87],v[220:223]\n" \
"ds_write_addtid_b32 v28 offset:39936\nds_write_addtid_b32 v29 offset:40192\nv_mfma_f32_16x16x32_bf16 v[224:227],a[16:19],v[80:83],v[224:227]\nds_write_addtid_b32 v30 offset:40448\n" \
"ds_write_addtid_b32 v31 offset:40704\nv_mfma_f32_16x16x32_bf16 v[224:227],a[48:51],v[84:87],v[224:227]\nds_read_b128 v[32:35],v58\nds_read_b128 v[36:39],v58 offset:8192\n" \
"v_mfma_f32_16x16x32_bf16 v[228:231],a[20:23],v[80:83],v[228:231]\nds_read_b128 v[40:43],v58 offset:16384\nds_read_b128 v[44:47],v58 offset:24576\nv_mfma_f32_16x16x32_bf16 v[228:231],a[52:55],v[84:87],v[228:231]\n" \
"ds_read_b128 v[48:51],v58 offset:4096\nds_read_b128 v[240:243],v58 offset:12288\nv_mfma_f32_16x16x32_bf16 v[232:235],a[24:27],v[80:83],v[232:235]\nds_read_b128 v[244:247],v58 offset:20480\n" \
"ds_read_b128 v[248:251],v58 offset:28672\nv_mfma_f32_16x16x32_bf16 v[232:235],a[56:59],v[84:87],v[232:235]\nv_mfma_f32_16x16x32_bf16 v[236:239],a[28:31],v[80:83],v[236:239]\ns_waitcnt lgkmcnt(0)\n" \
"s_barrier\nv_mfma_f32_16x16x32_bf16 v[236:239],a[60:63],v[84:87],v[236:239]\ns_waitcnt vmcnt(13)\nv_mfma_f32_16x16x32_bf16 v[208:211],a[64:67],v[88:91],v[208:211]\n" \
"v_mfma_f32_16x16x32_bf16 v[208:211],a[96:99],v[92:95],v[208:211]\nv_mfma_f32_16x16x32_bf16 v[212:215],a[68:71],v[88:91],v[212:215]\nv_add_f32 v32,v32,v36\nv_mfma_f32_16x16x32_bf16 v[212:215],a[100:103],v[92:95],v[212:215]\n" \
"v_add_f32 v33,v33,v37\nv_add_f32 v34,v34,v38\nv_mfma_f32_16x16x32_bf16 v[216:219],a[72:75],v[88:91],v[216:219]\nv_add_f32 v35,v35,v39\n" \
"v_add_f32 v40,v40,v44\nv_mfma_f32_16x16x32_bf16 v[216:219],a[104:107],v[92:95],v[216:219]\nv_add_f32 v41,v41,v45\nv_add_f32 v42,v42,v46\n" \
"v_mfma_f32_16x16x32_bf16 v[220:223],a[76:79],v[88:91],v[220:223]\nv_add_f32 v43,v43,v47\nv_add_f32 v32,v32,v40\nv_mfma_f32_16x16x32_bf16 v[220:223],a[108:111],v[92:95],v[220:223]\n" \
"v_add_f32 v33,v33,v41\nv_add_f32 v34,v34,v42\nv_mfma_f32_16x16x32_bf16 v[224:227],a[80:83],v[88:91],v[224:227]\nv_add_f32 v35,v35,v43\n" \
"v_cvt_pk_bf16_f32 v36,v32,v33\nv_mfma_f32_16x16x32_bf16 v[224:227],a[112:115],v[92:95],v[224:227]\nv_cvt_pk_bf16_f32 v37,v34,v35\nv_mfma_f32_16x16x32_bf16 v[228:231],a[84:87],v[88:91],v[228:231]\n" \
"v_add_f32 v48,v48,v240\nv_add_f32 v49,v49,v241\nv_mfma_f32_16x16x32_bf16 v[228:231],a[116:119],v[92:95],v[228:231]\nv_add_f32 v50,v50,v242\n" \
"v_add_f32 v51,v51,v243\nv_mfma_f32_16x16x32_bf16 v[232:235],a[88:91],v[88:91],v[232:235]\nv_add_f32 v244,v244,v248\nv_add_f32 v245,v245,v249\n" \
"v_mfma_f32_16x16x32_bf16 v[232:235],a[120:123],v[92:95],v[232:235]\nv_add_f32 v246,v246,v250\nv_add_f32 v247,v247,v251\nv_mfma_f32_16x16x32_bf16 v[236:239],a[92:95],v[88:91],v[236:239]\n" \
"v_add_f32 v48,v48,v244\nv_add_f32 v49,v49,v245\nv_mfma_f32_16x16x32_bf16 v[236:239],a[124:127],v[92:95],v[236:239]\nv_add_f32 v50,v50,v246\n" \
"s_waitcnt vmcnt(11)\nv_mfma_f32_16x16x32_bf16 v[208:211],a[128:131],v[96:99],v[208:211]\nv_add_f32 v51,v51,v247\nv_cvt_pk_bf16_f32 v38,v48,v49\n" \
"v_mfma_f32_16x16x32_bf16 v[208:211],a[160:163],v[100:103],v[208:211]\nv_cvt_pk_bf16_f32 v39,v50,v51\nbuffer_store_dwordx4 v[36:39],v59,s[36:39],s74 offen sc1\nds_read_b128 v[32:35],v58 offset:32768\n" \
"v_mfma_f32_16x16x32_bf16 v[212:215],a[132:135],v[96:99],v[212:215]\nds_read_b128 v[36:39],v58 offset:40960\nds_read_b128 v[40:43],v58 offset:49152\nv_mfma_f32_16x16x32_bf16 v[212:215],a[164:167],v[100:103],v[212:215]\n" \
"ds_read_b128 v[44:47],v58 offset:57344\nds_read_b128 v[48:51],v58 offset:36864\nv_mfma_f32_16x16x32_bf16 v[216:219],a[136:139],v[96:99],v[216:219]\nds_read_b128 v[240:243],v58 offset:45056\n" \
"ds_read_b128 v[244:247],v58 offset:53248\nv_mfma_f32_16x16x32_bf16 v[216:219],a[168:171],v[100:103],v[216:219]\nds_read_b128 v[248:251],v58 offset:61440\nv_mfma_f32_16x16x32_bf16 v[220:223],a[140:143],v[96:99],v[220:223]\n" \
"v_mfma_f32_16x16x32_bf16 v[220:223],a[172:175],v[100:103],v[220:223]\nv_mfma_f32_16x16x32_bf16 v[224:227],a[144:147],v[96:99],v[224:227]\nv_mfma_f32_16x16x32_bf16 v[224:227],a[176:179],v[100:103],v[224:227]\nv_mfma_f32_16x16x32_bf16 v[228:231],a[148:151],v[96:99],v[228:231]\n" \
"v_mfma_f32_16x16x32_bf16 v[228:231],a[180:183],v[100:103],v[228:231]\ns_waitcnt lgkmcnt(4)\nv_add_f32 v32,v32,v36\nv_add_f32 v33,v33,v37\n" \
"v_mfma_f32_16x16x32_bf16 v[232:235],a[152:155],v[96:99],v[232:235]\nv_add_f32 v34,v34,v38\nv_add_f32 v35,v35,v39\nv_add_f32 v40,v40,v44\n" \
"v_mfma_f32_16x16x32_bf16 v[232:235],a[184:187],v[100:103],v[232:235]\nv_add_f32 v41,v41,v45\nv_add_f32 v42,v42,v46\nv_add_f32 v43,v43,v47\n" \
"v_mfma_f32_16x16x32_bf16 v[236:239],a[156:159],v[96:99],v[236:239]\nv_add_f32 v32,v32,v40\nv_add_f32 v33,v33,v41\nv_add_f32 v34,v34,v42\n" \
"v_mfma_f32_16x16x32_bf16 v[236:239],a[188:191],v[100:103],v[236:239]\nv_add_f32 v35,v35,v43\nv_cvt_pk_bf16_f32 v36,v32,v33\ns_waitcnt vmcnt(11)\n" \
"v_mfma_f32_16x16x32_bf16 v[208:211],a[192:195],v[104:107],v[208:211]\nv_cvt_pk_bf16_f32 v37,v34,v35\ns_waitcnt lgkmcnt(0)\nv_add_f32 v48,v48,v240\n" \
"v_mfma_f32_16x16x32_bf16 v[212:215],a[196:199],v[104:107],v[212:215]\nv_add_f32 v49,v49,v241\nv_add_f32 v50,v50,v242\nv_add_f32 v51,v51,v243\n" \
"v_mfma_f32_16x16x32_bf16 v[216:219],a[200:203],v[104:107],v[216:219]\nv_add_f32 v244,v244,v248\nv_add_f32 v245,v245,v249\nv_mfma_f32_16x16x32_bf16 v[220:223],a[204:207],v[104:107],v[220:223]\n" \
"v_add_f32 v246,v246,v250\nv_add_f32 v247,v247,v251\nv_mfma_f32_16x16x32_bf16 v[224:227],a[208:211],v[104:107],v[224:227]\nv_add_f32 v48,v48,v244\n" \
"v_add_f32 v49,v49,v245\nv_mfma_f32_16x16x32_bf16 v[228:231],a[212:215],v[104:107],v[228:231]\nv_add_f32 v50,v50,v246\nv_add_f32 v51,v51,v247\n" \
"v_mfma_f32_16x16x32_bf16 v[232:235],a[216:219],v[104:107],v[232:235]\nv_cvt_pk_bf16_f32 v38,v48,v49\nv_cvt_pk_bf16_f32 v39,v50,v51\nv_mfma_f32_16x16x32_bf16 v[236:239],a[220:223],v[104:107],v[236:239]\n" \
"v_add_u32 v59,409600,v59\nbuffer_store_dwordx4 v[36:39],v59,s[36:39],s74 offen sc1\n.Le_%=:\ns_waitcnt vmcnt(9)\n" \
"v_mfma_f32_16x16x32_bf16 v[0:3],v[128:131],v[108:111],0\nv_add_u32 v59,409600,v59\nv_mfma_f32_16x16x32_bf16 v[0:3],v[160:163],v[112:115],v[0:3]\nds_write_addtid_b32 v208\n" \
"v_mfma_f32_16x16x32_bf16 v[4:7],v[132:135],v[108:111],0\nglobal_load_dwordx4 v[72:75],v59,s[12:13] sc1\nds_write_addtid_b32 v209 offset:256\nv_mfma_f32_16x16x32_bf16 v[4:7],v[164:167],v[112:115],v[4:7]\n" \
"ds_write_addtid_b32 v210 offset:512\nv_mfma_f32_16x16x32_bf16 v[8:11],v[136:139],v[108:111],0\nds_write_addtid_b32 v211 offset:768\nv_mfma_f32_16x16x32_bf16 v[8:11],v[168:171],v[112:115],v[8:11]\n" \
"ds_write_addtid_b32 v212 offset:1024\nv_mfma_f32_16x16x32_bf16 v[12:15],v[140:143],v[108:111],0\nds_write_addtid_b32 v213 offset:1280\nv_mfma_f32_16x16x32_bf16 v[12:15],v[172:175],v[112:115],v[12:15]\n" \
"ds_write_addtid_b32 v214 offset:1536\nv_mfma_f32_16x16x32_bf16 v[16:19],v[144:147],v[108:111],0\nglobal_load_dwordx4 v[76:79],v59,s[12:13] offset:1024 sc1\nds_write_addtid_b32 v215 offset:1792\n" \
"v_mfma_f32_16x16x32_bf16 v[16:19],v[176:179],v[112:115],v[16:19]\nds_write_addtid_b32 v216 offset:2048\nv_mfma_f32_16x16x32_bf16 v[20:23],v[148:151],v[108:111],0\nds_write_addtid_b32 v217 offset:2304\n" \
"v_mfma_f32_16x16x32_bf16 v[20:23],v[180:183],v[112:115],v[20:23]\nds_write_addtid_b32 v218 offset:2560\nv_mfma_f32_16x16x32_bf16 v[24:27],v[152:155],v[108:111],0\nds_write_addtid_b32 v219 offset:2816\n" \
"v_mfma_f32_16x16x32_bf16 v[24:27],v[184:187],v[112:115],v[24:27]\nds_write_addtid_b32 v220 offset:3072\nv_mfma_f32_16x16x32_bf16 v[28:31],v[156:159],v[108:111],0\nglobal_load_dwordx4 v[80:83],v59,s[12:13] offset:2048 sc1\n" \
"ds_write_addtid_b32 v221 offset:3328\nv_mfma_f32_16x16x32_bf16 v[28:31],v[188:191],v[112:115],v[28:31]\nds_write_addtid_b32 v222 offset:3584\ns_waitcnt vmcnt(10)\n" \
"v_mfma_f32_16x16x32_bf16 v[0:3],a[0:3],v[116:119],v[0:3]\nds_write_addtid_b32 v223 offset:3840\nv_bfe_u32 v69,v59,2,8\nv_mfma_f32_16x16x32_bf16 v[0:3],a[32:35],v[120:123],v[0:3]\n" \
"ds_write_addtid_b32 v224 offset:4096\nbuffer_load_dword v64,v69,s[36:39],s79 offen sc1\nv_mfma_f32_16x16x32_bf16 v[4:7],a[4:7],v[116:119],v[4:7]\nds_write_addtid_b32 v225 offset:4352\n" \
"buffer_load_dword v65,v69,s[36:39],s80 offen sc1\nv_mfma_f32_16x16x32_bf16 v[4:7],a[36:39],v[120:123],v[4:7]\nds_write_addtid_b32 v226 offset:4608\nbuffer_load_dword v66,v69,s[36:39],s81 offen sc1\n" \
"v_mfma_f32_16x16x32_bf16 v[8:11],a[8:11],v[116:119],v[8:11]\nglobal_load_dwordx4 v[84:87],v59,s[14:15] sc1\nds_write_addtid_b32 v227 offset:4864\nv_mfma_f32_16x16x32_bf16 v[8:11],a[40:43],v[120:123],v[8:11]\n" \
"ds_write_addtid_b32 v228 offset:5120\nbuffer_load_dword v67,v69,s[36:39],s82 offen sc1\nv_mfma_f32_16x16x32_bf16 v[12:15],a[12:15],v[116:119],v[12:15]\nds_write_addtid_b32 v229 offset:5376\n" \
"buffer_load_dword v68,v57,s[36:39],s90 offen sc1\nv_mfma_f32_16x16x32_bf16 v[12:15],a[44:47],v[120:123],v[12:15]\nds_write_addtid_b32 v230 offset:5632\nv_add_u32 v70,s98,v58\n" \
"v_mfma_f32_16x16x32_bf16 v[16:19],a[16:19],v[116:119],v[16:19]\ns_add_u32 s59,s59,0x10000\nbuffer_load_dword v63,v61,s[36:39],s59 offen sc1\nv_mfma_f32_16x16x32_bf16 v[16:19],a[48:51],v[120:123],v[16:19]\n" \
"ds_write_addtid_b32 v231 offset:5888\nv_mfma_f32_16x16x32_bf16 v[20:23],a[20:23],v[116:119],v[20:23]\nglobal_load_dwordx4 v[88:91],v59,s[14:15] offset:1024 sc1\nds_write_addtid_b32 v232 offset:6144\n" \
"v_mfma_f32_16x16x32_bf16 v[20:23],a[52:55],v[120:123],v[20:23]\nds_write_addtid_b32 v233 offset:6400\nv_mfma_f32_16x16x32_bf16 v[24:27],a[24:27],v[116:119],v[24:27]\nds_write_addtid_b32 v234 offset:6656\n" \
"v_mfma_f32_16x16x32_bf16 v[24:27],a[56:59],v[120:123],v[24:27]\nds_write_addtid_b32 v235 offset:6912\nv_mfma_f32_16x16x32_bf16 v[28:31],a[28:31],v[116:119],v[28:31]\nds_write_addtid_b32 v236 offset:7168\n" \
"v_mfma_f32_16x16x32_bf16 v[28:31],a[60:63],v[120:123],v[28:31]\nds_write_addtid_b32 v237 offset:7424\ns_waitcnt vmcnt(16)\nv_mfma_f32_16x16x32_bf16 v[0:3],a[64:67],v[124:127],v[0:3]\n" \
"global_load_dwordx4 v[92:95],v59,s[14:15] offset:2048 sc1\nds_write_addtid_b32 v238 offset:7680\nv_mfma_f32_16x16x32_bf16 v[0:3],a[96:99],v[192:195],v[0:3]\nds_write_addtid_b32 v239 offset:7936\n" \
"v_mfma_f32_16x16x32_bf16 v[4:7],a[68:71],v[124:127],v[4:7]\nds_read_b64 v[70:71],v70 offset:29704\nv_mfma_f32_16x16x32_bf16 v[4:7],a[100:103],v[192:195],v[4:7]\nv_mfma_f32_16x16x32_bf16 v[8:11],a[72:75],v[124:127],v[8:11]\n" \
"v_mfma_f32_16x16x32_bf16 v[8:11],a[104:107],v[192:195],v[8:11]\nv_mfma_f32_16x16x32_bf16 v[12:15],a[76:79],v[124:127],v[12:15]\nglobal_load_dwordx4 v[96:99],v59,s[16:17] sc1\nv_mfma_f32_16x16x32_bf16 v[12:15],a[108:111],v[192:195],v[12:15]\n" \
"s_waitcnt lgkmcnt(0)\ns_barrier\nv_mfma_f32_16x16x32_bf16 v[16:19],a[80:83],v[124:127],v[16:19]\nds_read_b128 v[32:35],v58\n" \
"v_mfma_f32_16x16x32_bf16 v[16:19],a[112:115],v[192:195],v[16:19]\nds_read_b128 v[36:39],v58 offset:8192\nv_mfma_f32_16x16x32_bf16 v[20:23],a[84:87],v[124:127],v[20:23]\nds_read_b128 v[40:43],v58 offset:16384\n" \
"v_mfma_f32_16x16x32_bf16 v[20:23],a[116:119],v[192:195],v[20:23]\nds_read_b128 v[44:47],v58 offset:24576\nv_mfma_f32_16x16x32_bf16 v[24:27],a[88:91],v[124:127],v[24:27]\nglobal_load_dwordx4 v[100:103],v59,s[16:17] offset:1024 sc1\n" \
"ds_read_b128 v[48:51],v58 offset:4096\nv_mfma_f32_16x16x32_bf16 v[24:27],a[120:123],v[192:195],v[24:27]\nds_read_b128 v[240:243],v58 offset:12288\nv_mfma_f32_16x16x32_bf16 v[28:31],a[92:95],v[124:127],v[28:31]\n" \
"ds_read_b128 v[244:247],v58 offset:20480\nv_mfma_f32_16x16x32_bf16 v[28:31],a[124:127],v[192:195],v[28:31]\nds_read_b128 v[248:251],v58 offset:28672\ns_waitcnt vmcnt(17)\n" \
"v_mfma_f32_16x16x32_bf16 v[0:3],a[128:131],v[196:199],v[0:3]\nv_mfma_f32_16x16x32_bf16 v[0:3],a[160:163],v[200:203],v[0:3]\nv_mfma_f32_16x16x32_bf16 v[4:7],a[132:135],v[196:199],v[4:7]\nglobal_load_dwordx4 v[104:107],v59,s[16:17] offset:2048 sc1\n" \
"v_mfma_f32_16x16x32_bf16 v[4:7],a[164:167],v[200:203],v[4:7]\ns_waitcnt lgkmcnt(4)\nv_add_f32 v32,v32,v36\nv_mfma_f32_16x16x32_bf16 v[8:11],a[136:139],v[196:199],v[8:11]\n" \
"v_add_f32 v33,v33,v37\nv_add_f32 v34,v34,v38\nv_mfma_f32_16x16x32_bf16 v[8:11],a[168:171],v[200:203],v[8:11]\nv_add_f32 v35,v35,v39\n" \
"v_add_f32 v40,v40,v44\nv_mfma_f32_16x16x32_bf16 v[12:15],a[140:143],v[196:199],v[12:15]\nv_add_f32 v41,v41,v45\nv_add_f32 v42,v42,v46\n" \
"v_mfma_f32_16x16x32_bf16 v[12:15],a[172:175],v[200:203],v[12:15]\nv_add_f32 v43,v43,v47\nv_add_f32 v32,v32,v40\nv_mfma_f32_16x16x32_bf16 v[16:19],a[144:147],v[196:199],v[16:19]\n" \
"v_add_f32 v33,v33,v41\nv_add_f32 v34,v34,v42\nv_mfma_f32_16x16x32_bf16 v[16:19],a[176:179],v[200:203],v[16:19]\nv_add_f32 v35,v35,v43\n" \
"v_cvt_pk_bf16_f32 v36,v32,v33\nv_mfma_f32_16x16x32_bf16 v[20:23],a[148:151],v[196:199],v[20:23]\nv_cvt_pk_bf16_f32 v37,v34,v35\ns_waitcnt lgkmcnt(0)\n" \
"v_mfma_f32_16x16x32_bf16 v[20:23],a[180:183],v[200:203],v[20:23]\nv_add_f32 v48,v48,v240\nv_add_f32 v49,v49,v241\nv_mfma_f32_16x16x32_bf16 v[24:27],a[152:155],v[196:199],v[24:27]\n" \
"v_add_f32 v50,v50,v242\nv_add_f32 v51,v51,v243\nv_mfma_f32_16x16x32_bf16 v[24:27],a[184:187],v[200:203],v[24:27]\nv_add_f32 v244,v244,v248\n" \
"v_add_f32 v245,v245,v249\nv_mfma_f32_16x16x32_bf16 v[28:31],a[156:159],v[196:199],v[28:31]\nv_add_f32 v246,v246,v250\nv_add_f32 v247,v247,v251\n" \
"v_mfma_f32_16x16x32_bf16 v[28:31],a[188:191],v[200:203],v[28:31]\nv_add_f32 v48,v48,v244\ns_waitcnt vmcnt(17)\nv_mfma_f32_16x16x32_bf16 v[0:3],a[192:195],v[204:207],v[0:3]\n" \
"v_add_f32 v49,v49,v245\nv_add_f32 v50,v50,v246\nv_mfma_f32_16x16x32_bf16 v[4:7],a[196:199],v[204:207],v[4:7]\nv_add_f32 v51,v51,v247\n" \
"v_cvt_pk_bf16_f32 v38,v48,v49\nv_mfma_f32_16x16x32_bf16 v[8:11],a[200:203],v[204:207],v[8:11]\nv_cvt_pk_bf16_f32 v39,v50,v51\nbuffer_store_dwordx4 v[36:39],v59,s[36:39],s74 offen sc1\n" \
"v_mfma_f32_16x16x32_bf16 v[12:15],a[204:207],v[204:207],v[12:15]\nv_mfma_f32_16x16x32_bf16 v[16:19],a[208:211],v[204:207],v[16:19]\nv_mfma_f32_16x16x32_bf16 v[20:23],a[212:215],v[204:207],v[20:23]\nv_mfma_f32_16x16x32_bf16 v[24:27],a[216:219],v[204:207],v[24:27]\n" \
"v_mfma_f32_16x16x32_bf16 v[28:31],a[220:223],v[204:207],v[28:31]\ns_waitcnt vmcnt(14)\nv_mfma_f32_16x16x32_bf16 v[208:211],v[128:131],v[72:75],0\nv_add_u32 v59,409600,v59\n" \
"v_mfma_f32_16x16x32_bf16 v[208:211],v[160:163],v[76:79],v[208:211]\ns_waitcnt vmcnt(6)\nv_cmp_eq_u32 vcc,0,v63\nv_mfma_f32_16x16x32_bf16 v[212:215],v[132:135],v[72:75],0\n" \
"ds_write_addtid_b32 v0 offset:32768\nv_mfma_f32_16x16x32_bf16 v[212:215],v[164:167],v[76:79],v[212:215]\ns_and_b64 vcc,vcc,s[86:87]\ns_cbranch_scc1 .Lsg4_%=\n" \
".Lbg4_%=:\nv_mfma_f32_16x16x32_bf16 v[216:219],v[136:139],v[72:75],0\nglobal_load_dwordx4 v[108:111],v59,s[12:13] sc1\nds_write_addtid_b32 v1 offset:33024\n" \
"v_mfma_f32_16x16x32_bf16 v[216:219],v[168:171],v[76:79],v[216:219]\nds_write_addtid_b32 v2 offset:33280\nv_mfma_f32_16x16x32_bf16 v[220:223],v[140:143],v[72:75],0\nds_write_addtid_b32 v3 offset:33536\n" \
"v_max3_u32 v52,v64,v65,v66\nv_mfma_f32_16x16x32_bf16 v[220:223],v[172:175],v[76:79],v[220:223]\nds_write_addtid_b32 v4 offset:33792\nv_max_u32 v52,v52,v67\n" \
"v_mfma_f32_16x16x32_bf16 v[224:227],v[144:147],v[72:75],0\nglobal_load_dwordx4 v[112:115],v59,s[12:13] offset:1024 sc1\nds_write_addtid_b32 v5 offset:34048\nv_mfma_f32_16x16x32_bf16 v[224:227],v[176:179],v[76:79],v[224:227]\n" \
"ds_write_addtid_b32 v6 offset:34304\nv_cmp_eq_u32 vcc,-1,v52\nv_mfma_f32_16x16x32_bf16 v[228:231],v[148:151],v[72:75],0\nds_write_addtid_b32 v7 offset:34560\n" \
"v_mfma_f32_16x16x32_bf16 v[228:231],v[180:183],v[76:79],v[228:231]\nds_write_addtid_b32 v8 offset:34816\ns_cbranch_vccnz .Lse_%=\n.Lbe_%=:\n" \
"v_mfma_f32_16x16x32_bf16 v[232:235],v[152:155],v[72:75],0\nds_write_addtid_b32 v9 offset:35072\nv_lshlrev_b32 v52,16,v64\nv_mfma_f32_16x16x32_bf16 v[232:235],v[184:187],v[76:79],v[232:235]\n" \
"ds_write_addtid_b32 v10 offset:35328\nv_and_b32 v53,0xffff0000,v64\nv_mfma_f32_16x16x32_bf16 v[236:239],v[156:159],v[72:75],0\nglobal_load_dwordx4 v[116:119],v59,s[12:13] offset:2048 sc1\n" \
"ds_write_addtid_b32 v11 offset:35584\nv_mfma_f32_16x16x32_bf16 v[236:239],v[188:191],v[76:79],v[236:239]\nds_write_addtid_b32 v12 offset:35840\nv_mfma_f32_16x16x32_bf16 v[208:211],a[0:3],v[80:83],v[208:211]\n" \
"ds_write_addtid_b32 v13 offset:36096\nv_lshlrev_b32 v54,16,v65\nv_mfma_f32_16x16x32_bf16 v[208:211],a[32:35],v[84:87],v[208:211]\nds_write_addtid_b32 v14 offset:36352\n" \
"v_and_b32 v55,0xffff0000,v65\nv_mfma_f32_16x16x32_bf16 v[212:215],a[4:7],v[80:83],v[212:215]\nds_write_addtid_b32 v15 offset:36608\nv_lshlrev_b32 v252,16,v66\n" \
"v_mfma_f32_16x16x32_bf16 v[212:215],a[36:39],v[84:87],v[212:215]\nds_write_addtid_b32 v16 offset:36864\nv_and_b32 v253,0xffff0000,v66\nv_mfma_f32_16x16x32_bf16 v[216:219],a[8:11],v[80:83],v[216:219]\n" \
"global_load_dwordx4 v[120:123],v59,s[14:15] sc1\nds_write_addtid_b32 v17 offset:37120\nv_mfma_f32_16x16x32_bf16 v[216:219],a[40:43],v[84:87],v[216:219]\nds_write_addtid_b32 v18 offset:37376\n" \
"v_lshlrev_b32 v254,16,v67\nv_mfma_f32_16x16x32_bf16 v[220:223],a[12:15],v[80:83],v[220:223]\nds_write_addtid_b32 v19 offset:37632\nv_and_b32 v255,0xffff0000,v67\n" \
"v_mfma_f32_16x16x32_bf16 v[220:223],a[44:47],v[84:87],v[220:223]\nds_write_addtid_b32 v20 offset:37888\nv_add_f32 v52,v52,v54\nv_mfma_f32_16x16x32_bf16 v[224:227],a[16:19],v[80:83],v[224:227]\n" \
"ds_write_addtid_b32 v21 offset:38144\nv_add_f32 v53,v53,v55\nv_mfma_f32_16x16x32_bf16 v[224:227],a[48:51],v[84:87],v[224:227]\nds_write_addtid_b32 v22 offset:38400\n" \
"v_add_f32 v252,v252,v254\nv_mfma_f32_16x16x32_bf16 v[228:231],a[20:23],v[80:83],v[228:231]\nglobal_load_dwordx4 v[124:127],v59,s[14:15] offset:1024 sc1\nds_write_addtid_b32 v23 offset:38656\n" \
"v_mfma_f32_16x16x32_bf16 v[228:231],a[52:55],v[84:87],v[228:231]\nds_write_addtid_b32 v24 offset:38912\nv_add_f32 v253,v253,v255\nv_mfma_f32_16x16x32_bf16 v[232:235],a[24:27],v[80:83],v[232:235]\n" \
"ds_write_addtid_b32 v25 offset:39168\ns_waitcnt lgkmcnt(15)\nv_mfma_f32_16x16x32_bf16 v[232:235],a[56:59],v[84:87],v[232:235]\nds_write_addtid_b32 v26 offset:39424\n" \
"v_add_f32 v52,v52,v70\nv_mfma_f32_16x16x32_bf16 v[236:239],a[28:31],v[80:83],v[236:239]\nds_write_addtid_b32 v27 offset:39680\nv_add_f32 v53,v53,v70\n" \
"v_mfma_f32_16x16x32_bf16 v[236:239],a[60:63],v[84:87],v[236:239]\nds_write_addtid_b32 v28 offset:39936\ns_waitcnt vmcnt(9)\nv_mfma_f32_16x16x32_bf16 v[208:211],a[64:67],v[88:91],v[208:211]\n" \
"global_load_dwordx4 v[192:195],v59,s[14:15] offset:2048 sc1\nds_write_addtid_b32 v29 offset:40192\nv_mfma_f32_16x16x32_bf16 v[208:211],a[96:99],v[92:95],v[208:211]\nds_write_addtid_b32 v30 offset:40448\n" \
"v_lshlrev_b32 v54,16,v71\nv_mfma_f32_16x16x32_bf16 v[212:215],a[68:71],v[88:91],v[212:215]\nds_write_addtid_b32 v31 offset:40704\nv_and_b32 v55,0xffff0000,v71\n" \
"v_mfma_f32_16x16x32_bf16 v[212:215],a[100:103],v[92:95],v[212:215]\nv_lshlrev_b32 v254,16,v68\nv_and_b32 v255,0xffff0000,v68\nv_mfma_f32_16x16x32_bf16 v[216:219],a[72:75],v[88:91],v[216:219]\n" \
"v_add_f32 v52,v52,v252\nv_add_f32 v53,v53,v253\nv_mfma_f32_16x16x32_bf16 v[216:219],a[104:107],v[92:95],v[216:219]\nv_fma_f32 v52,v54,v52,v254\n" \
"v_fma_f32 v53,v55,v53,v255\nv_mfma_f32_16x16x32_bf16 v[220:223],a[76:79],v[88:91],v[220:223]\nglobal_load_dwordx4 v[196:199],v59,s[16:17] sc1\nv_cvt_pk_bf16_f32 v52,v52,v53\n" \
"v_mfma_f32_16x16x32_bf16 v[220:223],a[108:111],v[92:95],v[220:223]\nbuffer_store_dword v52,v57,s[44:47],0 offen sc1\nv_mfma_f32_16x16x32_bf16 v[224:227],a[80:83],v[88:91],v[224:227]\ns_waitcnt lgkmcnt(0)\n" \
"s_barrier\nv_mfma_f32_16x16x32_bf16 v[224:227],a[112:115],v[92:95],v[224:227]\nds_read_b128 v[32:35],v58 offset:32768\nv_mfma_f32_16x16x32_bf16 v[228:231],a[84:87],v[88:91],v[228:231]\n" \
"ds_read_b128 v[36:39],v58 offset:40960\nv_mfma_f32_16x16x32_bf16 v[228:231],a[116:119],v[92:95],v[228:231]\nds_read_b128 v[40:43],v58 offset:49152\nv_mfma_f32_16x16x32_bf16 v[232:235],a[88:91],v[88:91],v[232:235]\n" \
"global_load_dwordx4 v[200:203],v59,s[16:17] offset:1024 sc1\nds_read_b128 v[44:47],v58 offset:57344\nv_mfma_f32_16x16x32_bf16 v[232:235],a[120:123],v[92:95],v[232:235]\nds_read_b128 v[48:51],v58 offset:36864\n" \
"v_mfma_f32_16x16x32_bf16 v[236:239],a[92:95],v[88:91],v[236:239]\nds_read_b128 v[240:243],v58 offset:45056\nv_mfma_f32_16x16x32_bf16 v[236:239],a[124:127],v[92:95],v[236:239]\nds_read_b128 v[244:247],v58 offset:53248\n" \
"s_waitcnt vmcnt(11)\nv_mfma_f32_16x16x32_bf16 v[208:211],a[128:131],v[96:99],v[208:211]\nds_read_b128 v[248:251],v58 offset:61440\nv_mfma_f32_16x16x32_bf16 v[208:211],a[160:163],v[100:103],v[208:211]\n" \
"v_mfma_f32_16x16x32_bf16 v[212:215],a[132:135],v[96:99],v[212:215]\nglobal_load_dwordx4 v[204:207],v59,s[16:17] offset:2048 sc1\nv_mfma_f32_16x16x32_bf16 v[212:215],a[164:167],v[100:103],v[212:215]\nv_mfma_f32_16x16x32_bf16 v[216:219],a[136:139],v[96:99],v[216:219]\n" \
"s_waitcnt lgkmcnt(4)\nv_add_f32 v32,v32,v36\nv_mfma_f32_16x16x32_bf16 v[216:219],a[168:171],v[100:103],v[216:219]\nv_add_f32 v33,v33,v37\n" \
"v_add_f32 v34,v34,v38\nv_mfma_f32_16x16x32_bf16 v[220:223],a[140:143],v[96:99],v[220:223]\nv_add_f32 v35,v35,v39\nv_add_f32 v40,v40,v44\n" \
"v_mfma_f32_16x16x32_bf16 v[220:223],a[172:175],v[100:103],v[220:223]\nv_add_f32 v41,v41,v45\nv_add_f32 v42,v42,v46\nv_mfma_f32_16x16x32_bf16 v[224:227],a[144:147],v[96:99],v[224:227]\n" \
"v_add_f32 v43,v43,v47\nv_add_f32 v32,v32,v40\nv_mfma_f32_16x16x32_bf16 v[224:227],a[176:179],v[100:103],v[224:227]\nv_add_f32 v33,v33,v41\n" \
"v_add_f32 v34,v34,v42\nv_mfma_f32_16x16x32_bf16 v[228:231],a[148:151],v[96:99],v[228:231]\nv_add_f32 v35,v35,v43\nv_cvt_pk_bf16_f32 v36,v32,v33\n" \
"v_mfma_f32_16x16x32_bf16 v[228:231],a[180:183],v[100:103],v[228:231]\nv_cvt_pk_bf16_f32 v37,v34,v35\ns_waitcnt lgkmcnt(0)\nv_mfma_f32_16x16x32_bf16 v[232:235],a[152:155],v[96:99],v[232:235]\n" \
"v_add_f32 v48,v48,v240\nv_add_f32 v49,v49,v241\nv_mfma_f32_16x16x32_bf16 v[232:235],a[184:187],v[100:103],v[232:235]\nv_add_f32 v50,v50,v242\n" \
"v_add_f32 v51,v51,v243\nv_mfma_f32_16x16x32_bf16 v[236:239],a[156:159],v[96:99],v[236:239]\nv_add_f32 v244,v244,v248\nv_add_f32 v245,v245,v249\n" \
"v_mfma_f32_16x16x32_bf16 v[236:239],a[188:191],v[100:103],v[236:239]\nv_add_f32 v246,v246,v250\ns_waitcnt vmcnt(11)\nv_mfma_f32_16x16x32_bf16 v[208:211],a[192:195],v[104:107],v[208:211]\n" \
"v_add_f32 v247,v247,v251\nv_add_f32 v48,v48,v244\nv_mfma_f32_16x16x32_bf16 v[212:215],a[196:199],v[104:107],v[212:215]\nv_add_f32 v49,v49,v245\n" \
"v_add_f32 v50,v50,v246\nv_mfma_f32_16x16x32_bf16 v[216:219],a[200:203],v[104:107],v[216:219]\nv_add_f32 v51,v51,v247\nv_cvt_pk_bf16_f32 v38,v48,v49\n" \
"v_mfma_f32_16x16x32_bf16 v[220:223],a[204:207],v[104:107],v[220:223]\nv_cvt_pk_bf16_f32 v39,v50,v51\nbuffer_store_dwordx4 v[36:39],v59,s[36:39],s74 offen sc1\nv_mfma_f32_16x16x32_bf16 v[224:227],a[208:211],v[104:107],v[224:227]\n" \
"v_mfma_f32_16x16x32_bf16 v[228:231],a[212:215],v[104:107],v[228:231]\nv_mfma_f32_16x16x32_bf16 v[232:235],a[216:219],v[104:107],v[232:235]\nv_mfma_f32_16x16x32_bf16 v[236:239],a[220:223],v[104:107],v[236:239]\ns_waitcnt vmcnt(9)\n" \
"v_mfma_f32_16x16x32_bf16 v[0:3],v[128:131],v[108:111],0\nv_add_u32 v59,409600,v59\nv_add_u32 v60,s98,v58\nv_mfma_f32_16x16x32_bf16 v[0:3],v[160:163],v[112:115],v[0:3]\n" \
"ds_write_addtid_b32 v208\nv_mfma_f32_16x16x32_bf16 v[4:7],v[132:135],v[108:111],0\nglobal_load_dwordx4 v[72:75],v59,s[12:13] sc1\nds_write_addtid_b32 v209 offset:256\n" \
"v_mfma_f32_16x16x32_bf16 v[4:7],v[164:167],v[112:115],v[4:7]\nds_write_addtid_b32 v210 offset:512\nv_mfma_f32_16x16x32_bf16 v[8:11],v[136:139],v[108:111],0\nds_write_addtid_b32 v211 offset:768\n" \
"v_mfma_f32_16x16x32_bf16 v[8:11],v[168:171],v[112:115],v[8:11]\nbuffer_load_dwordx4 v[64:67],v59,s[36:39],s71 offen sc1\nds_write_addtid_b32 v212 offset:1024\nv_mfma_f32_16x16x32_bf16 v[12:15],v[140:143],v[108:111],0\n" \
"ds_write_addtid_b32 v213 offset:1280\nv_mfma_f32_16x16x32_bf16 v[12:15],v[172:175],v[112:115],v[12:15]\nds_write_addtid_b32 v214 offset:1536\nv_mfma_f32_16x16x32_bf16 v[16:19],v[144:147],v[108:111],0\n" \
"global_load_dwordx4 v[76:79],v59,s[12:13] offset:1024 sc1\nds_write_addtid_b32 v215 offset:1792\nv_mfma_f32_16x16x32_bf16 v[16:19],v[176:179],v[112:115],v[16:19]\nds_write_addtid_b32 v216 offset:2048\n" \
"v_mfma_f32_16x16x32_bf16 v[20:23],v[148:151],v[108:111],0\nds_write_addtid_b32 v217 offset:2304\nv_mfma_f32_16x16x32_bf16 v[20:23],v[180:183],v[112:115],v[20:23]\nds_write_addtid_b32 v218 offset:2560\n" \
"v_mfma_f32_16x16x32_bf16 v[24:27],v[152:155],v[108:111],0\nds_write_addtid_b32 v219 offset:2816\nv_mfma_f32_16x16x32_bf16 v[24:27],v[184:187],v[112:115],v[24:27]\nds_write_addtid_b32 v220 offset:3072\n" \
"v_mfma_f32_16x16x32_bf16 v[28:31],v[156:159],v[108:111],0\nglobal_load_dwordx4 v[80:83],v59,s[12:13] offset:2048 sc1\nds_write_addtid_b32 v221 offset:3328\nv_mfma_f32_16x16x32_bf16 v[28:31],v[188:191],v[112:115],v[28:31]\n" \
"ds_write_addtid_b32 v222 offset:3584\ns_waitcnt vmcnt(11)\nv_mfma_f32_16x16x32_bf16 v[0:3],a[0:3],v[116:119],v[0:3]\nds_write_addtid_b32 v223 offset:3840\n" \
"v_mfma_f32_16x16x32_bf16 v[0:3],a[32:35],v[120:123],v[0:3]\nbuffer_load_dwordx4 v[52:55],v59,s[36:39],s76 offen sc1\nds_write_addtid_b32 v224 offset:4096\nv_mfma_f32_16x16x32_bf16 v[4:7],a[4:7],v[116:119],v[4:7]\n" \
"ds_write_addtid_b32 v225 offset:4352\nv_mfma_f32_16x16x32_bf16 v[4:7],a[36:39],v[120:123],v[4:7]\nds_write_addtid_b32 v226 offset:4608\nv_mfma_f32_16x16x32_bf16 v[8:11],a[8:11],v[116:119],v[8:11]\n" \
"global_load_dwordx4 v[84:87],v59,s[14:15] sc1\nds_write_addtid_b32 v227 offset:4864\nv_mfma_f32_16x16x32_bf16 v[8:11],a[40:43],v[120:123],v[8:11]\nds_write_addtid_b32 v228 offset:5120\n" \
"v_mfma_f32_16x16x32_bf16 v[12:15],a[12:15],v[116:119],v[12:15]\nds_write_addtid_b32 v229 offset:5376\nv_mfma_f32_16x16x32_bf16 v[12:15],a[44:47],v[120:123],v[12:15]\nds_write_addtid_b32 v230 offset:5632\n" \
"v_mfma_f32_16x16x32_bf16 v[16:19],a[16:19],v[116:119],v[16:19]\nds_write_addtid_b32 v231 offset:5888\nv_mfma_f32_16x16x32_bf16 v[16:19],a[48:51],v[120:123],v[16:19]\nds_write_addtid_b32 v232 offset:6144\n" \
"v_mfma_f32_16x16x32_bf16 v[20:23],a[20:23],v[116:119],v[20:23]\nglobal_load_dwordx4 v[88:91],v59,s[14:15] offset:1024 sc1\nds_write_addtid_b32 v233 offset:6400\nv_mfma_f32_16x16x32_bf16 v[20:23],a[52:55],v[120:123],v[20:23]\n" \
"ds_write_addtid_b32 v234 offset:6656\nv_mfma_f32_16x16x32_bf16 v[24:27],a[24:27],v[116:119],v[24:27]\nds_write_addtid_b32 v235 offset:6912\nv_mfma_f32_16x16x32_bf16 v[24:27],a[56:59],v[120:123],v[24:27]\n" \
"ds_write_addtid_b32 v236 offset:7168\nv_mfma_f32_16x16x32_bf16 v[28:31],a[28:31],v[116:119],v[28:31]\nds_write_addtid_b32 v237 offset:7424\nv_mfma_f32_16x16x32_bf16 v[28:31],a[60:63],v[120:123],v[28:31]\n" \
"ds_write_addtid_b32 v238 offset:7680\ns_waitcnt vmcnt(12)\nv_mfma_f32_16x16x32_bf16 v[0:3],a[64:67],v[124:127],v[0:3]\nglobal_load_dwordx4 v[92:95],v59,s[14:15] offset:2048 sc1\n" \
"ds_write_addtid_b32 v239 offset:7936\nv_mfma_f32_16x16x32_bf16 v[0:3],a[96:99],v[192:195],v[0:3]\nds_read_b32 v61,v60 offset:28676\nv_mfma_f32_16x16x32_bf16 v[4:7],a[68:71],v[124:127],v[4:7]\n" \
"v_mfma_f32_16x16x32_bf16 v[4:7],a[100:103],v[192:195],v[4:7]\nv_mfma_f32_16x16x32_bf16 v[8:11],a[72:75],v[124:127],v[8:11]\nv_mfma_f32_16x16x32_bf16 v[8:11],a[104:107],v[192:195],v[8:11]\nv_mfma_f32_16x16x32_bf16 v[12:15],a[76:79],v[124:127],v[12:15]\n" \
"global_load_dwordx4 v[96:99],v59,s[16:17] sc1\nv_mfma_f32_16x16x32_bf16 v[12:15],a[108:111],v[192:195],v[12:15]\ns_waitcnt lgkmcnt(0)\ns_barrier\n" \
"v_mfma_f32_16x16x32_bf16 v[16:19],a[80:83],v[124:127],v[16:19]\nds_read_b128 v[32:35],v58\nv_mfma_f32_16x16x32_bf16 v[16:19],a[112:115],v[192:195],v[16:19]\nds_read_b128 v[36:39],v58 offset:8192\n" \
"v_mfma_f32_16x16x32_bf16 v[20:23],a[84:87],v[124:127],v[20:23]\nds_read_b128 v[40:43],v58 offset:16384\nv_lshlrev_b32 v60,16,v61\nv_mfma_f32_16x16x32_bf16 v[20:23],a[116:119],v[192:195],v[20:23]\n" \
"ds_read_b128 v[44:47],v58 offset:24576\nv_and_b32 v61,0xffff0000,v61\nv_mfma_f32_16x16x32_bf16 v[24:27],a[88:91],v[124:127],v[24:27]\nglobal_load_dwordx4 v[100:103],v59,s[16:17] offset:1024 sc1\n" \
"ds_read_b128 v[48:51],v58 offset:4096\nv_mfma_f32_16x16x32_bf16 v[24:27],a[120:123],v[192:195],v[24:27]\nds_read_b128 v[240:243],v58 offset:12288\nv_mfma_f32_16x16x32_bf16 v[28:31],a[92:95],v[124:127],v[28:31]\n" \
"ds_read_b128 v[244:247],v58 offset:20480\nv_mfma_f32_16x16x32_bf16 v[28:31],a[124:127],v[192:195],v[28:31]\nds_read_b128 v[248:251],v58 offset:28672\ns_waitcnt vmcnt(12)\n" \
"v_mfma_f32_16x16x32_bf16 v[0:3],a[128:131],v[196:199],v[0:3]\nv_mfma_f32_16x16x32_bf16 v[0:3],a[160:163],v[200:203],v[0:3]\nv_mfma_f32_16x16x32_bf16 v[4:7],a[132:135],v[196:199],v[4:7]\nglobal_load_dwordx4 v[104:107],v59,s[16:17] offset:2048 sc1\n" \
"v_mfma_f32_16x16x32_bf16 v[4:7],a[164:167],v[200:203],v[4:7]\ns_waitcnt lgkmcnt(4)\nv_add_f32 v32,v32,v36\nv_mfma_f32_16x16x32_bf16 v[8:11],a[136:139],v[196:199],v[8:11]\n" \
"v_add_f32 v33,v33,v37\nv_add_f32 v34,v34,v38\nv_mfma_f32_16x16x32_bf16 v[8:11],a[168:171],v[200:203],v[8:11]\nv_add_f32 v35,v35,v39\n" \
"v_add_f32 v40,v40,v44\nv_mfma_f32_16x16x32_bf16 v[12:15],a[140:143],v[196:199],v[12:15]\nv_add_f32 v41,v41,v45\nv_add_f32 v42,v42,v46\n" \
"v_mfma_f32_16x16x32_bf16 v[12:15],a[172:175],v[200:203],v[12:15]\nv_add_f32 v43,v43,v47\nv_add_f32 v32,v32,v40\nv_mfma_f32_16x16x32_bf16 v[16:19],a[144:147],v[196:199],v[16:19]\n" \
"v_add_f32 v33,v33,v41\nv_add_f32 v34,v34,v42\nv_mfma_f32_16x16x32_bf16 v[16:19],a[176:179],v[200:203],v[16:19]\nv_add_f32 v35,v35,v43\n" \
"v_cvt_pk_bf16_f32 v36,v32,v33\nv_mfma_f32_16x16x32_bf16 v[20:23],a[148:151],v[196:199],v[20:23]\nv_cvt_pk_bf16_f32 v37,v34,v35\ns_waitcnt lgkmcnt(0)\n" \
"v_mfma_f32_16x16x32_bf16 v[20:23],a[180:183],v[200:203],v[20:23]\nv_add_f32 v48,v48,v240\nv_add_f32 v49,v49,v241\nv_mfma_f32_16x16x32_bf16 v[24:27],a[152:155],v[196:199],v[24:27]\n" \
"v_add_f32 v50,v50,v242\nv_add_f32 v51,v51,v243\nv_mfma_f32_16x16x32_bf16 v[24:27],a[184:187],v[200:203],v[24:27]\nv_add_f32 v244,v244,v248\n" \
"v_add_f32 v245,v245,v249\nv_mfma_f32_16x16x32_bf16 v[28:31],a[156:159],v[196:199],v[28:31]\nv_add_f32 v246,v246,v250\nv_add_f32 v247,v247,v251\n" \
"v_mfma_f32_16x16x32_bf16 v[28:31],a[188:191],v[200:203],v[28:31]\nv_add_f32 v48,v48,v244\ns_waitcnt vmcnt(12)\nv_mfma_f32_16x16x32_bf16 v[0:3],a[192:195],v[204:207],v[0:3]\n" \
"v_add_f32 v49,v49,v245\nv_add_f32 v50,v50,v246\nv_mfma_f32_16x16x32_bf16 v[4:7],a[196:199],v[204:207],v[4:7]\nv_add_f32 v51,v51,v247\n" \
"v_cvt_pk_bf16_f32 v38,v48,v49\nv_mfma_f32_16x16x32_bf16 v[8:11],a[200:203],v[204:207],v[8:11]\nv_cvt_pk_bf16_f32 v39,v50,v51\nbuffer_store_dwordx4 v[36:39],v59,s[36:39],s74 offen sc1\n" \
"v_mfma_f32_16x16x32_bf16 v[12:15],a[204:207],v[204:207],v[12:15]\nv_mfma_f32_16x16x32_bf16 v[16:19],a[208:211],v[204:207],v[16:19]\nv_mfma_f32_16x16x32_bf16 v[20:23],a[212:215],v[204:207],v[20:23]\nv_mfma_f32_16x16x32_bf16 v[24:27],a[216:219],v[204:207],v[24:27]\n" \
"v_mfma_f32_16x16x32_bf16 v[28:31],a[220:223],v[204:207],v[28:31]\ns_waitcnt vmcnt(9)\nv_mfma_f32_16x16x32_bf16 v[208:211],v[128:131],v[72:75],0\nv_add_u32 v59,409600,v59\n" \
"s_waitcnt vmcnt(7)\nv_mfma_f32_16x16x32_bf16 v[208:211],v[160:163],v[76:79],v[208:211]\nds_write_addtid_b32 v0 offset:32768\nv_max_u32 v62,v64,v52\n" \
"v_mfma_f32_16x16x32_bf16 v[212:215],v[132:135],v[72:75],0\nglobal_load_dwordx4 v[108:111],v59,s[12:13] sc1\nds_write_addtid_b32 v1 offset:33024\nv_mfma_f32_16x16x32_bf16 v[212:215],v[164:167],v[76:79],v[212:215]\n" \
"ds_write_addtid_b32 v2 offset:33280\nv_cmp_eq_u32 vcc,-1,v62\nv_mfma_f32_16x16x32_bf16 v[216:219],v[136:139],v[72:75],0\nds_write_addtid_b32 v3 offset:33536\n" \
"v_mfma_f32_16x16x32_bf16 v[216:219],v[168:171],v[76:79],v[216:219]\nds_write_addtid_b32 v4 offset:33792\ns_cbranch_vccnz .Lsp1_%=\n.Lbp1_%=:\n" \
"v_mfma_f32_16x16x32_bf16 v[220:223],v[140:143],v[72:75],0\nds_write_addtid_b32 v5 offset:34048\nv_and_b32 v71,0xffff0000,v67\nv_mfma_f32_16x16x32_bf16 v[220:223],v[172:175],v[76:79],v[220:223]\n" \
"ds_write_addtid_b32 v6 offset:34304\nv_lshlrev_b32 v70,16,v67\nv_mfma_f32_16x16x32_bf16 v[224:227],v[144:147],v[72:75],0\nglobal_load_dwordx4 v[112:115],v59,s[12:13] offset:1024 sc1\n" \
"ds_write_addtid_b32 v7 offset:34560\nv_mfma_f32_16x16x32_bf16 v[224:227],v[176:179],v[76:79],v[224:227]\nds_write_addtid_b32 v8 offset:34816\nv_and_b32 v69,0xffff0000,v66\n" \
"v_mfma_f32_16x16x32_bf16 v[228:231],v[148:151],v[72:75],0\nds_write_addtid_b32 v9 offset:35072\nv_lshlrev_b32 v68,16,v66\nv_mfma_f32_16x16x32_bf16 v[228:231],v[180:183],v[76:79],v[228:231]\n" \
"ds_write_addtid_b32 v10 offset:35328\nv_and_b32 v67,0xffff0000,v65\nv_mfma_f32_16x16x32_bf16 v[232:235],v[152:155],v[72:75],0\nds_write_addtid_b32 v11 offset:35584\n" \
"v_lshlrev_b32 v66,16,v65\nv_mfma_f32_16x16x32_bf16 v[232:235],v[184:187],v[76:79],v[232:235]\nds_write_addtid_b32 v12 offset:35840\nv_and_b32 v65,0xffff0000,v64\n" \
"v_mfma_f32_16x16x32_bf16 v[236:239],v[156:159],v[72:75],0\nglobal_load_dwordx4 v[116:119],v59,s[12:13] offset:2048 sc1\nds_write_addtid_b32 v13 offset:36096\nv_mfma_f32_16x16x32_bf16 v[236:239],v[188:191],v[76:79],v[236:239]\n" \
"ds_write_addtid_b32 v14 offset:36352\ns_waitcnt vmcnt(9)\nv_mfma_f32_16x16x32_bf16 v[208:211],a[0:3],v[80:83],v[208:211]\nds_write_addtid_b32 v15 offset:36608\n" \
"v_lshlrev_b32 v64,16,v64\nv_mfma_f32_16x16x32_bf16 v[208:211],a[32:35],v[84:87],v[208:211]\nds_write_addtid_b32 v16 offset:36864\nv_add_f32 v64,v64,v60\n" \
"v_mfma_f32_16x16x32_bf16 v[212:215],a[4:7],v[80:83],v[212:215]\nds_write_addtid_b32 v17 offset:37120\nv_add_f32 v65,v65,v60\nv_mfma_f32_16x16x32_bf16 v[212:215],a[36:39],v[84:87],v[212:215]\n" \
"ds_write_addtid_b32 v18 offset:37376\nv_add_f32 v66,v66,v60\nv_mfma_f32_16x16x32_bf16 v[216:219],a[8:11],v[80:83],v[216:219]\nglobal_load_dwordx4 v[120:123],v59,s[14:15] sc1\n" \
"ds_write_addtid_b32 v19 offset:37632\nv_mfma_f32_16x16x32_bf16 v[216:219],a[40:43],v[84:87],v[216:219]\nds_write_addtid_b32 v20 offset:37888\nv_add_f32 v67,v67,v60\n" \
"v_mfma_f32_16x16x32_bf16 v[220:223],a[12:15],v[80:83],v[220:223]\nds_write_addtid_b32 v21 offset:38144\nv_add_f32 v68,v68,v61\nv_mfma_f32_16x16x32_bf16 v[220:223],a[44:47],v[84:87],v[220:223]\n" \
"ds_write_addtid_b32 v22 offset:38400\nv_add_f32 v69,v69,v61\nv_mfma_f32_16x16x32_bf16 v[224:227],a[16:19],v[80:83],v[224:227]\nds_write_addtid_b32 v23 offset:38656\n" \
"v_add_f32 v70,v70,v61\nv_mfma_f32_16x16x32_bf16 v[224:227],a[48:51],v[84:87],v[224:227]\nds_write_addtid_b32 v24 offset:38912\nv_add_f32 v71,v71,v61\n" \
"v_mfma_f32_16x16x32_bf16 v[228:231],a[20:23],v[80:83],v[228:231]\nglobal_load_dwordx4 v[124:127],v59,s[14:15] offset:1024 sc1\nds_write_addtid_b32 v25 offset:39168\nv_mfma_f32_16x16x32_bf16 v[228:231],a[52:55],v[84:87],v[228:231]\n" \
"ds_write_addtid_b32 v26 offset:39424\nv_and_b32 v255,0xffff0000,v55\nv_mfma_f32_16x16x32_bf16 v[232:235],a[24:27],v[80:83],v[232:235]\nds_write_addtid_b32 v27 offset:39680\n" \
"v_lshlrev_b32 v254,16,v55\nv_mfma_f32_16x16x32_bf16 v[232:235],a[56:59],v[84:87],v[232:235]\nds_write_addtid_b32 v28 offset:39936\nv_and_b32 v253,0xffff0000,v54\n" \
"v_mfma_f32_16x16x32_bf16 v[236:239],a[28:31],v[80:83],v[236:239]\nds_write_addtid_b32 v29 offset:40192\nv_lshlrev_b32 v252,16,v54\nv_mfma_f32_16x16x32_bf16 v[236:239],a[60:63],v[84:87],v[236:239]\n" \
"ds_write_addtid_b32 v30 offset:40448\ns_waitcnt vmcnt(9)\nv_mfma_f32_16x16x32_bf16 v[208:211],a[64:67],v[88:91],v[208:211]\nglobal_load_dwordx4 v[192:195],v59,s[14:15] offset:2048 sc1\n" \
"ds_write_addtid_b32 v31 offset:40704\nv_mfma_f32_16x16x32_bf16 v[208:211],a[96:99],v[92:95],v[208:211]\nv_and_b32 v55,0xffff0000,v53\nv_lshlrev_b32 v54,16,v53\n" \
"v_mfma_f32_16x16x32_bf16 v[212:215],a[68:71],v[88:91],v[212:215]\nv_and_b32 v53,0xffff0000,v52\nv_lshlrev_b32 v52,16,v52\nv_mfma_f32_16x16x32_bf16 v[212:215],a[100:103],v[92:95],v[212:215]\n" \
"v_add_f32 v52,v52,v60\nv_add_f32 v53,v53,v60\nv_mfma_f32_16x16x32_bf16 v[216:219],a[72:75],v[88:91],v[216:219]\nv_add_f32 v54,v54,v60\n" \
"v_add_f32 v55,v55,v60\nv_mfma_f32_16x16x32_bf16 v[216:219],a[104:107],v[92:95],v[216:219]\nv_add_f32 v252,v252,v61\nv_add_f32 v253,v253,v61\n" \
"v_mfma_f32_16x16x32_bf16 v[220:223],a[76:79],v[88:91],v[220:223]\nglobal_load_dwordx4 v[196:199],v59,s[16:17] sc1\nv_add_f32 v254,v254,v61\nv_mfma_f32_16x16x32_bf16 v[220:223],a[108:111],v[92:95],v[220:223]\n" \
"s_waitcnt lgkmcnt(0)\ns_barrier\nv_mfma_f32_16x16x32_bf16 v[224:227],a[80:83],v[88:91],v[224:227]\nds_read_b128 v[32:35],v58 offset:32768\n" \
"v_add_f32 v255,v255,v61\nv_mfma_f32_16x16x32_bf16 v[224:227],a[112:115],v[92:95],v[224:227]\nds_read_b128 v[36:39],v58 offset:40960\nv_mfma_f32_16x16x32_bf16 v[228:231],a[84:87],v[88:91],v[228:231]\n" \
"ds_read_b128 v[40:43],v58 offset:49152\nv_mfma_f32_16x16x32_bf16 v[228:231],a[116:119],v[92:95],v[228:231]\nds_read_b128 v[44:47],v58 offset:57344\nv_mfma_f32_16x16x32_bf16 v[232:235],a[88:91],v[88:91],v[232:235]\n" \
"global_load_dwordx4 v[200:203],v59,s[16:17] offset:1024 sc1\nds_read_b128 v[48:51],v58 offset:36864\nv_mfma_f32_16x16x32_bf16 v[232:235],a[120:123],v[92:95],v[232:235]\nds_read_b128 v[240:243],v58 offset:45056\n" \
"v_mfma_f32_16x16x32_bf16 v[236:239],a[92:95],v[88:91],v[236:239]\nds_read_b128 v[244:247],v58 offset:53248\nv_mfma_f32_16x16x32_bf16 v[236:239],a[124:127],v[92:95],v[236:239]\nbuffer_load_dwordx4 v[60:63],v59,s[36:39],s72 offen sc1\n" \
"s_waitcnt vmcnt(11)\nv_mfma_f32_16x16x32_bf16 v[208:211],a[128:131],v[96:99],v[208:211]\nds_read_b128 v[248:251],v58 offset:61440\nv_mfma_f32_16x16x32_bf16 v[208:211],a[160:163],v[100:103],v[208:211]\n" \
"v_mfma_f32_16x16x32_bf16 v[212:215],a[132:135],v[96:99],v[212:215]\nglobal_load_dwordx4 v[204:207],v59,s[16:17] offset:2048 sc1\nv_mfma_f32_16x16x32_bf16 v[212:215],a[164:167],v[100:103],v[212:215]\ns_waitcnt lgkmcnt(4)\n" \
"v_add_f32 v32,v32,v36\nv_mfma_f32_16x16x32_bf16 v[216:219],a[136:139],v[96:99],v[216:219]\nv_add_f32 v33,v33,v37\nv_add_f32 v34,v34,v38\n" \
"v_mfma_f32_16x16x32_bf16 v[216:219],a[168:171],v[100:103],v[216:219]\nv_add_f32 v35,v35,v39\nv_add_f32 v40,v40,v44\nv_mfma_f32_16x16x32_bf16 v[220:223],a[140:143],v[96:99],v[220:223]\n" \
"v_add_f32 v41,v41,v45\nv_add_f32 v42,v42,v46\nv_mfma_f32_16x16x32_bf16 v[220:223],a[172:175],v[100:103],v[220:223]\nv_add_f32 v43,v43,v47\n" \
"v_add_f32 v32,v32,v40\nv_mfma_f32_16x16x32_bf16 v[224:227],a[144:147],v[96:99],v[224:227]\nv_add_f32 v33,v33,v41\nv_add_f32 v34,v34,v42\n" \
"v_mfma_f32_16x16x32_bf16 v[224:227],a[176:179],v[100:103],v[224:227]\nv_add_f32 v35,v35,v43\nv_cvt_pk_bf16_f32 v36,v32,v33\nv_mfma_f32_16x16x32_bf16 v[228:231],a[148:151],v[96:99],v[228:231]\n" \
"v_cvt_pk_bf16_f32 v37,v34,v35\ns_waitcnt lgkmcnt(0)\nv_mfma_f32_16x16x32_bf16 v[228:231],a[180:183],v[100:103],v[228:231]\nv_add_f32 v48,v48,v240\n" \
"v_add_f32 v49,v49,v241\nv_mfma_f32_16x16x32_bf16 v[232:235],a[152:155],v[96:99],v[232:235]\nv_add_f32 v50,v50,v242\nv_add_f32 v51,v51,v243\n" \
"v_mfma_f32_16x16x32_bf16 v[232:235],a[184:187],v[100:103],v[232:235]\nv_add_f32 v244,v244,v248\nv_add_f32 v245,v245,v249\nv_mfma_f32_16x16x32_bf16 v[236:239],a[156:159],v[96:99],v[236:239]\n" \
"v_add_f32 v246,v246,v250\nv_add_f32 v247,v247,v251\nv_mfma_f32_16x16x32_bf16 v[236:239],a[188:191],v[100:103],v[236:239]\nv_add_f32 v48,v48,v244\n" \
"s_waitcnt vmcnt(11)\nv_mfma_f32_16x16x32_bf16 v[208:211],a[192:195],v[104:107],v[208:211]\nv_add_f32 v49,v49,v245\nv_add_f32 v50,v50,v246\n" \
"v_mfma_f32_16x16x32_bf16 v[212:215],a[196:199],v[104:107],v[212:215]\nv_add_f32 v51,v51,v247\nv_cvt_pk_bf16_f32 v38,v48,v49\nv_mfma_f32_16x16x32_bf16 v[216:219],a[200:203],v[104:107],v[216:219]\n" \
"v_cvt_pk_bf16_f32 v39,v50,v51\nbuffer_store_dwordx4 v[36:39],v59,s[36:39],s74 offen sc1\nv_mfma_f32_16x16x32_bf16 v[220:223],a[204:207],v[104:107],v[220:223]\nv_mfma_f32_16x16x32_bf16 v[224:227],a[208:211],v[104:107],v[224:227]\n" \
"v_mfma_f32_16x16x32_bf16 v[228:231],a[212:215],v[104:107],v[228:231]\nv_mfma_f32_16x16x32_bf16 v[232:235],a[216:219],v[104:107],v[232:235]\nv_mfma_f32_16x16x32_bf16 v[236:239],a[220:223],v[104:107],v[236:239]\ns_waitcnt vmcnt(9)\n" \
"v_mfma_f32_16x16x32_bf16 v[0:3],v[128:131],v[108:111],0\nv_add_u32 v59,409600,v59\nv_mfma_f32_16x16x32_bf16 v[0:3],v[160:163],v[112:115],v[0:3]\nds_write_addtid_b32 v208\n" \
"ds_write_addtid_b32 v209 offset:256\nv_mfma_f32_16x16x32_bf16 v[4:7],v[132:135],v[108:111],0\nglobal_load_dwordx4 v[72:75],v59,s[12:13] sc1\nds_write_addtid_b32 v210 offset:512\n" \
"v_mfma_f32_16x16x32_bf16 v[4:7],v[164:167],v[112:115],v[4:7]\nds_write_addtid_b32 v211 offset:768\nds_write_addtid_b32 v212 offset:1024\nv_mfma_f32_16x16x32_bf16 v[8:11],v[136:139],v[108:111],0\n" \
"ds_write_addtid_b32 v213 offset:1280\nds_write_addtid_b32 v214 offset:1536\nv_mfma_f32_16x16x32_bf16 v[8:11],v[168:171],v[112:115],v[8:11]\nds_write_addtid_b32 v215 offset:1792\n" \
"ds_write_addtid_b32 v216 offset:2048\nv_mfma_f32_16x16x32_bf16 v[12:15],v[140:143],v[108:111],0\nds_write_addtid_b32 v217 offset:2304\nds_write_addtid_b32 v218 offset:2560\n" \
"v_mfma_f32_16x16x32_bf16 v[12:15],v[172:175],v[112:115],v[12:15]\nds_write_addtid_b32 v219 offset:2816\nds_write_addtid_b32 v220 offset:3072\nv_mfma_f32_16x16x32_bf16 v[16:19],v[144:147],v[108:111],0\n" \
"global_load_dwordx4 v[76:79],v59,s[12:13] offset:1024 sc1\nds_write_addtid_b32 v221 offset:3328\nv_mfma_f32_16x16x32_bf16 v[16:19],v[176:179],v[112:115],v[16:19]\nds_write_addtid_b32 v222 offset:3584\n" \
"ds_write_addtid_b32 v223 offset:3840\nv_mfma_f32_16x16x32_bf16 v[20:23],v[148:151],v[108:111],0\nds_write_addtid_b32 v224 offset:4096\nds_write_addtid_b32 v225 offset:4352\n" \
"v_mfma_f32_16x16x32_bf16 v[20:23],v[180:183],v[112:115],v[20:23]\nds_write_addtid_b32 v226 offset:4608\nds_write_addtid_b32 v227 offset:4864\nv_mfma_f32_16x16x32_bf16 v[24:27],v[152:155],v[108:111],0\n" \
"ds_write_addtid_b32 v228 offset:5120\nds_write_addtid_b32 v229 offset:5376\nv_mfma_f32_16x16x32_bf16 v[24:27],v[184:187],v[112:115],v[24:27]\nds_write_addtid_b32 v230 offset:5632\n" \
"ds_write_addtid_b32 v231 offset:5888\nv_mfma_f32_16x16x32_bf16 v[28:31],v[156:159],v[108:111],0\nglobal_load_dwordx4 v[80:83],v59,s[12:13] offset:2048 sc1\nds_write_addtid_b32 v232 offset:6144\n" \
"v_mfma_f32_16x16x32_bf16 v[28:31],v[188:191],v[112:115],v[28:31]\nds_write_addtid_b32 v233 offset:6400\ns_waitcnt vmcnt(10)\nv_mfma_f32_16x16x32_bf16 v[0:3],a[0:3],v[116:119],v[0:3]\n" \
"ds_write_addtid_b32 v234 offset:6656\nds_write_addtid_b32 v235 offset:6912\nv_mfma_f32_16x16x32_bf16 v[0:3],a[32:35],v[120:123],v[0:3]\nbuffer_load_dwordx4 v[108:111],v59,s[36:39],s77 offen sc1\n" \
"ds_write_addtid_b32 v236 offset:7168\nv_mfma_f32_16x16x32_bf16 v[4:7],a[4:7],v[116:119],v[4:7]\nds_write_addtid_b32 v237 offset:7424\nds_write_addtid_b32 v238 offset:7680\n" \
"v_mfma_f32_16x16x32_bf16 v[4:7],a[36:39],v[120:123],v[4:7]\nds_write_addtid_b32 v239 offset:7936\nv_mfma_f32_16x16x32_bf16 v[8:11],a[8:11],v[116:119],v[8:11]\nglobal_load_dwordx4 v[84:87],v59,s[14:15] sc1\n" \
"v_mfma_f32_16x16x32_bf16 v[8:11],a[40:43],v[120:123],v[8:11]\nv_mfma_f32_16x16x32_bf16 v[12:15],a[12:15],v[116:119],v[12:15]\nv_mfma_f32_16x16x32_bf16 v[12:15],a[44:47],v[120:123],v[12:15]\nv_mfma_f32_16x16x32_bf16 v[16:19],a[16:19],v[116:119],v[16:19]\n" \
"v_mfma_f32_16x16x32_bf16 v[16:19],a[48:51],v[120:123],v[16:19]\ns_waitcnt lgkmcnt(0)\ns_barrier\nv_mfma_f32_16x16x32_bf16 v[20:23],a[20:23],v[116:119],v[20:23]\n" \
"global_load_dwordx4 v[88:91],v59,s[14:15] offset:1024 sc1\nds_read_b128 v[32:35],v58\nv_mfma_f32_16x16x32_bf16 v[20:23],a[52:55],v[120:123],v[20:23]\nds_read_b128 v[36:39],v58 offset:8192\n" \
"ds_read_b128 v[40:43],v58 offset:16384\nv_mfma_f32_16x16x32_bf16 v[24:27],a[24:27],v[116:119],v[24:27]\nds_read_b128 v[44:47],v58 offset:24576\nds_read_b128 v[48:51],v58 offset:4096\n" \
"v_mfma_f32_16x16x32_bf16 v[24:27],a[56:59],v[120:123],v[24:27]\nds_read_b128 v[240:243],v58 offset:12288\nds_read_b128 v[244:247],v58 offset:20480\nv_mfma_f32_16x16x32_bf16 v[28:31],a[28:31],v[116:119],v[28:31]\n" \
"ds_read_b128 v[248:251],v58 offset:28672\ns_waitcnt vmcnt(8)\nv_mfma_f32_16x16x32_bf16 v[28:31],a[60:63],v[120:123],v[28:31]\nv_cmp_eq_u32 vcc,-1,v60\n" \
"v_mfma_f32_16x16x32_bf16 v[0:3],a[64:67],v[124:127],v[0:3]\nglobal_load_dwordx4 v[92:95],v59,s[14:15] offset:2048 sc1\nv_mfma_f32_16x16x32_bf16 v[0:3],a[96:99],v[192:195],v[0:3]\nbuffer_load_dwordx2 v[116:117],v56,s[36:39],s90 offen sc1\n" \
"s_cbranch_vccnz .Lsp2A_%=\n.Lbp2A_%=:\nv_mfma_f32_16x16x32_bf16 v[4:7],a[68:71],v[124:127],v[4:7]\nbuffer_load_dwordx2 v[118:119],v56,s[36:39],s90 offen offset:32 sc1\n" \
"v_lshlrev_b32 v112,16,v60\nv_mfma_f32_16x16x32_bf16 v[4:7],a[100:103],v[192:195],v[4:7]\nv_and_b32 v113,0xffff0000,v60\nv_add_f32 v64,v64,v112\n" \
"v_mfma_f32_16x16x32_bf16 v[8:11],a[72:75],v[124:127],v[8:11]\ns_waitcnt lgkmcnt(4)\nv_add_f32 v32,v32,v36\nv_mfma_f32_16x16x32_bf16 v[8:11],a[104:107],v[192:195],v[8:11]\n" \
"v_add_f32 v33,v33,v37\nv_add_f32 v34,v34,v38\nv_mfma_f32_16x16x32_bf16 v[12:15],a[76:79],v[124:127],v[12:15]\nglobal_load_dwordx4 v[96:99],v59,s[16:17] sc1\n" \
"v_add_f32 v35,v35,v39\nv_mfma_f32_16x16x32_bf16 v[12:15],a[108:111],v[192:195],v[12:15]\nbuffer_load_dwordx2 v[120:121],v56,s[36:39],s100 offen sc1\nv_add_f32 v40,v40,v44\n" \
"v_mfma_f32_16x16x32_bf16 v[16:19],a[80:83],v[124:127],v[16:19]\nbuffer_load_dwordx2 v[122:123],v56,s[36:39],s100 offen offset:32 sc1\nv_add_f32 v41,v41,v45\nv_mfma_f32_16x16x32_bf16 v[16:19],a[112:115],v[192:195],v[16:19]\n" \
"v_add_f32 v42,v42,v46\nv_add_f32 v43,v43,v47\nv_mfma_f32_16x16x32_bf16 v[20:23],a[84:87],v[124:127],v[20:23]\nv_add_f32 v32,v32,v40\n" \
"v_add_f32 v33,v33,v41\nv_mfma_f32_16x16x32_bf16 v[20:23],a[116:119],v[192:195],v[20:23]\nv_add_f32 v34,v34,v42\nv_add_f32 v35,v35,v43\n" \
"v_mfma_f32_16x16x32_bf16 v[24:27],a[88:91],v[124:127],v[24:27]\nglobal_load_dwordx4 v[100:103],v59,s[16:17] offset:1024 sc1\nv_cvt_pk_bf16_f32 v36,v32,v33\nv_mfma_f32_16x16x32_bf16 v[24:27],a[120:123],v[192:195],v[24:27]\n" \
"v_cvt_pk_bf16_f32 v37,v34,v35\ns_waitcnt lgkmcnt(0)\nv_mfma_f32_16x16x32_bf16 v[28:31],a[92:95],v[124:127],v[28:31]\nv_add_f32 v48,v48,v240\n" \
"v_add_f32 v49,v49,v241\nv_mfma_f32_16x16x32_bf16 v[28:31],a[124:127],v[192:195],v[28:31]\nv_add_f32 v50,v50,v242\nv_mfma_f32_16x16x32_bf16 v[0:3],a[128:131],v[196:199],v[0:3]\n" \
"v_add_f32 v65,v65,v113\nv_add_f32 v51,v51,v243\nv_mfma_f32_16x16x32_bf16 v[0:3],a[160:163],v[200:203],v[0:3]\nv_lshlrev_b32 v112,16,v61\n" \
"v_add_f32 v244,v244,v248\nv_mfma_f32_16x16x32_bf16 v[4:7],a[132:135],v[196:199],v[4:7]\nglobal_load_dwordx4 v[104:107],v59,s[16:17] offset:2048 sc1\nv_and_b32 v113,0xffff0000,v61\n" \
"v_mfma_f32_16x16x32_bf16 v[4:7],a[164:167],v[200:203],v[4:7]\nv_add_f32 v245,v245,v249\nv_add_f32 v66,v66,v112\nv_mfma_f32_16x16x32_bf16 v[8:11],a[136:139],v[196:199],v[8:11]\n" \
"v_add_f32 v246,v246,v250\nv_add_f32 v67,v67,v113\nv_mfma_f32_16x16x32_bf16 v[8:11],a[168:171],v[200:203],v[8:11]\nv_add_f32 v247,v247,v251\n" \
"v_lshlrev_b32 v112,16,v62\nv_mfma_f32_16x16x32_bf16 v[12:15],a[140:143],v[196:199],v[12:15]\nv_add_f32 v48,v48,v244\nv_and_b32 v113,0xffff0000,v62\n" \
"v_mfma_f32_16x16x32_bf16 v[12:15],a[172:175],v[200:203],v[12:15]\nv_add_f32 v49,v49,v245\nv_add_f32 v68,v68,v112\nv_mfma_f32_16x16x32_bf16 v[16:19],a[144:147],v[196:199],v[16:19]\n" \
"v_add_f32 v50,v50,v246\nv_add_f32 v69,v69,v113\nv_mfma_f32_16x16x32_bf16 v[16:19],a[176:179],v[200:203],v[16:19]\nv_add_f32 v51,v51,v247\n" \
"v_lshlrev_b32 v112,16,v63\nv_mfma_f32_16x16x32_bf16 v[20:23],a[148:151],v[196:199],v[20:23]\nv_cvt_pk_bf16_f32 v38,v48,v49\nv_and_b32 v113,0xffff0000,v63\n" \
"v_mfma_f32_16x16x32_bf16 v[20:23],a[180:183],v[200:203],v[20:23]\nv_cvt_pk_bf16_f32 v39,v50,v51\nv_add_f32 v70,v70,v112\nv_mfma_f32_16x16x32_bf16 v[24:27],a[152:155],v[196:199],v[24:27]\n" \
"buffer_store_dwordx4 v[36:39],v59,s[36:39],s74 offen sc1\nv_add_f32 v71,v71,v113\nv_mfma_f32_16x16x32_bf16 v[24:27],a[184:187],v[200:203],v[24:27]\nv_mfma_f32_16x16x32_bf16 v[28:31],a[156:159],v[196:199],v[28:31]\n" \
"v_mfma_f32_16x16x32_bf16 v[28:31],a[188:191],v[200:203],v[28:31]\ns_waitcnt vmcnt(16)\nv_mfma_f32_16x16x32_bf16 v[0:3],a[192:195],v[204:207],v[0:3]\nv_mfma_f32_16x16x32_bf16 v[4:7],a[196:199],v[204:207],v[4:7]\n" \
"v_mfma_f32_16x16x32_bf16 v[8:11],a[200:203],v[204:207],v[8:11]\nv_mfma_f32_16x16x32_bf16 v[12:15],a[204:207],v[204:207],v[12:15]\nv_mfma_f32_16x16x32_bf16 v[16:19],a[208:211],v[204:207],v[16:19]\nv_mfma_f32_16x16x32_bf16 v[20:23],a[212:215],v[204:207],v[20:23]\n" \
"v_mfma_f32_16x16x32_bf16 v[24:27],a[216:219],v[204:207],v[24:27]\nv_mfma_f32_16x16x32_bf16 v[28:31],a[220:223],v[204:207],v[28:31]\ns_waitcnt vmcnt(13)\nv_mfma_f32_16x16x32_bf16 v[208:211],v[128:131],v[72:75],0\n" \
"v_add_u32 v59,409600,v59\ns_waitcnt vmcnt(11)\nv_mfma_f32_16x16x32_bf16 v[208:211],v[160:163],v[76:79],v[208:211]\nds_write_addtid_b32 v0 offset:32768\n" \
"ds_write_addtid_b32 v1 offset:33024\nv_mfma_f32_16x16x32_bf16 v[212:215],v[132:135],v[72:75],0\nds_write_addtid_b32 v2 offset:33280\nds_write_addtid_b32 v3 offset:33536\n" \
"v_mfma_f32_16x16x32_bf16 v[212:215],v[164:167],v[76:79],v[212:215]\nbuffer_load_dwordx4 v[112:115],v59,s[36:39],s73 offen sc1\nds_write_addtid_b32 v4 offset:33792\nv_mfma_f32_16x16x32_bf16 v[216:219],v[136:139],v[72:75],0\n" \
"ds_write_addtid_b32 v5 offset:34048\nds_write_addtid_b32 v6 offset:34304\nv_mfma_f32_16x16x32_bf16 v[216:219],v[168:171],v[76:79],v[216:219]\nds_write_addtid_b32 v7 offset:34560\n" \
"ds_write_addtid_b32 v8 offset:34816\nv_mfma_f32_16x16x32_bf16 v[220:223],v[140:143],v[72:75],0\nds_write_addtid_b32 v9 offset:35072\nds_write_addtid_b32 v10 offset:35328\n" \
"v_mfma_f32_16x16x32_bf16 v[220:223],v[172:175],v[76:79],v[220:223]\nds_write_addtid_b32 v11 offset:35584\ns_waitcnt vmcnt(11)\nv_mfma_f32_16x16x32_bf16 v[208:211],a[0:3],v[80:83],v[208:211]\n" \
"ds_write_addtid_b32 v12 offset:35840\nds_write_addtid_b32 v13 offset:36096\nv_mfma_f32_16x16x32_bf16 v[208:211],a[32:35],v[84:87],v[208:211]\nds_write_addtid_b32 v14 offset:36352\n" \
"ds_write_addtid_b32 v15 offset:36608\nv_mfma_f32_16x16x32_bf16 v[212:215],a[4:7],v[80:83],v[212:215]\nds_write_addtid_b32 v16 offset:36864\nds_write_addtid_b32 v17 offset:37120\n" \
"v_mfma_f32_16x16x32_bf16 v[212:215],a[36:39],v[84:87],v[212:215]\nds_write_addtid_b32 v18 offset:37376\nds_write_addtid_b32 v19 offset:37632\nv_mfma_f32_16x16x32_bf16 v[216:219],a[8:11],v[80:83],v[216:219]\n" \
"ds_write_addtid_b32 v20 offset:37888\nds_write_addtid_b32 v21 offset:38144\nv_mfma_f32_16x16x32_bf16 v[216:219],a[40:43],v[84:87],v[216:219]\nds_write_addtid_b32 v22 offset:38400\n" \
"ds_write_addtid_b32 v23 offset:38656\nv_mfma_f32_16x16x32_bf16 v[220:223],a[12:15],v[80:83],v[220:223]\nds_write_addtid_b32 v24 offset:38912\nds_write_addtid_b32 v25 offset:39168\n" \
"v_mfma_f32_16x16x32_bf16 v[220:223],a[44:47],v[84:87],v[220:223]\nds_write_addtid_b32 v26 offset:39424\ns_waitcnt vmcnt(9)\nv_mfma_f32_16x16x32_bf16 v[208:211],a[64:67],v[88:91],v[208:211]\n" \
"ds_write_addtid_b32 v27 offset:39680\nds_write_addtid_b32 v28 offset:39936\nv_mfma_f32_16x16x32_bf16 v[208:211],a[96:99],v[92:95],v[208:211]\nds_write_addtid_b32 v29 offset:40192\n" \
"ds_write_addtid_b32 v30 offset:40448\nv_mfma_f32_16x16x32_bf16 v[212:215],a[68:71],v[88:91],v[212:215]\nds_write_addtid_b32 v31 offset:40704\nv_cmp_eq_u32 vcc,-1,v108\n" \
"v_mfma_f32_16x16x32_bf16 v[212:215],a[100:103],v[92:95],v[212:215]\nv_and_b32 v62,0x30,v59\nv_add_u32 v62,s67,v62\nv_mfma_f32_16x16x32_bf16 v[216:219],a[72:75],v[88:91],v[216:219]\n" \
"s_cbranch_vccnz .Lsp2B_%=\n.Lbp2B_%=:\nv_lshlrev_b32 v60,16,v108\nv_mfma_f32_16x16x32_bf16 v[216:219],a[104:107],v[92:95],v[216:219]\n" \
"v_and_b32 v61,0xffff0000,v108\nv_add_f32 v52,v52,v60\nv_mfma_f32_16x16x32_bf16 v[220:223],a[76:79],v[88:91],v[220:223]\nv_add_f32 v53,v53,v61\n" \
"v_lshlrev_b32 v60,16,v109\nv_mfma_f32_16x16x32_bf16 v[220:223],a[108:111],v[92:95],v[220:223]\nv_and_b32 v61,0xffff0000,v109\ns_waitcnt vmcnt(3)\n" \
"v_mfma_f32_16x16x32_bf16 v[208:211],a[128:131],v[96:99],v[208:211]\ns_waitcnt lgkmcnt(0)\ns_barrier\nv_mfma_f32_16x16x32_bf16 v[208:211],a[160:163],v[100:103],v[208:211]\n" \
"ds_read_b128 v[32:35],v58 offset:32768\nds_read_b128 v[36:39],v58 offset:40960\nv_mfma_f32_16x16x32_bf16 v[212:215],a[132:135],v[96:99],v[212:215]\nds_read_b128 v[40:43],v58 offset:49152\n" \
"ds_read_b128 v[44:47],v58 offset:57344\nv_mfma_f32_16x16x32_bf16 v[212:215],a[164:167],v[100:103],v[212:215]\nds_read_b128 v[48:51],v58 offset:36864\nds_read_b128 v[240:243],v58 offset:45056\n" \
"v_mfma_f32_16x16x32_bf16 v[216:219],a[136:139],v[96:99],v[216:219]\nds_read_b128 v[244:247],v58 offset:53248\nds_read_b128 v[248:251],v58 offset:61440\nv_mfma_f32_16x16x32_bf16 v[216:219],a[168:171],v[100:103],v[216:219]\n" \
"v_add_f32 v54,v54,v60\nv_add_f32 v55,v55,v61\nv_mfma_f32_16x16x32_bf16 v[220:223],a[140:143],v[96:99],v[220:223]\nv_lshlrev_b32 v60,16,v110\n" \
"v_and_b32 v61,0xffff0000,v110\nv_mfma_f32_16x16x32_bf16 v[220:223],a[172:175],v[100:103],v[220:223]\nv_add_f32 v252,v252,v60\ns_waitcnt vmcnt(2)\n" \
"v_mfma_f32_16x16x32_bf16 v[208:211],a[192:195],v[104:107],v[208:211]\nv_add_f32 v253,v253,v61\nv_lshlrev_b32 v60,16,v111\nv_mfma_f32_16x16x32_bf16 v[212:215],a[196:199],v[104:107],v[212:215]\n" \
"v_and_b32 v61,0xffff0000,v111\nv_add_f32 v254,v254,v60\nv_mfma_f32_16x16x32_bf16 v[216:219],a[200:203],v[104:107],v[216:219]\ns_waitcnt lgkmcnt(4)\n" \
"v_add_f32 v32,v32,v36\nv_mfma_f32_16x16x32_bf16 v[220:223],a[204:207],v[104:107],v[220:223]\nv_add_f32 v33,v33,v37\nv_add_f32 v34,v34,v38\n" \
"v_mfma_f32_16x16x32_bf16 v[224:227],v[144:147],v[72:75],0\nv_add_f32 v35,v35,v39\nv_add_f32 v40,v40,v44\nv_mfma_f32_16x16x32_bf16 v[224:227],v[176:179],v[76:79],v[224:227]\n" \
"v_add_f32 v41,v41,v45\nv_add_f32 v42,v42,v46\nv_mfma_f32_16x16x32_bf16 v[228:231],v[148:151],v[72:75],0\nds_write_addtid_b32 v208\n" \
"ds_write_addtid_b32 v209 offset:256\nv_mfma_f32_16x16x32_bf16 v[228:231],v[180:183],v[76:79],v[228:231]\nds_write_addtid_b32 v210 offset:512\nds_write_addtid_b32 v211 offset:768\n" \
"v_mfma_f32_16x16x32_bf16 v[232:235],v[152:155],v[72:75],0\nds_write_addtid_b32 v212 offset:1024\nds_write_addtid_b32 v213 offset:1280\nv_mfma_f32_16x16x32_bf16 v[232:235],v[184:187],v[76:79],v[232:235]\n" \
"ds_write_addtid_b32 v214 offset:1536\nds_write_addtid_b32 v215 offset:1792\nv_mfma_f32_16x16x32_bf16 v[236:239],v[156:159],v[72:75],0\nds_write_addtid_b32 v216 offset:2048\n" \
"ds_write_addtid_b32 v217 offset:2304\nv_mfma_f32_16x16x32_bf16 v[236:239],v[188:191],v[76:79],v[236:239]\nds_write_addtid_b32 v218 offset:2560\nds_write_addtid_b32 v219 offset:2816\n" \
"v_mfma_f32_16x16x32_bf16 v[224:227],a[16:19],v[80:83],v[224:227]\nds_write_addtid_b32 v220 offset:3072\nds_write_addtid_b32 v221 offset:3328\nv_mfma_f32_16x16x32_bf16 v[224:227],a[48:51],v[84:87],v[224:227]\n" \
"ds_write_addtid_b32 v222 offset:3584\nds_write_addtid_b32 v223 offset:3840\nv_mfma_f32_16x16x32_bf16 v[228:231],a[20:23],v[80:83],v[228:231]\nv_add_f32 v43,v43,v47\n" \
"v_add_f32 v32,v32,v40\nv_mfma_f32_16x16x32_bf16 v[228:231],a[52:55],v[84:87],v[228:231]\nv_add_f32 v33,v33,v41\nv_add_f32 v34,v34,v42\n" \
"v_mfma_f32_16x16x32_bf16 v[232:235],a[24:27],v[80:83],v[232:235]\nv_add_f32 v255,v255,v61\nv_add_f32 v35,v35,v43\nv_mfma_f32_16x16x32_bf16 v[232:235],a[56:59],v[84:87],v[232:235]\n" \
"s_waitcnt vmcnt(0)\nv_mfma_f32_16x16x32_bf16 v[236:239],a[28:31],v[80:83],v[236:239]\nbuffer_load_dwordx4 v[124:127],v59,s[36:39],s78 offen sc1\nv_cmp_eq_u32 vcc,-1,v112\n" \
"v_mfma_f32_16x16x32_bf16 v[236:239],a[60:63],v[84:87],v[236:239]\ns_waitcnt lgkmcnt(0)\ns_barrier\nv_mfma_f32_16x16x32_bf16 v[224:227],a[80:83],v[88:91],v[224:227]\n" \
"ds_read_b128 v[16:19],v58\nds_read_b128 v[20:23],v58 offset:8192\nv_mfma_f32_16x16x32_bf16 v[224:227],a[112:115],v[92:95],v[224:227]\nds_read_b128 v[24:27],v58 offset:16384\n" \
"ds_read_b128 v[28:31],v58 offset:24576\nv_mfma_f32_16x16x32_bf16 v[228:231],a[84:87],v[88:91],v[228:231]\nv_add_f32 v48,v48,v240\ns_cbranch_vccnz .Lsp3A_%=\n" \
".Lbp3A_%=:\nv_mfma_f32_16x16x32_bf16 v[228:231],a[116:119],v[92:95],v[228:231]\nv_add_f32 v49,v49,v241\nv_lshlrev_b32 v60,16,v112\n" \
"v_mfma_f32_16x16x32_bf16 v[232:235],a[88:91],v[88:91],v[232:235]\nv_add_f32 v50,v50,v242\nv_and_b32 v61,0xffff0000,v112\nv_mfma_f32_16x16x32_bf16 v[232:235],a[120:123],v[92:95],v[232:235]\n" \
"v_add_f32 v51,v51,v243\nv_add_f32 v64,v64,v60\nv_mfma_f32_16x16x32_bf16 v[236:239],a[92:95],v[88:91],v[236:239]\nv_add_f32 v244,v244,v248\n" \
"v_add_f32 v65,v65,v61\nv_mfma_f32_16x16x32_bf16 v[236:239],a[124:127],v[92:95],v[236:239]\nv_add_f32 v245,v245,v249\nv_lshlrev_b32 v60,16,v113\n" \
"v_mfma_f32_16x16x32_bf16 v[224:227],a[144:147],v[96:99],v[224:227]\nv_add_f32 v246,v246,v250\nv_and_b32 v61,0xffff0000,v113\nv_mfma_f32_16x16x32_bf16 v[224:227],a[176:179],v[100:103],v[224:227]\n" \
"v_add_f32 v247,v247,v251\nv_add_f32 v66,v66,v60\nv_mfma_f32_16x16x32_bf16 v[228:231],a[148:151],v[96:99],v[228:231]\nv_add_f32 v48,v48,v244\n" \
"v_add_f32 v67,v67,v61\nv_mfma_f32_16x16x32_bf16 v[228:231],a[180:183],v[100:103],v[228:231]\nv_add_f32 v49,v49,v245\nv_lshlrev_b32 v60,16,v114\n" \
"v_add_f32 v50,v50,v246\nv_mfma_f32_16x16x32_bf16 v[232:235],a[152:155],v[96:99],v[232:235]\nv_and_b32 v61,0xffff0000,v114\nv_add_f32 v51,v51,v247\n" \
"v_add_f32 v68,v68,v60\nv_mfma_f32_16x16x32_bf16 v[232:235],a[184:187],v[100:103],v[232:235]\nv_add_f32 v69,v69,v61\nv_lshlrev_b32 v60,16,v115\n" \
"v_and_b32 v61,0xffff0000,v115\nv_mfma_f32_16x16x32_bf16 v[236:239],a[156:159],v[96:99],v[236:239]\nv_add_f32 v70,v70,v60\nds_read_b128 v[8:11],v62\n" \
"v_mfma_f32_16x16x32_bf16 v[236:239],a[188:191],v[100:103],v[236:239]\nv_add_f32 v71,v71,v61\nds_read_b128 v[12:15],v62 offset:64\nv_mfma_f32_16x16x32_bf16 v[224:227],a[208:211],v[104:107],v[224:227]\n" \
"v_lshlrev_b32 v0,16,v116\nv_and_b32 v1,0xffff0000,v116\nv_mfma_f32_16x16x32_bf16 v[228:231],a[212:215],v[104:107],v[228:231]\nv_lshlrev_b32 v2,16,v117\n" \
"v_and_b32 v3,0xffff0000,v117\nv_mfma_f32_16x16x32_bf16 v[232:235],a[216:219],v[104:107],v[232:235]\nv_lshlrev_b32 v4,16,v120\nv_and_b32 v5,0xffff0000,v120\n" \
"v_mfma_f32_16x16x32_bf16 v[236:239],a[220:223],v[104:107],v[236:239]\nv_lshlrev_b32 v6,16,v121\nv_and_b32 v7,0xffff0000,v121\ns_waitcnt lgkmcnt(0)\n" \
"v_fma_f32 v0,v8,v64,v0\nv_fma_f32 v1,v9,v65,v1\nv_fma_f32 v2,v10,v66,v2\nv_fma_f32 v3,v11,v67,v3\n" \
"v_fma_f32 v4,v8,v68,v4\nv_fma_f32 v5,v9,v69,v5\nv_fma_f32 v6,v10,v70,v6\nv_fma_f32 v7,v11,v71,v7\n" \
"v_fma_f32 v0,v8,v32,v0\nv_fma_f32 v1,v9,v33,v1\nv_fma_f32 v2,v10,v34,v2\nv_fma_f32 v3,v11,v35,v3\n" \
"v_cvt_pk_bf16_f32 v0,v0,v1\nv_cvt_pk_bf16_f32 v1,v2,v3\nbuffer_store_dwordx2 v[0:1],v56,s[44:47],0 offen sc1\nv_fma_f32 v4,v8,v48,v4\n" \
"v_fma_f32 v5,v9,v49,v5\nv_fma_f32 v6,v10,v50,v6\nv_fma_f32 v7,v11,v51,v7\nv_cvt_pk_bf16_f32 v4,v4,v5\n" \
"v_cvt_pk_bf16_f32 v5,v6,v7\nbuffer_store_dwordx2 v[4:5],v56,s[44:47],s35 offen sc1\nv_lshlrev_b32 v64,16,v118\nv_and_b32 v65,0xffff0000,v118\n" \
"v_lshlrev_b32 v66,16,v119\nv_and_b32 v67,0xffff0000,v119\nds_write_addtid_b32 v224 offset:4096\nds_write_addtid_b32 v225 offset:4352\n" \
"ds_write_addtid_b32 v226 offset:4608\nds_write_addtid_b32 v227 offset:4864\nds_write_addtid_b32 v228 offset:5120\nds_write_addtid_b32 v229 offset:5376\n" \
"ds_write_addtid_b32 v230 offset:5632\nds_write_addtid_b32 v231 offset:5888\nds_write_addtid_b32 v232 offset:6144\nds_write_addtid_b32 v233 offset:6400\n" \
"ds_write_addtid_b32 v234 offset:6656\nds_write_addtid_b32 v235 offset:6912\nds_write_addtid_b32 v236 offset:7168\nds_write_addtid_b32 v237 offset:7424\n" \
"ds_write_addtid_b32 v238 offset:7680\nds_write_addtid_b32 v239 offset:7936\nv_lshlrev_b32 v68,16,v122\nv_and_b32 v69,0xffff0000,v122\n" \
"v_lshlrev_b32 v70,16,v123\nv_and_b32 v71,0xffff0000,v123\ns_waitcnt lgkmcnt(15)\nv_pk_add_f32 v[16:17],v[16:17],v[20:21]\n" \
"v_pk_add_f32 v[18:19],v[18:19],v[22:23]\nv_pk_add_f32 v[24:25],v[24:25],v[28:29]\nv_pk_add_f32 v[26:27],v[26:27],v[30:31]\nv_pk_add_f32 v[16:17],v[16:17],v[24:25]\n" \
"v_pk_add_f32 v[18:19],v[18:19],v[26:27]\ns_waitcnt lgkmcnt(0)\ns_barrier\nds_read_b128 v[32:35],v58 offset:4096\n" \
"ds_read_b128 v[36:39],v58 offset:12288\nds_read_b128 v[40:43],v58 offset:20480\nds_read_b128 v[44:47],v58 offset:28672\ns_waitcnt vmcnt(2)\n" \
"v_cmp_eq_u32 vcc,-1,v124\ns_cbranch_vccnz .Lsp3B_%=\n.Lbp3B_%=:\nv_lshlrev_b32 v20,16,v124\n" \
"v_and_b32 v21,0xffff0000,v124\nv_lshlrev_b32 v22,16,v125\nv_and_b32 v23,0xffff0000,v125\nv_lshlrev_b32 v24,16,v126\n" \
"v_and_b32 v25,0xffff0000,v126\nv_lshlrev_b32 v26,16,v127\nv_and_b32 v27,0xffff0000,v127\nv_pk_add_f32 v[52:53],v[52:53],v[20:21]\n" \
"v_pk_add_f32 v[54:55],v[54:55],v[22:23]\nv_pk_add_f32 v[252:253],v[252:253],v[24:25]\nv_pk_add_f32 v[254:255],v[254:255],v[26:27]\nv_pk_fma_f32 v[64:65],v[12:13],v[52:53],v[64:65]\n" \
"v_pk_fma_f32 v[66:67],v[14:15],v[54:55],v[66:67]\nv_pk_fma_f32 v[68:69],v[12:13],v[252:253],v[68:69]\nv_pk_fma_f32 v[70:71],v[14:15],v[254:255],v[70:71]\nv_pk_fma_f32 v[64:65],v[12:13],v[16:17],v[64:65]\n" \
"v_pk_fma_f32 v[66:67],v[14:15],v[18:19],v[66:67]\nv_cvt_pk_bf16_f32 v64,v64,v65\nv_cvt_pk_bf16_f32 v65,v66,v67\nbuffer_store_dwordx2 v[64:65],v56,s[44:47],0 offen offset:32 sc1\n" \
"s_waitcnt lgkmcnt(0)\nv_pk_add_f32 v[32:33],v[32:33],v[36:37]\nv_pk_add_f32 v[34:35],v[34:35],v[38:39]\nv_pk_add_f32 v[40:41],v[40:41],v[44:45]\n" \
"v_pk_add_f32 v[42:43],v[42:43],v[46:47]\nv_pk_add_f32 v[32:33],v[32:33],v[40:41]\nv_pk_add_f32 v[34:35],v[34:35],v[42:43]\nv_pk_fma_f32 v[68:69],v[12:13],v[32:33],v[68:69]\n" \
"v_pk_fma_f32 v[70:71],v[14:15],v[34:35],v[70:71]\nv_cvt_pk_bf16_f32 v68,v68,v69\nv_cvt_pk_bf16_f32 v69,v70,v71\nbuffer_store_dwordx2 v[68:69],v56,s[44:47],s35 offen offset:32 sc1\n" \
"s_branch .Lend_%=\n.LsaF_%=:\ns_memrealtime s[94:95]\ns_waitcnt lgkmcnt(0)\n" \
".LpaF_%=:\ns_sleep 2\nbuffer_load_dword v63,v61,s[36:39],s59 offen sc1\ns_memrealtime s[96:97]\n" \
"s_waitcnt vmcnt(0) lgkmcnt(0)\ns_sub_u32 s96,s96,s94\ns_cmp_gt_u32 s96,0x4c4b40\ns_cbranch_scc1 .LbaF_%=\n" \
"v_cmp_eq_u32 vcc,0,v63\ns_and_b64 vcc,vcc,s[86:87]\ns_cbranch_scc1 .LpaF_%=\ns_branch .LbaF_%=\n" \
".LsbF_%=:\ns_memrealtime s[94:95]\ns_waitcnt lgkmcnt(0)\n.LpbF_%=:\n" \
"s_sleep 2\nbuffer_load_dword v63,v61,s[36:39],s59 offen sc1\ns_memrealtime s[96:97]\ns_waitcnt vmcnt(0) lgkmcnt(0)\n" \
"s_sub_u32 s96,s96,s94\ns_cmp_gt_u32 s96,0x4c4b40\ns_cbranch_scc1 .LbbF_%=\nv_cmp_eq_u32 vcc,0,v63\n" \
"s_and_b64 vcc,vcc,s[86:87]\ns_cbranch_scc1 .LpbF_%=\ns_branch .LbbF_%=\n.LsaS_%=:\n" \
"s_memrealtime s[94:95]\ns_waitcnt lgkmcnt(0)\n.LpaS_%=:\ns_sleep 2\n" \
"buffer_load_dword v63,v61,s[36:39],s59 offen sc1\ns_memrealtime s[96:97]\ns_waitcnt vmcnt(0) lgkmcnt(0)\ns_sub_u32 s96,s96,s94\n" \
"s_cmp_gt_u32 s96,0x4c4b40\ns_cbranch_scc1 .LbaS_%=\nv_cmp_eq_u32 vcc,0,v63\ns_and_b64 vcc,vcc,s[86:87]\n" \
"s_cbranch_scc1 .LpaS_%=\ns_branch .LbaS_%=\n.LsbS_%=:\ns_memrealtime s[94:95]\n" \
"s_waitcnt lgkmcnt(0)\n.LpbS_%=:\ns_sleep 2\nbuffer_load_dword v63,v61,s[36:39],s59 offen sc1\n" \
"s_memrealtime s[96:97]\ns_waitcnt vmcnt(0) lgkmcnt(0)\ns_sub_u32 s96,s96,s94\ns_cmp_gt_u32 s96,0x4c4b40\n" \
"s_cbranch_scc1 .LbbS_%=\nv_cmp_eq_u32 vcc,0,v63\ns_and_b64 vcc,vcc,s[86:87]\ns_cbranch_scc1 .LpbS_%=\n" \
"s_branch .LbbS_%=\n.Lsp3B_%=:\ns_memrealtime s[94:95]\ns_waitcnt lgkmcnt(0)\n" \
".Lpp3B_%=:\ns_sub_u32 s101,s78,0\nbuffer_load_dwordx4 v[124:127],v59,s[36:39],s101 offen sc1\ns_memrealtime s[96:97]\n" \
"s_waitcnt vmcnt(0) lgkmcnt(0)\ns_sub_u32 s96,s96,s94\ns_cmp_gt_u32 s96,0x4c4b40\ns_cbranch_scc1 .Lbp3B_%=\n" \
"v_cmp_eq_u32 vcc,-1,v124\ns_cbranch_vccnz .Lpp3B_%=\ns_branch .Lbp3B_%=\n.Lsg4_%=:\n" \
"s_memrealtime s[94:95]\ns_waitcnt lgkmcnt(0)\n.Lpg4_%=:\ns_sleep 2\n" \
"buffer_load_dword v63,v61,s[36:39],s59 offen sc1\ns_memrealtime s[96:97]\ns_waitcnt vmcnt(0) lgkmcnt(0)\ns_sub_u32 s96,s96,s94\n" \
"s_cmp_gt_u32 s96,0x4c4b40\ns_cbranch_scc1 .Lbg4_%=\nv_cmp_eq_u32 vcc,0,v63\ns_and_b64 vcc,vcc,s[86:87]\n" \
"s_cbranch_scc1 .Lpg4_%=\ns_branch .Lbg4_%=\n.Lse_%=:\ns_memrealtime s[94:95]\n" \
"s_waitcnt lgkmcnt(0)\n.Lpe_%=:\nbuffer_load_dword v64,v69,s[36:39],s79 offen sc1\nbuffer_load_dword v65,v69,s[36:39],s80 offen sc1\n" \
"buffer_load_dword v66,v69,s[36:39],s81 offen sc1\nbuffer_load_dword v67,v69,s[36:39],s82 offen sc1\ns_memrealtime s[96:97]\ns_waitcnt vmcnt(0) lgkmcnt(0)\n" \
"s_sub_u32 s96,s96,s94\ns_cmp_gt_u32 s96,0x4c4b40\ns_cbranch_scc1 .Lbe_%=\nv_max3_u32 v52,v64,v65,v66\n" \
"v_max_u32 v52,v52,v67\nv_cmp_eq_u32 vcc,-1,v52\ns_cbranch_vccnz .Lpe_%=\ns_branch .Lbe_%=\n" \
".Lsp1_%=:\ns_memrealtime s[94:95]\ns_waitcnt lgkmcnt(0)\n.Lpp1_%=:\n" \
"s_sub_u32 s101,s71,409600\nbuffer_load_dwordx4 v[64:67],v59,s[36:39],s101 offen sc1\ns_sub_u32 s101,s76,409600\nbuffer_load_dwordx4 v[52:55],v59,s[36:39],s101 offen sc1\n" \
"s_memrealtime s[96:97]\ns_waitcnt vmcnt(0) lgkmcnt(0)\ns_sub_u32 s96,s96,s94\ns_cmp_gt_u32 s96,0x4c4b40\n" \
"s_cbranch_scc1 .Lbp1_%=\nv_max_u32 v62,v64,v52\nv_cmp_eq_u32 vcc,-1,v62\ns_cbranch_vccnz .Lpp1_%=\n" \
"s_branch .Lbp1_%=\n.Lsp2A_%=:\ns_memrealtime s[94:95]\ns_waitcnt lgkmcnt(0)\n" \
".Lpp2A_%=:\ns_sub_u32 s101,s72,409600\nbuffer_load_dwordx4 v[60:63],v59,s[36:39],s101 offen sc1\ns_memrealtime s[96:97]\n" \
"s_waitcnt vmcnt(0) lgkmcnt(0)\ns_sub_u32 s96,s96,s94\ns_cmp_gt_u32 s96,0x4c4b40\ns_cbranch_scc1 .Lbp2A_%=\n" \
"v_cmp_eq_u32 vcc,-1,v60\ns_cbranch_vccnz .Lpp2A_%=\ns_branch .Lbp2A_%=\n.Lsp2B_%=:\n" \
"s_memrealtime s[94:95]\ns_waitcnt lgkmcnt(0)\n.Lpp2B_%=:\ns_sub_u32 s101,s77,409600\n" \
"buffer_load_dwordx4 v[108:111],v59,s[36:39],s101 offen sc1\ns_memrealtime s[96:97]\ns_waitcnt vmcnt(0) lgkmcnt(0)\ns_sub_u32 s96,s96,s94\n" \
"s_cmp_gt_u32 s96,0x4c4b40\ns_cbranch_scc1 .Lbp2B_%=\nv_cmp_eq_u32 vcc,-1,v108\ns_cbranch_vccnz .Lpp2B_%=\n" \
"s_branch .Lbp2B_%=\n.Lsp3A_%=:\ns_memrealtime s[94:95]\ns_waitcnt lgkmcnt(0)\n" \
".Lpp3A_%=:\ns_sub_u32 s101,s73,0\nbuffer_load_dwordx4 v[112:115],v59,s[36:39],s101 offen sc1\ns_memrealtime s[96:97]\n" \
"s_waitcnt vmcnt(0) lgkmcnt(0)\ns_sub_u32 s96,s96,s94\ns_cmp_gt_u32 s96,0x4c4b40\ns_cbranch_scc1 .Lbp3A_%=\n" \
"v_cmp_eq_u32 vcc,-1,v112\ns_cbranch_vccnz .Lpp3A_%=\ns_branch .Lbp3A_%=\n.Lz0_%=:\n" \
"s_branch .Lz1_%=\n.Lxq5_%=:\ns_mov_b64 exec,1\nbuffer_store_dword v61,off,s[36:39],s54 sc1\n" \
"s_mov_b64 exec,-1\nbuffer_load_dword v63,v61,s[36:39],s75 offen sc1\ns_mov_b32 s75,s59\ns_add_u32 s54,s54,0x10000\n" \
"s_add_u32 s83,s83,819200\ns_branch .Lyq5_%=\n.Lz1_%=:\ns_branch .Lz2_%=\n" \
".Lxt5_%=:\nv_and_b32 v20,0x300,v59\nv_and_b32 v21,0xf0,v59\nv_lshlrev_b32 v20,4,v20\n" \
"s_mov_b32 m0,162176\nv_lshl_or_b32 v20,v21,3,v20\nbuffer_load_dword v20,s[40:43],s101 offen lds\ns_mov_b32 m0,s56\n" \
"s_branch .Lyt5_%=\n.Lz2_%=:\ns_branch .Lz3_%=\n.Lxq4_%=:\n" \
"s_mov_b64 exec,1\nbuffer_store_dword v61,off,s[36:39],s54 sc1\ns_mov_b64 exec,-1\nbuffer_load_dword v63,v61,s[36:39],s75 offen sc1\n" \
"s_mov_b32 s75,s59\ns_add_u32 s54,s54,0x10000\ns_add_u32 s83,s83,819200\ns_branch .Lyq4_%=\n" \
".Lz3_%=:\ns_branch .Lz4_%=\n.Lxt4_%=:\nv_and_b32 v0,0x300,v59\n" \
"v_and_b32 v1,0xf0,v59\nv_lshlrev_b32 v0,4,v0\ns_mov_b32 m0,162176\nv_lshl_or_b32 v0,v1,3,v0\n" \
"buffer_load_dword v0,s[40:43],s101 offen lds\ns_mov_b32 m0,s56\ns_branch .Lyt4_%=\n.Lz4_%=:\n" \
"s_branch .Lzl_%=\n.LsmP_%=:\ns_sleep 2\nbuffer_load_dwordx4 v[48:51],v2,s[36:39],s73 offen sc1\n" \
"buffer_load_dwordx4 v[60:63],v2,s[36:39],s75 offen sc1\nbuffer_load_dwordx4 v[52:55],v2,s[36:39],s74 offen sc1\nbuffer_load_dwordx4 v[64:67],v2,s[36:39],s76 offen sc1\nbuffer_load_dwordx4 v[56:59],v1,s[36:39],s77 offen sc1\n" \
"buffer_load_dwordx4 v[68:71],v1,s[36:39],s78 offen sc1\ns_memrealtime s[96:97]\ns_waitcnt vmcnt(0) lgkmcnt(0)\ns_sub_u32 s96,s96,s30\n" \
"s_cmp_gt_u32 s96,0x4c4b40\ns_cbranch_scc1 .LbmP_%=\nv_cmp_ne_u64 vcc,s[28:29],v[48:49]\nv_cmp_ne_u64 s[84:85],s[28:29],v[52:53]\n" \
"v_cmp_ne_u64 s[82:83],s[28:29],v[56:57]\ns_or_b64 vcc,vcc,s[84:85]\nv_cmp_ne_u64 s[84:85],s[28:29],v[60:61]\ns_or_b64 vcc,vcc,s[82:83]\n" \
"v_cmp_ne_u64 s[82:83],s[28:29],v[64:65]\ns_or_b64 vcc,vcc,s[84:85]\nv_cmp_ne_u64 s[84:85],s[28:29],v[68:69]\ns_or_b64 vcc,vcc,s[82:83]\n" \
"s_or_b64 vcc,vcc,s[84:85]\ns_cbranch_vccnz .LsmP_%=\ns_branch .LbmP_%=\n.LsxP_%=:\n" \
"s_waitcnt lgkmcnt(0)\nv_mul_f32 v74,0.5,v74\nv_pk_add_f32 v[124:125],v[54:55],v[74:75] op_sel_hi:[1,0]\nv_pk_add_f32 v[126:127],v[58:59],1.0 op_sel_hi:[1,0]\n" \
"v_max3_f32 v112,|v50|,|v51|,|v124|\nv_max3_f32 v112,|v125|,|v126|,v112\nv_max_f32 v112,|v127|,v112\ns_nop 1\n" \
"v_max_u32_dpp v112,v112,v112 quad_perm:[1,0,3,2] row_mask:0xf bank_mask:0xf bound_ctrl:1\ns_nop 1\nv_max_u32_dpp v112,v112,v112 quad_perm:[2,3,0,1] row_mask:0xf bank_mask:0xf bound_ctrl:1\ns_nop 1\n" \
"v_max_u32_dpp v112,v112,v112 row_half_mirror row_mask:0xf bank_mask:0xf bound_ctrl:1\ns_nop 1\nv_max_u32_dpp v112,v112,v112 row_mirror row_mask:0xf bank_mask:0xf bound_ctrl:1\ns_nop 0\n" \
"v_readlane_b32 s82,v112,0\nv_readlane_b32 s100,v112,16\nv_readlane_b32 s101,v112,32\nv_readlane_b32 s31,v112,48\n" \
"s_max_u32 s100,s100,s101\ns_max_u32 s82,s82,s31\ns_max_u32 s82,s82,s100\nv_mov_b32 v113,s82\n" \
"ds_write_b32 v251,v113 offset:16\ns_waitcnt lgkmcnt(0)\ns_barrier\nds_read_b128 v[124:127],v114 offset:16\n" \
"s_waitcnt lgkmcnt(0)\nv_max3_u32 v124,v124,v125,v126\nv_max_u32 v124,v124,v127\ns_nop 0\n" \
"v_readfirstlane_b32 s93,v124\ns_nop 0\ns_lshr_b32 s93,s93,23\ns_max_u32 s93,s93,64\n" \
"s_min_u32 s93,s93,254\ns_sub_u32 s82,256,s93\ns_lshl_b32 s82,s82,23\nv_mov_b32 v124,s82\n" \
"v_mul_f32 v74,v74,v124\nv_pk_mul_f32 v[62:63],v[50:51],v[124:125] op_sel_hi:[1,0]\nv_max3_f32 v112,|v62|,|v63|,0\nv_cvt_pk_f16_f32 v62,v62,v63\n" \
"ds_write_b32 v252,v62 offset:62848\nv_pk_fma_f32 v[66:67],v[54:55],v[124:125],v[74:75] op_sel_hi:[1,0,0]\nv_max3_f32 v112,|v66|,|v67|,v112\nv_cvt_pk_f16_f32 v66,v66,v67\n" \
"ds_write_b32 v253,v66 offset:62848\nv_pk_fma_f32 v[70:71],v[58:59],v[124:125],v[124:125] op_sel_hi:[1,0,0]\nv_max3_f32 v112,|v70|,|v71|,v112\nv_cvt_pk_f16_f32 v70,v70,v71\n" \
"ds_write_b32 v254,v70 offset:62848\ns_waitcnt lgkmcnt(0)\ns_barrier\nds_read_b64 v[96:97],v234 offset:62848\n" \
"ds_read_b64 v[98:99],v235 offset:62848\nds_read_b64 v[100:101],v236 offset:62848\nds_read_b64 v[102:103],v237 offset:62848\nds_read_b64 v[104:105],v238 offset:62848\n" \
"ds_read_b64 v[106:107],v239 offset:62848\nds_read_b64 v[108:109],v240 offset:62848\nds_read_b64 v[110:111],v241 offset:62848\ns_waitcnt lgkmcnt(0)\n" \
"s_branch .LbxP_%=\n.LsrP_%=:\ns_sleep 2\nbuffer_load_dwordx4 v[104:107],v1,s[36:39],s7 offen sc1\n" \
"buffer_load_dwordx4 v[108:111],v1,s[36:39],s7 offen offset:1024 sc1\ns_memrealtime s[96:97]\ns_waitcnt vmcnt(0) lgkmcnt(0)\ns_sub_u32 s96,s96,s30\n" \
"s_cmp_gt_u32 s96,0x4c4b40\ns_cbranch_scc1 .LbrP_%=\nv_xor_b32 v112,s29,v105\nv_xor_b32 v113,s29,v107\n" \
"v_xor_b32 v114,s29,v109\nv_xor_b32 v115,s29,v111\nv_or3_b32 v112,v112,v113,v114\nv_xor_b32 v113,s28,v104\n" \
"v_xor_b32 v114,s28,v106\nv_or_b32 v112,v112,v115\nv_xor_b32 v115,s28,v108\nv_lshl_or_b32 v112,v112,8,v113\n" \
"v_xor_b32 v113,s28,v110\nv_or3_b32 v112,v112,v114,v115\nv_or_b32 v112,v112,v113\nv_cmp_ne_u32 vcc,0,v112\n" \
"s_nop 0\ns_cbranch_vccnz .LsrP_%=\ns_branch .LbrP_%=\n.LsnP_%=:\n" \
"s_sleep 2\nbuffer_load_dwordx4 v[80:83],v120,s[36:39],s5 offen sc1\nbuffer_load_dwordx4 v[84:87],v120,s[36:39],s5 offen offset:2304 sc1\nbuffer_load_dwordx4 v[88:91],v120,s[36:39],s6 offen sc1\n" \
"buffer_load_dwordx4 v[92:95],v120,s[36:39],s6 offen offset:2304 sc1\ns_memrealtime s[96:97]\ns_waitcnt vmcnt(0) lgkmcnt(0)\ns_sub_u32 s96,s96,s30\n" \
"s_cmp_gt_u32 s96,0x4c4b40\ns_cbranch_scc1 .LbnP_%=\nv_cmp_ne_u64 vcc,s[28:29],v[80:81]\nv_cmp_ne_u64 s[24:25],s[28:29],v[84:85]\n" \
"v_cmp_ne_u64 s[26:27],s[28:29],v[88:89]\ns_or_b64 vcc,vcc,s[24:25]\nv_cmp_ne_u64 s[24:25],s[28:29],v[92:93]\ns_or_b64 vcc,vcc,s[26:27]\n" \
"s_or_b64 vcc,vcc,s[24:25]\ns_cbranch_vccnz .LsnP_%=\ns_branch .LbnP_%=\n.LsuP_%=:\n" \
"v_and_b32 v125,1,v112\nv_cmpx_eq_u32 vcc,0,v125\nbuffer_load_dwordx4 v[112:115],v3,s[36:39],s16 offen sc1\ns_mov_b64 exec,-1\n" \
"v_and_b32 v125,1,v116\nv_cmpx_eq_u32 vcc,0,v125\nbuffer_load_dwordx4 v[116:119],v3,s[36:39],s17 offen sc1\ns_mov_b64 exec,-1\n" \
"v_and_b32 v125,1,v120\nv_cmpx_eq_u32 vcc,0,v125\nbuffer_load_dwordx4 v[120:123],v124,s[36:39],s18 offen sc1\ns_mov_b64 exec,-1\n" \
"s_memrealtime s[96:97]\ns_waitcnt vmcnt(0) lgkmcnt(0)\ns_sub_u32 s96,s96,s30\ns_cmp_gt_u32 s96,0x4c4b40\n" \
"s_cbranch_scc1 .LbuP_%=\nv_and_b32 v125,v112,v116\nv_bitop3_b32 v125,v125,v120,1 bitop3:0x80\nv_cmp_eq_u32 vcc,0,v125\n" \
"s_nop 0\ns_cbranch_vccnz .LsuP_%=\ns_branch .LbuP_%=\n.LsmN_%=:\n" \
"s_sleep 2\nbuffer_load_dwordx4 v[48:51],v2,s[36:39],s73 offen sc1\nbuffer_load_dwordx4 v[60:63],v2,s[36:39],s75 offen sc1\nbuffer_load_dwordx4 v[52:55],v2,s[36:39],s74 offen sc1\n" \
"buffer_load_dwordx4 v[64:67],v2,s[36:39],s76 offen sc1\nbuffer_load_dwordx4 v[56:59],v1,s[36:39],s77 offen sc1\nbuffer_load_dwordx4 v[68:71],v1,s[36:39],s78 offen sc1\ns_memrealtime s[96:97]\n" \
"s_waitcnt vmcnt(0) lgkmcnt(0)\ns_sub_u32 s96,s96,s30\ns_cmp_gt_u32 s96,0x4c4b40\ns_cbranch_scc1 .LbmN_%=\n" \
"v_cmp_ne_u64 vcc,s[28:29],v[48:49]\nv_cmp_ne_u64 s[84:85],s[28:29],v[52:53]\nv_cmp_ne_u64 s[82:83],s[28:29],v[56:57]\ns_or_b64 vcc,vcc,s[84:85]\n" \
"v_cmp_ne_u64 s[84:85],s[28:29],v[60:61]\ns_or_b64 vcc,vcc,s[82:83]\nv_cmp_ne_u64 s[82:83],s[28:29],v[64:65]\ns_or_b64 vcc,vcc,s[84:85]\n" \
"v_cmp_ne_u64 s[84:85],s[28:29],v[68:69]\ns_or_b64 vcc,vcc,s[82:83]\ns_or_b64 vcc,vcc,s[84:85]\ns_cbranch_vccnz .LsmN_%=\n" \
"s_branch .LbmN_%=\n.LsxN_%=:\ns_waitcnt lgkmcnt(0)\nv_mul_f32 v74,0.5,v74\n" \
"v_pk_add_f32 v[124:125],v[54:55],v[74:75] op_sel_hi:[1,0]\nv_pk_add_f32 v[126:127],v[58:59],1.0 op_sel_hi:[1,0]\nv_max3_f32 v112,|v50|,|v51|,|v124|\nv_max3_f32 v112,|v125|,|v126|,v112\n" \
"v_max_f32 v112,|v127|,v112\ns_nop 1\nv_max_u32_dpp v112,v112,v112 quad_perm:[1,0,3,2] row_mask:0xf bank_mask:0xf bound_ctrl:1\ns_nop 1\n" \
"v_max_u32_dpp v112,v112,v112 quad_perm:[2,3,0,1] row_mask:0xf bank_mask:0xf bound_ctrl:1\ns_nop 1\nv_max_u32_dpp v112,v112,v112 row_half_mirror row_mask:0xf bank_mask:0xf bound_ctrl:1\ns_nop 1\n" \
"v_max_u32_dpp v112,v112,v112 row_mirror row_mask:0xf bank_mask:0xf bound_ctrl:1\ns_nop 0\nv_readlane_b32 s82,v112,0\nv_readlane_b32 s100,v112,16\n" \
"v_readlane_b32 s101,v112,32\nv_readlane_b32 s31,v112,48\ns_max_u32 s100,s100,s101\ns_max_u32 s82,s82,s31\n" \
"s_max_u32 s82,s82,s100\nv_mov_b32 v113,s82\nds_write_b32 v251,v113 offset:16\ns_waitcnt lgkmcnt(0)\n" \
"s_barrier\nds_read_b128 v[124:127],v114 offset:16\ns_waitcnt lgkmcnt(0)\nv_max3_u32 v124,v124,v125,v126\n" \
"v_max_u32 v124,v124,v127\ns_nop 0\nv_readfirstlane_b32 s93,v124\ns_nop 0\n" \
"s_lshr_b32 s93,s93,23\ns_max_u32 s93,s93,64\ns_min_u32 s93,s93,254\ns_sub_u32 s82,256,s93\n" \
"s_lshl_b32 s82,s82,23\nv_mov_b32 v124,s82\nv_mul_f32 v74,v74,v124\nv_pk_mul_f32 v[62:63],v[50:51],v[124:125] op_sel_hi:[1,0]\n" \
"v_max3_f32 v112,|v62|,|v63|,0\nv_cvt_pk_f16_f32 v62,v62,v63\nds_write_b32 v252,v62 offset:62848\nv_pk_fma_f32 v[66:67],v[54:55],v[124:125],v[74:75] op_sel_hi:[1,0,0]\n" \
"v_max3_f32 v112,|v66|,|v67|,v112\nv_cvt_pk_f16_f32 v66,v66,v67\nds_write_b32 v253,v66 offset:62848\nv_pk_fma_f32 v[70:71],v[58:59],v[124:125],v[124:125] op_sel_hi:[1,0,0]\n" \
"v_max3_f32 v112,|v70|,|v71|,v112\nv_cvt_pk_f16_f32 v70,v70,v71\nds_write_b32 v254,v70 offset:62848\ns_waitcnt lgkmcnt(0)\n" \
"s_barrier\nds_read_b64 v[96:97],v234 offset:62848\nds_read_b64 v[98:99],v235 offset:62848\nds_read_b64 v[100:101],v236 offset:62848\n" \
"ds_read_b64 v[102:103],v237 offset:62848\nds_read_b64 v[104:105],v238 offset:62848\nds_read_b64 v[106:107],v239 offset:62848\nds_read_b64 v[108:109],v240 offset:62848\n" \
"ds_read_b64 v[110:111],v241 offset:62848\ns_waitcnt lgkmcnt(0)\ns_branch .LbxN_%=\n.LsrN_%=:\n" \
"s_sleep 2\nbuffer_load_dwordx4 v[104:107],v1,s[36:39],s7 offen sc1\nbuffer_load_dwordx4 v[108:111],v1,s[36:39],s7 offen offset:1024 sc1\ns_memrealtime s[96:97]\n" \
"s_waitcnt vmcnt(0) lgkmcnt(0)\ns_sub_u32 s96,s96,s30\ns_cmp_gt_u32 s96,0x4c4b40\ns_cbranch_scc1 .LbrN_%=\n" \
"v_xor_b32 v112,s29,v105\nv_xor_b32 v113,s29,v107\nv_xor_b32 v114,s29,v109\nv_xor_b32 v115,s29,v111\n" \
"v_or3_b32 v112,v112,v113,v114\nv_xor_b32 v113,s28,v104\nv_xor_b32 v114,s28,v106\nv_or_b32 v112,v112,v115\n" \
"v_xor_b32 v115,s28,v108\nv_lshl_or_b32 v112,v112,8,v113\nv_xor_b32 v113,s28,v110\nv_or3_b32 v112,v112,v114,v115\n" \
"v_or_b32 v112,v112,v113\nv_cmp_ne_u32 vcc,0,v112\ns_nop 0\ns_cbranch_vccnz .LsrN_%=\n" \
"s_branch .LbrN_%=\n.LsnN_%=:\ns_sleep 2\nbuffer_load_dwordx4 v[80:83],v120,s[36:39],s5 offen sc1\n" \
"buffer_load_dwordx4 v[84:87],v120,s[36:39],s5 offen offset:2304 sc1\nbuffer_load_dwordx4 v[88:91],v120,s[36:39],s6 offen sc1\nbuffer_load_dwordx4 v[92:95],v120,s[36:39],s6 offen offset:2304 sc1\ns_memrealtime s[96:97]\n" \
"s_waitcnt vmcnt(0) lgkmcnt(0)\ns_sub_u32 s96,s96,s30\ns_cmp_gt_u32 s96,0x4c4b40\ns_cbranch_scc1 .LbnN_%=\n" \
"v_cmp_ne_u64 vcc,s[28:29],v[80:81]\nv_cmp_ne_u64 s[24:25],s[28:29],v[84:85]\nv_cmp_ne_u64 s[26:27],s[28:29],v[88:89]\ns_or_b64 vcc,vcc,s[24:25]\n" \
"v_cmp_ne_u64 s[24:25],s[28:29],v[92:93]\ns_or_b64 vcc,vcc,s[26:27]\ns_or_b64 vcc,vcc,s[24:25]\ns_cbranch_vccnz .LsnN_%=\n" \
"s_branch .LbnN_%=\n.LsuN_%=:\nv_and_b32 v125,1,v112\nv_cmpx_eq_u32 vcc,0,v125\n" \
"buffer_load_dwordx4 v[112:115],v3,s[36:39],s16 offen sc1\ns_mov_b64 exec,-1\nv_and_b32 v125,1,v116\nv_cmpx_eq_u32 vcc,0,v125\n" \
"buffer_load_dwordx4 v[116:119],v3,s[36:39],s17 offen sc1\ns_mov_b64 exec,-1\nv_and_b32 v125,1,v120\nv_cmpx_eq_u32 vcc,0,v125\n" \
"buffer_load_dwordx4 v[120:123],v124,s[36:39],s18 offen sc1\ns_mov_b64 exec,-1\ns_memrealtime s[96:97]\ns_waitcnt vmcnt(0) lgkmcnt(0)\n" \
"s_sub_u32 s96,s96,s30\ns_cmp_gt_u32 s96,0x4c4b40\ns_cbranch_scc1 .LbuN_%=\nv_and_b32 v125,v112,v116\n" \
"v_bitop3_b32 v125,v125,v120,1 bitop3:0x80\nv_cmp_eq_u32 vcc,0,v125\ns_nop 0\ns_cbranch_vccnz .LsuN_%=\n" \
"s_branch .LbuN_%=\n.LsmC_%=:\ns_sleep 2\nbuffer_load_dwordx4 v[48:51],v2,s[36:39],s73 offen sc1\n" \
"buffer_load_dwordx4 v[60:63],v2,s[36:39],s75 offen sc1\nbuffer_load_dwordx4 v[52:55],v2,s[36:39],s74 offen sc1\nbuffer_load_dwordx4 v[64:67],v2,s[36:39],s76 offen sc1\nbuffer_load_dwordx4 v[56:59],v1,s[36:39],s77 offen sc1\n" \
"buffer_load_dwordx4 v[68:71],v1,s[36:39],s78 offen sc1\ns_memrealtime s[96:97]\ns_waitcnt vmcnt(0) lgkmcnt(0)\ns_sub_u32 s96,s96,s30\n" \
"s_cmp_gt_u32 s96,0x4c4b40\ns_cbranch_scc1 .LbmC_%=\nv_cmp_ne_u64 vcc,s[28:29],v[48:49]\nv_cmp_ne_u64 s[84:85],s[28:29],v[52:53]\n" \
"v_cmp_ne_u64 s[82:83],s[28:29],v[56:57]\ns_or_b64 vcc,vcc,s[84:85]\nv_cmp_ne_u64 s[84:85],s[28:29],v[60:61]\ns_or_b64 vcc,vcc,s[82:83]\n" \
"v_cmp_ne_u64 s[82:83],s[28:29],v[64:65]\ns_or_b64 vcc,vcc,s[84:85]\nv_cmp_ne_u64 s[84:85],s[28:29],v[68:69]\ns_or_b64 vcc,vcc,s[82:83]\n" \
"s_or_b64 vcc,vcc,s[84:85]\ns_cbranch_vccnz .LsmC_%=\ns_branch .LbmC_%=\n.LsxC_%=:\n" \
"s_waitcnt lgkmcnt(0)\nv_mul_f32 v74,0.5,v74\nv_pk_add_f32 v[124:125],v[54:55],v[74:75] op_sel_hi:[1,0]\nv_pk_add_f32 v[126:127],v[58:59],1.0 op_sel_hi:[1,0]\n" \
"v_max3_f32 v112,|v50|,|v51|,|v124|\nv_max3_f32 v112,|v125|,|v126|,v112\nv_max_f32 v112,|v127|,v112\ns_nop 1\n" \
"v_max_u32_dpp v112,v112,v112 quad_perm:[1,0,3,2] row_mask:0xf bank_mask:0xf bound_ctrl:1\ns_nop 1\nv_max_u32_dpp v112,v112,v112 quad_perm:[2,3,0,1] row_mask:0xf bank_mask:0xf bound_ctrl:1\ns_nop 1\n" \
"v_max_u32_dpp v112,v112,v112 row_half_mirror row_mask:0xf bank_mask:0xf bound_ctrl:1\ns_nop 1\nv_max_u32_dpp v112,v112,v112 row_mirror row_mask:0xf bank_mask:0xf bound_ctrl:1\ns_nop 0\n" \
"v_readlane_b32 s82,v112,0\nv_readlane_b32 s100,v112,16\nv_readlane_b32 s101,v112,32\nv_readlane_b32 s31,v112,48\n" \
"s_max_u32 s100,s100,s101\ns_max_u32 s82,s82,s31\ns_max_u32 s82,s82,s100\nv_mov_b32 v113,s82\n" \
"ds_write_b32 v251,v113 offset:16\ns_waitcnt lgkmcnt(0)\ns_barrier\nds_read_b128 v[124:127],v114 offset:16\n" \
"s_waitcnt lgkmcnt(0)\nv_max3_u32 v124,v124,v125,v126\nv_max_u32 v124,v124,v127\ns_nop 0\n" \
"v_readfirstlane_b32 s93,v124\ns_nop 0\ns_lshr_b32 s93,s93,23\ns_max_u32 s93,s93,64\n" \
"s_min_u32 s93,s93,254\ns_sub_u32 s82,256,s93\ns_lshl_b32 s82,s82,23\nv_mov_b32 v124,s82\n" \
"v_mul_f32 v74,v74,v124\nv_pk_mul_f32 v[62:63],v[50:51],v[124:125] op_sel_hi:[1,0]\nv_max3_f32 v112,|v62|,|v63|,0\nv_cvt_pk_f16_f32 v62,v62,v63\n" \
"ds_write_b32 v252,v62 offset:62848\nv_pk_fma_f32 v[66:67],v[54:55],v[124:125],v[74:75] op_sel_hi:[1,0,0]\nv_max3_f32 v112,|v66|,|v67|,v112\nv_cvt_pk_f16_f32 v66,v66,v67\n" \
"ds_write_b32 v253,v66 offset:62848\nv_pk_fma_f32 v[70:71],v[58:59],v[124:125],v[124:125] op_sel_hi:[1,0,0]\nv_max3_f32 v112,|v70|,|v71|,v112\nv_cvt_pk_f16_f32 v70,v70,v71\n" \
"ds_write_b32 v254,v70 offset:62848\ns_waitcnt lgkmcnt(0)\ns_barrier\nds_read_b64 v[96:97],v234 offset:62848\n" \
"ds_read_b64 v[98:99],v235 offset:62848\nds_read_b64 v[100:101],v236 offset:62848\nds_read_b64 v[102:103],v237 offset:62848\nds_read_b64 v[104:105],v238 offset:62848\n" \
"ds_read_b64 v[106:107],v239 offset:62848\nds_read_b64 v[108:109],v240 offset:62848\nds_read_b64 v[110:111],v241 offset:62848\ns_waitcnt lgkmcnt(0)\n" \
"s_branch .LbxC_%=\n.Lsw_%=:\nbuffer_store_dwordx4 v[80:83],v3,s[36:39],s70 offen sc1\nbuffer_store_dwordx4 v[84:87],v3,s[36:39],s71 offen sc1\n" \
"buffer_store_dwordx4 v[88:91],v115,s[36:39],s72 offen sc1\n.Lpw_%=:\nbuffer_load_dwordx4 v[104:107],v1,s[36:39],s7 offen sc1\nbuffer_load_dwordx4 v[108:111],v1,s[36:39],s7 offen offset:1024 sc1\n" \
"s_memrealtime s[96:97]\ns_waitcnt vmcnt(0) lgkmcnt(0)\ns_sub_u32 s96,s96,s30\ns_cmp_gt_u32 s96,0x4c4b40\n" \
"s_cbranch_scc1 .Lqw_%=\nv_xor_b32 v116,s29,v105\nv_xor_b32 v117,s29,v107\nv_xor_b32 v118,s29,v109\n" \
"v_xor_b32 v119,s29,v111\nv_or3_b32 v116,v116,v117,v118\nv_xor_b32 v117,s28,v104\nv_xor_b32 v118,s28,v106\n" \
"v_or_b32 v116,v116,v119\nv_xor_b32 v119,s28,v108\nv_lshl_or_b32 v116,v116,8,v117\nv_xor_b32 v117,s28,v110\n" \
"v_or3_b32 v116,v116,v118,v119\nv_or_b32 v116,v116,v117\nv_cmp_ne_u32 vcc,0,v116\ns_nop 0\n" \
"s_cbranch_vccnz .Lpw_%=\n.Lqw_%=:\ns_add_u32 m0,s88,16384\ns_nop 0\n" \
"buffer_load_dwordx4 v124,s[36:39],s66 offen sc1 lds\ns_add_u32 m0,s88,20480\ns_nop 0\nbuffer_load_dwordx4 v125,s[36:39],s67 offen sc1 lds\n" \
"s_add_u32 m0,s88,24576\ns_nop 0\nbuffer_load_dwordx4 v124,s[36:39],s68 offen sc1 lds\ns_add_u32 m0,s88,28672\n" \
"s_nop 0\nbuffer_load_dwordx4 v125,s[36:39],s69 offen sc1 lds\ns_waitcnt vmcnt(0)\ns_branch .Lbw_%=\n" \
".Lsz_%=:\nv_subrev_u32 v124,16384,v80\nv_lshrrev_b32 v124,6,v124\nv_mul_u32_u24 v125,9344,v124\n" \
"v_lshl_add_u32 v124,v84,2,v125\nv_sub_u32 v125,2382780,v124\ns_mov_b32 s79,2925632\nbuffer_load_dword v64,v124,s[36:39],s79 offen sc1\n" \
"v_subrev_u32 v126,0,v125\ns_mov_b32 s79,2925632\nbuffer_load_dword v72,v126,s[36:39],s79 offen sc1\ns_mov_b32 s79,3075136\n" \
"buffer_load_dword v65,v124,s[36:39],s79 offen sc1\nv_subrev_u32 v126,149504,v125\ns_mov_b32 s79,2925632\nbuffer_load_dword v73,v126,s[36:39],s79 offen sc1\n" \
"s_mov_b32 s79,3224640\nbuffer_load_dword v66,v124,s[36:39],s79 offen sc1\nv_subrev_u32 v126,299008,v125\ns_mov_b32 s79,2925632\n" \
"buffer_load_dword v74,v126,s[36:39],s79 offen sc1\ns_mov_b32 s79,3374144\nbuffer_load_dword v67,v124,s[36:39],s79 offen sc1\nv_subrev_u32 v126,448512,v125\n" \
"s_mov_b32 s79,2925632\nbuffer_load_dword v75,v126,s[36:39],s79 offen sc1\ns_mov_b32 s79,2944320\nbuffer_load_dword v68,v124,s[36:39],s79 offen sc1\n" \
"v_subrev_u32 v126,18688,v125\ns_mov_b32 s79,2925632\nbuffer_load_dword v76,v126,s[36:39],s79 offen sc1\ns_mov_b32 s79,3093824\n" \
"buffer_load_dword v69,v124,s[36:39],s79 offen sc1\nv_subrev_u32 v126,168192,v125\ns_mov_b32 s79,2925632\nbuffer_load_dword v77,v126,s[36:39],s79 offen sc1\n" \
"s_mov_b32 s79,3243328\nbuffer_load_dword v70,v124,s[36:39],s79 offen sc1\nv_subrev_u32 v126,317696,v125\ns_mov_b32 s79,2925632\n" \
"buffer_load_dword v78,v126,s[36:39],s79 offen sc1\ns_mov_b32 s79,3392832\nbuffer_load_dword v71,v124,s[36:39],s79 offen sc1\nv_subrev_u32 v126,467200,v125\n" \
"s_mov_b32 s79,2925632\nbuffer_load_dword v79,v126,s[36:39],s79 offen sc1\ns_waitcnt vmcnt(0)\nv_lshlrev_b32 v124,16,v72\n" \
"v_and_b32 v125,0xffff0000,v72\nv_add_f32 v16,v16,v124\nv_sub_f32 v20,v20,v125\nv_lshlrev_b32 v124,16,v64\n" \
"v_and_b32 v125,0xffff0000,v64\nv_add_f32 v32,v32,v124\nv_sub_f32 v36,v36,v125\nv_lshlrev_b32 v124,16,v73\n" \
"v_and_b32 v125,0xffff0000,v73\nv_add_f32 v17,v17,v124\nv_sub_f32 v21,v21,v125\nv_lshlrev_b32 v124,16,v65\n" \
"v_and_b32 v125,0xffff0000,v65\nv_add_f32 v33,v33,v124\nv_sub_f32 v37,v37,v125\nv_lshlrev_b32 v124,16,v74\n" \
"v_and_b32 v125,0xffff0000,v74\nv_add_f32 v18,v18,v124\nv_sub_f32 v22,v22,v125\nv_lshlrev_b32 v124,16,v66\n" \
"v_and_b32 v125,0xffff0000,v66\nv_add_f32 v34,v34,v124\nv_sub_f32 v38,v38,v125\nv_lshlrev_b32 v124,16,v75\n" \
"v_and_b32 v125,0xffff0000,v75\nv_add_f32 v19,v19,v124\nv_sub_f32 v23,v23,v125\nv_lshlrev_b32 v124,16,v67\n" \
"v_and_b32 v125,0xffff0000,v67\nv_add_f32 v35,v35,v124\nv_sub_f32 v39,v39,v125\nv_lshlrev_b32 v124,16,v76\n" \
"v_and_b32 v125,0xffff0000,v76\nv_add_f32 v40,v40,v124\nv_sub_f32 v44,v44,v125\nv_lshlrev_b32 v124,16,v68\n" \
"v_and_b32 v125,0xffff0000,v68\nv_add_f32 v56,v56,v124\nv_sub_f32 v60,v60,v125\nv_lshlrev_b32 v124,16,v77\n" \
"v_and_b32 v125,0xffff0000,v77\nv_add_f32 v41,v41,v124\nv_sub_f32 v45,v45,v125\nv_lshlrev_b32 v124,16,v69\n" \
"v_and_b32 v125,0xffff0000,v69\nv_add_f32 v57,v57,v124\nv_sub_f32 v61,v61,v125\nv_lshlrev_b32 v124,16,v78\n" \
"v_and_b32 v125,0xffff0000,v78\nv_add_f32 v42,v42,v124\nv_sub_f32 v46,v46,v125\nv_lshlrev_b32 v124,16,v70\n" \
"v_and_b32 v125,0xffff0000,v70\nv_add_f32 v58,v58,v124\nv_sub_f32 v62,v62,v125\nv_lshlrev_b32 v124,16,v79\n" \
"v_and_b32 v125,0xffff0000,v79\nv_add_f32 v43,v43,v124\nv_sub_f32 v47,v47,v125\nv_lshlrev_b32 v124,16,v71\n" \
"v_and_b32 v125,0xffff0000,v71\nv_add_f32 v59,v59,v124\nv_sub_f32 v63,v63,v125\ns_branch .Lbz_%=\n" \
".Lst_%=:\ns_sleep 1\nbuffer_load_dword v96,v120,s[36:39],s80 offen sc1\nbuffer_load_dword v97,v121,s[36:39],s80 offen sc1\n" \
"buffer_load_dword v98,v122,s[36:39],s80 offen sc1\nbuffer_load_dword v99,v123,s[36:39],s80 offen sc1\nbuffer_load_dword v100,v120,s[36:39],s81 offen sc1\nbuffer_load_dword v101,v121,s[36:39],s81 offen sc1\n" \
"buffer_load_dword v102,v122,s[36:39],s81 offen sc1\nbuffer_load_dword v103,v123,s[36:39],s81 offen sc1\nbuffer_load_dword v104,v120,s[36:39],s82 offen sc1\nbuffer_load_dword v105,v121,s[36:39],s82 offen sc1\n" \
"buffer_load_dword v106,v122,s[36:39],s82 offen sc1\nbuffer_load_dword v107,v123,s[36:39],s82 offen sc1\nbuffer_load_dword v108,v120,s[36:39],s83 offen sc1\nbuffer_load_dword v109,v121,s[36:39],s83 offen sc1\n" \
"buffer_load_dword v110,v122,s[36:39],s83 offen sc1\nbuffer_load_dword v111,v123,s[36:39],s83 offen sc1\ns_memrealtime s[96:97]\ns_waitcnt vmcnt(0) lgkmcnt(0)\n" \
"s_sub_u32 s96,s96,s30\ns_cmp_gt_u32 s96,0x4c4b40\ns_cbranch_scc1 .Lbt_%=\nv_mfma_f32_16x16x32_f16 v[64:67],v[128:131],v[96:99],0\n" \
"v_or3_b32 v124,v96,v97,v98\nv_or3_b32 v124,v124,v99,v100\nv_mfma_f32_16x16x32_f16 v[68:71],v[132:135],v[96:99],0\nv_or3_b32 v124,v124,v101,v102\n" \
"v_mfma_f32_16x16x32_f16 v[72:75],v[136:139],v[100:103],0\nv_or3_b32 v124,v124,v103,v104\nv_mfma_f32_16x16x32_f16 v[76:79],v[140:143],v[100:103],0\nv_or3_b32 v124,v124,v105,v106\n" \
"v_mfma_f32_16x16x32_f16 v[80:83],v[144:147],v[104:107],0\nv_or3_b32 v124,v124,v107,v108\nv_mfma_f32_16x16x32_f16 v[84:87],v[148:151],v[104:107],0\nv_or3_b32 v124,v124,v109,v110\n" \
"v_mfma_f32_16x16x32_f16 v[88:91],v[152:155],v[108:111],0\nv_or_b32 v124,v124,v111\nv_mfma_f32_16x16x32_f16 v[92:95],v[156:159],v[108:111],0\nv_and_b32 v124,1,v124\n" \
"v_cmp_ne_u32 vcc,0,v124\ns_nop 0\ns_and_b64 vcc,vcc,s[78:79]\ns_cbranch_vccnz .Lst_%=\n" \
"s_branch .Lbt_%=\n.LsnC_%=:\ns_sleep 2\nbuffer_load_dwordx4 v[16:19],v32,s[36:39],s5 offen sc1\n" \
"buffer_load_dwordx4 v[20:23],v32,s[36:39],s5 offen offset:2304 sc1\nbuffer_load_dwordx4 v[24:27],v32,s[36:39],s6 offen sc1\nbuffer_load_dwordx4 v[28:31],v32,s[36:39],s6 offen offset:2304 sc1\ns_memrealtime s[96:97]\n" \
"s_waitcnt vmcnt(0) lgkmcnt(0)\ns_sub_u32 s96,s96,s30\ns_cmp_gt_u32 s96,0x4c4b40\ns_cbranch_scc1 .LbnC_%=\n" \
"v_cmp_ne_u64 vcc,s[28:29],v[16:17]\nv_cmp_ne_u64 s[24:25],s[28:29],v[20:21]\nv_cmp_ne_u64 s[26:27],s[28:29],v[24:25]\ns_or_b64 vcc,vcc,s[24:25]\n" \
"v_cmp_ne_u64 s[24:25],s[28:29],v[28:29]\ns_or_b64 vcc,vcc,s[26:27]\ns_or_b64 vcc,vcc,s[24:25]\ns_cbranch_vccnz .LsnC_%=\n" \
"s_branch .LbnC_%=\n.LsuC_%=:\nv_and_b32 v125,1,v112\nv_cmpx_eq_u32 vcc,0,v125\n" \
"buffer_load_dwordx4 v[112:115],v3,s[36:39],s16 offen sc1\ns_mov_b64 exec,-1\nv_and_b32 v125,1,v116\nv_cmpx_eq_u32 vcc,0,v125\n" \
"buffer_load_dwordx4 v[116:119],v3,s[36:39],s17 offen sc1\ns_mov_b64 exec,-1\nv_and_b32 v125,1,v120\nv_cmpx_eq_u32 vcc,0,v125\n" \
"buffer_load_dwordx4 v[120:123],v124,s[36:39],s18 offen sc1\ns_mov_b64 exec,-1\ns_memrealtime s[96:97]\ns_waitcnt vmcnt(0) lgkmcnt(0)\n" \
"s_sub_u32 s96,s96,s30\ns_cmp_gt_u32 s96,0x4c4b40\ns_cbranch_scc1 .LbuC_%=\nv_and_b32 v125,v112,v116\n" \
"v_bitop3_b32 v125,v125,v120,1 bitop3:0x80\nv_cmp_eq_u32 vcc,0,v125\ns_nop 0\ns_cbranch_vccnz .LsuC_%=\n" \
"s_branch .LbuC_%=\n.Lend_%=:\n"
#define ASM_CLOB "v1", "v2", "v3", "v4", "v5", "v6", "v7", "v8", "v9", "v10", "v11", "v12", "v13", "v14", "v15", "v16", "v17", "v18", "v19", "v20", "v21", "v22", "v23", "v24", "v25", "v26", "v27", "v28", "v29", "v30", "v31", "v32", "v33", "v34", "v35", "v36", "v37", "v38", "v39", "v40", "v41", "v42", "v43", "v44", "v45", "v46", "v47", "v48", "v49", "v50", "v51", "v52", "v53", "v54", "v55", "v56", "v57", "v58", "v59", "v60", "v61", "v62", "v63", "v64", "v65", "v66", "v67", "v68", "v69", "v70", "v71", "v72", "v73", "v74", "v75", "v76", "v77", "v78", "v79", "v80", "v81", "v82", "v83", "v84", "v85", "v86", "v87", "v88", "v89", "v90", "v91", "v92", "v93", "v94", "v95", "v96", "v97", "v98", "v99", "v100", "v101", "v102", "v103", "v104", "v105", "v106", "v107", "v108", "v109", "v110", "v111", "v112", "v113", "v114", "v115", "v116", "v117", "v118", "v119", "v120", "v121", "v122", "v123", "v124", "v125", "v126", "v127", "v128", "v129", "v130", "v131", "v132", "v133", "v134", "v135", "v136", "v137", "v138", "v139", "v140", "v141", "v142", "v143", "v144", "v145", "v146", "v147", "v148", "v149", "v150", "v151", "v152", "v153", "v154", "v155", "v156", "v157", "v158", "v159", "v160", "v161", "v162", "v163", "v164", "v165", "v166", "v167", "v168", "v169", "v170", "v171", "v172", "v173", "v174", "v175", "v176", "v177", "v178", "v179", "v180", "v181", "v182", "v183", "v184", "v185", "v186", "v187", "v188", "v189", "v190", "v191", "v192", "v193", "v194", "v195", "v196", "v197", "v198", "v199", "v200", "v201", "v202", "v203", "v204", "v205", "v206", "v207", "v208", "v209", "v210", "v211", "v212", "v213", "v214", "v215", "v216", "v217", "v218", "v219", "v220", "v221", "v222", "v223", "v224", "v225", "v226", "v227", "v228", "v229", "v230", "v231", "v232", "v233", "v234", "v235", "v236", "v237", "v238", "v239", "v240", "v241", "v242", "v243", "v244", "v245", "v246", "v247", "v248", "v249", "v250", "v251", "v252", "v253", "v254", "v255", "a0", "a1", "a2", "a3", "a4", "a5", "a6", "a7", "a8", "a9", "a10", "a11", "a12", "a13", "a14", "a15", "a16", "a17", "a18", "a19", "a20", "a21", "a22", "a23", "a24", "a25", "a26", "a27", "a28", "a29", "a30", "a31", "a32", "a33", "a34", "a35", "a36", "a37", "a38", "a39", "a40", "a41", "a42", "a43", "a44", "a45", "a46", "a47", "a48", "a49", "a50", "a51", "a52", "a53", "a54", "a55", "a56", "a57", "a58", "a59", "a60", "a61", "a62", "a63", "a64", "a65", "a66", "a67", "a68", "a69", "a70", "a71", "a72", "a73", "a74", "a75", "a76", "a77", "a78", "a79", "a80", "a81", "a82", "a83", "a84", "a85", "a86", "a87", "a88", "a89", "a90", "a91", "a92", "a93", "a94", "a95", "a96", "a97", "a98", "a99", "a100", "a101", "a102", "a103", "a104", "a105", "a106", "a107", "a108", "a109", "a110", "a111", "a112", "a113", "a114", "a115", "a116", "a117", "a118", "a119", "a120", "a121", "a122", "a123", "a124", "a125", "a126", "a127", "a128", "a129", "a130", "a131", "a132", "a133", "a134", "a135", "a136", "a137", "a138", "a139", "a140", "a141", "a142", "a143", "a144", "a145", "a146", "a147", "a148", "a149", "a150", "a151", "a152", "a153", "a154", "a155", "a156", "a157", "a158", "a159", "a160", "a161", "a162", "a163", "a164", "a165", "a166", "a167", "a168", "a169", "a170", "a171", "a172", "a173", "a174", "a175", "a176", "a177", "a178", "a179", "a180", "a181", "a182", "a183", "a184", "a185", "a186", "a187", "a188", "a189", "a190", "a191", "a192", "a193", "a194", "a195", "a196", "a197", "a198", "a199", "a200", "a201", "a202", "a203", "a204", "a205", "a206", "a207", "a208", "a209", "a210", "a211", "a212", "a213", "a214", "a215", "a216", "a217", "a218", "a219", "a220", "a221", "a222", "a223", "a224", "a225", "a226", "a227", "a228", "a229", "a230", "a231", "a232", "a233", "a234", "a235", "a236", "a237", "a238", "a239", "a240", "a241", "a242", "a243", "a244", "a245", "a246", "a247", "a248", "a249", "a250", "a251", "a252", "a253", "a254", "a255", "s3", "s6", "s7", "s8", "s9", "s10", "s11", "s12", "s13", "s14", "s15", "s16", "s17", "s18", "s19", "s20", "s21", "s22", "s23", "s24", "s25", "s26", "s27", "s28", "s29", "s30", "s31", "s32", "s33", "s34", "s35", "s36", "s37", "s38", "s39", "s40", "s41", "s42", "s43", "s44", "s45", "s46", "s47", "s48", "s49", "s50", "s51", "s52", "s53", "s54", "s55", "s56", "s57", "s58", "s59", "s60", "s61", "s62", "s63", "s64", "s65", "s66", "s67", "s68", "s69", "s70", "s71", "s72", "s73", "s74", "s75", "s76", "s77", "s78", "s79", "s80", "s81", "s82", "s83", "s84", "s85", "s86", "s87", "s88", "s89", "s90", "s91", "s92", "s93", "s94", "s95", "s96", "s97", "s98", "s99", "s100", "s101"
extern "C" __global__ __launch_bounds__(256, 1) void adm(
    const u16* x, const u16* wr, const u16* wi, const u16* w1, const u16* cvec, const u16* wmod, char* wsb, const u16* b1, const u16* b2, const u16* w2, u16* outp) {
  unsigned long long ka = (unsigned long long)__builtin_amdgcn_kernarg_segment_ptr(), tab = (unsigned long long)TAB;
  unsigned bid = blockIdx.x, tid = threadIdx.x;
  asm volatile(ASM_ALL : "+{s[0:1]}"(ka), "+{s2}"(bid), "+{s[4:5]}"(tab), "+{v0}"(tid) : : "vcc", "scc", "memory", ASM_CLOB);
}

extern "C" void kernel_launch(void* const* d_in, const int* in_sizes, int n_in, void* d_out, int out_size, void* d_ws,
                              size_t ws_size, hipStream_t stream) {
  (void)hipFuncSetAttribute((const void*)adm, hipFuncAttributeMaxDynamicSharedMemorySize, 163840);
  adm<<<256, 256, 163840, stream>>>((const u16*)d_in[0], (const u16*)d_in[1], (const u16*)d_in[2], (const u16*)d_in[4], (const u16*)d_in[8], (const u16*)d_in[9],
                                    (char*)d_ws, (const u16*)d_in[5], (const u16*)d_in[7], (const u16*)d_in[6], (u16*)d_out);
}
```

```cpp
#include <hip/hip_runtime.h>
typedef unsigned short u16;
static __device__ const unsigned __attribute__((aligned(128))) TAB[5632] = {
13312,13312,13312,13312,13312,0x2e1f3364,0x31a831a8,0x33642e1f,13312,0x31a831a8,0x34000000,0x31a8b1a8,13312,0x33642e1f,0x31a8b1a8,0xae1fb364,
13312,0x34000000,46080,0xb4000000,13312,0x3364ae1f,0xb1a8b1a8,0xae1f3364,13312,0x31a8b1a8,0xb4000000,0x31a831a8,13312,0x2e1fb364,0xb1a831a8,0x3364ae1f,
13312,46080,13312,46080,13312,0xae1fb364,0x31a831a8,0xb364ae1f,13312,0xb1a8b1a8,0x34000000,0xb1a831a8,13312,0xb364ae1f,0x31a8b1a8,0x2e1f3364,
13312,0xb4000000,46080,0x34000000,13312,0xb3642e1f,0xb1a8b1a8,0x2e1fb364,13312,0xb1a831a8,0xb4000000,0xb1a8b1a8,13312,0xae1f3364,0xb1a831a8,0xb3642e1f,
13312,13312,13312,13312,0x34000000,0x3364ae1f,0x31a8b1a8,0x2e1fb364,46080,0xb1a8b1a8,0xb4000000,0xb1a831a8,0xb4000000,0xae1f3364,0x31a831a8,0x3364ae1f,
13312,0x34000000,46080,0xb4000000,0x34000000,0xae1fb364,0xb1a831a8,0x33642e1f,46080,0xb1a831a8,0x34000000,0xb1a8b1a8,0xb4000000,0x33642e1f,0xb1a8b1a8,0x2e1f3364,
13312,46080,13312,46080,0x34000000,0xb3642e1f,0x31a8b1a8,0xae1f3364,46080,0x31a831a8,0xb4000000,0x31a8b1a8,0xb4000000,0x2e1fb364,0x31a831a8,0xb3642e1f,
13312,0xb4000000,46080,0x34000000,0x34000000,0x2e1f3364,0xb1a831a8,0xb364ae1f,46080,0x31a8b1a8,0x34000000,0x31a831a8,0xb4000000,0xb364ae1f,0xb1a8b1a8,0xae1fb364,
13312,13312,13312,13312,46080,0xae1fb364,0xb1a8b1a8,0xb364ae1f,13312,0x31a831a8,0x34000000,0x31a8b1a8,46080,0xb364ae1f,0xb1a831a8,0x2e1f3364,
13312,0x34000000,46080,0xb4000000,46080,0xb3642e1f,0x31a831a8,0x2e1fb364,13312,0x31a8b1a8,0xb4000000,0x31a831a8,46080,0xae1f3364,0x31a8b1a8,0xb3642e1f,
13312,46080,13312,46080,46080,0x2e1f3364,0xb1a8b1a8,0x33642e1f,13312,0xb1a8b1a8,0x34000000,0xb1a831a8,46080,0x33642e1f,0xb1a831a8,0xae1fb364,
13312,0xb4000000,46080,0x34000000,46080,0x3364ae1f,0x31a831a8,0xae1f3364,13312,0xb1a831a8,0xb4000000,0xb1a8b1a8,46080,0x2e1fb364,0x31a8b1a8,0x3364ae1f,
13312,13312,13312,13312,0xb4000000,0xb3642e1f,0xb1a831a8,0xae1f3364,46080,0xb1a8b1a8,0xb4000000,0xb1a831a8,0x34000000,0x2e1fb364,0xb1a8b1a8,0xb3642e1f,
13312,0x34000000,46080,0xb4000000,0xb4000000,0x2e1f3364,0x31a8b1a8,0xb364ae1f,46080,0xb1a831a8,0x34000000,0xb1a8b1a8,0x34000000,0xb364ae1f,0x31a831a8,0xae1fb364,
13312,46080,13312,46080,0xb4000000,0x3364ae1f,0xb1a831a8,0x2e1fb364,46080,0x31a831a8,0xb4000000,0x31a8b1a8,0x34000000,0xae1f3364,0xb1a8b1a8,0x3364ae1f,
13312,0xb4000000,46080,0x34000000,0xb4000000,0xae1fb364,0x31a8b1a8,0x33642e1f,46080,0x31a8b1a8,0x34000000,0x31a831a8,0x34000000,0x33642e1f,0x31a831a8,0x2e1f3364,
0x34000000,0x34000000,0x34000000,0x34000000,0x34000000,0x3364ae1f,0x31a8b1a8,0x2e1fb364,0x34000000,0x31a8b1a8,46080,0xb1a8b1a8,0x34000000,0x2e1fb364,0xb1a8b1a8,0xb3642e1f,
0x34000000,46080,0xb4000000,13312,0x34000000,0xae1fb364,0xb1a831a8,0x33642e1f,0x34000000,0xb1a8b1a8,13312,0x31a8b1a8,0x34000000,0xb364ae1f,0x31a831a8,0xae1fb364,
0x34000000,0xb4000000,0x34000000,0xb4000000,0x34000000,0xb3642e1f,0x31a8b1a8,0xae1f3364,0x34000000,0xb1a831a8,46080,0x31a831a8,0x34000000,0xae1f3364,0xb1a8b1a8,0x3364ae1f,
0x34000000,13312,0xb4000000,46080,0x34000000,0x2e1f3364,0xb1a831a8,0xb364ae1f,0x34000000,0x31a831a8,13312,0xb1a831a8,0x34000000,0x33642e1f,0x31a831a8,0x2e1f3364,
0x34000000,0x34000000,0x34000000,0x34000000,46080,0xae1fb364,0xb1a8b1a8,0xb364ae1f,0xb4000000,0xb1a831a8,13312,0x31a831a8,13312,0x33642e1f,0x31a8b1a8,0xae1fb364,
0x34000000,46080,0xb4000000,13312,46080,0xb3642e1f,0x31a831a8,0x2e1fb364,0xb4000000,0x31a831a8,46080,0xb1a831a8,13312,0x2e1fb364,0xb1a831a8,0x3364ae1f,
0x34000000,0xb4000000,0x34000000,0xb4000000,46080,0x2e1f3364,0xb1a8b1a8,0x33642e1f,0xb4000000,0x31a8b1a8,13312,0xb1a8b1a8,13312,0xb364ae1f,0x31a8b1a8,0x2e1f3364,
0x34000000,13312,0xb4000000,46080,46080,0x3364ae1f,0x31a831a8,0xae1f3364,0xb4000000,0xb1a8b1a8,46080,0x31a8b1a8,13312,0xae1f3364,0xb1a831a8,0xb3642e1f,
0x34000000,0x34000000,0x34000000,0x34000000,0xb4000000,0xb3642e1f,0xb1a831a8,0xae1f3364,0x34000000,0x31a8b1a8,46080,0xb1a8b1a8,0xb4000000,0xae1f3364,0x31a831a8,0x3364ae1f,
0x34000000,46080,0xb4000000,13312,0xb4000000,0x2e1f3364,0x31a8b1a8,0xb364ae1f,0x34000000,0xb1a8b1a8,13312,0x31a8b1a8,0xb4000000,0x33642e1f,0xb1a8b1a8,0x2e1f3364,
0x34000000,0xb4000000,0x34000000,0xb4000000,0xb4000000,0x3364ae1f,0xb1a831a8,0x2e1fb364,0x34000000,0xb1a831a8,46080,0x31a831a8,0xb4000000,0x2e1fb364,0x31a831a8,0xb3642e1f,
0x34000000,13312,0xb4000000,46080,0xb4000000,0xae1fb364,0x31a8b1a8,0x33642e1f,0x34000000,0x31a831a8,13312,0xb1a831a8,0xb4000000,0xb364ae1f,0xb1a8b1a8,0xae1fb364,
0x34000000,0x34000000,0x34000000,0x34000000,13312,0x2e1f3364,0x31a831a8,0x33642e1f,0xb4000000,0xb1a831a8,13312,0x31a831a8,46080,0xb364ae1f,0xb1a831a8,0x2e1f3364,
0x34000000,46080,0xb4000000,13312,13312,0x3364ae1f,0xb1a8b1a8,0xae1f3364,0xb4000000,0x31a831a8,46080,0xb1a831a8,46080,0xae1f3364,0x31a8b1a8,0xb3642e1f,
0x34000000,0xb4000000,0x34000000,0xb4000000,13312,0xae1fb364,0x31a831a8,0xb364ae1f,0xb4000000,0x31a8b1a8,13312,0xb1a8b1a8,46080,0x33642e1f,0xb1a831a8,0xae1fb364,
0x34000000,13312,0xb4000000,46080,13312,0xb3642e1f,0xb1a8b1a8,0x2e1fb364,0xb4000000,0xb1a8b1a8,46080,0x31a8b1a8,46080,0x2e1fb364,0x31a8b1a8,0x3364ae1f,
13312,13312,13312,0,13312,0x31243221,0x33e1298f,0,13312,0x33e1298f,0x2d79b384,0,0,0,0,0,
13312,0x32eeb000,0xb2eeb000,0,13312,0x2d79b384,0xb1243221,0,13312,0xad79b384,0x31243221,0,0,0,0,0,
13312,0xb2eeb000,0x32eeb000,0,13312,0xb3e1298f,0xad79b384,0,13312,0xb1243221,0xb3e1298f,0,0,0,0,0,
0,0,0,0,0,0,0,0,0,0,0,0,0,0,0,0,
13312,13312,13312,0,0x32eeb000,0x2d79b384,0xad79b384,0,0xb2eeb000,0xb1243221,0x31243221,0,0,0,0,0,
13312,0x32eeb000,0xb2eeb000,0,0x32eeb000,0xb3e1298f,0x33e1298f,0,0xb2eeb000,0x33e1298f,0xb3e1298f,0,0,0,0,0,
13312,0xb2eeb000,0x32eeb000,0,0x32eeb000,0x31243221,0xb1243221,0,0xb2eeb000,0xad79b384,0x2d79b384,0,0,0,0,0,
0,0,0,0,0,0,0,0,0,0,0,0,0,0,0,0,
13312,13312,13312,0,0xb2eeb000,0xb3e1298f,0xb1243221,0,0x32eeb000,0xad79b384,0xb3e1298f,0,0,0,0,0,
13312,0x32eeb000,0xb2eeb000,0,0xb2eeb000,0x31243221,0xad79b384,0,0x32eeb000,0xb1243221,0x2d79b384,0,0,0,0,0,
13312,0xb2eeb000,0x32eeb000,0,0xb2eeb000,0x2d79b384,0x33e1298f,0,0x32eeb000,0x33e1298f,0x31243221,0,0,0,0,0,
0,0,0,0,0,0,0,0,0,0,0,0,0,0,0,0,
0,0,0,0,0,0,0,0,0,0,0,0,0,0,0,0,
0,0,0,0,0,0,0,0,0,0,0,0,0,0,0,0,
0,0,0,0,0,0,0,0,0,0,0,0,0,0,0,0,
0,0,0,0,0,0,0,0,0,0,0,0,0,0,0,0,
0x34000000,0x34000000,0x34000000,0,0x34000000,0x3221b124,0x298fb3e1,0,0x34000000,0x298fb3e1,0xb384ad79,0,0,0,0,0,
0x34000000,0xb000b2ee,0xb00032ee,0,0x34000000,0xb384ad79,0x32213124,0,0x34000000,0xb3842d79,0x3221b124,0,0,0,0,0,
0x34000000,0xb00032ee,0xb000b2ee,0,0x34000000,0x298f33e1,0xb3842d79,0,0x34000000,0x32213124,0x298f33e1,0,0,0,0,0,
0,0,0,0,0,0,0,0,0,0,0,0,0,0,0,0,
0x34000000,0x34000000,0x34000000,0,0xb000b2ee,0xb384ad79,0xb3842d79,0,0xb00032ee,0x32213124,0x3221b124,0,0,0,0,0,
0x34000000,0xb000b2ee,0xb00032ee,0,0xb000b2ee,0x298f33e1,0x298fb3e1,0,0xb00032ee,0x298fb3e1,0x298f33e1,0,0,0,0,0,
0x34000000,0xb00032ee,0xb000b2ee,0,0xb000b2ee,0x3221b124,0x32213124,0,0xb00032ee,0xb3842d79,0xb384ad79,0,0,0,0,0,
0,0,0,0,0,0,0,0,0,0,0,0,0,0,0,0,
0x34000000,0x34000000,0x34000000,0,0xb00032ee,0x298f33e1,0x32213124,0,0xb000b2ee,0xb3842d79,0x298f33e1,0,0,0,0,0,
0x34000000,0xb000b2ee,0xb00032ee,0,0xb00032ee,0x3221b124,0xb3842d79,0,0xb000b2ee,0x32213124,0xb384ad79,0,0,0,0,0,
0x34000000,0xb00032ee,0xb000b2ee,0,0xb00032ee,0xb384ad79,0x298fb3e1,0,0xb000b2ee,0x298fb3e1,0x3221b124,0,0,0,0,0,
0,0,0,0,0,0,0,0,0,0,0,0,0,0,0,0,
0,0,0,0,0,0,0,0,0,0,0,0,0,0,0,0,
0,0,0,0,0,0,0,0,0,0,0,0,0,0,0,0,
0,0,0,0,0,0,0,0,0,0,0,0,0,0,0,0,
0,0,0,0,0,0,0,0,0,0,0,0,0,0,0,0,
13312,0x2a3e33d9,0x2e1f3364,0x307232a7,13312,0x307232a7,0x33642e1f,0x33d9aa3e,13312,0x32a73072,0x3364ae1f,0x2a3eb3d9,13312,0x33d92a3e,0x2e1fb364,0xb2a7b072,
13312,0x33d9aa3e,0xae1fb364,0xb2a73072,13312,0x32a7b072,0xb364ae1f,0x2a3e33d9,13312,0x3072b2a7,0xb3642e1f,0x33d92a3e,13312,0x2a3eb3d9,0xae1f3364,0x3072b2a7,
13312,0xaa3eb3d9,0x2e1f3364,0xb072b2a7,13312,0xb072b2a7,0x33642e1f,0xb3d92a3e,13312,0xb2a7b072,0x3364ae1f,0xaa3e33d9,13312,0xb3d9aa3e,0x2e1fb364,0x32a73072,
13312,0xb3d92a3e,0xae1fb364,0x32a7b072,13312,0xb2a73072,0xb364ae1f,0xaa3eb3d9,13312,0xb07232a7,0xb3642e1f,0xb3d9aa3e,13312,0xaa3e33d9,0xae1f3364,0xb07232a7,
0x31a831a8,0x32a73072,0x33642e1f,0x33d92a3e,0x31a8b1a8,0x2a3eb3d9,0xae1fb364,0xb2a7b072,0xb1a8b1a8,0xb3d92a3e,0xae1f3364,0x307232a7,0xb1a831a8,0x307232a7,0x3364ae1f,0xaa3eb3d9,
0x31a831a8,0x3072b2a7,0xb364ae1f,0xaa3e33d9,0x31a8b1a8,0xb3d9aa3e,0x2e1f3364,0x3072b2a7,0xb1a8b1a8,0x2a3e33d9,0x2e1fb364,0xb2a73072,0xb1a831a8,0x32a7b072,0xb3642e1f,0x33d9aa3e,
0x31a831a8,0xb2a7b072,0x33642e1f,0xb3d9aa3e,0x31a8b1a8,0xaa3e33d9,0xae1fb364,0x32a73072,0xb1a8b1a8,0x33d9aa3e,0xae1f3364,0xb072b2a7,0xb1a831a8,0xb072b2a7,0x3364ae1f,0x2a3e33d9,
0x31a831a8,0xb07232a7,0xb364ae1f,0x2a3eb3d9,0x31a8b1a8,0x33d92a3e,0x2e1f3364,0xb07232a7,0xb1a8b1a8,0xaa3eb3d9,0x2e1fb364,0x32a7b072,0xb1a831a8,0xb2a73072,0xb3642e1f,0xb3d92a3e,
0x34000000,0x33d9aa3e,0x3364ae1f,0x32a7b072,0xb4000000,0xb2a73072,0xae1f3364,0x2a3e33d9,0x34000000,0x3072b2a7,0xae1fb364,0xb3d9aa3e,0xb4000000,0xaa3e33d9,0x33642e1f,0x3072b2a7,
0x34000000,0xaa3eb3d9,0xb3642e1f,0x307232a7,0xb4000000,0x307232a7,0x2e1fb364,0xb3d92a3e,0x34000000,0xb2a7b072,0x2e1f3364,0x2a3eb3d9,0xb4000000,0x33d92a3e,0xb364ae1f,0x32a73072,
0x34000000,0xb3d92a3e,0x3364ae1f,0xb2a73072,0xb4000000,0x32a7b072,0xae1f3364,0xaa3eb3d9,0x34000000,0xb07232a7,0xae1fb364,0x33d92a3e,0xb4000000,0x2a3eb3d9,0x33642e1f,0xb07232a7,
0x34000000,0x2a3e33d9,0xb3642e1f,0xb072b2a7,0xb4000000,0xb072b2a7,0x2e1fb364,0x33d9aa3e,0x34000000,0x32a73072,0x2e1f3364,0xaa3e33d9,0xb4000000,0xb3d9aa3e,0xb364ae1f,0xb2a7b072,
0x31a8b1a8,0x3072b2a7,0x2e1fb364,0x2a3eb3d9,0x31a831a8,0x33d92a3e,0x3364ae1f,0x3072b2a7,0xb1a831a8,0x2a3e33d9,0x33642e1f,0x32a7b072,0xb1a8b1a8,0xb2a73072,0x2e1f3364,0x33d9aa3e,
0x31a8b1a8,0xb2a7b072,0xae1f3364,0x33d92a3e,0x31a831a8,0x2a3eb3d9,0xb3642e1f,0x32a73072,0xb1a831a8,0x33d9aa3e,0xb364ae1f,0x307232a7,0xb1a8b1a8,0x307232a7,0xae1fb364,0x2a3e33d9,
0x31a8b1a8,0xb07232a7,0x2e1fb364,0xaa3e33d9,0x31a831a8,0xb3d9aa3e,0x3364ae1f,0xb07232a7,0xb1a831a8,0xaa3eb3d9,0x33642e1f,0xb2a73072,0xb1a8b1a8,0x32a7b072,0x2e1f3364,0xb3d92a3e,
0x31a8b1a8,0x32a73072,0xae1f3364,0xb3d9aa3e,0x31a831a8,0xaa3e33d9,0xb3642e1f,0xb2a7b072,0xb1a831a8,0xb3d92a3e,0xb364ae1f,0xb072b2a7,0xb1a8b1a8,0xb072b2a7,0xae1fb364,0xaa3eb3d9,
0x34000000,0x33d9aa3e,0x3364ae1f,0x32a7b072,0x34000000,0x32a7b072,0x2e1fb364,0xaa3eb3d9,0x34000000,0x3072b2a7,0xae1fb364,0xb3d9aa3e,0x34000000,0x2a3eb3d9,0xb364ae1f,0xb07232a7,
0x34000000,0xaa3eb3d9,0xb3642e1f,0x307232a7,0x34000000,0xb072b2a7,0xae1f3364,0x33d9aa3e,0x34000000,0xb2a7b072,0x2e1f3364,0x2a3eb3d9,0x34000000,0xb3d9aa3e,0x33642e1f,0xb2a7b072,
0x34000000,0xb3d92a3e,0x3364ae1f,0xb2a73072,0x34000000,0xb2a73072,0x2e1fb364,0x2a3e33d9,0x34000000,0xb07232a7,0xae1fb364,0x33d92a3e,0x34000000,0xaa3e33d9,0xb364ae1f,0x3072b2a7,
0x34000000,0x2a3e33d9,0xb3642e1f,0xb072b2a7,0x34000000,0x307232a7,0xae1f3364,0xb3d92a3e,0x34000000,0x32a73072,0x2e1f3364,0xaa3e33d9,0x34000000,0x33d92a3e,0x33642e1f,0x32a73072,
0x31a8b1a8,0x3072b2a7,0x2e1fb364,0x2a3eb3d9,0xb1a8b1a8,0xb3d9aa3e,0xb3642e1f,0xb07232a7,0xb1a831a8,0x2a3e33d9,0x33642e1f,0x32a7b072,0x31a831a8,0x32a7b072,0xae1fb364,0xb3d92a3e,
0x31a8b1a8,0xb2a7b072,0xae1f3364,0x33d92a3e,0xb1a8b1a8,0xaa3e33d9,0x3364ae1f,0xb2a7b072,0xb1a831a8,0x33d9aa3e,0xb364ae1f,0x307232a7,0x31a831a8,0xb072b2a7,0x2e1f3364,0xaa3eb3d9,
0x31a8b1a8,0xb07232a7,0x2e1fb364,0xaa3e33d9,0xb1a8b1a8,0x33d92a3e,0xb3642e1f,0x3072b2a7,0xb1a831a8,0xaa3eb3d9,0x33642e1f,0xb2a73072,0x31a831a8,0xb2a73072,0xae1fb364,0x33d9aa3e,
0x31a8b1a8,0x32a73072,0xae1f3364,0xb3d9aa3e,0xb1a8b1a8,0x2a3eb3d9,0x3364ae1f,0x32a73072,0xb1a831a8,0xb3d92a3e,0xb364ae1f,0xb072b2a7,0x31a831a8,0x307232a7,0x2e1f3364,0x2a3e33d9,
46080,0xaa3eb3d9,0xae1fb364,0xb072b2a7,13312,0x307232a7,0x33642e1f,0x33d9aa3e,46080,0xb2a7b072,0xb3642e1f,0xaa3e33d9,13312,0x33d92a3e,0x2e1fb364,0xb2a7b072,
46080,0xb3d92a3e,0x2e1f3364,0x32a7b072,13312,0x32a7b072,0xb364ae1f,0x2a3e33d9,46080,0xb07232a7,0x3364ae1f,0xb3d9aa3e,13312,0x2a3eb3d9,0xae1f3364,0x3072b2a7,
46080,0x2a3e33d9,0xae1fb364,0x307232a7,13312,0xb072b2a7,0x33642e1f,0xb3d92a3e,46080,0x32a73072,0xb3642e1f,0x2a3eb3d9,13312,0xb3d9aa3e,0x2e1fb364,0x32a73072,
46080,0x33d9aa3e,0x2e1f3364,0xb2a73072,13312,0xb2a73072,0xb364ae1f,0xaa3eb3d9,46080,0x3072b2a7,0x3364ae1f,0x33d92a3e,13312,0xaa3e33d9,0xae1f3364,0xb07232a7,
0xb1a8b1a8,0xb2a7b072,0xb364ae1f,0xb3d9aa3e,0x31a8b1a8,0x2a3eb3d9,0xae1fb364,0xb2a7b072,0x31a831a8,0x33d9aa3e,0x2e1fb364,0xb072b2a7,0xb1a831a8,0x307232a7,0x3364ae1f,0xaa3eb3d9,
0xb1a8b1a8,0xb07232a7,0x33642e1f,0x2a3eb3d9,0x31a8b1a8,0xb3d9aa3e,0x2e1f3364,0x3072b2a7,0x31a831a8,0xaa3eb3d9,0xae1f3364,0x32a7b072,0xb1a831a8,0x32a7b072,0xb3642e1f,0x33d9aa3e,
0xb1a8b1a8,0x32a73072,0xb364ae1f,0x33d92a3e,0x31a8b1a8,0xaa3e33d9,0xae1fb364,0x32a73072,0x31a831a8,0xb3d92a3e,0x2e1fb364,0x307232a7,0xb1a831a8,0xb072b2a7,0x3364ae1f,0x2a3e33d9,
0xb1a8b1a8,0x3072b2a7,0x33642e1f,0xaa3e33d9,0x31a8b1a8,0x33d92a3e,0x2e1f3364,0xb07232a7,0x31a831a8,0x2a3e33d9,0xae1f3364,0xb2a73072,0xb1a831a8,0xb2a73072,0xb3642e1f,0xb3d92a3e,
13312,13312,13312,13312,0x2a3e33d9,0x307232a7,0x32a73072,0x33d92a3e,0x2e1f3364,0x33642e1f,0x3364ae1f,0x2e1fb364,0x307232a7,0x33d9aa3e,0x2a3eb3d9,0xb2a7b072,
0x31a831a8,0x31a8b1a8,0xb1a8b1a8,0xb1a831a8,0x32a73072,0x2a3eb3d9,0xb3d92a3e,0x307232a7,0x33642e1f,0xae1fb364,0xae1f3364,0x3364ae1f,0x33d92a3e,0xb2a7b072,0x307232a7,0xaa3eb3d9,
0x34000000,0xb4000000,0x34000000,0xb4000000,0x33d9aa3e,0xb2a73072,0x3072b2a7,0xaa3e33d9,0x3364ae1f,0xae1f3364,0xae1fb364,0x33642e1f,0x32a7b072,0x2a3e33d9,0xb3d9aa3e,0x3072b2a7,
0x31a8b1a8,0x31a831a8,0xb1a831a8,0xb1a8b1a8,0x3072b2a7,0x33d92a3e,0x2a3e33d9,0xb2a73072,0x2e1fb364,0x3364ae1f,0x33642e1f,0x2e1f3364,0x2a3eb3d9,0x3072b2a7,0x32a7b072,0x33d9aa3e,
13312,13312,13312,13312,0x33d9aa3e,0x32a7b072,0x3072b2a7,0x2a3eb3d9,0xae1fb364,0xb364ae1f,0xb3642e1f,0xae1f3364,0xb2a73072,0x2a3e33d9,0x33d92a3e,0x3072b2a7,
0x31a831a8,0x31a8b1a8,0xb1a8b1a8,0xb1a831a8,0x3072b2a7,0xb3d9aa3e,0x2a3e33d9,0x32a7b072,0xb364ae1f,0x2e1f3364,0x2e1fb364,0xb3642e1f,0xaa3e33d9,0x3072b2a7,0xb2a73072,0x33d9aa3e,
0x34000000,0xb4000000,0x34000000,0xb4000000,0xaa3eb3d9,0x307232a7,0xb2a7b072,0x33d92a3e,0xb3642e1f,0x2e1fb364,0x2e1f3364,0xb364ae1f,0x307232a7,0xb3d92a3e,0x2a3eb3d9,0x32a73072,
0x31a8b1a8,0x31a831a8,0xb1a831a8,0xb1a8b1a8,0xb2a7b072,0x2a3eb3d9,0x33d9aa3e,0x307232a7,0xae1f3364,0xb3642e1f,0xb364ae1f,0xae1fb364,0x33d92a3e,0x32a73072,0x307232a7,0x2a3e33d9,
13312,13312,13312,13312,0xaa3eb3d9,0xb072b2a7,0xb2a7b072,0xb3d9aa3e,0x2e1f3364,0x33642e1f,0x3364ae1f,0x2e1fb364,0xb072b2a7,0xb3d92a3e,0xaa3e33d9,0x32a73072,
0x31a831a8,0x31a8b1a8,0xb1a8b1a8,0xb1a831a8,0xb2a7b072,0xaa3e33d9,0x33d9aa3e,0xb072b2a7,0x33642e1f,0xae1fb364,0xae1f3364,0x3364ae1f,0xb3d9aa3e,0x32a73072,0xb072b2a7,0x2a3e33d9,
0x34000000,0xb4000000,0x34000000,0xb4000000,0xb3d92a3e,0x32a7b072,0xb07232a7,0x2a3eb3d9,0x3364ae1f,0xae1f3364,0xae1fb364,0x33642e1f,0xb2a73072,0xaa3eb3d9,0x33d92a3e,0xb07232a7,
0x31a8b1a8,0x31a831a8,0xb1a831a8,0xb1a8b1a8,0xb07232a7,0xb3d9aa3e,0xaa3eb3d9,0x32a7b072,0x2e1fb364,0x3364ae1f,0x33642e1f,0x2e1f3364,0xaa3e33d9,0xb07232a7,0xb2a73072,0xb3d92a3e,
13312,13312,13312,13312,0xb3d92a3e,0xb2a73072,0xb07232a7,0xaa3e33d9,0xae1fb364,0xb364ae1f,0xb3642e1f,0xae1f3364,0x32a7b072,0xaa3eb3d9,0xb3d9aa3e,0xb07232a7,
0x31a831a8,0x31a8b1a8,0xb1a8b1a8,0xb1a831a8,0xb07232a7,0x33d92a3e,0xaa3eb3d9,0xb2a73072,0xb364ae1f,0x2e1f3364,0x2e1fb364,0xb3642e1f,0x2a3eb3d9,0xb07232a7,0x32a7b072,0xb3d92a3e,
0x34000000,0xb4000000,0x34000000,0xb4000000,0x2a3e33d9,0xb072b2a7,0x32a73072,0xb3d9aa3e,0xb3642e1f,0x2e1fb364,0x2e1f3364,0xb364ae1f,0xb072b2a7,0x33d9aa3e,0xaa3e33d9,0xb2a7b072,
0x31a8b1a8,0x31a831a8,0xb1a831a8,0xb1a8b1a8,0x32a73072,0xaa3e33d9,0xb3d92a3e,0xb072b2a7,0xae1f3364,0xb3642e1f,0xb364ae1f,0xae1fb364,0xb3d9aa3e,0xb2a7b072,0xb072b2a7,0xaa3eb3d9,
0x34000000,0x34000000,0x34000000,0x34000000,0x33d9aa3e,0x32a7b072,0x3072b2a7,0x2a3eb3d9,0x3364ae1f,0x2e1fb364,0xae1fb364,0xb364ae1f,0x32a7b072,0xaa3eb3d9,0xb3d9aa3e,0xb07232a7,
0x31a8b1a8,0xb1a8b1a8,0xb1a831a8,0x31a831a8,0x3072b2a7,0xb3d9aa3e,0x2a3e33d9,0x32a7b072,0x2e1fb364,0xb3642e1f,0x33642e1f,0xae1fb364,0x2a3eb3d9,0xb07232a7,0x32a7b072,0xb3d92a3e,
46080,13312,46080,13312,0xaa3eb3d9,0x307232a7,0xb2a7b072,0x33d92a3e,0xae1fb364,0x33642e1f,0xb3642e1f,0x2e1fb364,0xb072b2a7,0x33d9aa3e,0xaa3e33d9,0xb2a7b072,
0xb1a8b1a8,0x31a8b1a8,0x31a831a8,0xb1a831a8,0xb2a7b072,0x2a3eb3d9,0x33d9aa3e,0x307232a7,0xb364ae1f,0xae1fb364,0x2e1fb364,0x3364ae1f,0xb3d9aa3e,0xb2a7b072,0xb072b2a7,0xaa3eb3d9,
0x34000000,0x34000000,0x34000000,0x34000000,0xaa3eb3d9,0xb072b2a7,0xb2a7b072,0xb3d9aa3e,0xb3642e1f,0xae1f3364,0x2e1f3364,0x33642e1f,0x307232a7,0x33d9aa3e,0x2a3eb3d9,0xb2a7b072,
0x31a8b1a8,0xb1a8b1a8,0xb1a831a8,0x31a831a8,0xb2a7b072,0xaa3e33d9,0x33d9aa3e,0xb072b2a7,0xae1f3364,0x3364ae1f,0xb364ae1f,0x2e1f3364,0x33d92a3e,0xb2a7b072,0x307232a7,0xaa3eb3d9,
46080,13312,46080,13312,0xb3d92a3e,0x32a7b072,0xb07232a7,0x2a3eb3d9,0x2e1f3364,0xb364ae1f,0x3364ae1f,0xae1f3364,0x32a7b072,0x2a3e33d9,0xb3d9aa3e,0x3072b2a7,
0xb1a8b1a8,0x31a8b1a8,0x31a831a8,0xb1a831a8,0xb07232a7,0xb3d9aa3e,0xaa3eb3d9,0x32a7b072,0x33642e1f,0x2e1f3364,0xae1f3364,0xb3642e1f,0x2a3eb3d9,0x3072b2a7,0x32a7b072,0x33d9aa3e,
0x34000000,0x34000000,0x34000000,0x34000000,0xb3d92a3e,0xb2a73072,0xb07232a7,0xaa3e33d9,0x3364ae1f,0x2e1fb364,0xae1fb364,0xb364ae1f,0xb2a73072,0x2a3e33d9,0x33d92a3e,0x3072b2a7,
0x31a8b1a8,0xb1a8b1a8,0xb1a831a8,0x31a831a8,0xb07232a7,0x33d92a3e,0xaa3eb3d9,0xb2a73072,0x2e1fb364,0xb3642e1f,0x33642e1f,0xae1fb364,0xaa3e33d9,0x3072b2a7,0xb2a73072,0x33d9aa3e,
46080,13312,46080,13312,0x2a3e33d9,0xb072b2a7,0x32a73072,0xb3d9aa3e,0xae1fb364,0x33642e1f,0xb3642e1f,0x2e1fb364,0x307232a7,0xb3d92a3e,0x2a3eb3d9,0x32a73072,
0xb1a8b1a8,0x31a8b1a8,0x31a831a8,0xb1a831a8,0x32a73072,0xaa3e33d9,0xb3d92a3e,0xb072b2a7,0xb364ae1f,0xae1fb364,0x2e1fb364,0x3364ae1f,0x33d92a3e,0x32a73072,0x307232a7,0x2a3e33d9,
0x34000000,0x34000000,0x34000000,0x34000000,0x2a3e33d9,0x307232a7,0x32a73072,0x33d92a3e,0xb3642e1f,0xae1f3364,0x2e1f3364,0x33642e1f,0xb072b2a7,0xb3d92a3e,0xaa3e33d9,0x32a73072,
0x31a8b1a8,0xb1a8b1a8,0xb1a831a8,0x31a831a8,0x32a73072,0x2a3eb3d9,0xb3d92a3e,0x307232a7,0xae1f3364,0x3364ae1f,0xb364ae1f,0x2e1f3364,0xb3d9aa3e,0x32a73072,0xb072b2a7,0x2a3e33d9,
46080,13312,46080,13312,0x33d9aa3e,0xb2a73072,0x3072b2a7,0xaa3e33d9,0x2e1f3364,0xb364ae1f,0x3364ae1f,0xae1f3364,0xb2a73072,0xaa3eb3d9,0x33d92a3e,0xb07232a7,
0xb1a8b1a8,0x31a8b1a8,0x31a831a8,0xb1a831a8,0x3072b2a7,0x33d92a3e,0x2a3e33d9,0xb2a73072,0x33642e1f,0x2e1f3364,0xae1f3364,0xb3642e1f,0xaa3e33d9,0xb07232a7,0xb2a73072,0xb3d92a3e,
0,304,648,952,576,880,1224,1528,1152,1456,1800,2104,1728,2032,104,408,
1728,2032,104,408,32,336,680,984,608,912,1256,1560,1184,1488,1832,2136,
1184,1488,1832,2136,1760,2064,352,1648,64,1360,72,376,64,1360,72,376,
64,1360,72,376,64,1360,72,376,64,1360,72,376,64,1360,72,376,
1296,1600,1944,2248,1872,2176,248,552,176,480,824,1128,752,1056,1400,1704,
752,1056,1400,1704,1328,1632,1976,2280,1904,2208,928,2224,640,1936,216,520,
640,1936,216,520,144,448,792,1096,720,1024,1368,1672,720,1024,1368,1672,
720,1024,1368,1672,720,1024,1368,1672,720,1024,1368,1672,720,1024,1368,1672,
320,624,968,1272,896,1200,1544,1848,1472,1776,2120,496,2048,208,1504,88,
2048,208,1504,88,1216,16,360,664,288,592,936,1240,864,1168,1512,1816,
864,1168,1512,1816,1440,1744,2088,120,2016,48,392,696,2016,48,392,696,
2016,48,392,696,2016,48,392,696,2016,48,392,696,2016,48,392,696,
1616,1920,2264,1072,2192,784,2080,232,1792,160,504,808,432,736,1080,1384,
432,736,1080,1384,1008,1312,1656,1960,1584,1888,2232,264,2160,192,536,840,
2160,192,536,840,464,768,1112,1416,1040,1344,1688,1992,1040,1344,1688,1992,
1040,1344,1688,1992,1040,1344,1688,1992,1040,1344,1688,1992,1040,1344,1688,1992,
152,456,800,1104,728,1032,1376,1680,1304,1608,1952,2256,1880,2184,256,560,
1880,2184,256,560,184,488,832,1136,760,1064,1408,1712,1336,1640,1984,2288,
1336,1640,1984,2288,1912,2216,1000,2296,712,2008,224,528,712,2008,224,528,
712,2008,224,528,712,2008,224,528,712,2008,224,528,712,2008,224,528,
1448,1752,2096,128,2024,56,400,704,328,632,976,1280,904,1208,1552,1856,
904,1208,1552,1856,1480,1784,2128,568,2056,280,1576,96,1288,24,368,672,
1288,24,368,672,296,600,944,1248,872,1176,1520,1824,872,1176,1520,1824,
872,1176,1520,1824,872,1176,1520,1824,872,1176,1520,1824,872,1176,1520,1824,
472,776,1120,1424,1048,1352,1696,2000,1624,1928,2272,1144,2200,856,2152,240,
2200,856,2152,240,1864,168,512,816,440,744,1088,1392,1016,1320,1664,1968,
1016,1320,1664,1968,1592,1896,2240,272,2168,200,544,848,2168,200,544,848,
2168,200,544,848,2168,200,544,848,2168,200,544,848,2168,200,544,848,
1768,2072,424,1720,136,1432,80,384,8,312,656,960,584,888,1232,1536,
584,888,1232,1536,1160,1464,1808,2112,1736,2040,112,416,40,344,688,992,
40,344,688,992,616,920,1264,1568,1192,1496,1840,2144,1192,1496,1840,2144,
1192,1496,1840,2144,1192,1496,1840,2144,1192,1496,1840,2144,1192,1496,1840,2144,
9920,10176,9792,0,12232,12488,12104,16,10818,10562,10946,32,13130,12874,13258,48,
9794,10050,10242,64,12106,12362,12554,80,10944,10688,10496,96,13256,13000,12808,112,
10244,9924,10116,128,12556,12236,12428,144,10494,10814,10622,160,12806,13126,12934,176,
10118,9798,9990,192,12430,12110,12302,208,10620,10940,10748,224,12932,13252,13060,240,
9992,10248,9864,4608,12304,12560,12176,4624,10746,10490,10874,4640,13058,12802,13186,4656,
9866,10122,10314,4672,12178,12434,12626,4688,10872,10616,10424,4704,13184,12928,12736,4720,
10316,9996,10188,4736,12628,12308,12500,4752,10422,10742,10550,4768,12734,13054,12862,4784,
10190,9870,10062,4800,12502,12182,12374,4816,10548,10868,10676,4832,12860,13180,12988,4848,
10064,10320,9936,1048576,12376,12632,12248,496,10674,10418,10802,480,12986,12730,13114,464,
9938,10194,9810,448,12250,12506,12122,432,10800,10544,10928,416,13112,12856,13240,400,
9812,10068,10260,384,12124,12380,12572,368,10926,10670,10478,352,13238,12982,12790,336,
10262,9942,10134,320,12574,12254,12446,304,10476,10796,10604,288,12788,13108,12916,272,
10136,9816,10008,1048576,12448,12128,12320,5104,10602,10922,10730,5088,12914,13234,13042,5072,
10010,10266,9882,5056,12322,12578,12194,5040,10728,10472,10856,5024,13040,12784,13168,5008,
9884,10140,10332,4992,12196,12452,12644,4976,10854,10598,10406,4960,13166,12910,12718,4944,
10334,10014,10206,4928,12646,12326,12518,4912,10404,10724,10532,4896,12716,13036,12844,4880,
10208,9888,10080,256,12520,12200,12392,1048576,10530,10850,10658,1048576,12842,13162,12970,1048576,
10082,10338,10368,1048576,12394,12650,12680,1048576,10656,10400,10370,1048576,12968,12712,12682,1048576,
9956,10212,10368,1048576,12268,12524,12680,1048576,10782,10526,10370,1048576,13094,12838,12682,1048576,
9830,10086,10368,1048576,12142,12398,12680,1048576,10908,10652,10370,1048576,13220,12964,12682,1048576,
10280,9960,10368,4864,12592,12272,12680,1048576,10458,10778,10370,1048576,12770,13090,12682,1048576,
10154,9834,10368,1048576,12466,12146,12680,1048576,10584,10904,10370,1048576,12896,13216,12682,1048576,
10028,10284,10368,1048576,12340,12596,12680,1048576,10710,10454,10370,1048576,13022,12766,12682,1048576,
9902,10158,10368,1048576,12214,12470,12680,1048576,10836,10580,10370,1048576,13148,12892,12682,1048576,
10352,10032,10368,1048576,12664,12344,12680,1048576,10386,10706,10370,1048576,12698,13018,12682,1048576,
10226,9906,10368,1048576,12538,12218,12680,1048576,10512,10832,10370,1048576,12824,13144,12682,1048576,
10100,10356,10368,1048576,12412,12668,12680,1048576,10638,10382,10370,1048576,12950,12694,12682,1048576,
9974,10230,10368,1048576,12286,12542,12680,1048576,10764,10508,10370,1048576,13076,12820,12682,1048576,
9848,10104,10368,1048576,12160,12416,12680,1048576,10890,10634,10370,1048576,13202,12946,12682,1048576,
10298,9978,10368,1048576,12610,12290,12680,1048576,10440,10760,10370,1048576,12752,13072,12682,1048576,
10172,9852,10368,1048576,12484,12164,12680,1048576,10566,10886,10370,1048576,12878,13198,12682,1048576,
10046,10302,10368,1048576,12358,12614,12680,1048576,10692,10436,10370,1048576,13004,12748,12682,1048576,
10048,10304,10368,1048576,12360,12616,12680,1048576,10690,10434,10370,1048576,13002,12746,12682,1048576,
9922,10178,10368,1048576,12234,12490,12680,1048576,10816,10560,10370,1048576,13128,12872,12682,1048576,
9796,10052,10368,1048576,12108,12364,12680,1048576,10942,10686,10370,1048576,13254,12998,12682,1048576,
10246,9926,10368,1048576,12558,12238,12680,1048576,10492,10812,10370,1048576,12804,13124,12682,1048576,
10120,9800,10368,1048576,12432,12112,12680,1048576,10618,10938,10370,1048576,12930,13250,12682,1048576,
9994,10250,10368,1048576,12306,12562,12680,1048576,10744,10488,10370,1048576,13056,12800,12682,1048576,
9868,10124,10368,1048576,12180,12436,12680,1048576,10870,10614,10370,1048576,13182,12926,12682,1048576,
10318,9998,10368,1048576,12630,12310,12680,1048576,10420,10740,10370,1048576,12732,13052,12682,1048576,
10192,9872,10368,1048576,12504,12184,12680,1048576,10546,10866,10370,1048576,12858,13178,12682,1048576,
10066,10322,10368,1048576,12378,12634,12680,1048576,10672,10416,10370,1048576,12984,12728,12682,1048576,
9940,10196,10368,1048576,12252,12508,12680,1048576,10798,10542,10370,1048576,13110,12854,12682,1048576,
9814,10070,10368,1048576,12126,12382,12680,1048576,10924,10668,10370,1048576,13236,12980,12682,1048576,
10264,9944,10368,1048576,12576,12256,12680,1048576,10474,10794,10370,1048576,12786,13106,12682,1048576,
10138,9818,10368,1048576,12450,12130,12680,1048576,10600,10920,10370,1048576,12912,13232,12682,1048576,
10012,10268,10368,1048576,12324,12580,12680,1048576,10726,10470,10370,1048576,13038,12782,12682,1048576,
9886,10142,10368,1048576,12198,12454,12680,1048576,10852,10596,10370,1048576,13164,12908,12682,1048576,
10336,10016,10368,1048576,12648,12328,12680,1048576,10402,10722,10370,1048576,12714,13034,12682,1048576,
10210,9890,10368,1048576,12522,12202,12680,1048576,10528,10848,10370,1048576,12840,13160,12682,1048576,
10084,10340,10368,1048576,12396,12652,12680,1048576,10654,10398,10370,1048576,12966,12710,12682,1048576,
9958,10214,10368,1048576,12270,12526,12680,1048576,10780,10524,10370,1048576,13092,12836,12682,1048576,
9832,10088,10368,1048576,12144,12400,12680,1048576,10906,10650,10370,1048576,13218,12962,12682,1048576,
10282,9962,10368,1048576,12594,12274,12680,1048576,10456,10776,10370,1048576,12768,13088,12682,1048576,
10156,9836,10368,1048576,12468,12148,12680,1048576,10582,10902,10370,1048576,12894,13214,12682,1048576,
10030,10286,10368,1048576,12342,12598,12680,1048576,10708,10452,10370,1048576,13020,12764,12682,1048576,
9904,10160,10368,1048576,12216,12472,12680,1048576,10834,10578,10370,1048576,13146,12890,12682,1048576,
10354,10034,10368,1048576,12666,12346,12680,1048576,10384,10704,10370,1048576,12696,13016,12682,1048576,
10228,9908,10368,1048576,12540,12220,12680,1048576,10510,10830,10370,1048576,12822,13142,12682,1048576,
10102,10358,10368,1048576,12414,12670,12680,1048576,10636,10380,10370,1048576,12948,12692,12682,1048576,
9976,10232,10368,1048576,12288,12544,12680,1048576,10762,10506,10370,1048576,13074,12818,12682,1048576,
9850,10106,10368,1048576,12162,12418,12680,1048576,10888,10632,10370,1048576,13200,12944,12682,1048576,
10300,9980,10368,1048576,12612,12292,12680,1048576,10438,10758,10370,1048576,12750,13070,12682,1048576,
10174,9854,10368,1048576,12486,12166,12680,1048576,10564,10884,10370,1048576,12876,13196,12682,1048576,
0,512,1536,0,16,528,1552,16,32,544,1568,32,48,560,1584,48,
64,576,1600,64,80,592,1616,80,96,608,1632,96,112,624,1648,112,
128,640,1664,128,144,656,1680,144,160,672,1696,160,176,688,1712,176,
192,704,1728,192,208,720,1744,208,224,736,1760,224,240,752,1776,240,
1536,5120,6144,4608,1552,5136,6160,4624,1568,5152,6176,4640,1584,5168,6192,4656,
1600,5184,6208,4672,1616,5200,6224,4688,1632,5216,6240,4704,1648,5232,6256,4720,
1664,5248,6272,4736,1680,5264,6288,4752,1696,5280,6304,4768,1712,5296,6320,4784,
1728,5312,6336,4800,1744,5328,6352,4816,1760,5344,6368,4832,1776,5360,6384,4848,
3072,4096,3072,0,3088,4592,3568,496,3104,4576,3552,480,3120,4560,3536,464,
3136,4544,3520,448,3152,4528,3504,432,3168,4512,3488,416,3184,4496,3472,400,
3200,4480,3456,384,3216,4464,3440,368,3232,4448,3424,352,3248,4432,3408,336,
3264,4416,3392,320,3280,4400,3376,304,3296,4384,3360,288,3312,4368,3344,272,
3072,8704,7680,4608,3088,9200,8176,5104,3104,9184,8160,5088,3120,9168,8144,5072,
3136,9152,8128,5056,3152,9136,8112,5040,3168,9120,8096,5024,3184,9104,8080,5008,
3200,9088,8064,4992,3216,9072,8048,4976,3232,9056,8032,4960,3248,9040,8016,4944,
3264,9024,8000,4928,3280,9008,7984,4912,3296,8992,7968,4896,3312,8976,7952,4880,
8,768,1792,256,24,784,1808,272,40,800,1824,288,56,816,1840,304,
72,832,1856,320,88,848,1872,336,104,864,1888,352,120,880,1904,368,
136,896,1920,384,152,912,1936,400,168,928,1952,416,184,944,1968,432,
200,960,1984,448,216,976,2000,464,232,992,2016,480,248,1008,2032,496,
1544,5376,6400,4864,1560,5392,6416,4880,1576,5408,6432,4896,1592,5424,6448,4912,
1608,5440,6464,4928,1624,5456,6480,4944,1640,5472,6496,4960,1656,5488,6512,4976,
1672,5504,6528,4992,1688,5520,6544,5008,1704,5536,6560,5024,1720,5552,6576,5040,
1736,5568,6592,5056,1752,5584,6608,5072,1768,5600,6624,5088,1784,5616,6640,5104,
3080,4352,3328,256,3096,4336,3312,240,3112,4320,3296,224,3128,4304,3280,208,
3144,4288,3264,192,3160,4272,3248,176,3176,4256,3232,160,3192,4240,3216,144,
3208,4224,3200,128,3224,4208,3184,112,3240,4192,3168,96,3256,4176,3152,80,
3272,4160,3136,64,3288,4144,3120,48,3304,4128,3104,32,3320,4112,3088,16,
3080,8960,7936,4864,3096,8944,7920,4848,3112,8928,7904,4832,3128,8912,7888,4816,
3144,8896,7872,4800,3160,8880,7856,4784,3176,8864,7840,4768,3192,8848,7824,4752,
3208,8832,7808,4736,3224,8816,7792,4720,3240,8800,7776,4704,3256,8784,7760,4688,
3272,8768,7744,4672,3288,8752,7728,4656,3304,8736,7712,4640,3320,8720,7696,4624,
4608,1024,2048,0,4624,1040,2064,16,4640,1056,2080,32,4656,1072,2096,48,
4672,1088,2112,64,4688,1104,2128,80,4704,1120,2144,96,4720,1136,2160,112,
4736,1152,2176,128,4752,1168,2192,144,4768,1184,2208,160,4784,1200,2224,176,
4800,1216,2240,192,4816,1232,2256,208,4832,1248,2272,224,4848,1264,2288,240,
6144,5632,6656,4608,6160,5648,6672,4624,6176,5664,6688,4640,6192,5680,6704,4656,
6208,5696,6720,4672,6224,5712,6736,4688,6240,5728,6752,4704,6256,5744,6768,4720,
6272,5760,6784,4736,6288,5776,6800,4752,6304,5792,6816,4768,6320,5808,6832,4784,
6336,5824,6848,4800,6352,5840,6864,4816,6368,5856,6880,4832,6384,5872,6896,4848,
7680,3584,2560,0,7696,4080,3056,496,7712,4064,3040,480,7728,4048,3024,464,
7744,4032,3008,448,7760,4016,2992,432,7776,4000,2976,416,7792,3984,2960,400,
7808,3968,2944,384,7824,3952,2928,368,7840,3936,2912,352,7856,3920,2896,336,
7872,3904,2880,320,7888,3888,2864,304,7904,3872,2848,288,7920,3856,2832,272,
7680,8192,7168,4608,7696,8688,7664,5104,7712,8672,7648,5088,7728,8656,7632,5072,
7744,8640,7616,5056,7760,8624,7600,5040,7776,8608,7584,5024,7792,8592,7568,5008,
7808,8576,7552,4992,7824,8560,7536,4976,7840,8544,7520,4960,7856,8528,7504,4944,
7872,8512,7488,4928,7888,8496,7472,4912,7904,8480,7456,4896,7920,8464,7440,4880,
4616,1280,2304,256,4632,1296,2320,272,4648,1312,2336,288,4664,1328,2352,304,
4680,1344,2368,320,4696,1360,2384,336,4712,1376,2400,352,4728,1392,2416,368,
4744,1408,2432,384,4760,1424,2448,400,4776,1440,2464,416,4792,1456,2480,432,
4808,1472,2496,448,4824,1488,2512,464,4840,1504,2528,480,4856,1520,2544,496,
6152,5888,6912,4864,6168,5904,6928,4880,6184,5920,6944,4896,6200,5936,6960,4912,
6216,5952,6976,4928,6232,5968,6992,4944,6248,5984,7008,4960,6264,6000,7024,4976,
6280,6016,7040,4992,6296,6032,7056,5008,6312,6048,7072,5024,6328,6064,7088,5040,
6344,6080,7104,5056,6360,6096,7120,5072,6376,6112,7136,5088,6392,6128,7152,5104,
7688,3840,2816,256,7704,3824,2800,240,7720,3808,2784,224,7736,3792,2768,208,
7752,3776,2752,192,7768,3760,2736,176,7784,3744,2720,160,7800,3728,2704,144,
7816,3712,2688,128,7832,3696,2672,112,7848,3680,2656,96,7864,3664,2640,80,
7880,3648,2624,64,7896,3632,2608,48,7912,3616,2592,32,7928,3600,2576,16,
7688,8448,7424,4864,7704,8432,7408,4848,7720,8416,7392,4832,7736,8400,7376,4816,
7752,8384,7360,4800,7768,8368,7344,4784,7784,8352,7328,4768,7800,8336,7312,4752,
7816,8320,7296,4736,7832,8304,7280,4720,7848,8288,7264,4704,7864,8272,7248,4688,
7880,8256,7232,4672,7896,8240,7216,4656,7912,8224,7200,4640,7928,8208,7184,4624,
4,68,1760,0,12,140,1768,16,20,212,1776,32,28,284,1784,48,
76,356,1832,64,84,428,1840,80,92,500,1848,96,100,572,1856,112,
148,644,1904,128,156,716,1912,144,164,788,1920,160,172,860,1928,176,
220,932,1976,192,228,1004,1984,208,236,1076,1992,224,244,1148,2000,240,
292,1220,2048,1536,300,1292,2056,1552,308,1364,2064,1568,316,1436,2072,1584,
364,1508,2120,1600,372,1580,2128,1616,380,1652,2136,1632,388,1724,2144,1648,
436,1796,2192,1664,444,1868,2200,1680,452,1940,2208,1696,460,2012,2216,1712,
508,2084,2264,1728,516,2156,2272,1744,524,2228,2280,1760,532,2300,2288,1776,
580,0,64,3072,588,8,136,3088,596,16,208,3104,604,24,280,3120,
652,72,352,3136,660,80,424,3152,668,88,496,3168,676,96,568,3184,
724,144,640,3200,732,152,712,3216,740,160,784,3232,748,168,856,3248,
796,216,928,3264,804,224,1000,3280,812,232,1072,3296,820,240,1144,3312,
868,288,1216,1048576,876,296,1288,1048576,884,304,1360,1048576,892,312,1432,1048576,
940,360,1504,1048576,948,368,1576,1048576,956,376,1648,1048576,964,384,1720,1048576,
1012,432,1792,1048576,1020,440,1864,1048576,1028,448,1936,1048576,1036,456,2008,1048576,
1084,504,2080,1048576,1092,512,2152,1048576,1100,520,2224,1048576,1108,528,2296,1048576,
1156,576,1760,8,1164,584,1768,24,1172,592,1776,40,1180,600,1784,56,
1228,648,1832,72,1236,656,1840,88,1244,664,1848,104,1252,672,1856,120,
1300,720,1904,136,1308,728,1912,152,1316,736,1920,168,1324,744,1928,184,
1372,792,1976,200,1380,800,1984,216,1388,808,1992,232,1396,816,2000,248,
1444,864,2048,1544,1452,872,2056,1560,1460,880,2064,1576,1468,888,2072,1592,
1516,936,2120,1608,1524,944,2128,1624,1532,952,2136,1640,1540,960,2144,1656,
1588,1008,2192,1672,1596,1016,2200,1688,1604,1024,2208,1704,1612,1032,2216,1720,
1660,1080,2264,1736,1668,1088,2272,1752,1676,1096,2280,1768,1684,1104,2288,1784,
1732,1152,64,3080,1740,1160,136,3096,1748,1168,208,3112,1756,1176,280,3128,
1804,1224,352,3144,1812,1232,424,3160,1820,1240,496,3176,1828,1248,568,3192,
1876,1296,640,3208,1884,1304,712,3224,1892,1312,784,3240,1900,1320,856,3256,
1948,1368,928,3272,1956,1376,1000,3288,1964,1384,1072,3304,1972,1392,1144,3320,
2020,1440,1216,1048576,2028,1448,1288,1048576,2036,1456,1360,1048576,2044,1464,1432,1048576,
2092,1512,1504,1048576,2100,1520,1576,1048576,2108,1528,1648,1048576,2116,1536,1720,1048576,
2164,1584,1792,1048576,2172,1592,1864,1048576,2180,1600,1936,1048576,2188,1608,2008,1048576,
2236,1656,2080,1048576,2244,1664,2152,1048576,2252,1672,2224,1048576,2260,1680,2296,1048576,
36,1728,1760,4608,44,1736,1768,4624,52,1744,1776,4640,60,1752,1784,4656,
108,1800,1832,4672,116,1808,1840,4688,124,1816,1848,4704,132,1824,1856,4720,
180,1872,1904,4736,188,1880,1912,4752,196,1888,1920,4768,204,1896,1928,4784,
252,1944,1976,4800,260,1952,1984,4816,268,1960,1992,4832,276,1968,2000,4848,
324,2016,2048,6144,332,2024,2056,6160,340,2032,2064,6176,348,2040,2072,6192,
396,2088,2120,6208,404,2096,2128,6224,412,2104,2136,6240,420,2112,2144,6256,
468,2160,2192,6272,476,2168,2200,6288,484,2176,2208,6304,492,2184,2216,6320,
540,2232,2264,6336,548,2240,2272,6352,556,2248,2280,6368,564,2256,2288,6384,
612,32,64,7680,620,40,136,7696,628,48,208,7712,636,56,280,7728,
684,104,352,7744,692,112,424,7760,700,120,496,7776,708,128,568,7792,
756,176,640,7808,764,184,712,7824,772,192,784,7840,780,200,856,7856,
828,248,928,7872,836,256,1000,7888,844,264,1072,7904,852,272,1144,7920,
900,320,1216,1048576,908,328,1288,1048576,916,336,1360,1048576,924,344,1432,1048576,
972,392,1504,1048576,980,400,1576,1048576,988,408,1648,1048576,996,416,1720,1048576,
1044,464,1792,1048576,1052,472,1864,1048576,1060,480,1936,1048576,1068,488,2008,1048576,
1116,536,2080,1048576,1124,544,2152,1048576,1132,552,2224,1048576,1140,560,2296,1048576,
1188,608,1760,4616,1196,616,1768,4632,1204,624,1776,4648,1212,632,1784,4664,
1260,680,1832,4680,1268,688,1840,4696,1276,696,1848,4712,1284,704,1856,4728,
1332,752,1904,4744,1340,760,1912,4760,1348,768,1920,4776,1356,776,1928,4792,
1404,824,1976,4808,1412,832,1984,4824,1420,840,1992,4840,1428,848,2000,4856,
1476,896,2048,6152,1484,904,2056,6168,1492,912,2064,6184,1500,920,2072,6200,
1548,968,2120,6216,1556,976,2128,6232,1564,984,2136,6248,1572,992,2144,6264,
1620,1040,2192,6280,1628,1048,2200,6296,1636,1056,2208,6312,1644,1064,2216,6328,
1692,1112,2264,6344,1700,1120,2272,6360,1708,1128,2280,6376,1716,1136,2288,6392,
1764,1184,64,7688,1772,1192,136,7704,1780,1200,208,7720,1788,1208,280,7736,
1836,1256,352,7752,1844,1264,424,7768,1852,1272,496,7784,1860,1280,568,7800,
1908,1328,640,7816,1916,1336,712,7832,1924,1344,784,7848,1932,1352,856,7864,
1980,1400,928,7880,1988,1408,1000,7896,1996,1416,1072,7912,2004,1424,1144,7928,
2052,1472,1216,1048576,2060,1480,1288,1048576,2068,1488,1360,1048576,2076,1496,1432,1048576,
2124,1544,1504,1048576,2132,1552,1576,1048576,2140,1560,1648,1048576,2148,1568,1720,1048576,
2196,1616,1792,1048576,2204,1624,1864,1048576,2212,1632,1936,1048576,2220,1640,2008,1048576,
2268,1688,2080,1048576,2276,1696,2152,1048576,2284,1704,2224,1048576,2292,1712,2296,1048576,
};
#define ASM_ALL \
"s_load_dwordx2 s[12:13],s[0:1],0\ns_load_dwordx4 s[16:19],s[0:1],8\ns_load_dwordx2 s[36:37],s[0:1],48\ns_load_dwordx2 s[8:9],s[0:1],24\n" \
"s_load_dwordx2 s[20:21],s[0:1],32\ns_load_dwordx2 s[24:25],s[0:1],40\ns_getpc_b64 s[96:97]\n.Lc0_%=:\n" \
"v_and_b32 v1,0xe0,v0\nv_lshlrev_b32 v2,7,v0\ns_lshr_b32 s33,s2,3\ns_mov_b32 s98,.Lend_%=-.Lc0_%=\n" \
"s_mov_b32 s99,0x20000\ns_sub_u32 s95,s33,9\nv_lshl_or_b32 v1,v1,24,v2\ns_lshl_b32 s95,s95,12\n" \
"s_mov_b32 m0,162176\nv_readfirstlane_b32 s3,v0\nbuffer_load_dword v1,s[96:99],s95 offen lds\ns_and_b32 s32,s2,7\n" \
"s_mov_b32 s15,0x20000\ns_mov_b32 s14,2359296\ns_lshr_b32 s3,s3,6\ns_lshl_b32 s34,s32,5\n" \
"v_and_b32 v13,31,v0\nv_bfe_u32 v12,v0,5,1\ns_or_b32 s34,s34,s33\nv_mul_u32_u24 v12,1179648,v12\n" \
"s_lshl_b32 s100,s3,8\nv_lshlrev_b32 v2,4,v0\ns_add_u32 s100,s100,s34\nv_lshl_add_u32 v15,v13,4,v12\n" \
"s_mul_i32 s89,s100,1152\nv_lshl_add_u32 v13,v13,2,v12\nv_mov_b32 v12,v15\nv_and_b32 v1,0x3f0,v2\n" \
"s_mov_b32 s38,134217728\ns_mov_b32 s39,0x20000\ns_cmp_gt_u32 s2,71\ns_cbranch_scc1 .Lnp_%=\n" \
"s_and_b32 s95,s32,3\ns_lshr_b32 s100,s32,2\ns_mul_i32 s95,s95,9\ns_mul_i32 s101,s100,576\n" \
"s_add_u32 s95,s95,s33\ns_mul_i32 s100,s100,1990656\ns_lshl_b32 s95,s95,6\ns_mov_b32 s22,576\n" \
"s_mov_b32 s23,0x20000\ns_mov_b32 s26,1990656\ns_mov_b32 s27,0x20000\ns_waitcnt lgkmcnt(0)\n" \
"s_add_u32 s20,s20,s101\ns_addc_u32 s21,s21,0\ns_add_u32 s24,s24,s100\ns_addc_u32 s25,s25,0\n" \
"buffer_load_dwordx2 v[126:127],off,s[36:39],0 sc1\nv_lshrrev_b32 v120,2,v0\nv_and_b32 v121,3,v0\nv_mul_u32_u24 v122,6912,v120\n" \
"v_lshlrev_b32 v123,3,v120\nv_lshl_add_u32 v121,v121,4,s95\nbuffer_load_dwordx2 v[24:25],v123,s[20:23],0 offen\nv_lshlrev_b32 v120,1,v120\n" \
"v_lshl_add_u32 v123,v122,2,v121\nbuffer_load_ushort v28,v120,s[20:23],0 offen offset:512\nv_add_u32 v121,v121,v122\nbuffer_load_dwordx4 v[80:83],v123,s[24:27],0 offen\n" \
"s_mov_b32 s95,6912\nbuffer_load_dwordx4 v[84:87],v123,s[24:27],s95 offen\ns_mov_b32 s95,13824\nbuffer_load_dwordx4 v[88:91],v123,s[24:27],s95 offen\n" \
"s_mov_b32 s95,20736\nbuffer_load_dwordx4 v[92:95],v123,s[24:27],s95 offen\ns_mov_b32 s95,1769472\nbuffer_load_dwordx4 v[96:99],v121,s[24:27],s95 offen\n" \
"s_branch .Lnq_%=\n.Lnp_%=:\ns_waitcnt lgkmcnt(0)\nbuffer_load_dwordx2 v[126:127],off,s[36:39],0 sc1\n" \
".Lnq_%=:\ns_memrealtime s[30:31]\nbuffer_load_dwordx4 v[224:227],v12,s[12:15],s89 offen\nbuffer_load_dwordx4 v[228:231],v12,s[12:15],s89 offen offset:512\n" \
"buffer_load_dword v232,v13,s[12:15],s89 offen offset:1024\ns_cmp_lt_u32 s3,2\ns_cselect_b64 s[16:17],s[16:17],s[18:19]\ns_mov_b32 s18,1183744\n" \
"s_mov_b32 s19,0x20000\ns_mul_i32 s100,s34,4624\nv_and_b32 v16,0x7f0,v2\ns_lshr_b32 s95,s3,1\n" \
"s_mul_i32 s95,s95,3168\ns_lshl_b32 s88,s3,10\ns_add_u32 s95,s95,s88\ns_add_u32 m0,s95,9792\n" \
"v_add_u32 v17,s100,v16\nbuffer_load_dwordx4 v16,s[16:19],s100 offen lds\nbuffer_load_dwordx4 v16,s[16:19],s100 offen offset:2048 lds\nv_add_u32 v17,4096,v17\n" \
"v_subrev_u32 v18,1183744,v17\ns_add_u32 m0,s95,13888\nv_min_u32 v17,v17,v18\ns_bitcmp1_b32 s3,0\n" \
"s_cselect_b64 exec,63,-1\nbuffer_load_dwordx4 v17,s[16:19],0 offen lds\ns_mov_b64 exec,-1\ns_mov_b32 s6,22528\n" \
"s_mov_b32 s7,0x20000\ns_mov_b32 s95,10240\nbuffer_load_dwordx4 v[246:249],v2,s[4:7],s95 offen\ns_cmp_gt_u32 s2,110\n" \
"s_cbranch_scc1 .LC_%=\ns_cmp_gt_u32 s2,71\ns_cbranch_scc1 .LN_%=\n.LP_%=:\n" \
"v_bfe_u32 v112,v0,3,2\nv_bfe_u32 v113,v0,5,1\nv_lshl_add_u32 v112,v112,8,v113\ns_lshl_b32 s48,s33,2\n" \
"v_lshrrev_b32 v113,3,v0\ns_add_u32 s48,s48,s3\nv_and_b32 v113,6,v113\nv_and_b32 v114,7,v0\n" \
"s_mul_i32 s48,s48,18432\ns_mul_i32 s49,s32,1152\nv_mul_u32_u24 v112,9216,v112\nv_xor_b32 v113,v113,v114\n" \
"s_add_u32 s48,s48,s49\nv_lshl_add_u32 v4,v113,4,v112\ns_mov_b32 s10,18874368\ns_mov_b32 s11,0x20000\n" \
"s_add_u32 s48,s48,1024\ns_add_u32 m0,s88,20224\ns_add_u32 s50,s48,9437184\nbuffer_load_dwordx4 v4,s[8:11],s48 offen lds\n" \
"s_add_u32 m0,s88,24320\nv_add_u32 v5,17408,v4\nbuffer_load_dwordx4 v4,s[8:11],s50 offen lds\nv_add_u32 v6,34816,v4\n" \
"v_add_u32 v7,52224,v4\nv_lshlrev_b32 v158,3,v0\nv_add_u32 v158,161152,v158\ns_mul_i32 s63,s32,9\n" \
"s_add_u32 s63,s63,s33\nv_bfrev_b32 v159,1\nv_cmp_gt_u32 s[64:65],16,v0\ns_lshl_b32 s86,s63,8\n" \
"s_add_u32 s63,s86,8192\nv_cndmask_b32 v159,v159,v2,s[64:65]\ns_mov_b32 s51,4096\ns_add_u32 m0,s88,28416\n" \
"s_waitcnt vmcnt(15)\nv_lshlrev_b32 v128,16,v24\nv_mul_f32 v129,0xbfb8aa3b,v128\nv_exp_f32 v129,v129\n" \
"v_and_b32 v130,0xffff0000,v24\nv_add_f32 v129,1.0,v129\nv_mul_f32 v131,0xbfb8aa3b,v130\nv_lshlrev_b32 v132,16,v25\n" \
"v_rcp_f32 v129,v129\nv_exp_f32 v131,v131\nv_mul_f32 v133,0xbfb8aa3b,v132\nv_exp_f32 v133,v133\n" \
"v_mul_f32 v128,v128,v129\nv_add_f32 v131,1.0,v131\nv_and_b32 v134,0xffff0000,v25\nv_cvt_pk_bf16_f32 v128,v128,v128\n" \
"v_rcp_f32 v131,v131\nv_add_f32 v133,1.0,v133\nv_mul_f32 v135,0xbfb8aa3b,v134\nv_lshlrev_b32 v128,16,v128\n" \
"s_waitcnt vmcnt(14)\nv_lshlrev_b32 v136,16,v28\nv_rcp_f32 v133,v133\nv_exp_f32 v135,v135\n" \
"v_mul_f32 v137,0xbfb8aa3b,v136\nv_mul_f32 v130,v130,v131\nv_exp_f32 v137,v137\nv_cvt_pk_bf16_f32 v130,v130,v130\n" \
"v_mul_f32 v132,v132,v133\nv_add_f32 v135,1.0,v135\nv_lshlrev_b32 v130,16,v130\nv_cvt_pk_bf16_f32 v132,v132,v132\n" \
"v_rcp_f32 v135,v135\nv_add_f32 v137,1.0,v137\nv_lshlrev_b32 v132,16,v132\nv_rcp_f32 v137,v137\n" \
"v_mul_f32 v134,v134,v135\nv_cvt_pk_bf16_f32 v134,v134,v134\nv_mul_f32 v136,v136,v137\nv_lshlrev_b32 v134,16,v134\n" \
"v_cvt_pk_bf16_f32 v136,v136,v136\nv_lshlrev_b32 v136,16,v136\ns_mov_b32 s52,8192\ns_mov_b32 s53,14336\n" \
"s_mul_i32 s57,s3,30720\ns_add_u32 s57,s57,38272\ns_and_b32 s60,s33,7\ns_mul_i32 s92,s3,589824\n" \
"s_add_u32 s92,s92,s49\ns_lshl_b32 s62,s60,7\ns_add_u32 s92,s92,s62\ns_load_dwordx4 s[44:47],s[0:1],72\n" \
"v_mov_b32 v16,-1\nv_mov_b32 v20,0\nv_mov_b32 v17,-1\nv_mov_b32 v21,0\n" \
"v_mov_b32 v18,-1\nv_mov_b32 v22,0\nv_mov_b32 v19,-1\nv_mov_b32 v23,0\n" \
"s_mul_i32 s54,s34,9344\ns_add_u32 s35,s54,524288\ns_add_u32 s55,s35,4096\nv_and_b32 v112,15,v0\n" \
"v_bfe_u32 v113,v0,4,2\nv_mul_u32_u24 v113,589824,v113\nv_lshl_add_u32 v113,v112,4,v113\ns_lshl_b32 s56,s34,8\n" \
"s_add_u32 s56,s56,10289152\ns_cmp_eq_u32 s3,2\ns_cselect_b32 s56,s56,2147483648\nv_add_u32 v113,s56,v113\n" \
"s_add_u32 s58,s35,8192\nv_add_u32 v114,s58,v2\nv_cmp_gt_u32 vcc,68,v0\ns_lshr_b32 s59,s33,3\n" \
"s_lshl_b32 s59,s59,3\nv_cndmask_b32 v113,v113,v114,vcc\ns_add_u32 s59,s59,s32\ns_lshl_b32 s59,s59,3\n" \
"s_add_u32 s59,s59,s60\ns_lshl_b32 s59,s59,7\ns_add_u32 s87,s59,262144\nv_lshrrev_b32 v115,3,v0\n" \
"v_and_b32 v116,0x70,v2\nv_lshl_add_u32 v115,v115,16,v116\nv_add_u32 v115,s87,v115\ns_lshl_b32 s61,s34,4\n" \
"s_add_u32 s61,s61,65536\nv_mov_b32 v116,s61\nv_cmp_gt_u32 vcc,24,v0\nv_and_b32 v118,15,v0\n" \
"v_bfe_u32 v119,v0,4,2\nv_cndmask_b32 v115,v116,v115,vcc\nv_bfrev_b32 v116,1\nv_cmp_gt_u32 vcc,25,v0\n" \
"v_lshlrev_b32 v3,4,v119\nv_lshl_add_u32 v3,v118,6,v3\nv_cndmask_b32 v115,v116,v115,vcc\nv_lshrrev_b32 v15,1,v1\n" \
"v_add_u32 v15,s57,v15\nv_and_b32 v120,0x3c0,v1\nv_bfe_u32 v121,v0,3,2\nv_and_b32 v122,3,v0\n" \
"v_lshlrev_b32 v120,1,v120\nv_lshl_add_u32 v120,v121,5,v120\nv_lshl_add_u32 v120,v122,3,v120\nv_xor_b32 v8,0,v120\n" \
"v_add_u32 v8,s57,v8\nv_xor_b32 v9,32,v120\nv_add_u32 v9,s57,v9\nv_xor_b32 v10,64,v120\n" \
"v_add_u32 v10,s57,v10\nv_xor_b32 v11,96,v120\nv_add_u32 v11,s57,v11\nv_cvt_f32_u32 v120,s34\n" \
"v_lshrrev_b32 v48,1,v118\nv_cvt_f32_u32 v48,v48\nv_and_b32 v49,1,v119\nv_cvt_f32_u32 v49,v49\n" \
"v_mul_f32 v48,0xbe000000,v48\nv_mul_f32 v50,0x3b800000,v120\nv_fma_f32 v49,v49,2.0,v50\nv_mul_f32 v49,v48,v49\n" \
"v_lshrrev_b32 v50,1,v119\nv_and_b32 v51,1,v0\nv_cmp_eq_u32 vcc,v50,v51\nv_mov_b32 v50,0x3e800000\n" \
"v_lshlrev_b32 v51,31,v0\nv_cndmask_b32 v52,0,v50,vcc\nv_xor_b32 v54,v51,v52\nv_cos_f32 v56,v49\n" \
"v_sin_f32 v57,v49\nv_fmamk_f32 v63,v48,0x40800000,v49\nv_pk_mul_f32 v[58:59],v[56:57],v[52:53] op_sel_hi:[1,0]\nv_pk_mul_f32 v[60:61],v[56:57],v[54:55] op_sel_hi:[1,0]\n" \
"v_cvt_pk_f16_f32 v40,v58,-v61\nv_cvt_pk_f16_f32 v44,v59,v60\nv_cos_f32 v62,v63\nv_sin_f32 v63,v63\n" \
"v_fmamk_f32 v57,v48,0x3f800000,v49\nv_pk_mul_f32 v[64:65],v[62:63],v[52:53] op_sel_hi:[1,0]\nv_pk_mul_f32 v[66:67],v[62:63],v[54:55] op_sel_hi:[1,0]\nv_cvt_pk_f16_f32 v41,v64,-v67\n" \
"v_cvt_pk_f16_f32 v45,v65,v66\nv_cos_f32 v56,v57\nv_sin_f32 v57,v57\nv_fmamk_f32 v63,v48,0x40a00000,v49\n" \
"v_pk_mul_f32 v[58:59],v[56:57],v[52:53] op_sel_hi:[1,0]\nv_pk_mul_f32 v[60:61],v[56:57],v[54:55] op_sel_hi:[1,0]\nv_cvt_pk_f16_f32 v42,v58,-v61\nv_cvt_pk_f16_f32 v46,v59,v60\n" \
"v_cos_f32 v62,v63\nv_sin_f32 v63,v63\nv_bfe_u32 v100,v0,1,2\nv_pk_mul_f32 v[64:65],v[62:63],v[52:53] op_sel_hi:[1,0]\n" \
"v_pk_mul_f32 v[66:67],v[62:63],v[54:55] op_sel_hi:[1,0]\nv_cvt_pk_f16_f32 v43,v64,-v67\nv_cvt_pk_f16_f32 v47,v65,v66\nv_and_b32 v101,1,v0\n" \
"v_lshl_add_u32 v100,v101,2,v100\nv_cvt_f32_u32 v100,v100\nv_mul_f32 v101,0x3a000000,v120\nv_fmamk_f32 v100,v100,0x3e000000,v101\n" \
"v_cvt_f32_u32 v101,v119\nv_add_f32 v101,v101,v101\nv_mul_f32 v101,v101,v100\nv_cmp_gt_u32 vcc,8,v118\n" \
"v_cos_f32 v106,v101\nv_sin_f32 v107,v101\nv_cndmask_b32 v102,0,0.5,vcc\nv_sub_f32_e64 v104,0.5,v102\n" \
"v_pk_mul_f32 v[108:109],v[106:107],v[102:103] op_sel_hi:[1,0]\nv_pk_mul_f32 v[110:111],v[106:107],v[104:105] op_sel_hi:[1,0]\nv_cvt_pk_f16_f32 v216,v108,-v109\nv_cvt_pk_f16_f32 v220,v109,v108\n" \
"v_cvt_pk_f16_f32 v217,v110,-v111\nv_cvt_pk_f16_f32 v221,-v111,-v110\nv_fmamk_f32 v107,v100,0x3f800000,v101\nv_cos_f32 v106,v107\n" \
"v_sin_f32 v107,v107\ns_nop 0\nv_pk_mul_f32 v[108:109],v[106:107],v[102:103] op_sel_hi:[1,0]\nv_pk_mul_f32 v[110:111],v[106:107],v[104:105] op_sel_hi:[1,0]\n" \
"v_cvt_pk_f16_f32 v218,v108,-v109\nv_cvt_pk_f16_f32 v222,v109,v108\nv_cvt_pk_f16_f32 v219,v110,-v111\nv_cvt_pk_f16_f32 v223,-v111,-v110\n" \
"v_readfirstlane_b32 s28,v126\nv_readfirstlane_b32 s29,v127\ns_add_u32 s28,s28,1\ns_addc_u32 s29,s29,0\n" \
"v_mov_b32 v168,s28\nv_mov_b32 v169,s29\ns_waitcnt vmcnt(13)\nv_and_b32 v149,0xffff0000,v80\n" \
"v_and_b32 v151,0xffff0000,v81\nv_lshlrev_b32 v148,16,v80\nv_lshlrev_b32 v150,16,v81\nv_pk_mul_f32 v[140:141],v[148:149],v[128:129] op_sel_hi:[1,0]\n" \
"v_pk_mul_f32 v[142:143],v[150:151],v[128:129] op_sel_hi:[1,0]\nv_and_b32 v149,0xffff0000,v82\nv_and_b32 v151,0xffff0000,v83\nv_lshlrev_b32 v148,16,v82\n" \
"v_lshlrev_b32 v150,16,v83\nv_pk_mul_f32 v[144:145],v[148:149],v[128:129] op_sel_hi:[1,0]\nv_pk_mul_f32 v[146:147],v[150:151],v[128:129] op_sel_hi:[1,0]\ns_waitcnt vmcnt(12)\n" \
"v_and_b32 v153,0xffff0000,v84\nv_and_b32 v155,0xffff0000,v85\nv_lshlrev_b32 v152,16,v84\nv_lshlrev_b32 v154,16,v85\n" \
"v_pk_fma_f32 v[140:141],v[152:153],v[130:131],v[140:141] op_sel_hi:[1,0,1]\nv_pk_fma_f32 v[142:143],v[154:155],v[130:131],v[142:143] op_sel_hi:[1,0,1]\ns_waitcnt vmcnt(11)\nv_and_b32 v149,0xffff0000,v88\n" \
"v_and_b32 v151,0xffff0000,v89\nv_lshlrev_b32 v148,16,v88\nv_lshlrev_b32 v150,16,v89\nv_and_b32 v153,0xffff0000,v86\n" \
"v_and_b32 v155,0xffff0000,v87\nv_pk_fma_f32 v[140:141],v[148:149],v[132:133],v[140:141] op_sel_hi:[1,0,1]\nv_pk_fma_f32 v[142:143],v[150:151],v[132:133],v[142:143] op_sel_hi:[1,0,1]\nv_lshlrev_b32 v152,16,v86\n" \
"v_lshlrev_b32 v154,16,v87\nv_pk_fma_f32 v[144:145],v[152:153],v[130:131],v[144:145] op_sel_hi:[1,0,1]\nv_pk_fma_f32 v[146:147],v[154:155],v[130:131],v[146:147] op_sel_hi:[1,0,1]\nv_and_b32 v149,0xffff0000,v90\n" \
"v_and_b32 v151,0xffff0000,v91\nv_lshlrev_b32 v148,16,v90\nv_lshlrev_b32 v150,16,v91\nv_pk_fma_f32 v[144:145],v[148:149],v[132:133],v[144:145] op_sel_hi:[1,0,1]\n" \
"v_pk_fma_f32 v[146:147],v[150:151],v[132:133],v[146:147] op_sel_hi:[1,0,1]\ns_waitcnt vmcnt(10)\nv_and_b32 v153,0xffff0000,v92\nv_and_b32 v155,0xffff0000,v93\n" \
"v_lshlrev_b32 v152,16,v92\nv_lshlrev_b32 v154,16,v93\nv_pk_fma_f32 v[140:141],v[152:153],v[134:135],v[140:141] op_sel_hi:[1,0,1]\nv_pk_fma_f32 v[142:143],v[154:155],v[134:135],v[142:143] op_sel_hi:[1,0,1]\n" \
"s_waitcnt vmcnt(9)\nv_and_b32 v149,0xffff0000,v96\nv_and_b32 v151,0xffff0000,v97\nv_lshlrev_b32 v148,16,v96\n" \
"v_lshlrev_b32 v150,16,v97\nv_and_b32 v153,0xffff0000,v94\nv_and_b32 v155,0xffff0000,v95\nv_pk_fma_f32 v[140:141],v[148:149],v[136:137],v[140:141] op_sel_hi:[1,0,1]\n" \
"v_pk_fma_f32 v[142:143],v[150:151],v[136:137],v[142:143] op_sel_hi:[1,0,1]\nv_lshlrev_b32 v152,16,v94\nv_lshlrev_b32 v154,16,v95\nv_pk_fma_f32 v[144:145],v[152:153],v[134:135],v[144:145] op_sel_hi:[1,0,1]\n" \
"v_pk_fma_f32 v[146:147],v[154:155],v[134:135],v[146:147] op_sel_hi:[1,0,1]\nv_and_b32 v149,0xffff0000,v98\nv_and_b32 v151,0xffff0000,v99\nv_lshlrev_b32 v148,16,v98\n" \
"v_lshlrev_b32 v150,16,v99\nv_pk_fma_f32 v[144:145],v[148:149],v[136:137],v[144:145] op_sel_hi:[1,0,1]\nv_pk_fma_f32 v[146:147],v[150:151],v[136:137],v[146:147] op_sel_hi:[1,0,1]\nv_and_b32 v148,3,v0\n" \
"v_permlane32_swap_b32 v140,v144\nv_permlane32_swap_b32 v141,v145\nv_permlane32_swap_b32 v142,v146\nv_permlane32_swap_b32 v143,v147\n" \
"v_pk_add_f32 v[140:141],v[140:141],v[144:145]\nv_pk_add_f32 v[142:143],v[142:143],v[146:147]\nv_lshrrev_b32 v149,4,v0\nv_and_b32 v150,12,v149\n" \
"v_permlane16_swap_b32 v140,v142\nv_permlane16_swap_b32 v141,v143\nv_lshlrev_b32 v148,5,v148\nv_pk_add_f32 v[140:141],v[140:141],v[142:143]\n" \
"v_mad_u32_u24 v148,v150,24,v148\nv_lshl_add_u32 v148,v149,3,v148\nv_add_f32_dpp v140,v140,v140 row_ror:8 row_mask:0xf bank_mask:0xf bound_ctrl:1\nv_add_f32_dpp v141,v141,v141 row_ror:8 row_mask:0xf bank_mask:0xf bound_ctrl:1\n" \
"v_add_u32 v148,161152,v148\nv_add_f32_dpp v140,v140,v140 row_ror:4 row_mask:0xf bank_mask:0xf bound_ctrl:1\nv_add_f32_dpp v141,v141,v141 row_ror:4 row_mask:0xf bank_mask:0xf bound_ctrl:1\nds_write_b64 v148,v[140:141]\n" \
"s_waitcnt lgkmcnt(0)\ns_barrier\nds_read_b64 v[160:161],v158\nds_read_b64 v[162:163],v158 offset:128\n" \
"ds_read_b64 v[164:165],v158 offset:256\nds_read_b64 v[166:167],v158 offset:384\ns_waitcnt lgkmcnt(1)\nv_pk_add_f32 v[160:161],v[160:161],v[162:163]\n" \
"s_waitcnt lgkmcnt(0)\nv_pk_add_f32 v[164:165],v[164:165],v[166:167]\nv_pk_add_f32 v[170:171],v[160:161],v[164:165]\nbuffer_store_dwordx4 v[168:171],v159,s[36:39],s63 offen sc1\n" \
"buffer_load_dwordx4 v2,s[4:7],0 offen lds\ns_add_u32 m0,s88,32512\ns_nop 0\nbuffer_load_dwordx4 v2,s[4:7],s51 offen lds\n" \
"buffer_load_dwordx4 v[234:237],v1,s[4:7],s52 offen\nbuffer_load_dwordx4 v[238:241],v1,s[4:7],s52 offen offset:1024\nbuffer_load_dwordx4 v[242:245],v2,s[4:7],s53 offen\ns_mov_b32 s52,18432\n" \
"buffer_load_dwordx4 v[252:255],v2,s[4:7],s52 offen\nbuffer_store_dwordx4 v[16:19],v2,s[36:39],s35 offen sc1\nbuffer_store_dwordx4 v[16:19],v2,s[36:39],s55 offen sc1\nbuffer_store_dwordx4 v[16:19],v113,s[36:39],0 offen sc1\n" \
"buffer_store_dwordx4 v[20:23],v115,s[36:39],0 offen sc1\nv_lshrrev_b32 v77,5,v0\nv_bfe_u32 v113,v0,1,5\nv_and_b32 v115,1,v0\n" \
"s_lshl_b32 s63,s3,1\nv_mul_u32_u24 v77,1160,v77\nv_and_b32 v76,31,v0\nv_and_b32 v114,6,v113\n" \
"v_add_u32 v115,s63,v115\nv_mad_u32_u24 v78,v76,36,v77\nv_xor_b32 v114,v114,v115\nv_lshlrev_b32 v113,7,v113\n" \
"v_lshl_add_u32 v113,v114,4,v113\ns_add_u32 s64,s54,2916352\ns_add_u32 s65,s64,4096\nv_bfrev_b32 v114,1\n" \
"v_cmp_gt_u32 vcc,72,v0\ns_lshl_b32 s67,s32,2\ns_add_u32 s66,s64,8192\nv_cndmask_b32 v114,v114,v2,vcc\n" \
"s_add_u32 s67,s67,s3\ns_lshl_b32 s67,s67,5\ns_add_u32 s67,s67,s33\ns_lshl_b32 s67,s67,11\n" \
"s_add_u32 s67,s67,5308416\ns_lshl2_add_u32 s69,s3,161664\ns_mul_i32 s72,s3,2320\nv_mov_b32 v251,s69\n" \
"v_cmp_gt_u32 vcc,32,v0\ns_lshl_b32 s7,s32,11\ns_mov_b32 s93,128\nv_cndmask_b32 v74,2.0,0,vcc\n" \
"s_mov_b32 s73,8192\ns_mov_b32 s74,12288\ns_mov_b32 s75,17408\ns_mov_b32 s76,21504\n" \
"s_mov_b32 s77,16384\ns_mov_b32 s78,25600\ns_add_u32 s7,s7,40960\ns_mov_b32 s81,0x3d800000\n" \
"s_mov_b32 s42,18874368\ns_mov_b32 s43,0x20000\ns_waitcnt vmcnt(7)\nv_lshrrev_b32 v96,1,v234\n" \
"v_lshrrev_b32 v97,1,v235\nv_lshrrev_b32 v98,1,v236\nv_lshrrev_b32 v99,1,v237\nv_add_u32 v96,s72,v96\n" \
"s_waitcnt vmcnt(6)\nv_lshrrev_b32 v100,1,v238\nv_lshrrev_b32 v101,1,v239\nv_lshrrev_b32 v102,1,v240\n" \
"v_lshrrev_b32 v103,1,v241\nv_add_u32 v97,s72,v97\nv_add_u32 v98,s72,v98\nv_add_u32 v99,s72,v99\n" \
"v_add_u32 v100,s72,v100\nv_add_u32 v101,s72,v101\nv_add_u32 v102,s72,v102\nv_add_u32 v103,s72,v103\n" \
"v_lshlrev_b32 v48,16,v224\nv_and_b32 v49,0xffff0000,v224\nv_readlane_b32 s70,v48,0\nv_readlane_b32 s71,v48,32\n" \
"v_and_b32 v51,0xffff0000,v225\nv_mov_b32 v72,s70\nv_mov_b32 v73,s71\nv_lshlrev_b32 v50,16,v225\n" \
"v_and_b32 v53,0xffff0000,v226\nv_permlane32_swap_b32 v72,v73\nv_pk_add_f32 v[48:49],v[48:49],v[72:73] op_sel_hi:[1,0] neg_lo:[0,1] neg_hi:[0,1]\nv_pk_add_f32 v[50:51],v[50:51],v[72:73] op_sel_hi:[1,0] neg_lo:[0,1] neg_hi:[0,1]\n" \
"v_pk_mul_f32 v[68:69],v[48:49],v[48:49]\nv_lshlrev_b32_e64 v52,16,v226\nv_and_b32 v55,0xffff0000,v227\nv_pk_add_f32 v[52:53],v[52:53],v[72:73] op_sel_hi:[1,0] neg_lo:[0,1] neg_hi:[0,1]\n" \
"v_pk_add_f32 v[66:67],v[48:49],v[50:51]\nv_pk_fma_f32 v[68:69],v[50:51],v[50:51],v[68:69]\nv_lshlrev_b32_e64 v54,16,v227\nv_and_b32 v57,0xffff0000,v228\n" \
"v_pk_add_f32 v[54:55],v[54:55],v[72:73] op_sel_hi:[1,0] neg_lo:[0,1] neg_hi:[0,1]\nv_pk_add_f32 v[66:67],v[66:67],v[52:53]\nv_pk_fma_f32 v[68:69],v[52:53],v[52:53],v[68:69]\nv_lshlrev_b32_e64 v56,16,v228\n" \
"v_and_b32 v59,0xffff0000,v229\nv_pk_add_f32 v[56:57],v[56:57],v[72:73] op_sel_hi:[1,0] neg_lo:[0,1] neg_hi:[0,1]\nv_pk_add_f32 v[66:67],v[66:67],v[54:55]\nv_pk_fma_f32 v[68:69],v[54:55],v[54:55],v[68:69]\n" \
"v_lshlrev_b32_e64 v58,16,v229\nv_and_b32 v61,0xffff0000,v230\nv_pk_add_f32 v[58:59],v[58:59],v[72:73] op_sel_hi:[1,0] neg_lo:[0,1] neg_hi:[0,1]\nv_pk_add_f32 v[66:67],v[66:67],v[56:57]\n" \
"v_pk_fma_f32 v[68:69],v[56:57],v[56:57],v[68:69]\nv_lshlrev_b32_e64 v60,16,v230\nv_and_b32 v63,0xffff0000,v231\nv_pk_add_f32 v[60:61],v[60:61],v[72:73] op_sel_hi:[1,0] neg_lo:[0,1] neg_hi:[0,1]\n" \
"v_pk_add_f32 v[66:67],v[66:67],v[58:59]\nv_pk_fma_f32 v[68:69],v[58:59],v[58:59],v[68:69]\nv_lshlrev_b32_e64 v62,16,v231\nv_and_b32 v65,0xffff0000,v232\n" \
"v_pk_add_f32 v[62:63],v[62:63],v[72:73] op_sel_hi:[1,0] neg_lo:[0,1] neg_hi:[0,1]\nv_pk_add_f32 v[66:67],v[66:67],v[60:61]\nv_pk_fma_f32 v[68:69],v[60:61],v[60:61],v[68:69]\nv_lshlrev_b32_e64 v64,16,v232\n" \
"v_pk_add_f32 v[64:65],v[64:65],v[72:73] op_sel_hi:[1,0] neg_lo:[0,1] neg_hi:[0,1]\nv_pk_add_f32 v[66:67],v[66:67],v[62:63]\nv_pk_fma_f32 v[68:69],v[62:63],v[62:63],v[68:69]\nv_pk_add_f32 v[66:67],v[66:67],v[64:65]\n" \
"v_pk_fma_f32 v[68:69],v[64:65],v[64:65],v[68:69]\nv_add_f32 v66,v66,v67\nv_add_f32 v68,v68,v69\ns_mov_b64 s[40:41],s[44:45]\n" \
"s_mov_b64 s[44:45],s[46:47]\nv_permlane16_swap_b32 v66,v68\nv_add_f32 v66,v66,v68\ns_mov_b32 s46,2359296\n" \
"s_mov_b32 s47,0x20000\nv_add_f32_dpp v66,v66,v66 quad_perm:[1,0,3,2] row_mask:0xf bank_mask:0xf bound_ctrl:1\ns_nop 1\nv_add_f32_dpp v66,v66,v66 quad_perm:[2,3,0,1] row_mask:0xf bank_mask:0xf bound_ctrl:1\n" \
"s_nop 1\nv_add_f32_dpp v66,v66,v66 row_half_mirror row_mask:0xf bank_mask:0xf bound_ctrl:1\ns_nop 1\nv_add_f32_dpp v66,v66,v66 row_mirror row_mask:0xf bank_mask:0xf bound_ctrl:1\n" \
"v_mul_f32 v70,0x3ae38e39,v66\nv_mul_f32 v68,0x3ae38e39,v66\ns_nop 1\nv_permlane16_swap_b32 v70,v68\n" \
"v_fma_f32 v68,-v70,v70,v68\nv_max_f32 v68,0,v68\nv_add_f32 v68,0x3727c5ac,v68\nv_rsq_f32 v66,v68\n" \
"v_add_f32 v250,v70,v72\nv_mul_f32 v67,-v70,v66\nv_pk_fma_f32 v[48:49],v[48:49],v[66:67],v[66:67] op_sel:[0,0,1] op_sel_hi:[1,0,1]\nv_pk_fma_f32 v[50:51],v[50:51],v[66:67],v[66:67] op_sel:[0,0,1] op_sel_hi:[1,0,1]\n" \
"v_cvt_pk_f16_f32 v80,v48,v49\nv_cvt_pk_f16_f32 v81,v50,v51\nv_pk_fma_f32 v[52:53],v[52:53],v[66:67],v[66:67] op_sel:[0,0,1] op_sel_hi:[1,0,1]\nv_pk_fma_f32 v[54:55],v[54:55],v[66:67],v[66:67] op_sel:[0,0,1] op_sel_hi:[1,0,1]\n" \
"ds_write2_b32 v78,v80,v81 offset0:0 offset1:1\nv_cvt_pk_f16_f32 v82,v52,v53\nv_cvt_pk_f16_f32 v83,v54,v55\nv_pk_fma_f32 v[56:57],v[56:57],v[66:67],v[66:67] op_sel:[0,0,1] op_sel_hi:[1,0,1]\n" \
"v_pk_fma_f32 v[58:59],v[58:59],v[66:67],v[66:67] op_sel:[0,0,1] op_sel_hi:[1,0,1]\nds_write2_b32 v78,v82,v83 offset0:2 offset1:3\nv_cvt_pk_f16_f32 v84,v56,v57\nv_cvt_pk_f16_f32 v85,v58,v59\n" \
"v_pk_fma_f32 v[60:61],v[60:61],v[66:67],v[66:67] op_sel:[0,0,1] op_sel_hi:[1,0,1]\nv_pk_fma_f32 v[62:63],v[62:63],v[66:67],v[66:67] op_sel:[0,0,1] op_sel_hi:[1,0,1]\nds_write2_b32 v78,v84,v85 offset0:4 offset1:5\nv_cvt_pk_f16_f32 v86,v60,v61\n" \
"v_cvt_pk_f16_f32 v87,v62,v63\nv_pk_fma_f32 v[64:65],v[64:65],v[66:67],v[66:67] op_sel:[0,0,1] op_sel_hi:[1,0,1]\nds_write2_b32 v78,v86,v87 offset0:6 offset1:7\nv_cvt_pk_f16_f32 v88,v64,v65\n" \
"ds_write_b32 v78,v88 offset:32\ns_barrier\nds_read_u16 v16,v246\nds_read_u16 v17,v246 offset:5216\n" \
"ds_read_u16 v18,v246 offset:578\nds_read_u16 v19,v246 offset:5794\nds_read_u16 v20,v246 offset:1156\nds_read_u16 v21,v246 offset:6372\n" \
"ds_read_u16 v22,v246 offset:1734\nds_read_u16 v23,v246 offset:6950\nds_read_u16 v24,v247\ns_waitcnt lgkmcnt(7)\n" \
"v_lshl_or_b32 v16,v17,16,v16\nds_read_u16 v25,v247 offset:5216\nds_read_u16 v26,v247 offset:578\ns_waitcnt lgkmcnt(7)\n" \
"v_lshl_or_b32 v17,v19,16,v18\nv_pk_maximum3_f16 v124,v16,v17,v16\nds_read_u16 v27,v247 offset:5794\nv_pk_minimum3_f16 v125,v16,v17,v16\n" \
"ds_read_u16 v28,v247 offset:1156\ns_waitcnt lgkmcnt(7)\nv_lshl_or_b32 v18,v21,16,v20\nds_read_u16 v29,v247 offset:6372\n" \
"ds_read_u16 v30,v247 offset:1734\ns_waitcnt lgkmcnt(7)\nv_lshl_or_b32 v19,v23,16,v22\nbuffer_store_dwordx4 v[16:19],v2,s[36:39],s64 offen sc1\n" \
"ds_read_u16 v31,v247 offset:6950\nds_read_u16 v104,v248\nv_pk_maximum3_f16 v124,v18,v19,v124\nv_pk_minimum3_f16 v125,v18,v19,v125\n" \
"s_waitcnt lgkmcnt(7)\nv_lshl_or_b32 v24,v25,16,v24\nds_read_u16 v105,v248 offset:5216\nds_read_u16 v106,v248 offset:578\n" \
"s_waitcnt lgkmcnt(7)\nv_lshl_or_b32 v25,v27,16,v26\nv_pk_maximum3_f16 v124,v24,v25,v124\nds_read_u16 v107,v248 offset:5794\n" \
"v_pk_minimum3_f16 v125,v24,v25,v125\nds_read_u16 v108,v248 offset:1156\ns_waitcnt lgkmcnt(7)\nv_lshl_or_b32 v26,v29,16,v28\n" \
"ds_read_u16 v109,v248 offset:6372\nds_read_u16 v110,v248 offset:1734\ns_waitcnt lgkmcnt(7)\nv_lshl_or_b32 v27,v31,16,v30\n" \
"buffer_store_dwordx4 v[24:27],v2,s[36:39],s65 offen sc1\nds_read_u16 v111,v248 offset:6950\nds_read_b128 v[116:119],v113 offset:20224\nv_pk_maximum3_f16 v124,v26,v27,v124\n" \
"v_pk_minimum3_f16 v125,v26,v27,v125\nds_read_b128 v[120:123],v113 offset:24320\ns_waitcnt lgkmcnt(8)\nv_lshl_or_b32 v104,v105,16,v104\n" \
"ds_read_b32 v16,v96\nds_read_b32 v24,v96 offset:1160\nds_read_b32 v17,v97\nds_read_b32 v25,v97 offset:1160\n" \
"s_waitcnt lgkmcnt(10)\nv_lshl_or_b32 v105,v107,16,v106\nds_read_b32 v18,v98\nv_pk_maximum3_f16 v124,v104,v105,v124\n" \
"ds_read_b32 v26,v98 offset:1160\nv_pk_minimum3_f16 v125,v104,v105,v125\nds_read_b32 v19,v99\nds_read_b32 v27,v99 offset:1160\n" \
"ds_read_b32 v20,v100\ns_waitcnt lgkmcnt(13)\nv_lshl_or_b32 v106,v109,16,v108\nds_read_b32 v28,v100 offset:1160\n" \
"ds_read_b32 v21,v101\nds_read_b32 v29,v101 offset:1160\ns_waitcnt lgkmcnt(14)\nv_lshl_or_b32 v107,v111,16,v110\n" \
"buffer_store_dwordx4 v[104:107],v114,s[36:39],s66 offen sc1\ns_waitcnt lgkmcnt(12)\nbuffer_store_dwordx4 v[116:119],v1,s[36:39],s67 offen sc1\nbuffer_store_dwordx4 v[120:123],v1,s[36:39],s67 offen offset:1024 sc1\n" \
"ds_read_b32 v22,v102\nds_read_b32 v30,v102 offset:1160\nds_read_b32 v23,v103\nds_read_b32 v31,v103 offset:1160\n" \
"v_pk_maximum3_f16 v124,v106,v107,v124\nv_and_b32 v124,0x7fff7fff,v124\nv_pk_minimum3_f16 v125,v106,v107,v125\nv_and_b32 v125,0x7fff7fff,v125\n" \
"v_pk_max_u16 v124,v124,v125\nv_lshlrev_b32 v125,16,v124\nv_max_u32 v124,v124,v125\ns_nop 1\n" \
"v_max_u32_dpp v124,v124,v124 quad_perm:[1,0,3,2] row_mask:0xf bank_mask:0xf bound_ctrl:1\ns_nop 1\nv_max_u32_dpp v124,v124,v124 quad_perm:[2,3,0,1] row_mask:0xf bank_mask:0xf bound_ctrl:1\ns_nop 1\n" \
"v_max_u32_dpp v124,v124,v124 row_half_mirror row_mask:0xf bank_mask:0xf bound_ctrl:1\ns_nop 1\nv_max_u32_dpp v124,v124,v124 row_mirror row_mask:0xf bank_mask:0xf bound_ctrl:1\ns_nop 0\n" \
"v_readlane_b32 s68,v124,0\nv_readlane_b32 s100,v124,16\nv_readlane_b32 s101,v124,32\nv_readlane_b32 s31,v124,48\n" \
"s_max_u32 s100,s100,s101\ns_max_u32 s68,s68,s31\ns_max_u32 s68,s68,s100\nv_mov_b32_e64 v124,s68\n" \
"ds_write_b32 v251,v124 offset:32\nv_mov_b32 v114,161664\nbuffer_load_dwordx4 v[48:51],v2,s[36:39],s73 offen sc1\nbuffer_load_dwordx4 v[60:63],v2,s[36:39],s75 offen sc1\n" \
"buffer_load_dwordx4 v[52:55],v2,s[36:39],s74 offen sc1\nbuffer_load_dwordx4 v[64:67],v2,s[36:39],s76 offen sc1\nbuffer_load_dwordx4 v[56:59],v1,s[36:39],s77 offen sc1\ns_lshl3_add_u32 s80,s2,4294615040\n" \
"buffer_load_dwordx4 v[68:71],v1,s[36:39],s78 offen sc1\nv_and_b32 v113,15,v0\ns_mov_b32 s79,0xffffff\nv_bfrev_b32 v115,1\n" \
"s_add_u32 s70,s35,s88\ns_add_u32 s72,s70,8192\ns_cmp_lt_u32 s3,2\ns_cselect_b32 s72,s72,2147483648\n" \
"v_add_u32 v112,28416,v1\ns_add_u32 s71,s70,4096\ns_waitcnt vmcnt(6) lgkmcnt(0)\ns_barrier\n" \
"ds_read_b128 v[116:119],v114 offset:32\nds_read_b128 v[192:195],v112\nds_read_b128 v[196:199],v112 offset:1024\nds_read_b128 v[200:203],v112 offset:2048\n" \
"ds_read_b128 v[204:207],v112 offset:3072\nds_read_b128 v[208:211],v112 offset:4096\nds_read_b128 v[212:215],v112 offset:5120\nds_read_b128 v[32:35],v112 offset:6144\n" \
"ds_read_b128 v[36:39],v112 offset:7168\ns_waitcnt lgkmcnt(8)\nv_max3_u32 v116,v116,v117,v118\nv_max_u32 v116,v116,v119\n" \
"v_and_b32 v119,0xf8,v0\nv_cmp_ne_u32 vcc,192,v119\nv_lshlrev_b32 v118,11,v0\nv_add_u32 v118,s80,v118\n" \
"v_bfrev_b32 v119,1\nv_cndmask_b32 v118,v118,v119,vcc\nv_lshlrev_b32 v116,1,v116\nv_mov_b32 v117,s29\n" \
"v_cmp_eq_u32 vcc,0,v113\nv_bfi_b32 v117,s79,v117,v116\nv_mov_b32 v116,s28\nv_cndmask_b32 v115,v115,v3,vcc\n" \
"v_cmp_gt_u32 vcc,64,v0\nbuffer_store_dwordx2 v[116:117],v118,s[36:39],0 offen sc1\ns_nop 0\nv_cndmask_b32 v115,v115,v3,vcc\n" \
"s_waitcnt vmcnt(6)\nv_cmp_ne_u64 vcc,s[28:29],v[48:49]\ns_waitcnt vmcnt(4)\nv_cmp_ne_u64 s[84:85],s[28:29],v[52:53]\n" \
"s_waitcnt vmcnt(2)\nv_cmp_ne_u64 s[82:83],s[28:29],v[56:57]\ns_or_b64 vcc,vcc,s[84:85]\nv_cmp_ne_u64 s[84:85],s[28:29],v[60:61]\n" \
"s_or_b64 vcc,vcc,s[82:83]\nv_cmp_ne_u64 s[82:83],s[28:29],v[64:65]\ns_or_b64 vcc,vcc,s[84:85]\ns_waitcnt vmcnt(1)\n" \
"v_cmp_ne_u64 s[84:85],s[28:29],v[68:69]\ns_or_b64 vcc,vcc,s[82:83]\ns_or_b64 vcc,vcc,s[84:85]\ns_cbranch_vccnz .LsmP_%=\n" \
".LbmP_%=:\nv_pk_add_f32 v[50:51],v[50:51],v[62:63]\nv_pk_add_f32 v[54:55],v[54:55],v[66:67]\nv_pk_mul_f32 v[62:63],v[50:51],2.0 op_sel_hi:[1,0]\n" \
"v_pk_add_f32 v[58:59],v[58:59],v[70:71]\nv_max3_f32 v112,|v62|,|v63|,0\nv_pk_fma_f32 v[66:67],v[54:55],2.0,v[74:75] op_sel_hi:[1,0,0]\nv_max3_f32 v112,|v66|,|v67|,v112\n" \
"v_pk_fma_f32 v[70:71],v[58:59],2.0,2.0 op_sel_hi:[1,0,0]\nv_max3_f32 v112,|v70|,|v71|,v112\nv_cmp_lt_f32 s[82:83],s81,v112\nv_cmp_ngt_f32 vcc,0x41000000,v112\n" \
"s_cmp_lg_u64 s[82:83],0\ns_cselect_b32 s82,2,0\nv_cvt_pk_f16_f32 v62,v62,v63\ns_cmp_lg_u64 vcc,0\n" \
"s_addc_u32 s82,s82,0\nds_write_b32 v252,v62 offset:62848\nv_cvt_pk_f16_f32 v66,v66,v67\nds_write_b32 v253,v66 offset:62848\n" \
"v_cvt_pk_f16_f32 v70,v70,v71\nv_mov_b32 v113,s82\nds_write_b32 v254,v70 offset:62848\nds_write_b32 v251,v113\n" \
"s_waitcnt lgkmcnt(0)\ns_barrier\nds_read_b128 v[124:127],v114\nds_read_b64 v[96:97],v234 offset:62848\n" \
"ds_read_b64 v[98:99],v235 offset:62848\nds_read_b64 v[100:101],v236 offset:62848\nds_read_b64 v[102:103],v237 offset:62848\nds_read_b64 v[104:105],v238 offset:62848\n" \
"ds_read_b64 v[106:107],v239 offset:62848\nds_read_b64 v[108:109],v240 offset:62848\nds_read_b64 v[110:111],v241 offset:62848\ns_waitcnt lgkmcnt(7)\n" \
"v_or3_b32 v124,v124,v125,v126\nv_or_b32 v124,v124,v127\nv_cmp_ne_u32 vcc,2,v124\ns_nop 0\n" \
"s_cbranch_vccnz .LsxP_%=\n.LbxP_%=:\nv_pk_fma_f16 v80,v16,v96,v97\ns_waitcnt lgkmcnt(4)\n" \
"v_pk_fma_f16 v81,v17,v98,v99\nv_pk_fma_f16 v82,v18,v100,v101\nv_pk_fma_f16 v83,v19,v102,v103\nv_pk_fma_f16 v88,v24,v96,v97\n" \
"s_waitcnt lgkmcnt(2)\nv_pk_fma_f16 v84,v20,v104,v105\nv_pk_fma_f16 v85,v21,v106,v107\nv_pk_fma_f16 v89,v25,v98,v99\n" \
"s_waitcnt lgkmcnt(0)\nv_pk_fma_f16 v86,v22,v108,v109\nv_pk_fma_f16 v90,v26,v100,v101\nv_pk_fma_f16 v87,v23,v110,v111\n" \
"v_pk_fma_f16 v91,v27,v102,v103\nv_pk_fma_f16 v92,v28,v104,v105\nv_pk_fma_f16 v93,v29,v106,v107\nv_pk_fma_f16 v94,v30,v108,v109\n" \
"v_bfe_u32 v233,v0,3,3\nv_mul_u32_u24 v233,8192,v233\nv_lshl_add_u32 v233,v1,3,v233\ns_lshl2_add_u32 s96,s33,s3\n" \
"s_add_u32 s96,s96,138\ns_and_b32 s97,s96,31\ns_lshl_b32 s97,s97,3\ns_cmp_lt_u32 s96,256\n" \
"s_cselect_b32 s96,s96,0x40000\ns_lshr_b32 s96,s96,5\ns_add_u32 s96,s96,s97\ns_lshl_b32 s96,s96,6\n" \
"s_add_u32 s96,s96,s32\ns_mul_i32 s96,s96,1152\ns_mov_b32 m0,162176\nv_pk_fma_f16 v95,v31,v110,v111\n" \
"buffer_load_dword v233,s[8:11],s96 offen lds\nv_mfma_f32_16x16x32_f16 v[48:51],v[80:83],v[192:195],0\nv_mfma_f32_16x16x32_f16 v[52:55],v[80:83],v[196:199],0\nv_mfma_f32_16x16x32_f16 v[56:59],v[84:87],v[32:35],0\n" \
"v_mfma_f32_16x16x32_f16 v[60:63],v[84:87],v[36:39],0\nv_mfma_f32_16x16x32_f16 v[64:67],v[88:91],v[192:195],0\nv_mfma_f32_16x16x32_f16 v[68:71],v[88:91],v[196:199],0\nv_mfma_f32_16x16x32_f16 v[72:75],v[92:95],v[32:35],0\n" \
"v_mfma_f32_16x16x32_f16 v[76:79],v[92:95],v[36:39],0\ns_nop 2\nv_pk_add_f32 v[56:57],v[48:49],v[56:57] neg_lo:[0,1] neg_hi:[0,1]\nv_sub_f32 v58,v50,v58\n" \
"v_pk_fma_f32 v[48:49],v[48:49],2.0,v[56:57] op_sel_hi:[1,0,1] neg_lo:[0,0,1] neg_hi:[0,0,1]\nv_fma_f32 v50,v50,2.0,-v58\nv_pk_add_f32 v[60:61],v[52:53],v[60:61] neg_lo:[0,1] neg_hi:[0,1]\nv_sub_f32 v62,v54,v62\n" \
"v_pk_fma_f32 v[52:53],v[52:53],2.0,v[60:61] op_sel_hi:[1,0,1] neg_lo:[0,0,1] neg_hi:[0,0,1]\nv_pk_add_f32 v[72:73],v[64:65],v[72:73] neg_lo:[0,1] neg_hi:[0,1]\nv_pk_add_f32 v[76:77],v[68:69],v[76:77] neg_lo:[0,1] neg_hi:[0,1]\nv_sub_f32 v74,v66,v74\n" \
"v_sub_f32 v78,v70,v78\nv_fma_f32 v54,v54,2.0,-v62\nv_pk_fma_f32 v[64:65],v[64:65],2.0,v[72:73] op_sel_hi:[1,0,1] neg_lo:[0,0,1] neg_hi:[0,0,1]\nv_fma_f32 v66,v66,2.0,-v74\n" \
"v_pk_fma_f32 v[68:69],v[68:69],2.0,v[76:77] op_sel_hi:[1,0,1] neg_lo:[0,0,1] neg_hi:[0,0,1]\nv_fma_f32 v70,v70,2.0,-v78\nv_cvt_pk_f16_f32 v16,v48,v52\nv_cvt_pk_f16_f32 v17,v49,v53\n" \
"v_cvt_pk_f16_f32 v18,v50,v54\nv_cvt_pk_f16_f32 v24,v64,v68\nv_cvt_pk_f16_f32 v25,v65,v69\nv_cvt_pk_f16_f32 v26,v66,v70\n" \
"v_mfma_f32_16x16x32_f16 v[48:51],v[200:203],v[16:19],0\nv_cvt_pk_f16_f32 v20,v56,v60\nv_cvt_pk_f16_f32 v21,v57,v61\nv_mfma_f32_16x16x32_f16 v[52:55],v[204:207],v[16:19],0\n" \
"v_cvt_pk_f16_f32 v22,v58,v62\nv_cvt_pk_f16_f32 v28,v72,v76\nv_mfma_f32_16x16x32_f16 v[64:67],v[200:203],v[24:27],0\nv_cvt_pk_f16_f32 v29,v73,v77\n" \
"v_cvt_pk_f16_f32 v30,v74,v78\nv_mfma_f32_16x16x32_f16 v[68:71],v[204:207],v[24:27],0\nv_mfma_f32_16x16x32_f16 v[56:59],v[200:203],v[20:23],0\ns_nop 0\n" \
"v_cvt_pk_f16_f32 v80,v48,v52\nv_mfma_f32_16x16x32_f16 v[60:63],v[204:207],v[20:23],0\nv_cvt_pk_f16_f32 v82,v49,v53\nv_cvt_pk_f16_f32 v84,v50,v54\n" \
"v_mfma_f32_16x16x32_f16 v[72:75],v[200:203],v[28:31],0\nv_mfma_f32_16x16x32_f16 v[76:79],v[204:207],v[28:31],0\nv_cvt_pk_f16_f32 v81,v64,v68\nds_write_b64 v255,v[80:81]\n" \
"v_cvt_pk_f16_f32 v83,v65,v69\nds_write_b64 v255,v[82:83] offset:512\nv_cvt_pk_f16_f32 v85,v66,v70\nds_write_b64 v255,v[84:85] offset:1024\n" \
"v_cvt_pk_f16_f32 v86,v56,v60\nv_cvt_pk_f16_f32 v88,v57,v61\nv_cvt_pk_f16_f32 v87,v72,v76\nds_write_b64 v255,v[86:87] offset:256\n" \
"v_cvt_pk_f16_f32 v89,v73,v77\nds_write_b64 v255,v[88:89] offset:768\nv_cvt_pk_f16_f32 v90,v58,v62\nv_cvt_pk_f16_f32 v91,v74,v78\n" \
"ds_write_b64 v255,v[90:91] offset:1280\ns_waitcnt lgkmcnt(0)\ns_barrier\nds_read_b128 v[16:19],v243\n" \
"ds_read_b128 v[20:23],v244\nds_read_b128 v[24:27],v245\ns_waitcnt lgkmcnt(2)\nv_mfma_f32_16x16x32_f16 v[48:51],v[40:43],v[16:19],0\n" \
"v_mfma_f32_16x16x32_f16 v[52:55],v[44:47],v[16:19],0\ns_waitcnt lgkmcnt(0)\nv_mfma_f32_16x16x32_f16 v[56:59],v[40:43],v[20:23],0\nv_mfma_f32_16x16x32_f16 v[60:63],v[44:47],v[20:23],0\n" \
"v_mfma_f32_16x16x32_f16 v[64:67],v[40:43],v[24:27],0\nv_mfma_f32_16x16x32_f16 v[68:71],v[44:47],v[24:27],0\ns_nop 2\nv_cvt_pk_f16_f32 v80,v48,v52\n" \
"v_and_b32_e64 v80,-2,v80\nv_cvt_pk_f16_f32 v81,v49,v53\nv_cvt_pk_f16_f32 v82,v50,v54\nv_cvt_pk_f16_f32 v83,v51,v55\n" \
"buffer_store_dwordx4 v[80:83],v3,s[36:39],s70 offen sc1\nv_cvt_pk_f16_f32 v84,v56,v60\nv_cvt_pk_f16_f32 v85,v57,v61\nv_cvt_pk_f16_f32 v86,v58,v62\n" \
"v_cvt_pk_f16_f32 v87,v59,v63\nv_and_b32_e64 v84,-2,v84\nbuffer_store_dwordx4 v[84:87],v3,s[36:39],s71 offen sc1\nv_cvt_pk_f16_f32 v88,v64,v68\n" \
"v_cvt_pk_f16_f32 v89,v65,v69\nv_cvt_pk_f16_f32 v90,v66,v70\nv_cvt_pk_f16_f32 v91,v67,v71\nv_and_b32 v88,-2,v88\n" \
"buffer_store_dwordx4 v[88:91],v115,s[36:39],s72 offen sc1\ns_cmp_lt_u32 s3,2\ns_cselect_b32 s18,s72,s70\ns_mov_b32 s12,s88\n" \
"s_mov_b32 s13,s89\ns_mov_b32 s14,s87\ns_mov_b32 s16,s70\ns_mov_b32 s17,s71\n" \
"s_load_dwordx2 s[20:21],s[0:1],32\ns_load_dwordx2 s[24:25],s[0:1],40\ns_mov_b32 s22,1152\ns_mov_b32 s26,3981312\n" \
"s_lshr_b32 s95,s86,2\ns_add_u32 s95,s95,2304\ns_waitcnt lgkmcnt(0)\nv_lshrrev_b32 v120,2,v0\n" \
"v_and_b32 v121,3,v0\nv_mul_u32_u24 v122,6912,v120\nv_lshlrev_b32 v123,4,v120\nv_lshl_add_u32 v121,v121,4,s95\n" \
"buffer_load_dwordx4 v[112:115],v123,s[20:23],0 offen\nv_lshlrev_b32 v120,1,v120\nv_lshl_add_u32 v123,v122,3,v121\nbuffer_load_ushort v116,v120,s[20:23],0 offen offset:1024\n" \
"v_add_u32_e64 v121,v121,v122\nbuffer_load_dwordx4 v[16:19],v123,s[24:27],0 offen\ns_mov_b32 s95,6912\nbuffer_load_dwordx4 v[20:23],v123,s[24:27],s95 offen\n" \
"s_mov_b32 s95,13824\nbuffer_load_dwordx4 v[24:27],v123,s[24:27],s95 offen\ns_mov_b32 s95,20736\nbuffer_load_dwordx4 v[28:31],v123,s[24:27],s95 offen\n" \
"s_mov_b32 s95,27648\nbuffer_load_dwordx4 v[32:35],v123,s[24:27],s95 offen\ns_mov_b32 s95,34560\nbuffer_load_dwordx4 v[36:39],v123,s[24:27],s95 offen\n" \
"s_mov_b32 s95,41472\nbuffer_load_dwordx4 v[40:43],v123,s[24:27],s95 offen\ns_mov_b32 s95,48384\nbuffer_load_dwordx4 v[44:47],v123,s[24:27],s95 offen\n" \
"s_mov_b32 s95,3538944\nbuffer_load_dwordx4 v[48:51],v121,s[24:27],s95 offen\ns_lshr_b32 s26,s32,1\ns_cmp_eq_u32 s26,2\n" \
"s_cselect_b32 s26,1.0,0\nv_cmp_gt_u32 vcc,8,v0\nv_mov_b32 v100,s28\nv_mov_b32 v101,s29\n" \
"s_waitcnt vmcnt(10)\nv_lshlrev_b32 v52,16,v112\nv_mul_f32 v53,0xbfb8aa3b,v52\nv_exp_f32 v53,v53\n" \
"v_and_b32 v54,0xffff0000,v112\nv_add_f32 v53,1.0,v53\nv_mul_f32 v55,0xbfb8aa3b,v54\nv_lshlrev_b32 v56,16,v113\n" \
"v_rcp_f32 v53,v53\nv_exp_f32 v55,v55\nv_mul_f32 v57,0xbfb8aa3b,v56\nv_exp_f32 v57,v57\n" \
"v_mul_f32 v52,v52,v53\nv_add_f32 v55,1.0,v55\nv_and_b32 v58,0xffff0000,v113\nv_cvt_pk_bf16_f32 v52,v52,v52\n" \
"v_rcp_f32 v55,v55\nv_add_f32 v57,1.0,v57\nv_mul_f32 v59,0xbfb8aa3b,v58\nv_lshlrev_b32 v52,16,v52\n" \
"v_lshlrev_b32 v60,16,v114\nv_rcp_f32 v57,v57\nv_exp_f32 v59,v59\nv_mul_f32 v61,0xbfb8aa3b,v60\n" \
"v_mul_f32 v54,v54,v55\nv_exp_f32 v61,v61\nv_cvt_pk_bf16_f32 v54,v54,v54\nv_mul_f32 v56,v56,v57\n" \
"v_add_f32 v59,1.0,v59\nv_and_b32 v62,0xffff0000,v114\nv_lshlrev_b32 v54,16,v54\nv_cvt_pk_bf16_f32 v56,v56,v56\n" \
"v_rcp_f32 v59,v59\nv_add_f32 v61,1.0,v61\nv_mul_f32 v63,0xbfb8aa3b,v62\nv_lshlrev_b32 v56,16,v56\n" \
"v_lshlrev_b32 v64,16,v115\nv_rcp_f32 v61,v61\nv_exp_f32 v63,v63\nv_mul_f32 v65,0xbfb8aa3b,v64\n" \
"v_mul_f32 v58,v58,v59\nv_exp_f32 v65,v65\nv_cvt_pk_bf16_f32 v58,v58,v58\nv_mul_f32 v60,v60,v61\n" \
"v_add_f32 v63,1.0,v63\nv_and_b32 v66,0xffff0000,v115\nv_lshlrev_b32 v58,16,v58\nv_cvt_pk_bf16_f32 v60,v60,v60\n" \
"v_rcp_f32 v63,v63\nv_add_f32 v65,1.0,v65\nv_mul_f32 v67,0xbfb8aa3b,v66\nv_lshlrev_b32 v60,16,v60\n" \
"s_waitcnt vmcnt(9)\nv_lshlrev_b32 v68,16,v116\nv_rcp_f32 v65,v65\nv_exp_f32 v67,v67\n" \
"v_mul_f32 v69,0xbfb8aa3b,v68\nv_mul_f32 v62,v62,v63\nv_exp_f32 v69,v69\nv_cvt_pk_bf16_f32 v62,v62,v62\n" \
"v_mul_f32 v64,v64,v65\nv_add_f32 v67,1.0,v67\nv_lshlrev_b32 v62,16,v62\nv_cvt_pk_bf16_f32 v64,v64,v64\n" \
"v_rcp_f32 v67,v67\nv_add_f32 v69,1.0,v69\nv_lshlrev_b32 v64,16,v64\nv_rcp_f32 v69,v69\n" \
"v_mul_f32 v66,v66,v67\nv_cvt_pk_bf16_f32 v66,v66,v66\nv_mul_f32 v68,v68,v69\nv_lshlrev_b32 v66,16,v66\n" \
"v_cvt_pk_bf16_f32 v68,v68,v68\nv_lshlrev_b32 v68,16,v68\ns_waitcnt vmcnt(8)\nv_and_b32 v81,0xffff0000,v16\n" \
"v_and_b32 v83,0xffff0000,v17\nv_lshlrev_b32 v80,16,v16\nv_lshlrev_b32_e64 v82,16,v17\nv_pk_mul_f32 v[70:71],v[80:81],v[52:53] op_sel_hi:[1,0]\n" \
"v_pk_mul_f32 v[72:73],v[82:83],v[52:53] op_sel_hi:[1,0]\nv_and_b32 v81,0xffff0000,v18\nv_and_b32 v83,0xffff0000,v19\nv_lshlrev_b32 v80,16,v18\n" \
"v_lshlrev_b32 v82,16,v19\nv_pk_mul_f32 v[74:75],v[80:81],v[52:53] op_sel_hi:[1,0]\nv_pk_mul_f32 v[76:77],v[82:83],v[52:53] op_sel_hi:[1,0]\ns_waitcnt vmcnt(7)\n" \
"v_and_b32 v85,0xffff0000,v20\nv_and_b32 v87,0xffff0000,v21\nv_lshlrev_b32 v84,16,v20\nv_lshlrev_b32 v86,16,v21\n" \
"v_pk_fma_f32 v[70:71],v[84:85],v[54:55],v[70:71] op_sel_hi:[1,0,1]\nv_pk_fma_f32 v[72:73],v[86:87],v[54:55],v[72:73] op_sel_hi:[1,0,1]\ns_waitcnt vmcnt(6)\nv_and_b32 v81,0xffff0000,v24\n" \
"v_and_b32 v83,0xffff0000,v25\nv_lshlrev_b32 v80,16,v24\nv_lshlrev_b32 v82,16,v25\nv_and_b32 v85,0xffff0000,v22\n" \
"v_and_b32 v87,0xffff0000,v23\nv_pk_fma_f32 v[70:71],v[80:81],v[56:57],v[70:71] op_sel_hi:[1,0,1]\nv_pk_fma_f32 v[72:73],v[82:83],v[56:57],v[72:73] op_sel_hi:[1,0,1]\nv_lshlrev_b32 v84,16,v22\n" \
"v_lshlrev_b32 v86,16,v23\nv_pk_fma_f32 v[74:75],v[84:85],v[54:55],v[74:75] op_sel_hi:[1,0,1]\nv_pk_fma_f32 v[76:77],v[86:87],v[54:55],v[76:77] op_sel_hi:[1,0,1]\nv_and_b32 v81,0xffff0000,v26\n" \
"v_and_b32 v83,0xffff0000,v27\nv_lshlrev_b32 v80,16,v26\nv_lshlrev_b32 v82,16,v27\nv_pk_fma_f32 v[74:75],v[80:81],v[56:57],v[74:75] op_sel_hi:[1,0,1]\n" \
"v_pk_fma_f32 v[76:77],v[82:83],v[56:57],v[76:77] op_sel_hi:[1,0,1]\ns_waitcnt vmcnt(5)\nv_and_b32 v85,0xffff0000,v28\nv_and_b32 v87,0xffff0000,v29\n" \
"v_lshlrev_b32 v84,16,v28\nv_lshlrev_b32 v86,16,v29\nv_pk_fma_f32 v[70:71],v[84:85],v[58:59],v[70:71] op_sel_hi:[1,0,1]\nv_pk_fma_f32 v[72:73],v[86:87],v[58:59],v[72:73] op_sel_hi:[1,0,1]\n" \
"s_waitcnt vmcnt(4)\nv_and_b32 v81,0xffff0000,v32\nv_and_b32 v83,0xffff0000,v33\nv_lshlrev_b32 v80,16,v32\n" \
"v_lshlrev_b32 v82,16,v33\nv_and_b32 v85,0xffff0000,v30\nv_and_b32 v87,0xffff0000,v31\nv_pk_fma_f32 v[70:71],v[80:81],v[60:61],v[70:71] op_sel_hi:[1,0,1]\n" \
"v_pk_fma_f32 v[72:73],v[82:83],v[60:61],v[72:73] op_sel_hi:[1,0,1]\nv_lshlrev_b32 v84,16,v30\nv_lshlrev_b32 v86,16,v31\nv_pk_fma_f32 v[74:75],v[84:85],v[58:59],v[74:75] op_sel_hi:[1,0,1]\n" \
"v_pk_fma_f32 v[76:77],v[86:87],v[58:59],v[76:77] op_sel_hi:[1,0,1]\nv_and_b32 v81,0xffff0000,v34\nv_and_b32 v83,0xffff0000,v35\nv_lshlrev_b32 v80,16,v34\n" \
"v_lshlrev_b32 v82,16,v35\nv_pk_fma_f32 v[74:75],v[80:81],v[60:61],v[74:75] op_sel_hi:[1,0,1]\nv_pk_fma_f32 v[76:77],v[82:83],v[60:61],v[76:77] op_sel_hi:[1,0,1]\ns_waitcnt vmcnt(3)\n" \
"v_and_b32 v85,0xffff0000,v36\nv_and_b32 v87,0xffff0000,v37\nv_lshlrev_b32 v84,16,v36\nv_lshlrev_b32 v86,16,v37\n" \
"v_pk_fma_f32 v[70:71],v[84:85],v[62:63],v[70:71] op_sel_hi:[1,0,1]\nv_pk_fma_f32 v[72:73],v[86:87],v[62:63],v[72:73] op_sel_hi:[1,0,1]\ns_waitcnt vmcnt(2)\nv_and_b32 v81,0xffff0000,v40\n" \
"v_and_b32 v83,0xffff0000,v41\nv_lshlrev_b32 v80,16,v40\nv_lshlrev_b32 v82,16,v41\nv_and_b32 v85,0xffff0000,v38\n" \
"v_and_b32 v87,0xffff0000,v39\nv_pk_fma_f32 v[70:71],v[80:81],v[64:65],v[70:71] op_sel_hi:[1,0,1]\nv_pk_fma_f32 v[72:73],v[82:83],v[64:65],v[72:73] op_sel_hi:[1,0,1]\nv_lshlrev_b32 v84,16,v38\n" \
"v_lshlrev_b32 v86,16,v39\nv_pk_fma_f32 v[74:75],v[84:85],v[62:63],v[74:75] op_sel_hi:[1,0,1]\nv_pk_fma_f32 v[76:77],v[86:87],v[62:63],v[76:77] op_sel_hi:[1,0,1]\nv_and_b32 v81,0xffff0000,v42\n" \
"v_and_b32 v83,0xffff0000,v43\nv_lshlrev_b32 v80,16,v42\nv_lshlrev_b32 v82,16,v43\nv_pk_fma_f32 v[74:75],v[80:81],v[64:65],v[74:75] op_sel_hi:[1,0,1]\n" \
"v_pk_fma_f32 v[76:77],v[82:83],v[64:65],v[76:77] op_sel_hi:[1,0,1]\ns_waitcnt vmcnt(1)\nv_and_b32 v85,0xffff0000,v44\nv_and_b32 v87,0xffff0000,v45\n" \
"v_lshlrev_b32 v84,16,v44\nv_lshlrev_b32 v86,16,v45\nv_pk_fma_f32 v[70:71],v[84:85],v[66:67],v[70:71] op_sel_hi:[1,0,1]\nv_pk_fma_f32 v[72:73],v[86:87],v[66:67],v[72:73] op_sel_hi:[1,0,1]\n" \
"s_waitcnt vmcnt(0)\nv_and_b32 v81,0xffff0000,v48\nv_and_b32 v83,0xffff0000,v49\nv_lshlrev_b32 v80,16,v48\n" \
"v_lshlrev_b32 v82,16,v49\nv_and_b32 v85,0xffff0000,v46\nv_and_b32 v87,0xffff0000,v47\nv_pk_fma_f32 v[70:71],v[80:81],v[68:69],v[70:71] op_sel_hi:[1,0,1]\n" \
"v_pk_fma_f32 v[72:73],v[82:83],v[68:69],v[72:73] op_sel_hi:[1,0,1]\nv_lshlrev_b32 v84,16,v46\nv_lshlrev_b32 v86,16,v47\nv_pk_fma_f32 v[74:75],v[84:85],v[66:67],v[74:75] op_sel_hi:[1,0,1]\n" \
"v_pk_fma_f32 v[76:77],v[86:87],v[66:67],v[76:77] op_sel_hi:[1,0,1]\nv_and_b32 v81,0xffff0000,v50\nv_and_b32 v83,0xffff0000,v51\nv_lshlrev_b32 v80,16,v50\n" \
"v_lshlrev_b32 v82,16,v51\nv_pk_fma_f32 v[74:75],v[80:81],v[68:69],v[74:75] op_sel_hi:[1,0,1]\nv_pk_fma_f32 v[76:77],v[82:83],v[68:69],v[76:77] op_sel_hi:[1,0,1]\nv_and_b32 v80,3,v0\n" \
"v_permlane32_swap_b32 v70,v74\nv_permlane32_swap_b32 v71,v75\nv_permlane32_swap_b32 v72,v76\nv_permlane32_swap_b32 v73,v77\n" \
"v_pk_add_f32 v[70:71],v[70:71],v[74:75]\nv_pk_add_f32 v[72:73],v[72:73],v[76:77]\nv_lshrrev_b32 v81,4,v0\nv_and_b32 v82,12,v81\n" \
"v_permlane16_swap_b32 v70,v72\nv_permlane16_swap_b32 v71,v73\nv_lshlrev_b32 v80,5,v80\nv_pk_add_f32 v[70:71],v[70:71],v[72:73]\n" \
"v_mad_u32_u24 v80,v82,24,v80\nv_lshl_add_u32 v80,v81,3,v80\nv_add_f32_dpp v70,v70,v70 row_ror:8 row_mask:0xf bank_mask:0xf bound_ctrl:1\nv_add_f32_dpp v71,v71,v71 row_ror:8 row_mask:0xf bank_mask:0xf bound_ctrl:1\n" \
"v_add_u32 v80,161152,v80\nv_add_f32_dpp v70,v70,v70 row_ror:4 row_mask:0xf bank_mask:0xf bound_ctrl:1\nv_add_f32_dpp v71,v71,v71 row_ror:4 row_mask:0xf bank_mask:0xf bound_ctrl:1\nds_write_b64 v80,v[70:71]\n" \
"v_and_b32 v80,7,v0\nv_lshlrev_b32 v80,4,v80\nv_add_u32 v80,161152,v80\nv_bfrev_b32 v81,1\n" \
"v_cndmask_b32 v81,v81,v2,vcc\ns_waitcnt lgkmcnt(0)\ns_barrier\nds_read_b128 v[84:87],v80\n" \
"ds_read_b128 v[88:91],v80 offset:128\nds_read_b128 v[92:95],v80 offset:256\nds_read_b128 v[96:99],v80 offset:384\ns_waitcnt lgkmcnt(2)\n" \
"v_pk_add_f32 v[84:85],v[84:85],v[88:89]\nv_pk_add_f32 v[86:87],v[86:87],v[90:91]\ns_waitcnt lgkmcnt(0)\nv_pk_add_f32 v[92:93],v[92:93],v[96:97]\n" \
"v_pk_add_f32 v[94:95],v[94:95],v[98:99]\nv_pk_add_f32 v[84:85],v[84:85],v[92:93]\nv_pk_add_f32 v[86:87],v[86:87],v[94:95]\nv_cvt_pk_bf16_f32 v88,v84,v85\n" \
"v_cvt_pk_bf16_f32 v90,v86,v87\nv_and_b32 v85,0xffff0000,v88\nv_and_b32 v87,0xffff0000,v90\nv_lshlrev_b32 v84,16,v88\n" \
"v_lshlrev_b32 v86,16,v90\nv_add_f32 v84,s26,v84\nv_add_f32 v85,s26,v85\nv_add_f32 v86,s26,v86\n" \
"v_add_f32 v87,s26,v87\ns_lshr_b32 s26,s86,1\nv_cvt_pk_bf16_f32 v102,v84,v85\nv_cvt_pk_bf16_f32 v103,v86,v87\n" \
"s_add_u32 s26,s26,26624\nbuffer_store_dwordx4 v[100:103],v81,s[36:39],s26 offen sc1\ns_and_b32 s22,s33,7\ns_and_b32 s24,s32,1\n" \
"s_lshl3_add_u32 s22,s24,s22\ns_bfe_u32 s24,s33,0x10002\ns_lshl1_add_u32 s24,s32,s24\ns_mul_i32 s19,s24,18\n" \
"s_and_b32 s24,s33,3\ns_lshl2_add_u32 s23,s24,s19\ns_lshl_b32 s24,s3,5\ns_lshr_b32 s20,s33,3\n" \
"s_lshr_b32 s21,s32,1\ns_add_u32 s54,s14,s24\ns_lshl2_add_u32 s24,s20,s21\ns_add_u32 s23,s23,s3\n" \
"s_lshl2_add_u32 s24,s24,s3\ns_add_u32 s23,s23,1\ns_lshl_b32 s24,s24,9\ns_lshl_b32 s23,s23,9\n" \
"v_bfe_u32 v127,v0,3,3\nv_mul_u32_u24 v127,8192,v127\nv_lshl_add_u32 v127,v1,3,v127\ns_lshl2_add_u32 s95,s33,s3\n" \
"s_add_u32 s95,s95,190\ns_and_b32 s100,s95,31\ns_lshl_b32 s100,s100,3\ns_cmp_lt_u32 s95,256\n" \
"s_cselect_b32 s95,s95,0x40000\ns_lshr_b32 s95,s95,5\ns_add_u32 s95,s95,s100\ns_lshl_b32 s95,s95,6\n" \
"s_add_u32 s95,s95,s32\ns_mul_i32 s95,s95,1152\ns_mov_b32 m0,162176\ns_add_u32 s101,s92,0\n" \
"buffer_load_dword v127,s[8:11],s95 offen lds\ns_add_u32 s24,s24,262144\ns_sub_u32 s59,s24,s23\ns_lshr_b32 s25,s3,1\n" \
"s_mul_i32 s24,s21,1152\ns_mul_i32 s25,s25,576\ns_add_u32 s24,s24,s25\ns_lshl4_add_u32 s24,s24,s22\n" \
"s_and_b32 s25,s3,1\ns_lshl_b32 s24,s24,8\ns_lshl_b32 s65,s25,17\ns_add_u32 s65,s65,s24\n" \
"s_lshl_b32 s25,s25,20\ns_add_u32 s24,s24,s25\ns_add_u32 s60,s24,655488\ns_add_u32 s61,s24,786432\n" \
"s_add_u32 s62,s24,786560\ns_add_u32 m0,s57,0\ns_add_u32 s31,s92,9437184\nbuffer_load_dwordx4 v4,s[8:11],s101 offen lds\n" \
"s_add_u32 s63,s24,917504\ns_add_u32 s64,s24,917632\ns_mul_i32 s52,s20,4505600\ns_lshl2_add_u32 s24,s21,s3\n" \
"s_add_u32 s52,s52,12123648\ns_mul_i32 s24,s24,9216\ns_add_u32 s24,s24,s52\ns_add_u32 s68,s24,819712\n" \
"s_lshl_b32 s24,s22,14\nbuffer_load_dwordx4 v5,s[8:11],s101 offen offset:1024 lds\ns_add_u32 s24,s24,s52\ns_lshl2_add_u32 s25,s21,s3\n" \
"s_add_u32 s24,s24,967168\ns_lshl_b32 s25,s25,8\ns_add_u32 s79,s25,s24\ns_add_u32 s25,s21,4\n" \
"s_and_b32 s25,s25,3\ns_add_u32 s24,s24,s12\ns_lshl_b32 s25,s25,12\nbuffer_load_dwordx4 v6,s[8:11],s101 offen offset:2048 lds\n" \
"s_add_u32 s25,s25,s24\ns_sub_u32 s74,s25,819200\ns_add_u32 s25,s21,3\ns_and_b32 s25,s25,3\n" \
"s_lshl_b32 s25,s25,12\ns_add_u32 s25,s25,s24\ns_sub_u32 s71,s25,2048000\ns_sub_u32 s76,s25,1638400\n" \
"s_add_u32 s25,s21,2\nbuffer_load_dwordx4 v7,s[8:11],s101 offen offset:3072 lds\ns_and_b32 s25,s25,3\ns_lshl_b32 s25,s25,12\n" \
"s_add_u32 s25,s25,s24\ns_sub_u32 s72,s25,1638400\ns_sub_u32 s77,s25,1638400\ns_add_u32 s25,s21,1\n" \
"s_and_b32 s25,s25,3\ns_lshl_b32 s25,s25,12\ns_add_u32 s25,s25,s24\ns_add_u32 m0,s57,4096\n" \
"s_add_u32 s96,s92,73728\nbuffer_load_dwordx4 v4,s[8:11],s31 offen lds\ns_sub_u32 s73,s25,1638400\ns_sub_u32 s78,s25,1228800\n" \
"s_lshl2_add_u32 s26,s33,s3\ns_and_b32 s24,s26,15\ns_and_b32 s25,s26,8\ns_lshl2_add_u32 s24,s24,192\n" \
"s_mul_i32 s25,s25,28\ns_add_u32 s25,s25,s24\ns_sub_u32 s25,s25,32\nbuffer_load_dwordx4 v5,s[8:11],s31 offen offset:1024 lds\n" \
"s_bitcmp1_b32 s26,4\ns_cselect_b32 s24,s25,s24\ns_and_b32 s25,s26,31\ns_lshl2_add_u32 s25,s25,448\n" \
"s_bitcmp1_b32 s26,5\ns_cselect_b32 s24,s25,s24\ns_lshr_b32 s25,s26,6\ns_lshl1_add_u32 s25,s21,s25\n" \
"s_mul_i32 s25,s25,576\nbuffer_load_dwordx4 v6,s[8:11],s31 offen offset:2048 lds\ns_add_u32 s24,s24,s25\ns_and_b32 s25,s32,1\n" \
"s_lshl1_add_u32 s24,s24,s25\nv_and_b32 v112,3,v0\ns_lshl_b32 s24,s24,11\ns_bfe_u32 s25,s33,131072\n" \
"s_mul_i32 s27,s3,4896\nv_and_b32 v113,12,v0\nv_lshlrev_b32 v112,2,v112\nbuffer_load_dwordx4 v7,s[8:11],s31 offen offset:3072 lds\n" \
"s_add_u32 s24,s24,s25\ns_add_u32 s27,s27,9216\nv_max_u32 v112,v112,v113\ns_mul_i32 s48,s20,589824\n" \
"s_add_u32 s89,s24,1\nv_mov_b32 v114,1048576\nv_mov_b32 v113,s27\nv_cmp_gt_u32 vcc,12,v112\n" \
"s_add_u32 m0,s57,8192\ns_add_u32 s97,s92,9510912\nbuffer_load_dwordx4 v4,s[8:11],s96 offen lds\ns_lshl4_add_u32 s48,s12,s48\n" \
"s_lshl_b32 s86,s21,11\ns_lshl2_add_u32 s24,s2,s3\nv_and_b32 v63,0xffff0000,v224\nv_and_b32 v65,0xffff0000,v225\n" \
"v_and_b32 v67,0xffff0000,v226\nv_and_b32 v69,0xffff0000,v227\nv_and_b32 v71,0xffff0000,v228\nbuffer_load_dwordx4 v5,s[8:11],s96 offen offset:1024 lds\n" \
"v_and_b32 v73,0xffff0000,v229\nv_and_b32 v75,0xffff0000,v230\nv_and_b32 v77,0xffff0000,v231\nv_and_b32 v79,0xffff0000,v232\n" \
"v_cndmask_b32 v112,v114,v113,vcc\nv_lshlrev_b32 v62,16,v224\nv_lshlrev_b32 v64,16,v225\nv_lshlrev_b32 v66,16,v226\n" \
"buffer_load_dwordx4 v6,s[8:11],s96 offen offset:2048 lds\nv_lshlrev_b32 v68,16,v227\nv_lshlrev_b32 v70,16,v228\nv_lshlrev_b32 v72,16,v229\n" \
"v_lshlrev_b32 v74,16,v230\nv_lshlrev_b32 v76,16,v231\nv_lshlrev_b32 v78,16,v232\ns_add_u32 s48,s48,9764864\n" \
"s_add_u32 s65,s65,2097152\ns_lshl_b32 s67,s21,7\nbuffer_load_dwordx4 v7,s[8:11],s96 offen offset:3072 lds\ns_lshl_b32 s85,s21,17\n" \
"s_sub_u32 s86,s14,s86\ns_lshl_b32 s24,s24,8\nv_pk_add_f32 v[62:63],v[62:63],v[250:251] op_sel_hi:[1,0] neg_lo:[0,1] neg_hi:[0,1]\nv_pk_add_f32 v[64:65],v[64:65],v[250:251] op_sel_hi:[1,0] neg_lo:[0,1] neg_hi:[0,1]\n" \
"v_pk_add_f32 v[66:67],v[66:67],v[250:251] op_sel_hi:[1,0] neg_lo:[0,1] neg_hi:[0,1]\nv_pk_add_f32 v[68:69],v[68:69],v[250:251] op_sel_hi:[1,0] neg_lo:[0,1] neg_hi:[0,1]\nv_pk_add_f32 v[70:71],v[70:71],v[250:251] op_sel_hi:[1,0] neg_lo:[0,1] neg_hi:[0,1]\ns_add_u32 m0,s57,12288\n" \
"s_add_u32 s100,s92,9584640\nbuffer_load_dwordx4 v4,s[8:11],s97 offen lds\nv_pk_add_f32 v[72:73],v[72:73],v[250:251] op_sel_hi:[1,0] neg_lo:[0,1] neg_hi:[0,1]\nv_pk_add_f32 v[74:75],v[74:75],v[250:251] op_sel_hi:[1,0] neg_lo:[0,1] neg_hi:[0,1]\n" \
"v_pk_add_f32 v[76:77],v[76:77],v[250:251] op_sel_hi:[1,0] neg_lo:[0,1] neg_hi:[0,1]\nv_pk_add_f32 v[78:79],v[78:79],v[250:251] op_sel_hi:[1,0] neg_lo:[0,1] neg_hi:[0,1]\nv_mov_b64 v[16:17],v[192:193]\nv_mov_b64 v[18:19],v[194:195]\n" \
"v_mov_b64 v[20:21],v[196:197]\nv_mov_b64 v[22:23],v[198:199]\nbuffer_load_dwordx4 v5,s[8:11],s97 offen offset:1024 lds\nv_mov_b64 v[24:25],v[208:209]\n" \
"v_mov_b64 v[26:27],v[210:211]\nv_mov_b64 v[28:29],v[212:213]\nv_mov_b64 v[30:31],v[214:215]\nv_mov_b64 v[32:33],v[216:217]\n" \
"v_mov_b64 v[34:35],v[218:219]\nv_mov_b64 v[36:37],v[220:221]\nv_mov_b64 v[38:39],v[222:223]\nv_mov_b64 v[40:41],v[200:201]\n" \
"buffer_load_dwordx4 v6,s[8:11],s97 offen offset:2048 lds\nv_mov_b64 v[42:43],v[202:203]\nv_mov_b64 v[44:45],v[204:205]\nv_mov_b64 v[46:47],v[206:207]\n" \
"v_mov_b64 v[56:57],v[242:243]\nv_mov_b64 v[58:59],v[244:245]\nv_mov_b32 v60,v249\nv_mov_b32 v61,v250\n" \
"v_add_u32 v48,v234,v112\nv_add_u32 v49,v235,v112\nbuffer_load_dwordx4 v7,s[8:11],s97 offen offset:3072 lds\nv_add_u32 v50,v236,v112\n" \
"v_add_u32 v51,v237,v112\nv_add_u32 v52,v238,v112\nv_add_u32 v53,v239,v112\nv_add_u32 v54,v240,v112\n" \
"v_add_u32_e64 v55,v241,v112\ns_add_u32 s49,s48,4096\ns_add_u32 s50,s48,8192\ns_add_u32 s51,s48,12288\n" \
"s_add_u32 m0,s57,16384\ns_add_u32 s53,s52,3276800\ns_add_u32 s83,s52,2048000\ns_mov_b32 s55,0xbdd2d3e7\n" \
"s_mul_i32 s56,s3,9568\ns_lshl_b32 s58,s3,13\ns_add_u32 s75,s59,196608\ns_add_u32 s66,s65,128\n" \
"s_add_u32 s67,s67,163200\ns_add_u32 s69,s68,3072\ns_add_u32 s70,s68,6144\ns_add_u32 s80,s79,4096\n" \
"s_add_u32 s81,s79,8192\ns_add_u32 s82,s79,12288\ns_and_b32 s84,s33,1\ns_add_u32 s85,s85,65536\n" \
"s_add_u32 s87,s12,65536\ns_add_u32 s88,s74,819200\ns_add_u32 s86,s86,0x10000\ns_mov_b32 s90,7405568\n" \
"s_add_u32 s91,s24,69206016\ns_sub_u32 s98,s57,s12\ns_mov_b32 s99,s12\ns_add_u32 s95,s92,147456\n" \
"buffer_load_dwordx4 v4,s[8:11],s95 offen lds\ns_add_u32 s101,s92,221184\nbuffer_load_dwordx4 v5,s[8:11],s95 offen offset:1024 lds\nbuffer_load_dwordx4 v6,s[8:11],s95 offen offset:2048 lds\n" \
"buffer_load_dwordx4 v7,s[8:11],s95 offen offset:3072 lds\ns_add_u32 m0,s57,20480\ns_nop 0\nbuffer_load_dwordx4 v4,s[8:11],s100 offen lds\n" \
"buffer_load_dwordx4 v5,s[8:11],s100 offen offset:1024 lds\nbuffer_load_dwordx4 v6,s[8:11],s100 offen offset:2048 lds\nbuffer_load_dwordx4 v7,s[8:11],s100 offen offset:3072 lds\ns_add_u32 m0,s57,24576\n" \
"s_nop 0\nbuffer_load_dwordx4 v4,s[8:11],s101 offen lds\nbuffer_load_dwordx4 v5,s[8:11],s101 offen offset:1024 lds\nbuffer_load_dwordx4 v6,s[8:11],s101 offen offset:2048 lds\n" \
"buffer_load_dwordx4 v7,s[8:11],s101 offen offset:3072 lds\nbuffer_load_dwordx4 v[104:107],v1,s[36:39],s7 offen sc1\nbuffer_load_dwordx4 v[108:111],v1,s[36:39],s7 offen offset:1024 sc1\ns_waitcnt vmcnt(1)\n" \
"v_xor_b32 v112,s29,v105\nv_xor_b32 v113,s29,v107\ns_waitcnt vmcnt(0)\nv_xor_b32 v114,s29,v109\n" \
"v_xor_b32 v115,s29,v111\nv_or3_b32 v112,v112,v113,v114\nv_xor_b32 v113,s28,v104\nv_xor_b32 v114,s28,v106\n" \
"v_or_b32 v112,v112,v115\nv_xor_b32 v115,s28,v108\nv_lshl_or_b32 v112,v112,8,v113\nv_xor_b32 v113,s28,v110\n" \
"v_or3_b32 v112,v112,v114,v115\nv_or_b32 v112,v112,v113\nv_cmp_ne_u32 vcc,0,v112\ns_nop 0\n" \
"s_cbranch_vccnz .LsrP_%=\n.LbrP_%=:\nv_min_u32 v120,2288,v2\ns_mov_b32 s5,26624\n" \
"v_max3_u32 v112,v105,v107,v109\nv_max_u32 v112,v112,v111\ns_mov_b32 s6,31232\nv_or_b32 v102,s2,v0\n" \
"v_max_u32_dpp v112,v112,v112 quad_perm:[1,0,3,2] row_mask:0xf bank_mask:0xf bound_ctrl:1\nv_bfrev_b32 v103,1\nv_cmp_eq_u32 vcc,0,v102\nv_max_u32_dpp v112,v112,v112 quad_perm:[2,3,0,1] row_mask:0xf bank_mask:0xf bound_ctrl:1\n" \
"v_mov_b32 v100,s28\nv_cndmask_b32 v102,v103,v102,vcc\nv_max_u32_dpp v112,v112,v112 row_half_mirror row_mask:0xf bank_mask:0xf bound_ctrl:1\nv_mov_b32 v101,s29\n" \
"buffer_store_dwordx2 v[100:101],v102,s[36:39],0 offen sc1\nv_max_u32_dpp v112,v112,v112 row_mirror row_mask:0xf bank_mask:0xf bound_ctrl:1\nds_read_b64_tr_b16 a[0:1],v8\nv_readlane_b32 s94,v112,0\n" \
"v_readlane_b32 s31,v112,16\nv_readlane_b32 s96,v112,32\nv_readlane_b32 s97,v112,48\ns_max_u32 s31,s31,s96\n" \
"s_max_u32 s94,s94,s97\ns_max_u32 s94,s94,s31\ns_lshr_b32 s94,s94,24\ns_max_u32 s94,s94,64\n" \
"s_min_u32 s94,s94,250\nds_read_b64_tr_b16 a[4:5],v9\nds_read_b64_tr_b16 a[8:9],v10\nds_read_b64_tr_b16 a[12:13],v11\n" \
"ds_read_b64_tr_b16 a[16:17],v8 offset:2048\nds_read_b64_tr_b16 a[20:21],v9 offset:2048\nds_read_b64_tr_b16 a[24:25],v10 offset:2048\nds_read_b64_tr_b16 a[28:29],v11 offset:2048\n" \
"s_add_u32 s95,s92,9658368\ns_add_u32 m0,s57,0\nds_read_b64_tr_b16 a[2:3],v8 offset:4096\ns_waitcnt lgkmcnt(1)\n" \
"buffer_load_dwordx4 v4,s[8:11],s95 offen lds\nbuffer_load_dwordx4 v5,s[8:11],s95 offen offset:1024 lds\nbuffer_load_dwordx4 v6,s[8:11],s95 offen offset:2048 lds\nbuffer_load_dwordx4 v7,s[8:11],s95 offen offset:3072 lds\n" \
"ds_read_b64_tr_b16 a[6:7],v9 offset:4096\nds_read_b64_tr_b16 a[10:11],v10 offset:4096\nds_read_b64_tr_b16 a[14:15],v11 offset:4096\nds_read_b64_tr_b16 a[18:19],v8 offset:6144\n" \
"ds_read_b64_tr_b16 a[22:23],v9 offset:6144\nds_read_b64_tr_b16 a[26:27],v10 offset:6144\nds_read_b64_tr_b16 a[30:31],v11 offset:6144\ns_add_u32 s100,s92,294912\n" \
"s_add_u32 m0,s57,4096\nds_read_b64_tr_b16 a[32:33],v8 offset:8192\ns_waitcnt lgkmcnt(1)\nbuffer_load_dwordx4 v4,s[8:11],s100 offen lds\n" \
"buffer_load_dwordx4 v5,s[8:11],s100 offen offset:1024 lds\nbuffer_load_dwordx4 v6,s[8:11],s100 offen offset:2048 lds\nbuffer_load_dwordx4 v7,s[8:11],s100 offen offset:3072 lds\nds_read_b64_tr_b16 a[36:37],v9 offset:8192\n" \
"ds_read_b64_tr_b16 a[40:41],v10 offset:8192\nds_read_b64_tr_b16 a[44:45],v11 offset:8192\nds_read_b64_tr_b16 a[48:49],v8 offset:10240\nds_read_b64_tr_b16 a[52:53],v9 offset:10240\n" \
"ds_read_b64_tr_b16 a[56:57],v10 offset:10240\nds_read_b64_tr_b16 a[60:61],v11 offset:10240\ns_add_u32 s101,s92,9732096\ns_add_u32 m0,s57,8192\n" \
"ds_read_b64_tr_b16 a[34:35],v8 offset:12288\ns_waitcnt lgkmcnt(1)\nbuffer_load_dwordx4 v4,s[8:11],s101 offen lds\nbuffer_load_dwordx4 v5,s[8:11],s101 offen offset:1024 lds\n" \
"buffer_load_dwordx4 v6,s[8:11],s101 offen offset:2048 lds\nbuffer_load_dwordx4 v7,s[8:11],s101 offen offset:3072 lds\nds_read_b64_tr_b16 a[38:39],v9 offset:12288\nds_read_b64_tr_b16 a[42:43],v10 offset:12288\n" \
"ds_read_b64_tr_b16 a[46:47],v11 offset:12288\nds_read_b64_tr_b16 a[50:51],v8 offset:14336\nds_read_b64_tr_b16 a[54:55],v9 offset:14336\nds_read_b64_tr_b16 a[58:59],v10 offset:14336\n" \
"ds_read_b64_tr_b16 a[62:63],v11 offset:14336\ns_add_u32 s31,s92,368640\ns_add_u32 m0,s57,12288\nds_read_b64_tr_b16 a[64:65],v8 offset:16384\n" \
"s_waitcnt lgkmcnt(1)\nbuffer_load_dwordx4 v4,s[8:11],s31 offen lds\nbuffer_load_dwordx4 v5,s[8:11],s31 offen offset:1024 lds\nbuffer_load_dwordx4 v6,s[8:11],s31 offen offset:2048 lds\n" \
"buffer_load_dwordx4 v7,s[8:11],s31 offen offset:3072 lds\nbuffer_load_dwordx4 v[80:83],v120,s[36:39],s5 offen sc1\nbuffer_load_dwordx4 v[84:87],v120,s[36:39],s5 offen offset:2304 sc1\nbuffer_load_dwordx4 v[88:91],v120,s[36:39],s6 offen sc1\n" \
"buffer_load_dwordx4 v[92:95],v120,s[36:39],s6 offen offset:2304 sc1\nds_read_b64_tr_b16 a[68:69],v9 offset:16384\nds_read_b64_tr_b16 a[72:73],v10 offset:16384\nds_read_b64_tr_b16 a[76:77],v11 offset:16384\n" \
"ds_read_b64_tr_b16 a[80:81],v8 offset:18432\nds_read_b64_tr_b16 a[84:85],v9 offset:18432\nds_read_b64_tr_b16 a[88:89],v10 offset:18432\nds_read_b64_tr_b16 a[92:93],v11 offset:18432\n" \
"s_add_u32 s96,s92,9805824\ns_add_u32 m0,s57,16384\nds_read_b64_tr_b16 a[66:67],v8 offset:20480\ns_waitcnt lgkmcnt(1)\n" \
"buffer_load_dwordx4 v4,s[8:11],s96 offen lds\nbuffer_load_dwordx4 v5,s[8:11],s96 offen offset:1024 lds\nbuffer_load_dwordx4 v6,s[8:11],s96 offen offset:2048 lds\nbuffer_load_dwordx4 v7,s[8:11],s96 offen offset:3072 lds\n" \
"ds_read_b64_tr_b16 a[70:71],v9 offset:20480\nds_read_b64_tr_b16 a[74:75],v10 offset:20480\nds_read_b64_tr_b16 a[78:79],v11 offset:20480\nds_read_b64_tr_b16 a[82:83],v8 offset:22528\n" \
"ds_read_b64_tr_b16 a[86:87],v9 offset:22528\nds_read_b64_tr_b16 a[90:91],v10 offset:22528\nds_read_b64_tr_b16 a[94:95],v11 offset:22528\ns_add_u32 s97,s92,442368\n" \
"s_add_u32 m0,s57,20480\nds_read_b64_tr_b16 a[96:97],v8 offset:24576\ns_waitcnt lgkmcnt(1)\nbuffer_load_dwordx4 v4,s[8:11],s97 offen lds\n" \
"buffer_load_dwordx4 v5,s[8:11],s97 offen offset:1024 lds\nbuffer_load_dwordx4 v6,s[8:11],s97 offen offset:2048 lds\nbuffer_load_dwordx4 v7,s[8:11],s97 offen offset:3072 lds\nds_read_b64_tr_b16 a[100:101],v9 offset:24576\n" \
"ds_read_b64_tr_b16 a[104:105],v10 offset:24576\nds_read_b64_tr_b16 a[108:109],v11 offset:24576\nds_read_b64_tr_b16 a[112:113],v8 offset:26624\nds_read_b64_tr_b16 a[116:117],v9 offset:26624\n" \
"ds_read_b64_tr_b16 a[120:121],v10 offset:26624\nds_read_b64_tr_b16 a[124:125],v11 offset:26624\ns_add_u32 s95,s92,9879552\ns_add_u32 m0,s57,24576\n" \
"s_waitcnt vmcnt(24)\nds_read_b64_tr_b16 a[98:99],v8\ns_waitcnt lgkmcnt(1)\nbuffer_load_dwordx4 v4,s[8:11],s95 offen lds\n" \
"buffer_load_dwordx4 v5,s[8:11],s95 offen offset:1024 lds\nbuffer_load_dwordx4 v6,s[8:11],s95 offen offset:2048 lds\nbuffer_load_dwordx4 v7,s[8:11],s95 offen offset:3072 lds\nds_read_b64_tr_b16 a[102:103],v9\n" \
"ds_read_b64_tr_b16 a[106:107],v10\nds_read_b64_tr_b16 a[110:111],v11\nds_read_b64_tr_b16 a[114:115],v8 offset:2048\nds_read_b64_tr_b16 a[118:119],v9 offset:2048\n" \
"ds_read_b64_tr_b16 a[122:123],v10 offset:2048\nds_read_b64_tr_b16 a[126:127],v11 offset:2048\ns_add_u32 s100,s92,516096\ns_add_u32 m0,s57,0\n" \
"s_waitcnt vmcnt(24)\nds_read_b64_tr_b16 a[128:129],v8 offset:4096\ns_waitcnt lgkmcnt(1)\nbuffer_load_dwordx4 v4,s[8:11],s100 offen lds\n" \
"buffer_load_dwordx4 v5,s[8:11],s100 offen offset:1024 lds\nbuffer_load_dwordx4 v6,s[8:11],s100 offen offset:2048 lds\nbuffer_load_dwordx4 v7,s[8:11],s100 offen offset:3072 lds\nds_read_b64_tr_b16 a[132:133],v9 offset:4096\n" \
"ds_read_b64_tr_b16 a[136:137],v10 offset:4096\nds_read_b64_tr_b16 a[140:141],v11 offset:4096\nds_read_b64_tr_b16 a[144:145],v8 offset:6144\nds_read_b64_tr_b16 a[148:149],v9 offset:6144\n" \
"ds_read_b64_tr_b16 a[152:153],v10 offset:6144\nds_read_b64_tr_b16 a[156:157],v11 offset:6144\ns_add_u32 s101,s92,9953280\ns_add_u32 m0,s57,4096\n" \
"s_waitcnt vmcnt(24)\nds_read_b64_tr_b16 a[130:131],v8 offset:8192\ns_waitcnt lgkmcnt(1)\nbuffer_load_dwordx4 v4,s[8:11],s101 offen lds\n" \
"buffer_load_dwordx4 v5,s[8:11],s101 offen offset:1024 lds\nbuffer_load_dwordx4 v6,s[8:11],s101 offen offset:2048 lds\nbuffer_load_dwordx4 v7,s[8:11],s101 offen offset:3072 lds\nds_read_b64_tr_b16 a[134:135],v9 offset:8192\n" \
"ds_read_b64_tr_b16 a[138:139],v10 offset:8192\nds_read_b64_tr_b16 a[142:143],v11 offset:8192\nds_read_b64_tr_b16 a[146:147],v8 offset:10240\nds_read_b64_tr_b16 a[150:151],v9 offset:10240\n" \
"ds_read_b64_tr_b16 a[154:155],v10 offset:10240\nds_read_b64_tr_b16 a[158:159],v11 offset:10240\ns_bfe_u32 s26,s33,0x20001\ns_lshl2_add_u32 s15,s32,s26\n" \
"s_lshl2_add_u32 s15,s15,s3\ns_lshl_b32 s15,s15,14\ns_add_u32 s15,s15,5308416\ns_add_u32 s31,s15,0\n" \
"s_add_u32 m0,s57,8192\ns_waitcnt vmcnt(24)\nds_read_b64_tr_b16 a[160:161],v8 offset:12288\ns_waitcnt lgkmcnt(1)\n" \
"buffer_load_dwordx4 v1,s[36:39],s31 offen sc1 lds\nbuffer_load_dwordx4 v1,s[36:39],s31 offen offset:1024 sc1 lds\nbuffer_load_dwordx4 v1,s[36:39],s31 offen offset:2048 sc1 lds\nbuffer_load_dwordx4 v1,s[36:39],s31 offen offset:3072 sc1 lds\n" \
"ds_read_b64_tr_b16 a[164:165],v9 offset:12288\nds_read_b64_tr_b16 a[168:169],v10 offset:12288\nds_read_b64_tr_b16 a[172:173],v11 offset:12288\nds_read_b64_tr_b16 a[176:177],v8 offset:14336\n" \
"ds_read_b64_tr_b16 a[180:181],v9 offset:14336\nds_read_b64_tr_b16 a[184:185],v10 offset:14336\nds_read_b64_tr_b16 a[188:189],v11 offset:14336\ns_add_u32 s96,s15,4096\n" \
"s_add_u32 m0,s57,12288\ns_waitcnt vmcnt(20)\nds_read_b64_tr_b16 a[162:163],v8 offset:16384\ns_waitcnt lgkmcnt(1)\n" \
"buffer_load_dwordx4 v1,s[36:39],s96 offen sc1 lds\nbuffer_load_dwordx4 v1,s[36:39],s96 offen offset:1024 sc1 lds\nbuffer_load_dwordx4 v1,s[36:39],s96 offen offset:2048 sc1 lds\nbuffer_load_dwordx4 v1,s[36:39],s96 offen offset:3072 sc1 lds\n" \
"ds_read_b64_tr_b16 a[166:167],v9 offset:16384\nds_read_b64_tr_b16 a[170:171],v10 offset:16384\nds_read_b64_tr_b16 a[174:175],v11 offset:16384\nds_read_b64_tr_b16 a[178:179],v8 offset:18432\n" \
"ds_read_b64_tr_b16 a[182:183],v9 offset:18432\nds_read_b64_tr_b16 a[186:187],v10 offset:18432\nds_read_b64_tr_b16 a[190:191],v11 offset:18432\ns_add_u32 s97,s15,8192\n" \
"s_add_u32 m0,s57,16384\ns_waitcnt vmcnt(20)\nds_read_b64_tr_b16 a[192:193],v8 offset:20480\ns_waitcnt lgkmcnt(1)\n" \
"buffer_load_dwordx4 v1,s[36:39],s97 offen sc1 lds\nbuffer_load_dwordx4 v1,s[36:39],s97 offen offset:1024 sc1 lds\nbuffer_load_dwordx4 v1,s[36:39],s97 offen offset:2048 sc1 lds\nbuffer_load_dwordx4 v1,s[36:39],s97 offen offset:3072 sc1 lds\n" \
"ds_read_b64_tr_b16 a[196:197],v9 offset:20480\nds_read_b64_tr_b16 a[200:201],v10 offset:20480\nds_read_b64_tr_b16 a[204:205],v11 offset:20480\nds_read_b64_tr_b16 a[208:209],v8 offset:22528\n" \
"ds_read_b64_tr_b16 a[212:213],v9 offset:22528\nds_read_b64_tr_b16 a[216:217],v10 offset:22528\nds_read_b64_tr_b16 a[220:221],v11 offset:22528\ns_add_u32 s95,s15,12288\n" \
"s_add_u32 m0,s57,20480\ns_waitcnt lgkmcnt(0)\nbuffer_load_dwordx4 v1,s[36:39],s95 offen sc1 lds\nbuffer_load_dwordx4 v1,s[36:39],s95 offen offset:1024 sc1 lds\n" \
"buffer_load_dwordx4 v1,s[36:39],s95 offen offset:2048 sc1 lds\nbuffer_load_dwordx4 v1,s[36:39],s95 offen offset:3072 sc1 lds\nv_cmp_ne_u64 vcc,s[28:29],v[80:81]\nv_cmp_ne_u64 s[24:25],s[28:29],v[84:85]\n" \
"v_cmp_ne_u64 s[26:27],s[28:29],v[88:89]\ns_or_b64 vcc,vcc,s[24:25]\nv_cmp_ne_u64 s[24:25],s[28:29],v[92:93]\ns_or_b64 vcc,vcc,s[26:27]\n" \
"s_or_b64 vcc,vcc,s[24:25]\ns_cbranch_vccnz .LsnP_%=\n.LbnP_%=:\nv_bfe_u32 v112,v0,1,5\n" \
"v_mul_u32_u24 v112,72,v112\nv_lshrrev_b32 v113,6,v0\nv_mul_u32_u24 v114,144,v0\nv_lshl_add_u32 v112,v113,5,v112\n" \
"v_subrev_u32 v114,18368,v114\nv_mov_b32 v115,1048576\nv_cmp_gt_u32 vcc,144,v0\nv_and_b32 v113,1,v0\n" \
"v_lshl_add_u32 v112,v113,4,v112\nv_cndmask_b32 v114,v115,v114,vcc\nv_and_b32 v97,0xffff0000,v82\nv_lshlrev_b32 v96,16,v82\n" \
"s_add_u32 s24,s93,s94\nv_cmp_gt_u32 vcc,128,v0\nv_add_u32 v113,8,v112\nv_mul_f32 v96,0x3ee38e39,v96\n" \
"v_mul_f32 v97,0x3ee38e39,v97\ns_sub_u32 s24,s24,253\nv_cndmask_b32 v112,v114,v112,vcc\nv_and_b32 v99,0xffff0000,v83\n" \
"v_ldexp_f32 v96,v96,s24\nv_ldexp_f32 v97,v97,s24\nv_lshlrev_b32_e64 v98,16,v83\nds_write_b64 v112,v[96:97] offset:28800\n" \
"v_add_u32 v115,72,v114\nv_mul_f32 v98,0x3ee38e39,v98\nv_mul_f32 v99,0x3ee38e39,v99\nv_cndmask_b32_e64 v113,v115,v113,vcc\n" \
"v_ldexp_f32 v98,v98,s24\nv_ldexp_f32 v99,v99,s24\nds_write_b64 v113,v[98:99] offset:28800\nv_and_b32 v101,0xffff0000,v86\n" \
"v_lshlrev_b32 v100,16,v86\nds_write_b64 v112,v[100:101] offset:31104\nv_and_b32 v103,0xffff0000,v87\nv_lshlrev_b32 v102,16,v87\n" \
"s_lshr_b32 s25,s33,3\nds_write_b64 v113,v[102:103] offset:31104\ns_mul_i32 s25,s25,576\nv_and_b32 v105,0xffff0000,v90\n" \
"v_lshlrev_b32 v104,16,v90\ns_sub_u32 s25,163200,s25\nds_write_b64 v112,v[104:105] offset:33408\nv_add_u32 v114,s25,v2\n" \
"v_subrev_u32 v115,163200,v114\nv_and_b32 v107,0xffff0000,v91\nv_lshlrev_b32 v106,16,v91\nv_mov_b32 v116,1048576\n" \
"v_cmp_gt_u32 vcc,576,v115\nds_write_b64 v113,v[106:107] offset:33408\nv_and_b32 v109,0xffff0000,v94\nv_cndmask_b32 v114,v116,v114,vcc\n" \
"v_and_b32 v111,0xffff0000,v95\nv_lshlrev_b32 v108,16,v94\nv_lshlrev_b32 v110,16,v95\nds_write_b128 v114,v[108:111]\n" \
"buffer_load_dwordx4 v[112:115],v3,s[36:39],s16 offen sc1\nbuffer_load_dwordx4 v[116:119],v3,s[36:39],s17 offen sc1\nv_and_b32 v124,48,v0\nv_cmp_gt_u32 vcc,64,v0\n" \
"v_bfe_u32 v127,v0,5,1\nv_and_b32 v13,31,v0\nv_cndmask_b32_e64 v124,v124,v3,vcc\nbuffer_load_dwordx4 v[120:123],v124,s[36:39],s18 offen sc1\n" \
"v_mul_u32_u24 v127,2448,v127\nv_mul_u32_u24 v13,72,v13\ns_mul_i32 s27,s3,4896\nv_add3_u32 v13,v13,v127,s27\n" \
"v_mov_b32 v127,0\ns_waitcnt vmcnt(27)\nds_read_b64_tr_b16 a[194:195],v8 offset:24576\nds_read_b64_tr_b16 a[198:199],v9 offset:24576\n" \
"s_waitcnt lgkmcnt(2)\ns_barrier\nds_read_b64_tr_b16 a[202:203],v10 offset:24576\nds_read_b64_tr_b16 a[206:207],v11 offset:24576\n" \
"ds_read_b64_tr_b16 a[210:211],v8 offset:26624\nds_read_b64_tr_b16 a[214:215],v9 offset:26624\nds_read_b64_tr_b16 a[218:219],v10 offset:26624\nds_read_b64_tr_b16 a[222:223],v11 offset:26624\n" \
"s_waitcnt vmcnt(23)\nds_read_b64_tr_b16 a[224:225],v8\nds_read_b64_tr_b16 a[228:229],v9\nds_read_b64_tr_b16 a[232:233],v10\n" \
"ds_read_b64_tr_b16 a[236:237],v11\nds_read_b64_tr_b16 a[240:241],v8 offset:2048\nds_read_b64_tr_b16 a[244:245],v9 offset:2048\nds_read_b64_tr_b16 a[248:249],v10 offset:2048\n" \
"ds_read_b64_tr_b16 a[252:253],v11 offset:2048\ns_waitcnt vmcnt(19)\nds_read_b64_tr_b16 a[226:227],v8 offset:4096\nds_read_b64_tr_b16 a[230:231],v9 offset:4096\n" \
"ds_read_b64_tr_b16 a[234:235],v10 offset:4096\nds_read_b64_tr_b16 a[238:239],v11 offset:4096\nds_read_b64_tr_b16 a[242:243],v8 offset:6144\nds_read_b64_tr_b16 a[246:247],v9 offset:6144\n" \
"ds_read_b64_tr_b16 a[250:251],v10 offset:6144\nds_read_b64_tr_b16 a[254:255],v11 offset:6144\ns_waitcnt vmcnt(15)\nds_read_b64_tr_b16 v[192:193],v15 offset:8192\n" \
"ds_read_b64_tr_b16 v[196:197],v15 offset:8704\nds_read_b64_tr_b16 v[194:195],v15 offset:9216\nds_read_b64_tr_b16 v[198:199],v15 offset:9728\nds_read_b64_tr_b16 v[200:201],v15 offset:10240\n" \
"ds_read_b64_tr_b16 v[204:205],v15 offset:10752\nds_read_b64_tr_b16 v[202:203],v15 offset:11264\nds_read_b64_tr_b16 v[206:207],v15 offset:11776\ns_waitcnt vmcnt(11)\n" \
"ds_read_b64_tr_b16 v[208:209],v15 offset:12288\nds_read_b64_tr_b16 v[212:213],v15 offset:12800\nds_read_b64_tr_b16 v[210:211],v15 offset:13312\nds_read_b64_tr_b16 v[214:215],v15 offset:13824\n" \
"ds_read_b64_tr_b16 v[216:217],v15 offset:14336\nds_read_b64_tr_b16 v[220:221],v15 offset:14848\nds_read_b64_tr_b16 v[218:219],v15 offset:15360\nds_read_b64_tr_b16 v[222:223],v15 offset:15872\n" \
"s_waitcnt vmcnt(7)\nds_read_b64_tr_b16 v[224:225],v15 offset:16384\nds_read_b64_tr_b16 v[228:229],v15 offset:16896\nds_read_b64_tr_b16 v[226:227],v15 offset:17408\n" \
"ds_read_b64_tr_b16 v[230:231],v15 offset:17920\nds_read_b64_tr_b16 v[232:233],v15 offset:18432\nds_read_b64_tr_b16 v[236:237],v15 offset:18944\nds_read_b64_tr_b16 v[234:235],v15 offset:19456\n" \
"ds_read_b64_tr_b16 v[238:239],v15 offset:19968\ns_waitcnt vmcnt(3)\nds_read_b64_tr_b16 v[240:241],v15 offset:20480\nds_read_b64_tr_b16 v[244:245],v15 offset:20992\n" \
"ds_read_b64_tr_b16 v[242:243],v15 offset:21504\nds_read_b64_tr_b16 v[246:247],v15 offset:22016\nds_read_b64_tr_b16 v[248:249],v15 offset:22528\nds_read_b64_tr_b16 v[252:253],v15 offset:23040\n" \
"ds_read_b64_tr_b16 v[250:251],v15 offset:23552\nds_read_b64_tr_b16 v[254:255],v15 offset:24064\ns_waitcnt vmcnt(1)\nv_and_b32 v125,v112,v116\n" \
"s_waitcnt vmcnt(0)\nv_bitop3_b32 v125,v125,v120,1 bitop3:0x80\nv_cmp_eq_u32 vcc,0,v125\ns_nop 0\n" \
"s_cbranch_vccnz .LsuP_%=\n.LbuP_%=:\nv_mfma_f32_16x16x32_f16 v[80:83],v[32:35],v[112:115],0\nv_and_b32 v3,31,v0\n" \
"v_mul_u32_u24 v3,72,v3\nv_mfma_f32_16x16x32_f16 v[84:87],v[36:39],v[112:115],0\nv_add_u32 v13,9216,v13\nv_mov_b32 v104,-1\n" \
"v_mfma_f32_16x16x32_f16 v[88:91],v[32:35],v[116:119],0\nv_mov_b32 v105,-1\nv_mov_b32 v106,-1\nv_mfma_f32_16x16x32_f16 v[92:95],v[36:39],v[116:119],0\n" \
"v_mov_b32 v107,-1\ns_add_u32 s27,s2,0\nv_mfma_f32_16x16x32_f16 v[96:99],v[32:35],v[120:123],0\ns_lshr_b32 s100,s27,4\n" \
"s_mul_i32 s101,s100,37\ns_lshr_b32 s101,s101,8\ns_lshl2_add_u32 s100,s101,s100\ns_mul_i32 s100,s100,409600\n" \
"s_and_b32 s101,s27,15\ns_lshl_b32 s101,s101,14\ns_add_u32 s100,s100,s101\ns_add_u32 s100,s100,13090816\n" \
"s_cmp_lt_u32 s27,158\ns_cselect_b32 s100,s100,2147483648\nbuffer_store_dwordx4 v[104:107],v2,s[36:39],s100 offen sc1\nv_mfma_f32_16x16x32_f16 v[100:103],v[36:39],v[120:123],0\n" \
"v_cvt_pk_f16_f32 v112,v84,v80\nv_cvt_pk_f16_f32 v113,v85,v81\nv_cvt_pk_f16_f32 v114,v86,v82\nv_cvt_pk_f16_f32 v115,v87,v83\n" \
"ds_write_b128 v57,v[112:115]\nv_cvt_pk_f16_f32 v116,v92,v88\nv_cvt_pk_f16_f32 v117,v93,v89\ns_add_u32 s100,s100,4096\n" \
"buffer_store_dwordx4 v[104:107],v2,s[36:39],s100 offen sc1\nv_cvt_pk_f16_f32 v118,v94,v90\nv_cvt_pk_f16_f32 v119,v95,v91\nds_write_b128 v58,v[116:119]\n" \
"v_cvt_pk_f16_f32 v120,v100,v96\nv_cvt_pk_f16_f32 v121,v101,v97\nv_cvt_pk_f16_f32 v122,v102,v98\nv_cvt_pk_f16_f32 v123,v103,v99\n" \
"ds_write_b128 v60,v[120:123]\ns_add_u32 s100,s100,4096\nbuffer_store_dwordx4 v[104:107],v2,s[36:39],s100 offen sc1\nv_add_u32 v60,31104,v3\n" \
"v_add_u32 v3,28800,v3\nds_read2_b64 v[32:35],v3 offset0:0 offset1:1\nds_read2_b64 v[36:39],v3 offset0:2 offset1:3\ns_add_u32 s100,s100,4096\n" \
"s_add_u32 s27,s2,111\ns_lshr_b32 s31,s27,4\ns_mul_i32 s96,s31,37\ns_lshr_b32 s96,s96,8\n" \
"s_lshl2_add_u32 s31,s96,s31\ns_mul_i32 s31,s31,409600\ns_and_b32 s96,s27,15\ns_lshl_b32 s96,s96,14\n" \
"buffer_store_dwordx4 v[104:107],v2,s[36:39],s100 offen sc1\ns_add_u32 s31,s31,s96\ns_add_u32 s31,s31,13090816\ns_cmp_lt_u32 s27,158\n" \
"s_cselect_b32 s31,s31,2147483648\ns_waitcnt lgkmcnt(2)\ns_barrier\nds_read_b64 v[112:113],v56\n" \
"ds_read_b64 v[116:117],v56 offset:512\nds_read_b64 v[120:121],v56 offset:256\nds_read_b64 v[124:125],v56 offset:768\nds_read_b64 v[114:115],v56 offset:1024\n" \
"ds_read_b64 v[122:123],v56 offset:1280\nds_read_b64 v[56:57],v3 offset:64\nbuffer_store_dwordx4 v[104:107],v2,s[36:39],s31 offen sc1\ns_add_u32 s31,s31,4096\n" \
"v_bfe_u32 v110,v0,1,2\nv_bfe_u32 v111,v0,4,1\nv_xor_b32 v110,v110,v111\nv_and_b32 v111,1,v0\n" \
"v_lshl_add_u32 v110,v110,1,v111\nv_bfe_u32 v111,v0,3,3\nv_lshlrev_b32 v111,12,v111\nv_lshl_add_u32 v4,v110,4,v111\n" \
"v_xor_b32 v5,64,v4\nv_add_u32 v5,31744,v5\nv_add_u32 v6,63488,v4\nbuffer_store_dwordx4 v[104:107],v2,s[36:39],s31 offen sc1\n" \
"s_waitcnt lgkmcnt(3)\nv_swap_b32 v113,v116\ns_add_u32 s31,s31,4096\nv_swap_b32 v121,v124\n" \
"v_add_u32 v7,63488,v5\ns_waitcnt lgkmcnt(0)\nv_mfma_f32_16x16x32_f16 v[80:83],v[112:115],v[40:43],0\nv_mov_b32 v118,v115\n" \
"v_mov_b32 v126,v123\nv_mfma_f32_16x16x32_f16 v[84:87],v[112:115],v[44:47],0\ns_lshr_b32 s26,s32,1\ns_mul_i32 s92,s26,1152\n" \
"v_mfma_f32_16x16x32_f16 v[88:91],v[120:123],v[40:43],0\nbuffer_store_dwordx4 v[104:107],v2,s[36:39],s31 offen sc1\nv_mfma_f32_16x16x32_f16 v[92:95],v[120:123],v[44:47],0\ns_add_u32 s31,s31,4096\n" \
"s_lshr_b32 s26,s3,1\nv_mfma_f32_16x16x32_f16 v[96:99],v[116:119],v[40:43],0\ns_lshl_b32 s26,s26,6\ns_add_u32 s92,s92,s26\n" \
"v_mfma_f32_16x16x32_f16 v[100:103],v[116:119],v[44:47],0\nv_pk_add_f32 v[88:89],v[80:81],v[88:89] neg_lo:[0,1] neg_hi:[0,1]\nv_pk_add_f32 v[90:91],v[82:83],v[90:91] neg_lo:[0,1] neg_hi:[0,1]\nv_pk_add_f32 v[92:93],v[84:85],v[92:93] neg_lo:[0,1] neg_hi:[0,1]\n" \
"buffer_store_dwordx4 v[104:107],v2,s[36:39],s31 offen sc1\nv_pk_add_f32 v[94:95],v[86:87],v[94:95] neg_lo:[0,1] neg_hi:[0,1]\nv_pk_fma_f32 v[80:81],v[80:81],2.0,v[88:89] op_sel_hi:[1,0,1] neg_lo:[0,0,1] neg_hi:[0,0,1]\nv_pk_fma_f32 v[82:83],v[82:83],2.0,v[90:91] op_sel_hi:[1,0,1] neg_lo:[0,0,1] neg_hi:[0,0,1]\n" \
"v_pk_fma_f32 v[84:85],v[84:85],2.0,v[92:93] op_sel_hi:[1,0,1] neg_lo:[0,0,1] neg_hi:[0,0,1]\nv_pk_fma_f32 v[86:87],v[86:87],2.0,v[94:95] op_sel_hi:[1,0,1] neg_lo:[0,0,1] neg_hi:[0,0,1]\nv_cvt_pk_f16_f32 v112,v80,v84\nv_cvt_pk_f16_f32 v113,v81,v85\n" \
"v_cvt_pk_f16_f32 v114,v82,v86\nv_cvt_pk_f16_f32 v115,v83,v87\nv_mfma_f32_16x16x32_f16 v[104:107],v[124:127],v[40:43],0\nv_cvt_pk_f16_f32 v116,v88,v92\n" \
"s_lshr_b32 s97,s33,3\ns_add_u32 s97,s97,0\ns_sub_u32 s95,s97,4\ns_min_u32 s97,s97,s95\n" \
"s_lshl_b32 s95,s97,12\ns_add_u32 s95,s95,s57\ns_add_u32 m0,s95,0\ns_and_b32 s95,s97,1\n" \
"s_lshr_b32 s97,s97,1\ns_lshl_b32 s97,s97,10\ns_add_u32 s97,s97,s95\ns_lshl_b32 s97,s97,7\n" \
"s_lshr_b32 s100,s33,3\ns_add_u32 s100,s100,1\ns_sub_u32 s101,s100,4\ns_min_u32 s100,s100,s101\n" \
"s_lshl_b32 s101,s100,12\ns_add_u32 s101,s101,s57\ns_lshr_b32 s31,s33,3\ns_add_u32 s31,s31,2\n" \
"s_sub_u32 s96,s31,4\ns_min_u32 s31,s31,s96\ns_lshl_b32 s96,s31,12\ns_add_u32 s96,s96,s57\n" \
"v_mfma_f32_16x16x32_f16 v[80:83],v[16:19],v[112:115],0\nv_cvt_pk_f16_f32 v117,v89,v93\nv_cvt_pk_f16_f32 v118,v90,v94\nv_mfma_f32_16x16x32_f16 v[84:87],v[20:23],v[112:115],0\n" \
"v_cvt_pk_f16_f32 v119,v91,v95\nv_pk_add_f32 v[104:105],v[96:97],v[104:105] neg_lo:[0,1] neg_hi:[0,1]\nv_mfma_f32_16x16x32_f16 v[108:111],v[124:127],v[44:47],0\nv_pk_add_f32 v[106:107],v[98:99],v[106:107] neg_lo:[0,1] neg_hi:[0,1]\n" \
"v_pk_fma_f32 v[96:97],v[96:97],2.0,v[104:105] op_sel_hi:[1,0,1] neg_lo:[0,0,1] neg_hi:[0,0,1]\nv_mfma_f32_16x16x32_f16 v[88:91],v[24:27],v[116:119],0\nv_pk_fma_f32 v[98:99],v[98:99],2.0,v[106:107] op_sel_hi:[1,0,1] neg_lo:[0,0,1] neg_hi:[0,0,1]\nds_read2_b64 v[40:43],v3 offset0:4 offset1:5\n" \
"v_mfma_f32_16x16x32_f16 v[92:95],v[28:31],v[116:119],0\nds_write2_b32 v48,v84,v80 offset0:0 offset1:1\nds_write2_b32 v49,v85,v81 offset0:0 offset1:1\nds_write2_b32 v50,v86,v82 offset0:0 offset1:1\n" \
"v_pk_add_f32 v[108:109],v[100:101],v[108:109] neg_lo:[0,1] neg_hi:[0,1]\nv_pk_add_f32 v[110:111],v[102:103],v[110:111] neg_lo:[0,1] neg_hi:[0,1]\nv_pk_fma_f32 v[100:101],v[100:101],2.0,v[108:109] op_sel_hi:[1,0,1] neg_lo:[0,0,1] neg_hi:[0,0,1]\nv_pk_fma_f32 v[102:103],v[102:103],2.0,v[110:111] op_sel_hi:[1,0,1] neg_lo:[0,0,1] neg_hi:[0,0,1]\n" \
"ds_write2_b32 v51,v87,v83 offset0:0 offset1:1\nv_cvt_pk_f16_f32 v120,v96,v100\nv_cvt_pk_f16_f32 v121,v97,v101\nv_cvt_pk_f16_f32 v122,v98,v102\n" \
"v_cvt_pk_f16_f32 v123,v99,v103\nds_write2_b32 v52,v92,v88 offset0:0 offset1:1\nds_write2_b32 v53,v93,v89 offset0:0 offset1:1\nv_mfma_f32_16x16x32_f16 v[96:99],v[16:19],v[120:123],0\n" \
"ds_write2_b32 v54,v94,v90 offset0:0 offset1:1\nv_mfma_f32_16x16x32_f16 v[100:103],v[20:23],v[120:123],0\nds_write2_b32 v55,v95,v91 offset0:0 offset1:1\nv_cvt_pk_f16_f32 v124,v104,v108\n" \
"v_cvt_pk_f16_f32 v125,v105,v109\nv_cvt_pk_f16_f32 v126,v106,v110\nv_cvt_pk_f16_f32 v127,v107,v111\nv_add_u32 v48,2448,v48\n" \
"v_add_u32 v49,2448,v49\nv_mfma_f32_16x16x32_f16 v[104:107],v[24:27],v[124:127],0\nds_write2_b32 v48,v100,v96 offset0:0 offset1:1\nv_mfma_f32_16x16x32_f16 v[108:111],v[28:31],v[124:127],0\n" \
"ds_write2_b32 v49,v101,v97 offset0:0 offset1:1\nv_add_u32 v50,2448,v50\nds_write2_b32 v50,v102,v98 offset0:0 offset1:1\nv_add_u32 v51,2448,v51\n" \
"ds_write2_b32 v51,v103,v99 offset0:0 offset1:1\nv_add_u32 v52,2448,v52\nv_add_u32 v53,2448,v53\nv_add_u32 v54,2448,v54\n" \
"ds_write2_b32 v52,v108,v104 offset0:0 offset1:1\nds_write2_b32 v53,v109,v105 offset0:0 offset1:1\nds_write2_b32 v54,v110,v106 offset0:0 offset1:1\nv_add_u32 v55,2448,v55\n" \
"ds_write2_b32 v55,v111,v107 offset0:0 offset1:1\nds_read2_b64 v[80:83],v13 offset0:0 offset1:1\nds_read2_b64 v[84:87],v13 offset0:2 offset1:3\ns_lshl_b32 s26,s3,8\n" \
"s_add_u32 s92,s92,s26\ns_lshl_b32 s92,s92,4\ns_and_b32 s26,s32,1\ns_lshl3_add_u32 s92,s26,s92\n" \
"s_and_b32 s26,s33,7\ns_add_u32 s92,s92,s26\nds_read2_b64 v[88:91],v13 offset0:4 offset1:5\ns_lshl_b32 s92,s92,8\n" \
"s_add_u32 s97,s97,s92\nbuffer_load_dwordx4 v4,s[40:43],s97 offen lds\nds_read2_b64 v[44:47],v3 offset0:6 offset1:7\nv_and_b32 v48,31,v0\n" \
"ds_read2_b64 v[92:95],v13 offset0:6 offset1:7\nv_mad_u32_u24 v49,v48,56,0\nv_sub_u32 v48,v13,v49\nv_and_b32 v49,31,v0\n" \
"v_mul_u32_u24 v49,68,v49\nv_sub_u32 v49,v13,v49\nbuffer_load_dwordx4 v5,s[40:43],s97 offen offset:1024 lds\nds_read_b64 v[96:97],v13 offset:64\n" \
"s_waitcnt lgkmcnt(5)\nv_pk_fma_f32 v[62:63],v[80:81],v[32:33],v[62:63]\nv_pk_fma_f32 v[64:65],v[82:83],v[34:35],v[64:65]\nv_pk_mul_f32 v[102:103],v[62:63],v[62:63]\n" \
"s_waitcnt lgkmcnt(4)\nv_pk_fma_f32 v[66:67],v[84:85],v[36:37],v[66:67]\nv_pk_add_f32 v[100:101],v[62:63],v[64:65]\nv_pk_fma_f32 v[102:103],v[64:65],v[64:65],v[102:103]\n" \
"v_pk_fma_f32 v[68:69],v[86:87],v[38:39],v[68:69]\nbuffer_load_dwordx4 v6,s[40:43],s97 offen offset:2048 lds\nv_pk_add_f32 v[100:101],v[100:101],v[66:67]\nv_pk_fma_f32 v[102:103],v[66:67],v[66:67],v[102:103]\n" \
"ds_read2_b64 v[112:115],v60 offset0:0 offset1:1\nds_read2_b64 v[116:119],v60 offset0:2 offset1:3\ns_waitcnt lgkmcnt(4)\nv_pk_fma_f32 v[70:71],v[88:89],v[40:41],v[70:71]\n" \
"v_pk_add_f32 v[100:101],v[100:101],v[68:69]\nds_read2_b64 v[120:123],v60 offset0:4 offset1:5\nv_pk_fma_f32 v[102:103],v[68:69],v[68:69],v[102:103]\nbuffer_load_dwordx4 v7,s[40:43],s97 offen offset:3072 lds\n" \
"ds_read2_b64 v[124:127],v60 offset0:6 offset1:7\nv_pk_fma_f32 v[72:73],v[90:91],v[42:43],v[72:73]\nv_pk_add_f32 v[100:101],v[100:101],v[70:71]\nds_read_b64 v[98:99],v60 offset:64\n" \
"v_add_u32 v60,2304,v60\nv_pk_fma_f32 v[102:103],v[70:71],v[70:71],v[102:103]\nds_read2_b64 v[16:19],v60 offset0:0 offset1:1\ns_waitcnt lgkmcnt(6)\n" \
"v_pk_fma_f32 v[74:75],v[92:93],v[44:45],v[74:75]\ns_add_u32 m0,s101,0\ns_and_b32 s101,s100,1\ns_lshr_b32 s100,s100,1\n" \
"s_lshl_b32 s100,s100,10\ns_add_u32 s100,s100,s101\ns_lshl_b32 s100,s100,7\ns_add_u32 s100,s100,s92\n" \
"buffer_load_dwordx4 v4,s[40:43],s100 offen lds\nv_pk_add_f32 v[100:101],v[100:101],v[72:73]\nv_pk_fma_f32 v[102:103],v[72:73],v[72:73],v[102:103]\nv_pk_fma_f32 v[76:77],v[94:95],v[46:47],v[76:77]\n" \
"v_pk_add_f32 v[100:101],v[100:101],v[74:75]\nv_pk_fma_f32 v[102:103],v[74:75],v[74:75],v[102:103]\nds_read2_b64 v[20:23],v60 offset0:2 offset1:3\nv_pk_fma_f32 v[78:79],v[96:97],v[56:57],v[78:79]\n" \
"v_pk_add_f32 v[100:101],v[100:101],v[76:77]\nbuffer_load_dwordx4 v5,s[40:43],s100 offen offset:1024 lds\nv_pk_fma_f32 v[102:103],v[76:77],v[76:77],v[102:103]\nv_pk_add_f32 v[100:101],v[100:101],v[78:79]\n" \
"v_pk_fma_f32 v[102:103],v[78:79],v[78:79],v[102:103]\nds_read2_b64 v[24:27],v60 offset0:4 offset1:5\nv_add_f32 v100,v100,v101\nv_add_f32 v102,v102,v103\n" \
"ds_read2_b64 v[28:31],v60 offset0:6 offset1:7\nds_read_b64 v[58:59],v60 offset:64\nbuffer_load_dwordx4 v6,s[40:43],s100 offen offset:2048 lds\nv_permlane16_swap_b32 v100,v102\n" \
"v_add_f32 v100,v100,v102\ns_lshr_b32 s97,s33,3\ns_add_u32 s97,s97,3\nv_add_f32_dpp v100,v100,v100 quad_perm:[1,0,3,2] row_mask:0xf bank_mask:0xf bound_ctrl:1\n" \
"s_sub_u32 s95,s97,4\ns_min_u32 s97,s97,s95\nv_add_f32_dpp v100,v100,v100 quad_perm:[2,3,0,1] row_mask:0xf bank_mask:0xf bound_ctrl:1\nbuffer_load_dwordx4 v7,s[40:43],s100 offen offset:3072 lds\n" \
"s_add_u32 m0,s96,0\nv_add_f32_dpp v100,v100,v100 row_half_mirror row_mask:0xf bank_mask:0xf bound_ctrl:1\ns_and_b32 s96,s31,1\ns_lshr_b32 s31,s31,1\n" \
"v_add_f32_dpp v100,v100,v100 row_mirror row_mask:0xf bank_mask:0xf bound_ctrl:1\nv_mul_f32 v104,0x3ae38e39,v100\nv_mul_f32 v102,0x3ae38e39,v100\ns_lshl_b32 s31,s31,10\n" \
"s_add_u32 s31,s31,s96\ns_lshl_b32 s31,s31,7\ns_add_u32 s31,s31,s92\nbuffer_load_dwordx4 v4,s[40:43],s31 offen lds\n" \
"v_permlane16_swap_b32 v104,v102\nv_fma_f32 v102,-v104,v104,v102\nv_max_f32 v102,0,v102\nv_add_f32 v102,0x3727c5ac,v102\n" \
"v_rsq_f32 v100,v102\ns_lshl_b32 s95,s97,12\nbuffer_load_dwordx4 v5,s[40:43],s31 offen offset:1024 lds\nv_mul_f32 v101,-v104,v100\n" \
"v_pk_fma_f32 v[80:81],v[62:63],v[100:101],v[100:101] op_sel:[0,0,1] op_sel_hi:[1,0,1]\nv_pk_fma_f32 v[82:83],v[64:65],v[100:101],v[100:101] op_sel:[0,0,1] op_sel_hi:[1,0,1]\nv_pk_fma_f32 v[84:85],v[66:67],v[100:101],v[100:101] op_sel:[0,0,1] op_sel_hi:[1,0,1]\nv_pk_fma_f32 v[86:87],v[68:69],v[100:101],v[100:101] op_sel:[0,0,1] op_sel_hi:[1,0,1]\n" \
"v_pk_fma_f32 v[88:89],v[70:71],v[100:101],v[100:101] op_sel:[0,0,1] op_sel_hi:[1,0,1]\nv_pk_fma_f32 v[90:91],v[72:73],v[100:101],v[100:101] op_sel:[0,0,1] op_sel_hi:[1,0,1]\nv_pk_fma_f32 v[92:93],v[74:75],v[100:101],v[100:101] op_sel:[0,0,1] op_sel_hi:[1,0,1]\nbuffer_load_dwordx4 v6,s[40:43],s31 offen offset:2048 lds\n" \
"v_pk_fma_f32 v[94:95],v[76:77],v[100:101],v[100:101] op_sel:[0,0,1] op_sel_hi:[1,0,1]\nv_pk_fma_f32 v[96:97],v[78:79],v[100:101],v[100:101] op_sel:[0,0,1] op_sel_hi:[1,0,1]\ns_add_u32 s95,s95,s57\ns_waitcnt lgkmcnt(0)\n" \
"v_readlane_b32 s24,v112,0\nv_mov_b32 v60,v61\ns_mov_b32 s25,s24\nv_pk_add_f32 v[112:113],v[112:113],s[24:25] neg_lo:[0,1] neg_hi:[0,1]\n" \
"v_pk_fma_f32 v[80:81],v[80:81],v[16:17],v[112:113]\nbuffer_load_dwordx4 v7,s[40:43],s31 offen offset:3072 lds\nv_pk_mul_f32 v[102:103],v[80:81],v[80:81]\ns_add_u32 m0,s95,0\n" \
"s_and_b32 s95,s97,1\ns_lshr_b32 s97,s97,1\ns_lshl_b32 s97,s97,10\ns_add_u32 s97,s97,s95\n" \
"s_lshl_b32 s97,s97,7\ns_add_u32 s97,s97,s92\nbuffer_load_dwordx4 v4,s[40:43],s97 offen lds\nv_pk_add_f32 v[114:115],v[114:115],s[24:25] neg_lo:[0,1] neg_hi:[0,1]\n" \
"v_pk_fma_f32 v[82:83],v[82:83],v[18:19],v[114:115]\nv_pk_add_f32 v[100:101],v[80:81],v[82:83]\nv_pk_fma_f32 v[102:103],v[82:83],v[82:83],v[102:103]\nv_pk_add_f32 v[116:117],v[116:117],s[24:25] neg_lo:[0,1] neg_hi:[0,1]\n" \
"v_pk_fma_f32 v[84:85],v[84:85],v[20:21],v[116:117]\nv_pk_add_f32 v[100:101],v[100:101],v[84:85]\nbuffer_load_dwordx4 v5,s[40:43],s97 offen offset:1024 lds\nv_pk_fma_f32 v[102:103],v[84:85],v[84:85],v[102:103]\n" \
"v_pk_add_f32 v[118:119],v[118:119],s[24:25] neg_lo:[0,1] neg_hi:[0,1]\nv_pk_fma_f32 v[86:87],v[86:87],v[22:23],v[118:119]\nv_pk_add_f32 v[100:101],v[100:101],v[86:87]\nv_pk_fma_f32 v[102:103],v[86:87],v[86:87],v[102:103]\n" \
"v_pk_add_f32 v[120:121],v[120:121],s[24:25] neg_lo:[0,1] neg_hi:[0,1]\nv_pk_fma_f32 v[88:89],v[88:89],v[24:25],v[120:121]\nbuffer_load_dwordx4 v6,s[40:43],s97 offen offset:2048 lds\nv_pk_add_f32 v[100:101],v[100:101],v[88:89]\n" \
"v_pk_fma_f32 v[102:103],v[88:89],v[88:89],v[102:103]\nv_pk_add_f32 v[122:123],v[122:123],s[24:25] neg_lo:[0,1] neg_hi:[0,1]\nv_pk_fma_f32 v[90:91],v[90:91],v[26:27],v[122:123]\nv_pk_add_f32 v[100:101],v[100:101],v[90:91]\n" \
"v_pk_fma_f32 v[102:103],v[90:91],v[90:91],v[102:103]\nv_pk_add_f32 v[124:125],v[124:125],s[24:25] neg_lo:[0,1] neg_hi:[0,1]\nbuffer_load_dwordx4 v7,s[40:43],s97 offen offset:3072 lds\nv_pk_fma_f32 v[92:93],v[92:93],v[28:29],v[124:125]\n" \
"v_pk_add_f32 v[100:101],v[100:101],v[92:93]\nv_pk_fma_f32 v[102:103],v[92:93],v[92:93],v[102:103]\nv_pk_add_f32 v[126:127],v[126:127],s[24:25] neg_lo:[0,1] neg_hi:[0,1]\nv_pk_fma_f32 v[94:95],v[94:95],v[30:31],v[126:127]\n" \
"v_pk_add_f32 v[100:101],v[100:101],v[94:95]\nv_pk_fma_f32 v[102:103],v[94:95],v[94:95],v[102:103]\nv_pk_add_f32 v[98:99],v[98:99],s[24:25] neg_lo:[0,1] neg_hi:[0,1]\nv_pk_fma_f32 v[96:97],v[96:97],v[58:59],v[98:99]\n" \
"v_pk_add_f32 v[100:101],v[100:101],v[96:97]\nv_pk_fma_f32 v[102:103],v[96:97],v[96:97],v[102:103]\nv_add_f32 v100,v100,v101\nv_add_f32 v102,v102,v103\n" \
"v_pk_add_f32 v[62:63],v[62:63],v[60:61] op_sel_hi:[1,0]\nv_cvt_pk_bf16_f32 v32,v62,v63\nv_permlane16_swap_b32 v100,v102\nv_add_f32 v100,v100,v102\n" \
"v_pk_add_f32 v[64:65],v[64:65],v[60:61] op_sel_hi:[1,0]\nv_cvt_pk_bf16_f32 v33,v64,v65\nv_add_f32_dpp v100,v100,v100 quad_perm:[1,0,3,2] row_mask:0xf bank_mask:0xf bound_ctrl:1\nv_pk_add_f32 v[66:67],v[66:67],v[60:61] op_sel_hi:[1,0]\n" \
"v_cvt_pk_bf16_f32 v34,v66,v67\nv_add_f32_dpp v100,v100,v100 quad_perm:[2,3,0,1] row_mask:0xf bank_mask:0xf bound_ctrl:1\nv_pk_add_f32 v[68:69],v[68:69],v[60:61] op_sel_hi:[1,0]\nv_cvt_pk_bf16_f32 v35,v68,v69\n" \
"v_add_f32_dpp v100,v100,v100 row_half_mirror row_mask:0xf bank_mask:0xf bound_ctrl:1\nv_pk_add_f32 v[70:71],v[70:71],v[60:61] op_sel_hi:[1,0]\nv_cvt_pk_bf16_f32 v36,v70,v71\nv_add_f32_dpp v100,v100,v100 row_mirror row_mask:0xf bank_mask:0xf bound_ctrl:1\n" \
"v_mul_f32 v104,0x3ae38e39,v100\nv_mul_f32 v102,0x3ae38e39,v100\nv_pk_add_f32 v[72:73],v[72:73],v[60:61] op_sel_hi:[1,0]\nv_cvt_pk_bf16_f32 v37,v72,v73\n" \
"v_permlane16_swap_b32 v104,v102\nv_fma_f32 v102,-v104,v104,v102\nv_max_f32 v102,0,v102\nv_add_f32 v102,0x3727c5ac,v102\n" \
"v_rsq_f32_e64 v100,v102\nv_pk_add_f32 v[74:75],v[74:75],v[60:61] op_sel_hi:[1,0]\nv_mul_f32 v101,-v104,v100\nv_pk_fma_f32 v[80:81],v[80:81],v[100:101],v[100:101] op_sel:[0,0,1] op_sel_hi:[1,0,1]\n" \
"v_pk_fma_f32 v[82:83],v[82:83],v[100:101],v[100:101] op_sel:[0,0,1] op_sel_hi:[1,0,1]\nv_pk_fma_f32 v[84:85],v[84:85],v[100:101],v[100:101] op_sel:[0,0,1] op_sel_hi:[1,0,1]\nv_pk_fma_f32 v[86:87],v[86:87],v[100:101],v[100:101] op_sel:[0,0,1] op_sel_hi:[1,0,1]\nv_cvt_pk_bf16_f32 v16,v80,v81\n" \
"v_cvt_pk_bf16_f32 v17,v82,v83\nv_cvt_pk_bf16_f32 v18,v84,v85\nv_cvt_pk_bf16_f32 v19,v86,v87\nds_write_b128 v48,v[16:19]\n" \
"v_pk_fma_f32 v[88:89],v[88:89],v[100:101],v[100:101] op_sel:[0,0,1] op_sel_hi:[1,0,1]\nv_pk_fma_f32 v[90:91],v[90:91],v[100:101],v[100:101] op_sel:[0,0,1] op_sel_hi:[1,0,1]\nv_pk_fma_f32 v[92:93],v[92:93],v[100:101],v[100:101] op_sel:[0,0,1] op_sel_hi:[1,0,1]\nv_pk_fma_f32 v[94:95],v[94:95],v[100:101],v[100:101] op_sel:[0,0,1] op_sel_hi:[1,0,1]\n" \
"v_cvt_pk_bf16_f32 v20,v88,v89\nv_cvt_pk_bf16_f32 v21,v90,v91\nv_cvt_pk_bf16_f32 v22,v92,v93\nv_cvt_pk_bf16_f32 v23,v94,v95\n" \
"ds_write_b128 v48,v[20:23] offset:512\nv_pk_fma_f32 v[96:97],v[96:97],v[100:101],v[100:101] op_sel:[0,0,1] op_sel_hi:[1,0,1]\nv_cvt_pk_bf16_f32 v24,v96,v97\nds_write_b32 v49,v24 offset:1024\n" \
"v_cvt_pk_bf16_f32 v38,v74,v75\nv_pk_add_f32 v[76:77],v[76:77],v[60:61] op_sel_hi:[1,0]\nv_cvt_pk_bf16_f32 v39,v76,v77\nv_pk_add_f32 v[78:79],v[78:79],v[60:61] op_sel_hi:[1,0]\n" \
"v_cvt_pk_bf16_f32 v40,v78,v79\nv_bfe_u32 v50,v0,2,2\nv_mul_u32_u24 v50,4896,v50\nv_bfe_u32 v51,v0,4,2\n" \
"v_lshl_add_u32 v51,v51,2,s3\nv_lshl_add_u32 v50,v51,5,v50\nv_and_b32 v52,3,v0\nv_lshl_add_u32 v50,v52,3,v50\n" \
"v_and_b32 v52,0xf0,v1\nv_lshl_add_u32 v51,v51,16,v52\nv_bfrev_b32 v52,1\nv_cmp_gt_u32 vcc,256,v1\n" \
"v_and_b32 v53,31,v0\nv_mul_u32_u24 v53,12,v53\nv_cndmask_b32 v52,v52,v51,vcc\nv_sub_u32 v53,v12,v53\n" \
"s_lshl_b32 s24,s34,8\ns_add_u32 s24,s24,9764864\ns_waitcnt lgkmcnt(0)\ns_barrier\n" \
"ds_read_b64_tr_b16 v[112:113],v50 offset:9216\nds_read_b64_tr_b16 v[114:115],v50 offset:11664\nds_read_b64_tr_b16 v[116:117],v50 offset:9728\nds_read_b64_tr_b16 v[118:119],v50 offset:12176\n" \
"ds_read_b64_tr_b16 v[120:121],v50 offset:10240\nds_read_b64_tr_b16 v[122:123],v50 offset:12688\ns_add_u32 s25,s24,1048576\ns_add_u32 s26,s24,2097152\n" \
"s_add_u32 s13,s13,7405568\ns_load_dwordx2 s[4:5],s[0:1],56\ns_load_dwordx2 s[16:17],s[0:1],64\ns_waitcnt lgkmcnt(0)\n" \
"buffer_store_dwordx4 v[112:115],v51,s[36:39],s24 offen sc1\nbuffer_store_dwordx4 v[116:119],v51,s[36:39],s25 offen sc1\nbuffer_store_dwordx4 v[120:123],v52,s[36:39],s26 offen sc1\nbuffer_store_dwordx4 v[32:35],v12,s[36:39],s13 offen sc1\n" \
"buffer_store_dwordx4 v[36:39],v12,s[36:39],s13 offen offset:512 sc1\nbuffer_store_dword v40,v53,s[36:39],s13 offen offset:1024 sc1\ns_lshr_b32 s100,s33,3\ns_lshl1_add_u32 s100,s100,0\n" \
"s_sub_u32 s101,s100,6\ns_min_u32 s100,s100,s101\ns_lshl_b32 s101,s100,12\ns_add_u32 s101,s101,s57\n" \
"s_add_u32 m0,s101,0\ns_and_b32 s101,s100,1\ns_lshr_b32 s100,s100,1\ns_lshl_b32 s100,s100,10\n" \
"s_add_u32 s100,s100,s101\ns_lshl_b32 s100,s100,7\ns_add_u32 s100,s100,s92\ns_add_u32 s100,s100,262144\n" \
"s_lshr_b32 s31,s33,3\ns_lshl1_add_u32 s31,s31,1\ns_sub_u32 s96,s31,6\ns_min_u32 s31,s31,s96\n" \
"s_lshl_b32 s96,s31,12\ns_add_u32 s96,s96,s57\ns_lshr_b32 s97,s33,3\ns_lshl1_add_u32 s97,s97,2\n" \
"s_sub_u32 s95,s97,6\ns_min_u32 s97,s97,s95\ns_lshl_b32 s95,s97,12\ns_add_u32 s95,s95,s57\n" \
"v_and_b32 v64,63,v0\ns_and_b32 s25,s33,7\nv_lshrrev_b32 v66,5,v64\nv_and_b32 v67,1,v0\n" \
"s_lshl2_add_u32 s25,s25,s3\ns_and_b32 s26,s33,6\ns_lshr_b32 s27,s3,1\nv_lshl_add_u32 v66,v66,1,v67\n" \
"s_mul_i32 s24,s32,1152\ns_lshl_b32 s25,s25,5\ns_add_u32 s26,s26,s27\ns_mov_b32 s6,9216\n" \
"s_mov_b32 s7,0x20000\nv_lshlrev_b32 v68,3,v66\ns_add_u32 s25,s25,s24\nv_bfe_u32 v72,v0,1,1\n" \
"v_lshrrev_b32 v74,2,v64\ns_lshl4_add_u32 s26,s26,s24\nv_and_b32 v73,12,v0\nv_lshrrev_b32 v71,4,v64\n" \
"v_and_b32 v69,7,v0\nbuffer_load_dwordx2 v[80:81],v68,s[4:7],s25 offen\nv_lshl_add_u32 v72,v72,2,s21\nv_and_b32 v74,12,v74\n" \
"v_add_u32 v71,v71,v73\ns_lshl_b32 s24,s3,4\nv_lshlrev_b32 v70,1,v69\nv_lshl_add_u32 v72,v72,4,v74\n" \
"s_mov_b32 s15,s19\nv_add_u32 v71,s24,v71\nbuffer_load_ushort v82,v70,s[4:7],s26 offen offset:1024\ns_mov_b32 s18,4096\n" \
"s_mov_b32 s19,0x20000\nv_lshlrev_b32 v74,1,v71\ns_lshl_b32 s24,s22,8\nbuffer_load_ushort v83,v74,s[16:19],s24 offen\n" \
"v_add_u32 v72,s3,v72\nbuffer_load_ushort v84,v74,s[16:19],s24 offen offset:128\nv_lshlrev_b32_e64 v75,1,v72\nbuffer_load_ushort v85,v75,s[16:19],s24 offen\n" \
"v_lshl_add_u32 v73,v67,1,v73\nv_mul_u32_u24 v91,1152,v72\nv_lshl_add_u32 v91,v73,1,v91\nv_lshlrev_b32_e64 v73,2,v73\n" \
"v_add_u32 v73,163712,v73\nds_read_b64 v[86:87],v73\ns_mul_i32 s24,s22,147456\ns_mul_i32 s25,s20,288\n" \
"v_bfe_u32 v58,v0,1,1\ns_add_u32 s24,s24,s25\nv_mul_u32_u24 v58,544,v58\nv_bfe_u32 v75,v0,2,1\n" \
"s_lshl_b32 s25,s21,6\nv_lshl_add_u32 v58,v75,8,v58\ns_add_u32 s25,s25,s24\nv_bfe_u32 v75,v0,3,2\n" \
"s_add_u32 s24,s24,256\nv_lshl_add_u32 v58,v75,6,v58\nv_add_u32 v91,s24,v91\nv_mul_u32_u24 v90,1152,v71\n" \
"v_and_b32 v74,3,v0\nv_lshl_add_u32 v58,v66,4,v58\ns_mul_i32 s24,s3,1056\nv_lshl_add_u32 v90,v74,3,v90\n" \
"v_add_u32 v58,s24,v58\nv_add_u32 v90,s25,v90\ns_and_b32 s24,s3,1\nv_mul_u32_u24 v60,288,v75\n" \
"s_lshl_b32 s24,s24,2\ns_lshr_b32 s25,s3,1\nv_lshrrev_b32 v75,5,v64\ns_add_u32 s24,s24,s25\n" \
"v_lshl_add_u32 v60,v75,6,v60\ns_lshl_b32 s24,s24,5\nv_lshl_add_u32 v60,v69,2,v60\ns_add_u32 s24,s24,4224\n" \
"v_add_u32 v60,s24,v60\ns_bfe_u32 s24,s33,0x10001\ns_add_u32 s24,s24,s15\ns_add_u32 s24,s24,17\n" \
"s_lshl2_add_u32 s24,s24,s3\ns_waitcnt lgkmcnt(0)\nv_lshrrev_b32 v86,16,v86\ns_lshl_b32 s24,s24,7\n" \
"v_mov_b32 v92,v4\nv_mov_b32 v93,v5\nv_add_u32 v74,s57,v1\nv_or_b32 v95,v86,v87\n" \
"v_lshl_add_u32 v62,v64,1,s24\nv_bfrev_b32 v76,1\nv_cmp_eq_u32 vcc,0,v64\ns_lshl2_add_u32 s24,s34,s3\n" \
"s_lshl2_add_u32 s24,s24,65536\nv_cndmask_b32 v76,v76,0,vcc\nv_mov_b32 v77,1\ns_barrier\n" \
"v_mov_b32 v57,0xc0135761\nv_add_u32 v59,0x80000,v1\nv_lshl_add_u32 v61,v64,3,s23\nv_mov_b32 v63,0\n" \
"v_bfe_u32 v100,v0,4,2\nv_bfe_u32 v101,v0,2,2\nv_lshl_add_u32 v100,v100,3,v101\nv_bfe_u32 v101,v0,3,2\n" \
"v_lshl_add_u32 v100,v100,2,v101\nv_and_b32 v101,3,v0\nv_lshl_add_u32 v100,v100,2,v101\nv_lshlrev_b32 v100,3,v100\n" \
"v_xor_b32 v8,0,v100\nv_add_u32 v8,s57,v8\nv_mov_b32 v88,v8\nv_xor_b32 v9,32,v100\n" \
"v_add_u32 v9,s57,v9\nv_xor_b32 v10,64,v100\nv_add_u32 v10,s57,v10\nv_xor_b32 v11,96,v100\n" \
"v_add_u32 v11,s57,v11\ns_waitcnt vmcnt(5)\nbuffer_store_dword v77,v76,s[36:39],s24 offen sc1\nds_read_b64_tr_b16 v[128:129],v8\n" \
"ds_read_b64_tr_b16 v[130:131],v8 offset:512\nds_read_b64_tr_b16 v[132:133],v9\nds_read_b64_tr_b16 v[134:135],v9 offset:512\nds_read_b64_tr_b16 v[136:137],v10\n" \
"ds_read_b64_tr_b16 v[138:139],v10 offset:512\nds_read_b64_tr_b16 v[140:141],v11\nds_read_b64_tr_b16 v[142:143],v11 offset:512\nds_read_b64_tr_b16 v[144:145],v8 offset:4096\n" \
"ds_read_b64_tr_b16 v[146:147],v8 offset:4608\nds_read_b64_tr_b16 v[148:149],v9 offset:4096\nds_read_b64_tr_b16 v[150:151],v9 offset:4608\nds_read_b64_tr_b16 v[152:153],v10 offset:4096\n" \
"ds_read_b64_tr_b16 v[154:155],v10 offset:4608\nds_read_b64_tr_b16 v[156:157],v11 offset:4096\nds_read_b64_tr_b16 v[158:159],v11 offset:4608\nds_read_b64_tr_b16 v[160:161],v8 offset:8192\n" \
"ds_read_b64_tr_b16 v[162:163],v8 offset:8704\ns_waitcnt vmcnt(5)\nv_and_b32 v53,0xffff0000,v80\nv_and_b32 v55,0xffff0000,v81\n" \
"v_lshlrev_b32 v52,16,v80\nv_lshlrev_b32 v54,16,v81\nds_read_b64_tr_b16 v[164:165],v9 offset:8192\nds_read_b64_tr_b16 v[166:167],v9 offset:8704\n" \
"ds_read_b64_tr_b16 v[168:169],v10 offset:8192\nds_read_b64_tr_b16 v[170:171],v10 offset:8704\nds_read_b64_tr_b16 v[172:173],v11 offset:8192\ns_waitcnt vmcnt(4)\n" \
"v_lshlrev_b32 v56,16,v82\nds_read_b64_tr_b16 v[174:175],v11 offset:8704\nds_read_b64_tr_b16 v[176:177],v8 offset:12288\nds_read_b64_tr_b16 v[178:179],v8 offset:12800\n" \
"ds_read_b64_tr_b16 v[180:181],v9 offset:12288\nds_read_b64_tr_b16 v[182:183],v9 offset:12800\ns_waitcnt vmcnt(2)\nv_lshl_or_b32 v89,v84,16,v83\n" \
"ds_write_b128 v74,v[88:91] offset:28672\ns_waitcnt vmcnt(1)\nv_lshlrev_b32_e64 v94,16,v85\nds_write_b128 v74,v[92:95] offset:29696\n" \
"ds_read_b64_tr_b16 v[184:185],v10 offset:12288\nds_read_b64_tr_b16 v[186:187],v10 offset:12800\nds_read_b64_tr_b16 v[188:189],v11 offset:12288\nds_read_b64_tr_b16 v[190:191],v11 offset:12800\n" \
"s_waitcnt lgkmcnt(0)\nbuffer_load_dwordx4 v4,s[40:43],s100 offen lds\nbuffer_load_dwordx4 v5,s[40:43],s100 offen offset:1024 lds\nbuffer_load_dwordx4 v6,s[40:43],s100 offen offset:2048 lds\n" \
"buffer_load_dwordx4 v7,s[40:43],s100 offen offset:3072 lds\ns_add_u32 m0,s96,0\ns_and_b32 s96,s31,1\ns_lshr_b32 s31,s31,1\n" \
"s_lshl_b32 s31,s31,10\ns_add_u32 s31,s31,s96\ns_lshl_b32 s31,s31,7\ns_add_u32 s31,s31,s92\n" \
"s_add_u32 s31,s31,262144\nbuffer_load_dwordx4 v4,s[40:43],s31 offen lds\nbuffer_load_dwordx4 v5,s[40:43],s31 offen offset:1024 lds\nbuffer_load_dwordx4 v6,s[40:43],s31 offen offset:2048 lds\n" \
"buffer_load_dwordx4 v7,s[40:43],s31 offen offset:3072 lds\ns_add_u32 m0,s95,0\ns_and_b32 s95,s97,1\ns_lshr_b32 s97,s97,1\n" \
"s_lshl_b32 s97,s97,10\ns_add_u32 s97,s97,s95\ns_lshl_b32 s97,s97,7\ns_add_u32 s97,s97,s92\n" \
"s_add_u32 s97,s97,262144\nbuffer_load_dwordx4 v4,s[40:43],s97 offen lds\nbuffer_load_dwordx4 v5,s[40:43],s97 offen offset:1024 lds\nbuffer_load_dwordx4 v6,s[40:43],s97 offen offset:2048 lds\n" \
"buffer_load_dwordx4 v7,s[40:43],s97 offen offset:3072 lds\ns_lshr_b32 s100,s33,3\ns_lshl1_add_u32 s100,s100,3\ns_sub_u32 s101,s100,6\n" \
"s_min_u32 s100,s100,s101\ns_lshl_b32 s101,s100,12\ns_add_u32 s101,s101,s57\ns_add_u32 m0,s101,0\n" \
"s_and_b32 s101,s100,1\ns_lshr_b32 s100,s100,1\ns_lshl_b32 s100,s100,10\ns_add_u32 s100,s100,s101\n" \
"s_lshl_b32 s100,s100,7\ns_add_u32 s100,s100,s92\ns_add_u32 s100,s100,262144\nbuffer_load_dwordx4 v4,s[40:43],s100 offen lds\n" \
"buffer_load_dwordx4 v5,s[40:43],s100 offen offset:1024 lds\nbuffer_load_dwordx4 v6,s[40:43],s100 offen offset:2048 lds\nbuffer_load_dwordx4 v7,s[40:43],s100 offen offset:3072 lds\ns_lshr_b32 s31,s33,3\n" \
"s_lshl1_add_u32 s31,s31,4\ns_sub_u32 s96,s31,6\ns_min_u32 s31,s31,s96\ns_lshl_b32 s96,s31,12\n" \
"s_add_u32 s96,s96,s57\ns_add_u32 m0,s96,0\ns_and_b32 s96,s31,1\ns_lshr_b32 s31,s31,1\n" \
"s_lshl_b32 s31,s31,10\ns_add_u32 s31,s31,s96\ns_lshl_b32 s31,s31,7\ns_add_u32 s31,s31,s92\n" \
"s_add_u32 s31,s31,262144\nbuffer_load_dwordx4 v4,s[40:43],s31 offen lds\nbuffer_load_dwordx4 v5,s[40:43],s31 offen offset:1024 lds\nbuffer_load_dwordx4 v6,s[40:43],s31 offen offset:2048 lds\n" \
"buffer_load_dwordx4 v7,s[40:43],s31 offen offset:3072 lds\ns_lshr_b32 s97,s33,3\ns_lshl1_add_u32 s97,s97,5\ns_sub_u32 s95,s97,6\n" \
"s_min_u32 s97,s97,s95\ns_lshl_b32 s95,s97,12\ns_add_u32 s95,s95,s57\ns_add_u32 m0,s95,0\n" \
"s_and_b32 s95,s97,1\ns_lshr_b32 s97,s97,1\ns_lshl_b32 s97,s97,10\ns_add_u32 s97,s97,s95\n" \
"s_lshl_b32 s97,s97,7\ns_add_u32 s97,s97,s92\ns_add_u32 s97,s97,262144\nbuffer_load_dwordx4 v4,s[40:43],s97 offen lds\n" \
"buffer_load_dwordx4 v5,s[40:43],s97 offen offset:1024 lds\nbuffer_load_dwordx4 v6,s[40:43],s97 offen offset:2048 lds\nbuffer_load_dwordx4 v7,s[40:43],s97 offen offset:3072 lds\ns_branch .Lgemm_%=\n" \
".LN_%=:\nv_bfe_u32 v112,v0,3,2\nv_bfe_u32 v113,v0,5,1\nv_lshl_add_u32 v112,v112,8,v113\n" \
"s_lshl_b32 s48,s33,2\nv_lshrrev_b32 v113,3,v0\ns_add_u32 s48,s48,s3\nv_and_b32 v113,6,v113\n" \
"v_and_b32 v114,7,v0\ns_mul_i32 s48,s48,18432\ns_mul_i32 s49,s32,1152\nv_mul_u32_u24 v112,9216,v112\n" \
"v_xor_b32 v113,v113,v114\ns_add_u32 s48,s48,s49\nv_lshl_add_u32 v4,v113,4,v112\ns_mov_b32 s10,18874368\n" \
"s_mov_b32 s11,0x20000\ns_add_u32 s48,s48,1024\ns_add_u32 m0,s88,20224\ns_add_u32 s50,s48,9437184\n" \
"buffer_load_dwordx4 v4,s[8:11],s48 offen lds\ns_add_u32 m0,s88,24320\ns_mov_b32 s51,4096\nbuffer_load_dwordx4 v4,s[8:11],s50 offen lds\n" \
"s_add_u32 m0,s88,28416\ns_mov_b32 s52,8192\nbuffer_load_dwordx4 v2,s[4:7],0 offen lds\ns_add_u32 m0,s88,32512\n" \
"s_mov_b32 s53,14336\nbuffer_load_dwordx4 v2,s[4:7],s51 offen lds\nbuffer_load_dwordx4 v[234:237],v1,s[4:7],s52 offen\nbuffer_load_dwordx4 v[238:241],v1,s[4:7],s52 offen offset:1024\n" \
"buffer_load_dwordx4 v[242:245],v2,s[4:7],s53 offen\ns_mov_b32 s52,18432\nbuffer_load_dwordx4 v[252:255],v2,s[4:7],s52 offen\ns_and_b32 s60,s33,7\n" \
"s_mul_i32 s92,s3,589824\ns_mul_i32 s57,s3,30720\ns_add_u32 s92,s92,s49\ns_lshl_b32 s62,s60,7\n" \
"v_add_u32 v5,17408,v4\nv_add_u32 v6,34816,v4\nv_add_u32 v7,52224,v4\ns_add_u32 s57,s57,38272\n" \
"s_add_u32 s92,s92,s62\ns_load_dwordx4 s[44:47],s[0:1],72\nv_and_b32 v120,0x3c0,v1\ns_lshr_b32 s59,s33,3\n" \
"v_bfe_u32 v121,v0,3,2\nv_lshlrev_b32 v120,1,v120\ns_lshl_b32 s56,s34,8\ns_lshl_b32 s59,s59,3\n" \
"v_bfe_u32 v113,v0,4,2\nv_lshl_add_u32 v120,v121,5,v120\nv_and_b32 v122,3,v0\ns_mul_i32 s54,s34,9344\n" \
"s_add_u32 s56,s56,10289152\ns_add_u32 s59,s59,s32\nv_mul_u32_u24 v113,589824,v113\nv_and_b32 v112,15,v0\n" \
"v_lshl_add_u32 v120,v122,3,v120\ns_add_u32 s35,s54,524288\ns_cmp_eq_u32 s3,2\ns_cselect_b32 s56,s56,2147483648\n" \
"s_lshl_b32 s59,s59,3\nv_lshl_add_u32 v113,v112,4,v113\ns_add_u32 s58,s35,8192\nv_bfe_u32 v119,v0,4,2\n" \
"v_bfe_u32 v233,v0,3,3\nv_mul_u32_u24 v233,8192,v233\nv_lshl_add_u32 v233,v1,3,v233\ns_lshl2_add_u32 s100,s33,s3\n" \
"s_add_u32 s100,s100,4294967260\ns_and_b32 s101,s100,31\ns_lshl_b32 s101,s101,3\ns_cmp_lt_u32 s100,256\n" \
"s_cselect_b32 s100,s100,0x40000\ns_lshr_b32 s100,s100,5\ns_add_u32 s100,s100,s101\ns_lshl_b32 s100,s100,6\n" \
"s_add_u32 s100,s100,s32\ns_mul_i32 s100,s100,1152\ns_mov_b32 m0,162176\nv_xor_b32 v8,0,v120\n" \
"v_xor_b32 v9,32,v120\nv_xor_b32 v10,64,v120\nv_xor_b32 v11,96,v120\ns_add_u32 s59,s59,s60\n" \
"v_and_b32 v118,15,v0\nv_and_b32 v116,0x70,v2\nv_add_u32 v113,s56,v113\nv_add_u32 v114,s58,v2\n" \
"v_cmp_gt_u32 vcc,68,v0\nv_lshrrev_b32 v115,3,v0\nv_cvt_f32_u32 v120,s34\ns_lshl_b32 s59,s59,7\n" \
"s_lshl_b32 s61,s34,4\nv_lshrrev_b32 v48,1,v118\nv_and_b32 v49,1,v119\nv_cndmask_b32 v113,v113,v114,vcc\n" \
"v_lshl_add_u32 v115,v115,16,v116\nv_mul_f32 v50,0x3b800000,v120\ns_add_u32 s87,s59,262144\ns_add_u32 s61,s61,65536\n" \
"v_cvt_f32_u32 v48,v48\nv_cvt_f32_u32 v49,v49\nv_bfe_u32 v100,v0,1,2\nv_and_b32 v101,1,v0\n" \
"v_add_u32 v115,s87,v115\nv_mov_b32 v116,s61\nv_cmp_gt_u32 vcc,24,v0\nv_mul_f32 v48,0xbe000000,v48\n" \
"v_fma_f32 v49,v49,2.0,v50\nv_lshl_add_u32 v100,v101,2,v100\nv_cndmask_b32 v115,v116,v115,vcc\nv_mul_f32 v49,v48,v49\n" \
"v_mul_f32 v101,0x3a000000,v120\nv_cvt_f32_u32 v100,v100\nv_bfrev_b32 v116,1\nv_cmp_gt_u32 vcc,25,v0\n" \
"v_fmamk_f32 v63,v48,0x40800000,v49\nv_fmamk_f32 v100,v100,0x3e000000,v101\nv_cndmask_b32 v115,v116,v115,vcc\nv_mov_b32 v16,-1\n" \
"v_mov_b32 v17,-1\nv_mov_b32 v18,-1\nv_mov_b32 v19,-1\nv_lshrrev_b32 v50,1,v119\n" \
"v_and_b32 v51,1,v0\nv_cos_f32 v62,v63\nv_cvt_f32_u32 v101,v119\nbuffer_store_dwordx4 v[16:19],v2,s[36:39],s35 offen sc1\n" \
"v_cmp_eq_u32 vcc,v50,v51\nv_add_f32 v101,v101,v101\nv_mov_b32 v50,0x3e800000\nv_mul_f32 v101,v101,v100\n" \
"v_cndmask_b32 v52,0,v50,vcc\nv_cos_f32 v56,v49\nv_sin_f32 v57,v49\nv_sin_f32 v63,v63\n" \
"v_cos_f32 v106,v101\nv_sin_f32 v107,v101\nv_cmp_gt_u32 vcc,8,v118\ns_add_u32 s55,s35,4096\n" \
"v_lshlrev_b32 v51,31,v0\nv_cndmask_b32 v102,0,0.5,vcc\nbuffer_store_dwordx4 v[16:19],v2,s[36:39],s55 offen sc1\nv_xor_b32 v54,v51,v52\n" \
"v_sub_f32_e64 v104,0.5,v102\nv_pk_mul_f32 v[58:59],v[56:57],v[52:53] op_sel_hi:[1,0]\nv_pk_mul_f32 v[60:61],v[56:57],v[54:55] op_sel_hi:[1,0]\nv_pk_mul_f32 v[64:65],v[62:63],v[52:53] op_sel_hi:[1,0]\n" \
"v_pk_mul_f32 v[66:67],v[62:63],v[54:55] op_sel_hi:[1,0]\nv_pk_mul_f32 v[108:109],v[106:107],v[102:103] op_sel_hi:[1,0]\nv_pk_mul_f32 v[110:111],v[106:107],v[104:105] op_sel_hi:[1,0]\nv_fmamk_f32 v57,v48,0x3f800000,v49\n" \
"v_fmamk_f32 v63,v48,0x40a00000,v49\nv_fmamk_f32 v107,v100,0x3f800000,v101\nv_cos_f32 v56,v57\nv_cos_f32 v62,v63\n" \
"v_cos_f32 v106,v107\nbuffer_store_dwordx4 v[16:19],v113,s[36:39],0 offen sc1\nv_sin_f32 v57,v57\nv_sin_f32 v63,v63\n" \
"v_sin_f32 v107,v107\nv_mov_b32 v20,0\nv_mov_b32 v21,0\nv_mov_b32 v22,0\n" \
"v_mov_b32 v23,0\nbuffer_store_dwordx4 v[20:23],v115,s[36:39],0 offen sc1\nv_cvt_pk_f16_f32 v40,v58,-v61\nv_cvt_pk_f16_f32 v44,v59,v60\n" \
"v_cvt_pk_f16_f32 v41,v64,-v67\nv_cvt_pk_f16_f32 v45,v65,v66\nv_cvt_pk_f16_f32 v216,v108,-v109\nv_cvt_pk_f16_f32 v220,v109,v108\n" \
"v_cvt_pk_f16_f32 v217,v110,-v111\nv_cvt_pk_f16_f32 v221,-v111,-v110\nv_pk_mul_f32 v[58:59],v[56:57],v[52:53] op_sel_hi:[1,0]\nv_pk_mul_f32 v[60:61],v[56:57],v[54:55] op_sel_hi:[1,0]\n" \
"v_pk_mul_f32 v[64:65],v[62:63],v[52:53] op_sel_hi:[1,0]\nv_pk_mul_f32 v[66:67],v[62:63],v[54:55] op_sel_hi:[1,0]\nv_pk_mul_f32 v[108:109],v[106:107],v[102:103] op_sel_hi:[1,0]\nv_pk_mul_f32 v[110:111],v[106:107],v[104:105] op_sel_hi:[1,0]\n" \
"v_lshlrev_b32 v3,4,v119\nv_lshrrev_b32 v15,1,v1\nv_lshl_add_u32 v3,v118,6,v3\nbuffer_load_dword v233,s[8:11],s100 offen lds\n" \
"v_cvt_pk_f16_f32 v42,v58,-v61\nv_cvt_pk_f16_f32 v46,v59,v60\nv_cvt_pk_f16_f32 v43,v64,-v67\nv_cvt_pk_f16_f32 v47,v65,v66\n" \
"v_cvt_pk_f16_f32 v218,v108,-v109\nv_cvt_pk_f16_f32 v222,v109,v108\nv_cvt_pk_f16_f32 v219,v110,-v111\nv_cvt_pk_f16_f32 v223,-v111,-v110\n" \
"v_add_u32 v15,s57,v15\nv_add_u32 v8,s57,v8\nv_add_u32 v9,s57,v9\nv_add_u32 v10,s57,v10\n" \
"v_add_u32 v11,s57,v11\nv_lshrrev_b32 v77,5,v0\nv_bfe_u32 v113,v0,1,5\nv_and_b32 v115,1,v0\n" \
"s_lshl_b32 s63,s3,1\nv_mul_u32_u24 v77,1160,v77\nv_and_b32 v76,31,v0\nv_and_b32 v114,6,v113\n" \
"v_add_u32 v115,s63,v115\nv_xor_b32 v114,v114,v115\nv_lshlrev_b32 v113,7,v113\nv_lshl_add_u32 v113,v114,4,v113\n" \
"s_add_u32 s64,s54,2916352\ns_add_u32 s65,s64,4096\nv_bfrev_b32 v114,1\ns_lshl1_add_u32 s95,s33,s3\n" \
"s_add_u32 s95,s95,74\ns_cmp_lt_u32 s3,2\ns_cselect_b32 s95,s95,256\ns_and_b32 s100,s95,31\n" \
"s_lshl_b32 s100,s100,3\ns_cmp_lt_u32 s95,256\ns_cselect_b32 s95,s95,0x40000\ns_lshr_b32 s95,s95,5\n" \
"s_add_u32 s95,s95,s100\ns_lshl_b32 s95,s95,6\ns_add_u32 s95,s95,s32\ns_mul_i32 s95,s95,1152\n" \
"s_mov_b32 m0,162176\ns_lshl_b32 s101,s33,7\ns_sub_u32 s101,s101,1280\nv_add_u32 v79,s101,v0\n" \
"v_mul_u32_u24 v78,52429,v79\nv_lshrrev_b32 v78,18,v78\nv_lshlrev_b32 v79,7,v79\ns_mov_b32 s101,6272\n" \
"v_mad_u32_u24 v79,v78,s101,v79\ns_mov_b32 s26,3981312\ns_mov_b32 s27,0x20000\nv_mad_u32_u24 v78,v76,36,v77\n" \
"v_cmp_gt_u32 vcc,72,v0\ns_lshl_b32 s67,s32,2\ns_add_u32 s66,s64,8192\nv_cndmask_b32 v114,v114,v2,vcc\n" \
"s_add_u32 s67,s67,s3\ns_lshl_b32 s67,s67,5\ns_add_u32 s67,s67,s33\ns_lshl_b32 s67,s67,11\n" \
"s_add_u32 s67,s67,5308416\ns_lshl2_add_u32 s69,s3,161664\ns_waitcnt vmcnt(20)\nv_readfirstlane_b32 s28,v126\n" \
"v_readfirstlane_b32 s29,v127\ns_mul_i32 s72,s3,2320\nv_mov_b32 v251,s69\nv_cmp_gt_u32 vcc,32,v0\n" \
"s_add_u32 s28,s28,1\ns_addc_u32 s29,s29,0\ns_lshl_b32 s7,s32,11\nv_cndmask_b32 v74,2.0,0,vcc\n" \
"s_mov_b32 s93,128\ns_mov_b32 s73,8192\ns_mov_b32 s74,12288\ns_mov_b32 s75,17408\n" \
"s_mov_b32 s76,21504\ns_mov_b32 s77,16384\ns_mov_b32 s78,25600\ns_add_u32 s7,s7,40960\n" \
"s_mov_b32 s81,0x3d800000\ns_mov_b32 s42,18874368\ns_mov_b32 s43,0x20000\ns_waitcnt lgkmcnt(0)\n" \
"s_mov_b64 s[40:41],s[44:45]\ns_mov_b64 s[44:45],s[46:47]\ns_mov_b32 s46,2359296\ns_mov_b32 s47,0x20000\n" \
"s_waitcnt vmcnt(19)\nv_lshlrev_b32_e64 v48,16,v224\nv_and_b32 v49,0xffff0000,v224\nv_readlane_b32 s70,v48,0\n" \
"v_readlane_b32 s71,v48,32\nv_and_b32 v51,0xffff0000,v225\nv_mov_b32 v72,s70\nv_mov_b32 v73,s71\n" \
"v_lshlrev_b32 v50,16,v225\nv_and_b32 v53,0xffff0000,v226\nv_permlane32_swap_b32 v72,v73\nv_pk_add_f32 v[48:49],v[48:49],v[72:73] op_sel_hi:[1,0] neg_lo:[0,1] neg_hi:[0,1]\n" \
"v_pk_add_f32 v[50:51],v[50:51],v[72:73] op_sel_hi:[1,0] neg_lo:[0,1] neg_hi:[0,1]\nv_pk_mul_f32 v[68:69],v[48:49],v[48:49]\nv_lshlrev_b32_e64 v52,16,v226\nv_and_b32 v55,0xffff0000,v227\n" \
"v_pk_add_f32 v[52:53],v[52:53],v[72:73] op_sel_hi:[1,0] neg_lo:[0,1] neg_hi:[0,1]\nv_pk_add_f32 v[66:67],v[48:49],v[50:51]\nv_pk_fma_f32 v[68:69],v[50:51],v[50:51],v[68:69]\nv_lshlrev_b32 v54,16,v227\n" \
"s_waitcnt vmcnt(18)\nv_and_b32 v57,0xffff0000,v228\nv_pk_add_f32 v[54:55],v[54:55],v[72:73] op_sel_hi:[1,0] neg_lo:[0,1] neg_hi:[0,1]\nv_pk_add_f32 v[66:67],v[66:67],v[52:53]\n" \
"v_pk_fma_f32 v[68:69],v[52:53],v[52:53],v[68:69]\nv_lshlrev_b32_e64 v56,16,v228\nv_and_b32 v59,0xffff0000,v229\nv_pk_add_f32 v[56:57],v[56:57],v[72:73] op_sel_hi:[1,0] neg_lo:[0,1] neg_hi:[0,1]\n" \
"v_pk_add_f32 v[66:67],v[66:67],v[54:55]\nv_pk_fma_f32 v[68:69],v[54:55],v[54:55],v[68:69]\nv_lshlrev_b32_e64 v58,16,v229\nv_and_b32 v61,0xffff0000,v230\n" \
"v_pk_add_f32 v[58:59],v[58:59],v[72:73] op_sel_hi:[1,0] neg_lo:[0,1] neg_hi:[0,1]\nv_pk_add_f32 v[66:67],v[66:67],v[56:57]\nv_pk_fma_f32 v[68:69],v[56:57],v[56:57],v[68:69]\nv_lshlrev_b32_e64 v60,16,v230\n" \
"v_and_b32 v63,0xffff0000,v231\nv_pk_add_f32 v[60:61],v[60:61],v[72:73] op_sel_hi:[1,0] neg_lo:[0,1] neg_hi:[0,1]\nv_pk_add_f32 v[66:67],v[66:67],v[58:59]\nv_pk_fma_f32 v[68:69],v[58:59],v[58:59],v[68:69]\n" \
"v_lshlrev_b32 v62,16,v231\ns_waitcnt vmcnt(17)\nv_and_b32 v65,0xffff0000,v232\nv_pk_add_f32 v[62:63],v[62:63],v[72:73] op_sel_hi:[1,0] neg_lo:[0,1] neg_hi:[0,1]\n" \
"v_pk_add_f32 v[66:67],v[66:67],v[60:61]\nv_pk_fma_f32 v[68:69],v[60:61],v[60:61],v[68:69]\nv_lshlrev_b32_e64 v64,16,v232\nv_pk_add_f32 v[64:65],v[64:65],v[72:73] op_sel_hi:[1,0] neg_lo:[0,1] neg_hi:[0,1]\n" \
"v_pk_add_f32 v[66:67],v[66:67],v[62:63]\nv_pk_fma_f32 v[68:69],v[62:63],v[62:63],v[68:69]\nv_pk_add_f32 v[66:67],v[66:67],v[64:65]\nv_pk_fma_f32 v[68:69],v[64:65],v[64:65],v[68:69]\n" \
"v_add_f32 v66,v66,v67\nv_add_f32 v68,v68,v69\ns_nop 1\nv_permlane16_swap_b32 v66,v68\n" \
"v_add_f32 v66,v66,v68\ns_nop 1\nv_add_f32_dpp v66,v66,v66 quad_perm:[1,0,3,2] row_mask:0xf bank_mask:0xf bound_ctrl:1\ns_nop 1\n" \
"v_add_f32_dpp v66,v66,v66 quad_perm:[2,3,0,1] row_mask:0xf bank_mask:0xf bound_ctrl:1\ns_nop 1\nv_add_f32_dpp v66,v66,v66 row_half_mirror row_mask:0xf bank_mask:0xf bound_ctrl:1\ns_nop 1\n" \
"v_add_f32_dpp v66,v66,v66 row_mirror row_mask:0xf bank_mask:0xf bound_ctrl:1\nv_mul_f32 v70,0x3ae38e39,v66\nv_mul_f32 v68,0x3ae38e39,v66\ns_nop 1\n" \
"v_permlane16_swap_b32 v70,v68\nv_fma_f32 v68,-v70,v70,v68\nv_max_f32 v68,0,v68\nv_add_f32 v68,0x3727c5ac,v68\n" \
"v_rsq_f32 v66,v68\nv_add_f32 v250,v70,v72\nv_mul_f32 v67,-v70,v66\nv_pk_fma_f32 v[48:49],v[48:49],v[66:67],v[66:67] op_sel:[0,0,1] op_sel_hi:[1,0,1]\n" \
"v_pk_fma_f32 v[50:51],v[50:51],v[66:67],v[66:67] op_sel:[0,0,1] op_sel_hi:[1,0,1]\nv_cvt_pk_f16_f32 v80,v48,v49\nv_cvt_pk_f16_f32 v81,v50,v51\nv_pk_fma_f32 v[52:53],v[52:53],v[66:67],v[66:67] op_sel:[0,0,1] op_sel_hi:[1,0,1]\n" \
"v_pk_fma_f32 v[54:55],v[54:55],v[66:67],v[66:67] op_sel:[0,0,1] op_sel_hi:[1,0,1]\nds_write2_b32 v78,v80,v81 offset0:0 offset1:1\nv_cvt_pk_f16_f32 v82,v52,v53\nv_cvt_pk_f16_f32 v83,v54,v55\n" \
"v_pk_fma_f32 v[56:57],v[56:57],v[66:67],v[66:67] op_sel:[0,0,1] op_sel_hi:[1,0,1]\nv_pk_fma_f32 v[58:59],v[58:59],v[66:67],v[66:67] op_sel:[0,0,1] op_sel_hi:[1,0,1]\nds_write2_b32 v78,v82,v83 offset0:2 offset1:3\nv_cvt_pk_f16_f32 v84,v56,v57\n" \
"v_cvt_pk_f16_f32 v85,v58,v59\nv_pk_fma_f32 v[60:61],v[60:61],v[66:67],v[66:67] op_sel:[0,0,1] op_sel_hi:[1,0,1]\nv_pk_fma_f32 v[62:63],v[62:63],v[66:67],v[66:67] op_sel:[0,0,1] op_sel_hi:[1,0,1]\nds_write2_b32 v78,v84,v85 offset0:4 offset1:5\n" \
"v_cvt_pk_f16_f32 v86,v60,v61\nv_cvt_pk_f16_f32 v87,v62,v63\nv_pk_fma_f32 v[64:65],v[64:65],v[66:67],v[66:67] op_sel:[0,0,1] op_sel_hi:[1,0,1]\nds_write2_b32 v78,v86,v87 offset0:6 offset1:7\n" \
"v_cvt_pk_f16_f32 v88,v64,v65\nds_write_b32 v78,v88 offset:32\ns_waitcnt vmcnt(11)\ns_barrier\n" \
"ds_read_u16 v16,v246\nds_read_u16 v17,v246 offset:5216\nds_read_u16 v18,v246 offset:578\nds_read_u16 v19,v246 offset:5794\n" \
"ds_read_u16 v20,v246 offset:1156\nds_read_u16 v21,v246 offset:6372\nds_read_u16 v22,v246 offset:1734\nds_read_u16 v23,v246 offset:6950\n" \
"s_waitcnt vmcnt(8)\nv_lshrrev_b32 v96,1,v234\nds_read_u16 v24,v247\nv_lshrrev_b32 v97,1,v235\n" \
"v_lshrrev_b32 v98,1,v236\ns_waitcnt lgkmcnt(7)\nv_lshl_or_b32 v16,v17,16,v16\nv_lshrrev_b32 v99,1,v237\n" \
"ds_read_u16 v25,v247 offset:5216\ns_waitcnt vmcnt(7)\nv_lshrrev_b32 v100,1,v238\nv_lshrrev_b32 v101,1,v239\n" \
"v_lshrrev_b32 v102,1,v240\nv_lshrrev_b32 v103,1,v241\nds_read_u16 v26,v247 offset:578\nv_add_u32 v96,s72,v96\n" \
"v_add_u32 v97,s72,v97\ns_waitcnt lgkmcnt(7)\nv_lshl_or_b32 v17,v19,16,v18\nv_add_u32 v98,s72,v98\n" \
"ds_read_u16 v27,v247 offset:5794\nv_add_u32 v99,s72,v99\nv_add_u32 v100,s72,v100\nv_add_u32 v101,s72,v101\n" \
"v_add_u32 v102,s72,v102\nds_read_u16 v28,v247 offset:1156\nv_add_u32 v103,s72,v103\nv_pk_maximum3_f16 v124,v16,v17,v16\n" \
"s_waitcnt lgkmcnt(7)\nv_lshl_or_b32 v18,v21,16,v20\nv_pk_minimum3_f16 v125,v16,v17,v16\nds_read_u16 v29,v247 offset:6372\n" \
"ds_read_u16 v30,v247 offset:1734\ns_waitcnt lgkmcnt(7)\nv_lshl_or_b32 v19,v23,16,v22\nbuffer_store_dwordx4 v[16:19],v2,s[36:39],s64 offen sc1\n" \
"ds_read_u16 v31,v247 offset:6950\nds_read_u16 v104,v248\nv_pk_maximum3_f16 v124,v18,v19,v124\nv_pk_minimum3_f16 v125,v18,v19,v125\n" \
"s_waitcnt lgkmcnt(7)\nv_lshl_or_b32 v24,v25,16,v24\nds_read_u16 v105,v248 offset:5216\nds_read_u16 v106,v248 offset:578\n" \
"s_waitcnt lgkmcnt(7)\nv_lshl_or_b32 v25,v27,16,v26\nv_pk_maximum3_f16 v124,v24,v25,v124\nds_read_u16 v107,v248 offset:5794\n" \
"v_pk_minimum3_f16 v125,v24,v25,v125\nds_read_u16 v108,v248 offset:1156\ns_waitcnt lgkmcnt(7)\nv_lshl_or_b32 v26,v29,16,v28\n" \
"ds_read_u16 v109,v248 offset:6372\nds_read_u16 v110,v248 offset:1734\ns_waitcnt lgkmcnt(7)\nv_lshl_or_b32 v27,v31,16,v30\n" \
"buffer_store_dwordx4 v[24:27],v2,s[36:39],s65 offen sc1\nds_read_u16 v111,v248 offset:6950\nds_read_b128 v[116:119],v113 offset:20224\nv_pk_maximum3_f16 v124,v26,v27,v124\n" \
"v_pk_minimum3_f16 v125,v26,v27,v125\nds_read_b128 v[120:123],v113 offset:24320\ns_waitcnt lgkmcnt(8)\nv_lshl_or_b32 v104,v105,16,v104\n" \
"ds_read_b32 v16,v96\nds_read_b32 v24,v96 offset:1160\nds_read_b32 v17,v97\nds_read_b32 v25,v97 offset:1160\n" \
"s_waitcnt lgkmcnt(10)\nv_lshl_or_b32 v105,v107,16,v106\nds_read_b32 v18,v98\nv_pk_maximum3_f16 v124,v104,v105,v124\n" \
"ds_read_b32 v26,v98 offset:1160\nv_pk_minimum3_f16 v125,v104,v105,v125\nds_read_b32 v19,v99\nds_read_b32 v27,v99 offset:1160\n" \
"ds_read_b32 v20,v100\ns_waitcnt lgkmcnt(13)\nv_lshl_or_b32 v106,v109,16,v108\nds_read_b32 v28,v100 offset:1160\n" \
"ds_read_b32 v21,v101\nds_read_b32 v29,v101 offset:1160\ns_waitcnt lgkmcnt(14)\nv_lshl_or_b32 v107,v111,16,v110\n" \
"buffer_store_dwordx4 v[104:107],v114,s[36:39],s66 offen sc1\ns_waitcnt lgkmcnt(12)\nbuffer_store_dwordx4 v[116:119],v1,s[36:39],s67 offen sc1\nbuffer_store_dwordx4 v[120:123],v1,s[36:39],s67 offen offset:1024 sc1\n" \
"ds_read_b32 v22,v102\nds_read_b32 v30,v102 offset:1160\nds_read_b32 v23,v103\nds_read_b32 v31,v103 offset:1160\n" \
"v_pk_maximum3_f16 v124,v106,v107,v124\nbuffer_load_dword v233,s[8:11],s95 offen lds\nv_and_b32 v124,0x7fff7fff,v124\nv_pk_minimum3_f16 v125,v106,v107,v125\n" \
"v_and_b32 v125,0x7fff7fff,v125\nv_pk_max_u16 v124,v124,v125\nv_lshlrev_b32 v125,16,v124\nv_max_u32 v124,v124,v125\n" \
"s_mov_b32 m0,162176\ns_nop 0\nv_max_u32_dpp v124,v124,v124 quad_perm:[1,0,3,2] row_mask:0xf bank_mask:0xf bound_ctrl:1\ns_nop 1\n" \
"v_max_u32_dpp v124,v124,v124 quad_perm:[2,3,0,1] row_mask:0xf bank_mask:0xf bound_ctrl:1\ns_nop 1\nv_max_u32_dpp v124,v124,v124 row_half_mirror row_mask:0xf bank_mask:0xf bound_ctrl:1\ns_nop 1\n" \
"v_max_u32_dpp v124,v124,v124 row_mirror row_mask:0xf bank_mask:0xf bound_ctrl:1\ns_nop 0\nv_readlane_b32 s68,v124,0\nv_readlane_b32 s31,v124,16\n" \
"v_readlane_b32 s96,v124,32\nv_readlane_b32 s97,v124,48\ns_max_u32 s31,s31,s96\ns_max_u32 s68,s68,s97\n" \
"s_max_u32 s68,s68,s31\ns_mul_i32 s31,s32,576\ns_add_u32 s31,s31,2304\ns_andn2_b32 s31,s31,127\n" \
"s_cmp_gt_u32 s3,1\ns_cselect_b32 s31,s31,2147483648\nbuffer_load_dword v79,s[24:27],s31 offen lds\nv_mov_b32_e64 v124,s68\n" \
"ds_write_b32 v251,v124 offset:32\nv_mov_b32 v114,161664\nbuffer_load_dwordx4 v[48:51],v2,s[36:39],s73 offen sc1\nbuffer_load_dwordx4 v[60:63],v2,s[36:39],s75 offen sc1\n" \
"buffer_load_dwordx4 v[52:55],v2,s[36:39],s74 offen sc1\nbuffer_load_dwordx4 v[64:67],v2,s[36:39],s76 offen sc1\nbuffer_load_dwordx4 v[56:59],v1,s[36:39],s77 offen sc1\ns_lshl3_add_u32 s80,s2,4294615040\n" \
"buffer_load_dwordx4 v[68:71],v1,s[36:39],s78 offen sc1\nv_and_b32 v113,15,v0\ns_mov_b32 s79,0xffffff\nv_bfrev_b32 v115,1\n" \
"s_add_u32 s70,s35,s88\ns_add_u32 s72,s70,8192\ns_cmp_lt_u32 s3,2\ns_cselect_b32 s72,s72,2147483648\n" \
"v_add_u32 v112,28416,v1\ns_add_u32 s71,s70,4096\ns_waitcnt vmcnt(8) lgkmcnt(0)\ns_barrier\n" \
"ds_read_b128 v[116:119],v114 offset:32\nds_read_b128 v[192:195],v112\nds_read_b128 v[196:199],v112 offset:1024\nds_read_b128 v[200:203],v112 offset:2048\n" \
"ds_read_b128 v[204:207],v112 offset:3072\nds_read_b128 v[208:211],v112 offset:4096\nds_read_b128 v[212:215],v112 offset:5120\nds_read_b128 v[32:35],v112 offset:6144\n" \
"ds_read_b128 v[36:39],v112 offset:7168\ns_waitcnt lgkmcnt(8)\nv_max3_u32 v116,v116,v117,v118\nv_max_u32 v116,v116,v119\n" \
"v_and_b32 v119,0xf8,v0\nv_cmp_ne_u32 vcc,192,v119\nv_lshlrev_b32 v118,11,v0\nv_add_u32 v118,s80,v118\n" \
"v_bfrev_b32 v119,1\nv_cndmask_b32 v118,v118,v119,vcc\nv_lshlrev_b32 v116,1,v116\nv_mov_b32 v117,s29\n" \
"v_cmp_eq_u32 vcc,0,v113\nv_bfi_b32 v117,s79,v117,v116\nv_mov_b32 v116,s28\nv_cndmask_b32 v115,v115,v3,vcc\n" \
"v_cmp_gt_u32 vcc,64,v0\nbuffer_store_dwordx2 v[116:117],v118,s[36:39],0 offen sc1\ns_nop 0\nv_cndmask_b32 v115,v115,v3,vcc\n" \
"s_waitcnt vmcnt(6)\nv_cmp_ne_u64 vcc,s[28:29],v[48:49]\ns_waitcnt vmcnt(4)\nv_cmp_ne_u64 s[84:85],s[28:29],v[52:53]\n" \
"s_waitcnt vmcnt(2)\nv_cmp_ne_u64 s[82:83],s[28:29],v[56:57]\ns_or_b64 vcc,vcc,s[84:85]\nv_cmp_ne_u64 s[84:85],s[28:29],v[60:61]\n" \
"s_or_b64 vcc,vcc,s[82:83]\nv_cmp_ne_u64 s[82:83],s[28:29],v[64:65]\ns_or_b64 vcc,vcc,s[84:85]\ns_waitcnt vmcnt(1)\n" \
"v_cmp_ne_u64 s[84:85],s[28:29],v[68:69]\ns_or_b64 vcc,vcc,s[82:83]\ns_or_b64 vcc,vcc,s[84:85]\ns_cbranch_vccnz .LsmN_%=\n" \
".LbmN_%=:\nv_pk_add_f32 v[50:51],v[50:51],v[62:63]\nv_pk_add_f32 v[54:55],v[54:55],v[66:67]\nv_pk_mul_f32 v[62:63],v[50:51],2.0 op_sel_hi:[1,0]\n" \
"v_pk_add_f32 v[58:59],v[58:59],v[70:71]\nv_max3_f32 v112,|v62|,|v63|,0\nv_pk_fma_f32 v[66:67],v[54:55],2.0,v[74:75] op_sel_hi:[1,0,0]\nv_max3_f32 v112,|v66|,|v67|,v112\n" \
"v_pk_fma_f32 v[70:71],v[58:59],2.0,2.0 op_sel_hi:[1,0,0]\nv_max3_f32 v112,|v70|,|v71|,v112\nv_cmp_lt_f32 s[82:83],s81,v112\nv_cmp_ngt_f32 vcc,0x41000000,v112\n" \
"s_cmp_lg_u64 s[82:83],0\ns_cselect_b32 s82,2,0\nv_cvt_pk_f16_f32 v62,v62,v63\ns_cmp_lg_u64 vcc,0\n" \
"s_addc_u32 s82,s82,0\nds_write_b32 v252,v62 offset:62848\nv_cvt_pk_f16_f32 v66,v66,v67\nds_write_b32 v253,v66 offset:62848\n" \
"v_cvt_pk_f16_f32 v70,v70,v71\nv_mov_b32 v113,s82\nds_write_b32 v254,v70 offset:62848\nds_write_b32 v251,v113\n" \
"s_waitcnt lgkmcnt(0)\ns_barrier\nds_read_b128 v[124:127],v114\nds_read_b64 v[96:97],v234 offset:62848\n" \
"ds_read_b64 v[98:99],v235 offset:62848\nds_read_b64 v[100:101],v236 offset:62848\nds_read_b64 v[102:103],v237 offset:62848\nds_read_b64 v[104:105],v238 offset:62848\n" \
"ds_read_b64 v[106:107],v239 offset:62848\nds_read_b64 v[108:109],v240 offset:62848\nds_read_b64 v[110:111],v241 offset:62848\ns_waitcnt lgkmcnt(7)\n" \
"v_or3_b32 v124,v124,v125,v126\nv_or_b32 v124,v124,v127\nv_cmp_ne_u32 vcc,2,v124\ns_nop 0\n" \
"s_cbranch_vccnz .LsxN_%=\n.LbxN_%=:\nv_pk_fma_f16 v80,v16,v96,v97\ns_waitcnt lgkmcnt(4)\n" \
"v_pk_fma_f16 v81,v17,v98,v99\nv_pk_fma_f16 v82,v18,v100,v101\nv_pk_fma_f16 v83,v19,v102,v103\nv_pk_fma_f16 v88,v24,v96,v97\n" \
"s_waitcnt lgkmcnt(2)\nv_pk_fma_f16 v84,v20,v104,v105\nv_pk_fma_f16 v85,v21,v106,v107\nv_pk_fma_f16 v89,v25,v98,v99\n" \
"s_waitcnt lgkmcnt(0)\nv_pk_fma_f16 v86,v22,v108,v109\nv_pk_fma_f16 v90,v26,v100,v101\nv_pk_fma_f16 v87,v23,v110,v111\n" \
"v_pk_fma_f16 v91,v27,v102,v103\nv_pk_fma_f16 v92,v28,v104,v105\nv_pk_fma_f16 v93,v29,v106,v107\nv_pk_fma_f16 v94,v30,v108,v109\n" \
"v_bfe_u32 v233,v0,3,3\nv_mul_u32_u24 v233,8192,v233\nv_lshl_add_u32 v233,v1,3,v233\ns_lshl2_add_u32 s96,s33,s3\n" \
"s_add_u32 s96,s96,138\ns_cmp_lt_u32 s33,13\ns_cselect_b32 s96,s96,256\ns_and_b32 s97,s96,31\n" \
"s_lshl_b32 s97,s97,3\ns_cmp_lt_u32 s96,256\ns_cselect_b32 s96,s96,0x40000\ns_lshr_b32 s96,s96,5\n" \
"s_add_u32 s96,s96,s97\ns_lshl_b32 s96,s96,6\ns_add_u32 s96,s96,s32\ns_mul_i32 s96,s96,1152\n" \
"s_mov_b32 m0,162176\nv_pk_fma_f16 v95,v31,v110,v111\nbuffer_load_dword v233,s[8:11],s96 offen lds\nv_mfma_f32_16x16x32_f16 v[48:51],v[80:83],v[192:195],0\n" \
"v_mfma_f32_16x16x32_f16 v[52:55],v[80:83],v[196:199],0\nv_mfma_f32_16x16x32_f16 v[56:59],v[84:87],v[32:35],0\nv_mfma_f32_16x16x32_f16 v[60:63],v[84:87],v[36:39],0\nv_mfma_f32_16x16x32_f16 v[64:67],v[88:91],v[192:195],0\n" \
"v_mfma_f32_16x16x32_f16 v[68:71],v[88:91],v[196:199],0\nv_mfma_f32_16x16x32_f16 v[72:75],v[92:95],v[32:35],0\nv_mfma_f32_16x16x32_f16 v[76:79],v[92:95],v[36:39],0\ns_nop 2\n" \
"v_pk_add_f32 v[56:57],v[48:49],v[56:57] neg_lo:[0,1] neg_hi:[0,1]\nv_sub_f32 v58,v50,v58\nv_pk_fma_f32 v[48:49],v[48:49],2.0,v[56:57] op_sel_hi:[1,0,1] neg_lo:[0,0,1] neg_hi:[0,0,1]\nv_fma_f32 v50,v50,2.0,-v58\n" \
"v_pk_add_f32 v[60:61],v[52:53],v[60:61] neg_lo:[0,1] neg_hi:[0,1]\nv_sub_f32 v62,v54,v62\nv_pk_fma_f32 v[52:53],v[52:53],2.0,v[60:61] op_sel_hi:[1,0,1] neg_lo:[0,0,1] neg_hi:[0,0,1]\nv_pk_add_f32 v[72:73],v[64:65],v[72:73] neg_lo:[0,1] neg_hi:[0,1]\n" \
"v_pk_add_f32 v[76:77],v[68:69],v[76:77] neg_lo:[0,1] neg_hi:[0,1]\nv_sub_f32 v74,v66,v74\nv_sub_f32_e64 v78,v70,v78\nv_fma_f32 v54,v54,2.0,-v62\n" \
"v_pk_fma_f32 v[64:65],v[64:65],2.0,v[72:73] op_sel_hi:[1,0,1] neg_lo:[0,0,1] neg_hi:[0,0,1]\nv_fma_f32 v66,v66,2.0,-v74\nv_pk_fma_f32 v[68:69],v[68:69],2.0,v[76:77] op_sel_hi:[1,0,1] neg_lo:[0,0,1] neg_hi:[0,0,1]\nv_fma_f32 v70,v70,2.0,-v78\n" \
"v_cvt_pk_f16_f32 v16,v48,v52\nv_cvt_pk_f16_f32 v17,v49,v53\nv_cvt_pk_f16_f32 v18,v50,v54\nv_cvt_pk_f16_f32 v24,v64,v68\n" \
"v_cvt_pk_f16_f32 v25,v65,v69\nv_cvt_pk_f16_f32 v26,v66,v70\nv_mfma_f32_16x16x32_f16 v[48:51],v[200:203],v[16:19],0\nv_cvt_pk_f16_f32 v20,v56,v60\n" \
"v_cvt_pk_f16_f32 v21,v57,v61\nv_mfma_f32_16x16x32_f16 v[52:55],v[204:207],v[16:19],0\nv_cvt_pk_f16_f32 v22,v58,v62\nv_cvt_pk_f16_f32 v28,v72,v76\n" \
"v_mfma_f32_16x16x32_f16 v[64:67],v[200:203],v[24:27],0\nv_cvt_pk_f16_f32 v29,v73,v77\nv_cvt_pk_f16_f32 v30,v74,v78\nv_mfma_f32_16x16x32_f16 v[68:71],v[204:207],v[24:27],0\n" \
"v_mfma_f32_16x16x32_f16 v[56:59],v[200:203],v[20:23],0\ns_nop 0\nv_cvt_pk_f16_f32 v80,v48,v52\nv_mfma_f32_16x16x32_f16 v[60:63],v[204:207],v[20:23],0\n" \
"v_cvt_pk_f16_f32 v82,v49,v53\nv_cvt_pk_f16_f32 v84,v50,v54\nv_mfma_f32_16x16x32_f16 v[72:75],v[200:203],v[28:31],0\nv_mfma_f32_16x16x32_f16 v[76:79],v[204:207],v[28:31],0\n" \
"v_cvt_pk_f16_f32 v81,v64,v68\nds_write_b64 v255,v[80:81]\nv_cvt_pk_f16_f32 v83,v65,v69\nds_write_b64 v255,v[82:83] offset:512\n" \
"v_cvt_pk_f16_f32 v85,v66,v70\nds_write_b64 v255,v[84:85] offset:1024\nv_cvt_pk_f16_f32 v86,v56,v60\nv_cvt_pk_f16_f32 v88,v57,v61\n" \
"v_cvt_pk_f16_f32 v87,v72,v76\nds_write_b64 v255,v[86:87] offset:256\nv_cvt_pk_f16_f32 v89,v73,v77\nds_write_b64 v255,v[88:89] offset:768\n" \
"v_cvt_pk_f16_f32 v90,v58,v62\nv_cvt_pk_f16_f32 v91,v74,v78\nds_write_b64 v255,v[90:91] offset:1280\ns_waitcnt lgkmcnt(0)\n" \
"s_barrier\nds_read_b128 v[16:19],v243\nds_read_b128 v[20:23],v244\nds_read_b128 v[24:27],v245\n" \
"s_waitcnt lgkmcnt(2)\nv_mfma_f32_16x16x32_f16 v[48:51],v[40:43],v[16:19],0\nv_mfma_f32_16x16x32_f16 v[52:55],v[44:47],v[16:19],0\ns_waitcnt lgkmcnt(0)\n" \
"v_mfma_f32_16x16x32_f16 v[56:59],v[40:43],v[20:23],0\nv_mfma_f32_16x16x32_f16 v[60:63],v[44:47],v[20:23],0\nv_mfma_f32_16x16x32_f16 v[64:67],v[40:43],v[24:27],0\nv_mfma_f32_16x16x32_f16 v[68:71],v[44:47],v[24:27],0\n" \
"s_nop 2\nv_cvt_pk_f16_f32 v80,v48,v52\nv_and_b32_e64 v80,-2,v80\nv_cvt_pk_f16_f32 v81,v49,v53\n" \
"v_cvt_pk_f16_f32 v82,v50,v54\nv_cvt_pk_f16_f32 v83,v51,v55\nbuffer_store_dwordx4 v[80:83],v3,s[36:39],s70 offen sc1\nv_cvt_pk_f16_f32 v84,v56,v60\n" \
"v_cvt_pk_f16_f32 v85,v57,v61\nv_cvt_pk_f16_f32 v86,v58,v62\nv_cvt_pk_f16_f32 v87,v59,v63\nv_and_b32_e64 v84,-2,v84\n" \
"buffer_store_dwordx4 v[84:87],v3,s[36:39],s71 offen sc1\nv_cvt_pk_f16_f32 v88,v64,v68\nv_cvt_pk_f16_f32 v89,v65,v69\nv_cvt_pk_f16_f32 v90,v66,v70\n" \
"v_cvt_pk_f16_f32 v91,v67,v71\nv_and_b32 v88,-2,v88\nbuffer_store_dwordx4 v[88:91],v115,s[36:39],s72 offen sc1\ns_load_dwordx4 s[20:23],s[0:1],56\n" \
"s_mov_b32 s14,s87\ns_mov_b32 s12,s88\ns_mov_b32 s17,s71\ns_cmp_lt_u32 s3,2\n" \
"s_cselect_b32 s18,s72,s70\ns_mov_b32 s26,4096\ns_mov_b32 s27,0x20000\nv_and_b32 v112,3,v0\n" \
"v_and_b32 v113,12,v0\nv_lshlrev_b32 v112,2,v112\ns_mov_b32 s13,s89\nv_max_u32 v112,v112,v113\n" \
"v_mov_b32 v114,1048576\nv_cmp_gt_u32 vcc,12,v112\nv_and_b32 v63,0xffff0000,v224\nv_and_b32 v65,0xffff0000,v225\n" \
"s_lshl2_add_u32 s95,s33,s3\ns_add_u32 s95,s95,190\ns_cmp_lt_u32 s33,13\ns_cselect_b32 s95,s95,256\n" \
"s_and_b32 s100,s95,31\ns_lshl_b32 s100,s100,3\ns_cmp_lt_u32 s95,256\ns_cselect_b32 s95,s95,0x40000\n" \
"s_lshr_b32 s95,s95,5\ns_add_u32 s95,s95,s100\ns_lshl_b32 s95,s95,6\ns_add_u32 s95,s95,s32\n" \
"s_mul_i32 s95,s95,1152\ns_lshl2_add_u32 s101,s33,s3\ns_add_u32 s101,s101,206\ns_cmp_lt_u32 s33,13\n" \
"s_cselect_b32 s101,s101,256\ns_add_u32 s96,s92,0\ns_add_u32 s97,s92,9437184\ns_add_u32 s100,s92,9510912\n" \
"v_and_b32 v67,0xffff0000,v226\nv_and_b32 v69,0xffff0000,v227\nv_and_b32 v71,0xffff0000,v228\nv_and_b32 v73,0xffff0000,v229\n" \
"v_and_b32 v75,0xffff0000,v230\nv_and_b32 v77,0xffff0000,v231\nv_and_b32 v79,0xffff0000,v232\nv_lshlrev_b32 v126,3,v1\n" \
"v_lshlrev_b32 v62,16,v224\nv_lshlrev_b32 v64,16,v225\nv_lshlrev_b32 v66,16,v226\nv_lshlrev_b32 v68,16,v227\n" \
"v_lshlrev_b32 v70,16,v228\nv_lshlrev_b32 v72,16,v229\nv_lshlrev_b32 v74,16,v230\nv_lshlrev_b32 v76,16,v231\n" \
"v_lshlrev_b32 v78,16,v232\ns_mov_b32 s16,s70\nv_min_u32 v126,0x480,v126\nv_pk_add_f32 v[62:63],v[62:63],v[250:251] op_sel_hi:[1,0] neg_lo:[0,1] neg_hi:[0,1]\n" \
"v_pk_add_f32 v[64:65],v[64:65],v[250:251] op_sel_hi:[1,0] neg_lo:[0,1] neg_hi:[0,1]\nv_pk_add_f32 v[66:67],v[66:67],v[250:251] op_sel_hi:[1,0] neg_lo:[0,1] neg_hi:[0,1]\nv_pk_add_f32 v[68:69],v[68:69],v[250:251] op_sel_hi:[1,0] neg_lo:[0,1] neg_hi:[0,1]\nv_pk_add_f32 v[70:71],v[70:71],v[250:251] op_sel_hi:[1,0] neg_lo:[0,1] neg_hi:[0,1]\n" \
"v_pk_add_f32 v[72:73],v[72:73],v[250:251] op_sel_hi:[1,0] neg_lo:[0,1] neg_hi:[0,1]\nv_pk_add_f32 v[74:75],v[74:75],v[250:251] op_sel_hi:[1,0] neg_lo:[0,1] neg_hi:[0,1]\nv_pk_add_f32 v[76:77],v[76:77],v[250:251] op_sel_hi:[1,0] neg_lo:[0,1] neg_hi:[0,1]\nv_pk_add_f32 v[78:79],v[78:79],v[250:251] op_sel_hi:[1,0] neg_lo:[0,1] neg_hi:[0,1]\n" \
"s_mul_i32 s31,s32,1152\nv_lshlrev_b32 v127,3,v1\ns_waitcnt lgkmcnt(0)\ns_mov_b64 s[24:25],s[22:23]\n" \
"s_mov_b32 s22,9216\ns_mov_b32 s23,0x20000\nv_mov_b64 v[16:17],v[192:193]\nv_mov_b64 v[18:19],v[194:195]\n" \
"v_mov_b64 v[20:21],v[196:197]\nv_mov_b64 v[22:23],v[198:199]\nv_mov_b64 v[24:25],v[208:209]\nv_mov_b64 v[26:27],v[210:211]\n" \
"v_mov_b64 v[28:29],v[212:213]\nv_mov_b64 v[30:31],v[214:215]\nv_mov_b64 v[32:33],v[216:217]\nv_mov_b64 v[34:35],v[218:219]\n" \
"v_mov_b64 v[36:37],v[220:221]\nv_mov_b64 v[38:39],v[222:223]\nv_mov_b64 v[40:41],v[200:201]\nv_mov_b64 v[42:43],v[202:203]\n" \
"v_mov_b64 v[44:45],v[204:205]\nv_mov_b64 v[46:47],v[206:207]\nv_mov_b64 v[56:57],v[242:243]\nv_mov_b64 v[58:59],v[244:245]\n" \
"v_mov_b32 v60,v249\nv_mov_b32 v61,v250\ns_mov_b32 s55,0xbdd2d3e7\ns_mul_i32 s56,s3,9568\n" \
"s_lshl_b32 s58,s3,13\ns_and_b32 s84,s33,1\ns_add_u32 s87,s12,65536\ns_mov_b32 s90,7405568\n" \
"s_sub_u32 s98,s57,s12\ns_mov_b32 s99,s12\ns_mov_b32 m0,162176\ns_nop 0\n" \
"buffer_load_dword v126,s[20:23],s31 offen lds\ns_and_b32 s22,s33,7\ns_lshr_b32 s20,s33,3\ns_lshr_b32 s21,s32,1\n" \
"s_mul_i32 s52,s20,4505600\ns_add_u32 s52,s52,12123648\ns_mul_i32 s48,s20,589824\ns_lshl4_add_u32 s48,s12,s48\n" \
"s_lshl_b32 s86,s21,11\ns_add_u32 s48,s48,9764864\ns_lshl_b32 s67,s21,7\ns_lshl_b32 s85,s21,17\n" \
"s_sub_u32 s86,s14,s86\ns_add_u32 s49,s48,4096\ns_add_u32 s50,s48,8192\ns_add_u32 s51,s48,12288\n" \
"s_add_u32 s53,s52,3276800\ns_and_b32 s31,s101,31\ns_lshl_b32 s31,s31,3\ns_cmp_lt_u32 s101,256\n" \
"s_cselect_b32 s101,s101,0x40000\ns_lshr_b32 s101,s101,5\ns_add_u32 s101,s101,s31\ns_lshl_b32 s101,s101,6\n" \
"s_add_u32 s101,s101,s32\ns_mul_i32 s101,s101,1152\ns_add_u32 s31,s92,9584640\ns_add_u32 s83,s52,2048000\n" \
"s_add_u32 s67,s67,163200\ns_add_u32 s85,s85,65536\ns_add_u32 s86,s86,0x10000\ns_mov_b32 m0,162176\n" \
"s_nop 0\nbuffer_load_dword v127,s[24:27],0 offen lds\ns_and_b32 s24,s32,1\ns_lshl3_add_u32 s22,s24,s22\n" \
"s_bfe_u32 s24,s33,0x10002\ns_lshl1_add_u32 s24,s32,s24\ns_mul_i32 s19,s24,18\ns_and_b32 s24,s33,3\n" \
"s_lshl2_add_u32 s23,s24,s19\ns_lshl_b32 s24,s3,5\ns_add_u32 s54,s14,s24\ns_lshl2_add_u32 s24,s20,s21\n" \
"s_add_u32 s23,s23,s3\ns_lshl2_add_u32 s24,s24,s3\ns_add_u32 s23,s23,1\ns_lshl_b32 s24,s24,9\n" \
"s_lshl_b32 s23,s23,9\ns_add_u32 s24,s24,262144\nv_bfe_u32 v127,v0,3,3\nv_mul_u32_u24 v127,8192,v127\n" \
"v_lshl_add_u32 v127,v1,3,v127\ns_mov_b32 m0,162176\ns_sub_u32 s59,s24,s23\nbuffer_load_dword v127,s[8:11],s95 offen lds\n" \
"s_lshr_b32 s25,s3,1\ns_mul_i32 s24,s21,1152\ns_mul_i32 s25,s25,576\ns_add_u32 s24,s24,s25\n" \
"s_lshl4_add_u32 s24,s24,s22\ns_and_b32 s25,s3,1\ns_lshl_b32 s24,s24,8\ns_lshl_b32 s65,s25,17\n" \
"s_add_u32 s65,s65,s24\ns_lshl_b32 s25,s25,20\ns_add_u32 s24,s24,s25\ns_add_u32 s60,s24,655488\n" \
"s_add_u32 s61,s24,786432\ns_add_u32 s62,s24,786560\ns_add_u32 s63,s24,917504\ns_add_u32 s64,s24,917632\n" \
"s_mov_b32 m0,162176\ns_add_u32 s95,s92,73728\nbuffer_load_dword v127,s[8:11],s101 offen lds\ns_lshl2_add_u32 s24,s21,s3\n" \
"s_mul_i32 s24,s24,9216\ns_add_u32 s24,s24,s52\ns_add_u32 s68,s24,819712\ns_lshl_b32 s24,s22,14\n" \
"s_add_u32 s24,s24,s52\ns_lshl2_add_u32 s25,s21,s3\ns_add_u32 s24,s24,967168\ns_lshl_b32 s25,s25,8\n" \
"s_add_u32 s79,s25,s24\ns_add_u32 s25,s21,4\ns_and_b32 s25,s25,3\ns_add_u32 s24,s24,s12\n" \
"s_lshl_b32 s25,s25,12\ns_add_u32 s25,s25,s24\ns_sub_u32 s74,s25,819200\ns_add_u32 m0,s57,0\n" \
"s_add_u32 s101,s92,147456\nbuffer_load_dwordx4 v4,s[8:11],s96 offen lds\ns_add_u32 s25,s21,3\ns_and_b32 s25,s25,3\n" \
"s_lshl_b32 s25,s25,12\ns_add_u32 s25,s25,s24\ns_sub_u32 s71,s25,2048000\ns_sub_u32 s76,s25,1638400\n" \
"s_add_u32 s25,s21,2\ns_and_b32 s25,s25,3\ns_lshl_b32 s25,s25,12\nbuffer_load_dwordx4 v5,s[8:11],s96 offen offset:1024 lds\n" \
"s_add_u32 s25,s25,s24\ns_sub_u32 s72,s25,1638400\ns_sub_u32 s77,s25,1638400\ns_add_u32 s25,s21,1\n" \
"s_and_b32 s25,s25,3\ns_lshl_b32 s25,s25,12\ns_add_u32 s25,s25,s24\ns_sub_u32 s73,s25,1638400\n" \
"s_sub_u32 s78,s25,1228800\nbuffer_load_dwordx4 v6,s[8:11],s96 offen offset:2048 lds\ns_lshl2_add_u32 s26,s33,s3\ns_and_b32 s24,s26,15\n" \
"s_and_b32 s25,s26,8\ns_lshl2_add_u32 s24,s24,192\ns_mul_i32 s25,s25,28\ns_add_u32 s25,s25,s24\n" \
"s_sub_u32 s25,s25,32\ns_bitcmp1_b32 s26,4\ns_cselect_b32 s24,s25,s24\nbuffer_load_dwordx4 v7,s[8:11],s96 offen offset:3072 lds\n" \
"s_and_b32 s25,s26,31\ns_lshl2_add_u32 s25,s25,448\ns_bitcmp1_b32 s26,5\ns_cselect_b32 s24,s25,s24\n" \
"s_lshr_b32 s25,s26,6\ns_lshl1_add_u32 s25,s21,s25\ns_mul_i32 s25,s25,576\ns_add_u32 s24,s24,s25\n" \
"s_and_b32 s25,s32,1\ns_add_u32 m0,s57,4096\ns_add_u32 s96,s92,221184\nbuffer_load_dwordx4 v4,s[8:11],s97 offen lds\n" \
"s_lshl1_add_u32 s24,s24,s25\ns_lshl_b32 s24,s24,11\ns_bfe_u32 s25,s33,131072\ns_mul_i32 s27,s3,4896\n" \
"s_add_u32 s24,s24,s25\ns_add_u32 s27,s27,9216\ns_add_u32 s89,s24,1\nv_mov_b32 v113,s27\n" \
"s_lshl2_add_u32 s24,s2,s3\nbuffer_load_dwordx4 v5,s[8:11],s97 offen offset:1024 lds\nv_cndmask_b32 v112,v114,v113,vcc\ns_add_u32 s65,s65,2097152\n" \
"s_lshl_b32 s24,s24,8\nv_add_u32 v48,v234,v112\nv_add_u32 v49,v235,v112\nv_add_u32 v50,v236,v112\n" \
"v_add_u32 v51,v237,v112\nv_add_u32 v52,v238,v112\nbuffer_load_dwordx4 v6,s[8:11],s97 offen offset:2048 lds\nv_add_u32 v53,v239,v112\n" \
"v_add_u32 v54,v240,v112\nv_add_u32_e64 v55,v241,v112\ns_add_u32 s75,s59,196608\ns_add_u32 s66,s65,128\n" \
"s_add_u32 s69,s68,3072\ns_add_u32 s70,s68,6144\ns_add_u32 s80,s79,4096\ns_add_u32 s81,s79,8192\n" \
"buffer_load_dwordx4 v7,s[8:11],s97 offen offset:3072 lds\ns_add_u32 s82,s79,12288\ns_add_u32 s88,s74,819200\ns_add_u32 s91,s24,69206016\n" \
"s_add_u32 m0,s57,8192\ns_nop 0\nbuffer_load_dwordx4 v4,s[8:11],s95 offen lds\nbuffer_load_dwordx4 v5,s[8:11],s95 offen offset:1024 lds\n" \
"buffer_load_dwordx4 v6,s[8:11],s95 offen offset:2048 lds\nbuffer_load_dwordx4 v7,s[8:11],s95 offen offset:3072 lds\ns_add_u32 m0,s57,12288\ns_nop 0\n" \
"buffer_load_dwordx4 v4,s[8:11],s100 offen lds\nbuffer_load_dwordx4 v5,s[8:11],s100 offen offset:1024 lds\nbuffer_load_dwordx4 v6,s[8:11],s100 offen offset:2048 lds\nbuffer_load_dwordx4 v7,s[8:11],s100 offen offset:3072 lds\n" \
"s_add_u32 m0,s57,16384\ns_nop 0\nbuffer_load_dwordx4 v4,s[8:11],s101 offen lds\nbuffer_load_dwordx4 v5,s[8:11],s101 offen offset:1024 lds\n" \
"buffer_load_dwordx4 v6,s[8:11],s101 offen offset:2048 lds\nbuffer_load_dwordx4 v7,s[8:11],s101 offen offset:3072 lds\ns_add_u32 m0,s57,20480\ns_nop 0\n" \
"buffer_load_dwordx4 v4,s[8:11],s31 offen lds\nbuffer_load_dwordx4 v5,s[8:11],s31 offen offset:1024 lds\nbuffer_load_dwordx4 v6,s[8:11],s31 offen offset:2048 lds\nbuffer_load_dwordx4 v7,s[8:11],s31 offen offset:3072 lds\n" \
"s_add_u32 m0,s57,24576\ns_nop 0\nbuffer_load_dwordx4 v4,s[8:11],s96 offen lds\nbuffer_load_dwordx4 v5,s[8:11],s96 offen offset:1024 lds\n" \
"buffer_load_dwordx4 v6,s[8:11],s96 offen offset:2048 lds\nbuffer_load_dwordx4 v7,s[8:11],s96 offen offset:3072 lds\nbuffer_load_dwordx4 v[104:107],v1,s[36:39],s7 offen sc1\nbuffer_load_dwordx4 v[108:111],v1,s[36:39],s7 offen offset:1024 sc1\n" \
"s_waitcnt vmcnt(1)\nv_xor_b32 v112,s29,v105\nv_xor_b32 v113,s29,v107\ns_waitcnt vmcnt(0)\n" \
"v_xor_b32 v114,s29,v109\nv_xor_b32 v115,s29,v111\nv_or3_b32 v112,v112,v113,v114\nv_xor_b32 v113,s28,v104\n" \
"v_xor_b32 v114,s28,v106\nv_or_b32 v112,v112,v115\nv_xor_b32 v115,s28,v108\nv_lshl_or_b32 v112,v112,8,v113\n" \
"v_xor_b32 v113,s28,v110\nv_or3_b32 v112,v112,v114,v115\nv_or_b32 v112,v112,v113\nv_cmp_ne_u32 vcc,0,v112\n" \
"s_nop 0\ns_cbranch_vccnz .LsrN_%=\n.LbrN_%=:\nv_min_u32 v120,2288,v2\n" \
"s_mov_b32 s5,26624\nv_max3_u32 v112,v105,v107,v109\nv_max_u32_e64 v112,v112,v111\ns_mov_b32 s6,31232\n" \
"ds_read_b64_tr_b16 a[0:1],v8\nv_max_u32_dpp v112,v112,v112 quad_perm:[1,0,3,2] row_mask:0xf bank_mask:0xf bound_ctrl:1\nds_read_b64_tr_b16 a[4:5],v9\nds_read_b64_tr_b16 a[8:9],v10\n" \
"v_max_u32_dpp v112,v112,v112 quad_perm:[2,3,0,1] row_mask:0xf bank_mask:0xf bound_ctrl:1\nds_read_b64_tr_b16 a[12:13],v11\nds_read_b64_tr_b16 a[16:17],v8 offset:2048\nv_max_u32_dpp v112,v112,v112 row_half_mirror row_mask:0xf bank_mask:0xf bound_ctrl:1\n" \
"ds_read_b64_tr_b16 a[20:21],v9 offset:2048\nds_read_b64_tr_b16 a[24:25],v10 offset:2048\nv_max_u32_dpp v112,v112,v112 row_mirror row_mask:0xf bank_mask:0xf bound_ctrl:1\nds_read_b64_tr_b16 a[28:29],v11 offset:2048\n" \
"v_readlane_b32 s94,v112,0\nv_readlane_b32 s97,v112,16\nv_readlane_b32 s95,v112,32\nv_readlane_b32 s100,v112,48\n" \
"s_max_u32 s97,s97,s95\ns_max_u32 s94,s94,s100\ns_max_u32 s94,s94,s97\ns_lshr_b32 s94,s94,24\n" \
"s_max_u32 s94,s94,64\ns_min_u32 s94,s94,250\ns_add_u32 s101,s92,9658368\ns_add_u32 m0,s57,0\n" \
"ds_read_b64_tr_b16 a[2:3],v8 offset:4096\ns_waitcnt lgkmcnt(1)\nbuffer_load_dwordx4 v4,s[8:11],s101 offen lds\nbuffer_load_dwordx4 v5,s[8:11],s101 offen offset:1024 lds\n" \
"buffer_load_dwordx4 v6,s[8:11],s101 offen offset:2048 lds\nbuffer_load_dwordx4 v7,s[8:11],s101 offen offset:3072 lds\nds_read_b64_tr_b16 a[6:7],v9 offset:4096\nds_read_b64_tr_b16 a[10:11],v10 offset:4096\n" \
"ds_read_b64_tr_b16 a[14:15],v11 offset:4096\nds_read_b64_tr_b16 a[18:19],v8 offset:6144\nds_read_b64_tr_b16 a[22:23],v9 offset:6144\nds_read_b64_tr_b16 a[26:27],v10 offset:6144\n" \
"ds_read_b64_tr_b16 a[30:31],v11 offset:6144\ns_add_u32 s31,s92,294912\ns_add_u32 m0,s57,4096\nds_read_b64_tr_b16 a[32:33],v8 offset:8192\n" \
"s_waitcnt lgkmcnt(1)\nbuffer_load_dwordx4 v4,s[8:11],s31 offen lds\nbuffer_load_dwordx4 v5,s[8:11],s31 offen offset:1024 lds\nbuffer_load_dwordx4 v6,s[8:11],s31 offen offset:2048 lds\n" \
"buffer_load_dwordx4 v7,s[8:11],s31 offen offset:3072 lds\nds_read_b64_tr_b16 a[36:37],v9 offset:8192\nds_read_b64_tr_b16 a[40:41],v10 offset:8192\nds_read_b64_tr_b16 a[44:45],v11 offset:8192\n" \
"ds_read_b64_tr_b16 a[48:49],v8 offset:10240\nds_read_b64_tr_b16 a[52:53],v9 offset:10240\nds_read_b64_tr_b16 a[56:57],v10 offset:10240\nds_read_b64_tr_b16 a[60:61],v11 offset:10240\n" \
"s_add_u32 s96,s92,9732096\ns_add_u32 m0,s57,8192\nds_read_b64_tr_b16 a[34:35],v8 offset:12288\ns_waitcnt lgkmcnt(1)\n" \
"buffer_load_dwordx4 v4,s[8:11],s96 offen lds\nbuffer_load_dwordx4 v5,s[8:11],s96 offen offset:1024 lds\nbuffer_load_dwordx4 v6,s[8:11],s96 offen offset:2048 lds\nbuffer_load_dwordx4 v7,s[8:11],s96 offen offset:3072 lds\n" \
"ds_read_b64_tr_b16 a[38:39],v9 offset:12288\nds_read_b64_tr_b16 a[42:43],v10 offset:12288\nds_read_b64_tr_b16 a[46:47],v11 offset:12288\nds_read_b64_tr_b16 a[50:51],v8 offset:14336\n" \
"ds_read_b64_tr_b16 a[54:55],v9 offset:14336\nds_read_b64_tr_b16 a[58:59],v10 offset:14336\nds_read_b64_tr_b16 a[62:63],v11 offset:14336\ns_add_u32 s97,s92,368640\n" \
"s_add_u32 m0,s57,12288\nds_read_b64_tr_b16 a[64:65],v8 offset:16384\ns_waitcnt lgkmcnt(1)\nbuffer_load_dwordx4 v4,s[8:11],s97 offen lds\n" \
"buffer_load_dwordx4 v5,s[8:11],s97 offen offset:1024 lds\nbuffer_load_dwordx4 v6,s[8:11],s97 offen offset:2048 lds\nbuffer_load_dwordx4 v7,s[8:11],s97 offen offset:3072 lds\nbuffer_load_dwordx4 v[80:83],v120,s[36:39],s5 offen sc1\n" \
"buffer_load_dwordx4 v[84:87],v120,s[36:39],s5 offen offset:2304 sc1\nbuffer_load_dwordx4 v[88:91],v120,s[36:39],s6 offen sc1\nbuffer_load_dwordx4 v[92:95],v120,s[36:39],s6 offen offset:2304 sc1\nds_read_b64_tr_b16 a[68:69],v9 offset:16384\n" \
"ds_read_b64_tr_b16 a[72:73],v10 offset:16384\nds_read_b64_tr_b16 a[76:77],v11 offset:16384\nds_read_b64_tr_b16 a[80:81],v8 offset:18432\nds_read_b64_tr_b16 a[84:85],v9 offset:18432\n" \
"ds_read_b64_tr_b16 a[88:89],v10 offset:18432\nds_read_b64_tr_b16 a[92:93],v11 offset:18432\ns_add_u32 s95,s92,9805824\ns_add_u32 m0,s57,16384\n" \
"ds_read_b64_tr_b16 a[66:67],v8 offset:20480\ns_waitcnt lgkmcnt(1)\nbuffer_load_dwordx4 v4,s[8:11],s95 offen lds\nbuffer_load_dwordx4 v5,s[8:11],s95 offen offset:1024 lds\n" \
"buffer_load_dwordx4 v6,s[8:11],s95 offen offset:2048 lds\nbuffer_load_dwordx4 v7,s[8:11],s95 offen offset:3072 lds\nds_read_b64_tr_b16 a[70:71],v9 offset:20480\nds_read_b64_tr_b16 a[74:75],v10 offset:20480\n" \
"ds_read_b64_tr_b16 a[78:79],v11 offset:20480\nds_read_b64_tr_b16 a[82:83],v8 offset:22528\nds_read_b64_tr_b16 a[86:87],v9 offset:22528\nds_read_b64_tr_b16 a[90:91],v10 offset:22528\n" \
"ds_read_b64_tr_b16 a[94:95],v11 offset:22528\ns_add_u32 s100,s92,442368\ns_add_u32 m0,s57,20480\nds_read_b64_tr_b16 a[96:97],v8 offset:24576\n" \
"s_waitcnt lgkmcnt(1)\nbuffer_load_dwordx4 v4,s[8:11],s100 offen lds\nbuffer_load_dwordx4 v5,s[8:11],s100 offen offset:1024 lds\nbuffer_load_dwordx4 v6,s[8:11],s100 offen offset:2048 lds\n" \
"buffer_load_dwordx4 v7,s[8:11],s100 offen offset:3072 lds\nds_read_b64_tr_b16 a[100:101],v9 offset:24576\nds_read_b64_tr_b16 a[104:105],v10 offset:24576\nds_read_b64_tr_b16 a[108:109],v11 offset:24576\n" \
"ds_read_b64_tr_b16 a[112:113],v8 offset:26624\nds_read_b64_tr_b16 a[116:117],v9 offset:26624\nds_read_b64_tr_b16 a[120:121],v10 offset:26624\nds_read_b64_tr_b16 a[124:125],v11 offset:26624\n" \
"s_add_u32 s101,s92,9879552\ns_add_u32 m0,s57,24576\ns_waitcnt vmcnt(24)\nds_read_b64_tr_b16 a[98:99],v8\n" \
"s_waitcnt lgkmcnt(1)\nbuffer_load_dwordx4 v4,s[8:11],s101 offen lds\nbuffer_load_dwordx4 v5,s[8:11],s101 offen offset:1024 lds\nbuffer_load_dwordx4 v6,s[8:11],s101 offen offset:2048 lds\n" \
"buffer_load_dwordx4 v7,s[8:11],s101 offen offset:3072 lds\nds_read_b64_tr_b16 a[102:103],v9\nds_read_b64_tr_b16 a[106:107],v10\nds_read_b64_tr_b16 a[110:111],v11\n" \
"ds_read_b64_tr_b16 a[114:115],v8 offset:2048\nds_read_b64_tr_b16 a[118:119],v9 offset:2048\nds_read_b64_tr_b16 a[122:123],v10 offset:2048\nds_read_b64_tr_b16 a[126:127],v11 offset:2048\n" \
"s_add_u32 s31,s92,516096\ns_add_u32 m0,s57,0\ns_waitcnt vmcnt(24)\nds_read_b64_tr_b16 a[128:129],v8 offset:4096\n" \
"s_waitcnt lgkmcnt(1)\nbuffer_load_dwordx4 v4,s[8:11],s31 offen lds\nbuffer_load_dwordx4 v5,s[8:11],s31 offen offset:1024 lds\nbuffer_load_dwordx4 v6,s[8:11],s31 offen offset:2048 lds\n" \
"buffer_load_dwordx4 v7,s[8:11],s31 offen offset:3072 lds\nds_read_b64_tr_b16 a[132:133],v9 offset:4096\nds_read_b64_tr_b16 a[136:137],v10 offset:4096\nds_read_b64_tr_b16 a[140:141],v11 offset:4096\n" \
"ds_read_b64_tr_b16 a[144:145],v8 offset:6144\nds_read_b64_tr_b16 a[148:149],v9 offset:6144\nds_read_b64_tr_b16 a[152:153],v10 offset:6144\nds_read_b64_tr_b16 a[156:157],v11 offset:6144\n" \
"s_add_u32 s96,s92,9953280\ns_add_u32 m0,s57,4096\ns_waitcnt vmcnt(24)\nds_read_b64_tr_b16 a[130:131],v8 offset:8192\n" \
"s_waitcnt lgkmcnt(1)\nbuffer_load_dwordx4 v4,s[8:11],s96 offen lds\nbuffer_load_dwordx4 v5,s[8:11],s96 offen offset:1024 lds\nbuffer_load_dwordx4 v6,s[8:11],s96 offen offset:2048 lds\n" \
"buffer_load_dwordx4 v7,s[8:11],s96 offen offset:3072 lds\nds_read_b64_tr_b16 a[134:135],v9 offset:8192\nds_read_b64_tr_b16 a[138:139],v10 offset:8192\nds_read_b64_tr_b16 a[142:143],v11 offset:8192\n" \
"ds_read_b64_tr_b16 a[146:147],v8 offset:10240\nds_read_b64_tr_b16 a[150:151],v9 offset:10240\nds_read_b64_tr_b16 a[154:155],v10 offset:10240\nds_read_b64_tr_b16 a[158:159],v11 offset:10240\n" \
"s_bfe_u32 s26,s33,0x20001\ns_lshl2_add_u32 s15,s32,s26\ns_lshl2_add_u32 s15,s15,s3\ns_lshl_b32 s15,s15,14\n" \
"s_add_u32 s15,s15,5308416\ns_add_u32 s97,s15,0\ns_add_u32 m0,s57,8192\ns_waitcnt vmcnt(24)\n" \
"ds_read_b64_tr_b16 a[160:161],v8 offset:12288\ns_waitcnt lgkmcnt(1)\nbuffer_load_dwordx4 v1,s[36:39],s97 offen sc1 lds\nbuffer_load_dwordx4 v1,s[36:39],s97 offen offset:1024 sc1 lds\n" \
"buffer_load_dwordx4 v1,s[36:39],s97 offen offset:2048 sc1 lds\nbuffer_load_dwordx4 v1,s[36:39],s97 offen offset:3072 sc1 lds\nds_read_b64_tr_b16 a[164:165],v9 offset:12288\nds_read_b64_tr_b16 a[168:169],v10 offset:12288\n" \
"ds_read_b64_tr_b16 a[172:173],v11 offset:12288\nds_read_b64_tr_b16 a[176:177],v8 offset:14336\nds_read_b64_tr_b16 a[180:181],v9 offset:14336\nds_read_b64_tr_b16 a[184:185],v10 offset:14336\n" \
"ds_read_b64_tr_b16 a[188:189],v11 offset:14336\ns_add_u32 s95,s15,4096\ns_add_u32 m0,s57,12288\ns_waitcnt vmcnt(20)\n" \
"ds_read_b64_tr_b16 a[162:163],v8 offset:16384\ns_waitcnt lgkmcnt(1)\nbuffer_load_dwordx4 v1,s[36:39],s95 offen sc1 lds\nbuffer_load_dwordx4 v1,s[36:39],s95 offen offset:1024 sc1 lds\n" \
"buffer_load_dwordx4 v1,s[36:39],s95 offen offset:2048 sc1 lds\nbuffer_load_dwordx4 v1,s[36:39],s95 offen offset:3072 sc1 lds\nds_read_b64_tr_b16 a[166:167],v9 offset:16384\nds_read_b64_tr_b16 a[170:171],v10 offset:16384\n" \
"ds_read_b64_tr_b16 a[174:175],v11 offset:16384\nds_read_b64_tr_b16 a[178:179],v8 offset:18432\nds_read_b64_tr_b16 a[182:183],v9 offset:18432\nds_read_b64_tr_b16 a[186:187],v10 offset:18432\n" \
"ds_read_b64_tr_b16 a[190:191],v11 offset:18432\ns_add_u32 s100,s15,8192\ns_add_u32 m0,s57,16384\ns_waitcnt vmcnt(20)\n" \
"ds_read_b64_tr_b16 a[192:193],v8 offset:20480\ns_waitcnt lgkmcnt(1)\nbuffer_load_dwordx4 v1,s[36:39],s100 offen sc1 lds\nbuffer_load_dwordx4 v1,s[36:39],s100 offen offset:1024 sc1 lds\n" \
"buffer_load_dwordx4 v1,s[36:39],s100 offen offset:2048 sc1 lds\nbuffer_load_dwordx4 v1,s[36:39],s100 offen offset:3072 sc1 lds\nds_read_b64_tr_b16 a[196:197],v9 offset:20480\nds_read_b64_tr_b16 a[200:201],v10 offset:20480\n" \
"ds_read_b64_tr_b16 a[204:205],v11 offset:20480\nds_read_b64_tr_b16 a[208:209],v8 offset:22528\nds_read_b64_tr_b16 a[212:213],v9 offset:22528\nds_read_b64_tr_b16 a[216:217],v10 offset:22528\n" \
"ds_read_b64_tr_b16 a[220:221],v11 offset:22528\ns_add_u32 s101,s15,12288\ns_add_u32 m0,s57,20480\ns_waitcnt lgkmcnt(0)\n" \
"buffer_load_dwordx4 v1,s[36:39],s101 offen sc1 lds\nbuffer_load_dwordx4 v1,s[36:39],s101 offen offset:1024 sc1 lds\nbuffer_load_dwordx4 v1,s[36:39],s101 offen offset:2048 sc1 lds\nbuffer_load_dwordx4 v1,s[36:39],s101 offen offset:3072 sc1 lds\n" \
"v_cmp_ne_u64 vcc,s[28:29],v[80:81]\nv_cmp_ne_u64 s[24:25],s[28:29],v[84:85]\nv_cmp_ne_u64 s[26:27],s[28:29],v[88:89]\ns_or_b64 vcc,vcc,s[24:25]\n" \
"v_cmp_ne_u64 s[24:25],s[28:29],v[92:93]\ns_or_b64 vcc,vcc,s[26:27]\ns_or_b64 vcc,vcc,s[24:25]\ns_cbranch_vccnz .LsnN_%=\n" \
".LbnN_%=:\nv_bfe_u32 v112,v0,1,5\nv_mul_u32_u24 v112,72,v112\nv_lshrrev_b32 v113,6,v0\n" \
"v_mul_u32_u24 v114,144,v0\nv_lshl_add_u32 v112,v113,5,v112\nv_subrev_u32 v114,18368,v114\nv_mov_b32 v115,1048576\n" \
"v_cmp_gt_u32 vcc,144,v0\nv_and_b32 v113,1,v0\nv_lshl_add_u32 v112,v113,4,v112\nv_cndmask_b32 v114,v115,v114,vcc\n" \
"v_and_b32 v97,0xffff0000,v82\nv_lshlrev_b32 v96,16,v82\ns_add_u32 s24,s93,s94\nv_cmp_gt_u32 vcc,128,v0\n" \
"v_add_u32 v113,8,v112\nv_mul_f32 v96,0x3ee38e39,v96\nv_mul_f32 v97,0x3ee38e39,v97\ns_sub_u32 s24,s24,253\n" \
"v_cndmask_b32 v112,v114,v112,vcc\nv_and_b32 v99,0xffff0000,v83\nv_ldexp_f32 v96,v96,s24\nv_ldexp_f32 v97,v97,s24\n" \
"v_lshlrev_b32_e64 v98,16,v83\nds_write_b64 v112,v[96:97] offset:28800\nv_add_u32 v115,72,v114\nv_mul_f32 v98,0x3ee38e39,v98\n" \
"v_mul_f32 v99,0x3ee38e39,v99\nv_cndmask_b32_e64 v113,v115,v113,vcc\nv_ldexp_f32 v98,v98,s24\nv_ldexp_f32 v99,v99,s24\n" \
"ds_write_b64 v113,v[98:99] offset:28800\nv_and_b32 v101,0xffff0000,v86\nv_lshlrev_b32 v100,16,v86\nds_write_b64 v112,v[100:101] offset:31104\n" \
"v_and_b32 v103,0xffff0000,v87\nv_lshlrev_b32 v102,16,v87\ns_lshr_b32 s25,s33,3\nds_write_b64 v113,v[102:103] offset:31104\n" \
"s_mul_i32 s25,s25,576\nv_and_b32 v105,0xffff0000,v90\nv_lshlrev_b32 v104,16,v90\ns_sub_u32 s25,163200,s25\n" \
"ds_write_b64 v112,v[104:105] offset:33408\nv_add_u32 v114,s25,v2\nv_subrev_u32 v115,163200,v114\nv_and_b32 v107,0xffff0000,v91\n" \
"v_lshlrev_b32 v106,16,v91\nv_mov_b32 v116,1048576\nv_cmp_gt_u32 vcc,576,v115\nds_write_b64 v113,v[106:107] offset:33408\n" \
"v_and_b32 v109,0xffff0000,v94\nv_cndmask_b32 v114,v116,v114,vcc\nv_and_b32 v111,0xffff0000,v95\nv_lshlrev_b32 v108,16,v94\n" \
"v_lshlrev_b32 v110,16,v95\nds_write_b128 v114,v[108:111]\nbuffer_load_dwordx4 v[112:115],v3,s[36:39],s16 offen sc1\nbuffer_load_dwordx4 v[116:119],v3,s[36:39],s17 offen sc1\n" \
"v_and_b32 v124,48,v0\nv_cmp_gt_u32 vcc,64,v0\nv_bfe_u32 v127,v0,5,1\nv_and_b32 v13,31,v0\n" \
"v_cndmask_b32_e64 v124,v124,v3,vcc\nbuffer_load_dwordx4 v[120:123],v124,s[36:39],s18 offen sc1\nv_mul_u32_u24 v127,2448,v127\nv_mul_u32_u24 v13,72,v13\n" \
"s_mul_i32 s27,s3,4896\nv_add3_u32 v13,v13,v127,s27\nv_mov_b32 v127,0\ns_waitcnt vmcnt(27)\n" \
"ds_read_b64_tr_b16 a[194:195],v8 offset:24576\nds_read_b64_tr_b16 a[198:199],v9 offset:24576\ns_waitcnt lgkmcnt(2)\ns_barrier\n" \
"ds_read_b64_tr_b16 a[202:203],v10 offset:24576\nds_read_b64_tr_b16 a[206:207],v11 offset:24576\nds_read_b64_tr_b16 a[210:211],v8 offset:26624\nds_read_b64_tr_b16 a[214:215],v9 offset:26624\n" \
"ds_read_b64_tr_b16 a[218:219],v10 offset:26624\nds_read_b64_tr_b16 a[222:223],v11 offset:26624\ns_waitcnt vmcnt(23)\nds_read_b64_tr_b16 a[224:225],v8\n" \
"ds_read_b64_tr_b16 a[228:229],v9\nds_read_b64_tr_b16 a[232:233],v10\nds_read_b64_tr_b16 a[236:237],v11\nds_read_b64_tr_b16 a[240:241],v8 offset:2048\n" \
"ds_read_b64_tr_b16 a[244:245],v9 offset:2048\nds_read_b64_tr_b16 a[248:249],v10 offset:2048\nds_read_b64_tr_b16 a[252:253],v11 offset:2048\ns_waitcnt vmcnt(19)\n" \
"ds_read_b64_tr_b16 a[226:227],v8 offset:4096\nds_read_b64_tr_b16 a[230:231],v9 offset:4096\nds_read_b64_tr_b16 a[234:235],v10 offset:4096\nds_read_b64_tr_b16 a[238:239],v11 offset:4096\n" \
"ds_read_b64_tr_b16 a[242:243],v8 offset:6144\nds_read_b64_tr_b16 a[246:247],v9 offset:6144\nds_read_b64_tr_b16 a[250:251],v10 offset:6144\nds_read_b64_tr_b16 a[254:255],v11 offset:6144\n" \
"s_waitcnt vmcnt(15)\nds_read_b64_tr_b16 v[192:193],v15 offset:8192\nds_read_b64_tr_b16 v[196:197],v15 offset:8704\nds_read_b64_tr_b16 v[194:195],v15 offset:9216\n" \
"ds_read_b64_tr_b16 v[198:199],v15 offset:9728\nds_read_b64_tr_b16 v[200:201],v15 offset:10240\nds_read_b64_tr_b16 v[204:205],v15 offset:10752\nds_read_b64_tr_b16 v[202:203],v15 offset:11264\n" \
"ds_read_b64_tr_b16 v[206:207],v15 offset:11776\ns_waitcnt vmcnt(11)\nds_read_b64_tr_b16 v[208:209],v15 offset:12288\nds_read_b64_tr_b16 v[212:213],v15 offset:12800\n" \
"ds_read_b64_tr_b16 v[210:211],v15 offset:13312\nds_read_b64_tr_b16 v[214:215],v15 offset:13824\nds_read_b64_tr_b16 v[216:217],v15 offset:14336\nds_read_b64_tr_b16 v[220:221],v15 offset:14848\n" \
"ds_read_b64_tr_b16 v[218:219],v15 offset:15360\nds_read_b64_tr_b16 v[222:223],v15 offset:15872\ns_waitcnt vmcnt(7)\nds_read_b64_tr_b16 v[224:225],v15 offset:16384\n" \
"ds_read_b64_tr_b16 v[228:229],v15 offset:16896\nds_read_b64_tr_b16 v[226:227],v15 offset:17408\nds_read_b64_tr_b16 v[230:231],v15 offset:17920\nds_read_b64_tr_b16 v[232:233],v15 offset:18432\n" \
"ds_read_b64_tr_b16 v[236:237],v15 offset:18944\nds_read_b64_tr_b16 v[234:235],v15 offset:19456\nds_read_b64_tr_b16 v[238:239],v15 offset:19968\ns_waitcnt vmcnt(3)\n" \
"ds_read_b64_tr_b16 v[240:241],v15 offset:20480\nds_read_b64_tr_b16 v[244:245],v15 offset:20992\nds_read_b64_tr_b16 v[242:243],v15 offset:21504\nds_read_b64_tr_b16 v[246:247],v15 offset:22016\n" \
"ds_read_b64_tr_b16 v[248:249],v15 offset:22528\nds_read_b64_tr_b16 v[252:253],v15 offset:23040\nds_read_b64_tr_b16 v[250:251],v15 offset:23552\nds_read_b64_tr_b16 v[254:255],v15 offset:24064\n" \
"s_waitcnt vmcnt(1)\nv_and_b32 v125,v112,v116\ns_waitcnt vmcnt(0)\nv_bitop3_b32 v125,v125,v120,1 bitop3:0x80\n" \
"v_cmp_eq_u32 vcc,0,v125\ns_nop 0\ns_cbranch_vccnz .LsuN_%=\n.LbuN_%=:\n" \
"v_mfma_f32_16x16x32_f16 v[80:83],v[32:35],v[112:115],0\nv_and_b32 v3,31,v0\nv_mul_u32_u24 v3,72,v3\nv_mfma_f32_16x16x32_f16 v[84:87],v[36:39],v[112:115],0\n" \
"v_add_u32 v13,9216,v13\nv_mov_b32 v104,-1\nv_mfma_f32_16x16x32_f16 v[88:91],v[32:35],v[116:119],0\nv_mov_b32 v105,-1\n" \
"v_mov_b32 v106,-1\nv_mfma_f32_16x16x32_f16 v[92:95],v[36:39],v[116:119],0\nv_mov_b32 v107,-1\ns_add_u32 s27,s2,0\n" \
"v_mfma_f32_16x16x32_f16 v[96:99],v[32:35],v[120:123],0\ns_lshr_b32 s31,s27,4\ns_mul_i32 s96,s31,37\ns_lshr_b32 s96,s96,8\n" \
"s_lshl2_add_u32 s31,s96,s31\ns_mul_i32 s31,s31,409600\ns_and_b32 s96,s27,15\ns_lshl_b32 s96,s96,14\n" \
"s_add_u32 s31,s31,s96\ns_add_u32 s31,s31,13090816\ns_cmp_lt_u32 s27,158\ns_cselect_b32 s31,s31,2147483648\n" \
"buffer_store_dwordx4 v[104:107],v2,s[36:39],s31 offen sc1\nv_mfma_f32_16x16x32_f16 v[100:103],v[36:39],v[120:123],0\nv_cvt_pk_f16_f32 v112,v84,v80\nv_cvt_pk_f16_f32 v113,v85,v81\n" \
"v_cvt_pk_f16_f32 v114,v86,v82\nv_cvt_pk_f16_f32 v115,v87,v83\nds_write_b128 v57,v[112:115]\nv_cvt_pk_f16_f32 v116,v92,v88\n" \
"v_cvt_pk_f16_f32 v117,v93,v89\ns_add_u32 s31,s31,4096\nbuffer_store_dwordx4 v[104:107],v2,s[36:39],s31 offen sc1\nv_cvt_pk_f16_f32 v118,v94,v90\n" \
"v_cvt_pk_f16_f32 v119,v95,v91\nds_write_b128 v58,v[116:119]\nv_cvt_pk_f16_f32 v120,v100,v96\nv_cvt_pk_f16_f32 v121,v101,v97\n" \
"v_cvt_pk_f16_f32 v122,v102,v98\nv_cvt_pk_f16_f32 v123,v103,v99\nds_write_b128 v60,v[120:123]\ns_add_u32 s31,s31,4096\n" \
"buffer_store_dwordx4 v[104:107],v2,s[36:39],s31 offen sc1\nv_add_u32 v60,31104,v3\nv_add_u32 v3,28800,v3\nds_read2_b64 v[32:35],v3 offset0:0 offset1:1\n" \
"ds_read2_b64 v[36:39],v3 offset0:2 offset1:3\ns_add_u32 s31,s31,4096\ns_add_u32 s27,s2,111\ns_lshr_b32 s97,s27,4\n" \
"s_mul_i32 s95,s97,37\ns_lshr_b32 s95,s95,8\ns_lshl2_add_u32 s97,s95,s97\ns_mul_i32 s97,s97,409600\n" \
"s_and_b32 s95,s27,15\ns_lshl_b32 s95,s95,14\nbuffer_store_dwordx4 v[104:107],v2,s[36:39],s31 offen sc1\ns_add_u32 s97,s97,s95\n" \
"s_add_u32 s97,s97,13090816\ns_cmp_lt_u32 s27,158\ns_cselect_b32 s97,s97,2147483648\ns_waitcnt lgkmcnt(2)\n" \
"s_barrier\nds_read_b64 v[112:113],v56\nds_read_b64 v[116:117],v56 offset:512\nds_read_b64 v[120:121],v56 offset:256\n" \
"ds_read_b64 v[124:125],v56 offset:768\nds_read_b64 v[114:115],v56 offset:1024\nds_read_b64 v[122:123],v56 offset:1280\nds_read_b64 v[56:57],v3 offset:64\n" \
"buffer_store_dwordx4 v[104:107],v2,s[36:39],s97 offen sc1\ns_add_u32 s97,s97,4096\nv_bfe_u32 v110,v0,1,2\nv_bfe_u32 v111,v0,4,1\n" \
"v_xor_b32 v110,v110,v111\nv_and_b32 v111,1,v0\nv_lshl_add_u32 v110,v110,1,v111\nv_bfe_u32 v111,v0,3,3\n" \
"v_lshlrev_b32 v111,12,v111\nv_lshl_add_u32 v4,v110,4,v111\nv_xor_b32 v5,64,v4\nv_add_u32 v5,31744,v5\n" \
"v_add_u32 v6,63488,v4\nbuffer_store_dwordx4 v[104:107],v2,s[36:39],s97 offen sc1\ns_waitcnt lgkmcnt(3)\nv_swap_b32 v113,v116\n" \
"s_add_u32 s97,s97,4096\nv_swap_b32 v121,v124\nv_add_u32 v7,63488,v5\ns_waitcnt lgkmcnt(0)\n" \
"v_mfma_f32_16x16x32_f16 v[80:83],v[112:115],v[40:43],0\nv_mov_b32 v118,v115\nv_mov_b32 v126,v123\nv_mfma_f32_16x16x32_f16 v[84:87],v[112:115],v[44:47],0\n" \
"s_lshr_b32 s26,s32,1\ns_mul_i32 s92,s26,1152\nv_mfma_f32_16x16x32_f16 v[88:91],v[120:123],v[40:43],0\nbuffer_store_dwordx4 v[104:107],v2,s[36:39],s97 offen sc1\n" \
"v_mfma_f32_16x16x32_f16 v[92:95],v[120:123],v[44:47],0\ns_add_u32 s97,s97,4096\ns_lshr_b32 s26,s3,1\nv_mfma_f32_16x16x32_f16 v[96:99],v[116:119],v[40:43],0\n" \
"s_lshl_b32 s26,s26,6\ns_add_u32 s92,s92,s26\nv_mfma_f32_16x16x32_f16 v[100:103],v[116:119],v[44:47],0\nv_pk_add_f32 v[88:89],v[80:81],v[88:89] neg_lo:[0,1] neg_hi:[0,1]\n" \
"v_pk_add_f32 v[90:91],v[82:83],v[90:91] neg_lo:[0,1] neg_hi:[0,1]\nv_pk_add_f32 v[92:93],v[84:85],v[92:93] neg_lo:[0,1] neg_hi:[0,1]\nbuffer_store_dwordx4 v[104:107],v2,s[36:39],s97 offen sc1\nv_pk_add_f32 v[94:95],v[86:87],v[94:95] neg_lo:[0,1] neg_hi:[0,1]\n" \
"v_pk_fma_f32 v[80:81],v[80:81],2.0,v[88:89] op_sel_hi:[1,0,1] neg_lo:[0,0,1] neg_hi:[0,0,1]\nv_pk_fma_f32 v[82:83],v[82:83],2.0,v[90:91] op_sel_hi:[1,0,1] neg_lo:[0,0,1] neg_hi:[0,0,1]\nv_pk_fma_f32 v[84:85],v[84:85],2.0,v[92:93] op_sel_hi:[1,0,1] neg_lo:[0,0,1] neg_hi:[0,0,1]\nv_pk_fma_f32 v[86:87],v[86:87],2.0,v[94:95] op_sel_hi:[1,0,1] neg_lo:[0,0,1] neg_hi:[0,0,1]\n" \
"v_cvt_pk_f16_f32 v112,v80,v84\nv_cvt_pk_f16_f32 v113,v81,v85\nv_cvt_pk_f16_f32 v114,v82,v86\nv_cvt_pk_f16_f32 v115,v83,v87\n" \
"v_mfma_f32_16x16x32_f16 v[104:107],v[124:127],v[40:43],0\nv_cvt_pk_f16_f32 v116,v88,v92\ns_lshr_b32 s100,s33,3\ns_add_u32 s100,s100,0\n" \
"s_sub_u32 s101,s100,4\ns_min_u32 s100,s100,s101\ns_lshl_b32 s101,s100,12\ns_add_u32 s101,s101,s57\n" \
"s_add_u32 m0,s101,0\ns_and_b32 s101,s100,1\ns_lshr_b32 s100,s100,1\ns_lshl_b32 s100,s100,10\n" \
"s_add_u32 s100,s100,s101\ns_lshl_b32 s100,s100,7\ns_lshr_b32 s31,s33,3\ns_add_u32 s31,s31,1\n" \
"s_sub_u32 s96,s31,4\ns_min_u32 s31,s31,s96\ns_lshl_b32 s96,s31,12\ns_add_u32 s96,s96,s57\n" \
"s_lshr_b32 s97,s33,3\ns_add_u32 s97,s97,2\ns_sub_u32 s95,s97,4\ns_min_u32 s97,s97,s95\n" \
"s_lshl_b32 s95,s97,12\ns_add_u32 s95,s95,s57\nv_mfma_f32_16x16x32_f16 v[80:83],v[16:19],v[112:115],0\nv_cvt_pk_f16_f32 v117,v89,v93\n" \
"v_cvt_pk_f16_f32 v118,v90,v94\nv_mfma_f32_16x16x32_f16 v[84:87],v[20:23],v[112:115],0\nv_cvt_pk_f16_f32 v119,v91,v95\nv_pk_add_f32 v[104:105],v[96:97],v[104:105] neg_lo:[0,1] neg_hi:[0,1]\n" \
"v_mfma_f32_16x16x32_f16 v[108:111],v[124:127],v[44:47],0\nv_pk_add_f32 v[106:107],v[98:99],v[106:107] neg_lo:[0,1] neg_hi:[0,1]\nv_pk_fma_f32 v[96:97],v[96:97],2.0,v[104:105] op_sel_hi:[1,0,1] neg_lo:[0,0,1] neg_hi:[0,0,1]\nv_mfma_f32_16x16x32_f16 v[88:91],v[24:27],v[116:119],0\n" \
"v_pk_fma_f32 v[98:99],v[98:99],2.0,v[106:107] op_sel_hi:[1,0,1] neg_lo:[0,0,1] neg_hi:[0,0,1]\nds_read2_b64 v[40:43],v3 offset0:4 offset1:5\nv_mfma_f32_16x16x32_f16 v[92:95],v[28:31],v[116:119],0\nds_write2_b32 v48,v84,v80 offset0:0 offset1:1\n" \
"ds_write2_b32 v49,v85,v81 offset0:0 offset1:1\nds_write2_b32 v50,v86,v82 offset0:0 offset1:1\nv_pk_add_f32 v[108:109],v[100:101],v[108:109] neg_lo:[0,1] neg_hi:[0,1]\nv_pk_add_f32 v[110:111],v[102:103],v[110:111] neg_lo:[0,1] neg_hi:[0,1]\n" \
"v_pk_fma_f32 v[100:101],v[100:101],2.0,v[108:109] op_sel_hi:[1,0,1] neg_lo:[0,0,1] neg_hi:[0,0,1]\nv_pk_fma_f32 v[102:103],v[102:103],2.0,v[110:111] op_sel_hi:[1,0,1] neg_lo:[0,0,1] neg_hi:[0,0,1]\nds_write2_b32 v51,v87,v83 offset0:0 offset1:1\nv_cvt_pk_f16_f32 v120,v96,v100\n" \
"v_cvt_pk_f16_f32 v121,v97,v101\nv_cvt_pk_f16_f32 v122,v98,v102\nv_cvt_pk_f16_f32 v123,v99,v103\nds_write2_b32 v52,v92,v88 offset0:0 offset1:1\n" \
"ds_write2_b32 v53,v93,v89 offset0:0 offset1:1\nv_mfma_f32_16x16x32_f16 v[96:99],v[16:19],v[120:123],0\nds_write2_b32 v54,v94,v90 offset0:0 offset1:1\nv_mfma_f32_16x16x32_f16 v[100:103],v[20:23],v[120:123],0\n" \
"ds_write2_b32 v55,v95,v91 offset0:0 offset1:1\nv_cvt_pk_f16_f32 v124,v104,v108\nv_cvt_pk_f16_f32 v125,v105,v109\nv_cvt_pk_f16_f32 v126,v106,v110\n" \
"v_cvt_pk_f16_f32 v127,v107,v111\nv_add_u32 v48,2448,v48\nv_add_u32 v49,2448,v49\nv_mfma_f32_16x16x32_f16 v[104:107],v[24:27],v[124:127],0\n" \
"ds_write2_b32 v48,v100,v96 offset0:0 offset1:1\nv_mfma_f32_16x16x32_f16 v[108:111],v[28:31],v[124:127],0\nds_write2_b32 v49,v101,v97 offset0:0 offset1:1\nv_add_u32 v50,2448,v50\n" \
"ds_write2_b32 v50,v102,v98 offset0:0 offset1:1\nv_add_u32 v51,2448,v51\nds_write2_b32 v51,v103,v99 offset0:0 offset1:1\nv_add_u32 v52,2448,v52\n" \
"v_add_u32 v53,2448,v53\nv_add_u32 v54,2448,v54\nds_write2_b32 v52,v108,v104 offset0:0 offset1:1\nds_write2_b32 v53,v109,v105 offset0:0 offset1:1\n" \
"ds_write2_b32 v54,v110,v106 offset0:0 offset1:1\nv_add_u32 v55,2448,v55\nds_write2_b32 v55,v111,v107 offset0:0 offset1:1\nds_read2_b64 v[80:83],v13 offset0:0 offset1:1\n" \
"ds_read2_b64 v[84:87],v13 offset0:2 offset1:3\ns_lshl_b32 s26,s3,8\ns_add_u32 s92,s92,s26\ns_lshl_b32 s92,s92,4\n" \
"s_and_b32 s26,s32,1\ns_lshl3_add_u32 s92,s26,s92\ns_and_b32 s26,s33,7\ns_add_u32 s92,s92,s26\n" \
"ds_read2_b64 v[88:91],v13 offset0:4 offset1:5\ns_lshl_b32 s92,s92,8\ns_add_u32 s100,s100,s92\nbuffer_load_dwordx4 v4,s[40:43],s100 offen lds\n" \
"ds_read2_b64 v[44:47],v3 offset0:6 offset1:7\nv_and_b32 v48,31,v0\nds_read2_b64 v[92:95],v13 offset0:6 offset1:7\nv_mad_u32_u24 v49,v48,56,0\n" \
"v_sub_u32 v48,v13,v49\nv_and_b32 v49,31,v0\nv_mul_u32_u24 v49,68,v49\nv_sub_u32 v49,v13,v49\n" \
"buffer_load_dwordx4 v5,s[40:43],s100 offen offset:1024 lds\nds_read_b64 v[96:97],v13 offset:64\ns_waitcnt lgkmcnt(5)\nv_pk_fma_f32 v[62:63],v[80:81],v[32:33],v[62:63]\n" \
"v_pk_fma_f32 v[64:65],v[82:83],v[34:35],v[64:65]\nv_pk_mul_f32 v[102:103],v[62:63],v[62:63]\ns_waitcnt lgkmcnt(4)\nv_pk_fma_f32 v[66:67],v[84:85],v[36:37],v[66:67]\n" \
"v_pk_add_f32 v[100:101],v[62:63],v[64:65]\nv_pk_fma_f32 v[102:103],v[64:65],v[64:65],v[102:103]\nv_pk_fma_f32 v[68:69],v[86:87],v[38:39],v[68:69]\nbuffer_load_dwordx4 v6,s[40:43],s100 offen offset:2048 lds\n" \
"v_pk_add_f32 v[100:101],v[100:101],v[66:67]\nv_pk_fma_f32 v[102:103],v[66:67],v[66:67],v[102:103]\nds_read2_b64 v[112:115],v60 offset0:0 offset1:1\nds_read2_b64 v[116:119],v60 offset0:2 offset1:3\n" \
"s_waitcnt lgkmcnt(4)\nv_pk_fma_f32 v[70:71],v[88:89],v[40:41],v[70:71]\nv_pk_add_f32 v[100:101],v[100:101],v[68:69]\nds_read2_b64 v[120:123],v60 offset0:4 offset1:5\n" \
"v_pk_fma_f32 v[102:103],v[68:69],v[68:69],v[102:103]\nbuffer_load_dwordx4 v7,s[40:43],s100 offen offset:3072 lds\nds_read2_b64 v[124:127],v60 offset0:6 offset1:7\nv_pk_fma_f32 v[72:73],v[90:91],v[42:43],v[72:73]\n" \
"v_pk_add_f32 v[100:101],v[100:101],v[70:71]\nds_read_b64 v[98:99],v60 offset:64\nv_add_u32 v60,2304,v60\nv_pk_fma_f32 v[102:103],v[70:71],v[70:71],v[102:103]\n" \
"ds_read2_b64 v[16:19],v60 offset0:0 offset1:1\ns_waitcnt lgkmcnt(6)\nv_pk_fma_f32 v[74:75],v[92:93],v[44:45],v[74:75]\ns_add_u32 m0,s96,0\n" \
"s_and_b32 s96,s31,1\ns_lshr_b32 s31,s31,1\ns_lshl_b32 s31,s31,10\ns_add_u32 s31,s31,s96\n" \
"s_lshl_b32 s31,s31,7\ns_add_u32 s31,s31,s92\nbuffer_load_dwordx4 v4,s[40:43],s31 offen lds\nv_pk_add_f32 v[100:101],v[100:101],v[72:73]\n" \
"v_pk_fma_f32 v[102:103],v[72:73],v[72:73],v[102:103]\nv_pk_fma_f32 v[76:77],v[94:95],v[46:47],v[76:77]\nv_pk_add_f32 v[100:101],v[100:101],v[74:75]\nv_pk_fma_f32 v[102:103],v[74:75],v[74:75],v[102:103]\n" \
"ds_read2_b64 v[20:23],v60 offset0:2 offset1:3\nv_pk_fma_f32 v[78:79],v[96:97],v[56:57],v[78:79]\nv_pk_add_f32 v[100:101],v[100:101],v[76:77]\nbuffer_load_dwordx4 v5,s[40:43],s31 offen offset:1024 lds\n" \
"v_pk_fma_f32 v[102:103],v[76:77],v[76:77],v[102:103]\nv_pk_add_f32 v[100:101],v[100:101],v[78:79]\nv_pk_fma_f32 v[102:103],v[78:79],v[78:79],v[102:103]\nds_read2_b64 v[24:27],v60 offset0:4 offset1:5\n" \
"v_add_f32 v100,v100,v101\nv_add_f32 v102,v102,v103\nds_read2_b64 v[28:31],v60 offset0:6 offset1:7\nds_read_b64 v[58:59],v60 offset:64\n" \
"buffer_load_dwordx4 v6,s[40:43],s31 offen offset:2048 lds\nv_permlane16_swap_b32 v100,v102\nv_add_f32 v100,v100,v102\ns_lshr_b32 s100,s33,3\n" \
"s_add_u32 s100,s100,3\nv_add_f32_dpp v100,v100,v100 quad_perm:[1,0,3,2] row_mask:0xf bank_mask:0xf bound_ctrl:1\ns_sub_u32 s101,s100,4\ns_min_u32 s100,s100,s101\n" \
"v_add_f32_dpp v100,v100,v100 quad_perm:[2,3,0,1] row_mask:0xf bank_mask:0xf bound_ctrl:1\nbuffer_load_dwordx4 v7,s[40:43],s31 offen offset:3072 lds\ns_add_u32 m0,s95,0\nv_add_f32_dpp v100,v100,v100 row_half_mirror row_mask:0xf bank_mask:0xf bound_ctrl:1\n" \
"s_and_b32 s95,s97,1\ns_lshr_b32 s97,s97,1\nv_add_f32_dpp v100,v100,v100 row_mirror row_mask:0xf bank_mask:0xf bound_ctrl:1\nv_mul_f32 v104,0x3ae38e39,v100\n" \
"v_mul_f32 v102,0x3ae38e39,v100\ns_lshl_b32 s97,s97,10\ns_add_u32 s97,s97,s95\ns_lshl_b32 s97,s97,7\n" \
"s_add_u32 s97,s97,s92\nbuffer_load_dwordx4 v4,s[40:43],s97 offen lds\nv_permlane16_swap_b32 v104,v102\nv_fma_f32 v102,-v104,v104,v102\n" \
"v_max_f32 v102,0,v102\nv_add_f32 v102,0x3727c5ac,v102\nv_rsq_f32 v100,v102\ns_lshl_b32 s101,s100,12\n" \
"buffer_load_dwordx4 v5,s[40:43],s97 offen offset:1024 lds\nv_mul_f32 v101,-v104,v100\nv_pk_fma_f32 v[80:81],v[62:63],v[100:101],v[100:101] op_sel:[0,0,1] op_sel_hi:[1,0,1]\nv_pk_fma_f32 v[82:83],v[64:65],v[100:101],v[100:101] op_sel:[0,0,1] op_sel_hi:[1,0,1]\n" \
"v_pk_fma_f32 v[84:85],v[66:67],v[100:101],v[100:101] op_sel:[0,0,1] op_sel_hi:[1,0,1]\nv_pk_fma_f32 v[86:87],v[68:69],v[100:101],v[100:101] op_sel:[0,0,1] op_sel_hi:[1,0,1]\nv_pk_fma_f32 v[88:89],v[70:71],v[100:101],v[100:101] op_sel:[0,0,1] op_sel_hi:[1,0,1]\nv_pk_fma_f32 v[90:91],v[72:73],v[100:101],v[100:101] op_sel:[0,0,1] op_sel_hi:[1,0,1]\n" \
"v_pk_fma_f32 v[92:93],v[74:75],v[100:101],v[100:101] op_sel:[0,0,1] op_sel_hi:[1,0,1]\nbuffer_load_dwordx4 v6,s[40:43],s97 offen offset:2048 lds\nv_pk_fma_f32 v[94:95],v[76:77],v[100:101],v[100:101] op_sel:[0,0,1] op_sel_hi:[1,0,1]\nv_pk_fma_f32 v[96:97],v[78:79],v[100:101],v[100:101] op_sel:[0,0,1] op_sel_hi:[1,0,1]\n" \
"s_add_u32 s101,s101,s57\ns_waitcnt lgkmcnt(0)\nv_readlane_b32 s24,v112,0\nv_mov_b32 v60,v61\n" \
"s_mov_b32 s25,s24\nv_pk_add_f32 v[112:113],v[112:113],s[24:25] neg_lo:[0,1] neg_hi:[0,1]\nv_pk_fma_f32 v[80:81],v[80:81],v[16:17],v[112:113]\nbuffer_load_dwordx4 v7,s[40:43],s97 offen offset:3072 lds\n" \
"v_pk_mul_f32 v[102:103],v[80:81],v[80:81]\ns_add_u32 m0,s101,0\ns_and_b32 s101,s100,1\ns_lshr_b32 s100,s100,1\n" \
"s_lshl_b32 s100,s100,10\ns_add_u32 s100,s100,s101\ns_lshl_b32 s100,s100,7\ns_add_u32 s100,s100,s92\n" \
"buffer_load_dwordx4 v4,s[40:43],s100 offen lds\nv_pk_add_f32 v[114:115],v[114:115],s[24:25] neg_lo:[0,1] neg_hi:[0,1]\nv_pk_fma_f32 v[82:83],v[82:83],v[18:19],v[114:115]\nv_pk_add_f32 v[100:101],v[80:81],v[82:83]\n" \
"v_pk_fma_f32 v[102:103],v[82:83],v[82:83],v[102:103]\nv_pk_add_f32 v[116:117],v[116:117],s[24:25] neg_lo:[0,1] neg_hi:[0,1]\nv_pk_fma_f32 v[84:85],v[84:85],v[20:21],v[116:117]\nv_pk_add_f32 v[100:101],v[100:101],v[84:85]\n" \
"buffer_load_dwordx4 v5,s[40:43],s100 offen offset:1024 lds\nv_pk_fma_f32 v[102:103],v[84:85],v[84:85],v[102:103]\nv_pk_add_f32 v[118:119],v[118:119],s[24:25] neg_lo:[0,1] neg_hi:[0,1]\nv_pk_fma_f32 v[86:87],v[86:87],v[22:23],v[118:119]\n" \
"v_pk_add_f32 v[100:101],v[100:101],v[86:87]\nv_pk_fma_f32 v[102:103],v[86:87],v[86:87],v[102:103]\nv_pk_add_f32 v[120:121],v[120:121],s[24:25] neg_lo:[0,1] neg_hi:[0,1]\nv_pk_fma_f32 v[88:89],v[88:89],v[24:25],v[120:121]\n" \
"buffer_load_dwordx4 v6,s[40:43],s100 offen offset:2048 lds\nv_pk_add_f32 v[100:101],v[100:101],v[88:89]\nv_pk_fma_f32 v[102:103],v[88:89],v[88:89],v[102:103]\nv_pk_add_f32 v[122:123],v[122:123],s[24:25] neg_lo:[0,1] neg_hi:[0,1]\n" \
"v_pk_fma_f32 v[90:91],v[90:91],v[26:27],v[122:123]\nv_pk_add_f32 v[100:101],v[100:101],v[90:91]\nv_pk_fma_f32 v[102:103],v[90:91],v[90:91],v[102:103]\nv_pk_add_f32 v[124:125],v[124:125],s[24:25] neg_lo:[0,1] neg_hi:[0,1]\n" \
"buffer_load_dwordx4 v7,s[40:43],s100 offen offset:3072 lds\nv_pk_fma_f32 v[92:93],v[92:93],v[28:29],v[124:125]\nv_pk_add_f32 v[100:101],v[100:101],v[92:93]\nv_pk_fma_f32 v[102:103],v[92:93],v[92:93],v[102:103]\n" \
"v_pk_add_f32 v[126:127],v[126:127],s[24:25] neg_lo:[0,1] neg_hi:[0,1]\nv_pk_fma_f32 v[94:95],v[94:95],v[30:31],v[126:127]\nv_pk_add_f32 v[100:101],v[100:101],v[94:95]\nv_pk_fma_f32 v[102:103],v[94:95],v[94:95],v[102:103]\n" \
"v_pk_add_f32 v[98:99],v[98:99],s[24:25] neg_lo:[0,1] neg_hi:[0,1]\nv_pk_fma_f32 v[96:97],v[96:97],v[58:59],v[98:99]\nv_pk_add_f32 v[100:101],v[100:101],v[96:97]\nv_pk_fma_f32 v[102:103],v[96:97],v[96:97],v[102:103]\n" \
"v_add_f32 v100,v100,v101\nv_add_f32 v102,v102,v103\nv_pk_add_f32 v[62:63],v[62:63],v[60:61] op_sel_hi:[1,0]\nv_cvt_pk_bf16_f32 v32,v62,v63\n" \
"v_permlane16_swap_b32 v100,v102\nv_add_f32 v100,v100,v102\nv_pk_add_f32 v[64:65],v[64:65],v[60:61] op_sel_hi:[1,0]\nv_cvt_pk_bf16_f32 v33,v64,v65\n" \
"v_add_f32_dpp v100,v100,v100 quad_perm:[1,0,3,2] row_mask:0xf bank_mask:0xf bound_ctrl:1\nv_pk_add_f32 v[66:67],v[66:67],v[60:61] op_sel_hi:[1,0]\nv_cvt_pk_bf16_f32 v34,v66,v67\nv_add_f32_dpp v100,v100,v100 quad_perm:[2,3,0,1] row_mask:0xf bank_mask:0xf bound_ctrl:1\n" \
"v_pk_add_f32 v[68:69],v[68:69],v[60:61] op_sel_hi:[1,0]\nv_cvt_pk_bf16_f32 v35,v68,v69\nv_add_f32_dpp v100,v100,v100 row_half_mirror row_mask:0xf bank_mask:0xf bound_ctrl:1\nv_pk_add_f32 v[70:71],v[70:71],v[60:61] op_sel_hi:[1,0]\n" \
"v_cvt_pk_bf16_f32 v36,v70,v71\nv_add_f32_dpp v100,v100,v100 row_mirror row_mask:0xf bank_mask:0xf bound_ctrl:1\nv_mul_f32 v104,0x3ae38e39,v100\nv_mul_f32 v102,0x3ae38e39,v100\n" \
"v_pk_add_f32 v[72:73],v[72:73],v[60:61] op_sel_hi:[1,0]\nv_cvt_pk_bf16_f32 v37,v72,v73\nv_permlane16_swap_b32 v104,v102\nv_fma_f32 v102,-v104,v104,v102\n" \
"v_max_f32 v102,0,v102\nv_add_f32 v102,0x3727c5ac,v102\nv_rsq_f32_e64 v100,v102\nv_pk_add_f32 v[74:75],v[74:75],v[60:61] op_sel_hi:[1,0]\n" \
"v_mul_f32 v101,-v104,v100\nv_pk_fma_f32 v[80:81],v[80:81],v[100:101],v[100:101] op_sel:[0,0,1] op_sel_hi:[1,0,1]\nv_pk_fma_f32 v[82:83],v[82:83],v[100:101],v[100:101] op_sel:[0,0,1] op_sel_hi:[1,0,1]\nv_pk_fma_f32 v[84:85],v[84:85],v[100:101],v[100:101] op_sel:[0,0,1] op_sel_hi:[1,0,1]\n" \
"v_pk_fma_f32 v[86:87],v[86:87],v[100:101],v[100:101] op_sel:[0,0,1] op_sel_hi:[1,0,1]\nv_cvt_pk_bf16_f32 v16,v80,v81\nv_cvt_pk_bf16_f32 v17,v82,v83\nv_cvt_pk_bf16_f32 v18,v84,v85\n" \
"v_cvt_pk_bf16_f32 v19,v86,v87\nds_write_b128 v48,v[16:19]\nv_pk_fma_f32 v[88:89],v[88:89],v[100:101],v[100:101] op_sel:[0,0,1] op_sel_hi:[1,0,1]\nv_pk_fma_f32 v[90:91],v[90:91],v[100:101],v[100:101] op_sel:[0,0,1] op_sel_hi:[1,0,1]\n" \
"v_pk_fma_f32 v[92:93],v[92:93],v[100:101],v[100:101] op_sel:[0,0,1] op_sel_hi:[1,0,1]\nv_pk_fma_f32 v[94:95],v[94:95],v[100:101],v[100:101] op_sel:[0,0,1] op_sel_hi:[1,0,1]\nv_cvt_pk_bf16_f32 v20,v88,v89\nv_cvt_pk_bf16_f32 v21,v90,v91\n" \
"v_cvt_pk_bf16_f32 v22,v92,v93\nv_cvt_pk_bf16_f32 v23,v94,v95\nds_write_b128 v48,v[20:23] offset:512\nv_pk_fma_f32 v[96:97],v[96:97],v[100:101],v[100:101] op_sel:[0,0,1] op_sel_hi:[1,0,1]\n" \
"v_cvt_pk_bf16_f32 v24,v96,v97\nds_write_b32 v49,v24 offset:1024\nv_cvt_pk_bf16_f32 v38,v74,v75\nv_pk_add_f32 v[76:77],v[76:77],v[60:61] op_sel_hi:[1,0]\n" \
"v_cvt_pk_bf16_f32 v39,v76,v77\nv_pk_add_f32 v[78:79],v[78:79],v[60:61] op_sel_hi:[1,0]\nv_cvt_pk_bf16_f32 v40,v78,v79\nv_bfe_u32 v50,v0,2,2\n" \
"v_mul_u32_u24 v50,4896,v50\nv_bfe_u32 v51,v0,4,2\nv_lshl_add_u32 v51,v51,2,s3\nv_lshl_add_u32 v50,v51,5,v50\n" \
"v_and_b32 v52,3,v0\nv_lshl_add_u32 v50,v52,3,v50\nv_and_b32 v52,0xf0,v1\nv_lshl_add_u32 v51,v51,16,v52\n" \
"v_bfrev_b32 v52,1\nv_cmp_gt_u32 vcc,256,v1\nv_and_b32 v53,31,v0\nv_mul_u32_u24 v53,12,v53\n" \
"v_cndmask_b32 v52,v52,v51,vcc\nv_sub_u32 v53,v12,v53\ns_lshl_b32 s24,s34,8\ns_add_u32 s24,s24,9764864\n" \
"s_waitcnt lgkmcnt(0)\ns_barrier\nds_read_b64_tr_b16 v[112:113],v50 offset:9216\nds_read_b64_tr_b16 v[114:115],v50 offset:11664\n" \
"ds_read_b64_tr_b16 v[116:117],v50 offset:9728\nds_read_b64_tr_b16 v[118:119],v50 offset:12176\nds_read_b64_tr_b16 v[120:121],v50 offset:10240\nds_read_b64_tr_b16 v[122:123],v50 offset:12688\n" \
"s_add_u32 s25,s24,1048576\ns_add_u32 s26,s24,2097152\ns_add_u32 s13,s13,7405568\ns_load_dwordx2 s[4:5],s[0:1],56\n" \
"s_load_dwordx2 s[16:17],s[0:1],64\ns_waitcnt lgkmcnt(0)\nbuffer_store_dwordx4 v[112:115],v51,s[36:39],s24 offen sc1\nbuffer_store_dwordx4 v[116:119],v51,s[36:39],s25 offen sc1\n" \
"buffer_store_dwordx4 v[120:123],v52,s[36:39],s26 offen sc1\nbuffer_store_dwordx4 v[32:35],v12,s[36:39],s13 offen sc1\nbuffer_store_dwordx4 v[36:39],v12,s[36:39],s13 offen offset:512 sc1\nbuffer_store_dword v40,v53,s[36:39],s13 offen offset:1024 sc1\n" \
"s_lshr_b32 s31,s33,3\ns_lshl1_add_u32 s31,s31,0\ns_sub_u32 s96,s31,6\ns_min_u32 s31,s31,s96\n" \
"s_lshl_b32 s96,s31,12\ns_add_u32 s96,s96,s57\ns_add_u32 m0,s96,0\ns_and_b32 s96,s31,1\n" \
"s_lshr_b32 s31,s31,1\ns_lshl_b32 s31,s31,10\ns_add_u32 s31,s31,s96\ns_lshl_b32 s31,s31,7\n" \
"s_add_u32 s31,s31,s92\ns_add_u32 s31,s31,262144\ns_lshr_b32 s97,s33,3\ns_lshl1_add_u32 s97,s97,1\n" \
"s_sub_u32 s95,s97,6\ns_min_u32 s97,s97,s95\ns_lshl_b32 s95,s97,12\ns_add_u32 s95,s95,s57\n" \
"s_lshr_b32 s100,s33,3\ns_lshl1_add_u32 s100,s100,2\ns_sub_u32 s101,s100,6\ns_min_u32 s100,s100,s101\n" \
"s_lshl_b32 s101,s100,12\ns_add_u32 s101,s101,s57\nv_and_b32 v64,63,v0\ns_and_b32 s25,s33,7\n" \
"v_lshrrev_b32 v66,5,v64\nv_and_b32 v67,1,v0\ns_lshl2_add_u32 s25,s25,s3\ns_and_b32 s26,s33,6\n" \
"s_lshr_b32 s27,s3,1\nv_lshl_add_u32 v66,v66,1,v67\ns_mul_i32 s24,s32,1152\ns_lshl_b32 s25,s25,5\n" \
"s_add_u32 s26,s26,s27\ns_mov_b32 s6,9216\ns_mov_b32 s7,0x20000\nv_lshlrev_b32 v68,3,v66\n" \
"s_add_u32 s25,s25,s24\nv_bfe_u32 v72,v0,1,1\nv_lshrrev_b32 v74,2,v64\ns_lshl4_add_u32 s26,s26,s24\n" \
"v_and_b32 v73,12,v0\nv_lshrrev_b32 v71,4,v64\nv_and_b32 v69,7,v0\nbuffer_load_dwordx2 v[80:81],v68,s[4:7],s25 offen\n" \
"v_lshl_add_u32 v72,v72,2,s21\nv_and_b32 v74,12,v74\nv_add_u32 v71,v71,v73\ns_lshl_b32 s24,s3,4\n" \
"v_lshlrev_b32 v70,1,v69\nv_lshl_add_u32 v72,v72,4,v74\ns_mov_b32 s15,s19\nv_add_u32 v71,s24,v71\n" \
"buffer_load_ushort v82,v70,s[4:7],s26 offen offset:1024\ns_mov_b32 s18,4096\ns_mov_b32 s19,0x20000\nv_lshlrev_b32 v74,1,v71\n" \
"s_lshl_b32 s24,s22,8\nbuffer_load_ushort v83,v74,s[16:19],s24 offen\nv_add_u32 v72,s3,v72\nbuffer_load_ushort v84,v74,s[16:19],s24 offen offset:128\n" \
"v_lshlrev_b32_e64 v75,1,v72\nbuffer_load_ushort v85,v75,s[16:19],s24 offen\nv_lshl_add_u32 v73,v67,1,v73\nv_mul_u32_u24 v91,1152,v72\n" \
"v_lshl_add_u32 v91,v73,1,v91\nv_lshlrev_b32_e64 v73,2,v73\nv_add_u32 v73,163712,v73\nds_read_b64 v[86:87],v73\n" \
"s_mul_i32 s24,s22,147456\ns_mul_i32 s25,s20,288\nv_bfe_u32 v58,v0,1,1\ns_add_u32 s24,s24,s25\n" \
"v_mul_u32_u24 v58,544,v58\nv_bfe_u32 v75,v0,2,1\ns_lshl_b32 s25,s21,6\nv_lshl_add_u32 v58,v75,8,v58\n" \
"s_add_u32 s25,s25,s24\nv_bfe_u32 v75,v0,3,2\ns_add_u32 s24,s24,256\nv_lshl_add_u32 v58,v75,6,v58\n" \
"v_add_u32 v91,s24,v91\nv_mul_u32_u24 v90,1152,v71\nv_and_b32 v74,3,v0\nv_lshl_add_u32 v58,v66,4,v58\n" \
"s_mul_i32 s24,s3,1056\nv_lshl_add_u32 v90,v74,3,v90\nv_add_u32 v58,s24,v58\nv_add_u32 v90,s25,v90\n" \
"s_and_b32 s24,s3,1\nv_mul_u32_u24 v60,288,v75\ns_lshl_b32 s24,s24,2\ns_lshr_b32 s25,s3,1\n" \
"v_lshrrev_b32 v75,5,v64\ns_add_u32 s24,s24,s25\nv_lshl_add_u32 v60,v75,6,v60\ns_lshl_b32 s24,s24,5\n" \
"v_lshl_add_u32 v60,v69,2,v60\ns_add_u32 s24,s24,4224\nv_add_u32 v60,s24,v60\ns_bfe_u32 s24,s33,0x10001\n" \
"s_add_u32 s24,s24,s15\ns_add_u32 s24,s24,17\ns_lshl2_add_u32 s24,s24,s3\ns_waitcnt lgkmcnt(0)\n" \
"v_lshrrev_b32 v86,16,v86\ns_lshl_b32 s24,s24,7\nv_mov_b32 v92,v4\nv_mov_b32 v93,v5\n" \
"v_add_u32 v74,s57,v1\nv_or_b32 v95,v86,v87\nv_lshl_add_u32 v62,v64,1,s24\nv_bfrev_b32 v76,1\n" \
"v_cmp_eq_u32 vcc,0,v64\ns_lshl2_add_u32 s24,s34,s3\ns_lshl2_add_u32 s24,s24,65536\nv_cndmask_b32 v76,v76,0,vcc\n" \
"v_mov_b32 v77,1\ns_barrier\nv_mov_b32 v57,0xc0135761\nv_add_u32 v59,0x80000,v1\n" \
"v_lshl_add_u32 v61,v64,3,s23\nv_mov_b32 v63,0\nv_bfe_u32 v100,v0,4,2\nv_bfe_u32 v101,v0,2,2\n" \
"v_lshl_add_u32 v100,v100,3,v101\nv_bfe_u32 v101,v0,3,2\nv_lshl_add_u32 v100,v100,2,v101\nv_and_b32 v101,3,v0\n" \
"v_lshl_add_u32 v100,v100,2,v101\nv_lshlrev_b32 v100,3,v100\nv_xor_b32 v8,0,v100\nv_add_u32 v8,s57,v8\n" \
"v_mov_b32 v88,v8\nv_xor_b32 v9,32,v100\nv_add_u32 v9,s57,v9\nv_xor_b32 v10,64,v100\n" \
"v_add_u32 v10,s57,v10\nv_xor_b32 v11,96,v100\nv_add_u32 v11,s57,v11\ns_waitcnt vmcnt(5)\n" \
"buffer_store_dword v77,v76,s[36:39],s24 offen sc1\nds_read_b64_tr_b16 v[128:129],v8\nds_read_b64_tr_b16 v[130:131],v8 offset:512\nds_read_b64_tr_b16 v[132:133],v9\n" \
"ds_read_b64_tr_b16 v[134:135],v9 offset:512\nds_read_b64_tr_b16 v[136:137],v10\nds_read_b64_tr_b16 v[138:139],v10 offset:512\nds_read_b64_tr_b16 v[140:141],v11\n" \
"ds_read_b64_tr_b16 v[142:143],v11 offset:512\nds_read_b64_tr_b16 v[144:145],v8 offset:4096\nds_read_b64_tr_b16 v[146:147],v8 offset:4608\nds_read_b64_tr_b16 v[148:149],v9 offset:4096\n" \
"ds_read_b64_tr_b16 v[150:151],v9 offset:4608\nds_read_b64_tr_b16 v[152:153],v10 offset:4096\nds_read_b64_tr_b16 v[154:155],v10 offset:4608\nds_read_b64_tr_b16 v[156:157],v11 offset:4096\n" \
"ds_read_b64_tr_b16 v[158:159],v11 offset:4608\nds_read_b64_tr_b16 v[160:161],v8 offset:8192\nds_read_b64_tr_b16 v[162:163],v8 offset:8704\ns_waitcnt vmcnt(5)\n" \
"v_and_b32 v53,0xffff0000,v80\nv_and_b32 v55,0xffff0000,v81\nv_lshlrev_b32 v52,16,v80\nv_lshlrev_b32 v54,16,v81\n" \
"ds_read_b64_tr_b16 v[164:165],v9 offset:8192\nds_read_b64_tr_b16 v[166:167],v9 offset:8704\nds_read_b64_tr_b16 v[168:169],v10 offset:8192\nds_read_b64_tr_b16 v[170:171],v10 offset:8704\n" \
"ds_read_b64_tr_b16 v[172:173],v11 offset:8192\ns_waitcnt vmcnt(4)\nv_lshlrev_b32 v56,16,v82\nds_read_b64_tr_b16 v[174:175],v11 offset:8704\n" \
"ds_read_b64_tr_b16 v[176:177],v8 offset:12288\nds_read_b64_tr_b16 v[178:179],v8 offset:12800\nds_read_b64_tr_b16 v[180:181],v9 offset:12288\nds_read_b64_tr_b16 v[182:183],v9 offset:12800\n" \
"s_waitcnt vmcnt(2)\nv_lshl_or_b32 v89,v84,16,v83\nds_write_b128 v74,v[88:91] offset:28672\ns_waitcnt vmcnt(1)\n" \
"v_lshlrev_b32_e64 v94,16,v85\nds_write_b128 v74,v[92:95] offset:29696\nds_read_b64_tr_b16 v[184:185],v10 offset:12288\nds_read_b64_tr_b16 v[186:187],v10 offset:12800\n" \
"ds_read_b64_tr_b16 v[188:189],v11 offset:12288\nds_read_b64_tr_b16 v[190:191],v11 offset:12800\ns_waitcnt lgkmcnt(0)\nbuffer_load_dwordx4 v4,s[40:43],s31 offen lds\n" \
"buffer_load_dwordx4 v5,s[40:43],s31 offen offset:1024 lds\nbuffer_load_dwordx4 v6,s[40:43],s31 offen offset:2048 lds\nbuffer_load_dwordx4 v7,s[40:43],s31 offen offset:3072 lds\ns_add_u32 m0,s95,0\n" \
"s_and_b32 s95,s97,1\ns_lshr_b32 s97,s97,1\ns_lshl_b32 s97,s97,10\ns_add_u32 s97,s97,s95\n" \
"s_lshl_b32 s97,s97,7\ns_add_u32 s97,s97,s92\ns_add_u32 s97,s97,262144\nbuffer_load_dwordx4 v4,s[40:43],s97 offen lds\n" \
"buffer_load_dwordx4 v5,s[40:43],s97 offen offset:1024 lds\nbuffer_load_dwordx4 v6,s[40:43],s97 offen offset:2048 lds\nbuffer_load_dwordx4 v7,s[40:43],s97 offen offset:3072 lds\ns_add_u32 m0,s101,0\n" \
"s_and_b32 s101,s100,1\ns_lshr_b32 s100,s100,1\ns_lshl_b32 s100,s100,10\ns_add_u32 s100,s100,s101\n" \
"s_lshl_b32 s100,s100,7\ns_add_u32 s100,s100,s92\ns_add_u32 s100,s100,262144\nbuffer_load_dwordx4 v4,s[40:43],s100 offen lds\n" \
"buffer_load_dwordx4 v5,s[40:43],s100 offen offset:1024 lds\nbuffer_load_dwordx4 v6,s[40:43],s100 offen offset:2048 lds\nbuffer_load_dwordx4 v7,s[40:43],s100 offen offset:3072 lds\ns_lshr_b32 s31,s33,3\n" \
"s_lshl1_add_u32 s31,s31,3\ns_sub_u32 s96,s31,6\ns_min_u32 s31,s31,s96\ns_lshl_b32 s96,s31,12\n" \
"s_add_u32 s96,s96,s57\ns_add_u32 m0,s96,0\ns_and_b32 s96,s31,1\ns_lshr_b32 s31,s31,1\n" \
"s_lshl_b32 s31,s31,10\ns_add_u32 s31,s31,s96\ns_lshl_b32 s31,s31,7\ns_add_u32 s31,s31,s92\n" \
"s_add_u32 s31,s31,262144\nbuffer_load_dwordx4 v4,s[40:43],s31 offen lds\nbuffer_load_dwordx4 v5,s[40:43],s31 offen offset:1024 lds\nbuffer_load_dwordx4 v6,s[40:43],s31 offen offset:2048 lds\n" \
"buffer_load_dwordx4 v7,s[40:43],s31 offen offset:3072 lds\ns_lshr_b32 s97,s33,3\ns_lshl1_add_u32 s97,s97,4\ns_sub_u32 s95,s97,6\n" \
"s_min_u32 s97,s97,s95\ns_lshl_b32 s95,s97,12\ns_add_u32 s95,s95,s57\ns_add_u32 m0,s95,0\n" \
"s_and_b32 s95,s97,1\ns_lshr_b32 s97,s97,1\ns_lshl_b32 s97,s97,10\ns_add_u32 s97,s97,s95\n" \
"s_lshl_b32 s97,s97,7\ns_add_u32 s97,s97,s92\ns_add_u32 s97,s97,262144\nbuffer_load_dwordx4 v4,s[40:43],s97 offen lds\n" \
"buffer_load_dwordx4 v5,s[40:43],s97 offen offset:1024 lds\nbuffer_load_dwordx4 v6,s[40:43],s97 offen offset:2048 lds\nbuffer_load_dwordx4 v7,s[40:43],s97 offen offset:3072 lds\ns_lshr_b32 s100,s33,3\n" \
"s_lshl1_add_u32 s100,s100,5\ns_sub_u32 s101,s100,6\ns_min_u32 s100,s100,s101\ns_lshl_b32 s101,s100,12\n" \
"s_add_u32 s101,s101,s57\ns_add_u32 m0,s101,0\ns_and_b32 s101,s100,1\ns_lshr_b32 s100,s100,1\n" \
"s_lshl_b32 s100,s100,10\ns_add_u32 s100,s100,s101\ns_lshl_b32 s100,s100,7\ns_add_u32 s100,s100,s92\n" \
"s_add_u32 s100,s100,262144\nbuffer_load_dwordx4 v4,s[40:43],s100 offen lds\nbuffer_load_dwordx4 v5,s[40:43],s100 offen offset:1024 lds\nbuffer_load_dwordx4 v6,s[40:43],s100 offen offset:2048 lds\n" \
"buffer_load_dwordx4 v7,s[40:43],s100 offen offset:3072 lds\ns_branch .Lgemm_%=\n.LC_%=:\nv_bfe_u32 v112,v0,3,2\n" \
"v_bfe_u32 v113,v0,5,1\nv_lshl_add_u32 v112,v112,8,v113\ns_lshl_b32 s48,s33,2\nv_lshrrev_b32 v113,3,v0\n" \
"s_add_u32 s48,s48,s3\nv_and_b32 v113,6,v113\nv_and_b32 v114,7,v0\ns_mul_i32 s48,s48,18432\n" \
"s_mul_i32 s49,s32,1152\nv_mul_u32_u24 v112,9216,v112\nv_xor_b32 v113,v113,v114\ns_add_u32 s48,s48,s49\n" \
"v_lshl_add_u32 v4,v113,4,v112\ns_mov_b32 s10,18874368\ns_mov_b32 s11,0x20000\ns_add_u32 s48,s48,1024\n" \
"s_add_u32 m0,s88,20224\ns_add_u32 s50,s48,9437184\nbuffer_load_dwordx4 v4,s[8:11],s48 offen lds\ns_add_u32 m0,s88,24320\n" \
"s_mov_b32 s51,4096\nbuffer_load_dwordx4 v4,s[8:11],s50 offen lds\ns_add_u32 m0,s88,28416\ns_mov_b32 s52,8192\n" \
"buffer_load_dwordx4 v2,s[4:7],0 offen lds\ns_add_u32 m0,s88,32512\ns_mov_b32 s53,14336\nbuffer_load_dwordx4 v2,s[4:7],s51 offen lds\n" \
"buffer_load_dwordx4 v[234:237],v1,s[4:7],s52 offen\nbuffer_load_dwordx4 v[238:241],v1,s[4:7],s52 offen offset:1024\nbuffer_load_dwordx4 v[242:245],v2,s[4:7],s53 offen\ns_mov_b32 s52,18432\n" \
"buffer_load_dwordx4 v[252:255],v2,s[4:7],s52 offen\ns_and_b32 s60,s33,7\ns_mul_i32 s92,s3,589824\ns_mul_i32 s57,s3,30720\n" \
"s_add_u32 s92,s92,s49\ns_lshl_b32 s62,s60,7\nv_add_u32 v5,17408,v4\nv_add_u32 v6,34816,v4\n" \
"v_add_u32 v7,52224,v4\ns_add_u32 s57,s57,38272\ns_add_u32 s92,s92,s62\ns_load_dwordx4 s[44:47],s[0:1],72\n" \
"v_and_b32 v120,0x3c0,v1\ns_lshr_b32 s59,s33,3\nv_bfe_u32 v121,v0,3,2\nv_lshlrev_b32 v120,1,v120\n" \
"s_lshl_b32 s56,s34,8\nv_bfe_u32 v73,v0,4,2\ns_lshl_b32 s59,s59,3\nv_bfe_u32 v113,v0,4,2\n" \
"v_lshl_add_u32 v120,v121,5,v120\nv_and_b32 v122,3,v0\ns_mul_i32 s54,s34,9344\ns_add_u32 s56,s56,10289152\n" \
"s_add_u32 s59,s59,s32\nv_and_b32 v72,15,v0\nv_cvt_f32_u32 v73,v73\nv_cvt_f32_u32 v74,s3\n" \
"v_mul_u32_u24 v113,589824,v113\nv_and_b32 v112,15,v0\nv_lshl_add_u32 v120,v122,3,v120\ns_add_u32 s35,s54,524288\n" \
"s_cmp_eq_u32 s3,2\ns_cselect_b32 s56,s56,2147483648\ns_lshl_b32 s59,s59,3\nv_bfe_u32 v233,v0,3,3\n" \
"v_mul_u32_u24 v233,8192,v233\nv_lshl_add_u32 v233,v1,3,v233\ns_lshl2_add_u32 s100,s33,s3\ns_add_u32 s100,s100,4294967260\n" \
"s_and_b32 s101,s100,31\ns_lshl_b32 s101,s101,3\ns_cmp_lt_u32 s100,256\ns_cselect_b32 s100,s100,0x40000\n" \
"s_lshr_b32 s100,s100,5\ns_add_u32 s100,s100,s101\ns_lshl_b32 s100,s100,6\ns_add_u32 s100,s100,s32\n" \
"s_mul_i32 s100,s100,1152\ns_mov_b32 m0,162176\nv_cvt_f32_u32 v72,v72\nv_fmamk_f32 v73,v73,0x41800000,v74\n" \
"v_lshl_add_u32 v113,v112,4,v113\ns_add_u32 s58,s35,8192\nv_bfe_u32 v119,v0,4,2\nv_xor_b32 v8,0,v120\n" \
"v_xor_b32 v9,32,v120\nv_xor_b32 v10,64,v120\nv_xor_b32 v11,96,v120\ns_add_u32 s59,s59,s60\n" \
"v_mul_f32 v72,0xbb800000,v72\nv_and_b32 v118,15,v0\nv_mul_f32 v73,4.0,v73\nv_and_b32 v116,0x70,v2\n" \
"v_add_u32 v113,s56,v113\nv_add_u32 v114,s58,v2\nv_cmp_gt_u32 vcc,68,v0\nv_lshrrev_b32 v115,3,v0\n" \
"v_cvt_f32_u32 v120,s34\ns_lshl_b32 s59,s59,7\ns_lshl_b32 s61,s34,4\nv_lshrrev_b32 v48,1,v118\n" \
"v_and_b32 v49,1,v119\nv_mul_f32 v73,v73,v72\nv_cndmask_b32 v113,v113,v114,vcc\nv_lshl_add_u32 v115,v115,16,v116\n" \
"v_mul_f32 v50,0x3b800000,v120\ns_add_u32 s87,s59,262144\ns_add_u32 s61,s61,65536\nv_cvt_f32_u32 v48,v48\n" \
"v_cvt_f32_u32 v49,v49\nv_fmamk_f32 v79,v72,0x41800000,v73\nv_bfe_u32 v100,v0,1,2\nv_and_b32 v101,1,v0\n" \
"v_add_u32 v115,s87,v115\nv_mov_b32 v116,s61\nv_cmp_gt_u32 vcc,24,v0\nv_mul_f32 v48,0xbe000000,v48\n" \
"v_fma_f32 v49,v49,2.0,v50\nv_cos_f32 v78,v79\nv_lshl_add_u32 v100,v101,2,v100\nv_cndmask_b32 v115,v116,v115,vcc\n" \
"v_mul_f32 v49,v48,v49\nv_fmamk_f32 v81,v72,0x42000000,v73\nv_fmamk_f32 v83,v72,0x42400000,v73\nv_mul_f32 v101,0x3a000000,v120\n" \
"v_cvt_f32_u32 v100,v100\nv_bfrev_b32 v116,1\nv_cmp_gt_u32 vcc,25,v0\nv_fmamk_f32 v63,v48,0x40800000,v49\n" \
"v_cos_f32 v80,v81\nv_cos_f32 v82,v83\nv_fmamk_f32 v100,v100,0x3e000000,v101\nv_cndmask_b32 v115,v116,v115,vcc\n" \
"v_mov_b32 v16,-1\nv_mov_b32 v17,-1\nv_mov_b32 v18,-1\nv_mov_b32 v19,-1\n" \
"v_lshrrev_b32 v50,1,v119\nv_and_b32 v51,1,v0\nv_cos_f32 v62,v63\nv_cos_f32 v76,v73\n" \
"v_sin_f32 v77,v73\nv_sin_f32 v79,v79\nv_cos_f32 v84,v72\nv_sin_f32 v85,v72\n" \
"v_cvt_f32_u32 v101,v119\nbuffer_store_dwordx4 v[16:19],v2,s[36:39],s35 offen sc1\nv_cmp_eq_u32 vcc,v50,v51\nv_add_f32 v101,v101,v101\n" \
"v_mov_b32 v50,0x3e800000\nv_mul_f32 v101,v101,v100\nv_sin_f32 v81,v81\nv_sin_f32 v83,v83\n" \
"v_cndmask_b32 v52,0,v50,vcc\nv_cos_f32 v56,v49\nv_sin_f32 v57,v49\nv_sin_f32 v63,v63\n" \
"v_cos_f32 v106,v101\nv_sin_f32 v107,v101\nv_cmp_gt_u32 vcc,8,v118\nv_cvt_pk_f16_f32 v128,v76,-v77\n" \
"v_cvt_pk_f16_f32 v132,v77,v76\nv_cvt_pk_f16_f32 v129,v78,-v79\nv_cvt_pk_f16_f32 v133,v79,v78\nv_pk_mul_f32 v[92:93],v[76:77],v[84:85] op_sel:[1,1] op_sel_hi:[0,1] neg_lo:[1,0]\n" \
"v_pk_mul_f32 v[94:95],v[78:79],v[84:85] op_sel:[1,1] op_sel_hi:[0,1] neg_lo:[1,0]\ns_add_u32 s55,s35,4096\nv_cndmask_b32 v102,0,0.5,vcc\nv_lshlrev_b32 v51,31,v0\n" \
"buffer_store_dwordx4 v[16:19],v2,s[36:39],s55 offen sc1\nv_pk_fma_f32 v[76:77],v[76:77],v[84:85],v[92:93] op_sel_hi:[1,0,1]\nv_pk_fma_f32 v[78:79],v[78:79],v[84:85],v[94:95] op_sel_hi:[1,0,1]\nv_xor_b32 v54,v51,v52\n" \
"v_sub_f32_e64 v104,0.5,v102\nv_cvt_pk_f16_f32 v130,v80,-v81\nv_cvt_pk_f16_f32 v134,v81,v80\nv_cvt_pk_f16_f32 v131,v82,-v83\n" \
"v_cvt_pk_f16_f32 v135,v83,v82\nv_pk_mul_f32 v[92:93],v[80:81],v[84:85] op_sel:[1,1] op_sel_hi:[0,1] neg_lo:[1,0]\nv_pk_mul_f32 v[94:95],v[82:83],v[84:85] op_sel:[1,1] op_sel_hi:[0,1] neg_lo:[1,0]\nv_pk_mul_f32 v[58:59],v[56:57],v[52:53] op_sel_hi:[1,0]\n" \
"v_pk_mul_f32 v[60:61],v[56:57],v[54:55] op_sel_hi:[1,0]\nv_pk_mul_f32 v[64:65],v[62:63],v[52:53] op_sel_hi:[1,0]\nv_pk_mul_f32 v[66:67],v[62:63],v[54:55] op_sel_hi:[1,0]\nv_pk_mul_f32 v[108:109],v[106:107],v[102:103] op_sel_hi:[1,0]\n" \
"v_pk_mul_f32 v[110:111],v[106:107],v[104:105] op_sel_hi:[1,0]\nv_pk_fma_f32 v[80:81],v[80:81],v[84:85],v[92:93] op_sel_hi:[1,0,1]\nv_pk_fma_f32 v[82:83],v[82:83],v[84:85],v[94:95] op_sel_hi:[1,0,1]\nv_fmamk_f32 v57,v48,0x3f800000,v49\n" \
"v_fmamk_f32 v63,v48,0x40a00000,v49\nv_fmamk_f32 v107,v100,0x3f800000,v101\nv_cvt_pk_f16_f32 v136,v76,-v77\nv_cvt_pk_f16_f32 v140,v77,v76\n" \
"v_cvt_pk_f16_f32 v137,v78,-v79\nv_cvt_pk_f16_f32 v141,v79,v78\nv_pk_mul_f32 v[92:93],v[76:77],v[84:85] op_sel:[1,1] op_sel_hi:[0,1] neg_lo:[1,0]\nv_pk_mul_f32 v[94:95],v[78:79],v[84:85] op_sel:[1,1] op_sel_hi:[0,1] neg_lo:[1,0]\n" \
"v_cos_f32 v56,v57\nv_cos_f32 v62,v63\nv_cos_f32_e64 v106,v107\nbuffer_store_dwordx4 v[16:19],v113,s[36:39],0 offen sc1\n" \
"v_pk_fma_f32 v[76:77],v[76:77],v[84:85],v[92:93] op_sel_hi:[1,0,1]\nv_pk_fma_f32 v[78:79],v[78:79],v[84:85],v[94:95] op_sel_hi:[1,0,1]\nv_cvt_pk_f16_f32 v138,v80,-v81\nv_cvt_pk_f16_f32 v142,v81,v80\n" \
"v_cvt_pk_f16_f32 v139,v82,-v83\nv_cvt_pk_f16_f32 v143,v83,v82\nv_pk_mul_f32 v[92:93],v[80:81],v[84:85] op_sel:[1,1] op_sel_hi:[0,1] neg_lo:[1,0]\nv_pk_mul_f32 v[94:95],v[82:83],v[84:85] op_sel:[1,1] op_sel_hi:[0,1] neg_lo:[1,0]\n" \
"v_pk_fma_f32 v[80:81],v[80:81],v[84:85],v[92:93] op_sel_hi:[1,0,1]\nv_pk_fma_f32 v[82:83],v[82:83],v[84:85],v[94:95] op_sel_hi:[1,0,1]\nv_sin_f32 v57,v57\nv_sin_f32 v63,v63\n" \
"v_sin_f32_e64 v107,v107\nv_cvt_pk_f16_f32 v144,v76,-v77\nv_cvt_pk_f16_f32 v148,v77,v76\nv_cvt_pk_f16_f32 v145,v78,-v79\n" \
"v_cvt_pk_f16_f32 v149,v79,v78\nv_pk_mul_f32 v[92:93],v[76:77],v[84:85] op_sel:[1,1] op_sel_hi:[0,1] neg_lo:[1,0]\nv_pk_mul_f32 v[94:95],v[78:79],v[84:85] op_sel:[1,1] op_sel_hi:[0,1] neg_lo:[1,0]\nv_mov_b32 v20,0\n" \
"v_mov_b32 v21,0\nv_mov_b32 v22,0\nv_mov_b32 v23,0\nbuffer_store_dwordx4 v[20:23],v115,s[36:39],0 offen sc1\n" \
"v_pk_fma_f32 v[76:77],v[76:77],v[84:85],v[92:93] op_sel_hi:[1,0,1]\nv_pk_fma_f32 v[78:79],v[78:79],v[84:85],v[94:95] op_sel_hi:[1,0,1]\nv_cvt_pk_f16_f32 v40,v58,-v61\nv_cvt_pk_f16_f32 v44,v59,v60\n" \
"v_cvt_pk_f16_f32 v41,v64,-v67\nv_cvt_pk_f16_f32 v45,v65,v66\nv_cvt_pk_f16_f32 v216,v108,-v109\nv_cvt_pk_f16_f32 v220,v109,v108\n" \
"v_cvt_pk_f16_f32 v217,v110,-v111\nv_cvt_pk_f16_f32 v221,-v111,-v110\nv_cvt_pk_f16_f32 v146,v80,-v81\nv_cvt_pk_f16_f32 v150,v81,v80\n" \
"v_cvt_pk_f16_f32 v147,v82,-v83\nv_cvt_pk_f16_f32 v151,v83,v82\nv_pk_mul_f32 v[92:93],v[80:81],v[84:85] op_sel:[1,1] op_sel_hi:[0,1] neg_lo:[1,0]\nv_pk_mul_f32 v[94:95],v[82:83],v[84:85] op_sel:[1,1] op_sel_hi:[0,1] neg_lo:[1,0]\n" \
"buffer_load_dword v233,s[8:11],s100 offen lds\nv_pk_mul_f32 v[58:59],v[56:57],v[52:53] op_sel_hi:[1,0]\nv_pk_mul_f32 v[60:61],v[56:57],v[54:55] op_sel_hi:[1,0]\nv_pk_mul_f32 v[64:65],v[62:63],v[52:53] op_sel_hi:[1,0]\n" \
"v_pk_mul_f32 v[66:67],v[62:63],v[54:55] op_sel_hi:[1,0]\nv_pk_mul_f32 v[108:109],v[106:107],v[102:103] op_sel_hi:[1,0]\nv_pk_mul_f32 v[110:111],v[106:107],v[104:105] op_sel_hi:[1,0]\nv_pk_fma_f32 v[80:81],v[80:81],v[84:85],v[92:93] op_sel_hi:[1,0,1]\n" \
"v_pk_fma_f32 v[82:83],v[82:83],v[84:85],v[94:95] op_sel_hi:[1,0,1]\nv_lshlrev_b32 v3,4,v119\nv_lshrrev_b32 v15,1,v1\nv_lshl_add_u32 v3,v118,6,v3\n" \
"v_cvt_pk_f16_f32 v42,v58,-v61\nv_cvt_pk_f16_f32 v46,v59,v60\nv_cvt_pk_f16_f32 v43,v64,-v67\nv_cvt_pk_f16_f32 v47,v65,v66\n" \
"v_cvt_pk_f16_f32 v218,v108,-v109\nv_cvt_pk_f16_f32 v222,v109,v108\nv_cvt_pk_f16_f32 v219,v110,-v111\nv_cvt_pk_f16_f32 v223,-v111,-v110\n" \
"v_cvt_pk_f16_f32 v152,v76,-v77\nv_cvt_pk_f16_f32 v156,v77,v76\nv_cvt_pk_f16_f32 v153,v78,-v79\nv_cvt_pk_f16_f32 v157,v79,v78\n" \
"v_cvt_pk_f16_f32 v154,v80,-v81\nv_cvt_pk_f16_f32 v158,v81,v80\nv_cvt_pk_f16_f32 v155,v82,-v83\nv_cvt_pk_f16_f32 v159,v83,v82\n" \
"v_add_u32 v15,s57,v15\nv_add_u32 v8,s57,v8\nv_add_u32 v9,s57,v9\nv_add_u32 v10,s57,v10\n" \
"v_add_u32 v11,s57,v11\nv_lshrrev_b32 v77,5,v0\nv_bfe_u32 v113,v0,1,5\nv_and_b32 v115,1,v0\n" \
"s_lshl_b32 s63,s3,1\nv_mul_u32_u24 v77,1160,v77\nv_and_b32 v76,31,v0\nv_and_b32 v114,6,v113\n" \
"v_add_u32 v115,s63,v115\nv_mad_u32_u24 v78,v76,36,v77\nv_xor_b32 v114,v114,v115\nv_lshlrev_b32 v113,7,v113\n" \
"v_lshl_add_u32 v113,v114,4,v113\ns_add_u32 s64,s54,2916352\ns_add_u32 s65,s64,4096\nv_bfrev_b32 v114,1\n" \
"v_cmp_gt_u32 vcc,72,v0\ns_lshl_b32 s67,s32,2\ns_add_u32 s66,s64,8192\nv_cndmask_b32 v114,v114,v2,vcc\n" \
"s_add_u32 s67,s67,s3\ns_lshl_b32 s67,s67,5\ns_add_u32 s67,s67,s33\ns_lshl_b32 s67,s67,11\n" \
"s_add_u32 s67,s67,5308416\ns_lshl2_add_u32 s69,s3,161664\ns_waitcnt vmcnt(20)\nv_readfirstlane_b32 s28,v126\n" \
"v_readfirstlane_b32 s29,v127\ns_mul_i32 s72,s3,2320\nv_mov_b32 v251,s69\nv_cmp_gt_u32 vcc,32,v0\n" \
"s_add_u32 s28,s28,1\ns_addc_u32 s29,s29,0\ns_lshl_b32 s7,s32,11\nv_cndmask_b32 v74,2.0,0,vcc\n" \
"s_mov_b32 s93,128\ns_mov_b32 s73,8192\ns_mov_b32 s74,12288\ns_mov_b32 s75,17408\n" \
"s_mov_b32 s76,21504\ns_mov_b32 s77,16384\ns_mov_b32 s78,25600\ns_add_u32 s7,s7,40960\n" \
"s_mov_b32 s81,0x3d800000\ns_mov_b32 s42,18874368\ns_mov_b32 s43,0x20000\ns_waitcnt lgkmcnt(0)\n" \
"s_mov_b64 s[40:41],s[44:45]\ns_mov_b64 s[44:45],s[46:47]\ns_mov_b32 s46,2359296\ns_mov_b32 s47,0x20000\n" \
"s_waitcnt vmcnt(19)\nv_lshlrev_b32_e64 v48,16,v224\nv_and_b32 v49,0xffff0000,v224\nv_readlane_b32 s70,v48,0\n" \
"v_readlane_b32 s71,v48,32\nv_and_b32 v51,0xffff0000,v225\nv_mov_b32 v72,s70\nv_mov_b32 v73,s71\n" \
"v_lshlrev_b32 v50,16,v225\nv_and_b32 v53,0xffff0000,v226\nv_permlane32_swap_b32 v72,v73\nv_pk_add_f32 v[48:49],v[48:49],v[72:73] op_sel_hi:[1,0] neg_lo:[0,1] neg_hi:[0,1]\n" \
"v_pk_add_f32 v[50:51],v[50:51],v[72:73] op_sel_hi:[1,0] neg_lo:[0,1] neg_hi:[0,1]\nv_pk_mul_f32 v[68:69],v[48:49],v[48:49]\nv_lshlrev_b32_e64 v52,16,v226\nv_and_b32 v55,0xffff0000,v227\n" \
"v_pk_add_f32 v[52:53],v[52:53],v[72:73] op_sel_hi:[1,0] neg_lo:[0,1] neg_hi:[0,1]\nv_pk_add_f32 v[66:67],v[48:49],v[50:51]\nv_pk_fma_f32 v[68:69],v[50:51],v[50:51],v[68:69]\nv_lshlrev_b32 v54,16,v227\n" \
"s_waitcnt vmcnt(18)\nv_and_b32 v57,0xffff0000,v228\nv_pk_add_f32 v[54:55],v[54:55],v[72:73] op_sel_hi:[1,0] neg_lo:[0,1] neg_hi:[0,1]\nv_pk_add_f32 v[66:67],v[66:67],v[52:53]\n" \
"v_pk_fma_f32 v[68:69],v[52:53],v[52:53],v[68:69]\nv_lshlrev_b32_e64 v56,16,v228\nv_and_b32 v59,0xffff0000,v229\nv_pk_add_f32 v[56:57],v[56:57],v[72:73] op_sel_hi:[1,0] neg_lo:[0,1] neg_hi:[0,1]\n" \
"v_pk_add_f32 v[66:67],v[66:67],v[54:55]\nv_pk_fma_f32 v[68:69],v[54:55],v[54:55],v[68:69]\nv_lshlrev_b32_e64 v58,16,v229\nv_and_b32 v61,0xffff0000,v230\n" \
"v_pk_add_f32 v[58:59],v[58:59],v[72:73] op_sel_hi:[1,0] neg_lo:[0,1] neg_hi:[0,1]\nv_pk_add_f32 v[66:67],v[66:67],v[56:57]\nv_pk_fma_f32 v[68:69],v[56:57],v[56:57],v[68:69]\nv_lshlrev_b32_e64 v60,16,v230\n" \
"v_and_b32 v63,0xffff0000,v231\nv_pk_add_f32 v[60:61],v[60:61],v[72:73] op_sel_hi:[1,0] neg_lo:[0,1] neg_hi:[0,1]\nv_pk_add_f32 v[66:67],v[66:67],v[58:59]\nv_pk_fma_f32 v[68:69],v[58:59],v[58:59],v[68:69]\n" \
"v_lshlrev_b32 v62,16,v231\ns_waitcnt vmcnt(17)\nv_and_b32 v65,0xffff0000,v232\nv_pk_add_f32 v[62:63],v[62:63],v[72:73] op_sel_hi:[1,0] neg_lo:[0,1] neg_hi:[0,1]\n" \
"v_pk_add_f32 v[66:67],v[66:67],v[60:61]\nv_pk_fma_f32 v[68:69],v[60:61],v[60:61],v[68:69]\nv_lshlrev_b32_e64 v64,16,v232\nv_pk_add_f32 v[64:65],v[64:65],v[72:73] op_sel_hi:[1,0] neg_lo:[0,1] neg_hi:[0,1]\n" \
"v_pk_add_f32 v[66:67],v[66:67],v[62:63]\nv_pk_fma_f32 v[68:69],v[62:63],v[62:63],v[68:69]\nv_pk_add_f32 v[66:67],v[66:67],v[64:65]\nv_pk_fma_f32 v[68:69],v[64:65],v[64:65],v[68:69]\n" \
"v_add_f32 v66,v66,v67\nv_add_f32 v68,v68,v69\ns_nop 1\nv_permlane16_swap_b32 v66,v68\n" \
"v_add_f32 v66,v66,v68\ns_nop 1\nv_add_f32_dpp v66,v66,v66 quad_perm:[1,0,3,2] row_mask:0xf bank_mask:0xf bound_ctrl:1\ns_nop 1\n" \
"v_add_f32_dpp v66,v66,v66 quad_perm:[2,3,0,1] row_mask:0xf bank_mask:0xf bound_ctrl:1\ns_nop 1\nv_add_f32_dpp v66,v66,v66 row_half_mirror row_mask:0xf bank_mask:0xf bound_ctrl:1\ns_nop 1\n" \
"v_add_f32_dpp v66,v66,v66 row_mirror row_mask:0xf bank_mask:0xf bound_ctrl:1\nv_mul_f32 v70,0x3ae38e39,v66\nv_mul_f32 v68,0x3ae38e39,v66\ns_nop 1\n" \
"v_permlane16_swap_b32 v70,v68\nv_fma_f32 v68,-v70,v70,v68\nv_max_f32 v68,0,v68\nv_add_f32 v68,0x3727c5ac,v68\n" \
"v_rsq_f32 v66,v68\nv_add_f32 v250,v70,v72\nv_mul_f32 v67,-v70,v66\nv_pk_fma_f32 v[48:49],v[48:49],v[66:67],v[66:67] op_sel:[0,0,1] op_sel_hi:[1,0,1]\n" \
"v_pk_fma_f32 v[50:51],v[50:51],v[66:67],v[66:67] op_sel:[0,0,1] op_sel_hi:[1,0,1]\nv_cvt_pk_f16_f32 v80,v48,v49\nv_cvt_pk_f16_f32 v81,v50,v51\nv_pk_fma_f32 v[52:53],v[52:53],v[66:67],v[66:67] op_sel:[0,0,1] op_sel_hi:[1,0,1]\n" \
"v_pk_fma_f32 v[54:55],v[54:55],v[66:67],v[66:67] op_sel:[0,0,1] op_sel_hi:[1,0,1]\nds_write2_b32 v78,v80,v81 offset0:0 offset1:1\nv_cvt_pk_f16_f32 v82,v52,v53\nv_cvt_pk_f16_f32 v83,v54,v55\n" \
"v_pk_fma_f32 v[56:57],v[56:57],v[66:67],v[66:67] op_sel:[0,0,1] op_sel_hi:[1,0,1]\nv_pk_fma_f32 v[58:59],v[58:59],v[66:67],v[66:67] op_sel:[0,0,1] op_sel_hi:[1,0,1]\nds_write2_b32 v78,v82,v83 offset0:2 offset1:3\nv_cvt_pk_f16_f32 v84,v56,v57\n" \
"v_cvt_pk_f16_f32 v85,v58,v59\nv_pk_fma_f32 v[60:61],v[60:61],v[66:67],v[66:67] op_sel:[0,0,1] op_sel_hi:[1,0,1]\nv_pk_fma_f32 v[62:63],v[62:63],v[66:67],v[66:67] op_sel:[0,0,1] op_sel_hi:[1,0,1]\nds_write2_b32 v78,v84,v85 offset0:4 offset1:5\n" \
"v_cvt_pk_f16_f32 v86,v60,v61\nv_cvt_pk_f16_f32 v87,v62,v63\nv_pk_fma_f32 v[64:65],v[64:65],v[66:67],v[66:67] op_sel:[0,0,1] op_sel_hi:[1,0,1]\nds_write2_b32 v78,v86,v87 offset0:6 offset1:7\n" \
"v_cvt_pk_f16_f32 v88,v64,v65\nds_write_b32 v78,v88 offset:32\ns_waitcnt vmcnt(11)\ns_barrier\n" \
"ds_read_u16 v16,v246\nds_read_u16 v17,v246 offset:5216\nds_read_u16 v18,v246 offset:578\nds_read_u16 v19,v246 offset:5794\n" \
"ds_read_u16 v20,v246 offset:1156\nds_read_u16 v21,v246 offset:6372\nds_read_u16 v22,v246 offset:1734\nds_read_u16 v23,v246 offset:6950\n" \
"s_waitcnt vmcnt(8)\nv_lshrrev_b32 v96,1,v234\nds_read_u16 v24,v247\nv_lshrrev_b32 v97,1,v235\n" \
"v_lshrrev_b32 v98,1,v236\ns_waitcnt lgkmcnt(7)\nv_lshl_or_b32 v16,v17,16,v16\nv_lshrrev_b32 v99,1,v237\n" \
"ds_read_u16 v25,v247 offset:5216\ns_waitcnt vmcnt(7)\nv_lshrrev_b32 v100,1,v238\nv_lshrrev_b32 v101,1,v239\n" \
"v_lshrrev_b32 v102,1,v240\nv_lshrrev_b32 v103,1,v241\nds_read_u16 v26,v247 offset:578\nv_add_u32 v96,s72,v96\n" \
"v_add_u32 v97,s72,v97\ns_waitcnt lgkmcnt(7)\nv_lshl_or_b32 v17,v19,16,v18\nv_add_u32 v98,s72,v98\n" \
"ds_read_u16 v27,v247 offset:5794\nv_add_u32 v99,s72,v99\nv_add_u32 v100,s72,v100\nv_add_u32 v101,s72,v101\n" \
"v_add_u32 v102,s72,v102\nds_read_u16 v28,v247 offset:1156\nv_add_u32 v103,s72,v103\nv_pk_maximum3_f16 v124,v16,v17,v16\n" \
"s_waitcnt lgkmcnt(7)\nv_lshl_or_b32 v18,v21,16,v20\nv_pk_minimum3_f16 v125,v16,v17,v16\nds_read_u16 v29,v247 offset:6372\n" \
"ds_read_u16 v30,v247 offset:1734\ns_waitcnt lgkmcnt(7)\nv_lshl_or_b32 v19,v23,16,v22\nbuffer_store_dwordx4 v[16:19],v2,s[36:39],s64 offen sc1\n" \
"ds_read_u16 v31,v247 offset:6950\nds_read_u16 v104,v248\nv_pk_maximum3_f16 v124,v18,v19,v124\nv_pk_minimum3_f16 v125,v18,v19,v125\n" \
"s_waitcnt lgkmcnt(7)\nv_lshl_or_b32 v24,v25,16,v24\nds_read_u16 v105,v248 offset:5216\nds_read_u16 v106,v248 offset:578\n" \
"s_waitcnt lgkmcnt(7)\nv_lshl_or_b32 v25,v27,16,v26\nv_pk_maximum3_f16 v124,v24,v25,v124\nds_read_u16 v107,v248 offset:5794\n" \
"v_pk_minimum3_f16 v125,v24,v25,v125\nds_read_u16 v108,v248 offset:1156\ns_waitcnt lgkmcnt(7)\nv_lshl_or_b32 v26,v29,16,v28\n" \
"ds_read_u16 v109,v248 offset:6372\nds_read_u16 v110,v248 offset:1734\ns_waitcnt lgkmcnt(7)\nv_lshl_or_b32 v27,v31,16,v30\n" \
"buffer_store_dwordx4 v[24:27],v2,s[36:39],s65 offen sc1\nds_read_u16 v111,v248 offset:6950\nds_read_b128 v[116:119],v113 offset:20224\nv_pk_maximum3_f16 v124,v26,v27,v124\n" \
"v_pk_minimum3_f16 v125,v26,v27,v125\nds_read_b128 v[120:123],v113 offset:24320\ns_waitcnt lgkmcnt(8)\nv_lshl_or_b32 v104,v105,16,v104\n" \
"ds_read_b32 v16,v96\nds_read_b32 v24,v96 offset:1160\nds_read_b32 v17,v97\nds_read_b32 v25,v97 offset:1160\n" \
"s_waitcnt lgkmcnt(10)\nv_lshl_or_b32 v105,v107,16,v106\nds_read_b32 v18,v98\nv_pk_maximum3_f16 v124,v104,v105,v124\n" \
"ds_read_b32 v26,v98 offset:1160\nv_pk_minimum3_f16 v125,v104,v105,v125\nds_read_b32 v19,v99\nds_read_b32 v27,v99 offset:1160\n" \
"ds_read_b32 v20,v100\ns_waitcnt lgkmcnt(13)\nv_lshl_or_b32 v106,v109,16,v108\nds_read_b32 v28,v100 offset:1160\n" \
"ds_read_b32 v21,v101\nds_read_b32 v29,v101 offset:1160\ns_waitcnt lgkmcnt(14)\nv_lshl_or_b32 v107,v111,16,v110\n" \
"buffer_store_dwordx4 v[104:107],v114,s[36:39],s66 offen sc1\ns_waitcnt lgkmcnt(12)\nbuffer_store_dwordx4 v[116:119],v1,s[36:39],s67 offen sc1\nbuffer_store_dwordx4 v[120:123],v1,s[36:39],s67 offen offset:1024 sc1\n" \
"ds_read_b32 v22,v102\nds_read_b32 v30,v102 offset:1160\nds_read_b32 v23,v103\nds_read_b32 v31,v103 offset:1160\n" \
"v_pk_maximum3_f16 v124,v106,v107,v124\nv_and_b32 v124,0x7fff7fff,v124\nv_pk_minimum3_f16 v125,v106,v107,v125\nv_and_b32 v125,0x7fff7fff,v125\n" \
"v_pk_max_u16 v124,v124,v125\nv_lshlrev_b32 v125,16,v124\nv_max_u32 v124,v124,v125\ns_nop 1\n" \
"v_max_u32_dpp v124,v124,v124 quad_perm:[1,0,3,2] row_mask:0xf bank_mask:0xf bound_ctrl:1\ns_nop 1\nv_max_u32_dpp v124,v124,v124 quad_perm:[2,3,0,1] row_mask:0xf bank_mask:0xf bound_ctrl:1\ns_nop 1\n" \
"v_max_u32_dpp v124,v124,v124 row_half_mirror row_mask:0xf bank_mask:0xf bound_ctrl:1\ns_nop 1\nv_max_u32_dpp v124,v124,v124 row_mirror row_mask:0xf bank_mask:0xf bound_ctrl:1\ns_nop 0\n" \
"v_readlane_b32 s68,v124,0\nv_readlane_b32 s31,v124,16\nv_readlane_b32 s96,v124,32\nv_readlane_b32 s97,v124,48\n" \
"s_max_u32 s31,s31,s96\ns_max_u32 s68,s68,s97\ns_max_u32 s68,s68,s31\nv_mov_b32 v124,s68\n" \
"ds_write_b32 v251,v124 offset:32\nv_mov_b32 v114,161664\nv_and_b32 v80,15,v0\nv_bfe_u32 v81,v0,4,2\n" \
"v_cvt_f32_u32 v80,v80\nv_cvt_f32_u32 v82,s3\nv_fma_f32 v80,v80,4.0,v82\nv_cvt_f32_u32 v81,v81\n" \
"buffer_load_dwordx4 v[48:51],v2,s[36:39],s73 offen sc1\nv_mul_f32 v82,0xbc800000,v81\nv_cos_f32 v92,v82\nv_sin_f32 v93,v82\n" \
"v_mul_f32 v80,0xbc800000,v80\nv_mul_f32 v81,4.0,v81\nbuffer_load_dwordx4 v[60:63],v2,s[36:39],s75 offen sc1\ns_mov_b32 s64,0x3f7fb10f\n" \
"s_mov_b32 s65,0xbd48fb30\nv_mul_f32 v81,v81,v80\nv_pk_mul_f32 v[100:101],v[92:93],s[64:65] op_sel:[1,1] op_sel_hi:[0,1] neg_lo:[1,0]\nv_fmamk_f32 v87,v80,0x40000000,v81\n" \
"v_pk_fma_f32 v[94:95],v[92:93],s[64:65],v[100:101] op_sel_hi:[1,0,1]\nv_cos_f32_e64 v86,v87\ns_mov_b32 s64,0x3f7fec43\ns_mov_b32 s65,0xbcc90ab0\n" \
"v_fmamk_f32 v89,v80,0x3f800000,v81\nv_fmamk_f32 v91,v80,0x40400000,v81\nv_pk_mul_f32 v[100:101],v[92:93],s[64:65] op_sel:[1,1] op_sel_hi:[0,1] neg_lo:[1,0]\nbuffer_load_dwordx4 v[52:55],v2,s[36:39],s74 offen sc1\n" \
"v_cos_f32 v88,v89\nv_cos_f32 v90,v91\nv_pk_fma_f32 v[96:97],v[92:93],s[64:65],v[100:101] op_sel_hi:[1,0,1]\nv_cos_f32 v84,v81\n" \
"v_sin_f32 v85,v81\nv_sin_f32 v87,v87\ns_mov_b32 s64,0x3f7f4e6d\ns_mov_b32 s65,0xbd96a905\n" \
"v_pk_mul_f32 v[100:101],v[92:93],s[64:65] op_sel:[1,1] op_sel_hi:[0,1] neg_lo:[1,0]\nv_sin_f32 v89,v89\nv_sin_f32_e64 v91,v91\nv_pk_fma_f32 v[98:99],v[92:93],s[64:65],v[100:101] op_sel_hi:[1,0,1]\n" \
"buffer_load_dwordx4 v[64:67],v2,s[36:39],s76 offen sc1\nv_cvt_pk_f16_f32 v160,v84,-v85\nv_cvt_pk_f16_f32 v164,v85,v84\nv_cvt_pk_f16_f32 v161,v86,-v87\n" \
"v_cvt_pk_f16_f32 v165,v87,v86\nv_pk_mul_f32 v[100:101],v[84:85],v[92:93] op_sel:[1,1] op_sel_hi:[0,1] neg_lo:[1,0]\nv_pk_mul_f32 v[102:103],v[86:87],v[94:95] op_sel:[1,1] op_sel_hi:[0,1] neg_lo:[1,0]\nv_pk_fma_f32 v[84:85],v[84:85],v[92:93],v[100:101] op_sel_hi:[1,0,1]\n" \
"v_pk_fma_f32 v[86:87],v[86:87],v[94:95],v[102:103] op_sel_hi:[1,0,1]\nv_cvt_pk_f16_f32 v162,v88,-v89\nv_cvt_pk_f16_f32 v166,v89,v88\nv_cvt_pk_f16_f32 v163,v90,-v91\n" \
"v_cvt_pk_f16_f32 v167,v91,v90\nv_pk_mul_f32 v[100:101],v[88:89],v[96:97] op_sel:[1,1] op_sel_hi:[0,1] neg_lo:[1,0]\nv_pk_mul_f32 v[102:103],v[90:91],v[98:99] op_sel:[1,1] op_sel_hi:[0,1] neg_lo:[1,0]\nv_pk_fma_f32 v[88:89],v[88:89],v[96:97],v[100:101] op_sel_hi:[1,0,1]\n" \
"v_pk_fma_f32 v[90:91],v[90:91],v[98:99],v[102:103] op_sel_hi:[1,0,1]\nbuffer_load_dwordx4 v[56:59],v1,s[36:39],s77 offen sc1\nv_cvt_pk_f16_f32 v168,v84,-v85\nv_cvt_pk_f16_f32 v172,v85,v84\n" \
"v_cvt_pk_f16_f32 v169,v86,-v87\nv_cvt_pk_f16_f32 v173,v87,v86\nv_pk_mul_f32 v[100:101],v[84:85],v[92:93] op_sel:[1,1] op_sel_hi:[0,1] neg_lo:[1,0]\nv_pk_mul_f32 v[102:103],v[86:87],v[94:95] op_sel:[1,1] op_sel_hi:[0,1] neg_lo:[1,0]\n" \
"v_pk_fma_f32 v[84:85],v[84:85],v[92:93],v[100:101] op_sel_hi:[1,0,1]\nv_pk_fma_f32 v[86:87],v[86:87],v[94:95],v[102:103] op_sel_hi:[1,0,1]\nv_cvt_pk_f16_f32 v170,v88,-v89\nv_cvt_pk_f16_f32 v174,v89,v88\n" \
"v_cvt_pk_f16_f32 v171,v90,-v91\nv_cvt_pk_f16_f32 v175,v91,v90\nv_pk_mul_f32 v[100:101],v[88:89],v[96:97] op_sel:[1,1] op_sel_hi:[0,1] neg_lo:[1,0]\nv_pk_mul_f32 v[102:103],v[90:91],v[98:99] op_sel:[1,1] op_sel_hi:[0,1] neg_lo:[1,0]\n" \
"s_waitcnt vmcnt(5) lgkmcnt(0)\ns_barrier\nds_read_b128 v[116:119],v114 offset:32\ns_lshl3_add_u32 s80,s2,4294615040\n" \
"v_pk_fma_f32 v[88:89],v[88:89],v[96:97],v[100:101] op_sel_hi:[1,0,1]\nv_pk_fma_f32 v[90:91],v[90:91],v[98:99],v[102:103] op_sel_hi:[1,0,1]\nbuffer_load_dwordx4 v[68:71],v1,s[36:39],s78 offen sc1\nv_cvt_pk_f16_f32 v176,v84,-v85\n" \
"v_cvt_pk_f16_f32 v180,v85,v84\nv_cvt_pk_f16_f32 v177,v86,-v87\nv_cvt_pk_f16_f32 v181,v87,v86\nv_pk_mul_f32 v[100:101],v[84:85],v[92:93] op_sel:[1,1] op_sel_hi:[0,1] neg_lo:[1,0]\n" \
"v_pk_mul_f32 v[102:103],v[86:87],v[94:95] op_sel:[1,1] op_sel_hi:[0,1] neg_lo:[1,0]\nv_and_b32 v113,15,v0\ns_mov_b32 s79,0xffffff\nv_pk_fma_f32 v[84:85],v[84:85],v[92:93],v[100:101] op_sel_hi:[1,0,1]\n" \
"v_pk_fma_f32 v[86:87],v[86:87],v[94:95],v[102:103] op_sel_hi:[1,0,1]\nv_bfrev_b32 v115,1\ns_add_u32 s70,s35,s88\nv_cvt_pk_f16_f32 v178,v88,-v89\n" \
"v_cvt_pk_f16_f32 v182,v89,v88\nv_cvt_pk_f16_f32 v179,v90,-v91\nv_cvt_pk_f16_f32 v183,v91,v90\nv_pk_mul_f32 v[100:101],v[88:89],v[96:97] op_sel:[1,1] op_sel_hi:[0,1] neg_lo:[1,0]\n" \
"s_waitcnt lgkmcnt(0)\nv_max3_u32 v116,v116,v117,v118\nv_max_u32 v116,v116,v119\nv_and_b32 v119,0xf8,v0\n" \
"v_cmp_ne_u32 vcc,192,v119\nv_lshlrev_b32 v118,11,v0\nv_add_u32 v118,s80,v118\nv_bfrev_b32 v119,1\n" \
"v_cndmask_b32 v118,v118,v119,vcc\nv_lshlrev_b32 v116,1,v116\nv_mov_b32 v117,s29\nv_cmp_eq_u32 vcc,0,v113\n" \
"v_bfi_b32 v117,s79,v117,v116\nv_pk_mul_f32 v[102:103],v[90:91],v[98:99] op_sel:[1,1] op_sel_hi:[0,1] neg_lo:[1,0]\nv_cndmask_b32 v115,v115,v3,vcc\ns_add_u32 s72,s70,8192\n" \
"v_mov_b32 v116,s28\nv_pk_fma_f32 v[88:89],v[88:89],v[96:97],v[100:101] op_sel_hi:[1,0,1]\nv_pk_fma_f32 v[90:91],v[90:91],v[98:99],v[102:103] op_sel_hi:[1,0,1]\nv_cmp_gt_u32 vcc,64,v0\n" \
"s_cmp_lt_u32 s3,2\ns_cselect_b32 s72,s72,2147483648\nbuffer_store_dwordx2 v[116:117],v118,s[36:39],0 offen sc1\nv_add_u32 v112,28416,v1\n" \
"v_cvt_pk_f16_f32 v184,v84,-v85\nv_cvt_pk_f16_f32 v188,v85,v84\nv_cvt_pk_f16_f32 v185,v86,-v87\nv_cvt_pk_f16_f32 v189,v87,v86\n" \
"v_cvt_pk_f16_f32 v186,v88,-v89\nv_cvt_pk_f16_f32 v190,v89,v88\nv_cvt_pk_f16_f32 v187,v90,-v91\nv_cvt_pk_f16_f32 v191,v91,v90\n" \
"v_cndmask_b32_e64 v115,v115,v3,vcc\ns_add_u32 s71,s70,4096\nds_read_b128 v[192:195],v112\nds_read_b128 v[196:199],v112 offset:1024\n" \
"ds_read_b128 v[200:203],v112 offset:2048\nds_read_b128 v[204:207],v112 offset:3072\nds_read_b128 v[208:211],v112 offset:4096\ns_lshl1_add_u32 s95,s33,s3\n" \
"s_add_u32 s95,s95,74\ns_cmp_lt_u32 s3,2\ns_cselect_b32 s95,s95,256\nds_read_b128 v[212:215],v112 offset:5120\n" \
"s_and_b32 s100,s95,31\ns_lshl_b32 s100,s100,3\nds_read_b128 v[32:35],v112 offset:6144\ns_cmp_lt_u32 s95,256\n" \
"s_cselect_b32 s95,s95,0x40000\nds_read_b128 v[36:39],v112 offset:7168\ns_lshr_b32 s95,s95,5\ns_add_u32 s95,s95,s100\n" \
"s_lshl_b32 s95,s95,6\ns_add_u32 s95,s95,s32\ns_mul_i32 s95,s95,1152\ns_mov_b32 m0,162176\n" \
"s_lshl_b32 s101,s33,7\nbuffer_load_dword v233,s[8:11],s95 offen lds\ns_sub_u32 s101,s101,1280\nv_add_u32 v111,s101,v0\n" \
"v_mul_u32_u24 v110,52429,v111\nv_lshrrev_b32 v110,18,v110\nv_lshlrev_b32 v111,7,v111\ns_mov_b32 s101,6272\n" \
"v_mad_u32_u24 v111,v110,s101,v111\ns_mul_i32 s31,s32,576\ns_add_u32 s31,s31,2304\ns_andn2_b32 s31,s31,127\n" \
"s_cmp_gt_u32 s3,1\ns_cselect_b32 s31,s31,2147483648\ns_mov_b32 s26,3981312\ns_mov_b32 s27,0x20000\n" \
"s_mov_b32 m0,162176\ns_nop 0\nbuffer_load_dword v111,s[24:27],s31 offen lds\ns_waitcnt vmcnt(8)\n" \
"v_cmp_ne_u64 vcc,s[28:29],v[48:49]\ns_waitcnt vmcnt(6)\nv_cmp_ne_u64 s[84:85],s[28:29],v[52:53]\ns_waitcnt vmcnt(4)\n" \
"v_cmp_ne_u64 s[82:83],s[28:29],v[56:57]\ns_or_b64 vcc,vcc,s[84:85]\nv_cmp_ne_u64 s[84:85],s[28:29],v[60:61]\ns_or_b64 vcc,vcc,s[82:83]\n" \
"v_cmp_ne_u64 s[82:83],s[28:29],v[64:65]\ns_or_b64 vcc,vcc,s[84:85]\ns_waitcnt vmcnt(3)\nv_cmp_ne_u64 s[84:85],s[28:29],v[68:69]\n" \
"s_or_b64 vcc,vcc,s[82:83]\ns_or_b64 vcc,vcc,s[84:85]\ns_cbranch_vccnz .LsmC_%=\n.LbmC_%=:\n" \
"v_pk_add_f32 v[50:51],v[50:51],v[62:63]\nv_pk_add_f32 v[54:55],v[54:55],v[66:67]\nv_pk_mul_f32 v[62:63],v[50:51],2.0 op_sel_hi:[1,0]\nv_pk_add_f32 v[58:59],v[58:59],v[70:71]\n" \
"v_max3_f32 v112,|v62|,|v63|,0\nv_pk_fma_f32 v[66:67],v[54:55],2.0,v[74:75] op_sel_hi:[1,0,0]\nv_max3_f32 v112,|v66|,|v67|,v112\nv_pk_fma_f32 v[70:71],v[58:59],2.0,2.0 op_sel_hi:[1,0,0]\n" \
"v_max3_f32 v112,|v70|,|v71|,v112\nv_cmp_lt_f32 s[82:83],s81,v112\nv_cmp_ngt_f32 vcc,0x41000000,v112\ns_cmp_lg_u64 s[82:83],0\n" \
"s_cselect_b32 s82,2,0\nv_cvt_pk_f16_f32 v62,v62,v63\ns_cmp_lg_u64 vcc,0\ns_addc_u32 s82,s82,0\n" \
"ds_write_b32 v252,v62 offset:62848\nv_cvt_pk_f16_f32 v66,v66,v67\nds_write_b32 v253,v66 offset:62848\nv_cvt_pk_f16_f32 v70,v70,v71\n" \
"v_mov_b32 v113,s82\nds_write_b32 v254,v70 offset:62848\nds_write_b32 v251,v113\ns_waitcnt lgkmcnt(0)\n" \
"s_barrier\nds_read_b128 v[124:127],v114\nds_read_b64 v[96:97],v234 offset:62848\nds_read_b64 v[98:99],v235 offset:62848\n" \
"ds_read_b64 v[100:101],v236 offset:62848\nds_read_b64 v[102:103],v237 offset:62848\nds_read_b64 v[104:105],v238 offset:62848\nds_read_b64 v[106:107],v239 offset:62848\n" \
"ds_read_b64 v[108:109],v240 offset:62848\nds_read_b64 v[110:111],v241 offset:62848\ns_waitcnt lgkmcnt(7)\nv_or3_b32 v124,v124,v125,v126\n" \
"v_or_b32 v124,v124,v127\nv_cmp_ne_u32 vcc,2,v124\ns_nop 0\ns_cbranch_vccnz .LsxC_%=\n" \
".LbxC_%=:\nv_pk_fma_f16 v80,v16,v96,v97\ns_waitcnt lgkmcnt(4)\nv_pk_fma_f16 v81,v17,v98,v99\n" \
"v_pk_fma_f16 v82,v18,v100,v101\nv_pk_fma_f16 v83,v19,v102,v103\nv_pk_fma_f16 v88,v24,v96,v97\ns_waitcnt lgkmcnt(2)\n" \
"v_pk_fma_f16 v84,v20,v104,v105\nv_pk_fma_f16 v85,v21,v106,v107\nv_pk_fma_f16 v89,v25,v98,v99\ns_waitcnt lgkmcnt(0)\n" \
"v_pk_fma_f16 v86,v22,v108,v109\nv_pk_fma_f16 v90,v26,v100,v101\nv_pk_fma_f16 v87,v23,v110,v111\nv_pk_fma_f16 v91,v27,v102,v103\n" \
"v_pk_fma_f16 v92,v28,v104,v105\nv_pk_fma_f16 v93,v29,v106,v107\nv_pk_fma_f16 v94,v30,v108,v109\ns_add_u32 s96,s92,0\n" \
"s_add_u32 m0,s57,0\ns_add_u32 s97,s92,9437184\nbuffer_load_dwordx4 v4,s[8:11],s96 offen lds\nv_pk_fma_f16 v95,v31,v110,v111\n" \
"v_mfma_f32_16x16x32_f16 v[48:51],v[80:83],v[192:195],0\ns_add_u32 s95,s92,73728\ns_add_u32 s100,s92,9510912\nv_mfma_f32_16x16x32_f16 v[52:55],v[80:83],v[196:199],0\n" \
"s_add_u32 s101,s92,147456\ns_mul_i32 s76,s32,18\nbuffer_load_dwordx4 v5,s[8:11],s96 offen offset:1024 lds\nv_mfma_f32_16x16x32_f16 v[56:59],v[84:87],v[32:35],0\n" \
"s_add_u32 s76,s76,s33\ns_sub_u32 s76,s76,14\nv_mfma_f32_16x16x32_f16 v[60:63],v[84:87],v[36:39],0\ns_cmp_lt_u32 s33,14\n" \
"s_cselect_b32 s76,144,s76\nv_mfma_f32_16x16x32_f16 v[64:67],v[88:91],v[192:195],0\nbuffer_load_dwordx4 v6,s[8:11],s96 offen offset:2048 lds\nv_mfma_f32_16x16x32_f16 v[68:71],v[88:91],v[196:199],0\n" \
"v_pk_add_f32 v[56:57],v[48:49],v[56:57] neg_lo:[0,1] neg_hi:[0,1]\nv_sub_f32 v58,v50,v58\nv_mfma_f32_16x16x32_f16 v[72:75],v[92:95],v[32:35],0\nv_pk_add_f32 v[60:61],v[52:53],v[60:61] neg_lo:[0,1] neg_hi:[0,1]\n" \
"v_sub_f32 v62,v54,v62\nv_mfma_f32_16x16x32_f16 v[76:79],v[92:95],v[36:39],0\nbuffer_load_dwordx4 v7,s[8:11],s96 offen offset:3072 lds\nv_pk_fma_f32 v[48:49],v[48:49],2.0,v[56:57] op_sel_hi:[1,0,1] neg_lo:[0,0,1] neg_hi:[0,0,1]\n" \
"v_fma_f32 v50,v50,2.0,-v58\nv_pk_fma_f32 v[52:53],v[52:53],2.0,v[60:61] op_sel_hi:[1,0,1] neg_lo:[0,0,1] neg_hi:[0,0,1]\nv_fma_f32 v54,v54,2.0,-v62\nv_pk_add_f32 v[72:73],v[64:65],v[72:73] neg_lo:[0,1] neg_hi:[0,1]\n" \
"v_sub_f32 v74,v66,v74\nv_pk_fma_f32 v[64:65],v[64:65],2.0,v[72:73] op_sel_hi:[1,0,1] neg_lo:[0,0,1] neg_hi:[0,0,1]\nv_pk_add_f32 v[76:77],v[68:69],v[76:77] neg_lo:[0,1] neg_hi:[0,1]\ns_add_u32 m0,s57,4096\n" \
"v_sub_f32 v78,v70,v78\nbuffer_load_dwordx4 v4,s[8:11],s97 offen lds\nv_fma_f32 v66,v66,2.0,-v74\nv_pk_fma_f32 v[68:69],v[68:69],2.0,v[76:77] op_sel_hi:[1,0,1] neg_lo:[0,0,1] neg_hi:[0,0,1]\n" \
"v_fma_f32 v70,v70,2.0,-v78\nv_cvt_pk_f16_f32 v16,v48,v52\nv_cvt_pk_f16_f32 v17,v49,v53\nv_cvt_pk_f16_f32 v18,v50,v54\n" \
"v_cvt_pk_f16_f32 v24,v64,v68\nbuffer_load_dwordx4 v5,s[8:11],s97 offen offset:1024 lds\nv_cvt_pk_f16_f32 v25,v65,v69\nv_cvt_pk_f16_f32 v26,v66,v70\n" \
"v_mfma_f32_16x16x32_f16 v[48:51],v[200:203],v[16:19],0\nv_cvt_pk_f16_f32 v20,v56,v60\nv_cvt_pk_f16_f32 v21,v57,v61\nv_mfma_f32_16x16x32_f16 v[52:55],v[204:207],v[16:19],0\n" \
"buffer_load_dwordx4 v6,s[8:11],s97 offen offset:2048 lds\nv_mfma_f32_16x16x32_f16 v[64:67],v[200:203],v[24:27],0\nv_cvt_pk_f16_f32 v22,v58,v62\nv_cvt_pk_f16_f32 v28,v72,v76\n" \
"v_mfma_f32_16x16x32_f16 v[68:71],v[204:207],v[24:27],0\nv_cvt_pk_f16_f32 v29,v73,v77\nv_cvt_pk_f16_f32 v30,v74,v78\nbuffer_load_dwordx4 v7,s[8:11],s97 offen offset:3072 lds\n" \
"v_mfma_f32_16x16x32_f16 v[56:59],v[200:203],v[20:23],0\nv_cvt_pk_f16_f32 v80,v48,v52\nv_cvt_pk_f16_f32 v82,v49,v53\nv_mfma_f32_16x16x32_f16 v[60:63],v[204:207],v[20:23],0\n" \
"v_cvt_pk_f16_f32 v84,v50,v54\nv_cvt_pk_f16_f32 v81,v64,v68\ns_add_u32 m0,s57,8192\nv_mfma_f32_16x16x32_f16 v[72:75],v[200:203],v[28:31],0\n" \
"buffer_load_dwordx4 v4,s[8:11],s95 offen lds\nv_mfma_f32_16x16x32_f16 v[76:79],v[204:207],v[28:31],0\nds_write_b64 v255,v[80:81]\nv_cvt_pk_f16_f32 v83,v65,v69\n" \
"ds_write_b64 v255,v[82:83] offset:512\nv_cvt_pk_f16_f32 v85,v66,v70\nbuffer_load_dwordx4 v5,s[8:11],s95 offen offset:1024 lds\nds_write_b64 v255,v[84:85] offset:1024\n" \
"v_cvt_pk_f16_f32 v86,v56,v60\nv_cvt_pk_f16_f32 v88,v57,v61\nv_cvt_pk_f16_f32 v87,v72,v76\nds_write_b64 v255,v[86:87] offset:256\n" \
"buffer_load_dwordx4 v6,s[8:11],s95 offen offset:2048 lds\nv_cvt_pk_f16_f32 v89,v73,v77\nds_write_b64 v255,v[88:89] offset:768\nv_cvt_pk_f16_f32 v90,v58,v62\n" \
"v_cvt_pk_f16_f32 v91,v74,v78\nds_write_b64 v255,v[90:91] offset:1280\nbuffer_load_dwordx4 v7,s[8:11],s95 offen offset:3072 lds\ns_add_u32 m0,s57,12288\n" \
"v_lshrrev_b32 v124,2,v0\nv_mul_u32_u24 v124,9344,v124\nv_and_b32 v125,48,v2\nv_add_u32 v124,v124,v125\n" \
"v_xor_b32 v125,32,v124\ns_lshl_b32 s77,s76,6\ns_add_u32 s66,s77,2916352\nbuffer_load_dwordx4 v4,s[8:11],s100 offen lds\n" \
"s_waitcnt lgkmcnt(0)\ns_barrier\nds_read_b128 v[16:19],v243\nds_read_b128 v[20:23],v244\n" \
"ds_read_b128 v[24:27],v245\ns_add_u32 s67,s77,3514368\ns_add_u32 s68,s77,4112384\ns_add_u32 s69,s77,4710400\n" \
"buffer_load_dwordx4 v5,s[8:11],s100 offen offset:1024 lds\nbuffer_load_dwordx4 v6,s[8:11],s100 offen offset:2048 lds\ns_waitcnt lgkmcnt(2)\nv_mfma_f32_16x16x32_f16 v[48:51],v[40:43],v[16:19],0\n" \
"v_mfma_f32_16x16x32_f16 v[52:55],v[44:47],v[16:19],0\nbuffer_load_dwordx4 v7,s[8:11],s100 offen offset:3072 lds\nbuffer_load_dwordx4 v[104:107],v1,s[36:39],s7 offen sc1\nbuffer_load_dwordx4 v[108:111],v1,s[36:39],s7 offen offset:1024 sc1\n" \
"s_add_u32 m0,s57,16384\ns_waitcnt lgkmcnt(0)\nv_mfma_f32_16x16x32_f16 v[56:59],v[40:43],v[20:23],0\nv_mfma_f32_16x16x32_f16 v[60:63],v[44:47],v[20:23],0\n" \
"v_mfma_f32_16x16x32_f16 v[64:67],v[40:43],v[24:27],0\nv_cvt_pk_f16_f32 v80,v48,v52\nv_mfma_f32_16x16x32_f16 v[68:71],v[44:47],v[24:27],0\nbuffer_load_dwordx4 v4,s[8:11],s101 offen lds\n" \
"v_cvt_pk_f16_f32 v81,v49,v53\nv_cvt_pk_f16_f32 v82,v50,v54\nv_cvt_pk_f16_f32 v83,v51,v55\nv_and_b32 v80,-2,v80\n" \
"v_cvt_pk_f16_f32 v84,v56,v60\nv_cvt_pk_f16_f32 v85,v57,v61\nv_cvt_pk_f16_f32 v86,v58,v62\nv_cvt_pk_f16_f32 v88,v64,v68\n" \
"buffer_load_dwordx4 v5,s[8:11],s101 offen offset:1024 lds\nv_cvt_pk_f16_f32 v87,v59,v63\nv_cvt_pk_f16_f32 v89,v65,v69\nv_cvt_pk_f16_f32 v90,v66,v70\n" \
"v_cvt_pk_f16_f32 v91,v67,v71\nv_and_b32 v84,-2,v84\nv_and_b32 v88,-2,v88\nbuffer_load_dwordx4 v6,s[8:11],s101 offen offset:2048 lds\n" \
"s_waitcnt vmcnt(4)\nv_xor_b32 v116,s29,v105\nv_xor_b32 v117,s29,v107\ns_waitcnt vmcnt(3)\n" \
"v_xor_b32 v118,s29,v109\nv_or3_b32 v116,v116,v117,v118\nbuffer_load_dwordx4 v7,s[8:11],s101 offen offset:3072 lds\nv_xor_b32 v119,s29,v111\n" \
"v_xor_b32 v117,s28,v104\nv_or_b32 v116,v116,v119\nv_lshl_or_b32 v116,v116,8,v117\nv_xor_b32 v118,s28,v106\n" \
"v_xor_b32 v119,s28,v108\nv_or3_b32 v116,v116,v118,v119\nv_xor_b32 v117,s28,v110\nv_or_b32 v116,v116,v117\n" \
"v_cmp_ne_u32 vcc,0,v116\ns_nop 0\ns_cbranch_vccnz .Lsw_%=\nbuffer_store_dwordx4 v[80:83],v3,s[36:39],s70 offen sc1\n" \
"buffer_store_dwordx4 v[84:87],v3,s[36:39],s71 offen sc1\nbuffer_store_dwordx4 v[88:91],v115,s[36:39],s72 offen sc1\ns_add_u32 m0,s88,16384\ns_nop 0\n" \
"buffer_load_dwordx4 v124,s[36:39],s66 offen sc1 lds\ns_add_u32 m0,s88,20480\ns_nop 0\nbuffer_load_dwordx4 v125,s[36:39],s67 offen sc1 lds\n" \
"s_add_u32 m0,s88,24576\ns_nop 0\nbuffer_load_dwordx4 v124,s[36:39],s68 offen sc1 lds\ns_add_u32 m0,s88,28672\n" \
"s_nop 0\nbuffer_load_dwordx4 v125,s[36:39],s69 offen sc1 lds\n.Lbw_%=:\ns_cmp_lt_u32 s3,2\n" \
"s_cselect_b32 s18,s72,s70\ns_mov_b32 s12,s88\ns_mov_b32 s13,s89\ns_mov_b32 s14,s87\n" \
"s_mov_b32 s16,s70\ns_mov_b32 s17,s71\nv_bfe_u32 v86,v0,4,2\nv_and_b32 v88,1,v86\n" \
"v_bfe_u32 v85,v0,3,1\nv_and_b32 v84,7,v0\nv_lshlrev_b32 v88,3,v88\nv_lshl_add_u32 v87,v86,6,v85\n" \
"s_lshl_b32 s84,s3,2\nv_xor_b32 v89,v84,v88\nv_add_u32 v87,s84,v87\nv_lshlrev_b32_e64 v89,2,v89\n" \
"v_lshl_add_u32 v80,v87,6,v89\nv_and_b32 v87,0xf0,v1\nv_add_u32 v80,16384,v80\nv_lshl_add_u32 v246,v86,12,v87\n" \
"v_sub_u32 v82,46076,v80\nv_lshl_add_u32 v87,v84,1,v85\nv_xor_b32 v82,32,v82\nv_lshlrev_b32 v87,4,v87\n" \
"v_add_u32 v81,128,v80\nv_subrev_u32 v83,128,v82\nv_lshl_add_u32 v87,v85,10,v87\nv_lshl_add_u32 v87,v86,8,v87\n" \
"s_lshl_b32 s86,s3,12\nv_add_u32 v252,s86,v87\nv_lshlrev_b32 v87,3,v85\nv_lshl_add_u32 v87,v84,4,v87\n" \
"s_lshr_b32 s84,s76,5\nv_lshl_add_u32 v87,v86,12,v87\ns_lshl_b32 s85,s3,8\ns_add_u32 s84,s84,1\n" \
"v_add_u32 v246,s85,v246\nv_add_u32 v248,s85,v87\ns_cmp_lt_u32 s76,128\ns_cselect_b32 s84,s84,0\n" \
"v_max3_u32 v126,v105,v107,v109\ns_and_b32 s85,s76,31\nv_max_u32 v126,v126,v111\ns_lshl_b32 s84,s84,6\n" \
"s_mul_i32 s85,s85,225\nv_max_u32_dpp v126,v126,v126 quad_perm:[1,0,3,2] row_mask:0xf bank_mask:0xf bound_ctrl:1\ns_add_u32 s84,s84,s85\ns_mul_i32 s85,s84,58255\n" \
"v_max_u32_dpp v126,v126,v126 quad_perm:[2,3,0,1] row_mask:0xf bank_mask:0xf bound_ctrl:1\ns_lshr_b32 s85,s85,24\ns_mul_i32 s85,s85,288\nv_max_u32_dpp v126,v126,v126 row_half_mirror row_mask:0xf bank_mask:0xf bound_ctrl:1\n" \
"s_sub_u32 s84,s84,s85\nv_cvt_f32_u32 v92,s84\nv_max_u32_dpp v126,v126,v126 row_mirror row_mask:0xf bank_mask:0xf bound_ctrl:1\nv_mul_f32 v92,0x3ae38e39,v92\n" \
"v_readlane_b32 s94,v126,0\nv_readlane_b32 s31,v126,16\nv_readlane_b32 s96,v126,32\nv_readlane_b32 s97,v126,48\n" \
"s_max_u32 s31,s31,s96\ns_max_u32 s94,s94,s97\ns_max_u32 s94,s94,s31\nv_cos_f32 v90,v92\n" \
"s_lshr_b32 s94,s94,24\nv_bfe_u32 v87,v0,1,3\nv_and_b32 v88,1,v0\ns_max_u32 s94,s94,64\n" \
"v_lshl_add_u32 v87,v88,3,v87\ns_min_u32 s94,s94,250\nv_lshl_add_u32 v87,v86,4,v87\ns_sub_u32 s85,251,s94\n" \
"v_sin_f32 v92,v92\nv_lshlrev_b32 v87,4,v87\ns_cmp_eq_u32 s76,128\ns_subb_u32 s85,s85,0\n" \
"v_bfe_u32 v120,v0,4,2\nv_add_u32 v254,s86,v87\ns_mul_i32 s80,s3,37376\nv_mul_u32_u24 v120,598016,v120\n" \
"v_and_b32 v121,15,v0\nv_and_b32 v87,15,v0\ns_lshl_b32 s85,s85,23\ns_add_u32 s80,s80,s77\n" \
"v_lshl_add_u32 v120,v121,2,v120\nv_mul_u32_u24 v87,149504,v87\nv_fma_f32 v94,v92,s85,s85\ns_mov_b32 s79,0x34003400\n" \
"s_add_u32 s80,s80,524288\nv_add_u32 v121,149504,v120\nv_add_u32 v122,299008,v120\nv_add_u32 v123,448512,v120\n" \
"v_lshl_add_u32 v255,v86,4,v87\nv_pk_mul_f16 v112,v192,s79\nv_pk_mul_f16 v113,v193,s79\nv_pk_mul_f16 v114,v194,s79\n" \
"v_pk_mul_f16 v115,v195,s79\nv_pk_mul_f16 v116,v196,s79\nv_pk_mul_f16 v117,v197,s79\nv_pk_mul_f16 v118,v198,s79\n" \
"v_pk_mul_f16 v119,v199,s79\nv_fma_f32 v92,-v92,s85,s85\ns_add_u32 s81,s80,9344\ns_add_u32 s82,s80,18688\n" \
"s_add_u32 s83,s80,28032\nv_xor_b32 v247,16,v246\nv_xor_b32 v253,16,v252\nv_mul_f32 v90,s85,v90\n" \
"ds_read_b64_tr_b16 a[0:1],v8\nds_read_b64_tr_b16 a[4:5],v9\nds_read_b64_tr_b16 a[8:9],v10\nds_read_b64_tr_b16 a[12:13],v11\n" \
"ds_read_b64_tr_b16 a[16:17],v8 offset:2048\nds_read_b64_tr_b16 a[20:21],v9 offset:2048\nds_read_b64_tr_b16 a[24:25],v10 offset:2048\nds_read_b64_tr_b16 a[28:29],v11 offset:2048\n" \
"ds_read_b64_tr_b16 a[2:3],v8 offset:4096\nds_read_b64_tr_b16 a[6:7],v9 offset:4096\nds_read_b64_tr_b16 a[10:11],v10 offset:4096\nds_read_b64_tr_b16 a[14:15],v11 offset:4096\n" \
"ds_read_b64_tr_b16 a[18:19],v8 offset:6144\nds_read_b64_tr_b16 a[22:23],v9 offset:6144\nds_read_b64_tr_b16 a[26:27],v10 offset:6144\nds_read_b64_tr_b16 a[30:31],v11 offset:6144\n" \
"ds_read_b64_tr_b16 a[32:33],v8 offset:8192\nds_read_b64_tr_b16 a[36:37],v9 offset:8192\nds_read_b64_tr_b16 a[40:41],v10 offset:8192\nds_read_b64_tr_b16 a[44:45],v11 offset:8192\n" \
"ds_read_b64_tr_b16 a[48:49],v8 offset:10240\nds_read_b64_tr_b16 a[52:53],v9 offset:10240\nds_read_b64_tr_b16 a[56:57],v10 offset:10240\nds_read_b64_tr_b16 a[60:61],v11 offset:10240\n" \
"ds_read_b64_tr_b16 a[34:35],v8 offset:12288\nds_read_b64_tr_b16 a[38:39],v9 offset:12288\nds_read_b64_tr_b16 a[42:43],v10 offset:12288\nds_read_b64_tr_b16 a[46:47],v11 offset:12288\n" \
"ds_read_b64_tr_b16 a[50:51],v8 offset:14336\nds_read_b64_tr_b16 a[54:55],v9 offset:14336\nds_read_b64_tr_b16 a[58:59],v10 offset:14336\nds_read_b64_tr_b16 a[62:63],v11 offset:14336\n" \
"s_waitcnt vmcnt(0)\ns_barrier\nds_read2st64_b32 v[64:65],v80 offset0:0 offset1:4\nds_read2st64_b32 v[72:73],v82 offset0:12 offset1:8\n" \
"ds_read2st64_b32 v[66:67],v80 offset0:8 offset1:12\nds_read2st64_b32 v[74:75],v82 offset0:4 offset1:0\nds_read2st64_b32 v[68:69],v81 offset0:0 offset1:4\nds_read2st64_b32 v[76:77],v83 offset0:12 offset1:8\n" \
"ds_read2st64_b32 v[70:71],v81 offset0:8 offset1:12\nds_read2st64_b32 v[78:79],v83 offset0:4 offset1:0\ns_waitcnt lgkmcnt(7)\nv_and_b32 v20,0xffff0000,v64\n" \
"v_and_b32 v21,0xffff0000,v65\nv_lshlrev_b32 v16,16,v64\nv_lshlrev_b32 v17,16,v65\ns_waitcnt lgkmcnt(6)\n" \
"v_and_b32 v36,0xffff0000,v72\nv_and_b32 v37,0xffff0000,v73\nv_lshlrev_b32 v32,16,v72\ns_waitcnt lgkmcnt(5)\n" \
"v_and_b32 v22,0xffff0000,v66\nv_and_b32 v23,0xffff0000,v67\nv_lshlrev_b32 v33,16,v73\nv_lshlrev_b32 v18,16,v66\n" \
"s_waitcnt lgkmcnt(4)\nv_and_b32 v38,0xffff0000,v74\nv_and_b32 v39,0xffff0000,v75\nv_lshlrev_b32 v34,16,v74\n" \
"s_waitcnt lgkmcnt(3)\nv_and_b32 v44,0xffff0000,v68\nv_and_b32 v45,0xffff0000,v69\nv_lshlrev_b32 v19,16,v67\n" \
"s_waitcnt lgkmcnt(2)\nv_and_b32 v60,0xffff0000,v76\nv_and_b32 v61,0xffff0000,v77\nv_lshlrev_b32 v35,16,v75\n" \
"v_lshlrev_b32 v40,16,v68\ns_waitcnt lgkmcnt(1)\nv_and_b32 v46,0xffff0000,v70\nv_and_b32 v47,0xffff0000,v71\n" \
"v_lshlrev_b32 v56,16,v76\ns_waitcnt lgkmcnt(0)\nv_and_b32 v62,0xffff0000,v78\nv_and_b32 v63,0xffff0000,v79\n" \
"v_lshlrev_b32 v41,16,v69\nv_lshlrev_b32 v57,16,v77\nv_lshlrev_b32 v42,16,v70\nv_lshlrev_b32 v58,16,v78\n" \
"v_lshlrev_b32 v43,16,v71\nv_lshlrev_b32 v59,16,v79\ns_cmp_eq_u32 s76,128\ns_cbranch_scc1 .Lsz_%=\n" \
".Lbz_%=:\nv_pk_mul_f32 v[24:25],v[16:17],v[94:95] op_sel_hi:[1,0]\nv_pk_mul_f32 v[28:29],v[20:21],v[94:95] op_sel_hi:[1,0]\nv_pk_add_f32 v[64:65],v[20:21],v[36:37]\n" \
"v_pk_add_f32 v[66:67],v[16:17],v[32:33] neg_lo:[0,1] neg_hi:[0,1]\nv_pk_mul_f32 v[26:27],v[18:19],v[94:95] op_sel_hi:[1,0]\nv_pk_mul_f32 v[30:31],v[22:23],v[94:95] op_sel_hi:[1,0]\nv_pk_add_f32 v[68:69],v[22:23],v[38:39]\n" \
"v_pk_add_f32 v[70:71],v[18:19],v[34:35] neg_lo:[0,1] neg_hi:[0,1]\nv_pk_mul_f32 v[48:49],v[40:41],v[94:95] op_sel_hi:[1,0]\nv_pk_mul_f32 v[52:53],v[44:45],v[94:95] op_sel_hi:[1,0]\nv_pk_add_f32 v[72:73],v[44:45],v[60:61]\n" \
"v_pk_add_f32 v[74:75],v[40:41],v[56:57] neg_lo:[0,1] neg_hi:[0,1]\nv_pk_mul_f32 v[50:51],v[42:43],v[94:95] op_sel_hi:[1,0]\nv_pk_mul_f32 v[54:55],v[46:47],v[94:95] op_sel_hi:[1,0]\nv_pk_add_f32 v[76:77],v[46:47],v[62:63]\n" \
"v_pk_add_f32 v[78:79],v[42:43],v[58:59] neg_lo:[0,1] neg_hi:[0,1]\nv_pk_mul_f32 v[16:17],v[16:17],v[92:93] op_sel_hi:[1,0]\nv_pk_mul_f32 v[20:21],v[20:21],v[92:93] op_sel_hi:[1,0]\nv_pk_mul_f32 v[18:19],v[18:19],v[92:93] op_sel_hi:[1,0]\n" \
"v_pk_mul_f32 v[22:23],v[22:23],v[92:93] op_sel_hi:[1,0]\nbuffer_load_dword v96,v120,s[36:39],s80 offen sc1\nbuffer_load_dword v97,v121,s[36:39],s80 offen sc1\nbuffer_load_dword v98,v122,s[36:39],s80 offen sc1\n" \
"buffer_load_dword v99,v123,s[36:39],s80 offen sc1\nbuffer_load_dword v100,v120,s[36:39],s81 offen sc1\nbuffer_load_dword v101,v121,s[36:39],s81 offen sc1\nbuffer_load_dword v102,v122,s[36:39],s81 offen sc1\n" \
"buffer_load_dword v103,v123,s[36:39],s81 offen sc1\nbuffer_load_dword v104,v120,s[36:39],s82 offen sc1\nbuffer_load_dword v105,v121,s[36:39],s82 offen sc1\nbuffer_load_dword v106,v122,s[36:39],s82 offen sc1\n" \
"buffer_load_dword v107,v123,s[36:39],s82 offen sc1\nbuffer_load_dword v108,v120,s[36:39],s83 offen sc1\nbuffer_load_dword v109,v121,s[36:39],s83 offen sc1\nbuffer_load_dword v110,v122,s[36:39],s83 offen sc1\n" \
"buffer_load_dword v111,v123,s[36:39],s83 offen sc1\nv_pk_mul_f32 v[40:41],v[40:41],v[92:93] op_sel_hi:[1,0]\nv_pk_mul_f32 v[44:45],v[44:45],v[92:93] op_sel_hi:[1,0]\nv_pk_mul_f32 v[42:43],v[42:43],v[92:93] op_sel_hi:[1,0]\n" \
"v_pk_mul_f32 v[46:47],v[46:47],v[92:93] op_sel_hi:[1,0]\nv_pk_fma_f32 v[24:25],v[32:33],v[92:93],v[24:25] op_sel_hi:[1,0,1]\nv_pk_fma_f32 v[28:29],v[36:37],v[92:93],v[28:29] op_sel_hi:[1,0,1] neg_lo:[1,0,0] neg_hi:[1,0,0]\nv_pk_fma_f32 v[16:17],v[32:33],v[94:95],v[16:17] op_sel_hi:[1,0,1]\n" \
"v_pk_fma_f32 v[20:21],v[36:37],v[94:95],v[20:21] op_sel_hi:[1,0,1] neg_lo:[1,0,0] neg_hi:[1,0,0]\nv_pk_fma_f32 v[26:27],v[34:35],v[92:93],v[26:27] op_sel_hi:[1,0,1]\nv_pk_fma_f32 v[30:31],v[38:39],v[92:93],v[30:31] op_sel_hi:[1,0,1] neg_lo:[1,0,0] neg_hi:[1,0,0]\nv_pk_fma_f32 v[18:19],v[34:35],v[94:95],v[18:19] op_sel_hi:[1,0,1]\n" \
"v_pk_fma_f32 v[22:23],v[38:39],v[94:95],v[22:23] op_sel_hi:[1,0,1] neg_lo:[1,0,0] neg_hi:[1,0,0]\nv_pk_fma_f32 v[48:49],v[56:57],v[92:93],v[48:49] op_sel_hi:[1,0,1]\nv_pk_fma_f32 v[52:53],v[60:61],v[92:93],v[52:53] op_sel_hi:[1,0,1] neg_lo:[1,0,0] neg_hi:[1,0,0]\nv_pk_fma_f32 v[40:41],v[56:57],v[94:95],v[40:41] op_sel_hi:[1,0,1]\n" \
"v_pk_fma_f32 v[44:45],v[60:61],v[94:95],v[44:45] op_sel_hi:[1,0,1] neg_lo:[1,0,0] neg_hi:[1,0,0]\nv_pk_fma_f32 v[50:51],v[58:59],v[92:93],v[50:51] op_sel_hi:[1,0,1]\nv_pk_fma_f32 v[54:55],v[62:63],v[92:93],v[54:55] op_sel_hi:[1,0,1] neg_lo:[1,0,0] neg_hi:[1,0,0]\nv_pk_fma_f32 v[42:43],v[58:59],v[94:95],v[42:43] op_sel_hi:[1,0,1]\n" \
"v_pk_fma_f32 v[46:47],v[62:63],v[94:95],v[46:47] op_sel_hi:[1,0,1] neg_lo:[1,0,0] neg_hi:[1,0,0]\nv_pk_mul_f32 v[32:33],v[64:65],v[90:91] op_sel_hi:[1,0] neg_lo:[1,0] neg_hi:[1,0]\nv_pk_mul_f32 v[36:37],v[66:67],v[90:91] op_sel_hi:[1,0]\nv_pk_mul_f32 v[34:35],v[68:69],v[90:91] op_sel_hi:[1,0] neg_lo:[1,0] neg_hi:[1,0]\n" \
"v_pk_mul_f32 v[38:39],v[70:71],v[90:91] op_sel_hi:[1,0]\nv_pk_mul_f32 v[56:57],v[72:73],v[90:91] op_sel_hi:[1,0] neg_lo:[1,0] neg_hi:[1,0]\nv_pk_mul_f32 v[60:61],v[74:75],v[90:91] op_sel_hi:[1,0]\nv_pk_mul_f32 v[58:59],v[76:77],v[90:91] op_sel_hi:[1,0] neg_lo:[1,0] neg_hi:[1,0]\n" \
"v_pk_mul_f32 v[62:63],v[78:79],v[90:91] op_sel_hi:[1,0]\ns_mov_b32 s78,0x11111111\ns_mov_b32 s79,0x11111111\nds_read_b64_tr_b16 a[64:65],v8 offset:16384\n" \
"ds_read_b64_tr_b16 a[68:69],v9 offset:16384\nds_read_b64_tr_b16 a[72:73],v10 offset:16384\nds_read_b64_tr_b16 a[76:77],v11 offset:16384\nds_read_b64_tr_b16 a[80:81],v8 offset:18432\n" \
"ds_read_b64_tr_b16 a[84:85],v9 offset:18432\nds_read_b64_tr_b16 a[88:89],v10 offset:18432\nds_read_b64_tr_b16 a[92:93],v11 offset:18432\ns_waitcnt vmcnt(12)\n" \
"v_mfma_f32_16x16x32_f16 v[64:67],v[128:131],v[96:99],0\nv_or3_b32 v124,v96,v97,v98\ns_waitcnt vmcnt(11)\nv_or3_b32 v124,v124,v99,v100\n" \
"v_mfma_f32_16x16x32_f16 v[68:71],v[132:135],v[96:99],0\ns_waitcnt vmcnt(9)\nv_or3_b32 v124,v124,v101,v102\ns_waitcnt vmcnt(8)\n" \
"v_mfma_f32_16x16x32_f16 v[72:75],v[136:139],v[100:103],0\ns_waitcnt vmcnt(7)\nv_or3_b32 v124,v124,v103,v104\nv_mfma_f32_16x16x32_f16 v[76:79],v[140:143],v[100:103],0\n" \
"s_waitcnt vmcnt(5)\nv_or3_b32 v124,v124,v105,v106\ns_waitcnt vmcnt(4)\nv_mfma_f32_16x16x32_f16 v[80:83],v[144:147],v[104:107],0\n" \
"s_waitcnt vmcnt(3)\nv_or3_b32 v124,v124,v107,v108\nv_mfma_f32_16x16x32_f16 v[84:87],v[148:151],v[104:107],0\ns_waitcnt vmcnt(1)\n" \
"v_or3_b32 v124,v124,v109,v110\ns_waitcnt vmcnt(0)\nv_mfma_f32_16x16x32_f16 v[88:91],v[152:155],v[108:111],0\nv_or_b32 v124,v124,v111\n" \
"v_mfma_f32_16x16x32_f16 v[92:95],v[156:159],v[108:111],0\nv_and_b32 v124,1,v124\nv_cmp_ne_u32 vcc,0,v124\ns_nop 0\n" \
"s_and_b64 vcc,vcc,s[78:79]\ns_cbranch_vccnz .Lst_%=\n.Lbt_%=:\nv_cvt_pk_f16_f32 v96,v64,v68\n" \
"v_cvt_pk_f16_f32 v97,v72,v76\nv_cvt_pk_f16_f32 v98,v80,v84\nv_cvt_pk_f16_f32 v100,v65,v69\nv_cvt_pk_f16_f32 v99,v88,v92\n" \
"ds_write_b128 v246,v[96:99]\nv_cvt_pk_f16_f32 v101,v73,v77\nv_cvt_pk_f16_f32 v102,v81,v85\nv_cvt_pk_f16_f32 v103,v89,v93\n" \
"ds_write_b128 v247,v[100:103] offset:1024\ns_add_u32 s95,s92,9584640\ns_add_u32 m0,s57,20480\ns_add_u32 s100,s92,221184\n" \
"buffer_load_dwordx4 v4,s[8:11],s95 offen lds\nv_cvt_pk_f16_f32 v104,v66,v70\nv_cvt_pk_f16_f32 v105,v74,v78\nv_cvt_pk_f16_f32 v106,v82,v86\n" \
"v_cvt_pk_f16_f32 v107,v90,v94\nds_write_b128 v246,v[104:107] offset:2048\nbuffer_load_dwordx4 v5,s[8:11],s95 offen offset:1024 lds\nv_cvt_pk_f16_f32 v108,v67,v71\n" \
"v_cvt_pk_f16_f32 v109,v75,v79\nv_cvt_pk_f16_f32 v110,v83,v87\nv_cvt_pk_f16_f32 v111,v91,v95\nds_write_b128 v247,v[108:111] offset:3072\n" \
"buffer_load_dwordx4 v6,s[8:11],s95 offen offset:2048 lds\ns_add_u32 s101,s92,9658368\ns_add_u32 s31,s92,294912\ns_add_u32 s96,s92,9732096\n" \
"s_add_u32 s97,s92,368640\ns_mov_b32 s5,26624\ns_mov_b32 s6,31232\nbuffer_load_dwordx4 v7,s[8:11],s95 offen offset:3072 lds\n" \
"s_add_u32 m0,s57,24576\ns_add_u32 s95,s92,9805824\ns_waitcnt lgkmcnt(0)\ns_barrier\n" \
"ds_read_b128 v[96:99],v252\nds_read_b128 v[100:103],v253\nds_read_b128 v[104:107],v252 offset:2048\nds_read_b128 v[108:111],v253 offset:2048\n" \
"buffer_load_dwordx4 v4,s[8:11],s100 offen lds\nbuffer_load_dwordx4 v5,s[8:11],s100 offen offset:1024 lds\ns_waitcnt lgkmcnt(3)\nv_mfma_f32_16x16x32_f16 v[64:67],v[112:115],v[96:99],0\n" \
"v_mfma_f32_16x16x32_f16 v[68:71],v[116:119],v[96:99],0\nbuffer_load_dwordx4 v6,s[8:11],s100 offen offset:2048 lds\ns_waitcnt lgkmcnt(0)\nv_mfma_f32_16x16x32_f16 v[72:75],v[112:115],v[100:103],0\n" \
"v_mfma_f32_16x16x32_f16 v[76:79],v[116:119],v[100:103],0\nv_mfma_f32_16x16x32_f16 v[80:83],v[112:115],v[104:107],0\nbuffer_load_dwordx4 v7,s[8:11],s100 offen offset:3072 lds\nv_mfma_f32_16x16x32_f16 v[84:87],v[116:119],v[104:107],0\n" \
"s_add_u32 m0,s57,0\nv_pk_mul_f32 v[122:123],v[16:17],v[64:65]\nv_pk_mul_f32 v[120:121],v[16:17],v[68:69]\nv_pk_mul_f32 v[124:125],v[18:19],v[70:71]\n" \
"v_pk_mul_f32 v[126:127],v[18:19],v[66:67]\nv_pk_fma_f32 v[120:121],v[20:21],v[64:65],v[120:121]\nbuffer_load_dwordx4 v4,s[8:11],s101 offen lds\nv_pk_fma_f32 v[122:123],v[20:21],v[68:69],v[122:123] neg_lo:[1,0,0] neg_hi:[1,0,0]\n" \
"v_pk_fma_f32 v[124:125],v[22:23],v[66:67],v[124:125]\nv_pk_fma_f32 v[126:127],v[22:23],v[70:71],v[126:127] neg_lo:[1,0,0] neg_hi:[1,0,0]\nv_pk_fma_f32 v[120:121],v[32:33],v[76:77],v[120:121]\nv_pk_fma_f32 v[122:123],v[32:33],v[72:73],v[122:123]\n" \
"v_pk_fma_f32 v[124:125],v[34:35],v[78:79],v[124:125]\nv_pk_fma_f32 v[126:127],v[34:35],v[74:75],v[126:127]\nbuffer_load_dwordx4 v5,s[8:11],s101 offen offset:1024 lds\nv_pk_fma_f32 v[120:121],v[36:37],v[72:73],v[120:121]\n" \
"v_pk_fma_f32 v[122:123],v[36:37],v[76:77],v[122:123] neg_lo:[1,0,0] neg_hi:[1,0,0]\nv_pk_fma_f32 v[124:125],v[38:39],v[74:75],v[124:125]\nv_pk_fma_f32 v[126:127],v[38:39],v[78:79],v[126:127] neg_lo:[1,0,0] neg_hi:[1,0,0]\nv_mfma_f32_16x16x32_f16 v[88:91],v[112:115],v[108:111],0\n" \
"v_cvt_pk_f16_f32 v96,v120,v122\nv_cvt_pk_f16_f32 v97,v121,v123\nbuffer_load_dwordx4 v6,s[8:11],s101 offen offset:2048 lds\nv_mfma_f32_16x16x32_f16 v[92:95],v[116:119],v[108:111],0\n" \
"v_cvt_pk_f16_f32 v98,v124,v126\nv_cvt_pk_f16_f32 v99,v125,v127\nv_pk_mul_f32 v[120:121],v[24:25],v[76:77]\nv_pk_mul_f32 v[122:123],v[24:25],v[72:73]\n" \
"v_pk_mul_f32 v[124:125],v[26:27],v[78:79]\nv_pk_mul_f32 v[126:127],v[26:27],v[74:75]\nbuffer_load_dwordx4 v7,s[8:11],s101 offen offset:3072 lds\nv_pk_fma_f32 v[120:121],v[28:29],v[72:73],v[120:121]\n" \
"v_pk_fma_f32 v[122:123],v[28:29],v[76:77],v[122:123] neg_lo:[1,0,0] neg_hi:[1,0,0]\nv_pk_fma_f32 v[124:125],v[30:31],v[74:75],v[124:125]\nv_pk_fma_f32 v[126:127],v[30:31],v[78:79],v[126:127] neg_lo:[1,0,0] neg_hi:[1,0,0]\nv_pk_fma_f32 v[120:121],v[32:33],v[68:69],v[120:121] neg_lo:[1,0,0] neg_hi:[1,0,0]\n" \
"v_pk_fma_f32 v[122:123],v[32:33],v[64:65],v[122:123] neg_lo:[1,0,0] neg_hi:[1,0,0]\nv_pk_fma_f32 v[124:125],v[34:35],v[70:71],v[124:125] neg_lo:[1,0,0] neg_hi:[1,0,0]\ns_add_u32 m0,s57,4096\ns_add_u32 s100,s92,442368\n" \
"buffer_load_dwordx4 v4,s[8:11],s31 offen lds\nv_pk_fma_f32 v[126:127],v[34:35],v[66:67],v[126:127] neg_lo:[1,0,0] neg_hi:[1,0,0]\nv_pk_fma_f32 v[120:121],v[36:37],v[64:65],v[120:121] neg_lo:[1,0,0] neg_hi:[1,0,0]\nv_pk_fma_f32 v[122:123],v[36:37],v[68:69],v[122:123]\n" \
"v_pk_fma_f32 v[124:125],v[38:39],v[66:67],v[124:125] neg_lo:[1,0,0] neg_hi:[1,0,0]\nv_pk_fma_f32 v[126:127],v[38:39],v[70:71],v[126:127]\nv_cvt_pk_f16_f32 v100,v120,v122\nv_cvt_pk_f16_f32 v101,v121,v123\n" \
"buffer_load_dwordx4 v5,s[8:11],s31 offen offset:1024 lds\nv_cvt_pk_f16_f32 v102,v124,v126\nv_cvt_pk_f16_f32 v103,v125,v127\nv_pk_mul_f32 v[120:121],v[40:41],v[84:85]\n" \
"v_pk_mul_f32 v[122:123],v[40:41],v[80:81]\nv_pk_mul_f32 v[124:125],v[42:43],v[86:87]\nv_pk_mul_f32 v[126:127],v[42:43],v[82:83]\nv_pk_fma_f32 v[120:121],v[44:45],v[80:81],v[120:121]\n" \
"buffer_load_dwordx4 v6,s[8:11],s31 offen offset:2048 lds\nv_pk_fma_f32 v[122:123],v[44:45],v[84:85],v[122:123] neg_lo:[1,0,0] neg_hi:[1,0,0]\nv_pk_fma_f32 v[124:125],v[46:47],v[82:83],v[124:125]\nv_pk_fma_f32 v[126:127],v[46:47],v[86:87],v[126:127] neg_lo:[1,0,0] neg_hi:[1,0,0]\n" \
"v_pk_fma_f32 v[120:121],v[56:57],v[92:93],v[120:121]\nv_pk_fma_f32 v[122:123],v[56:57],v[88:89],v[122:123]\nv_pk_fma_f32 v[124:125],v[58:59],v[94:95],v[124:125]\nv_pk_fma_f32 v[126:127],v[58:59],v[90:91],v[126:127]\n" \
"buffer_load_dwordx4 v7,s[8:11],s31 offen offset:3072 lds\nv_pk_fma_f32 v[120:121],v[60:61],v[88:89],v[120:121]\nv_pk_fma_f32 v[122:123],v[60:61],v[92:93],v[122:123] neg_lo:[1,0,0] neg_hi:[1,0,0]\nv_pk_fma_f32 v[124:125],v[62:63],v[90:91],v[124:125]\n" \
"v_pk_fma_f32 v[126:127],v[62:63],v[94:95],v[126:127] neg_lo:[1,0,0] neg_hi:[1,0,0]\nv_cvt_pk_f16_f32 v104,v120,v122\nv_cvt_pk_f16_f32 v105,v121,v123\nv_cvt_pk_f16_f32 v106,v124,v126\n" \
"s_add_u32 m0,s57,8192\ns_add_u32 s101,s92,9879552\nbuffer_load_dwordx4 v4,s[8:11],s96 offen lds\nv_cvt_pk_f16_f32 v107,v125,v127\n" \
"v_pk_mul_f32 v[120:121],v[48:49],v[92:93]\nv_pk_mul_f32 v[122:123],v[48:49],v[88:89]\nv_pk_mul_f32 v[124:125],v[50:51],v[94:95]\nv_pk_mul_f32 v[126:127],v[50:51],v[90:91]\n" \
"v_pk_fma_f32 v[120:121],v[52:53],v[88:89],v[120:121]\nv_pk_fma_f32 v[122:123],v[52:53],v[92:93],v[122:123] neg_lo:[1,0,0] neg_hi:[1,0,0]\nbuffer_load_dwordx4 v5,s[8:11],s96 offen offset:1024 lds\nv_pk_fma_f32 v[124:125],v[54:55],v[90:91],v[124:125]\n" \
"v_pk_fma_f32 v[126:127],v[54:55],v[94:95],v[126:127] neg_lo:[1,0,0] neg_hi:[1,0,0]\nv_pk_fma_f32 v[120:121],v[56:57],v[84:85],v[120:121] neg_lo:[1,0,0] neg_hi:[1,0,0]\nv_pk_fma_f32 v[122:123],v[56:57],v[80:81],v[122:123] neg_lo:[1,0,0] neg_hi:[1,0,0]\nv_pk_fma_f32 v[124:125],v[58:59],v[86:87],v[124:125] neg_lo:[1,0,0] neg_hi:[1,0,0]\n" \
"v_pk_fma_f32 v[126:127],v[58:59],v[82:83],v[126:127] neg_lo:[1,0,0] neg_hi:[1,0,0]\nv_pk_fma_f32 v[120:121],v[60:61],v[80:81],v[120:121] neg_lo:[1,0,0] neg_hi:[1,0,0]\nbuffer_load_dwordx4 v6,s[8:11],s96 offen offset:2048 lds\nv_pk_fma_f32 v[122:123],v[60:61],v[84:85],v[122:123]\n" \
"v_pk_fma_f32 v[124:125],v[62:63],v[82:83],v[124:125] neg_lo:[1,0,0] neg_hi:[1,0,0]\nv_pk_fma_f32 v[126:127],v[62:63],v[86:87],v[126:127]\nv_mfma_f32_16x16x32_f16 v[64:67],v[112:115],v[96:99],0\nv_cvt_pk_f16_f32 v108,v120,v122\n" \
"v_cvt_pk_f16_f32 v109,v121,v123\nv_mfma_f32_16x16x32_f16 v[68:71],v[116:119],v[96:99],0\nbuffer_load_dwordx4 v7,s[8:11],s96 offen offset:3072 lds\nv_mfma_f32_16x16x32_f16 v[80:83],v[112:115],v[104:107],0\n" \
"v_cvt_pk_f16_f32 v110,v124,v126\nv_cvt_pk_f16_f32 v111,v125,v127\nv_mfma_f32_16x16x32_f16 v[84:87],v[116:119],v[104:107],0\ns_add_u32 m0,s57,12288\n" \
"s_waitcnt vmcnt(16)\nds_read_b64_tr_b16 a[66:67],v8 offset:20480\nv_mfma_f32_16x16x32_f16 v[72:75],v[112:115],v[100:103],0\nbuffer_load_dwordx4 v4,s[8:11],s97 offen lds\n" \
"v_mfma_f32_16x16x32_f16 v[76:79],v[116:119],v[100:103],0\nv_cvt_pk_f16_f32 v96,v64,v68\nv_cvt_pk_f16_f32 v98,v65,v69\nv_mfma_f32_16x16x32_f16 v[88:91],v[112:115],v[108:111],0\n" \
"v_cvt_pk_f16_f32 v97,v80,v84\nds_write_b64 v248,v[96:97] offset:16384\nbuffer_load_dwordx4 v5,s[8:11],s97 offen offset:1024 lds\nv_mfma_f32_16x16x32_f16 v[92:95],v[116:119],v[108:111],0\n" \
"v_cvt_pk_f16_f32 v99,v81,v85\nds_write_b64 v248,v[98:99] offset:17408\nv_cvt_pk_f16_f32 v100,v66,v70\nv_cvt_pk_f16_f32 v101,v82,v86\n" \
"ds_write_b64 v248,v[100:101] offset:18432\nbuffer_load_dwordx4 v6,s[8:11],s97 offen offset:2048 lds\nv_cvt_pk_f16_f32 v102,v67,v71\nv_cvt_pk_f16_f32 v103,v83,v87\n" \
"ds_write_b64 v248,v[102:103] offset:19456\nv_cvt_pk_f16_f32 v104,v72,v76\nv_cvt_pk_f16_f32 v105,v88,v92\nds_write_b64 v248,v[104:105] offset:16512\n" \
"buffer_load_dwordx4 v7,s[8:11],s97 offen offset:3072 lds\nv_cvt_pk_f16_f32 v106,v73,v77\nv_cvt_pk_f16_f32 v107,v89,v93\nds_write_b64 v248,v[106:107] offset:17536\n" \
"v_cvt_pk_f16_f32 v108,v74,v78\nv_cvt_pk_f16_f32 v109,v90,v94\nds_write_b64 v248,v[108:109] offset:18560\ns_add_u32 m0,s57,16384\n" \
"v_cvt_pk_f16_f32 v110,v75,v79\nbuffer_load_dwordx4 v4,s[8:11],s95 offen lds\nv_cvt_pk_f16_f32 v111,v91,v95\nds_write_b64 v248,v[110:111] offset:19584\n" \
"ds_read_b64_tr_b16 a[70:71],v9 offset:20480\nds_read_b64_tr_b16 a[74:75],v10 offset:20480\nds_read_b64_tr_b16 a[78:79],v11 offset:20480\nds_read_b64_tr_b16 a[82:83],v8 offset:22528\n" \
"ds_read_b64_tr_b16 a[86:87],v9 offset:22528\nbuffer_load_dwordx4 v5,s[8:11],s95 offen offset:1024 lds\nds_read_b64_tr_b16 a[90:91],v10 offset:22528\nds_read_b64_tr_b16 a[94:95],v11 offset:22528\n" \
"s_waitcnt vmcnt(18)\nds_read_b64_tr_b16 a[96:97],v8 offset:24576\nds_read_b64_tr_b16 a[100:101],v9 offset:24576\nds_read_b64_tr_b16 a[104:105],v10 offset:24576\n" \
"ds_read_b64_tr_b16 a[108:109],v11 offset:24576\nds_read_b64_tr_b16 a[112:113],v8 offset:26624\nds_read_b64_tr_b16 a[116:117],v9 offset:26624\nds_read_b64_tr_b16 a[120:121],v10 offset:26624\n" \
"buffer_load_dwordx4 v6,s[8:11],s95 offen offset:2048 lds\ns_waitcnt lgkmcnt(9)\ns_barrier\nds_read_b128 v[96:99],v254 offset:16384\n" \
"ds_read_b128 v[100:103],v254 offset:17408\nds_read_b128 v[104:107],v254 offset:18432\nds_read_b128 v[108:111],v254 offset:19456\nds_read_b64_tr_b16 a[124:125],v11 offset:26624\n" \
"v_min_u32 v32,2288,v2\nbuffer_load_dwordx4 v7,s[8:11],s95 offen offset:3072 lds\ns_add_u32 m0,s57,20480\ns_waitcnt lgkmcnt(5)\n" \
"buffer_load_dwordx4 v4,s[8:11],s100 offen lds\ns_waitcnt lgkmcnt(4)\nv_mfma_f32_16x16x32_f16 v[64:67],v[96:99],v[160:163],0\nv_mfma_f32_16x16x32_f16 v[68:71],v[96:99],v[164:167],0\n" \
"s_waitcnt lgkmcnt(2)\nv_mfma_f32_16x16x32_f16 v[72:75],v[100:103],v[168:171],0\nbuffer_load_dwordx4 v5,s[8:11],s100 offen offset:1024 lds\nv_mfma_f32_16x16x32_f16 v[76:79],v[100:103],v[172:175],0\n" \
"v_mfma_f32_16x16x32_f16 v[80:83],v[104:107],v[176:179],0\nv_mfma_f32_16x16x32_f16 v[84:87],v[104:107],v[180:183],0\nbuffer_load_dwordx4 v6,s[8:11],s100 offen offset:2048 lds\ns_waitcnt lgkmcnt(0)\n" \
"v_mfma_f32_16x16x32_f16 v[88:91],v[108:111],v[184:187],0\nv_cvt_pk_f16_f32 v96,v68,v64\nv_cvt_pk_f16_f32 v97,v69,v65\nv_cvt_pk_f16_f32 v98,v70,v66\n" \
"v_cvt_pk_f16_f32 v99,v71,v67\nv_or_b32_e64 v96,1,v96\nbuffer_load_dwordx4 v7,s[8:11],s100 offen offset:3072 lds\nv_mfma_f32_16x16x32_f16 v[92:95],v[108:111],v[188:191],0\n" \
"v_cvt_pk_f16_f32 v100,v76,v72\nv_cvt_pk_f16_f32 v101,v77,v73\nv_cvt_pk_f16_f32 v102,v78,v74\nv_cvt_pk_f16_f32 v103,v79,v75\n" \
"v_or_b32_e64 v100,1,v100\nv_cvt_pk_f16_f32 v104,v84,v80\ns_add_u32 m0,s57,24576\nv_cvt_pk_f16_f32 v105,v85,v81\n" \
"buffer_load_dwordx4 v4,s[8:11],s101 offen lds\nv_cvt_pk_f16_f32 v106,v86,v82\nv_cvt_pk_f16_f32 v107,v87,v83\nv_or_b32_e64 v104,1,v104\n" \
"v_cvt_pk_f16_f32 v108,v92,v88\nv_cvt_pk_f16_f32 v109,v93,v89\nv_cvt_pk_f16_f32 v110,v94,v90\nv_cvt_pk_f16_f32 v111,v95,v91\n" \
"v_or_b32_e64 v108,1,v108\nbuffer_load_dwordx4 v5,s[8:11],s101 offen offset:1024 lds\nbuffer_load_dwordx4 v6,s[8:11],s101 offen offset:2048 lds\nbuffer_load_dwordx4 v7,s[8:11],s101 offen offset:3072 lds\n" \
"buffer_load_dwordx4 v[16:19],v32,s[36:39],s5 offen sc1\nbuffer_load_dwordx4 v[20:23],v32,s[36:39],s5 offen offset:2304 sc1\nbuffer_load_dwordx4 v[24:27],v32,s[36:39],s6 offen sc1\nbuffer_load_dwordx4 v[28:31],v32,s[36:39],s6 offen offset:2304 sc1\n" \
"buffer_store_dwordx4 v[96:99],v255,s[36:39],s80 offen sc1\nbuffer_store_dwordx4 v[100:103],v255,s[36:39],s81 offen sc1\nbuffer_store_dwordx4 v[104:107],v255,s[36:39],s82 offen sc1\nbuffer_store_dwordx4 v[108:111],v255,s[36:39],s83 offen sc1\n" \
"s_barrier\ns_waitcnt vmcnt(32)\nds_read_b64_tr_b16 a[98:99],v8\nds_read_b64_tr_b16 a[102:103],v9\n" \
"ds_read_b64_tr_b16 a[106:107],v10\nds_read_b64_tr_b16 a[110:111],v11\nds_read_b64_tr_b16 a[114:115],v8 offset:2048\nds_read_b64_tr_b16 a[118:119],v9 offset:2048\n" \
"ds_read_b64_tr_b16 a[122:123],v10 offset:2048\nds_read_b64_tr_b16 a[126:127],v11 offset:2048\ns_waitcnt vmcnt(28)\nds_read_b64_tr_b16 a[128:129],v8 offset:4096\n" \
"ds_read_b64_tr_b16 a[132:133],v9 offset:4096\nds_read_b64_tr_b16 a[136:137],v10 offset:4096\nds_read_b64_tr_b16 a[140:141],v11 offset:4096\nds_read_b64_tr_b16 a[144:145],v8 offset:6144\n" \
"ds_read_b64_tr_b16 a[148:149],v9 offset:6144\nds_read_b64_tr_b16 a[152:153],v10 offset:6144\nds_read_b64_tr_b16 a[156:157],v11 offset:6144\ns_waitcnt vmcnt(24)\n" \
"ds_read_b64_tr_b16 a[130:131],v8 offset:8192\nds_read_b64_tr_b16 a[134:135],v9 offset:8192\nds_read_b64_tr_b16 a[138:139],v10 offset:8192\nds_read_b64_tr_b16 a[142:143],v11 offset:8192\n" \
"ds_read_b64_tr_b16 a[146:147],v8 offset:10240\nds_read_b64_tr_b16 a[150:151],v9 offset:10240\nds_read_b64_tr_b16 a[154:155],v10 offset:10240\nds_read_b64_tr_b16 a[158:159],v11 offset:10240\n" \
"s_waitcnt vmcnt(7)\nv_cmp_ne_u64 vcc,s[28:29],v[16:17]\ns_waitcnt vmcnt(6)\nv_cmp_ne_u64 s[24:25],s[28:29],v[20:21]\n" \
"s_waitcnt vmcnt(5)\nv_cmp_ne_u64 s[26:27],s[28:29],v[24:25]\ns_or_b64 vcc,vcc,s[24:25]\ns_waitcnt vmcnt(4)\n" \
"v_cmp_ne_u64 s[24:25],s[28:29],v[28:29]\ns_or_b64 vcc,vcc,s[26:27]\ns_or_b64 vcc,vcc,s[24:25]\ns_cbranch_vccnz .LsnC_%=\n" \
".LbnC_%=:\nv_bfe_u32 v112,v0,1,5\nv_mul_u32_u24 v112,72,v112\nv_lshrrev_b32 v113,6,v0\n" \
"v_mul_u32_u24 v114,144,v0\nv_lshl_add_u32 v112,v113,5,v112\nv_subrev_u32 v114,18368,v114\nv_mov_b32 v115,1048576\n" \
"v_cmp_gt_u32 vcc,144,v0\nv_and_b32 v113,1,v0\nv_lshl_add_u32 v112,v113,4,v112\nv_cndmask_b32 v114,v115,v114,vcc\n" \
"v_and_b32 v97,0xffff0000,v18\nv_lshlrev_b32 v96,16,v18\ns_add_u32 s24,s93,s94\nv_cmp_gt_u32 vcc,128,v0\n" \
"v_add_u32 v113,8,v112\nv_mul_f32 v96,0x3ee38e39,v96\nv_mul_f32 v97,0x3ee38e39,v97\ns_sub_u32 s24,s24,253\n" \
"v_cndmask_b32 v112,v114,v112,vcc\nv_and_b32 v99,0xffff0000,v19\nv_ldexp_f32 v96,v96,s24\nv_ldexp_f32 v97,v97,s24\n" \
"v_lshlrev_b32_e64 v98,16,v19\nds_write_b64 v112,v[96:97] offset:28800\nv_add_u32 v115,72,v114\nv_mul_f32 v98,0x3ee38e39,v98\n" \
"v_mul_f32 v99,0x3ee38e39,v99\nv_cndmask_b32_e64 v113,v115,v113,vcc\nv_ldexp_f32 v98,v98,s24\nv_ldexp_f32 v99,v99,s24\n" \
"ds_write_b64 v113,v[98:99] offset:28800\nv_and_b32 v101,0xffff0000,v22\nv_lshlrev_b32 v100,16,v22\nds_write_b64 v112,v[100:101] offset:31104\n" \
"v_and_b32 v103,0xffff0000,v23\nv_lshlrev_b32 v102,16,v23\ns_lshr_b32 s25,s33,3\nds_write_b64 v113,v[102:103] offset:31104\n" \
"s_mul_i32 s25,s25,576\nv_and_b32 v105,0xffff0000,v26\nv_lshlrev_b32 v104,16,v26\ns_sub_u32 s25,163200,s25\n" \
"ds_write_b64 v112,v[104:105] offset:33408\nv_add_u32 v114,s25,v2\nv_subrev_u32 v115,163200,v114\nv_and_b32 v107,0xffff0000,v27\n" \
"v_lshlrev_b32 v106,16,v27\nv_mov_b32 v116,1048576\nv_cmp_gt_u32 vcc,576,v115\nds_write_b64 v113,v[106:107] offset:33408\n" \
"v_and_b32 v109,0xffff0000,v30\nv_cndmask_b32 v114,v116,v114,vcc\nv_and_b32 v111,0xffff0000,v31\nv_lshlrev_b32 v108,16,v30\n" \
"v_lshlrev_b32 v110,16,v31\nds_write_b128 v114,v[108:111]\nv_and_b32 v112,3,v0\ns_mul_i32 s27,s3,4896\n" \
"v_and_b32 v113,12,v0\nv_lshlrev_b32 v112,2,v112\ns_add_u32 s27,s27,9216\nv_max_u32 v112,v112,v113\n" \
"v_mov_b32 v114,1048576\nv_mov_b32 v113,s27\nv_cmp_gt_u32 vcc,12,v112\nv_and_b32 v124,48,v0\n" \
"s_and_b32 s22,s33,7\nv_cndmask_b32 v112,v114,v113,vcc\nv_add_u32 v48,v234,v112\nv_add_u32 v49,v235,v112\n" \
"v_add_u32 v50,v236,v112\nv_add_u32 v51,v237,v112\nv_add_u32 v52,v238,v112\nv_add_u32 v53,v239,v112\n" \
"v_add_u32 v54,v240,v112\nv_add_u32 v55,v241,v112\nv_cmp_gt_u32 vcc,64,v0\ns_and_b32 s24,s32,1\n" \
"s_lshl3_add_u32 s22,s24,s22\nv_cndmask_b32 v124,v124,v3,vcc\ns_bfe_u32 s24,s33,0x10002\ns_lshl1_add_u32 s24,s32,s24\n" \
"s_waitcnt lgkmcnt(0)\ns_barrier\ns_mul_i32 s19,s24,18\ns_and_b32 s24,s33,3\n" \
"s_lshl2_add_u32 s23,s24,s19\ns_lshl_b32 s24,s3,5\ns_lshr_b32 s20,s33,3\ns_lshr_b32 s21,s32,1\n" \
"s_add_u32 s54,s14,s24\ns_lshl2_add_u32 s24,s20,s21\ns_add_u32 s23,s23,s3\ns_lshl2_add_u32 s24,s24,s3\n" \
"s_add_u32 s23,s23,1\ns_lshl_b32 s24,s24,9\ns_lshl_b32 s23,s23,9\ns_add_u32 s24,s24,262144\n" \
"s_sub_u32 s59,s24,s23\ns_lshr_b32 s25,s3,1\ns_mul_i32 s24,s21,1152\ns_mul_i32 s25,s25,576\n" \
"s_add_u32 s24,s24,s25\ns_lshl4_add_u32 s24,s24,s22\ns_and_b32 s25,s3,1\ns_lshl_b32 s24,s24,8\n" \
"s_lshl_b32 s65,s25,17\ns_add_u32 s65,s65,s24\ns_lshl_b32 s25,s25,20\ns_add_u32 s24,s24,s25\n" \
"s_add_u32 s60,s24,655488\ns_add_u32 s61,s24,786432\ns_add_u32 s62,s24,786560\ns_add_u32 s63,s24,917504\n" \
"s_add_u32 s64,s24,917632\ns_mul_i32 s52,s20,4505600\ns_lshl2_add_u32 s24,s21,s3\ns_add_u32 s52,s52,12123648\n" \
"s_mul_i32 s24,s24,9216\ns_add_u32 s24,s24,s52\ns_add_u32 s68,s24,819712\ns_lshl_b32 s24,s22,14\n" \
"s_add_u32 s24,s24,s52\ns_lshl2_add_u32 s25,s21,s3\ns_add_u32 s24,s24,967168\ns_lshl_b32 s25,s25,8\n" \
"s_add_u32 s79,s25,s24\ns_add_u32 s25,s21,4\ns_and_b32 s25,s25,3\ns_add_u32 s24,s24,s12\n" \
"s_lshl_b32 s25,s25,12\ns_add_u32 s25,s25,s24\ns_sub_u32 s74,s25,819200\ns_add_u32 s25,s21,3\n" \
"s_and_b32 s25,s25,3\ns_lshl_b32 s25,s25,12\ns_add_u32 s25,s25,s24\ns_sub_u32 s71,s25,2048000\n" \
"s_sub_u32 s76,s25,1638400\ns_add_u32 s25,s21,2\ns_and_b32 s25,s25,3\ns_lshl_b32 s25,s25,12\n" \
"s_add_u32 s25,s25,s24\ns_sub_u32 s72,s25,1638400\ns_sub_u32 s77,s25,1638400\nbuffer_load_dwordx4 v[112:115],v3,s[36:39],s16 offen sc1\n" \
"buffer_load_dwordx4 v[116:119],v3,s[36:39],s17 offen sc1\nbuffer_load_dwordx4 v[120:123],v124,s[36:39],s18 offen sc1\ns_add_u32 s25,s21,1\ns_and_b32 s25,s25,3\n" \
"s_lshl_b32 s25,s25,12\ns_add_u32 s25,s25,s24\ns_sub_u32 s73,s25,1638400\ns_sub_u32 s78,s25,1228800\n" \
"s_lshl2_add_u32 s26,s33,s3\ns_and_b32 s24,s26,15\ns_and_b32 s25,s26,8\ns_lshl2_add_u32 s24,s24,192\n" \
"s_mul_i32 s25,s25,28\ns_add_u32 s25,s25,s24\ns_sub_u32 s25,s25,32\ns_bitcmp1_b32 s26,4\n" \
"s_cselect_b32 s24,s25,s24\ns_and_b32 s25,s26,31\ns_lshl2_add_u32 s25,s25,448\ns_bitcmp1_b32 s26,5\n" \
"s_cselect_b32 s24,s25,s24\ns_lshr_b32 s25,s26,6\ns_lshl1_add_u32 s25,s21,s25\ns_mul_i32 s25,s25,576\n" \
"s_add_u32 s24,s24,s25\ns_and_b32 s25,s32,1\ns_lshl1_add_u32 s24,s24,s25\ns_lshl_b32 s24,s24,11\n" \
"s_bfe_u32 s25,s33,131072\ns_add_u32 s24,s24,s25\nv_bfe_u32 v127,v0,5,1\nv_and_b32 v13,31,v0\n" \
"s_mul_i32 s48,s20,589824\ns_add_u32 s89,s24,1\nv_mul_u32_u24 v127,2448,v127\nv_mul_u32_u24 v13,72,v13\n" \
"s_mul_i32 s27,s3,4896\ns_lshl4_add_u32 s48,s12,s48\ns_lshl_b32 s86,s21,11\ns_lshl2_add_u32 s24,s2,s3\n" \
"v_and_b32 v63,0xffff0000,v224\nv_and_b32 v65,0xffff0000,v225\nv_and_b32 v67,0xffff0000,v226\nv_and_b32 v69,0xffff0000,v227\n" \
"v_and_b32 v71,0xffff0000,v228\nv_and_b32 v73,0xffff0000,v229\nv_and_b32 v75,0xffff0000,v230\nv_and_b32 v77,0xffff0000,v231\n" \
"v_and_b32 v79,0xffff0000,v232\nv_lshlrev_b32 v62,16,v224\nv_lshlrev_b32 v64,16,v225\nv_lshlrev_b32 v66,16,v226\n" \
"v_lshlrev_b32 v68,16,v227\nv_lshlrev_b32 v70,16,v228\nv_lshlrev_b32 v72,16,v229\nv_lshlrev_b32 v74,16,v230\n" \
"v_lshlrev_b32 v76,16,v231\nv_lshlrev_b32 v78,16,v232\nv_add3_u32 v13,v13,v127,s27\ns_add_u32 s48,s48,9764864\n" \
"s_add_u32 s65,s65,2097152\ns_lshl_b32 s67,s21,7\ns_lshl_b32 s85,s21,17\ns_sub_u32 s86,s14,s86\n" \
"s_lshl_b32 s24,s24,8\nv_pk_add_f32 v[62:63],v[62:63],v[250:251] op_sel_hi:[1,0] neg_lo:[0,1] neg_hi:[0,1]\nv_pk_add_f32 v[64:65],v[64:65],v[250:251] op_sel_hi:[1,0] neg_lo:[0,1] neg_hi:[0,1]\nv_pk_add_f32 v[66:67],v[66:67],v[250:251] op_sel_hi:[1,0] neg_lo:[0,1] neg_hi:[0,1]\n" \
"v_pk_add_f32 v[68:69],v[68:69],v[250:251] op_sel_hi:[1,0] neg_lo:[0,1] neg_hi:[0,1]\nv_pk_add_f32 v[70:71],v[70:71],v[250:251] op_sel_hi:[1,0] neg_lo:[0,1] neg_hi:[0,1]\nv_pk_add_f32 v[72:73],v[72:73],v[250:251] op_sel_hi:[1,0] neg_lo:[0,1] neg_hi:[0,1]\nv_pk_add_f32 v[74:75],v[74:75],v[250:251] op_sel_hi:[1,0] neg_lo:[0,1] neg_hi:[0,1]\n" \
"v_pk_add_f32 v[76:77],v[76:77],v[250:251] op_sel_hi:[1,0] neg_lo:[0,1] neg_hi:[0,1]\nv_pk_add_f32 v[78:79],v[78:79],v[250:251] op_sel_hi:[1,0] neg_lo:[0,1] neg_hi:[0,1]\nv_mov_b64 v[16:17],v[192:193]\nv_mov_b64 v[18:19],v[194:195]\n" \
"v_mov_b64 v[20:21],v[196:197]\nv_mov_b64 v[22:23],v[198:199]\nv_mov_b64 v[24:25],v[208:209]\nv_mov_b64 v[26:27],v[210:211]\n" \
"v_mov_b64 v[28:29],v[212:213]\nv_mov_b64 v[30:31],v[214:215]\nv_mov_b64 v[32:33],v[216:217]\nv_mov_b64 v[34:35],v[218:219]\n" \
"v_mov_b64 v[36:37],v[220:221]\nv_mov_b64 v[38:39],v[222:223]\nv_mov_b64 v[40:41],v[200:201]\nv_mov_b64 v[42:43],v[202:203]\n" \
"v_mov_b64 v[44:45],v[204:205]\nv_mov_b64 v[46:47],v[206:207]\nv_mov_b64 v[56:57],v[242:243]\nv_mov_b64 v[58:59],v[244:245]\n" \
"v_mov_b32 v60,v249\nv_mov_b32 v61,v250\nds_read_b64_tr_b16 a[160:161],v8 offset:12288\nds_read_b64_tr_b16 a[164:165],v9 offset:12288\n" \
"ds_read_b64_tr_b16 a[168:169],v10 offset:12288\nds_read_b64_tr_b16 a[172:173],v11 offset:12288\nds_read_b64_tr_b16 a[176:177],v8 offset:14336\nds_read_b64_tr_b16 a[180:181],v9 offset:14336\n" \
"ds_read_b64_tr_b16 a[184:185],v10 offset:14336\nds_read_b64_tr_b16 a[188:189],v11 offset:14336\nds_read_b64_tr_b16 a[162:163],v8 offset:16384\nds_read_b64_tr_b16 a[166:167],v9 offset:16384\n" \
"ds_read_b64_tr_b16 a[170:171],v10 offset:16384\nds_read_b64_tr_b16 a[174:175],v11 offset:16384\nds_read_b64_tr_b16 a[178:179],v8 offset:18432\nv_mov_b32_e64 v127,0\n" \
"ds_read_b64_tr_b16 a[182:183],v9 offset:18432\nds_read_b64_tr_b16 a[186:187],v10 offset:18432\ns_add_u32 s49,s48,4096\ns_add_u32 s50,s48,8192\n" \
"s_add_u32 s51,s48,12288\ns_add_u32 s53,s52,3276800\ns_add_u32 s83,s52,2048000\ns_mov_b32 s55,0xbdd2d3e7\n" \
"s_mul_i32 s56,s3,9568\ns_lshl_b32 s58,s3,13\ns_add_u32 s75,s59,196608\ns_add_u32 s66,s65,128\n" \
"ds_read_b64_tr_b16 a[190:191],v11 offset:18432\nds_read_b64_tr_b16 a[192:193],v8 offset:20480\ns_add_u32 s67,s67,163200\nds_read_b64_tr_b16 a[196:197],v9 offset:20480\n" \
"s_add_u32 s69,s68,3072\nds_read_b64_tr_b16 a[200:201],v10 offset:20480\ns_add_u32 s70,s68,6144\nds_read_b64_tr_b16 a[204:205],v11 offset:20480\n" \
"s_add_u32 s80,s79,4096\nds_read_b64_tr_b16 a[208:209],v8 offset:22528\ns_add_u32 s81,s79,8192\nds_read_b64_tr_b16 a[212:213],v9 offset:22528\n" \
"s_add_u32 s82,s79,12288\nds_read_b64_tr_b16 a[216:217],v10 offset:22528\nds_read_b64_tr_b16 a[220:221],v11 offset:22528\ns_and_b32 s84,s33,1\n" \
"ds_read_b64_tr_b16 a[194:195],v8 offset:24576\ns_add_u32 s85,s85,65536\nds_read_b64_tr_b16 a[198:199],v9 offset:24576\ns_add_u32 s87,s12,65536\n" \
"ds_read_b64_tr_b16 a[202:203],v10 offset:24576\ns_add_u32 s88,s74,819200\nds_read_b64_tr_b16 a[206:207],v11 offset:24576\ns_add_u32 s86,s86,0x10000\n" \
"ds_read_b64_tr_b16 a[210:211],v8 offset:26624\ns_mov_b32 s90,7405568\nds_read_b64_tr_b16 a[214:215],v9 offset:26624\ns_add_u32 s91,s24,69206016\n" \
"ds_read_b64_tr_b16 a[218:219],v10 offset:26624\nds_read_b64_tr_b16 a[222:223],v11 offset:26624\ns_sub_u32 s98,s57,s12\ns_mov_b32 s99,s12\n" \
"s_waitcnt vmcnt(1)\nv_and_b32 v125,v112,v116\ns_waitcnt vmcnt(0)\nv_bitop3_b32 v125,v125,v120,1 bitop3:0x80\n" \
"v_cmp_eq_u32 vcc,0,v125\ns_nop 0\ns_cbranch_vccnz .LsuC_%=\n.LbuC_%=:\n" \
"v_mfma_f32_16x16x32_f16 v[80:83],v[32:35],v[112:115],0\nv_and_b32 v3,31,v0\nv_mul_u32_u24 v3,72,v3\nv_mfma_f32_16x16x32_f16 v[84:87],v[36:39],v[112:115],0\n" \
"v_add_u32 v13,9216,v13\nv_mov_b32 v104,-1\nv_mfma_f32_16x16x32_f16 v[88:91],v[32:35],v[116:119],0\nv_mov_b32 v105,-1\n" \
"v_mov_b32 v106,-1\nv_mfma_f32_16x16x32_f16 v[92:95],v[36:39],v[116:119],0\nv_mov_b32 v107,-1\ns_add_u32 s27,s2,192\n" \
"v_mfma_f32_16x16x32_f16 v[96:99],v[32:35],v[120:123],0\ns_lshr_b32 s31,s27,4\ns_mul_i32 s96,s31,37\ns_lshr_b32 s96,s96,8\n" \
"s_lshl2_add_u32 s31,s96,s31\ns_mul_i32 s31,s31,409600\ns_and_b32 s96,s27,15\ns_lshl_b32 s96,s96,14\n" \
"s_add_u32 s31,s31,s96\ns_add_u32 s31,s31,13090816\nbuffer_store_dwordx4 v[104:107],v2,s[36:39],s31 offen sc1\nv_mfma_f32_16x16x32_f16 v[100:103],v[36:39],v[120:123],0\n" \
"v_cvt_pk_f16_f32 v112,v84,v80\nv_cvt_pk_f16_f32 v113,v85,v81\nv_cvt_pk_f16_f32 v114,v86,v82\nv_cvt_pk_f16_f32 v115,v87,v83\n" \
"ds_write_b128 v57,v[112:115]\nv_cvt_pk_f16_f32 v116,v92,v88\nv_cvt_pk_f16_f32 v117,v93,v89\ns_add_u32 s31,s31,4096\n" \
"buffer_store_dwordx4 v[104:107],v2,s[36:39],s31 offen sc1\nv_cvt_pk_f16_f32 v118,v94,v90\nv_cvt_pk_f16_f32 v119,v95,v91\nds_write_b128 v58,v[116:119]\n" \
"v_cvt_pk_f16_f32 v120,v100,v96\nv_cvt_pk_f16_f32 v121,v101,v97\nv_cvt_pk_f16_f32 v122,v102,v98\nv_cvt_pk_f16_f32 v123,v103,v99\n" \
"ds_write_b128 v60,v[120:123]\ns_add_u32 s31,s31,4096\nbuffer_store_dwordx4 v[104:107],v2,s[36:39],s31 offen sc1\nv_add_u32 v60,31104,v3\n" \
"v_add_u32 v3,28800,v3\nds_read2_b64 v[32:35],v3 offset0:0 offset1:1\nds_read2_b64 v[36:39],v3 offset0:2 offset1:3\ns_add_u32 s31,s31,4096\n" \
"s_add_u32 s27,s2,47\ns_lshr_b32 s97,s27,4\ns_mul_i32 s95,s97,37\ns_lshr_b32 s95,s95,8\n" \
"s_lshl2_add_u32 s97,s95,s97\ns_mul_i32 s97,s97,409600\ns_and_b32 s95,s27,15\ns_lshl_b32 s95,s95,14\n" \
"buffer_store_dwordx4 v[104:107],v2,s[36:39],s31 offen sc1\ns_add_u32 s97,s97,s95\ns_add_u32 s97,s97,13090816\ns_add_u32 s100,s92,516096\n" \
"s_waitcnt lgkmcnt(2)\ns_barrier\nds_read_b64 v[112:113],v56\nds_read_b64 v[116:117],v56 offset:512\n" \
"ds_read_b64 v[120:121],v56 offset:256\nds_read_b64 v[124:125],v56 offset:768\nds_read_b64 v[114:115],v56 offset:1024\nds_read_b64 v[122:123],v56 offset:1280\n" \
"ds_read_b64 v[56:57],v3 offset:64\ns_add_u32 m0,s57,0\nbuffer_store_dwordx4 v[104:107],v2,s[36:39],s97 offen sc1\ns_add_u32 s97,s97,4096\n" \
"s_add_u32 s101,s92,9953280\ns_bfe_u32 s26,s33,0x20001\ns_lshl2_add_u32 s15,s32,s26\ns_lshl2_add_u32 s15,s15,s3\n" \
"s_lshl_b32 s15,s15,14\ns_add_u32 s15,s15,5308416\ns_add_u32 s31,s15,0\ns_add_u32 s96,s15,4096\n" \
"s_add_u32 s95,s15,12288\nv_bfe_u32 v110,v0,1,2\nv_bfe_u32 v111,v0,4,1\nv_xor_b32 v110,v110,v111\n" \
"buffer_store_dwordx4 v[104:107],v2,s[36:39],s97 offen sc1\ns_waitcnt lgkmcnt(2)\nv_swap_b32 v113,v116\nv_swap_b32 v121,v124\n" \
"v_mov_b32 v118,v115\nv_mfma_f32_16x16x32_f16 v[80:83],v[112:115],v[40:43],0\ns_waitcnt lgkmcnt(0)\nv_mov_b32 v126,v123\n" \
"s_add_u32 s97,s97,4096\nv_mfma_f32_16x16x32_f16 v[84:87],v[112:115],v[44:47],0\nv_and_b32_e64 v111,1,v0\nv_lshl_add_u32 v110,v110,1,v111\n" \
"v_mfma_f32_16x16x32_f16 v[88:91],v[120:123],v[40:43],0\nv_bfe_u32 v111,v0,3,3\nbuffer_store_dwordx4 v[104:107],v2,s[36:39],s97 offen sc1\nv_mfma_f32_16x16x32_f16 v[92:95],v[120:123],v[44:47],0\n" \
"s_add_u32 s97,s97,4096\nv_lshlrev_b32 v111,12,v111\nv_mfma_f32_16x16x32_f16 v[96:99],v[116:119],v[40:43],0\ns_lshr_b32 s26,s32,1\n" \
"s_mul_i32 s92,s26,1152\nv_pk_add_f32 v[88:89],v[80:81],v[88:89] neg_lo:[0,1] neg_hi:[0,1]\nv_pk_add_f32 v[90:91],v[82:83],v[90:91] neg_lo:[0,1] neg_hi:[0,1]\nv_pk_fma_f32 v[80:81],v[80:81],2.0,v[88:89] op_sel_hi:[1,0,1] neg_lo:[0,0,1] neg_hi:[0,0,1]\n" \
"v_pk_add_f32 v[92:93],v[84:85],v[92:93] neg_lo:[0,1] neg_hi:[0,1]\nv_pk_add_f32 v[94:95],v[86:87],v[94:95] neg_lo:[0,1] neg_hi:[0,1]\nbuffer_store_dwordx4 v[104:107],v2,s[36:39],s97 offen sc1\nv_pk_fma_f32 v[82:83],v[82:83],2.0,v[90:91] op_sel_hi:[1,0,1] neg_lo:[0,0,1] neg_hi:[0,0,1]\n" \
"v_pk_fma_f32 v[84:85],v[84:85],2.0,v[92:93] op_sel_hi:[1,0,1] neg_lo:[0,0,1] neg_hi:[0,0,1]\nv_pk_fma_f32 v[86:87],v[86:87],2.0,v[94:95] op_sel_hi:[1,0,1] neg_lo:[0,0,1] neg_hi:[0,0,1]\nv_cvt_pk_f16_f32 v112,v80,v84\nv_cvt_pk_f16_f32 v113,v81,v85\n" \
"v_cvt_pk_f16_f32 v114,v82,v86\nv_cvt_pk_f16_f32 v115,v83,v87\nv_mfma_f32_16x16x32_f16 v[100:103],v[116:119],v[44:47],0\nv_cvt_pk_f16_f32 v116,v88,v92\n" \
"v_cvt_pk_f16_f32 v117,v89,v93\nv_mfma_f32_16x16x32_f16 v[80:83],v[16:19],v[112:115],0\nbuffer_load_dwordx4 v4,s[8:11],s100 offen lds\nv_mfma_f32_16x16x32_f16 v[84:87],v[20:23],v[112:115],0\n" \
"v_cvt_pk_f16_f32 v118,v90,v94\nv_cvt_pk_f16_f32 v119,v91,v95\nv_mfma_f32_16x16x32_f16 v[104:107],v[124:127],v[40:43],0\nds_read2_b64 v[40:43],v3 offset0:4 offset1:5\n" \
"ds_read2_b64 v[112:115],v60 offset0:0 offset1:1\nv_mfma_f32_16x16x32_f16 v[88:91],v[24:27],v[116:119],0\nbuffer_load_dwordx4 v5,s[8:11],s100 offen offset:1024 lds\nv_mfma_f32_16x16x32_f16 v[92:95],v[28:31],v[116:119],0\n" \
"ds_write2_b32 v48,v84,v80 offset0:0 offset1:1\nds_write2_b32 v49,v85,v81 offset0:0 offset1:1\nds_write2_b32 v50,v86,v82 offset0:0 offset1:1\nbuffer_load_dwordx4 v6,s[8:11],s100 offen offset:2048 lds\n" \
"v_pk_add_f32 v[104:105],v[96:97],v[104:105] neg_lo:[0,1] neg_hi:[0,1]\nv_pk_add_f32 v[106:107],v[98:99],v[106:107] neg_lo:[0,1] neg_hi:[0,1]\nv_pk_fma_f32 v[96:97],v[96:97],2.0,v[104:105] op_sel_hi:[1,0,1] neg_lo:[0,0,1] neg_hi:[0,0,1]\nv_pk_fma_f32 v[98:99],v[98:99],2.0,v[106:107] op_sel_hi:[1,0,1] neg_lo:[0,0,1] neg_hi:[0,0,1]\n" \
"ds_write2_b32 v51,v87,v83 offset0:0 offset1:1\nds_write2_b32 v52,v92,v88 offset0:0 offset1:1\nbuffer_load_dwordx4 v7,s[8:11],s100 offen offset:3072 lds\nds_write2_b32 v53,v93,v89 offset0:0 offset1:1\n" \
"ds_write2_b32 v54,v94,v90 offset0:0 offset1:1\nds_write2_b32 v55,v95,v91 offset0:0 offset1:1\nv_add_u32 v48,2448,v48\ns_add_u32 m0,s57,4096\n" \
"s_add_u32 s97,s15,8192\nbuffer_load_dwordx4 v4,s[8:11],s101 offen lds\nv_add_u32 v49,2448,v49\nv_add_u32 v50,2448,v50\n" \
"v_add_u32 v51,2448,v51\nv_add_u32 v52,2448,v52\nv_add_u32 v53,2448,v53\nv_add_u32 v54,2448,v54\n" \
"v_add_u32 v55,2448,v55\nbuffer_load_dwordx4 v5,s[8:11],s101 offen offset:1024 lds\nds_read2_b64 v[116:119],v60 offset0:2 offset1:3\nv_lshl_add_u32 v4,v110,4,v111\n" \
"v_mfma_f32_16x16x32_f16 v[108:111],v[124:127],v[44:47],0\nds_read2_b64 v[44:47],v3 offset0:6 offset1:7\nv_xor_b32 v5,64,v4\nv_add_u32 v5,31744,v5\n" \
"s_lshr_b32 s26,s3,1\nbuffer_load_dwordx4 v6,s[8:11],s101 offen offset:2048 lds\nv_add_u32 v6,63488,v4\ns_lshl_b32 s26,s26,6\n" \
"s_add_u32 s92,s92,s26\nv_pk_add_f32 v[108:109],v[100:101],v[108:109] neg_lo:[0,1] neg_hi:[0,1]\nv_pk_add_f32 v[110:111],v[102:103],v[110:111] neg_lo:[0,1] neg_hi:[0,1]\nv_pk_fma_f32 v[100:101],v[100:101],2.0,v[108:109] op_sel_hi:[1,0,1] neg_lo:[0,0,1] neg_hi:[0,0,1]\n" \
"v_pk_fma_f32 v[102:103],v[102:103],2.0,v[110:111] op_sel_hi:[1,0,1] neg_lo:[0,0,1] neg_hi:[0,0,1]\nv_cvt_pk_f16_f32 v120,v96,v100\nbuffer_load_dwordx4 v7,s[8:11],s101 offen offset:3072 lds\nv_cvt_pk_f16_f32 v121,v97,v101\n" \
"v_cvt_pk_f16_f32 v122,v98,v102\nv_cvt_pk_f16_f32 v123,v99,v103\nv_cvt_pk_f16_f32 v124,v104,v108\nv_cvt_pk_f16_f32 v125,v105,v109\n" \
"v_mfma_f32_16x16x32_f16 v[96:99],v[16:19],v[120:123],0\nv_cvt_pk_f16_f32 v126,v106,v110\ns_add_u32 m0,s57,8192\ns_lshr_b32 s100,s33,3\n" \
"buffer_load_dwordx4 v1,s[36:39],s31 offen sc1 lds\nv_mfma_f32_16x16x32_f16 v[100:103],v[20:23],v[120:123],0\nv_cvt_pk_f16_f32 v127,v107,v111\nds_read2_b64 v[120:123],v60 offset0:4 offset1:5\n" \
"v_add_u32 v7,63488,v5\nv_mfma_f32_16x16x32_f16 v[104:107],v[24:27],v[124:127],0\ns_lshl_b32 s26,s3,8\ns_add_u32 s92,s92,s26\n" \
"buffer_load_dwordx4 v1,s[36:39],s31 offen offset:1024 sc1 lds\nv_mfma_f32_16x16x32_f16 v[108:111],v[28:31],v[124:127],0\nds_write2_b32 v48,v100,v96 offset0:0 offset1:1\nds_write2_b32 v49,v101,v97 offset0:0 offset1:1\n" \
"ds_write2_b32 v50,v102,v98 offset0:0 offset1:1\nbuffer_load_dwordx4 v1,s[36:39],s31 offen offset:2048 sc1 lds\nds_write2_b32 v51,v103,v99 offset0:0 offset1:1\nds_read2_b64 v[124:127],v60 offset0:6 offset1:7\n" \
"ds_read_b64 v[98:99],v60 offset:64\nv_add_u32 v60,2304,v60\nds_write2_b32 v52,v108,v104 offset0:0 offset1:1\nds_write2_b32 v53,v109,v105 offset0:0 offset1:1\n" \
"buffer_load_dwordx4 v1,s[36:39],s31 offen offset:3072 sc1 lds\nds_write2_b32 v54,v110,v106 offset0:0 offset1:1\nds_write2_b32 v55,v111,v107 offset0:0 offset1:1\nds_read2_b64 v[80:83],v13 offset0:0 offset1:1\n" \
"ds_read2_b64 v[84:87],v13 offset0:2 offset1:3\ns_add_u32 m0,s57,12288\ns_lshl_b32 s92,s92,4\nbuffer_load_dwordx4 v1,s[36:39],s96 offen sc1 lds\n" \
"ds_read2_b64 v[88:91],v13 offset0:4 offset1:5\ns_and_b32 s26,s32,1\nds_read2_b64 v[92:95],v13 offset0:6 offset1:7\ns_lshl3_add_u32 s92,s26,s92\n" \
"s_and_b32 s26,s33,7\ns_add_u32 s92,s92,s26\ns_lshl_b32 s92,s92,8\ns_add_u32 s100,s100,0\n" \
"s_sub_u32 s101,s100,4\nbuffer_load_dwordx4 v1,s[36:39],s96 offen offset:1024 sc1 lds\nds_read_b64 v[96:97],v13 offset:64\nds_read2_b64 v[16:19],v60 offset0:0 offset1:1\n" \
"ds_read2_b64 v[20:23],v60 offset0:2 offset1:3\ns_waitcnt lgkmcnt(6)\nv_pk_fma_f32 v[62:63],v[80:81],v[32:33],v[62:63]\nv_pk_fma_f32 v[64:65],v[82:83],v[34:35],v[64:65]\n" \
"v_pk_mul_f32 v[102:103],v[62:63],v[62:63]\nv_pk_add_f32 v[100:101],v[62:63],v[64:65]\nv_pk_fma_f32 v[102:103],v[64:65],v[64:65],v[102:103]\nbuffer_load_dwordx4 v1,s[36:39],s96 offen offset:2048 sc1 lds\n" \
"s_waitcnt lgkmcnt(4)\nv_pk_fma_f32 v[66:67],v[84:85],v[36:37],v[66:67]\nv_pk_fma_f32 v[68:69],v[86:87],v[38:39],v[68:69]\nv_pk_add_f32 v[100:101],v[100:101],v[66:67]\n" \
"v_pk_fma_f32 v[102:103],v[66:67],v[66:67],v[102:103]\nv_pk_fma_f32 v[70:71],v[88:89],v[40:41],v[70:71]\nv_pk_add_f32 v[100:101],v[100:101],v[68:69]\nv_pk_fma_f32 v[102:103],v[68:69],v[68:69],v[102:103]\n" \
"buffer_load_dwordx4 v1,s[36:39],s96 offen offset:3072 sc1 lds\nv_pk_fma_f32 v[72:73],v[90:91],v[42:43],v[72:73]\nv_pk_add_f32 v[100:101],v[100:101],v[70:71]\nv_pk_fma_f32 v[102:103],v[70:71],v[70:71],v[102:103]\n" \
"s_waitcnt lgkmcnt(1)\nv_pk_fma_f32 v[74:75],v[92:93],v[44:45],v[74:75]\nv_pk_add_f32 v[100:101],v[100:101],v[72:73]\nv_pk_fma_f32 v[102:103],v[72:73],v[72:73],v[102:103]\n" \
"v_pk_fma_f32 v[76:77],v[94:95],v[46:47],v[76:77]\ns_add_u32 m0,s57,16384\ns_min_u32 s100,s100,s101\nbuffer_load_dwordx4 v1,s[36:39],s97 offen sc1 lds\n" \
"v_pk_add_f32 v[100:101],v[100:101],v[74:75]\nv_pk_fma_f32 v[102:103],v[74:75],v[74:75],v[102:103]\nv_pk_fma_f32 v[78:79],v[96:97],v[56:57],v[78:79]\nv_pk_add_f32 v[100:101],v[100:101],v[76:77]\n" \
"v_pk_fma_f32 v[102:103],v[76:77],v[76:77],v[102:103]\nv_pk_add_f32 v[100:101],v[100:101],v[78:79]\nv_pk_fma_f32 v[102:103],v[78:79],v[78:79],v[102:103]\nbuffer_load_dwordx4 v1,s[36:39],s97 offen offset:1024 sc1 lds\n" \
"ds_read2_b64 v[24:27],v60 offset0:4 offset1:5\nv_add_f32 v100,v100,v101\nv_add_f32 v102,v102,v103\nds_read2_b64 v[28:31],v60 offset0:6 offset1:7\n" \
"ds_read_b64 v[58:59],v60 offset:64\nv_permlane16_swap_b32 v100,v102\nv_add_f32 v100,v100,v102\ns_waitcnt vmcnt(14)\n" \
"ds_read_b64_tr_b16 a[224:225],v8\nbuffer_load_dwordx4 v1,s[36:39],s97 offen offset:2048 sc1 lds\nv_add_f32_dpp v100,v100,v100 quad_perm:[1,0,3,2] row_mask:0xf bank_mask:0xf bound_ctrl:1\nds_read_b64_tr_b16 a[228:229],v9\n" \
"ds_read_b64_tr_b16 a[232:233],v10\nv_add_f32_dpp v100,v100,v100 quad_perm:[2,3,0,1] row_mask:0xf bank_mask:0xf bound_ctrl:1\nds_read_b64_tr_b16 a[236:237],v11\nds_read_b64_tr_b16 a[240:241],v8 offset:2048\n" \
"v_add_f32_dpp v100,v100,v100 row_half_mirror row_mask:0xf bank_mask:0xf bound_ctrl:1\nds_read_b64_tr_b16 a[244:245],v9 offset:2048\nbuffer_load_dwordx4 v1,s[36:39],s97 offen offset:3072 sc1 lds\nv_add_f32_dpp v100,v100,v100 row_mirror row_mask:0xf bank_mask:0xf bound_ctrl:1\n" \
"v_mul_f32 v104,0x3ae38e39,v100\nv_mul_f32 v102,0x3ae38e39,v100\nds_read_b64_tr_b16 a[248:249],v10 offset:2048\nds_read_b64_tr_b16 a[252:253],v11 offset:2048\n" \
"v_permlane16_swap_b32 v104,v102\nv_fma_f32 v102,-v104,v104,v102\ns_add_u32 m0,s57,20480\ns_lshl_b32 s101,s100,12\n" \
"buffer_load_dwordx4 v1,s[36:39],s95 offen sc1 lds\nv_max_f32 v102,0,v102\nv_add_f32 v102,0x3727c5ac,v102\nv_rsq_f32 v100,v102\n" \
"s_waitcnt vmcnt(13)\nds_read_b64_tr_b16 a[226:227],v8 offset:4096\nv_mul_f32 v101,-v104,v100\nv_pk_fma_f32 v[80:81],v[62:63],v[100:101],v[100:101] op_sel:[0,0,1] op_sel_hi:[1,0,1]\n" \
"v_pk_fma_f32 v[82:83],v[64:65],v[100:101],v[100:101] op_sel:[0,0,1] op_sel_hi:[1,0,1]\nbuffer_load_dwordx4 v1,s[36:39],s95 offen offset:1024 sc1 lds\nv_pk_fma_f32 v[84:85],v[66:67],v[100:101],v[100:101] op_sel:[0,0,1] op_sel_hi:[1,0,1]\nv_pk_fma_f32 v[86:87],v[68:69],v[100:101],v[100:101] op_sel:[0,0,1] op_sel_hi:[1,0,1]\n" \
"v_pk_fma_f32 v[88:89],v[70:71],v[100:101],v[100:101] op_sel:[0,0,1] op_sel_hi:[1,0,1]\nv_pk_fma_f32 v[90:91],v[72:73],v[100:101],v[100:101] op_sel:[0,0,1] op_sel_hi:[1,0,1]\nv_pk_fma_f32 v[92:93],v[74:75],v[100:101],v[100:101] op_sel:[0,0,1] op_sel_hi:[1,0,1]\nv_pk_fma_f32 v[94:95],v[76:77],v[100:101],v[100:101] op_sel:[0,0,1] op_sel_hi:[1,0,1]\n" \
"v_pk_fma_f32 v[96:97],v[78:79],v[100:101],v[100:101] op_sel:[0,0,1] op_sel_hi:[1,0,1]\nbuffer_load_dwordx4 v1,s[36:39],s95 offen offset:2048 sc1 lds\nds_read_b64_tr_b16 a[230:231],v9 offset:4096\nds_read_b64_tr_b16 a[234:235],v10 offset:4096\n" \
"ds_read_b64_tr_b16 a[238:239],v11 offset:4096\nds_read_b64_tr_b16 a[242:243],v8 offset:6144\nds_read_b64_tr_b16 a[246:247],v9 offset:6144\nds_read_b64_tr_b16 a[250:251],v10 offset:6144\n" \
"ds_read_b64_tr_b16 a[254:255],v11 offset:6144\ns_waitcnt vmcnt(11)\nds_read_b64_tr_b16 v[192:193],v15 offset:8192\nds_read_b64_tr_b16 v[196:197],v15 offset:8704\n" \
"buffer_load_dwordx4 v1,s[36:39],s95 offen offset:3072 sc1 lds\nds_read_b64_tr_b16 v[194:195],v15 offset:9216\nds_read_b64_tr_b16 v[198:199],v15 offset:9728\nds_read_b64_tr_b16 v[200:201],v15 offset:10240\n" \
"ds_read_b64_tr_b16 v[204:205],v15 offset:10752\nds_read_b64_tr_b16 v[202:203],v15 offset:11264\nds_read_b64_tr_b16 v[206:207],v15 offset:11776\ns_add_u32 s101,s101,s57\n" \
"s_add_u32 m0,s101,0\ns_and_b32 s101,s100,1\ns_lshr_b32 s100,s100,1\ns_lshl_b32 s100,s100,10\n" \
"s_add_u32 s100,s100,s101\ns_lshl_b32 s100,s100,7\ns_add_u32 s100,s100,s92\ns_lshr_b32 s31,s33,3\n" \
"s_add_u32 s31,s31,1\ns_sub_u32 s96,s31,4\ns_min_u32 s31,s31,s96\ns_lshl_b32 s96,s31,12\n" \
"s_add_u32 s96,s96,s57\ns_lshr_b32 s97,s33,3\ns_add_u32 s97,s97,2\ns_sub_u32 s95,s97,4\n" \
"s_min_u32 s97,s97,s95\ns_lshl_b32 s95,s97,12\ns_add_u32 s95,s95,s57\nv_readlane_b32 s24,v112,0\n" \
"v_mov_b32 v60,v61\ns_mov_b32 s25,s24\nv_pk_add_f32 v[112:113],v[112:113],s[24:25] neg_lo:[0,1] neg_hi:[0,1]\nv_pk_fma_f32 v[80:81],v[80:81],v[16:17],v[112:113]\n" \
"v_pk_mul_f32 v[102:103],v[80:81],v[80:81]\nv_pk_add_f32 v[114:115],v[114:115],s[24:25] neg_lo:[0,1] neg_hi:[0,1]\nv_pk_fma_f32 v[82:83],v[82:83],v[18:19],v[114:115]\nv_pk_add_f32 v[100:101],v[80:81],v[82:83]\n" \
"v_pk_fma_f32 v[102:103],v[82:83],v[82:83],v[102:103]\nv_pk_add_f32 v[116:117],v[116:117],s[24:25] neg_lo:[0,1] neg_hi:[0,1]\ns_waitcnt lgkmcnt(0)\nv_pk_fma_f32 v[84:85],v[84:85],v[20:21],v[116:117]\n" \
"v_pk_add_f32 v[100:101],v[100:101],v[84:85]\nv_pk_fma_f32 v[102:103],v[84:85],v[84:85],v[102:103]\nv_pk_add_f32 v[118:119],v[118:119],s[24:25] neg_lo:[0,1] neg_hi:[0,1]\nv_pk_fma_f32 v[86:87],v[86:87],v[22:23],v[118:119]\n" \
"v_pk_add_f32 v[100:101],v[100:101],v[86:87]\nv_pk_fma_f32 v[102:103],v[86:87],v[86:87],v[102:103]\nv_pk_add_f32 v[120:121],v[120:121],s[24:25] neg_lo:[0,1] neg_hi:[0,1]\nv_pk_fma_f32 v[88:89],v[88:89],v[24:25],v[120:121]\n" \
"v_pk_add_f32 v[100:101],v[100:101],v[88:89]\nv_pk_fma_f32 v[102:103],v[88:89],v[88:89],v[102:103]\nv_pk_add_f32 v[122:123],v[122:123],s[24:25] neg_lo:[0,1] neg_hi:[0,1]\nv_pk_fma_f32 v[90:91],v[90:91],v[26:27],v[122:123]\n" \
"v_pk_add_f32 v[100:101],v[100:101],v[90:91]\nv_pk_fma_f32 v[102:103],v[90:91],v[90:91],v[102:103]\ns_waitcnt vmcnt(8)\nds_read_b64_tr_b16 v[208:209],v15 offset:12288\n" \
"ds_read_b64_tr_b16 v[212:213],v15 offset:12800\nds_read_b64_tr_b16 v[210:211],v15 offset:13312\nds_read_b64_tr_b16 v[214:215],v15 offset:13824\nds_read_b64_tr_b16 v[216:217],v15 offset:14336\n" \
"ds_read_b64_tr_b16 v[220:221],v15 offset:14848\nds_read_b64_tr_b16 v[218:219],v15 offset:15360\nds_read_b64_tr_b16 v[222:223],v15 offset:15872\ns_waitcnt lgkmcnt(0)\n" \
"buffer_load_dwordx4 v4,s[40:43],s100 offen lds\nv_pk_add_f32 v[124:125],v[124:125],s[24:25] neg_lo:[0,1] neg_hi:[0,1]\nv_pk_fma_f32 v[92:93],v[92:93],v[28:29],v[124:125]\nv_pk_add_f32 v[100:101],v[100:101],v[92:93]\n" \
"v_pk_fma_f32 v[102:103],v[92:93],v[92:93],v[102:103]\nv_pk_add_f32 v[126:127],v[126:127],s[24:25] neg_lo:[0,1] neg_hi:[0,1]\nv_pk_fma_f32 v[94:95],v[94:95],v[30:31],v[126:127]\nv_pk_add_f32 v[100:101],v[100:101],v[94:95]\n" \
"buffer_load_dwordx4 v5,s[40:43],s100 offen offset:1024 lds\nv_pk_fma_f32 v[102:103],v[94:95],v[94:95],v[102:103]\nv_pk_add_f32 v[98:99],v[98:99],s[24:25] neg_lo:[0,1] neg_hi:[0,1]\nv_pk_fma_f32 v[96:97],v[96:97],v[58:59],v[98:99]\n" \
"v_pk_add_f32 v[100:101],v[100:101],v[96:97]\nv_pk_fma_f32 v[102:103],v[96:97],v[96:97],v[102:103]\nv_add_f32 v100,v100,v101\nv_add_f32 v102,v102,v103\n" \
"v_pk_add_f32 v[62:63],v[62:63],v[60:61] op_sel_hi:[1,0]\nbuffer_load_dwordx4 v6,s[40:43],s100 offen offset:2048 lds\nv_permlane16_swap_b32 v100,v102\nv_add_f32_e64 v100,v100,v102\n" \
"v_cvt_pk_bf16_f32 v32,v62,v63\nv_pk_add_f32 v[64:65],v[64:65],v[60:61] op_sel_hi:[1,0]\nv_add_f32_dpp v100,v100,v100 quad_perm:[1,0,3,2] row_mask:0xf bank_mask:0xf bound_ctrl:1\nv_cvt_pk_bf16_f32 v33,v64,v65\n" \
"v_pk_add_f32 v[66:67],v[66:67],v[60:61] op_sel_hi:[1,0]\nbuffer_load_dwordx4 v7,s[40:43],s100 offen offset:3072 lds\nv_add_f32_dpp v100,v100,v100 quad_perm:[2,3,0,1] row_mask:0xf bank_mask:0xf bound_ctrl:1\ns_add_u32 m0,s96,0\n" \
"s_and_b32 s96,s31,1\nv_add_f32_dpp v100,v100,v100 row_half_mirror row_mask:0xf bank_mask:0xf bound_ctrl:1\ns_lshr_b32 s31,s31,1\ns_lshl_b32 s31,s31,10\n" \
"v_add_f32_dpp v100,v100,v100 row_mirror row_mask:0xf bank_mask:0xf bound_ctrl:1\nv_mul_f32 v104,0x3ae38e39,v100\ns_add_u32 s31,s31,s96\ns_lshl_b32 s31,s31,7\n" \
"s_add_u32 s31,s31,s92\nbuffer_load_dwordx4 v4,s[40:43],s31 offen lds\nv_mul_f32 v102,0x3ae38e39,v100\ns_lshr_b32 s100,s33,3\n" \
"s_add_u32 s100,s100,3\nv_permlane16_swap_b32 v104,v102\nv_fma_f32 v102,-v104,v104,v102\nv_max_f32 v102,0,v102\n" \
"v_add_f32 v102,0x3727c5ac,v102\nbuffer_load_dwordx4 v5,s[40:43],s31 offen offset:1024 lds\nv_rsq_f32 v100,v102\ns_sub_u32 s101,s100,4\n" \
"v_mul_f32 v101,-v104,v100\nv_pk_fma_f32 v[80:81],v[80:81],v[100:101],v[100:101] op_sel:[0,0,1] op_sel_hi:[1,0,1]\nv_pk_fma_f32 v[82:83],v[82:83],v[100:101],v[100:101] op_sel:[0,0,1] op_sel_hi:[1,0,1]\nv_pk_fma_f32 v[84:85],v[84:85],v[100:101],v[100:101] op_sel:[0,0,1] op_sel_hi:[1,0,1]\n" \
"v_pk_fma_f32 v[86:87],v[86:87],v[100:101],v[100:101] op_sel:[0,0,1] op_sel_hi:[1,0,1]\nbuffer_load_dwordx4 v6,s[40:43],s31 offen offset:2048 lds\nv_cvt_pk_bf16_f32 v16,v80,v81\nv_cvt_pk_bf16_f32 v17,v82,v83\n" \
"v_cvt_pk_bf16_f32 v18,v84,v85\nv_cvt_pk_bf16_f32 v19,v86,v87\nv_pk_fma_f32 v[88:89],v[88:89],v[100:101],v[100:101] op_sel:[0,0,1] op_sel_hi:[1,0,1]\nv_pk_fma_f32 v[90:91],v[90:91],v[100:101],v[100:101] op_sel:[0,0,1] op_sel_hi:[1,0,1]\n" \
"v_pk_fma_f32 v[92:93],v[92:93],v[100:101],v[100:101] op_sel:[0,0,1] op_sel_hi:[1,0,1]\nbuffer_load_dwordx4 v7,s[40:43],s31 offen offset:3072 lds\nv_pk_fma_f32 v[94:95],v[94:95],v[100:101],v[100:101] op_sel:[0,0,1] op_sel_hi:[1,0,1]\nv_cvt_pk_bf16_f32 v20,v88,v89\n" \
"v_cvt_pk_bf16_f32 v21,v90,v91\nv_cvt_pk_bf16_f32 v22,v92,v93\nv_cvt_pk_bf16_f32 v23,v94,v95\nv_pk_fma_f32 v[96:97],v[96:97],v[100:101],v[100:101] op_sel:[0,0,1] op_sel_hi:[1,0,1]\n" \
"v_cvt_pk_bf16_f32 v24,v96,v97\ns_add_u32 m0,s95,0\ns_and_b32 s95,s97,1\ns_lshr_b32 s97,s97,1\n" \
"s_lshl_b32 s97,s97,10\ns_add_u32 s97,s97,s95\ns_lshl_b32 s97,s97,7\ns_add_u32 s97,s97,s92\n" \
"buffer_load_dwordx4 v4,s[40:43],s97 offen lds\ns_min_u32 s100,s100,s101\ns_lshl_b32 s101,s100,12\ns_add_u32 s101,s101,s57\n" \
"v_cvt_pk_bf16_f32 v34,v66,v67\nv_pk_add_f32 v[68:69],v[68:69],v[60:61] op_sel_hi:[1,0]\nv_cvt_pk_bf16_f32 v35,v68,v69\nv_pk_add_f32 v[70:71],v[70:71],v[60:61] op_sel_hi:[1,0]\n" \
"v_cvt_pk_bf16_f32 v36,v70,v71\nbuffer_load_dwordx4 v5,s[40:43],s97 offen offset:1024 lds\nv_pk_add_f32 v[72:73],v[72:73],v[60:61] op_sel_hi:[1,0]\nv_cvt_pk_bf16_f32 v37,v72,v73\n" \
"v_pk_add_f32 v[74:75],v[74:75],v[60:61] op_sel_hi:[1,0]\nv_cvt_pk_bf16_f32 v38,v74,v75\nv_pk_add_f32 v[76:77],v[76:77],v[60:61] op_sel_hi:[1,0]\nv_cvt_pk_bf16_f32 v39,v76,v77\n" \
"v_pk_add_f32 v[78:79],v[78:79],v[60:61] op_sel_hi:[1,0]\nbuffer_load_dwordx4 v6,s[40:43],s97 offen offset:2048 lds\nv_cvt_pk_bf16_f32 v40,v78,v79\nv_and_b32 v48,31,v0\n" \
"v_mad_u32_u24 v49,v48,56,0\nv_sub_u32 v48,v13,v49\nds_write_b128 v48,v[16:19]\nbuffer_load_dwordx4 v7,s[40:43],s97 offen offset:3072 lds\n" \
"ds_write_b128 v48,v[20:23] offset:512\ns_add_u32 m0,s101,0\ns_and_b32 s101,s100,1\ns_lshr_b32 s100,s100,1\n" \
"s_lshl_b32 s100,s100,10\ns_add_u32 s100,s100,s101\ns_lshl_b32 s100,s100,7\ns_add_u32 s100,s100,s92\n" \
"buffer_load_dwordx4 v4,s[40:43],s100 offen lds\nv_and_b32 v49,31,v0\nv_mul_u32_u24 v49,68,v49\nv_sub_u32 v49,v13,v49\n" \
"ds_write_b32 v49,v24 offset:1024\nv_bfe_u32 v50,v0,2,2\nv_mul_u32_u24 v50,4896,v50\nv_bfe_u32 v51,v0,4,2\n" \
"v_lshl_add_u32 v51,v51,2,s3\nbuffer_load_dwordx4 v5,s[40:43],s100 offen offset:1024 lds\nv_lshl_add_u32 v50,v51,5,v50\nv_and_b32 v52,3,v0\n" \
"v_lshl_add_u32 v50,v52,3,v50\nv_and_b32 v52,0xf0,v1\nv_lshl_add_u32 v51,v51,16,v52\nv_bfrev_b32 v52,1\n" \
"v_cmp_gt_u32 vcc,256,v1\nv_and_b32 v53,31,v0\nbuffer_load_dwordx4 v6,s[40:43],s100 offen offset:2048 lds\ns_waitcnt lgkmcnt(0)\n" \
"s_barrier\nds_read_b64_tr_b16 v[112:113],v50 offset:9216\nds_read_b64_tr_b16 v[114:115],v50 offset:11664\nds_read_b64_tr_b16 v[116:117],v50 offset:9728\n" \
"ds_read_b64_tr_b16 v[118:119],v50 offset:12176\nds_read_b64_tr_b16 v[120:121],v50 offset:10240\nds_read_b64_tr_b16 v[122:123],v50 offset:12688\nbuffer_load_dwordx4 v7,s[40:43],s100 offen offset:3072 lds\n" \
"v_cndmask_b32 v52,v52,v51,vcc\nv_mul_u32_u24 v53,12,v53\nv_sub_u32 v53,v12,v53\ns_lshl_b32 s24,s34,8\n" \
"s_add_u32 s24,s24,9764864\ns_add_u32 s25,s24,1048576\ns_add_u32 s26,s24,2097152\ns_add_u32 s13,s13,7405568\n" \
"s_load_dwordx2 s[4:5],s[0:1],56\ns_load_dwordx2 s[16:17],s[0:1],64\ns_waitcnt lgkmcnt(0)\nbuffer_store_dwordx4 v[112:115],v51,s[36:39],s24 offen sc1\n" \
"buffer_store_dwordx4 v[116:119],v51,s[36:39],s25 offen sc1\nbuffer_store_dwordx4 v[120:123],v52,s[36:39],s26 offen sc1\nbuffer_store_dwordx4 v[32:35],v12,s[36:39],s13 offen sc1\nbuffer_store_dwordx4 v[36:39],v12,s[36:39],s13 offen offset:512 sc1\n" \
"buffer_store_dword v40,v53,s[36:39],s13 offen offset:1024 sc1\ns_lshr_b32 s31,s33,3\ns_lshl1_add_u32 s31,s31,0\ns_sub_u32 s96,s31,6\n" \
"s_min_u32 s31,s31,s96\ns_lshl_b32 s96,s31,12\ns_add_u32 s96,s96,s57\ns_add_u32 m0,s96,0\n" \
"s_and_b32 s96,s31,1\ns_lshr_b32 s31,s31,1\ns_lshl_b32 s31,s31,10\ns_add_u32 s31,s31,s96\n" \
"s_lshl_b32 s31,s31,7\ns_add_u32 s31,s31,s92\ns_add_u32 s31,s31,262144\ns_lshr_b32 s97,s33,3\n" \
"s_lshl1_add_u32 s97,s97,1\ns_sub_u32 s95,s97,6\ns_min_u32 s97,s97,s95\ns_lshl_b32 s95,s97,12\n" \
"s_add_u32 s95,s95,s57\ns_lshr_b32 s100,s33,3\ns_lshl1_add_u32 s100,s100,2\ns_sub_u32 s101,s100,6\n" \
"s_min_u32 s100,s100,s101\ns_lshl_b32 s101,s100,12\ns_add_u32 s101,s101,s57\nv_and_b32 v64,63,v0\n" \
"s_and_b32 s25,s33,7\nv_lshrrev_b32 v66,5,v64\nv_and_b32 v67,1,v0\ns_lshl2_add_u32 s25,s25,s3\n" \
"s_and_b32 s26,s33,6\ns_lshr_b32 s27,s3,1\nv_lshl_add_u32 v66,v66,1,v67\ns_mul_i32 s24,s32,1152\n" \
"s_lshl_b32 s25,s25,5\ns_add_u32 s26,s26,s27\ns_mov_b32 s6,9216\ns_mov_b32 s7,0x20000\n" \
"v_lshlrev_b32 v68,3,v66\ns_add_u32 s25,s25,s24\nv_bfe_u32 v72,v0,1,1\nv_lshrrev_b32 v74,2,v64\n" \
"s_lshl4_add_u32 s26,s26,s24\nv_and_b32 v73,12,v0\nv_lshrrev_b32 v71,4,v64\nv_and_b32 v69,7,v0\n" \
"buffer_load_dwordx2 v[80:81],v68,s[4:7],s25 offen\nv_lshl_add_u32 v72,v72,2,s21\nv_and_b32 v74,12,v74\nv_add_u32 v71,v71,v73\n" \
"s_lshl_b32 s24,s3,4\nv_lshlrev_b32 v70,1,v69\nv_lshl_add_u32 v72,v72,4,v74\ns_mov_b32 s15,s19\n" \
"v_add_u32 v71,s24,v71\nbuffer_load_ushort v82,v70,s[4:7],s26 offen offset:1024\ns_mov_b32 s18,4096\ns_mov_b32 s19,0x20000\n" \
"v_lshlrev_b32 v74,1,v71\ns_lshl_b32 s24,s22,8\nbuffer_load_ushort v83,v74,s[16:19],s24 offen\nv_add_u32 v72,s3,v72\n" \
"buffer_load_ushort v84,v74,s[16:19],s24 offen offset:128\nv_lshlrev_b32_e64 v75,1,v72\nbuffer_load_ushort v85,v75,s[16:19],s24 offen\nv_lshl_add_u32 v73,v67,1,v73\n" \
"v_mul_u32_u24 v91,1152,v72\nv_lshl_add_u32 v91,v73,1,v91\nv_lshlrev_b32_e64 v73,2,v73\nv_add_u32 v73,163712,v73\n" \
"ds_read_b64 v[86:87],v73\ns_mul_i32 s24,s22,147456\ns_mul_i32 s25,s20,288\nv_bfe_u32 v58,v0,1,1\n" \
"s_add_u32 s24,s24,s25\nv_mul_u32_u24 v58,544,v58\nv_bfe_u32 v75,v0,2,1\ns_lshl_b32 s25,s21,6\n" \
"v_lshl_add_u32 v58,v75,8,v58\ns_add_u32 s25,s25,s24\nv_bfe_u32 v75,v0,3,2\ns_add_u32 s24,s24,256\n" \
"v_lshl_add_u32 v58,v75,6,v58\nv_add_u32 v91,s24,v91\nv_mul_u32_u24 v90,1152,v71\nv_and_b32 v74,3,v0\n" \
"v_lshl_add_u32 v58,v66,4,v58\ns_mul_i32 s24,s3,1056\nv_lshl_add_u32 v90,v74,3,v90\nv_add_u32 v58,s24,v58\n" \
"v_add_u32 v90,s25,v90\ns_and_b32 s24,s3,1\nv_mul_u32_u24 v60,288,v75\ns_lshl_b32 s24,s24,2\n" \
"s_lshr_b32 s25,s3,1\nv_lshrrev_b32 v75,5,v64\ns_add_u32 s24,s24,s25\nv_lshl_add_u32 v60,v75,6,v60\n" \
"s_lshl_b32 s24,s24,5\nv_lshl_add_u32 v60,v69,2,v60\ns_add_u32 s24,s24,4224\nv_add_u32 v60,s24,v60\n" \
"s_bfe_u32 s24,s33,0x10001\ns_add_u32 s24,s24,s15\ns_add_u32 s24,s24,17\ns_lshl2_add_u32 s24,s24,s3\n" \
"s_waitcnt lgkmcnt(0)\nv_lshrrev_b32 v86,16,v86\ns_lshl_b32 s24,s24,7\nv_mov_b32 v92,v4\n" \
"v_mov_b32 v93,v5\nv_add_u32 v74,s57,v1\nv_or_b32 v95,v86,v87\nv_lshl_add_u32 v62,v64,1,s24\n" \
"v_bfrev_b32 v76,1\nv_cmp_eq_u32 vcc,0,v64\ns_lshl2_add_u32 s24,s34,s3\ns_lshl2_add_u32 s24,s24,65536\n" \
"v_cndmask_b32 v76,v76,0,vcc\nv_mov_b32 v77,1\ns_barrier\nv_mov_b32 v57,0xc0135761\n" \
"v_add_u32 v59,0x80000,v1\nv_lshl_add_u32 v61,v64,3,s23\nv_mov_b32 v63,0\ns_waitcnt vmcnt(31)\n" \
"ds_read_b64_tr_b16 v[224:225],v15 offset:16384\nds_read_b64_tr_b16 v[228:229],v15 offset:16896\nds_read_b64_tr_b16 v[226:227],v15 offset:17408\nds_read_b64_tr_b16 v[230:231],v15 offset:17920\n" \
"ds_read_b64_tr_b16 v[232:233],v15 offset:18432\nds_read_b64_tr_b16 v[236:237],v15 offset:18944\nds_read_b64_tr_b16 v[234:235],v15 offset:19456\nds_read_b64_tr_b16 v[238:239],v15 offset:19968\n" \
"s_waitcnt vmcnt(27)\nds_read_b64_tr_b16 v[240:241],v15 offset:20480\nds_read_b64_tr_b16 v[244:245],v15 offset:20992\nds_read_b64_tr_b16 v[242:243],v15 offset:21504\n" \
"ds_read_b64_tr_b16 v[246:247],v15 offset:22016\nds_read_b64_tr_b16 v[248:249],v15 offset:22528\nv_bfe_u32 v100,v0,4,2\nds_read_b64_tr_b16 v[252:253],v15 offset:23040\n" \
"ds_read_b64_tr_b16 v[250:251],v15 offset:23552\nv_bfe_u32 v101,v0,2,2\nv_lshl_add_u32 v100,v100,3,v101\nv_bfe_u32 v101,v0,3,2\n" \
"v_lshl_add_u32 v100,v100,2,v101\nv_and_b32 v101,3,v0\nv_lshl_add_u32 v100,v100,2,v101\nv_lshlrev_b32 v100,3,v100\n" \
"v_xor_b32 v8,0,v100\nv_add_u32 v8,s57,v8\nv_mov_b32 v88,v8\ns_waitcnt vmcnt(5)\n" \
"buffer_store_dword v77,v76,s[36:39],s24 offen sc1\nds_read_b64_tr_b16 v[254:255],v15 offset:24064\nv_xor_b32 v9,32,v100\nv_add_u32 v9,s57,v9\n" \
"v_xor_b32 v10,64,v100\nv_add_u32 v10,s57,v10\nv_xor_b32 v11,96,v100\nv_add_u32 v11,s57,v11\n" \
"s_waitcnt vmcnt(5)\nv_and_b32 v53,0xffff0000,v80\nv_and_b32 v55,0xffff0000,v81\nv_lshlrev_b32 v52,16,v80\n" \
"v_lshlrev_b32 v54,16,v81\ns_waitcnt vmcnt(4)\nv_lshlrev_b32 v56,16,v82\ns_waitcnt vmcnt(2)\n" \
"v_lshl_or_b32 v89,v84,16,v83\nds_write_b128 v74,v[88:91] offset:28672\ns_waitcnt vmcnt(1)\nv_lshlrev_b32_e64 v94,16,v85\n" \
"ds_write_b128 v74,v[92:95] offset:29696\nds_read_b64_tr_b16 v[128:129],v8\nds_read_b64_tr_b16 v[130:131],v8 offset:512\nds_read_b64_tr_b16 v[132:133],v9\n" \
"ds_read_b64_tr_b16 v[134:135],v9 offset:512\nds_read_b64_tr_b16 v[136:137],v10\nds_read_b64_tr_b16 v[138:139],v10 offset:512\nds_read_b64_tr_b16 v[140:141],v11\n" \
"ds_read_b64_tr_b16 v[142:143],v11 offset:512\nds_read_b64_tr_b16 v[144:145],v8 offset:4096\nds_read_b64_tr_b16 v[146:147],v8 offset:4608\nds_read_b64_tr_b16 v[148:149],v9 offset:4096\n" \
"ds_read_b64_tr_b16 v[150:151],v9 offset:4608\nds_read_b64_tr_b16 v[152:153],v10 offset:4096\nds_read_b64_tr_b16 v[154:155],v10 offset:4608\nds_read_b64_tr_b16 v[156:157],v11 offset:4096\n" \
"ds_read_b64_tr_b16 v[158:159],v11 offset:4608\nds_read_b64_tr_b16 v[160:161],v8 offset:8192\nds_read_b64_tr_b16 v[162:163],v8 offset:8704\nds_read_b64_tr_b16 v[164:165],v9 offset:8192\n" \
"ds_read_b64_tr_b16 v[166:167],v9 offset:8704\nds_read_b64_tr_b16 v[168:169],v10 offset:8192\nds_read_b64_tr_b16 v[170:171],v10 offset:8704\nds_read_b64_tr_b16 v[172:173],v11 offset:8192\n" \
"ds_read_b64_tr_b16 v[174:175],v11 offset:8704\nds_read_b64_tr_b16 v[176:177],v8 offset:12288\nds_read_b64_tr_b16 v[178:179],v8 offset:12800\nds_read_b64_tr_b16 v[180:181],v9 offset:12288\n" \
"ds_read_b64_tr_b16 v[182:183],v9 offset:12800\nds_read_b64_tr_b16 v[184:185],v10 offset:12288\nds_read_b64_tr_b16 v[186:187],v10 offset:12800\nds_read_b64_tr_b16 v[188:189],v11 offset:12288\n" \
"ds_read_b64_tr_b16 v[190:191],v11 offset:12800\ns_waitcnt lgkmcnt(0)\nbuffer_load_dwordx4 v4,s[40:43],s31 offen lds\nbuffer_load_dwordx4 v5,s[40:43],s31 offen offset:1024 lds\n" \
"buffer_load_dwordx4 v6,s[40:43],s31 offen offset:2048 lds\nbuffer_load_dwordx4 v7,s[40:43],s31 offen offset:3072 lds\ns_add_u32 m0,s95,0\ns_and_b32 s95,s97,1\n" \
"s_lshr_b32 s97,s97,1\ns_lshl_b32 s97,s97,10\ns_add_u32 s97,s97,s95\ns_lshl_b32 s97,s97,7\n" \
"s_add_u32 s97,s97,s92\ns_add_u32 s97,s97,262144\nbuffer_load_dwordx4 v4,s[40:43],s97 offen lds\nbuffer_load_dwordx4 v5,s[40:43],s97 offen offset:1024 lds\n" \
"buffer_load_dwordx4 v6,s[40:43],s97 offen offset:2048 lds\nbuffer_load_dwordx4 v7,s[40:43],s97 offen offset:3072 lds\ns_add_u32 m0,s101,0\ns_and_b32 s101,s100,1\n" \
"s_lshr_b32 s100,s100,1\ns_lshl_b32 s100,s100,10\ns_add_u32 s100,s100,s101\ns_lshl_b32 s100,s100,7\n" \
"s_add_u32 s100,s100,s92\ns_add_u32 s100,s100,262144\nbuffer_load_dwordx4 v4,s[40:43],s100 offen lds\nbuffer_load_dwordx4 v5,s[40:43],s100 offen offset:1024 lds\n" \
"buffer_load_dwordx4 v6,s[40:43],s100 offen offset:2048 lds\nbuffer_load_dwordx4 v7,s[40:43],s100 offen offset:3072 lds\ns_lshr_b32 s31,s33,3\ns_lshl1_add_u32 s31,s31,3\n" \
"s_sub_u32 s96,s31,6\ns_min_u32 s31,s31,s96\ns_lshl_b32 s96,s31,12\ns_add_u32 s96,s96,s57\n" \
"s_add_u32 m0,s96,0\ns_and_b32 s96,s31,1\ns_lshr_b32 s31,s31,1\ns_lshl_b32 s31,s31,10\n" \
"s_add_u32 s31,s31,s96\ns_lshl_b32 s31,s31,7\ns_add_u32 s31,s31,s92\ns_add_u32 s31,s31,262144\n" \
"buffer_load_dwordx4 v4,s[40:43],s31 offen lds\nbuffer_load_dwordx4 v5,s[40:43],s31 offen offset:1024 lds\nbuffer_load_dwordx4 v6,s[40:43],s31 offen offset:2048 lds\nbuffer_load_dwordx4 v7,s[40:43],s31 offen offset:3072 lds\n" \
"s_lshr_b32 s97,s33,3\ns_lshl1_add_u32 s97,s97,4\ns_sub_u32 s95,s97,6\ns_min_u32 s97,s97,s95\n" \
"s_lshl_b32 s95,s97,12\ns_add_u32 s95,s95,s57\ns_add_u32 m0,s95,0\ns_and_b32 s95,s97,1\n" \
"s_lshr_b32 s97,s97,1\ns_lshl_b32 s97,s97,10\ns_add_u32 s97,s97,s95\ns_lshl_b32 s97,s97,7\n" \
"s_add_u32 s97,s97,s92\ns_add_u32 s97,s97,262144\nbuffer_load_dwordx4 v4,s[40:43],s97 offen lds\nbuffer_load_dwordx4 v5,s[40:43],s97 offen offset:1024 lds\n" \
"buffer_load_dwordx4 v6,s[40:43],s97 offen offset:2048 lds\nbuffer_load_dwordx4 v7,s[40:43],s97 offen offset:3072 lds\ns_lshr_b32 s100,s33,3\ns_lshl1_add_u32 s100,s100,5\n" \
"s_sub_u32 s101,s100,6\ns_min_u32 s100,s100,s101\ns_lshl_b32 s101,s100,12\ns_add_u32 s101,s101,s57\n" \
"s_add_u32 m0,s101,0\ns_and_b32 s101,s100,1\ns_lshr_b32 s100,s100,1\ns_lshl_b32 s100,s100,10\n" \
"s_add_u32 s100,s100,s101\ns_lshl_b32 s100,s100,7\ns_add_u32 s100,s100,s92\ns_add_u32 s100,s100,262144\n" \
"buffer_load_dwordx4 v4,s[40:43],s100 offen lds\nbuffer_load_dwordx4 v5,s[40:43],s100 offen offset:1024 lds\nbuffer_load_dwordx4 v6,s[40:43],s100 offen offset:2048 lds\nbuffer_load_dwordx4 v7,s[40:43],s100 offen offset:3072 lds\n" \
".Lgemm_%=:\ns_add_u32 s4,s36,s48\ns_addc_u32 s5,s37,0\ns_add_u32 s6,s36,s49\n" \
"s_addc_u32 s7,s37,0\ns_add_u32 s8,s36,s50\ns_addc_u32 s9,s37,0\ns_add_u32 s10,s36,s51\n" \
"s_addc_u32 s11,s37,0\ns_add_u32 s12,s36,s68\ns_addc_u32 s13,s37,0\ns_add_u32 s14,s36,s69\n" \
"s_addc_u32 s15,s37,0\ns_add_u32 s16,s36,s70\ns_addc_u32 s17,s37,0\ns_memrealtime s[94:95]\n" \
"global_load_dwordx4 v[64:67],v59,s[4:5] sc1\nglobal_load_dwordx4 v[68:71],v59,s[4:5] offset:1024 sc1\nglobal_load_dwordx4 v[72:75],v59,s[4:5] offset:2048 sc1\nglobal_load_dwordx4 v[76:79],v59,s[4:5] offset:3072 sc1\n" \
"global_load_dwordx4 v[80:83],v59,s[6:7] sc1\nglobal_load_dwordx4 v[84:87],v59,s[6:7] offset:1024 sc1\nglobal_load_dwordx4 v[88:91],v59,s[6:7] offset:2048 sc1\nglobal_load_dwordx4 v[92:95],v59,s[6:7] offset:3072 sc1\n" \
"global_load_dwordx4 v[96:99],v59,s[8:9] sc1\nglobal_load_dwordx4 v[100:103],v59,s[8:9] offset:1024 sc1\nglobal_load_dwordx4 v[104:107],v59,s[8:9] offset:2048 sc1\nglobal_load_dwordx4 v[108:111],v59,s[8:9] offset:3072 sc1\n" \
"global_load_dwordx4 v[112:115],v59,s[10:11] sc1\nglobal_load_dwordx4 v[116:119],v59,s[10:11] offset:1024 sc1\nglobal_load_dwordx4 v[120:123],v59,s[10:11] offset:2048 sc1\nglobal_load_dwordx4 v[124:127],v59,s[10:11] offset:3072 sc1\n" \
"s_branch .Lz0_%=\n.Lzr_%=:\nv_and_b32 v0,0x3ff,v59\nv_lshlrev_b32 v0,0,v0\n" \
"v_bfrev_b32 v1,1\ns_waitcnt lgkmcnt(0)\n.Lh_%=:\nbuffer_load_dwordx4 v[4:7],v0,s[36:39],s87 offen sc1\n" \
"s_memrealtime s[96:97]\ns_waitcnt vmcnt(0) lgkmcnt(0)\nv_bitop3_b32 v4,v4,v5,v6 bitop3:0x80\nv_and_b32 v4,v4,v7\n" \
"v_cmp_ne_u32 vcc,0,v4\nv_max3_u32 v2,v64,v68,v72\nv_max3_u32 v2,v2,v76,v80\nv_max3_u32 v2,v2,v84,v88\n" \
"v_max3_u32 v2,v2,v92,v96\nv_max3_u32 v2,v2,v100,v104\nv_max3_u32 v2,v2,v108,v112\nv_max3_u32 v2,v2,v116,v120\n" \
"v_max_u32 v2,v2,v124\nv_cndmask_b32 v0,v0,v1,vcc\nv_cmp_eq_u32 vcc,-1,v2\nv_cmp_ne_u32 s[100:101],v0,v1\n" \
"s_sub_u32 s96,s96,s94\ns_cbranch_vccz .Lj_%=\nv_cmpx_eq_u32 vcc,-1,v64\nglobal_load_dwordx4 v[64:67],v59,s[4:5] sc1\n" \
"s_mov_b64 exec,-1\nv_cmpx_eq_u32 vcc,-1,v68\nglobal_load_dwordx4 v[68:71],v59,s[4:5] offset:1024 sc1\ns_mov_b64 exec,-1\n" \
"v_cmpx_eq_u32 vcc,-1,v72\nglobal_load_dwordx4 v[72:75],v59,s[4:5] offset:2048 sc1\ns_mov_b64 exec,-1\nv_cmpx_eq_u32 vcc,-1,v76\n" \
"global_load_dwordx4 v[76:79],v59,s[4:5] offset:3072 sc1\ns_mov_b64 exec,-1\nv_cmpx_eq_u32 vcc,-1,v80\nglobal_load_dwordx4 v[80:83],v59,s[6:7] sc1\n" \
"s_mov_b64 exec,-1\nv_cmpx_eq_u32 vcc,-1,v84\nglobal_load_dwordx4 v[84:87],v59,s[6:7] offset:1024 sc1\ns_mov_b64 exec,-1\n" \
"v_cmpx_eq_u32 vcc,-1,v88\nglobal_load_dwordx4 v[88:91],v59,s[6:7] offset:2048 sc1\ns_mov_b64 exec,-1\nv_cmpx_eq_u32 vcc,-1,v92\n" \
"global_load_dwordx4 v[92:95],v59,s[6:7] offset:3072 sc1\ns_mov_b64 exec,-1\nv_cmpx_eq_u32 vcc,-1,v96\nglobal_load_dwordx4 v[96:99],v59,s[8:9] sc1\n" \
"s_mov_b64 exec,-1\nv_cmpx_eq_u32 vcc,-1,v100\nglobal_load_dwordx4 v[100:103],v59,s[8:9] offset:1024 sc1\ns_mov_b64 exec,-1\n" \
"v_cmpx_eq_u32 vcc,-1,v104\nglobal_load_dwordx4 v[104:107],v59,s[8:9] offset:2048 sc1\ns_mov_b64 exec,-1\nv_cmpx_eq_u32 vcc,-1,v108\n" \
"global_load_dwordx4 v[108:111],v59,s[8:9] offset:3072 sc1\ns_mov_b64 exec,-1\nv_cmpx_eq_u32 vcc,-1,v112\nglobal_load_dwordx4 v[112:115],v59,s[10:11] sc1\n" \
"s_mov_b64 exec,-1\nv_cmpx_eq_u32 vcc,-1,v116\nglobal_load_dwordx4 v[116:119],v59,s[10:11] offset:1024 sc1\ns_mov_b64 exec,-1\n" \
"v_cmpx_eq_u32 vcc,-1,v120\nglobal_load_dwordx4 v[120:123],v59,s[10:11] offset:2048 sc1\ns_mov_b64 exec,-1\nv_cmpx_eq_u32 vcc,-1,v124\n" \
"global_load_dwordx4 v[124:127],v59,s[10:11] offset:3072 sc1\ns_mov_b64 exec,-1\n.Lj_%=:\ns_cmp_gt_u32 s96,0x4c4b40\n" \
"s_cbranch_scc1 .Lg_%=\ns_cmp_lg_u64 s[100:101],0\ns_cbranch_scc1 .Lh_%=\n.Lg_%=:\n" \
"s_waitcnt vmcnt(0)\n.Lgo_%=:\ns_mov_b32 s33,s86\ns_nop 0\n" \
"s_mov_b32 m0,s56\ns_sub_u32 s21,s60,128\ns_and_b32 s95,s89,7\ns_andn2_b32 s101,s89,7\n" \
"s_mul_i32 s95,s95,409600\ns_add_u32 s95,s95,s52\ns_mov_b32 s86,0x11111111\ns_mov_b32 s87,0x11111111\n" \
"v_add_u32 v59,s85,v59\ns_add_u32 s100,s90,73728\ns_mov_b32 s35,73728\ns_mov_b32 s34,s91\n" \
"s_waitcnt lgkmcnt(0)\ns_cmp_eq_u32 s84,0\ns_cbranch_scc0 .Ls_%=\n.Lf_%=:\n" \
"s_waitcnt vmcnt(15)\nv_mfma_f32_16x16x32_bf16 v[0:3],v[64:67],a[0:3],0\nds_read_b32 v44,v60\ns_waitcnt vmcnt(14)\n" \
"v_mfma_f32_16x16x32_bf16 v[0:3],v[68:71],a[16:19],v[0:3]\nds_read_b32 v45,v60 offset:9568\nv_add_f32 v40,1.0,v40\nv_mfma_f32_16x16x32_bf16 v[4:7],v[64:67],a[4:7],0\n" \
"ds_read_b32 v46,v60 offset:19136\nv_add_u32 v59,0x10000,v59\nv_mfma_f32_16x16x32_bf16 v[4:7],v[68:71],a[20:23],v[4:7]\nds_read_b32 v47,v60 offset:28704\n" \
"v_and_b32 v59,0x7ffff,v59\nv_mfma_f32_16x16x32_bf16 v[8:11],v[64:67],a[8:11],0\nv_add_f32 v41,1.0,v41\nv_add_f32 v42,1.0,v42\n" \
"v_mfma_f32_16x16x32_bf16 v[8:11],v[68:71],a[24:27],v[8:11]\nv_add_f32 v43,1.0,v43\nds_write_addtid_b32 v20 offset:5344\nv_mfma_f32_16x16x32_bf16 v[12:15],v[64:67],a[12:15],0\n" \
"v_rcp_f32 v40,v40\nv_mfma_f32_16x16x32_bf16 v[12:15],v[68:71],a[28:31],v[12:15]\nv_rcp_f32 v41,v41\nv_mfma_f32_16x16x32_bf16 v[16:19],v[64:67],v[192:195],0\n" \
"global_load_dwordx4 v[64:67],v59,s[4:5] sc1\nds_write_addtid_b32 v21 offset:5888\nv_mfma_f32_16x16x32_bf16 v[16:19],v[68:71],v[196:199],v[16:19]\nds_write_addtid_b32 v22 offset:5600\n" \
"s_waitcnt vmcnt(14)\nv_mfma_f32_16x16x32_bf16 v[0:3],v[72:75],a[32:35],v[0:3]\nds_write_addtid_b32 v23 offset:6144\ns_waitcnt vmcnt(13)\n" \
"v_mfma_f32_16x16x32_bf16 v[0:3],v[76:79],a[48:51],v[0:3]\nv_rcp_f32 v42,v42\nv_mfma_f32_16x16x32_bf16 v[4:7],v[72:75],a[36:39],v[4:7]\nv_rcp_f32 v43,v43\n" \
"v_mfma_f32_16x16x32_bf16 v[4:7],v[76:79],a[52:55],v[4:7]\nglobal_load_dwordx4 v[68:71],v59,s[4:5] offset:1024 sc1\nv_mul_f32 v36,v36,v40\nv_mfma_f32_16x16x32_bf16 v[8:11],v[72:75],a[40:43],v[8:11]\n" \
"v_mul_f32 v37,v37,v41\nv_mul_f32 v38,v38,v42\nv_mfma_f32_16x16x32_bf16 v[8:11],v[76:79],a[56:59],v[8:11]\nv_mul_f32 v39,v39,v43\n" \
"v_cvt_pk_bf16_f32 v40,v36,v37\nv_mfma_f32_16x16x32_bf16 v[12:15],v[72:75],a[44:47],v[12:15]\nds_write_addtid_b32 v24 offset:6400\nv_cvt_pk_bf16_f32 v41,v38,v39\n" \
"v_mfma_f32_16x16x32_bf16 v[12:15],v[76:79],a[60:63],v[12:15]\nds_write_addtid_b32 v25 offset:6944\nbuffer_store_dwordx2 v[40:41],v61,s[36:39],s52 offen sc1\nv_mfma_f32_16x16x32_bf16 v[16:19],v[72:75],v[200:203],v[16:19]\n" \
"global_load_dwordx4 v[72:75],v59,s[4:5] offset:2048 sc1\nds_write_addtid_b32 v26 offset:6656\nv_mfma_f32_16x16x32_bf16 v[16:19],v[76:79],v[204:207],v[16:19]\nds_write_addtid_b32 v27 offset:7200\n" \
"s_waitcnt vmcnt(15)\nv_mfma_f32_16x16x32_bf16 v[0:3],v[80:83],a[64:67],v[0:3]\nds_write_addtid_b32 v28 offset:7456\ns_waitcnt vmcnt(14)\n" \
"v_mfma_f32_16x16x32_bf16 v[0:3],v[84:87],a[80:83],v[0:3]\nds_write_addtid_b32 v29 offset:8000\ns_waitcnt lgkmcnt(10)\nv_mfma_f32_16x16x32_bf16 v[4:7],v[80:83],a[68:71],v[4:7]\n" \
"ds_write_addtid_b32 v30 offset:7712\nv_add_f32 v44,v44,v45\nv_mfma_f32_16x16x32_bf16 v[4:7],v[84:87],a[84:87],v[4:7]\nglobal_load_dwordx4 v[76:79],v59,s[4:5] offset:3072 sc1\n" \
"ds_write_addtid_b32 v31 offset:8256\nv_mfma_f32_16x16x32_bf16 v[8:11],v[80:83],a[72:75],v[8:11]\nds_write_addtid_b32 v32 offset:8512\nv_add_f32 v46,v46,v47\n" \
"v_mfma_f32_16x16x32_bf16 v[8:11],v[84:87],a[88:91],v[8:11]\nds_write_addtid_b32 v33 offset:9056\nv_add_f32 v44,v44,v56\nv_mfma_f32_16x16x32_bf16 v[12:15],v[80:83],a[76:79],v[12:15]\n" \
"ds_write_addtid_b32 v34 offset:8768\nv_add_f32 v44,v44,v46\nv_mfma_f32_16x16x32_bf16 v[12:15],v[84:87],a[92:95],v[12:15]\nds_write_addtid_b32 v35 offset:9312\n" \
"v_mul_f32 v45,v44,v44\nv_mfma_f32_16x16x32_bf16 v[16:19],v[80:83],v[208:211],v[16:19]\nglobal_load_dwordx4 v[80:83],v59,s[6:7] sc1\nv_fma_f32 v45,v45,s55,v57\n" \
"v_mfma_f32_16x16x32_bf16 v[16:19],v[84:87],v[212:215],v[16:19]\nv_mul_f32 v45,v44,v45\ns_waitcnt vmcnt(15)\nv_mfma_f32_16x16x32_bf16 v[0:3],v[88:91],a[96:99],v[0:3]\n" \
"s_waitcnt vmcnt(14)\nv_mfma_f32_16x16x32_bf16 v[0:3],v[92:95],a[112:115],v[0:3]\nv_exp_f32 v45,v45\nv_mfma_f32_16x16x32_bf16 v[4:7],v[88:91],a[100:103],v[4:7]\n" \
"v_add_f32 v45,1.0,v45\nv_mfma_f32_16x16x32_bf16 v[4:7],v[92:95],a[116:119],v[4:7]\nglobal_load_dwordx4 v[84:87],v59,s[6:7] offset:1024 sc1\nv_mfma_f32_16x16x32_bf16 v[8:11],v[88:91],a[104:107],v[8:11]\n" \
"s_waitcnt lgkmcnt(0)\ns_barrier\nv_mfma_f32_16x16x32_bf16 v[8:11],v[92:95],a[120:123],v[8:11]\nv_rcp_f32 v45,v45\n" \
"v_mfma_f32_16x16x32_bf16 v[12:15],v[88:91],a[108:111],v[12:15]\nv_mul_f32 v44,v44,v45\nv_cvt_pk_bf16_f32 v45,v44,v44\nv_mfma_f32_16x16x32_bf16 v[12:15],v[92:95],a[124:127],v[12:15]\n" \
"buffer_store_short v45,v62,s[36:39],s52 offen sc1\nds_read_b128 v[36:39],v58 offset:5344\nv_mfma_f32_16x16x32_bf16 v[16:19],v[88:91],v[216:219],v[16:19]\nglobal_load_dwordx4 v[88:91],v59,s[6:7] offset:2048 sc1\n" \
"ds_read_b128 v[40:43],v58 offset:14912\nv_mfma_f32_16x16x32_bf16 v[16:19],v[92:95],v[220:223],v[16:19]\nds_read_b128 v[44:47],v58 offset:24480\ns_waitcnt vmcnt(16)\n" \
"v_mfma_f32_16x16x32_bf16 v[0:3],v[96:99],a[128:131],v[0:3]\nds_read_b128 v[48:51],v58 offset:34048\ns_waitcnt vmcnt(15)\nv_mfma_f32_16x16x32_bf16 v[0:3],v[100:103],a[144:147],v[0:3]\n" \
"v_mfma_f32_16x16x32_bf16 v[4:7],v[96:99],a[132:135],v[4:7]\nv_mfma_f32_16x16x32_bf16 v[4:7],v[100:103],a[148:151],v[4:7]\nglobal_load_dwordx4 v[92:95],v59,s[6:7] offset:3072 sc1\nv_mfma_f32_16x16x32_bf16 v[8:11],v[96:99],a[136:139],v[8:11]\n" \
"v_mfma_f32_16x16x32_bf16 v[8:11],v[100:103],a[152:155],v[8:11]\ns_cmp_eq_u32 s52,s83\ns_cbranch_scc1 .Lxq5_%=\n.Lyq5_%=:\n" \
"v_mfma_f32_16x16x32_bf16 v[12:15],v[96:99],a[140:143],v[12:15]\ns_waitcnt lgkmcnt(2)\nv_add_f32 v36,v36,v40\nv_mfma_f32_16x16x32_bf16 v[12:15],v[100:103],a[156:159],v[12:15]\n" \
"v_add_f32 v37,v37,v41\nv_add_f32 v38,v38,v42\nv_mfma_f32_16x16x32_bf16 v[16:19],v[96:99],v[224:227],v[16:19]\nglobal_load_dwordx4 v[96:99],v59,s[8:9] sc1\n" \
"v_add_f32 v39,v39,v43\nv_mfma_f32_16x16x32_bf16 v[16:19],v[100:103],v[228:231],v[16:19]\nv_add_f32 v36,v36,v52\ns_waitcnt vmcnt(16)\n" \
"v_mfma_f32_16x16x32_bf16 v[0:3],v[104:107],a[160:163],v[0:3]\nv_add_f32 v37,v37,v53\ns_waitcnt vmcnt(15)\nv_mfma_f32_16x16x32_bf16 v[0:3],v[108:111],a[176:179],v[0:3]\n" \
"v_add_f32 v38,v38,v54\nv_add_f32 v39,v39,v55\nv_mfma_f32_16x16x32_bf16 v[4:7],v[104:107],a[164:167],v[4:7]\ns_waitcnt lgkmcnt(0)\n" \
"v_add_f32 v44,v44,v48\nv_mfma_f32_16x16x32_bf16 v[4:7],v[108:111],a[180:183],v[4:7]\nglobal_load_dwordx4 v[100:103],v59,s[8:9] offset:1024 sc1\nv_add_f32 v45,v45,v49\n" \
"v_mfma_f32_16x16x32_bf16 v[8:11],v[104:107],a[168:171],v[8:11]\nv_add_f32 v46,v46,v50\nv_add_f32 v47,v47,v51\nv_mfma_f32_16x16x32_bf16 v[8:11],v[108:111],a[184:187],v[8:11]\n" \
"v_add_f32 v36,v36,v44\nv_add_f32 v37,v37,v45\nv_mfma_f32_16x16x32_bf16 v[12:15],v[104:107],a[172:175],v[12:15]\nv_add_f32 v38,v38,v46\n" \
"v_add_f32 v39,v39,v47\nv_mfma_f32_16x16x32_bf16 v[12:15],v[108:111],a[188:191],v[12:15]\nv_mul_f32 v40,v36,v36\nv_mul_f32 v41,v37,v37\n" \
"v_mfma_f32_16x16x32_bf16 v[16:19],v[104:107],v[232:235],v[16:19]\nglobal_load_dwordx4 v[104:107],v59,s[8:9] offset:2048 sc1\nv_mul_f32 v42,v38,v38\nv_mfma_f32_16x16x32_bf16 v[16:19],v[108:111],v[236:239],v[16:19]\n" \
"v_mul_f32 v43,v39,v39\ns_waitcnt vmcnt(16)\nv_mfma_f32_16x16x32_bf16 v[0:3],v[112:115],a[192:195],v[0:3]\nv_fma_f32 v40,v40,s55,v57\n" \
"s_waitcnt vmcnt(15)\nv_mfma_f32_16x16x32_bf16 v[0:3],v[116:119],a[208:211],v[0:3]\nv_fma_f32 v41,v41,s55,v57\nv_fma_f32 v42,v42,s55,v57\n" \
"v_mfma_f32_16x16x32_bf16 v[4:7],v[112:115],a[196:199],v[4:7]\nv_fma_f32 v43,v43,s55,v57\nv_mul_f32 v40,v36,v40\nv_mfma_f32_16x16x32_bf16 v[4:7],v[116:119],a[212:215],v[4:7]\n" \
"global_load_dwordx4 v[108:111],v59,s[8:9] offset:3072 sc1\nv_mul_f32 v41,v37,v41\nv_mfma_f32_16x16x32_bf16 v[8:11],v[112:115],a[200:203],v[8:11]\nv_mul_f32 v42,v38,v42\n" \
"v_mul_f32 v43,v39,v43\nv_mfma_f32_16x16x32_bf16 v[8:11],v[116:119],a[216:219],v[8:11]\nv_exp_f32 v40,v40\nv_mfma_f32_16x16x32_bf16 v[12:15],v[112:115],a[204:207],v[12:15]\n" \
"v_exp_f32 v41,v41\nv_mfma_f32_16x16x32_bf16 v[12:15],v[116:119],a[220:223],v[12:15]\nv_exp_f32 v42,v42\nv_mfma_f32_16x16x32_bf16 v[16:19],v[112:115],v[240:243],v[16:19]\n" \
"global_load_dwordx4 v[112:115],v59,s[10:11] sc1\nv_mfma_f32_16x16x32_bf16 v[16:19],v[116:119],v[244:247],v[16:19]\ns_waitcnt vmcnt(16)\nv_mfma_f32_16x16x32_bf16 v[0:3],v[120:123],a[224:227],v[0:3]\n" \
"s_waitcnt vmcnt(15)\nv_mfma_f32_16x16x32_bf16 v[0:3],v[124:127],a[240:243],v[0:3]\nv_exp_f32 v43,v43\nv_mfma_f32_16x16x32_bf16 v[4:7],v[120:123],a[228:231],v[4:7]\n" \
"v_add_f32 v40,1.0,v40\nv_add_f32 v41,1.0,v41\nv_mfma_f32_16x16x32_bf16 v[4:7],v[124:127],a[244:247],v[4:7]\nglobal_load_dwordx4 v[116:119],v59,s[10:11] offset:1024 sc1\n" \
"v_add_f32 v42,1.0,v42\nv_mfma_f32_16x16x32_bf16 v[8:11],v[120:123],a[232:235],v[8:11]\nv_add_f32 v43,1.0,v43\nv_mfma_f32_16x16x32_bf16 v[8:11],v[124:127],a[248:251],v[8:11]\n" \
"v_rcp_f32 v40,v40\nv_mfma_f32_16x16x32_bf16 v[12:15],v[120:123],a[236:239],v[12:15]\ns_cmp_eq_u32 s52,s95\ns_cbranch_scc1 .Lxt5_%=\n" \
".Lyt5_%=:\nv_mfma_f32_16x16x32_bf16 v[12:15],v[124:127],a[252:255],v[12:15]\nv_rcp_f32 v41,v41\nv_mfma_f32_16x16x32_bf16 v[16:19],v[120:123],v[248:251],v[16:19]\n" \
"global_load_dwordx4 v[120:123],v59,s[10:11] offset:2048 sc1\nv_rcp_f32 v42,v42\nv_mfma_f32_16x16x32_bf16 v[16:19],v[124:127],v[252:255],v[16:19]\nglobal_load_dwordx4 v[124:127],v59,s[10:11] offset:3072 sc1\n" \
"v_rcp_f32 v43,v43\ns_add_u32 s52,s52,409600\ns_cmp_eq_u32 s52,s53\ns_cbranch_scc1 .Lls_%=\n" \
".Ls_%=:\ns_waitcnt vmcnt(17)\nv_mfma_f32_16x16x32_bf16 v[20:23],v[64:67],a[0:3],0\nv_mul_f32 v36,v36,v40\n" \
"s_waitcnt vmcnt(16)\nv_mfma_f32_16x16x32_bf16 v[20:23],v[68:71],a[16:19],v[20:23]\nv_add_u32 v59,0x10000,v59\nv_mul_f32 v37,v37,v41\n" \
"v_mfma_f32_16x16x32_bf16 v[24:27],v[64:67],a[4:7],0\nv_and_b32 v59,0x7ffff,v59\nv_mul_f32 v38,v38,v42\nv_mfma_f32_16x16x32_bf16 v[24:27],v[68:71],a[20:23],v[24:27]\n" \
"v_mul_f32 v39,v39,v43\nv_cvt_pk_bf16_f32 v40,v36,v37\nv_mfma_f32_16x16x32_bf16 v[28:31],v[64:67],a[8:11],0\nds_write_addtid_b32 v0\n" \
"v_cvt_pk_bf16_f32 v41,v38,v39\nv_mfma_f32_16x16x32_bf16 v[28:31],v[68:71],a[24:27],v[28:31]\nds_write_addtid_b32 v1 offset:544\nbuffer_store_dwordx2 v[40:41],v61,s[36:39],s52 offen sc1\n" \
"v_mfma_f32_16x16x32_bf16 v[32:35],v[64:67],a[12:15],0\nglobal_load_dwordx4 v[64:67],v59,s[4:5] sc1\nds_write_addtid_b32 v2 offset:256\nv_mfma_f32_16x16x32_bf16 v[32:35],v[68:71],a[28:31],v[32:35]\n" \
"ds_write_addtid_b32 v3 offset:800\ns_waitcnt vmcnt(16)\nv_mfma_f32_16x16x32_bf16 v[20:23],v[72:75],a[32:35],v[20:23]\nds_write_addtid_b32 v4 offset:1056\n" \
"s_waitcnt vmcnt(15)\nv_mfma_f32_16x16x32_bf16 v[20:23],v[76:79],a[48:51],v[20:23]\nds_write_addtid_b32 v5 offset:1600\nv_mfma_f32_16x16x32_bf16 v[24:27],v[72:75],a[36:39],v[24:27]\n" \
"global_load_dwordx4 v[68:71],v59,s[4:5] offset:1024 sc1\nds_write_addtid_b32 v6 offset:1312\nv_mfma_f32_16x16x32_bf16 v[24:27],v[76:79],a[52:55],v[24:27]\nds_write_addtid_b32 v7 offset:1856\n" \
"v_mfma_f32_16x16x32_bf16 v[28:31],v[72:75],a[40:43],v[28:31]\nds_write_addtid_b32 v8 offset:2112\nv_mfma_f32_16x16x32_bf16 v[28:31],v[76:79],a[56:59],v[28:31]\nds_write_addtid_b32 v9 offset:2656\n" \
"v_mfma_f32_16x16x32_bf16 v[32:35],v[72:75],a[44:47],v[32:35]\nglobal_load_dwordx4 v[72:75],v59,s[4:5] offset:2048 sc1\nds_write_addtid_b32 v10 offset:2368\nv_mfma_f32_16x16x32_bf16 v[32:35],v[76:79],a[60:63],v[32:35]\n" \
"ds_write_addtid_b32 v11 offset:2912\ns_waitcnt vmcnt(16)\nv_mfma_f32_16x16x32_bf16 v[20:23],v[80:83],a[64:67],v[20:23]\nds_write_addtid_b32 v12 offset:3168\n" \
"s_waitcnt vmcnt(15)\nv_mfma_f32_16x16x32_bf16 v[20:23],v[84:87],a[80:83],v[20:23]\nds_write_addtid_b32 v13 offset:3712\nv_mfma_f32_16x16x32_bf16 v[24:27],v[80:83],a[68:71],v[24:27]\n" \
"global_load_dwordx4 v[76:79],v59,s[4:5] offset:3072 sc1\nds_write_addtid_b32 v14 offset:3424\nv_mfma_f32_16x16x32_bf16 v[24:27],v[84:87],a[84:87],v[24:27]\nds_write_addtid_b32 v15 offset:3968\n" \
"v_mfma_f32_16x16x32_bf16 v[28:31],v[80:83],a[72:75],v[28:31]\nds_write_addtid_b32 v16 offset:4224\nv_mfma_f32_16x16x32_bf16 v[28:31],v[84:87],a[88:91],v[28:31]\nds_write_addtid_b32 v17 offset:4512\n" \
"v_mfma_f32_16x16x32_bf16 v[32:35],v[80:83],a[76:79],v[32:35]\nglobal_load_dwordx4 v[80:83],v59,s[6:7] sc1\nds_write_addtid_b32 v18 offset:4800\nv_mfma_f32_16x16x32_bf16 v[32:35],v[84:87],a[92:95],v[32:35]\n" \
"ds_write_addtid_b32 v19 offset:5088\ns_waitcnt vmcnt(15)\nv_mfma_f32_16x16x32_bf16 v[20:23],v[88:91],a[96:99],v[20:23]\ns_waitcnt vmcnt(14)\n" \
"v_mfma_f32_16x16x32_bf16 v[20:23],v[92:95],a[112:115],v[20:23]\nv_mfma_f32_16x16x32_bf16 v[24:27],v[88:91],a[100:103],v[24:27]\nglobal_load_dwordx4 v[84:87],v59,s[6:7] offset:1024 sc1\nv_mfma_f32_16x16x32_bf16 v[24:27],v[92:95],a[116:119],v[24:27]\n" \
"v_mfma_f32_16x16x32_bf16 v[28:31],v[88:91],a[104:107],v[28:31]\nv_mfma_f32_16x16x32_bf16 v[28:31],v[92:95],a[120:123],v[28:31]\ns_waitcnt lgkmcnt(0)\ns_barrier\n" \
"v_mfma_f32_16x16x32_bf16 v[32:35],v[88:91],a[108:111],v[32:35]\nglobal_load_dwordx4 v[88:91],v59,s[6:7] offset:2048 sc1\nds_read_b128 v[36:39],v58\nv_mfma_f32_16x16x32_bf16 v[32:35],v[92:95],a[124:127],v[32:35]\n" \
"ds_read_b128 v[40:43],v58 offset:9568\ns_waitcnt vmcnt(15)\nv_mfma_f32_16x16x32_bf16 v[20:23],v[96:99],a[128:131],v[20:23]\nds_read_b128 v[44:47],v58 offset:19136\n" \
"s_waitcnt vmcnt(14)\nv_mfma_f32_16x16x32_bf16 v[20:23],v[100:103],a[144:147],v[20:23]\nds_read_b128 v[48:51],v58 offset:28704\nv_mfma_f32_16x16x32_bf16 v[24:27],v[96:99],a[132:135],v[24:27]\n" \
"global_load_dwordx4 v[92:95],v59,s[6:7] offset:3072 sc1\nv_mfma_f32_16x16x32_bf16 v[24:27],v[100:103],a[148:151],v[24:27]\nv_mfma_f32_16x16x32_bf16 v[28:31],v[96:99],a[136:139],v[28:31]\ns_cmp_eq_u32 s52,s83\n" \
"s_cbranch_scc1 .Lxq4_%=\n.Lyq4_%=:\nv_mfma_f32_16x16x32_bf16 v[28:31],v[100:103],a[152:155],v[28:31]\nv_mfma_f32_16x16x32_bf16 v[32:35],v[96:99],a[140:143],v[32:35]\n" \
"global_load_dwordx4 v[96:99],v59,s[8:9] sc1\nv_mfma_f32_16x16x32_bf16 v[32:35],v[100:103],a[156:159],v[32:35]\ns_waitcnt lgkmcnt(2)\ns_waitcnt vmcnt(15)\n" \
"v_mfma_f32_16x16x32_bf16 v[20:23],v[104:107],a[160:163],v[20:23]\nv_add_f32 v36,v36,v40\ns_waitcnt vmcnt(14)\nv_mfma_f32_16x16x32_bf16 v[20:23],v[108:111],a[176:179],v[20:23]\n" \
"v_add_f32 v37,v37,v41\nv_add_f32 v38,v38,v42\nv_mfma_f32_16x16x32_bf16 v[24:27],v[104:107],a[164:167],v[24:27]\nglobal_load_dwordx4 v[100:103],v59,s[8:9] offset:1024 sc1\n" \
"v_add_f32 v39,v39,v43\nv_mfma_f32_16x16x32_bf16 v[24:27],v[108:111],a[180:183],v[24:27]\nv_add_f32 v36,v36,v52\nv_add_f32 v37,v37,v53\n" \
"v_mfma_f32_16x16x32_bf16 v[28:31],v[104:107],a[168:171],v[28:31]\nv_add_f32 v38,v38,v54\nv_add_f32 v39,v39,v55\nv_mfma_f32_16x16x32_bf16 v[28:31],v[108:111],a[184:187],v[28:31]\n" \
"s_waitcnt lgkmcnt(0)\nv_add_f32 v44,v44,v48\nv_mfma_f32_16x16x32_bf16 v[32:35],v[104:107],a[172:175],v[32:35]\nglobal_load_dwordx4 v[104:107],v59,s[8:9] offset:2048 sc1\n" \
"v_add_f32 v45,v45,v49\nv_mfma_f32_16x16x32_bf16 v[32:35],v[108:111],a[188:191],v[32:35]\nv_add_f32 v46,v46,v50\ns_waitcnt vmcnt(15)\n" \
"v_mfma_f32_16x16x32_bf16 v[20:23],v[112:115],a[192:195],v[20:23]\nv_add_f32 v47,v47,v51\ns_waitcnt vmcnt(14)\nv_mfma_f32_16x16x32_bf16 v[20:23],v[116:119],a[208:211],v[20:23]\n" \
"v_add_f32 v36,v36,v44\nv_add_f32 v37,v37,v45\nv_mfma_f32_16x16x32_bf16 v[24:27],v[112:115],a[196:199],v[24:27]\nglobal_load_dwordx4 v[108:111],v59,s[8:9] offset:3072 sc1\n" \
"v_add_f32 v38,v38,v46\nv_mfma_f32_16x16x32_bf16 v[24:27],v[116:119],a[212:215],v[24:27]\nv_add_f32 v39,v39,v47\nv_mul_f32 v40,v36,v36\n" \
"v_mfma_f32_16x16x32_bf16 v[28:31],v[112:115],a[200:203],v[28:31]\nv_mul_f32 v41,v37,v37\nv_mul_f32 v42,v38,v38\nv_mfma_f32_16x16x32_bf16 v[28:31],v[116:119],a[216:219],v[28:31]\n" \
"v_mul_f32 v43,v39,v39\nv_fma_f32 v40,v40,s55,v57\nv_mfma_f32_16x16x32_bf16 v[32:35],v[112:115],a[204:207],v[32:35]\nglobal_load_dwordx4 v[112:115],v59,s[10:11] sc1\n" \
"v_fma_f32 v41,v41,s55,v57\nv_mfma_f32_16x16x32_bf16 v[32:35],v[116:119],a[220:223],v[32:35]\nv_fma_f32 v42,v42,s55,v57\ns_waitcnt vmcnt(15)\n" \
"v_mfma_f32_16x16x32_bf16 v[20:23],v[120:123],a[224:227],v[20:23]\nv_fma_f32 v43,v43,s55,v57\ns_waitcnt vmcnt(14)\nv_mfma_f32_16x16x32_bf16 v[20:23],v[124:127],a[240:243],v[20:23]\n" \
"v_mul_f32 v40,v36,v40\nv_mul_f32 v41,v37,v41\nv_mfma_f32_16x16x32_bf16 v[24:27],v[120:123],a[228:231],v[24:27]\nglobal_load_dwordx4 v[116:119],v59,s[10:11] offset:1024 sc1\n" \
"v_mul_f32 v42,v38,v42\nv_mfma_f32_16x16x32_bf16 v[24:27],v[124:127],a[244:247],v[24:27]\nv_mul_f32 v43,v39,v43\nv_mfma_f32_16x16x32_bf16 v[28:31],v[120:123],a[232:235],v[28:31]\n" \
"s_cmp_eq_u32 s52,s95\ns_cbranch_scc1 .Lxt4_%=\n.Lyt4_%=:\nv_mfma_f32_16x16x32_bf16 v[28:31],v[124:127],a[248:251],v[28:31]\n" \
"v_exp_f32 v40,v40\nv_mfma_f32_16x16x32_bf16 v[32:35],v[120:123],a[236:239],v[32:35]\nglobal_load_dwordx4 v[120:123],v59,s[10:11] offset:2048 sc1\nv_exp_f32 v41,v41\n" \
"v_mfma_f32_16x16x32_bf16 v[32:35],v[124:127],a[252:255],v[32:35]\nglobal_load_dwordx4 v[124:127],v59,s[10:11] offset:3072 sc1\nv_exp_f32 v42,v42\nv_exp_f32 v43,v43\n" \
"s_add_u32 s52,s52,409600\ns_cmp_eq_u32 s52,s53\ns_cbranch_scc0 .Lf_%=\nv_mfma_f32_16x16x32_bf16 v[0:3],v[64:67],a[0:3],0\n" \
"ds_read_b32 v44,v60\ns_waitcnt vmcnt(16)\nv_mfma_f32_16x16x32_bf16 v[0:3],v[68:71],a[16:19],v[0:3]\nds_read_b32 v45,v60 offset:9568\n" \
"v_and_b32 v59,0x3ff,v59\nv_mfma_f32_16x16x32_bf16 v[4:7],v[64:67],a[4:7],0\nds_read_b32 v46,v60 offset:19136\nds_write_addtid_b32 v20 offset:5344\n" \
"v_mfma_f32_16x16x32_bf16 v[4:7],v[68:71],a[20:23],v[4:7]\nds_read_b32 v47,v60 offset:28704\nds_write_addtid_b32 v21 offset:5888\nv_mfma_f32_16x16x32_bf16 v[8:11],v[64:67],a[8:11],0\n" \
"s_add_u32 s4,s12,409600\ns_addc_u32 s5,s13,0\nv_mfma_f32_16x16x32_bf16 v[8:11],v[68:71],a[24:27],v[8:11]\ns_add_u32 s6,s14,409600\n" \
"s_addc_u32 s7,s15,0\nv_mfma_f32_16x16x32_bf16 v[12:15],v[64:67],a[12:15],0\ns_add_u32 s8,s16,409600\ns_addc_u32 s9,s17,0\n" \
"v_mfma_f32_16x16x32_bf16 v[12:15],v[68:71],a[28:31],v[12:15]\nds_write_addtid_b32 v22 offset:5600\nds_write_addtid_b32 v23 offset:6144\nv_mfma_f32_16x16x32_bf16 v[16:19],v[64:67],v[192:195],0\n" \
"ds_write_addtid_b32 v24 offset:6400\nds_write_addtid_b32 v25 offset:6944\nv_mfma_f32_16x16x32_bf16 v[16:19],v[68:71],v[196:199],v[16:19]\nds_write_addtid_b32 v26 offset:6656\n" \
"s_waitcnt vmcnt(15)\nv_mfma_f32_16x16x32_bf16 v[0:3],v[72:75],a[32:35],v[0:3]\nv_add_u32 v71,s57,v59\ns_waitcnt vmcnt(14)\n" \
"v_mfma_f32_16x16x32_bf16 v[0:3],v[76:79],a[48:51],v[0:3]\ns_waitcnt vmcnt(8)\nv_cmp_eq_u32 vcc,0,v63\nv_mfma_f32_16x16x32_bf16 v[4:7],v[72:75],a[36:39],v[4:7]\n" \
"ds_read_b32 v64,v71 offset:28672\nds_read_b64 v[68:69],v71 offset:29696\nv_mfma_f32_16x16x32_bf16 v[4:7],v[76:79],a[52:55],v[4:7]\ns_and_b64 vcc,vcc,s[86:87]\n" \
"s_cbranch_scc1 .LsaF_%=\n.LbaF_%=:\nv_mfma_f32_16x16x32_bf16 v[8:11],v[72:75],a[40:43],v[8:11]\nds_write_addtid_b32 v27 offset:7200\n" \
"ds_write_addtid_b32 v28 offset:7456\nv_mfma_f32_16x16x32_bf16 v[8:11],v[76:79],a[56:59],v[8:11]\nds_write_addtid_b32 v29 offset:8000\nds_write_addtid_b32 v30 offset:7712\n" \
"v_mfma_f32_16x16x32_bf16 v[12:15],v[72:75],a[44:47],v[12:15]\ns_waitcnt lgkmcnt(4)\nv_xor_b32 v65,32,v64\nv_mfma_f32_16x16x32_bf16 v[12:15],v[76:79],a[60:63],v[12:15]\n" \
"v_xor_b32 v66,64,v64\nv_xor_b32 v67,96,v64\nv_mfma_f32_16x16x32_bf16 v[16:19],v[72:75],v[200:203],v[16:19]\nv_add_u32 v70,0xf800,v68\n" \
"v_add_u32 v71,0xf800,v69\nv_mfma_f32_16x16x32_bf16 v[16:19],v[76:79],v[204:207],v[16:19]\nds_write_addtid_b32 v31 offset:8256\nv_mfma_f32_16x16x32_bf16 v[0:3],v[80:83],a[64:67],v[0:3]\n" \
"global_load_dwordx4 v[72:75],v59,s[12:13] sc1\nv_mfma_f32_16x16x32_bf16 v[0:3],v[84:87],a[80:83],v[0:3]\nds_write_addtid_b32 v32 offset:8512\nds_write_addtid_b32 v33 offset:9056\n" \
"v_mfma_f32_16x16x32_bf16 v[4:7],v[80:83],a[68:71],v[4:7]\nds_write_addtid_b32 v34 offset:8768\nds_write_addtid_b32 v35 offset:9312\nv_mfma_f32_16x16x32_bf16 v[4:7],v[84:87],a[84:87],v[4:7]\n" \
"s_add_u32 m0,s57,24576\nds_read_b64_tr_b16 a[0:1],v64\nv_mfma_f32_16x16x32_bf16 v[8:11],v[80:83],a[72:75],v[8:11]\nbuffer_load_dwordx4 v68,s[40:43],s21 offen lds\n" \
"ds_read_b64_tr_b16 a[2:3],v64 offset:512\nv_mfma_f32_16x16x32_bf16 v[8:11],v[84:87],a[88:91],v[8:11]\nds_read_b64_tr_b16 a[4:5],v65\nds_read_b64_tr_b16 a[6:7],v65 offset:512\n" \
"v_mfma_f32_16x16x32_bf16 v[12:15],v[80:83],a[76:79],v[12:15]\nbuffer_load_dwordx4 v69,s[40:43],s21 offen offset:1024 lds\nds_read_b64_tr_b16 a[8:9],v66\nv_mfma_f32_16x16x32_bf16 v[12:15],v[84:87],a[92:95],v[12:15]\n" \
"ds_read_b64_tr_b16 a[10:11],v66 offset:512\nds_read_b64_tr_b16 a[12:13],v67\nv_mfma_f32_16x16x32_bf16 v[16:19],v[80:83],v[208:211],v[16:19]\nbuffer_load_dwordx4 v70,s[40:43],s21 offen offset:2048 lds\n" \
"ds_read_b64_tr_b16 a[14:15],v67 offset:512\nv_mfma_f32_16x16x32_bf16 v[16:19],v[84:87],v[212:215],v[16:19]\nds_read_b64_tr_b16 a[16:17],v64 offset:4096\ns_waitcnt vmcnt(15)\n" \
"v_mfma_f32_16x16x32_bf16 v[0:3],v[88:91],a[96:99],v[0:3]\nbuffer_load_dwordx4 v71,s[40:43],s21 offen offset:3072 lds\nv_mfma_f32_16x16x32_bf16 v[0:3],v[92:95],a[112:115],v[0:3]\ns_waitcnt lgkmcnt(0)\n" \
"s_barrier\nv_mfma_f32_16x16x32_bf16 v[4:7],v[88:91],a[100:103],v[4:7]\nglobal_load_dwordx4 v[76:79],v59,s[12:13] offset:1024 sc1\nds_read_b64_tr_b16 a[18:19],v64 offset:4608\n" \
"v_mfma_f32_16x16x32_bf16 v[4:7],v[92:95],a[116:119],v[4:7]\ns_add_u32 m0,s57,0\nv_mfma_f32_16x16x32_bf16 v[8:11],v[88:91],a[104:107],v[8:11]\nbuffer_load_dwordx4 v68,s[40:43],s60 offen lds\n" \
"ds_read_b64_tr_b16 a[20:21],v65 offset:4096\nv_mfma_f32_16x16x32_bf16 v[8:11],v[92:95],a[120:123],v[8:11]\nds_read_b64_tr_b16 a[22:23],v65 offset:4608\nds_read_b64_tr_b16 a[24:25],v66 offset:4096\n" \
"v_mfma_f32_16x16x32_bf16 v[12:15],v[88:91],a[108:111],v[12:15]\nbuffer_load_dwordx4 v69,s[40:43],s60 offen offset:1024 lds\nds_read_b64_tr_b16 a[26:27],v66 offset:4608\nv_mfma_f32_16x16x32_bf16 v[12:15],v[92:95],a[124:127],v[12:15]\n" \
"ds_read_b64_tr_b16 a[28:29],v67 offset:4096\nds_read_b64_tr_b16 a[30:31],v67 offset:4608\nv_mfma_f32_16x16x32_bf16 v[16:19],v[88:91],v[216:219],v[16:19]\nbuffer_load_dwordx4 v70,s[40:43],s60 offen offset:2048 lds\n" \
"ds_read_b64_tr_b16 a[32:33],v64 offset:8192\nv_mfma_f32_16x16x32_bf16 v[16:19],v[92:95],v[220:223],v[16:19]\nds_read_b64_tr_b16 a[34:35],v64 offset:8704\ns_waitcnt vmcnt(16)\n" \
"v_mfma_f32_16x16x32_bf16 v[0:3],v[96:99],a[128:131],v[0:3]\nbuffer_load_dwordx4 v71,s[40:43],s60 offen offset:3072 lds\ns_waitcnt vmcnt(16)\nv_mfma_f32_16x16x32_bf16 v[0:3],v[100:103],a[144:147],v[0:3]\n" \
"ds_read_b64_tr_b16 a[36:37],v65 offset:8192\nds_read_b64_tr_b16 a[38:39],v65 offset:8704\nv_mfma_f32_16x16x32_bf16 v[4:7],v[96:99],a[132:135],v[4:7]\nglobal_load_dwordx4 v[80:83],v59,s[12:13] offset:2048 sc1\n" \
"ds_read_b64_tr_b16 a[40:41],v66 offset:8192\nv_mfma_f32_16x16x32_bf16 v[4:7],v[100:103],a[148:151],v[4:7]\ns_waitcnt lgkmcnt(5)\ns_add_u32 m0,s57,4096\n" \
"v_mfma_f32_16x16x32_bf16 v[8:11],v[96:99],a[136:139],v[8:11]\nbuffer_load_dwordx4 v68,s[40:43],s61 offen lds\nds_read_b64_tr_b16 a[42:43],v66 offset:8704\nv_mfma_f32_16x16x32_bf16 v[8:11],v[100:103],a[152:155],v[8:11]\n" \
"ds_read_b64_tr_b16 a[44:45],v67 offset:8192\nds_read_b64_tr_b16 a[46:47],v67 offset:8704\nv_mfma_f32_16x16x32_bf16 v[12:15],v[96:99],a[140:143],v[12:15]\nbuffer_load_dwordx4 v69,s[40:43],s61 offen offset:1024 lds\n" \
"ds_read_b64_tr_b16 a[48:49],v64 offset:12288\nv_mfma_f32_16x16x32_bf16 v[12:15],v[100:103],a[156:159],v[12:15]\nds_read_b64_tr_b16 a[50:51],v64 offset:12800\nds_read_b64_tr_b16 a[52:53],v65 offset:12288\n" \
"v_mfma_f32_16x16x32_bf16 v[16:19],v[96:99],v[224:227],v[16:19]\nbuffer_load_dwordx4 v70,s[40:43],s61 offen offset:2048 lds\nds_read_b64_tr_b16 a[54:55],v65 offset:12800\nv_mfma_f32_16x16x32_bf16 v[16:19],v[100:103],v[228:231],v[16:19]\n" \
"ds_read_b64_tr_b16 a[56:57],v66 offset:12288\ns_waitcnt vmcnt(19)\nv_mfma_f32_16x16x32_bf16 v[0:3],v[104:107],a[160:163],v[0:3]\nbuffer_load_dwordx4 v71,s[40:43],s61 offen offset:3072 lds\n" \
"s_waitcnt vmcnt(19)\nv_mfma_f32_16x16x32_bf16 v[0:3],v[108:111],a[176:179],v[0:3]\nds_read_b64_tr_b16 a[58:59],v66 offset:12800\nds_read_b64_tr_b16 a[60:61],v67 offset:12288\n" \
"v_mfma_f32_16x16x32_bf16 v[4:7],v[104:107],a[164:167],v[4:7]\nglobal_load_dwordx4 v[84:87],v59,s[14:15] sc1\nds_read_b64_tr_b16 a[62:63],v67 offset:12800\nv_mfma_f32_16x16x32_bf16 v[4:7],v[108:111],a[180:183],v[4:7]\n" \
"s_waitcnt lgkmcnt(8)\ns_add_u32 m0,s57,8192\nv_mfma_f32_16x16x32_bf16 v[8:11],v[104:107],a[168:171],v[8:11]\nbuffer_load_dwordx4 v68,s[40:43],s62 offen lds\n" \
"ds_read_b64_tr_b16 a[64:65],v64 offset:16384\nv_mfma_f32_16x16x32_bf16 v[8:11],v[108:111],a[184:187],v[8:11]\nds_read_b64_tr_b16 a[66:67],v64 offset:16896\nds_read_b64_tr_b16 a[68:69],v65 offset:16384\n" \
"v_mfma_f32_16x16x32_bf16 v[12:15],v[104:107],a[172:175],v[12:15]\nbuffer_load_dwordx4 v69,s[40:43],s62 offen offset:1024 lds\nds_read_b64_tr_b16 a[70:71],v65 offset:16896\nv_mfma_f32_16x16x32_bf16 v[12:15],v[108:111],a[188:191],v[12:15]\n" \
"ds_read_b64_tr_b16 a[72:73],v66 offset:16384\nds_read_b64_tr_b16 a[74:75],v66 offset:16896\nv_mfma_f32_16x16x32_bf16 v[16:19],v[104:107],v[232:235],v[16:19]\nbuffer_load_dwordx4 v70,s[40:43],s62 offen offset:2048 lds\n" \
"ds_read_b64_tr_b16 a[76:77],v67 offset:16384\nv_mfma_f32_16x16x32_bf16 v[16:19],v[108:111],v[236:239],v[16:19]\nds_read_b64_tr_b16 a[78:79],v67 offset:16896\ns_waitcnt vmcnt(22)\n" \
"v_mfma_f32_16x16x32_bf16 v[0:3],v[112:115],a[192:195],v[0:3]\nbuffer_load_dwordx4 v71,s[40:43],s62 offen offset:3072 lds\ns_waitcnt vmcnt(22)\nv_mfma_f32_16x16x32_bf16 v[0:3],v[116:119],a[208:211],v[0:3]\n" \
"ds_read_b64_tr_b16 a[80:81],v64 offset:20480\nds_read_b64_tr_b16 a[82:83],v64 offset:20992\nv_mfma_f32_16x16x32_bf16 v[4:7],v[112:115],a[196:199],v[4:7]\nglobal_load_dwordx4 v[88:91],v59,s[14:15] offset:1024 sc1\n" \
"ds_read_b64_tr_b16 a[84:85],v65 offset:20480\nv_mfma_f32_16x16x32_bf16 v[4:7],v[116:119],a[212:215],v[4:7]\ns_waitcnt lgkmcnt(11)\ns_add_u32 m0,s57,12288\n" \
"v_mfma_f32_16x16x32_bf16 v[8:11],v[112:115],a[200:203],v[8:11]\nbuffer_load_dwordx4 v68,s[40:43],s63 offen lds\nds_read_b64_tr_b16 a[86:87],v65 offset:20992\nv_mfma_f32_16x16x32_bf16 v[8:11],v[116:119],a[216:219],v[8:11]\n" \
"ds_read_b64_tr_b16 a[88:89],v66 offset:20480\nds_read_b64_tr_b16 a[90:91],v66 offset:20992\nv_mfma_f32_16x16x32_bf16 v[12:15],v[112:115],a[204:207],v[12:15]\nbuffer_load_dwordx4 v69,s[40:43],s63 offen offset:1024 lds\n" \
"ds_read_b64_tr_b16 a[92:93],v67 offset:20480\nv_mfma_f32_16x16x32_bf16 v[12:15],v[116:119],a[220:223],v[12:15]\nds_read_b64_tr_b16 a[94:95],v67 offset:20992\nv_add_f32 v40,1.0,v40\n" \
"v_mfma_f32_16x16x32_bf16 v[16:19],v[112:115],v[240:243],v[16:19]\nbuffer_load_dwordx4 v70,s[40:43],s63 offen offset:2048 lds\nv_add_f32 v41,1.0,v41\nv_mfma_f32_16x16x32_bf16 v[16:19],v[116:119],v[244:247],v[16:19]\n" \
"v_add_f32 v42,1.0,v42\ns_waitcnt vmcnt(25)\nv_mfma_f32_16x16x32_bf16 v[0:3],v[120:123],a[224:227],v[0:3]\nbuffer_load_dwordx4 v71,s[40:43],s63 offen offset:3072 lds\n" \
"s_waitcnt vmcnt(25)\nv_mfma_f32_16x16x32_bf16 v[0:3],v[124:127],a[240:243],v[0:3]\ns_waitcnt vmcnt(20)\nds_read_b64_tr_b16 a[96:97],v64 offset:24576\n" \
"ds_read_b64_tr_b16 a[98:99],v64 offset:25088\nv_mfma_f32_16x16x32_bf16 v[4:7],v[120:123],a[228:231],v[4:7]\nglobal_load_dwordx4 v[92:95],v59,s[14:15] offset:2048 sc1\nds_read_b64_tr_b16 a[100:101],v65 offset:24576\n" \
"v_mfma_f32_16x16x32_bf16 v[4:7],v[124:127],a[244:247],v[4:7]\ns_waitcnt lgkmcnt(11)\ns_add_u32 m0,s57,16384\nv_mfma_f32_16x16x32_bf16 v[8:11],v[120:123],a[232:235],v[8:11]\n" \
"buffer_load_dwordx4 v68,s[40:43],s64 offen lds\nds_read_b64_tr_b16 a[102:103],v65 offset:25088\nv_mfma_f32_16x16x32_bf16 v[8:11],v[124:127],a[248:251],v[8:11]\nds_read_b64_tr_b16 a[104:105],v66 offset:24576\n" \
"ds_read_b64_tr_b16 a[106:107],v66 offset:25088\nv_mfma_f32_16x16x32_bf16 v[12:15],v[120:123],a[236:239],v[12:15]\nbuffer_load_dwordx4 v69,s[40:43],s64 offen offset:1024 lds\nds_read_b64_tr_b16 a[108:109],v67 offset:24576\n" \
"v_mfma_f32_16x16x32_bf16 v[12:15],v[124:127],a[252:255],v[12:15]\nds_read_b64_tr_b16 a[110:111],v67 offset:25088\nv_add_f32 v43,1.0,v43\nv_mfma_f32_16x16x32_bf16 v[16:19],v[120:123],v[248:251],v[16:19]\n" \
"buffer_load_dwordx4 v70,s[40:43],s64 offen offset:2048 lds\ns_waitcnt lgkmcnt(15)\nv_mfma_f32_16x16x32_bf16 v[16:19],v[124:127],v[252:255],v[16:19]\nv_add_f32 v44,v44,v45\n" \
"v_mfma_f32_16x16x32_bf16 v[208:211],v[128:131],v[72:75],0\nbuffer_load_dwordx4 v71,s[40:43],s64 offen offset:3072 lds\ns_waitcnt vmcnt(24)\nv_mfma_f32_16x16x32_bf16 v[208:211],v[160:163],v[76:79],v[208:211]\n" \
"s_mov_b32 m0,s56\ns_nop 0\nds_write_addtid_b32 v0\nv_mfma_f32_16x16x32_bf16 v[212:215],v[132:135],v[72:75],0\n" \
"global_load_dwordx4 v[108:111],v59,s[4:5] sc1\nds_write_addtid_b32 v1 offset:544\nv_mfma_f32_16x16x32_bf16 v[212:215],v[164:167],v[76:79],v[212:215]\nds_write_addtid_b32 v2 offset:256\n" \
"ds_write_addtid_b32 v3 offset:800\nv_mfma_f32_16x16x32_bf16 v[216:219],v[136:139],v[72:75],0\nglobal_load_dwordx4 v[112:115],v59,s[4:5] offset:1024 sc1\nds_write_addtid_b32 v4 offset:1056\n" \
"v_mfma_f32_16x16x32_bf16 v[216:219],v[168:171],v[76:79],v[216:219]\nds_write_addtid_b32 v5 offset:1600\nds_write_addtid_b32 v6 offset:1312\nv_mfma_f32_16x16x32_bf16 v[220:223],v[140:143],v[72:75],0\n" \
"global_load_dwordx4 v[116:119],v59,s[4:5] offset:2048 sc1\nds_write_addtid_b32 v7 offset:1856\nv_mfma_f32_16x16x32_bf16 v[220:223],v[172:175],v[76:79],v[220:223]\nds_write_addtid_b32 v8 offset:2112\n" \
"ds_write_addtid_b32 v9 offset:2656\nv_mfma_f32_16x16x32_bf16 v[224:227],v[144:147],v[72:75],0\nds_write_addtid_b32 v10 offset:2368\nds_write_addtid_b32 v11 offset:2912\n" \
"v_mfma_f32_16x16x32_bf16 v[224:227],v[176:179],v[76:79],v[224:227]\nds_write_addtid_b32 v12 offset:3168\nds_write_addtid_b32 v13 offset:3712\nv_mfma_f32_16x16x32_bf16 v[228:231],v[148:151],v[72:75],0\n" \
"ds_write_addtid_b32 v14 offset:3424\nds_write_addtid_b32 v15 offset:3968\nv_mfma_f32_16x16x32_bf16 v[228:231],v[180:183],v[76:79],v[228:231]\nds_write_addtid_b32 v16 offset:4224\n" \
"ds_write_addtid_b32 v17 offset:4512\nv_mfma_f32_16x16x32_bf16 v[232:235],v[152:155],v[72:75],0\nds_write_addtid_b32 v18 offset:4800\nds_write_addtid_b32 v19 offset:5088\n" \
"v_mfma_f32_16x16x32_bf16 v[232:235],v[184:187],v[76:79],v[232:235]\ns_waitcnt lgkmcnt(15)\ns_add_u32 m0,s57,20480\nv_mfma_f32_16x16x32_bf16 v[236:239],v[156:159],v[72:75],0\n" \
"buffer_load_dwordx4 v68,s[40:43],s65 offen lds\ns_waitcnt vmcnt(24)\nv_mfma_f32_16x16x32_bf16 v[236:239],v[188:191],v[76:79],v[236:239]\ns_waitcnt vmcnt(23)\n" \
"v_mfma_f32_16x16x32_bf16 v[208:211],a[0:3],v[80:83],v[208:211]\ns_waitcnt vmcnt(18)\nv_mfma_f32_16x16x32_bf16 v[208:211],a[32:35],v[84:87],v[208:211]\nbuffer_load_dwordx4 v69,s[40:43],s65 offen offset:1024 lds\n" \
"ds_read_b64_tr_b16 a[112:113],v64\nv_mfma_f32_16x16x32_bf16 v[212:215],a[4:7],v[80:83],v[212:215]\ns_waitcnt lgkmcnt(0)\ns_barrier\n" \
"v_mfma_f32_16x16x32_bf16 v[212:215],a[36:39],v[84:87],v[212:215]\nbuffer_load_dwordx4 v70,s[40:43],s65 offen offset:2048 lds\nds_read_b64_tr_b16 a[114:115],v64 offset:512\nv_mfma_f32_16x16x32_bf16 v[216:219],a[8:11],v[80:83],v[216:219]\n" \
"ds_read_b64_tr_b16 a[116:117],v65\nds_read_b64_tr_b16 a[118:119],v65 offset:512\nv_mfma_f32_16x16x32_bf16 v[216:219],a[40:43],v[84:87],v[216:219]\nbuffer_load_dwordx4 v71,s[40:43],s65 offen offset:3072 lds\n" \
"ds_read_b64_tr_b16 a[120:121],v66\nv_mfma_f32_16x16x32_bf16 v[220:223],a[12:15],v[80:83],v[220:223]\nds_read_b64_tr_b16 a[122:123],v66 offset:512\nds_read_b64_tr_b16 a[124:125],v67\n" \
"v_mfma_f32_16x16x32_bf16 v[220:223],a[44:47],v[84:87],v[220:223]\nglobal_load_dwordx4 v[120:123],v59,s[6:7] sc1\nv_mfma_f32_16x16x32_bf16 v[224:227],a[16:19],v[80:83],v[224:227]\nds_read_b64_tr_b16 a[126:127],v67 offset:512\n" \
"v_mfma_f32_16x16x32_bf16 v[224:227],a[48:51],v[84:87],v[224:227]\ns_add_u32 m0,s57,24576\nv_mfma_f32_16x16x32_bf16 v[228:231],a[20:23],v[80:83],v[228:231]\nbuffer_load_dwordx4 v68,s[40:43],s66 offen lds\n" \
"v_mfma_f32_16x16x32_bf16 v[228:231],a[52:55],v[84:87],v[228:231]\nds_read_b64_tr_b16 a[128:129],v64 offset:4096\nds_read_b64_tr_b16 a[130:131],v64 offset:4608\nv_mfma_f32_16x16x32_bf16 v[232:235],a[24:27],v[80:83],v[232:235]\n" \
"buffer_load_dwordx4 v69,s[40:43],s66 offen offset:1024 lds\nds_read_b64_tr_b16 a[132:133],v65 offset:4096\nv_mfma_f32_16x16x32_bf16 v[232:235],a[56:59],v[84:87],v[232:235]\nds_read_b64_tr_b16 a[134:135],v65 offset:4608\n" \
"ds_read_b64_tr_b16 a[136:137],v66 offset:4096\nv_mfma_f32_16x16x32_bf16 v[236:239],a[28:31],v[80:83],v[236:239]\nbuffer_load_dwordx4 v70,s[40:43],s66 offen offset:2048 lds\nds_read_b64_tr_b16 a[138:139],v66 offset:4608\n" \
"v_mfma_f32_16x16x32_bf16 v[236:239],a[60:63],v[84:87],v[236:239]\ns_waitcnt vmcnt(20)\nv_mfma_f32_16x16x32_bf16 v[208:211],a[64:67],v[88:91],v[208:211]\ns_waitcnt vmcnt(15)\n" \
"v_mfma_f32_16x16x32_bf16 v[208:211],a[96:99],v[92:95],v[208:211]\nbuffer_load_dwordx4 v71,s[40:43],s66 offen offset:3072 lds\nds_read_b64_tr_b16 a[140:141],v67 offset:4096\nv_mfma_f32_16x16x32_bf16 v[212:215],a[68:71],v[88:91],v[212:215]\n" \
"ds_read_b64_tr_b16 a[142:143],v67 offset:4608\nv_mfma_f32_16x16x32_bf16 v[212:215],a[100:103],v[92:95],v[212:215]\nglobal_load_dwordx4 v[124:127],v59,s[6:7] offset:1024 sc1\nds_read_b64_tr_b16 a[144:145],v64 offset:8192\n" \
"v_mfma_f32_16x16x32_bf16 v[216:219],a[72:75],v[88:91],v[216:219]\nds_read_b64_tr_b16 a[146:147],v64 offset:8704\nds_read_b64_tr_b16 a[148:149],v65 offset:8192\nv_mfma_f32_16x16x32_bf16 v[216:219],a[104:107],v[92:95],v[216:219]\n" \
"global_load_dwordx4 v[192:195],v59,s[6:7] offset:2048 sc1\nds_read_b64_tr_b16 a[150:151],v65 offset:8704\nv_mfma_f32_16x16x32_bf16 v[220:223],a[76:79],v[88:91],v[220:223]\nds_read_b64_tr_b16 a[152:153],v66 offset:8192\n" \
"ds_read_b64_tr_b16 a[154:155],v66 offset:8704\nv_mfma_f32_16x16x32_bf16 v[220:223],a[108:111],v[92:95],v[220:223]\nglobal_load_dwordx4 v[96:99],v59,s[16:17] sc1\ns_waitcnt lgkmcnt(15)\n" \
"v_mfma_f32_16x16x32_bf16 v[224:227],a[80:83],v[88:91],v[224:227]\nds_read_b64_tr_b16 a[156:157],v67 offset:8192\nds_read_b64_tr_b16 a[158:159],v67 offset:8704\nv_mfma_f32_16x16x32_bf16 v[228:231],a[84:87],v[88:91],v[228:231]\n" \
"global_load_dwordx4 v[100:103],v59,s[16:17] offset:1024 sc1\ns_waitcnt vmcnt(21)\nv_mfma_f32_16x16x32_bf16 v[232:235],a[88:91],v[88:91],v[232:235]\nds_read_b64_tr_b16 a[160:161],v64 offset:12288\n" \
"ds_read_b64_tr_b16 a[162:163],v64 offset:12800\nv_mfma_f32_16x16x32_bf16 v[236:239],a[92:95],v[88:91],v[236:239]\nglobal_load_dwordx4 v[104:107],v59,s[16:17] offset:2048 sc1\ns_waitcnt vmcnt(16)\n" \
"v_mfma_f32_16x16x32_bf16 v[0:3],v[128:131],v[108:111],0\nds_read_b64_tr_b16 a[164:165],v65 offset:12288\ns_waitcnt vmcnt(15)\nv_mfma_f32_16x16x32_bf16 v[0:3],v[160:163],v[112:115],v[0:3]\n" \
"global_load_dwordx4 v[196:199],v59,s[8:9] sc1\nds_read_b64_tr_b16 a[166:167],v65 offset:12800\nv_mfma_f32_16x16x32_bf16 v[4:7],v[132:135],v[108:111],0\nds_read_b64_tr_b16 a[168:169],v66 offset:12288\n" \
"ds_read_b64_tr_b16 a[170:171],v66 offset:12800\nv_mfma_f32_16x16x32_bf16 v[4:7],v[164:167],v[112:115],v[4:7]\nglobal_load_dwordx4 v[200:203],v59,s[8:9] offset:1024 sc1\nds_read_b64_tr_b16 a[172:173],v67 offset:12288\n" \
"v_mfma_f32_16x16x32_bf16 v[8:11],v[136:139],v[108:111],0\nds_read_b64_tr_b16 a[174:175],v67 offset:12800\nds_read_b32 v252,v60\nv_mfma_f32_16x16x32_bf16 v[8:11],v[168:171],v[112:115],v[8:11]\n" \
"global_load_dwordx4 v[204:207],v59,s[8:9] offset:2048 sc1\nds_read_b32 v253,v60 offset:9568\nv_mfma_f32_16x16x32_bf16 v[12:15],v[140:143],v[108:111],0\ns_add_u32 s59,s59,0x10000\n" \
"buffer_load_dword v63,v61,s[36:39],s59 offen sc1\nv_mfma_f32_16x16x32_bf16 v[12:15],v[172:175],v[112:115],v[12:15]\nv_add_u32 v59,819200,v59\ns_waitcnt vmcnt(21)\n" \
"v_mfma_f32_16x16x32_bf16 v[16:19],v[144:147],v[108:111],0\nds_read_b64_tr_b16 a[176:177],v64 offset:16384\nds_read_b64_tr_b16 a[178:179],v64 offset:16896\nv_mfma_f32_16x16x32_bf16 v[16:19],v[176:179],v[112:115],v[16:19]\n" \
"ds_read_b64_tr_b16 a[180:181],v65 offset:16384\nds_read_b64_tr_b16 a[182:183],v65 offset:16896\nv_mfma_f32_16x16x32_bf16 v[20:23],v[148:151],v[108:111],0\nds_read_b64_tr_b16 a[184:185],v66 offset:16384\n" \
"ds_read_b64_tr_b16 a[186:187],v66 offset:16896\nv_mfma_f32_16x16x32_bf16 v[20:23],v[180:183],v[112:115],v[20:23]\nds_read_b64_tr_b16 a[188:189],v67 offset:16384\nds_read_b64_tr_b16 a[190:191],v67 offset:16896\n" \
"v_mfma_f32_16x16x32_bf16 v[24:27],v[152:155],v[108:111],0\nds_read_b32 v254,v60 offset:19136\nds_read_b32 v255,v60 offset:28704\nv_mfma_f32_16x16x32_bf16 v[24:27],v[184:187],v[112:115],v[24:27]\n" \
"v_rcp_f32 v40,v40\nv_mfma_f32_16x16x32_bf16 v[28:31],v[156:159],v[108:111],0\nv_add_f32 v46,v46,v47\nv_add_f32 v44,v44,v56\n" \
"v_mfma_f32_16x16x32_bf16 v[28:31],v[188:191],v[112:115],v[28:31]\nv_add_f32 v44,v44,v46\ns_waitcnt vmcnt(18)\nv_mfma_f32_16x16x32_bf16 v[0:3],a[0:3],v[116:119],v[0:3]\n" \
"v_mul_f32 v45,v44,v44\ns_waitcnt vmcnt(13)\nv_mfma_f32_16x16x32_bf16 v[0:3],a[32:35],v[120:123],v[0:3]\nv_rcp_f32 v41,v41\n" \
"v_mfma_f32_16x16x32_bf16 v[4:7],a[4:7],v[116:119],v[4:7]\ns_waitcnt lgkmcnt(0)\nv_add_f32 v252,v252,v253\nv_mfma_f32_16x16x32_bf16 v[4:7],a[36:39],v[120:123],v[4:7]\n" \
"v_add_f32 v254,v254,v255\nv_add_f32 v252,v252,v56\nv_mfma_f32_16x16x32_bf16 v[8:11],a[8:11],v[116:119],v[8:11]\nv_add_f32 v252,v252,v254\n" \
"v_mul_f32 v253,v252,v252\nv_mfma_f32_16x16x32_bf16 v[8:11],a[40:43],v[120:123],v[8:11]\nv_rcp_f32 v42,v42\nv_mfma_f32_16x16x32_bf16 v[12:15],a[12:15],v[116:119],v[12:15]\n" \
"v_fma_f32 v45,v45,s55,v57\nv_fma_f32 v253,v253,s55,v57\nv_mfma_f32_16x16x32_bf16 v[12:15],a[44:47],v[120:123],v[12:15]\nv_rcp_f32 v43,v43\n" \
"v_mfma_f32_16x16x32_bf16 v[16:19],a[16:19],v[116:119],v[16:19]\nv_mul_f32 v45,v44,v45\nv_mul_f32 v253,v252,v253\nv_mfma_f32_16x16x32_bf16 v[16:19],a[48:51],v[120:123],v[16:19]\n" \
"v_exp_f32 v45,v45\nv_mfma_f32_16x16x32_bf16 v[20:23],a[20:23],v[116:119],v[20:23]\nv_exp_f32 v253,v253\nv_mfma_f32_16x16x32_bf16 v[20:23],a[52:55],v[120:123],v[20:23]\n" \
"ds_read_b64_tr_b16 a[192:193],v64 offset:20480\nv_mfma_f32_16x16x32_bf16 v[24:27],a[24:27],v[116:119],v[24:27]\nds_read_b64_tr_b16 a[194:195],v64 offset:20992\nds_read_b64_tr_b16 a[196:197],v65 offset:20480\n" \
"v_mfma_f32_16x16x32_bf16 v[24:27],a[56:59],v[120:123],v[24:27]\nds_read_b64_tr_b16 a[198:199],v65 offset:20992\nds_read_b64_tr_b16 a[200:201],v66 offset:20480\nv_mfma_f32_16x16x32_bf16 v[28:31],a[28:31],v[116:119],v[28:31]\n" \
"ds_read_b64_tr_b16 a[202:203],v66 offset:20992\nds_read_b64_tr_b16 a[204:205],v67 offset:20480\nv_mfma_f32_16x16x32_bf16 v[28:31],a[60:63],v[120:123],v[28:31]\nds_read_b64_tr_b16 a[206:207],v67 offset:20992\n" \
"s_waitcnt vmcnt(8)\nv_mfma_f32_16x16x32_bf16 v[0:3],a[64:67],v[124:127],v[0:3]\nv_mul_f32 v36,v36,v40\ns_waitcnt vmcnt(7)\n" \
"v_mfma_f32_16x16x32_bf16 v[0:3],a[96:99],v[192:195],v[0:3]\nv_mul_f32 v37,v37,v41\nv_add_f32 v45,1.0,v45\nv_mfma_f32_16x16x32_bf16 v[4:7],a[68:71],v[124:127],v[4:7]\n" \
"v_add_f32 v253,1.0,v253\nv_mul_f32 v38,v38,v42\nv_mfma_f32_16x16x32_bf16 v[4:7],a[100:103],v[192:195],v[4:7]\nv_rcp_f32 v45,v45\n" \
"v_mfma_f32_16x16x32_bf16 v[8:11],a[72:75],v[124:127],v[8:11]\nv_rcp_f32 v253,v253\nv_mfma_f32_16x16x32_bf16 v[8:11],a[104:107],v[192:195],v[8:11]\nv_mul_f32 v39,v39,v43\n" \
"v_cvt_pk_bf16_f32 v40,v36,v37\nv_mfma_f32_16x16x32_bf16 v[12:15],a[76:79],v[124:127],v[12:15]\nv_mul_f32 v44,v44,v45\nv_mul_f32 v252,v252,v253\n" \
"v_mfma_f32_16x16x32_bf16 v[12:15],a[108:111],v[192:195],v[12:15]\nds_read_b64_tr_b16 a[208:209],v64 offset:24576\nv_mfma_f32_16x16x32_bf16 v[16:19],a[80:83],v[124:127],v[16:19]\nds_read_b64_tr_b16 a[210:211],v64 offset:25088\n" \
"ds_read_b64_tr_b16 a[212:213],v65 offset:24576\nv_mfma_f32_16x16x32_bf16 v[20:23],a[84:87],v[124:127],v[20:23]\nds_read_b64_tr_b16 a[214:215],v65 offset:25088\nds_read_b64_tr_b16 a[216:217],v66 offset:24576\n" \
"v_mfma_f32_16x16x32_bf16 v[24:27],a[88:91],v[124:127],v[24:27]\nds_read_b64_tr_b16 a[218:219],v66 offset:25088\nds_read_b64_tr_b16 a[220:221],v67 offset:24576\nv_mfma_f32_16x16x32_bf16 v[28:31],a[92:95],v[124:127],v[28:31]\n" \
"ds_read_b64_tr_b16 a[222:223],v67 offset:25088\ns_waitcnt lgkmcnt(15)\nv_mfma_f32_16x16x32_bf16 v[224:227],a[112:115],v[92:95],v[224:227]\ns_waitcnt vmcnt(6)\n" \
"v_mfma_f32_16x16x32_bf16 v[224:227],a[144:147],v[96:99],v[224:227]\nv_cvt_pk_bf16_f32 v41,v38,v39\nv_cvt_pk_bf16_f32 v45,v44,v44\nv_mfma_f32_16x16x32_bf16 v[228:231],a[116:119],v[92:95],v[228:231]\n" \
"v_cvt_pk_bf16_f32 v253,v252,v252\nbuffer_store_dwordx2 v[40:41],v61,s[36:39],s52 offen sc1\nv_mfma_f32_16x16x32_bf16 v[228:231],a[148:151],v[96:99],v[228:231]\nbuffer_store_short v45,v62,s[36:39],s52 offen sc1\n" \
"ds_read_b128 v[36:39],v58 offset:5344\nds_read_b128 v[40:43],v58 offset:14912\nds_read_b128 v[44:47],v58 offset:24480\nds_read_b128 v[48:51],v58 offset:34048\n" \
"s_waitcnt lgkmcnt(2)\nv_add_f32 v36,v36,v40\nv_add_f32 v37,v37,v41\nv_add_f32 v38,v38,v42\n" \
"v_add_f32 v39,v39,v43\nv_add_f32 v36,v36,v52\nv_add_f32 v37,v37,v53\nv_add_f32 v38,v38,v54\n" \
"v_add_f32 v39,v39,v55\ns_waitcnt lgkmcnt(0)\nv_add_f32 v44,v44,v48\nv_add_f32 v45,v45,v49\n" \
"v_add_f32 v46,v46,v50\nv_add_f32 v47,v47,v51\nv_add_f32 v36,v36,v44\nv_add_f32 v37,v37,v45\n" \
"v_add_f32 v38,v38,v46\nv_add_f32 v39,v39,v47\nv_mul_f32 v40,v36,v36\nv_mul_f32 v41,v37,v37\n" \
"v_mul_f32 v42,v38,v38\nv_mul_f32 v43,v39,v39\nv_fma_f32 v40,v40,s55,v57\nv_fma_f32 v41,v41,s55,v57\n" \
"v_fma_f32 v42,v42,s55,v57\nv_fma_f32 v43,v43,s55,v57\nv_mul_f32 v40,v36,v40\nv_mul_f32 v41,v37,v41\n" \
"v_mul_f32 v42,v38,v42\nv_mul_f32 v43,v39,v43\nv_exp_f32 v40,v40\nv_exp_f32 v41,v41\n" \
"v_exp_f32 v42,v42\nv_exp_f32 v43,v43\nv_add_f32 v40,1.0,v40\nv_add_f32 v41,1.0,v41\n" \
"v_add_f32 v42,1.0,v42\nv_add_f32 v43,1.0,v43\nv_rcp_f32 v40,v40\nv_rcp_f32 v41,v41\n" \
"v_rcp_f32 v42,v42\nv_rcp_f32 v43,v43\nv_mul_f32 v36,v36,v40\nv_mul_f32 v37,v37,v41\n" \
"v_mul_f32 v38,v38,v42\nv_mul_f32 v39,v39,v43\nv_cvt_pk_bf16_f32 v40,v36,v37\nv_cvt_pk_bf16_f32 v41,v38,v39\n" \
"v_mfma_f32_16x16x32_bf16 v[232:235],a[120:123],v[92:95],v[232:235]\ns_add_u32 s52,s52,409600\nbuffer_store_dwordx2 v[40:41],v61,s[36:39],s52 offen sc1\nds_read_b128 v[36:39],v58\n" \
"v_mfma_f32_16x16x32_bf16 v[232:235],a[152:155],v[96:99],v[232:235]\nds_read_b128 v[40:43],v58 offset:9568\nds_read_b128 v[44:47],v58 offset:19136\nv_mfma_f32_16x16x32_bf16 v[236:239],a[124:127],v[92:95],v[236:239]\n" \
"ds_read_b128 v[48:51],v58 offset:28704\ns_add_u32 s52,s52,409600\nv_mfma_f32_16x16x32_bf16 v[236:239],a[156:159],v[96:99],v[236:239]\nbuffer_store_short v253,v62,s[36:39],s52 offen sc1\n" \
"v_mfma_f32_16x16x32_bf16 v[208:211],a[128:131],v[96:99],v[208:211]\ns_waitcnt vmcnt(9)\nv_mfma_f32_16x16x32_bf16 v[208:211],a[160:163],v[100:103],v[208:211]\nv_mfma_f32_16x16x32_bf16 v[212:215],a[132:135],v[96:99],v[212:215]\n" \
"v_mfma_f32_16x16x32_bf16 v[212:215],a[164:167],v[100:103],v[212:215]\nv_mfma_f32_16x16x32_bf16 v[216:219],a[136:139],v[96:99],v[216:219]\nv_mfma_f32_16x16x32_bf16 v[216:219],a[168:171],v[100:103],v[216:219]\ns_waitcnt lgkmcnt(2)\n" \
"v_add_f32 v36,v36,v40\nv_add_f32 v37,v37,v41\nv_mfma_f32_16x16x32_bf16 v[220:223],a[140:143],v[96:99],v[220:223]\nv_add_f32 v38,v38,v42\n" \
"v_add_f32 v39,v39,v43\nv_add_f32 v36,v36,v52\nv_mfma_f32_16x16x32_bf16 v[220:223],a[172:175],v[100:103],v[220:223]\nv_add_f32 v37,v37,v53\n" \
"v_add_f32 v38,v38,v54\nv_mfma_f32_16x16x32_bf16 v[224:227],a[176:179],v[100:103],v[224:227]\nv_add_f32 v39,v39,v55\ns_waitcnt vmcnt(8)\n" \
"v_mfma_f32_16x16x32_bf16 v[224:227],a[208:211],v[104:107],v[224:227]\ns_waitcnt lgkmcnt(0)\nv_add_f32 v44,v44,v48\nv_add_f32 v45,v45,v49\n" \
"v_mfma_f32_16x16x32_bf16 v[228:231],a[180:183],v[100:103],v[228:231]\nv_add_f32 v46,v46,v50\nv_add_f32 v47,v47,v51\nv_add_f32 v36,v36,v44\n" \
"v_mfma_f32_16x16x32_bf16 v[228:231],a[212:215],v[104:107],v[228:231]\nv_add_f32 v37,v37,v45\nv_add_f32 v38,v38,v46\nv_add_f32 v39,v39,v47\n" \
"v_mfma_f32_16x16x32_bf16 v[232:235],a[184:187],v[100:103],v[232:235]\nv_mul_f32 v40,v36,v36\nv_mul_f32 v41,v37,v37\nv_mul_f32 v42,v38,v38\n" \
"v_mfma_f32_16x16x32_bf16 v[232:235],a[216:219],v[104:107],v[232:235]\nv_mul_f32 v43,v39,v39\nv_fma_f32 v40,v40,s55,v57\nv_fma_f32 v41,v41,s55,v57\n" \
"v_mfma_f32_16x16x32_bf16 v[236:239],a[188:191],v[100:103],v[236:239]\nv_fma_f32 v42,v42,s55,v57\nv_fma_f32 v43,v43,s55,v57\nv_mul_f32 v40,v36,v40\n" \
"v_mul_f32 v41,v37,v41\nv_mul_f32 v42,v38,v42\nv_mul_f32 v43,v39,v43\nv_exp_f32 v40,v40\n" \
"v_exp_f32 v41,v41\nv_exp_f32 v42,v42\nv_exp_f32 v43,v43\nv_add_f32 v40,1.0,v40\n" \
"v_add_f32 v41,1.0,v41\nv_add_f32 v42,1.0,v42\nv_add_f32 v43,1.0,v43\nv_rcp_f32 v40,v40\n" \
"v_rcp_f32 v41,v41\nv_rcp_f32 v42,v42\nv_rcp_f32 v43,v43\nv_mul_f32 v36,v36,v40\n" \
"v_mul_f32 v37,v37,v41\nv_mul_f32 v38,v38,v42\nv_mul_f32 v39,v39,v43\nv_cvt_pk_bf16_f32 v40,v36,v37\n" \
"v_cvt_pk_bf16_f32 v41,v38,v39\nv_mfma_f32_16x16x32_bf16 v[236:239],a[220:223],v[104:107],v[236:239]\nv_and_b32 v58,0x3ff,v59\nv_mfma_f32_16x16x32_bf16 v[208:211],a[192:195],v[104:107],v[208:211]\n" \
"v_add_u32 v58,s99,v58\nv_add_u32 v46,s98,v58\nv_mfma_f32_16x16x32_bf16 v[212:215],a[196:199],v[104:107],v[212:215]\nds_read_b64 v[56:57],v46 offset:28680\n" \
"buffer_store_dwordx2 v[40:41],v61,s[36:39],s52 offen sc1\nv_mfma_f32_16x16x32_bf16 v[216:219],a[200:203],v[104:107],v[216:219]\nglobal_load_dwordx4 v[72:75],v59,s[12:13] sc1\nv_mfma_f32_16x16x32_bf16 v[220:223],a[204:207],v[104:107],v[220:223]\n" \
"v_mfma_f32_16x16x32_bf16 v[16:19],a[112:115],v[192:195],v[16:19]\nglobal_load_dwordx4 v[76:79],v59,s[12:13] offset:1024 sc1\ns_waitcnt vmcnt(10)\nv_mfma_f32_16x16x32_bf16 v[16:19],a[144:147],v[196:199],v[16:19]\n" \
"s_waitcnt lgkmcnt(0)\ns_barrier\ns_mov_b32 m0,s58\nv_mfma_f32_16x16x32_bf16 v[20:23],a[116:119],v[192:195],v[20:23]\n" \
"global_load_dwordx4 v[80:83],v59,s[12:13] offset:2048 sc1\nds_write_addtid_b32 v208\nv_mfma_f32_16x16x32_bf16 v[20:23],a[148:151],v[196:199],v[20:23]\nds_write_addtid_b32 v209 offset:256\n" \
"ds_write_addtid_b32 v210 offset:512\nv_mfma_f32_16x16x32_bf16 v[24:27],a[120:123],v[192:195],v[24:27]\nglobal_load_dwordx4 v[84:87],v59,s[14:15] sc1\nds_write_addtid_b32 v211 offset:768\n" \
"v_mfma_f32_16x16x32_bf16 v[24:27],a[152:155],v[196:199],v[24:27]\nds_write_addtid_b32 v212 offset:1024\nds_write_addtid_b32 v213 offset:1280\nv_mfma_f32_16x16x32_bf16 v[28:31],a[124:127],v[192:195],v[28:31]\n" \
"global_load_dwordx4 v[88:91],v59,s[14:15] offset:1024 sc1\nds_write_addtid_b32 v214 offset:1536\nv_mfma_f32_16x16x32_bf16 v[28:31],a[156:159],v[196:199],v[28:31]\nds_write_addtid_b32 v215 offset:1792\n" \
"ds_write_addtid_b32 v216 offset:2048\nv_mfma_f32_16x16x32_bf16 v[0:3],a[128:131],v[196:199],v[0:3]\nglobal_load_dwordx4 v[92:95],v59,s[14:15] offset:2048 sc1\ns_waitcnt vmcnt(13)\n" \
"v_mfma_f32_16x16x32_bf16 v[0:3],a[160:163],v[200:203],v[0:3]\nds_write_addtid_b32 v217 offset:2304\nds_write_addtid_b32 v218 offset:2560\nv_mfma_f32_16x16x32_bf16 v[4:7],a[132:135],v[196:199],v[4:7]\n" \
"global_load_dwordx4 v[96:99],v59,s[16:17] sc1\nds_write_addtid_b32 v219 offset:2816\nv_mfma_f32_16x16x32_bf16 v[4:7],a[164:167],v[200:203],v[4:7]\nds_write_addtid_b32 v220 offset:3072\n" \
"ds_write_addtid_b32 v221 offset:3328\nv_mfma_f32_16x16x32_bf16 v[8:11],a[136:139],v[196:199],v[8:11]\nglobal_load_dwordx4 v[100:103],v59,s[16:17] offset:1024 sc1\nds_write_addtid_b32 v222 offset:3584\n" \
"v_mfma_f32_16x16x32_bf16 v[8:11],a[168:171],v[200:203],v[8:11]\nds_write_addtid_b32 v223 offset:3840\nds_write_addtid_b32 v224 offset:4096\nv_mfma_f32_16x16x32_bf16 v[12:15],a[140:143],v[196:199],v[12:15]\n" \
"global_load_dwordx4 v[104:107],v59,s[16:17] offset:2048 sc1\nds_write_addtid_b32 v225 offset:4352\nv_mfma_f32_16x16x32_bf16 v[12:15],a[172:175],v[200:203],v[12:15]\nds_write_addtid_b32 v226 offset:4608\n" \
"ds_write_addtid_b32 v227 offset:4864\nv_mfma_f32_16x16x32_bf16 v[16:19],a[176:179],v[200:203],v[16:19]\nds_write_addtid_b32 v228 offset:5120\ns_waitcnt vmcnt(15)\n" \
"v_mfma_f32_16x16x32_bf16 v[16:19],a[208:211],v[204:207],v[16:19]\nds_write_addtid_b32 v229 offset:5376\nds_write_addtid_b32 v230 offset:5632\nv_mfma_f32_16x16x32_bf16 v[20:23],a[180:183],v[200:203],v[20:23]\n" \
"ds_write_addtid_b32 v231 offset:5888\nds_write_addtid_b32 v232 offset:6144\nv_mfma_f32_16x16x32_bf16 v[20:23],a[212:215],v[204:207],v[20:23]\nds_write_addtid_b32 v233 offset:6400\n" \
"ds_write_addtid_b32 v234 offset:6656\nv_mfma_f32_16x16x32_bf16 v[24:27],a[184:187],v[200:203],v[24:27]\nds_write_addtid_b32 v235 offset:6912\nds_write_addtid_b32 v236 offset:7168\n" \
"v_mfma_f32_16x16x32_bf16 v[24:27],a[216:219],v[204:207],v[24:27]\nds_write_addtid_b32 v237 offset:7424\nds_write_addtid_b32 v238 offset:7680\nv_mfma_f32_16x16x32_bf16 v[28:31],a[188:191],v[200:203],v[28:31]\n" \
"ds_write_addtid_b32 v239 offset:7936\nv_mfma_f32_16x16x32_bf16 v[28:31],a[220:223],v[204:207],v[28:31]\nv_mfma_f32_16x16x32_bf16 v[0:3],a[192:195],v[204:207],v[0:3]\nv_mfma_f32_16x16x32_bf16 v[4:7],a[196:199],v[204:207],v[4:7]\n" \
"v_mfma_f32_16x16x32_bf16 v[8:11],a[200:203],v[204:207],v[8:11]\nv_mfma_f32_16x16x32_bf16 v[12:15],a[204:207],v[204:207],v[12:15]\ns_waitcnt vmcnt(7)\nv_mfma_f32_16x16x32_bf16 v[208:211],v[128:131],v[72:75],0\n" \
"s_mov_b64 exec,1\nbuffer_store_dword v61,off,s[36:39],s54 sc1\ns_mov_b64 exec,-1\nv_mfma_f32_16x16x32_bf16 v[208:211],v[160:163],v[76:79],v[208:211]\n" \
"v_cmp_eq_u32 vcc,0,v63\nv_mfma_f32_16x16x32_bf16 v[212:215],v[132:135],v[72:75],0\ns_waitcnt lgkmcnt(0)\ns_barrier\n" \
"v_mfma_f32_16x16x32_bf16 v[212:215],v[164:167],v[76:79],v[212:215]\ns_and_b64 vcc,vcc,s[86:87]\ns_cbranch_scc1 .LsbF_%=\n.LbbF_%=:\n" \
"v_mfma_f32_16x16x32_bf16 v[216:219],v[136:139],v[72:75],0\nglobal_load_dwordx4 v[108:111],v59,s[4:5] sc1\nds_write_addtid_b32 v0 offset:32768\nv_mfma_f32_16x16x32_bf16 v[216:219],v[168:171],v[76:79],v[216:219]\n" \
"ds_write_addtid_b32 v1 offset:33024\nds_write_addtid_b32 v2 offset:33280\nv_mfma_f32_16x16x32_bf16 v[220:223],v[140:143],v[72:75],0\nglobal_load_dwordx4 v[112:115],v59,s[4:5] offset:1024 sc1\n" \
"ds_write_addtid_b32 v3 offset:33536\nv_mfma_f32_16x16x32_bf16 v[220:223],v[172:175],v[76:79],v[220:223]\nds_write_addtid_b32 v4 offset:33792\nds_write_addtid_b32 v5 offset:34048\n" \
"v_mfma_f32_16x16x32_bf16 v[224:227],v[144:147],v[72:75],0\nglobal_load_dwordx4 v[116:119],v59,s[4:5] offset:2048 sc1\nds_write_addtid_b32 v6 offset:34304\nv_mfma_f32_16x16x32_bf16 v[224:227],v[176:179],v[76:79],v[224:227]\n" \
"ds_write_addtid_b32 v7 offset:34560\nds_write_addtid_b32 v8 offset:34816\nv_mfma_f32_16x16x32_bf16 v[228:231],v[148:151],v[72:75],0\nglobal_load_dwordx4 v[120:123],v59,s[6:7] sc1\n" \
"ds_write_addtid_b32 v9 offset:35072\nv_mfma_f32_16x16x32_bf16 v[228:231],v[180:183],v[76:79],v[228:231]\nds_write_addtid_b32 v10 offset:35328\nds_write_addtid_b32 v11 offset:35584\n" \
"v_mfma_f32_16x16x32_bf16 v[232:235],v[152:155],v[72:75],0\nglobal_load_dwordx4 v[124:127],v59,s[6:7] offset:1024 sc1\nds_write_addtid_b32 v12 offset:35840\nv_mfma_f32_16x16x32_bf16 v[232:235],v[184:187],v[76:79],v[232:235]\n" \
"ds_write_addtid_b32 v13 offset:36096\nds_write_addtid_b32 v14 offset:36352\nv_mfma_f32_16x16x32_bf16 v[236:239],v[156:159],v[72:75],0\nglobal_load_dwordx4 v[192:195],v59,s[6:7] offset:2048 sc1\n" \
"ds_write_addtid_b32 v15 offset:36608\nv_mfma_f32_16x16x32_bf16 v[236:239],v[188:191],v[76:79],v[236:239]\nds_write_addtid_b32 v16 offset:36864\ns_waitcnt vmcnt(12)\n" \
"v_mfma_f32_16x16x32_bf16 v[208:211],a[0:3],v[80:83],v[208:211]\nglobal_load_dwordx4 v[196:199],v59,s[8:9] sc1\nds_write_addtid_b32 v17 offset:37120\nv_mfma_f32_16x16x32_bf16 v[208:211],a[32:35],v[84:87],v[208:211]\n" \
"ds_write_addtid_b32 v18 offset:37376\nds_write_addtid_b32 v19 offset:37632\nv_mfma_f32_16x16x32_bf16 v[212:215],a[4:7],v[80:83],v[212:215]\nglobal_load_dwordx4 v[200:203],v59,s[8:9] offset:1024 sc1\n" \
"ds_write_addtid_b32 v20 offset:37888\nv_mfma_f32_16x16x32_bf16 v[212:215],a[36:39],v[84:87],v[212:215]\nds_write_addtid_b32 v21 offset:38144\nds_write_addtid_b32 v22 offset:38400\n" \
"v_mfma_f32_16x16x32_bf16 v[216:219],a[8:11],v[80:83],v[216:219]\nglobal_load_dwordx4 v[204:207],v59,s[8:9] offset:2048 sc1\nds_write_addtid_b32 v23 offset:38656\nv_mfma_f32_16x16x32_bf16 v[216:219],a[40:43],v[84:87],v[216:219]\n" \
"ds_write_addtid_b32 v24 offset:38912\nds_write_addtid_b32 v25 offset:39168\nv_mfma_f32_16x16x32_bf16 v[220:223],a[12:15],v[80:83],v[220:223]\nds_write_addtid_b32 v26 offset:39424\n" \
"ds_write_addtid_b32 v27 offset:39680\nv_mfma_f32_16x16x32_bf16 v[220:223],a[44:47],v[84:87],v[220:223]\nds_write_addtid_b32 v28 offset:39936\nds_write_addtid_b32 v29 offset:40192\n" \
"v_mfma_f32_16x16x32_bf16 v[224:227],a[16:19],v[80:83],v[224:227]\nds_write_addtid_b32 v30 offset:40448\nds_write_addtid_b32 v31 offset:40704\nv_mfma_f32_16x16x32_bf16 v[224:227],a[48:51],v[84:87],v[224:227]\n" \
"ds_read_b128 v[32:35],v58\nds_read_b128 v[36:39],v58 offset:8192\nv_mfma_f32_16x16x32_bf16 v[228:231],a[20:23],v[80:83],v[228:231]\nds_read_b128 v[40:43],v58 offset:16384\n" \
"ds_read_b128 v[44:47],v58 offset:24576\nv_mfma_f32_16x16x32_bf16 v[228:231],a[52:55],v[84:87],v[228:231]\nds_read_b128 v[48:51],v58 offset:4096\nds_read_b128 v[240:243],v58 offset:12288\n" \
"v_mfma_f32_16x16x32_bf16 v[232:235],a[24:27],v[80:83],v[232:235]\nds_read_b128 v[244:247],v58 offset:20480\nds_read_b128 v[248:251],v58 offset:28672\nv_mfma_f32_16x16x32_bf16 v[232:235],a[56:59],v[84:87],v[232:235]\n" \
"v_mfma_f32_16x16x32_bf16 v[236:239],a[28:31],v[80:83],v[236:239]\ns_waitcnt lgkmcnt(0)\ns_barrier\nv_mfma_f32_16x16x32_bf16 v[236:239],a[60:63],v[84:87],v[236:239]\n" \
"s_waitcnt vmcnt(13)\nv_mfma_f32_16x16x32_bf16 v[208:211],a[64:67],v[88:91],v[208:211]\nv_mfma_f32_16x16x32_bf16 v[208:211],a[96:99],v[92:95],v[208:211]\nv_mfma_f32_16x16x32_bf16 v[212:215],a[68:71],v[88:91],v[212:215]\n" \
"v_add_f32 v32,v32,v36\nv_mfma_f32_16x16x32_bf16 v[212:215],a[100:103],v[92:95],v[212:215]\nv_add_f32 v33,v33,v37\nv_add_f32 v34,v34,v38\n" \
"v_mfma_f32_16x16x32_bf16 v[216:219],a[72:75],v[88:91],v[216:219]\nv_add_f32 v35,v35,v39\nv_add_f32 v40,v40,v44\nv_mfma_f32_16x16x32_bf16 v[216:219],a[104:107],v[92:95],v[216:219]\n" \
"v_add_f32 v41,v41,v45\nv_add_f32 v42,v42,v46\nv_mfma_f32_16x16x32_bf16 v[220:223],a[76:79],v[88:91],v[220:223]\nv_add_f32 v43,v43,v47\n" \
"v_add_f32 v32,v32,v40\nv_mfma_f32_16x16x32_bf16 v[220:223],a[108:111],v[92:95],v[220:223]\nv_add_f32 v33,v33,v41\nv_add_f32 v34,v34,v42\n" \
"v_mfma_f32_16x16x32_bf16 v[224:227],a[80:83],v[88:91],v[224:227]\nv_add_f32 v35,v35,v43\nv_cvt_pk_bf16_f32 v36,v32,v33\nv_mfma_f32_16x16x32_bf16 v[224:227],a[112:115],v[92:95],v[224:227]\n" \
"v_cvt_pk_bf16_f32 v37,v34,v35\nv_mfma_f32_16x16x32_bf16 v[228:231],a[84:87],v[88:91],v[228:231]\nv_add_f32 v48,v48,v240\nv_add_f32 v49,v49,v241\n" \
"v_mfma_f32_16x16x32_bf16 v[228:231],a[116:119],v[92:95],v[228:231]\nv_add_f32 v50,v50,v242\nv_add_f32 v51,v51,v243\nv_mfma_f32_16x16x32_bf16 v[232:235],a[88:91],v[88:91],v[232:235]\n" \
"v_add_f32 v244,v244,v248\nv_add_f32 v245,v245,v249\nv_mfma_f32_16x16x32_bf16 v[232:235],a[120:123],v[92:95],v[232:235]\nv_add_f32 v246,v246,v250\n" \
"v_add_f32 v247,v247,v251\nv_mfma_f32_16x16x32_bf16 v[236:239],a[92:95],v[88:91],v[236:239]\nv_add_f32 v48,v48,v244\nv_add_f32 v49,v49,v245\n" \
"v_mfma_f32_16x16x32_bf16 v[236:239],a[124:127],v[92:95],v[236:239]\nv_add_f32 v50,v50,v246\ns_waitcnt vmcnt(11)\nv_mfma_f32_16x16x32_bf16 v[208:211],a[128:131],v[96:99],v[208:211]\n" \
"v_add_f32 v51,v51,v247\nv_cvt_pk_bf16_f32 v38,v48,v49\nv_mfma_f32_16x16x32_bf16 v[208:211],a[160:163],v[100:103],v[208:211]\nv_cvt_pk_bf16_f32 v39,v50,v51\n" \
"buffer_store_dwordx4 v[36:39],v59,s[36:39],s74 offen sc1\nds_read_b128 v[32:35],v58 offset:32768\nv_mfma_f32_16x16x32_bf16 v[212:215],a[132:135],v[96:99],v[212:215]\nds_read_b128 v[36:39],v58 offset:40960\n" \
"ds_read_b128 v[40:43],v58 offset:49152\nv_mfma_f32_16x16x32_bf16 v[212:215],a[164:167],v[100:103],v[212:215]\nds_read_b128 v[44:47],v58 offset:57344\nds_read_b128 v[48:51],v58 offset:36864\n" \
"v_mfma_f32_16x16x32_bf16 v[216:219],a[136:139],v[96:99],v[216:219]\nds_read_b128 v[240:243],v58 offset:45056\nds_read_b128 v[244:247],v58 offset:53248\nv_mfma_f32_16x16x32_bf16 v[216:219],a[168:171],v[100:103],v[216:219]\n" \
"ds_read_b128 v[248:251],v58 offset:61440\nv_mfma_f32_16x16x32_bf16 v[220:223],a[140:143],v[96:99],v[220:223]\nv_mfma_f32_16x16x32_bf16 v[220:223],a[172:175],v[100:103],v[220:223]\nv_mfma_f32_16x16x32_bf16 v[224:227],a[144:147],v[96:99],v[224:227]\n" \
"v_mfma_f32_16x16x32_bf16 v[224:227],a[176:179],v[100:103],v[224:227]\nv_mfma_f32_16x16x32_bf16 v[228:231],a[148:151],v[96:99],v[228:231]\nv_mfma_f32_16x16x32_bf16 v[228:231],a[180:183],v[100:103],v[228:231]\ns_waitcnt lgkmcnt(4)\n" \
"v_add_f32 v32,v32,v36\nv_add_f32 v33,v33,v37\nv_mfma_f32_16x16x32_bf16 v[232:235],a[152:155],v[96:99],v[232:235]\nv_add_f32 v34,v34,v38\n" \
"v_add_f32 v35,v35,v39\nv_add_f32 v40,v40,v44\nv_mfma_f32_16x16x32_bf16 v[232:235],a[184:187],v[100:103],v[232:235]\nv_add_f32 v41,v41,v45\n" \
"v_add_f32 v42,v42,v46\nv_add_f32 v43,v43,v47\nv_mfma_f32_16x16x32_bf16 v[236:239],a[156:159],v[96:99],v[236:239]\nv_add_f32 v32,v32,v40\n" \
"v_add_f32 v33,v33,v41\nv_add_f32 v34,v34,v42\nv_mfma_f32_16x16x32_bf16 v[236:239],a[188:191],v[100:103],v[236:239]\nv_add_f32 v35,v35,v43\n" \
"v_cvt_pk_bf16_f32 v36,v32,v33\ns_waitcnt vmcnt(11)\nv_mfma_f32_16x16x32_bf16 v[208:211],a[192:195],v[104:107],v[208:211]\nv_cvt_pk_bf16_f32 v37,v34,v35\n" \
"s_waitcnt lgkmcnt(0)\nv_add_f32 v48,v48,v240\nv_mfma_f32_16x16x32_bf16 v[212:215],a[196:199],v[104:107],v[212:215]\nv_add_f32 v49,v49,v241\n" \
"v_add_f32 v50,v50,v242\nv_add_f32 v51,v51,v243\nv_mfma_f32_16x16x32_bf16 v[216:219],a[200:203],v[104:107],v[216:219]\nv_add_f32 v244,v244,v248\n" \
"v_add_f32 v245,v245,v249\nv_mfma_f32_16x16x32_bf16 v[220:223],a[204:207],v[104:107],v[220:223]\nv_add_f32 v246,v246,v250\nv_add_f32 v247,v247,v251\n" \
"v_mfma_f32_16x16x32_bf16 v[224:227],a[208:211],v[104:107],v[224:227]\nv_add_f32 v48,v48,v244\nv_add_f32 v49,v49,v245\nv_mfma_f32_16x16x32_bf16 v[228:231],a[212:215],v[104:107],v[228:231]\n" \
"v_add_f32 v50,v50,v246\nv_add_f32 v51,v51,v247\nv_mfma_f32_16x16x32_bf16 v[232:235],a[216:219],v[104:107],v[232:235]\nv_cvt_pk_bf16_f32 v38,v48,v49\n" \
"v_cvt_pk_bf16_f32 v39,v50,v51\nv_mfma_f32_16x16x32_bf16 v[236:239],a[220:223],v[104:107],v[236:239]\nv_add_u32 v59,409600,v59\nbuffer_store_dwordx4 v[36:39],v59,s[36:39],s74 offen sc1\n" \
"s_branch .Le_%=\n.Lzl_%=:\ns_branch .Lzr_%=\n.Lls_%=:\n" \
"v_mfma_f32_16x16x32_bf16 v[20:23],v[64:67],a[0:3],0\nv_and_b32 v59,0x3ff,v59\nv_mfma_f32_16x16x32_bf16 v[20:23],v[68:71],a[16:19],v[20:23]\ns_add_u32 s4,s12,409600\n" \
"s_addc_u32 s5,s13,0\nv_mfma_f32_16x16x32_bf16 v[24:27],v[64:67],a[4:7],0\ns_add_u32 s6,s14,409600\ns_addc_u32 s7,s15,0\n" \
"v_mfma_f32_16x16x32_bf16 v[24:27],v[68:71],a[20:23],v[24:27]\ns_add_u32 s8,s16,409600\ns_addc_u32 s9,s17,0\nv_mfma_f32_16x16x32_bf16 v[28:31],v[64:67],a[8:11],0\n" \
"ds_write_addtid_b32 v0\nds_write_addtid_b32 v1 offset:544\nv_mfma_f32_16x16x32_bf16 v[28:31],v[68:71],a[24:27],v[28:31]\nds_write_addtid_b32 v2 offset:256\n" \
"ds_write_addtid_b32 v3 offset:800\nv_mfma_f32_16x16x32_bf16 v[32:35],v[64:67],a[12:15],0\nds_write_addtid_b32 v4 offset:1056\nds_write_addtid_b32 v5 offset:1600\n" \
"v_mfma_f32_16x16x32_bf16 v[32:35],v[68:71],a[28:31],v[32:35]\nds_write_addtid_b32 v6 offset:1312\ns_waitcnt vmcnt(16)\nv_mfma_f32_16x16x32_bf16 v[20:23],v[72:75],a[32:35],v[20:23]\n" \
"v_add_u32 v71,s57,v59\ns_waitcnt vmcnt(15)\nv_mfma_f32_16x16x32_bf16 v[20:23],v[76:79],a[48:51],v[20:23]\ns_waitcnt vmcnt(8)\n" \
"v_cmp_eq_u32 vcc,0,v63\nv_mfma_f32_16x16x32_bf16 v[24:27],v[72:75],a[36:39],v[24:27]\nds_read_b32 v64,v71 offset:28672\nds_read_b64 v[68:69],v71 offset:29696\n" \
"v_mfma_f32_16x16x32_bf16 v[24:27],v[76:79],a[52:55],v[24:27]\ns_and_b64 vcc,vcc,s[86:87]\ns_cbranch_scc1 .LsaS_%=\n.LbaS_%=:\n" \
"v_mfma_f32_16x16x32_bf16 v[28:31],v[72:75],a[40:43],v[28:31]\nds_write_addtid_b32 v7 offset:1856\nds_write_addtid_b32 v8 offset:2112\nv_mfma_f32_16x16x32_bf16 v[28:31],v[76:79],a[56:59],v[28:31]\n" \
"ds_write_addtid_b32 v9 offset:2656\nds_write_addtid_b32 v10 offset:2368\nv_mfma_f32_16x16x32_bf16 v[32:35],v[72:75],a[44:47],v[32:35]\ns_waitcnt lgkmcnt(4)\n" \
"v_xor_b32 v65,32,v64\nv_mfma_f32_16x16x32_bf16 v[32:35],v[76:79],a[60:63],v[32:35]\nv_xor_b32 v66,64,v64\nv_mfma_f32_16x16x32_bf16 v[20:23],v[80:83],a[64:67],v[20:23]\n" \
"global_load_dwordx4 v[72:75],v59,s[12:13] sc1\nv_mfma_f32_16x16x32_bf16 v[20:23],v[84:87],a[80:83],v[20:23]\nv_xor_b32 v67,96,v64\nv_add_u32 v70,0xf800,v68\n" \
"v_mfma_f32_16x16x32_bf16 v[24:27],v[80:83],a[68:71],v[24:27]\nv_add_u32 v71,0xf800,v69\nds_write_addtid_b32 v11 offset:2912\nv_mfma_f32_16x16x32_bf16 v[24:27],v[84:87],a[84:87],v[24:27]\n" \
"ds_write_addtid_b32 v12 offset:3168\nds_write_addtid_b32 v13 offset:3712\nv_mfma_f32_16x16x32_bf16 v[28:31],v[80:83],a[72:75],v[28:31]\nds_write_addtid_b32 v14 offset:3424\n" \
"ds_write_addtid_b32 v15 offset:3968\nv_mfma_f32_16x16x32_bf16 v[28:31],v[84:87],a[88:91],v[28:31]\nds_write_addtid_b32 v16 offset:4224\nds_write_addtid_b32 v17 offset:4512\n" \
"v_mfma_f32_16x16x32_bf16 v[32:35],v[80:83],a[76:79],v[32:35]\nds_write_addtid_b32 v18 offset:4800\nds_write_addtid_b32 v19 offset:5088\nv_mfma_f32_16x16x32_bf16 v[32:35],v[84:87],a[92:95],v[32:35]\n" \
"s_add_u32 m0,s57,24576\nv_mfma_f32_16x16x32_bf16 v[20:23],v[88:91],a[96:99],v[20:23]\nbuffer_load_dwordx4 v68,s[40:43],s21 offen lds\nv_mfma_f32_16x16x32_bf16 v[20:23],v[92:95],a[112:115],v[20:23]\n" \
"ds_read_b64_tr_b16 a[0:1],v64\nds_read_b64_tr_b16 a[2:3],v64 offset:512\nv_mfma_f32_16x16x32_bf16 v[24:27],v[88:91],a[100:103],v[24:27]\nbuffer_load_dwordx4 v69,s[40:43],s21 offen offset:1024 lds\n" \
"ds_read_b64_tr_b16 a[4:5],v65\nv_mfma_f32_16x16x32_bf16 v[24:27],v[92:95],a[116:119],v[24:27]\nds_read_b64_tr_b16 a[6:7],v65 offset:512\nds_read_b64_tr_b16 a[8:9],v66\n" \
"v_mfma_f32_16x16x32_bf16 v[28:31],v[88:91],a[104:107],v[28:31]\nbuffer_load_dwordx4 v70,s[40:43],s21 offen offset:2048 lds\nds_read_b64_tr_b16 a[10:11],v66 offset:512\nv_mfma_f32_16x16x32_bf16 v[28:31],v[92:95],a[120:123],v[28:31]\n" \
"s_waitcnt lgkmcnt(0)\ns_barrier\nv_mfma_f32_16x16x32_bf16 v[32:35],v[88:91],a[108:111],v[32:35]\nbuffer_load_dwordx4 v71,s[40:43],s21 offen offset:3072 lds\n" \
"ds_read_b64_tr_b16 a[12:13],v67\nv_mfma_f32_16x16x32_bf16 v[32:35],v[92:95],a[124:127],v[32:35]\nds_read_b64_tr_b16 a[14:15],v67 offset:512\ns_waitcnt vmcnt(12)\n" \
"v_mfma_f32_16x16x32_bf16 v[20:23],v[96:99],a[128:131],v[20:23]\nglobal_load_dwordx4 v[76:79],v59,s[12:13] offset:1024 sc1\ns_waitcnt vmcnt(12)\nv_mfma_f32_16x16x32_bf16 v[20:23],v[100:103],a[144:147],v[20:23]\n" \
"ds_read_b64_tr_b16 a[16:17],v64 offset:4096\nds_read_b64_tr_b16 a[18:19],v64 offset:4608\nv_mfma_f32_16x16x32_bf16 v[24:27],v[96:99],a[132:135],v[24:27]\nglobal_load_dwordx4 v[80:83],v59,s[12:13] offset:2048 sc1\n" \
"ds_read_b64_tr_b16 a[20:21],v65 offset:4096\nv_mfma_f32_16x16x32_bf16 v[24:27],v[100:103],a[148:151],v[24:27]\ns_waitcnt lgkmcnt(3)\ns_add_u32 m0,s57,0\n" \
"v_mfma_f32_16x16x32_bf16 v[28:31],v[96:99],a[136:139],v[28:31]\nbuffer_load_dwordx4 v68,s[40:43],s60 offen lds\nds_read_b64_tr_b16 a[22:23],v65 offset:4608\nv_mfma_f32_16x16x32_bf16 v[28:31],v[100:103],a[152:155],v[28:31]\n" \
"ds_read_b64_tr_b16 a[24:25],v66 offset:4096\nds_read_b64_tr_b16 a[26:27],v66 offset:4608\nv_mfma_f32_16x16x32_bf16 v[32:35],v[96:99],a[140:143],v[32:35]\nbuffer_load_dwordx4 v69,s[40:43],s60 offen offset:1024 lds\n" \
"ds_read_b64_tr_b16 a[28:29],v67 offset:4096\nv_mfma_f32_16x16x32_bf16 v[32:35],v[100:103],a[156:159],v[32:35]\nds_read_b64_tr_b16 a[30:31],v67 offset:4608\ns_waitcnt vmcnt(14)\n" \
"v_mfma_f32_16x16x32_bf16 v[20:23],v[104:107],a[160:163],v[20:23]\nbuffer_load_dwordx4 v70,s[40:43],s60 offen offset:2048 lds\ns_waitcnt vmcnt(14)\nv_mfma_f32_16x16x32_bf16 v[20:23],v[108:111],a[176:179],v[20:23]\n" \
"ds_read_b64_tr_b16 a[32:33],v64 offset:8192\nds_read_b64_tr_b16 a[34:35],v64 offset:8704\nv_mfma_f32_16x16x32_bf16 v[24:27],v[104:107],a[164:167],v[24:27]\nbuffer_load_dwordx4 v71,s[40:43],s60 offen offset:3072 lds\n" \
"ds_read_b64_tr_b16 a[36:37],v65 offset:8192\nv_mfma_f32_16x16x32_bf16 v[24:27],v[108:111],a[180:183],v[24:27]\nds_read_b64_tr_b16 a[38:39],v65 offset:8704\nds_read_b64_tr_b16 a[40:41],v66 offset:8192\n" \
"v_mfma_f32_16x16x32_bf16 v[28:31],v[104:107],a[168:171],v[28:31]\nglobal_load_dwordx4 v[84:87],v59,s[14:15] sc1\nds_read_b64_tr_b16 a[42:43],v66 offset:8704\nv_mfma_f32_16x16x32_bf16 v[28:31],v[108:111],a[184:187],v[28:31]\n" \
"s_waitcnt lgkmcnt(6)\ns_add_u32 m0,s57,4096\nv_mfma_f32_16x16x32_bf16 v[32:35],v[104:107],a[172:175],v[32:35]\nbuffer_load_dwordx4 v68,s[40:43],s61 offen lds\n" \
"ds_read_b64_tr_b16 a[44:45],v67 offset:8192\nv_mfma_f32_16x16x32_bf16 v[32:35],v[108:111],a[188:191],v[32:35]\nds_read_b64_tr_b16 a[46:47],v67 offset:8704\ns_waitcnt vmcnt(16)\n" \
"v_mfma_f32_16x16x32_bf16 v[20:23],v[112:115],a[192:195],v[20:23]\nbuffer_load_dwordx4 v69,s[40:43],s61 offen offset:1024 lds\ns_waitcnt vmcnt(16)\nv_mfma_f32_16x16x32_bf16 v[20:23],v[116:119],a[208:211],v[20:23]\n" \
"ds_read_b64_tr_b16 a[48:49],v64 offset:12288\nds_read_b64_tr_b16 a[50:51],v64 offset:12800\nv_mfma_f32_16x16x32_bf16 v[24:27],v[112:115],a[196:199],v[24:27]\nbuffer_load_dwordx4 v70,s[40:43],s61 offen offset:2048 lds\n" \
"ds_read_b64_tr_b16 a[52:53],v65 offset:12288\nv_mfma_f32_16x16x32_bf16 v[24:27],v[116:119],a[212:215],v[24:27]\nds_read_b64_tr_b16 a[54:55],v65 offset:12800\nds_read_b64_tr_b16 a[56:57],v66 offset:12288\n" \
"v_mfma_f32_16x16x32_bf16 v[28:31],v[112:115],a[200:203],v[28:31]\nbuffer_load_dwordx4 v71,s[40:43],s61 offen offset:3072 lds\nds_read_b64_tr_b16 a[58:59],v66 offset:12800\nv_mfma_f32_16x16x32_bf16 v[28:31],v[116:119],a[216:219],v[28:31]\n" \
"ds_read_b64_tr_b16 a[60:61],v67 offset:12288\nds_read_b64_tr_b16 a[62:63],v67 offset:12800\nv_mfma_f32_16x16x32_bf16 v[32:35],v[112:115],a[204:207],v[32:35]\nglobal_load_dwordx4 v[88:91],v59,s[14:15] offset:1024 sc1\n" \
"ds_read_b64_tr_b16 a[64:65],v64 offset:16384\nv_mfma_f32_16x16x32_bf16 v[32:35],v[116:119],a[220:223],v[32:35]\nds_read_b64_tr_b16 a[66:67],v64 offset:16896\ns_waitcnt vmcnt(18)\n" \
"v_mfma_f32_16x16x32_bf16 v[20:23],v[120:123],a[224:227],v[20:23]\nds_read_b64_tr_b16 a[68:69],v65 offset:16384\ns_waitcnt vmcnt(17)\nv_mfma_f32_16x16x32_bf16 v[20:23],v[124:127],a[240:243],v[20:23]\n" \
"s_waitcnt lgkmcnt(11)\ns_add_u32 m0,s57,8192\nv_mfma_f32_16x16x32_bf16 v[24:27],v[120:123],a[228:231],v[24:27]\nbuffer_load_dwordx4 v68,s[40:43],s62 offen lds\n" \
"ds_read_b64_tr_b16 a[70:71],v65 offset:16896\nv_mfma_f32_16x16x32_bf16 v[24:27],v[124:127],a[244:247],v[24:27]\nds_read_b64_tr_b16 a[72:73],v66 offset:16384\nds_read_b64_tr_b16 a[74:75],v66 offset:16896\n" \
"v_mfma_f32_16x16x32_bf16 v[28:31],v[120:123],a[232:235],v[28:31]\nbuffer_load_dwordx4 v69,s[40:43],s62 offen offset:1024 lds\nds_read_b64_tr_b16 a[76:77],v67 offset:16384\nv_mfma_f32_16x16x32_bf16 v[28:31],v[124:127],a[248:251],v[28:31]\n" \
"ds_read_b64_tr_b16 a[78:79],v67 offset:16896\nds_read_b64_tr_b16 a[80:81],v64 offset:20480\nv_mfma_f32_16x16x32_bf16 v[32:35],v[120:123],a[236:239],v[32:35]\nbuffer_load_dwordx4 v70,s[40:43],s62 offen offset:2048 lds\n" \
"ds_read_b64_tr_b16 a[82:83],v64 offset:20992\nv_mfma_f32_16x16x32_bf16 v[32:35],v[124:127],a[252:255],v[32:35]\nds_read_b64_tr_b16 a[84:85],v65 offset:20480\ns_waitcnt vmcnt(19)\n" \
"v_mfma_f32_16x16x32_bf16 v[208:211],v[128:131],v[72:75],0\nbuffer_load_dwordx4 v71,s[40:43],s62 offen offset:3072 lds\ns_waitcnt vmcnt(15)\nv_mfma_f32_16x16x32_bf16 v[208:211],v[160:163],v[76:79],v[208:211]\n" \
"ds_read_b64_tr_b16 a[86:87],v65 offset:20992\nds_read_b64_tr_b16 a[88:89],v66 offset:20480\nv_mfma_f32_16x16x32_bf16 v[212:215],v[132:135],v[72:75],0\nglobal_load_dwordx4 v[92:95],v59,s[14:15] offset:2048 sc1\n" \
"ds_read_b64_tr_b16 a[90:91],v66 offset:20992\nv_mfma_f32_16x16x32_bf16 v[212:215],v[164:167],v[76:79],v[212:215]\nds_read_b64_tr_b16 a[92:93],v67 offset:20480\nds_read_b64_tr_b16 a[94:95],v67 offset:20992\n" \
"v_mfma_f32_16x16x32_bf16 v[216:219],v[136:139],v[72:75],0\nglobal_load_dwordx4 v[108:111],v59,s[4:5] sc1\nv_mul_f32 v36,v36,v40\nv_mfma_f32_16x16x32_bf16 v[216:219],v[168:171],v[76:79],v[216:219]\n" \
"s_waitcnt lgkmcnt(15)\ns_add_u32 m0,s57,12288\nv_mfma_f32_16x16x32_bf16 v[220:223],v[140:143],v[72:75],0\nbuffer_load_dwordx4 v68,s[40:43],s63 offen lds\n" \
"ds_read_b64_tr_b16 a[96:97],v64 offset:24576\nv_mfma_f32_16x16x32_bf16 v[220:223],v[172:175],v[76:79],v[220:223]\nds_read_b64_tr_b16 a[98:99],v64 offset:25088\nds_read_b64_tr_b16 a[100:101],v65 offset:24576\n" \
"v_mfma_f32_16x16x32_bf16 v[224:227],v[144:147],v[72:75],0\nbuffer_load_dwordx4 v69,s[40:43],s63 offen offset:1024 lds\nds_read_b64_tr_b16 a[102:103],v65 offset:25088\nv_mfma_f32_16x16x32_bf16 v[224:227],v[176:179],v[76:79],v[224:227]\n" \
"ds_read_b64_tr_b16 a[104:105],v66 offset:24576\nds_read_b64_tr_b16 a[106:107],v66 offset:25088\nv_mfma_f32_16x16x32_bf16 v[228:231],v[148:151],v[72:75],0\nbuffer_load_dwordx4 v70,s[40:43],s63 offen offset:2048 lds\n" \
"ds_read_b64_tr_b16 a[108:109],v67 offset:24576\nv_mfma_f32_16x16x32_bf16 v[228:231],v[180:183],v[76:79],v[228:231]\nds_read_b64_tr_b16 a[110:111],v67 offset:25088\nv_mul_f32 v37,v37,v41\n" \
"v_mfma_f32_16x16x32_bf16 v[232:235],v[152:155],v[72:75],0\nbuffer_load_dwordx4 v71,s[40:43],s63 offen offset:3072 lds\ns_mov_b32 m0,s56\nv_mfma_f32_16x16x32_bf16 v[232:235],v[184:187],v[76:79],v[232:235]\n" \
"ds_write_addtid_b32 v20 offset:5344\nds_write_addtid_b32 v21 offset:5888\nv_mfma_f32_16x16x32_bf16 v[236:239],v[156:159],v[72:75],0\nglobal_load_dwordx4 v[112:115],v59,s[4:5] offset:1024 sc1\n" \
"ds_write_addtid_b32 v22 offset:5600\nv_mfma_f32_16x16x32_bf16 v[236:239],v[188:191],v[76:79],v[236:239]\ns_waitcnt lgkmcnt(15)\ns_waitcnt vmcnt(21)\n" \
"v_mfma_f32_16x16x32_bf16 v[208:211],a[0:3],v[80:83],v[208:211]\ns_waitcnt vmcnt(16)\nv_mfma_f32_16x16x32_bf16 v[208:211],a[32:35],v[84:87],v[208:211]\nglobal_load_dwordx4 v[116:119],v59,s[4:5] offset:2048 sc1\n" \
"ds_write_addtid_b32 v23 offset:6144\nv_mfma_f32_16x16x32_bf16 v[212:215],a[4:7],v[80:83],v[212:215]\nds_write_addtid_b32 v24 offset:6400\nds_write_addtid_b32 v25 offset:6944\n" \
"v_mfma_f32_16x16x32_bf16 v[212:215],a[36:39],v[84:87],v[212:215]\nds_write_addtid_b32 v26 offset:6656\nds_write_addtid_b32 v27 offset:7200\nv_mfma_f32_16x16x32_bf16 v[216:219],a[8:11],v[80:83],v[216:219]\n" \
"ds_write_addtid_b32 v28 offset:7456\nds_write_addtid_b32 v29 offset:8000\nv_mfma_f32_16x16x32_bf16 v[216:219],a[40:43],v[84:87],v[216:219]\nds_write_addtid_b32 v30 offset:7712\n" \
"ds_write_addtid_b32 v31 offset:8256\nv_mfma_f32_16x16x32_bf16 v[220:223],a[12:15],v[80:83],v[220:223]\nds_write_addtid_b32 v32 offset:8512\nds_write_addtid_b32 v33 offset:9056\n" \
"v_mfma_f32_16x16x32_bf16 v[220:223],a[44:47],v[84:87],v[220:223]\nds_write_addtid_b32 v34 offset:8768\ns_waitcnt lgkmcnt(15)\nv_mfma_f32_16x16x32_bf16 v[224:227],a[16:19],v[80:83],v[224:227]\n" \
"ds_write_addtid_b32 v35 offset:9312\ns_waitcnt lgkmcnt(15)\nv_mfma_f32_16x16x32_bf16 v[224:227],a[48:51],v[84:87],v[224:227]\ns_add_u32 m0,s57,16384\n" \
"v_mfma_f32_16x16x32_bf16 v[228:231],a[20:23],v[80:83],v[228:231]\nbuffer_load_dwordx4 v68,s[40:43],s64 offen lds\nv_mul_f32 v38,v38,v42\nv_mfma_f32_16x16x32_bf16 v[228:231],a[52:55],v[84:87],v[228:231]\n" \
"v_mul_f32 v39,v39,v43\nv_cvt_pk_bf16_f32 v40,v36,v37\nv_mfma_f32_16x16x32_bf16 v[232:235],a[24:27],v[80:83],v[232:235]\nbuffer_load_dwordx4 v69,s[40:43],s64 offen offset:1024 lds\n" \
"v_mfma_f32_16x16x32_bf16 v[232:235],a[56:59],v[84:87],v[232:235]\nds_read_b64_tr_b16 a[112:113],v64\nds_read_b64_tr_b16 a[114:115],v64 offset:512\nv_mfma_f32_16x16x32_bf16 v[236:239],a[28:31],v[80:83],v[236:239]\n" \
"buffer_load_dwordx4 v70,s[40:43],s64 offen offset:2048 lds\nds_read_b64_tr_b16 a[116:117],v65\nv_mfma_f32_16x16x32_bf16 v[236:239],a[60:63],v[84:87],v[236:239]\ns_waitcnt lgkmcnt(15)\n" \
"s_waitcnt vmcnt(15)\nv_mfma_f32_16x16x32_bf16 v[208:211],a[64:67],v[88:91],v[208:211]\ns_waitcnt vmcnt(10)\nv_mfma_f32_16x16x32_bf16 v[208:211],a[96:99],v[92:95],v[208:211]\n" \
"buffer_load_dwordx4 v71,s[40:43],s64 offen offset:3072 lds\nds_read_b64_tr_b16 a[118:119],v65 offset:512\nv_mfma_f32_16x16x32_bf16 v[212:215],a[68:71],v[88:91],v[212:215]\ns_waitcnt lgkmcnt(0)\n" \
"s_barrier\nv_mfma_f32_16x16x32_bf16 v[212:215],a[100:103],v[92:95],v[212:215]\nglobal_load_dwordx4 v[120:123],v59,s[6:7] sc1\nds_read_b64_tr_b16 a[120:121],v66\n" \
"v_mfma_f32_16x16x32_bf16 v[216:219],a[72:75],v[88:91],v[216:219]\ns_add_u32 m0,s57,20480\nv_mfma_f32_16x16x32_bf16 v[216:219],a[104:107],v[92:95],v[216:219]\nbuffer_load_dwordx4 v68,s[40:43],s65 offen lds\n" \
"ds_read_b64_tr_b16 a[122:123],v66 offset:512\nv_mfma_f32_16x16x32_bf16 v[220:223],a[76:79],v[88:91],v[220:223]\nds_read_b64_tr_b16 a[124:125],v67\nds_read_b64_tr_b16 a[126:127],v67 offset:512\n" \
"v_mfma_f32_16x16x32_bf16 v[220:223],a[108:111],v[92:95],v[220:223]\nbuffer_load_dwordx4 v69,s[40:43],s65 offen offset:1024 lds\nv_mfma_f32_16x16x32_bf16 v[224:227],a[80:83],v[88:91],v[224:227]\nds_read_b64_tr_b16 a[128:129],v64 offset:4096\n" \
"v_mfma_f32_16x16x32_bf16 v[228:231],a[84:87],v[88:91],v[228:231]\nbuffer_load_dwordx4 v70,s[40:43],s65 offen offset:2048 lds\nds_read_b64_tr_b16 a[130:131],v64 offset:4608\nv_mfma_f32_16x16x32_bf16 v[232:235],a[88:91],v[88:91],v[232:235]\n" \
"ds_read_b64_tr_b16 a[132:133],v65 offset:4096\nds_read_b64_tr_b16 a[134:135],v65 offset:4608\nv_mfma_f32_16x16x32_bf16 v[236:239],a[92:95],v[88:91],v[236:239]\nbuffer_load_dwordx4 v71,s[40:43],s65 offen offset:3072 lds\n" \
"s_waitcnt vmcnt(15)\nv_mfma_f32_16x16x32_bf16 v[0:3],v[128:131],v[108:111],0\nds_read_b64_tr_b16 a[136:137],v66 offset:4096\ns_waitcnt vmcnt(10)\n" \
"v_mfma_f32_16x16x32_bf16 v[0:3],v[160:163],v[112:115],v[0:3]\nglobal_load_dwordx4 v[124:127],v59,s[6:7] offset:1024 sc1\nds_read_b64_tr_b16 a[138:139],v66 offset:4608\nv_mfma_f32_16x16x32_bf16 v[4:7],v[132:135],v[108:111],0\n" \
"s_add_u32 m0,s57,24576\nv_mfma_f32_16x16x32_bf16 v[4:7],v[164:167],v[112:115],v[4:7]\nbuffer_load_dwordx4 v68,s[40:43],s66 offen lds\nds_read_b64_tr_b16 a[140:141],v67 offset:4096\n" \
"v_mfma_f32_16x16x32_bf16 v[8:11],v[136:139],v[108:111],0\nds_read_b64_tr_b16 a[142:143],v67 offset:4608\nv_cvt_pk_bf16_f32 v41,v38,v39\nv_mfma_f32_16x16x32_bf16 v[8:11],v[168:171],v[112:115],v[8:11]\n" \
"buffer_load_dwordx4 v69,s[40:43],s66 offen offset:1024 lds\nbuffer_store_dwordx2 v[40:41],v61,s[36:39],s52 offen sc1\nds_read_b128 v[36:39],v58\nds_read_b128 v[40:43],v58 offset:9568\n" \
"ds_read_b128 v[44:47],v58 offset:19136\nds_read_b128 v[48:51],v58 offset:28704\ns_waitcnt lgkmcnt(2)\nv_add_f32 v36,v36,v40\n" \
"v_add_f32 v37,v37,v41\nv_add_f32 v38,v38,v42\nv_add_f32 v39,v39,v43\nv_add_f32 v36,v36,v52\n" \
"v_add_f32 v37,v37,v53\nv_add_f32 v38,v38,v54\nv_add_f32 v39,v39,v55\ns_waitcnt lgkmcnt(0)\n" \
"v_add_f32 v44,v44,v48\nv_add_f32 v45,v45,v49\nv_add_f32 v46,v46,v50\nv_add_f32 v47,v47,v51\n" \
"v_add_f32 v36,v36,v44\nv_add_f32 v37,v37,v45\nv_add_f32 v38,v38,v46\nv_add_f32 v39,v39,v47\n" \
"v_mul_f32 v40,v36,v36\nv_mul_f32 v41,v37,v37\nv_mul_f32 v42,v38,v38\nv_mul_f32 v43,v39,v39\n" \
"v_fma_f32 v40,v40,s55,v57\nv_fma_f32 v41,v41,s55,v57\nv_fma_f32 v42,v42,s55,v57\nv_fma_f32 v43,v43,s55,v57\n" \
"v_mul_f32 v40,v36,v40\nv_mul_f32 v41,v37,v41\nv_mul_f32 v42,v38,v42\nv_mul_f32 v43,v39,v43\n" \
"v_exp_f32 v40,v40\nv_exp_f32 v41,v41\nv_exp_f32 v42,v42\nv_exp_f32 v43,v43\n" \
"v_add_f32 v40,1.0,v40\nv_add_f32 v41,1.0,v41\nv_add_f32 v42,1.0,v42\nv_add_f32 v43,1.0,v43\n" \
"v_rcp_f32 v40,v40\nv_rcp_f32 v41,v41\nv_rcp_f32 v42,v42\nv_rcp_f32 v43,v43\n" \
"v_mul_f32 v36,v36,v40\nv_mul_f32 v37,v37,v41\nv_mul_f32 v38,v38,v42\nv_mul_f32 v39,v39,v43\n" \
"v_cvt_pk_bf16_f32 v40,v36,v37\nv_cvt_pk_bf16_f32 v41,v38,v39\nv_mfma_f32_16x16x32_bf16 v[12:15],v[140:143],v[108:111],0\ns_add_u32 s52,s52,409600\n" \
"buffer_store_dwordx2 v[40:41],v61,s[36:39],s52 offen sc1\nv_mfma_f32_16x16x32_bf16 v[12:15],v[172:175],v[112:115],v[12:15]\nbuffer_load_dwordx4 v70,s[40:43],s66 offen offset:2048 lds\nv_mfma_f32_16x16x32_bf16 v[16:19],v[144:147],v[108:111],0\n" \
"ds_read_b64_tr_b16 a[144:145],v64 offset:8192\nds_read_b64_tr_b16 a[146:147],v64 offset:8704\nv_mfma_f32_16x16x32_bf16 v[16:19],v[176:179],v[112:115],v[16:19]\nbuffer_load_dwordx4 v71,s[40:43],s66 offen offset:3072 lds\n" \
"ds_read_b64_tr_b16 a[148:149],v65 offset:8192\nv_mfma_f32_16x16x32_bf16 v[20:23],v[148:151],v[108:111],0\nds_read_b64_tr_b16 a[150:151],v65 offset:8704\nds_read_b64_tr_b16 a[152:153],v66 offset:8192\n" \
"v_mfma_f32_16x16x32_bf16 v[20:23],v[180:183],v[112:115],v[20:23]\nglobal_load_dwordx4 v[192:195],v59,s[6:7] offset:2048 sc1\nds_read_b64_tr_b16 a[154:155],v66 offset:8704\nv_mfma_f32_16x16x32_bf16 v[24:27],v[152:155],v[108:111],0\n" \
"ds_read_b64_tr_b16 a[156:157],v67 offset:8192\nds_read_b64_tr_b16 a[158:159],v67 offset:8704\nv_mfma_f32_16x16x32_bf16 v[24:27],v[184:187],v[112:115],v[24:27]\nglobal_load_dwordx4 v[96:99],v59,s[16:17] sc1\n" \
"ds_read_b32 v48,v60\nv_mfma_f32_16x16x32_bf16 v[28:31],v[156:159],v[108:111],0\nds_read_b32 v49,v60 offset:9568\nds_read_b32 v50,v60 offset:19136\n" \
"v_mfma_f32_16x16x32_bf16 v[28:31],v[188:191],v[112:115],v[28:31]\nglobal_load_dwordx4 v[100:103],v59,s[16:17] offset:1024 sc1\ns_waitcnt vmcnt(19)\nv_mfma_f32_16x16x32_bf16 v[0:3],a[0:3],v[116:119],v[0:3]\n" \
"ds_read_b32 v51,v60 offset:28704\ns_waitcnt vmcnt(14)\nv_mfma_f32_16x16x32_bf16 v[0:3],a[32:35],v[120:123],v[0:3]\nglobal_load_dwordx4 v[104:107],v59,s[16:17] offset:2048 sc1\n" \
"v_mfma_f32_16x16x32_bf16 v[4:7],a[4:7],v[116:119],v[4:7]\nds_read_b64_tr_b16 a[160:161],v64 offset:12288\nv_mfma_f32_16x16x32_bf16 v[4:7],a[36:39],v[120:123],v[4:7]\nglobal_load_dwordx4 v[196:199],v59,s[8:9] sc1\n" \
"ds_read_b64_tr_b16 a[162:163],v64 offset:12800\nv_mfma_f32_16x16x32_bf16 v[8:11],a[8:11],v[116:119],v[8:11]\nds_read_b64_tr_b16 a[164:165],v65 offset:12288\nds_read_b64_tr_b16 a[166:167],v65 offset:12800\n" \
"v_mfma_f32_16x16x32_bf16 v[8:11],a[40:43],v[120:123],v[8:11]\nglobal_load_dwordx4 v[200:203],v59,s[8:9] offset:1024 sc1\nds_read_b64_tr_b16 a[168:169],v66 offset:12288\nv_mfma_f32_16x16x32_bf16 v[12:15],a[12:15],v[116:119],v[12:15]\n" \
"ds_read_b64_tr_b16 a[170:171],v66 offset:12800\nds_read_b64_tr_b16 a[172:173],v67 offset:12288\nv_mfma_f32_16x16x32_bf16 v[12:15],a[44:47],v[120:123],v[12:15]\nglobal_load_dwordx4 v[204:207],v59,s[8:9] offset:2048 sc1\n" \
"ds_read_b64_tr_b16 a[174:175],v67 offset:12800\nv_mfma_f32_16x16x32_bf16 v[16:19],a[16:19],v[116:119],v[16:19]\ns_add_u32 s59,s59,0x10000\nbuffer_load_dword v63,v61,s[36:39],s59 offen sc1\n" \
"v_mfma_f32_16x16x32_bf16 v[16:19],a[48:51],v[120:123],v[16:19]\nv_add_u32 v59,819200,v59\ns_waitcnt lgkmcnt(8)\nv_mfma_f32_16x16x32_bf16 v[20:23],a[20:23],v[116:119],v[20:23]\n" \
"v_add_f32 v48,v48,v49\nv_add_f32 v50,v50,v51\nv_mfma_f32_16x16x32_bf16 v[20:23],a[52:55],v[120:123],v[20:23]\nv_add_f32 v48,v48,v56\n" \
"v_add_f32 v48,v48,v50\nv_mfma_f32_16x16x32_bf16 v[24:27],a[24:27],v[116:119],v[24:27]\nv_mul_f32 v49,v48,v48\nv_fma_f32 v49,v49,s55,v57\n" \
"v_mfma_f32_16x16x32_bf16 v[24:27],a[56:59],v[120:123],v[24:27]\nv_mul_f32 v49,v48,v49\nv_mfma_f32_16x16x32_bf16 v[28:31],a[28:31],v[116:119],v[28:31]\nv_exp_f32 v49,v49\n" \
"v_mfma_f32_16x16x32_bf16 v[28:31],a[60:63],v[120:123],v[28:31]\nv_add_f32 v49,1.0,v49\ns_waitcnt vmcnt(14)\nv_mfma_f32_16x16x32_bf16 v[0:3],a[64:67],v[124:127],v[0:3]\n" \
"s_waitcnt vmcnt(7)\nv_mfma_f32_16x16x32_bf16 v[0:3],a[96:99],v[192:195],v[0:3]\nv_rcp_f32 v49,v49\nv_mfma_f32_16x16x32_bf16 v[4:7],a[68:71],v[124:127],v[4:7]\n" \
"v_mul_f32 v48,v48,v49\nv_cvt_pk_bf16_f32 v49,v48,v48\nv_mfma_f32_16x16x32_bf16 v[4:7],a[100:103],v[192:195],v[4:7]\nbuffer_store_short v49,v62,s[36:39],s52 offen sc1\n" \
"ds_read_b128 v[36:39],v58 offset:5344\nds_read_b128 v[40:43],v58 offset:14912\nv_mfma_f32_16x16x32_bf16 v[8:11],a[72:75],v[124:127],v[8:11]\nds_read_b128 v[44:47],v58 offset:24480\n" \
"ds_read_b128 v[48:51],v58 offset:34048\nv_mfma_f32_16x16x32_bf16 v[8:11],a[104:107],v[192:195],v[8:11]\ns_add_u32 s52,s52,409600\nv_mfma_f32_16x16x32_bf16 v[12:15],a[76:79],v[124:127],v[12:15]\n" \
"v_mfma_f32_16x16x32_bf16 v[12:15],a[108:111],v[192:195],v[12:15]\nds_read_b64_tr_b16 a[176:177],v64 offset:16384\nv_mfma_f32_16x16x32_bf16 v[16:19],a[80:83],v[124:127],v[16:19]\nds_read_b64_tr_b16 a[178:179],v64 offset:16896\n" \
"ds_read_b64_tr_b16 a[180:181],v65 offset:16384\nv_mfma_f32_16x16x32_bf16 v[20:23],a[84:87],v[124:127],v[20:23]\nds_read_b64_tr_b16 a[182:183],v65 offset:16896\nds_read_b64_tr_b16 a[184:185],v66 offset:16384\n" \
"v_mfma_f32_16x16x32_bf16 v[24:27],a[88:91],v[124:127],v[24:27]\nds_read_b64_tr_b16 a[186:187],v66 offset:16896\nds_read_b64_tr_b16 a[188:189],v67 offset:16384\nv_mfma_f32_16x16x32_bf16 v[28:31],a[92:95],v[124:127],v[28:31]\n" \
"ds_read_b64_tr_b16 a[190:191],v67 offset:16896\ns_waitcnt lgkmcnt(10)\nv_mfma_f32_16x16x32_bf16 v[224:227],a[112:115],v[92:95],v[224:227]\nv_add_f32 v36,v36,v40\n" \
"s_waitcnt vmcnt(7)\nv_mfma_f32_16x16x32_bf16 v[224:227],a[144:147],v[96:99],v[224:227]\nv_add_f32 v37,v37,v41\nv_add_f32 v38,v38,v42\n" \
"v_add_f32 v39,v39,v43\nv_mfma_f32_16x16x32_bf16 v[228:231],a[116:119],v[92:95],v[228:231]\nv_add_f32 v36,v36,v52\nv_add_f32 v37,v37,v53\n" \
"v_add_f32 v38,v38,v54\nv_mfma_f32_16x16x32_bf16 v[228:231],a[148:151],v[96:99],v[228:231]\nv_add_f32 v39,v39,v55\ns_waitcnt lgkmcnt(8)\n" \
"v_add_f32 v44,v44,v48\nv_mfma_f32_16x16x32_bf16 v[232:235],a[120:123],v[92:95],v[232:235]\nv_add_f32 v45,v45,v49\nv_add_f32 v46,v46,v50\n" \
"v_add_f32 v47,v47,v51\nv_mfma_f32_16x16x32_bf16 v[232:235],a[152:155],v[96:99],v[232:235]\nds_read_b64_tr_b16 a[192:193],v64 offset:20480\nds_read_b64_tr_b16 a[194:195],v64 offset:20992\n" \
"v_mfma_f32_16x16x32_bf16 v[236:239],a[124:127],v[92:95],v[236:239]\nds_read_b64_tr_b16 a[196:197],v65 offset:20480\nds_read_b64_tr_b16 a[198:199],v65 offset:20992\nv_add_f32 v36,v36,v44\n" \
"v_mfma_f32_16x16x32_bf16 v[236:239],a[156:159],v[96:99],v[236:239]\nds_read_b64_tr_b16 a[200:201],v66 offset:20480\nds_read_b64_tr_b16 a[202:203],v66 offset:20992\nv_mfma_f32_16x16x32_bf16 v[208:211],a[128:131],v[96:99],v[208:211]\n" \
"ds_read_b64_tr_b16 a[204:205],v67 offset:20480\ns_waitcnt vmcnt(6)\nv_mfma_f32_16x16x32_bf16 v[208:211],a[160:163],v[100:103],v[208:211]\nds_read_b64_tr_b16 a[206:207],v67 offset:20992\n" \
"s_waitcnt vmcnt(9)\nds_read_b64_tr_b16 a[208:209],v64 offset:24576\nds_read_b64_tr_b16 a[210:211],v64 offset:25088\nv_mfma_f32_16x16x32_bf16 v[212:215],a[132:135],v[96:99],v[212:215]\n" \
"ds_read_b64_tr_b16 a[212:213],v65 offset:24576\nds_read_b64_tr_b16 a[214:215],v65 offset:25088\nv_add_f32 v37,v37,v45\nv_mfma_f32_16x16x32_bf16 v[212:215],a[164:167],v[100:103],v[212:215]\n" \
"ds_read_b64_tr_b16 a[216:217],v66 offset:24576\nds_read_b64_tr_b16 a[218:219],v66 offset:25088\nv_add_f32 v38,v38,v46\nv_mfma_f32_16x16x32_bf16 v[216:219],a[136:139],v[96:99],v[216:219]\n" \
"ds_read_b64_tr_b16 a[220:221],v67 offset:24576\nds_read_b64_tr_b16 a[222:223],v67 offset:25088\nv_add_f32 v39,v39,v47\nv_mfma_f32_16x16x32_bf16 v[216:219],a[168:171],v[100:103],v[216:219]\n" \
"v_mul_f32 v40,v36,v36\nv_mul_f32 v41,v37,v37\nv_mul_f32 v42,v38,v38\nv_mfma_f32_16x16x32_bf16 v[220:223],a[140:143],v[96:99],v[220:223]\n" \
"v_mul_f32 v43,v39,v39\nv_fma_f32 v40,v40,s55,v57\nv_fma_f32 v41,v41,s55,v57\nv_mfma_f32_16x16x32_bf16 v[220:223],a[172:175],v[100:103],v[220:223]\n" \
"v_fma_f32 v42,v42,s55,v57\nv_fma_f32 v43,v43,s55,v57\ns_waitcnt lgkmcnt(15)\nv_mfma_f32_16x16x32_bf16 v[224:227],a[176:179],v[100:103],v[224:227]\n" \
"v_mul_f32 v40,v36,v40\ns_waitcnt lgkmcnt(0)\ns_waitcnt vmcnt(5)\nv_mfma_f32_16x16x32_bf16 v[224:227],a[208:211],v[104:107],v[224:227]\n" \
"v_mul_f32 v41,v37,v41\nv_mul_f32 v42,v38,v42\nv_mul_f32 v43,v39,v43\nv_mfma_f32_16x16x32_bf16 v[228:231],a[180:183],v[100:103],v[228:231]\n" \
"v_exp_f32 v40,v40\nv_mfma_f32_16x16x32_bf16 v[228:231],a[212:215],v[104:107],v[228:231]\nv_exp_f32 v41,v41\nv_mfma_f32_16x16x32_bf16 v[232:235],a[184:187],v[100:103],v[232:235]\n" \
"v_exp_f32 v42,v42\nv_mfma_f32_16x16x32_bf16 v[232:235],a[216:219],v[104:107],v[232:235]\nv_exp_f32 v43,v43\nv_add_f32 v40,1.0,v40\n" \
"v_mfma_f32_16x16x32_bf16 v[236:239],a[188:191],v[100:103],v[236:239]\nv_add_f32 v41,1.0,v41\nv_add_f32 v42,1.0,v42\nv_add_f32 v43,1.0,v43\n" \
"v_rcp_f32 v40,v40\nv_rcp_f32 v41,v41\nv_rcp_f32 v42,v42\nv_rcp_f32 v43,v43\n" \
"v_mul_f32 v36,v36,v40\nv_mul_f32 v37,v37,v41\nv_mul_f32 v38,v38,v42\nv_mul_f32 v39,v39,v43\n" \
"v_cvt_pk_bf16_f32 v40,v36,v37\nv_cvt_pk_bf16_f32 v41,v38,v39\nv_mfma_f32_16x16x32_bf16 v[236:239],a[220:223],v[104:107],v[236:239]\nv_and_b32 v58,0x3ff,v59\n" \
"v_mfma_f32_16x16x32_bf16 v[208:211],a[192:195],v[104:107],v[208:211]\nv_add_u32 v58,s99,v58\nv_add_u32 v46,s98,v58\nv_mfma_f32_16x16x32_bf16 v[212:215],a[196:199],v[104:107],v[212:215]\n" \
"ds_read_b64 v[56:57],v46 offset:28680\nbuffer_store_dwordx2 v[40:41],v61,s[36:39],s52 offen sc1\nv_mfma_f32_16x16x32_bf16 v[216:219],a[200:203],v[104:107],v[216:219]\nglobal_load_dwordx4 v[72:75],v59,s[12:13] sc1\n" \
"v_mfma_f32_16x16x32_bf16 v[220:223],a[204:207],v[104:107],v[220:223]\nv_mfma_f32_16x16x32_bf16 v[16:19],a[112:115],v[192:195],v[16:19]\nglobal_load_dwordx4 v[76:79],v59,s[12:13] offset:1024 sc1\ns_waitcnt vmcnt(7)\n" \
"v_mfma_f32_16x16x32_bf16 v[16:19],a[144:147],v[196:199],v[16:19]\ns_waitcnt lgkmcnt(0)\ns_barrier\ns_mov_b32 m0,s58\n" \
"v_mfma_f32_16x16x32_bf16 v[20:23],a[116:119],v[192:195],v[20:23]\nglobal_load_dwordx4 v[80:83],v59,s[12:13] offset:2048 sc1\nds_write_addtid_b32 v208\nv_mfma_f32_16x16x32_bf16 v[20:23],a[148:151],v[196:199],v[20:23]\n" \
"ds_write_addtid_b32 v209 offset:256\nds_write_addtid_b32 v210 offset:512\nv_mfma_f32_16x16x32_bf16 v[24:27],a[120:123],v[192:195],v[24:27]\nglobal_load_dwordx4 v[84:87],v59,s[14:15] sc1\n" \
"ds_write_addtid_b32 v211 offset:768\nv_mfma_f32_16x16x32_bf16 v[24:27],a[152:155],v[196:199],v[24:27]\nds_write_addtid_b32 v212 offset:1024\nds_write_addtid_b32 v213 offset:1280\n" \
"v_mfma_f32_16x16x32_bf16 v[28:31],a[124:127],v[192:195],v[28:31]\nglobal_load_dwordx4 v[88:91],v59,s[14:15] offset:1024 sc1\nds_write_addtid_b32 v214 offset:1536\nv_mfma_f32_16x16x32_bf16 v[28:31],a[156:159],v[196:199],v[28:31]\n" \
"ds_write_addtid_b32 v215 offset:1792\nds_write_addtid_b32 v216 offset:2048\nv_mfma_f32_16x16x32_bf16 v[0:3],a[128:131],v[196:199],v[0:3]\nglobal_load_dwordx4 v[92:95],v59,s[14:15] offset:2048 sc1\n" \
"s_waitcnt vmcnt(10)\nv_mfma_f32_16x16x32_bf16 v[0:3],a[160:163],v[200:203],v[0:3]\nds_write_addtid_b32 v217 offset:2304\nds_write_addtid_b32 v218 offset:2560\n" \
"v_mfma_f32_16x16x32_bf16 v[4:7],a[132:135],v[196:199],v[4:7]\nglobal_load_dwordx4 v[96:99],v59,s[16:17] sc1\nds_write_addtid_b32 v219 offset:2816\nv_mfma_f32_16x16x32_bf16 v[4:7],a[164:167],v[200:203],v[4:7]\n" \
"ds_write_addtid_b32 v220 offset:3072\nds_write_addtid_b32 v221 offset:3328\nv_mfma_f32_16x16x32_bf16 v[8:11],a[136:139],v[196:199],v[8:11]\nglobal_load_dwordx4 v[100:103],v59,s[16:17] offset:1024 sc1\n" \
"ds_write_addtid_b32 v222 offset:3584\nv_mfma_f32_16x16x32_bf16 v[8:11],a[168:171],v[200:203],v[8:11]\nds_write_addtid_b32 v223 offset:3840\nds_write_addtid_b32 v224 offset:4096\n" \
"v_mfma_f32_16x16x32_bf16 v[12:15],a[140:143],v[196:199],v[12:15]\nglobal_load_dwordx4 v[104:107],v59,s[16:17] offset:2048 sc1\nds_write_addtid_b32 v225 offset:4352\nv_mfma_f32_16x16x32_bf16 v[12:15],a[172:175],v[200:203],v[12:15]\n" \
"ds_write_addtid_b32 v226 offset:4608\nds_write_addtid_b32 v227 offset:4864\nv_mfma_f32_16x16x32_bf16 v[16:19],a[176:179],v[200:203],v[16:19]\nds_write_addtid_b32 v228 offset:5120\n" \
"s_waitcnt vmcnt(12)\nv_mfma_f32_16x16x32_bf16 v[16:19],a[208:211],v[204:207],v[16:19]\nds_write_addtid_b32 v229 offset:5376\nds_write_addtid_b32 v230 offset:5632\n" \
"v_mfma_f32_16x16x32_bf16 v[20:23],a[180:183],v[200:203],v[20:23]\nds_write_addtid_b32 v231 offset:5888\nds_write_addtid_b32 v232 offset:6144\nv_mfma_f32_16x16x32_bf16 v[20:23],a[212:215],v[204:207],v[20:23]\n" \
"ds_write_addtid_b32 v233 offset:6400\nds_write_addtid_b32 v234 offset:6656\nv_mfma_f32_16x16x32_bf16 v[24:27],a[184:187],v[200:203],v[24:27]\nds_write_addtid_b32 v235 offset:6912\n" \
"ds_write_addtid_b32 v236 offset:7168\nv_mfma_f32_16x16x32_bf16 v[24:27],a[216:219],v[204:207],v[24:27]\nds_write_addtid_b32 v237 offset:7424\nds_write_addtid_b32 v238 offset:7680\n" \
"v_mfma_f32_16x16x32_bf16 v[28:31],a[188:191],v[200:203],v[28:31]\nds_write_addtid_b32 v239 offset:7936\nv_mfma_f32_16x16x32_bf16 v[28:31],a[220:223],v[204:207],v[28:31]\nv_mfma_f32_16x16x32_bf16 v[0:3],a[192:195],v[204:207],v[0:3]\n" \
"v_mfma_f32_16x16x32_bf16 v[4:7],a[196:199],v[204:207],v[4:7]\nv_mfma_f32_16x16x32_bf16 v[8:11],a[200:203],v[204:207],v[8:11]\nv_mfma_f32_16x16x32_bf16 v[12:15],a[204:207],v[204:207],v[12:15]\ns_waitcnt vmcnt(7)\n" \
"v_mfma_f32_16x16x32_bf16 v[208:211],v[128:131],v[72:75],0\ns_mov_b64 exec,1\nbuffer_store_dword v61,off,s[36:39],s54 sc1\ns_mov_b64 exec,-1\n" \
"v_mfma_f32_16x16x32_bf16 v[208:211],v[160:163],v[76:79],v[208:211]\nv_cmp_eq_u32 vcc,0,v63\nv_mfma_f32_16x16x32_bf16 v[212:215],v[132:135],v[72:75],0\ns_waitcnt lgkmcnt(0)\n" \
"s_barrier\nv_mfma_f32_16x16x32_bf16 v[212:215],v[164:167],v[76:79],v[212:215]\ns_and_b64 vcc,vcc,s[86:87]\ns_cbranch_scc1 .LsbS_%=\n" \
".LbbS_%=:\nv_mfma_f32_16x16x32_bf16 v[216:219],v[136:139],v[72:75],0\nglobal_load_dwordx4 v[108:111],v59,s[4:5] sc1\nds_write_addtid_b32 v0 offset:32768\n" \
"v_mfma_f32_16x16x32_bf16 v[216:219],v[168:171],v[76:79],v[216:219]\nds_write_addtid_b32 v1 offset:33024\nds_write_addtid_b32 v2 offset:33280\nv_mfma_f32_16x16x32_bf16 v[220:223],v[140:143],v[72:75],0\n" \
"global_load_dwordx4 v[112:115],v59,s[4:5] offset:1024 sc1\nds_write_addtid_b32 v3 offset:33536\nv_mfma_f32_16x16x32_bf16 v[220:223],v[172:175],v[76:79],v[220:223]\nds_write_addtid_b32 v4 offset:33792\n" \
"ds_write_addtid_b32 v5 offset:34048\nv_mfma_f32_16x16x32_bf16 v[224:227],v[144:147],v[72:75],0\nglobal_load_dwordx4 v[116:119],v59,s[4:5] offset:2048 sc1\nds_write_addtid_b32 v6 offset:34304\n" \
"v_mfma_f32_16x16x32_bf16 v[224:227],v[176:179],v[76:79],v[224:227]\nds_write_addtid_b32 v7 offset:34560\nds_write_addtid_b32 v8 offset:34816\nv_mfma_f32_16x16x32_bf16 v[228:231],v[148:151],v[72:75],0\n" \
"global_load_dwordx4 v[120:123],v59,s[6:7] sc1\nds_write_addtid_b32 v9 offset:35072\nv_mfma_f32_16x16x32_bf16 v[228:231],v[180:183],v[76:79],v[228:231]\nds_write_addtid_b32 v10 offset:35328\n" \
"ds_write_addtid_b32 v11 offset:35584\nv_mfma_f32_16x16x32_bf16 v[232:235],v[152:155],v[72:75],0\nglobal_load_dwordx4 v[124:127],v59,s[6:7] offset:1024 sc1\nds_write_addtid_b32 v12 offset:35840\n" \
"v_mfma_f32_16x16x32_bf16 v[232:235],v[184:187],v[76:79],v[232:235]\nds_write_addtid_b32 v13 offset:36096\nds_write_addtid_b32 v14 offset:36352\nv_mfma_f32_16x16x32_bf16 v[236:239],v[156:159],v[72:75],0\n" \
"global_load_dwordx4 v[192:195],v59,s[6:7] offset:2048 sc1\nds_write_addtid_b32 v15 offset:36608\nv_mfma_f32_16x16x32_bf16 v[236:239],v[188:191],v[76:79],v[236:239]\nds_write_addtid_b32 v16 offset:36864\n" \
"s_waitcnt vmcnt(12)\nv_mfma_f32_16x16x32_bf16 v[208:211],a[0:3],v[80:83],v[208:211]\nglobal_load_dwordx4 v[196:199],v59,s[8:9] sc1\nds_write_addtid_b32 v17 offset:37120\n" \
"v_mfma_f32_16x16x32_bf16 v[208:211],a[32:35],v[84:87],v[208:211]\nds_write_addtid_b32 v18 offset:37376\nds_write_addtid_b32 v19 offset:37632\nv_mfma_f32_16x16x32_bf16 v[212:215],a[4:7],v[80:83],v[212:215]\n" \
"global_load_dwordx4 v[200:203],v59,s[8:9] offset:1024 sc1\nds_write_addtid_b32 v20 offset:37888\nv_mfma_f32_16x16x32_bf16 v[212:215],a[36:39],v[84:87],v[212:215]\nds_write_addtid_b32 v21 offset:38144\n" \
"ds_write_addtid_b32 v22 offset:38400\nv_mfma_f32_16x16x32_bf16 v[216:219],a[8:11],v[80:83],v[216:219]\nglobal_load_dwordx4 v[204:207],v59,s[8:9] offset:2048 sc1\nds_write_addtid_b32 v23 offset:38656\n" \
"v_mfma_f32_16x16x32_bf16 v[216:219],a[40:43],v[84:87],v[216:219]\nds_write_addtid_b32 v24 offset:38912\nds_write_addtid_b32 v25 offset:39168\nv_mfma_f32_16x16x32_bf16 v[220:223],a[12:15],v[80:83],v[220:223]\n" \
"ds_write_addtid_b32 v26 offset:39424\nds_write_addtid_b32 v27 offset:39680\nv_mfma_f32_16x16x32_bf16 v[220:223],a[44:47],v[84:87],v[220:223]\nds_write_addtid_b32 v28 offset:39936\n" \
"ds_write_addtid_b32 v29 offset:40192\nv_mfma_f32_16x16x32_bf16 v[224:227],a[16:19],v[80:83],v[224:227]\nds_write_addtid_b32 v30 offset:40448\nds_write_addtid_b32 v31 offset:40704\n" \
"v_mfma_f32_16x16x32_bf16 v[224:227],a[48:51],v[84:87],v[224:227]\nds_read_b128 v[32:35],v58\nds_read_b128 v[36:39],v58 offset:8192\nv_mfma_f32_16x16x32_bf16 v[228:231],a[20:23],v[80:83],v[228:231]\n" \
"ds_read_b128 v[40:43],v58 offset:16384\nds_read_b128 v[44:47],v58 offset:24576\nv_mfma_f32_16x16x32_bf16 v[228:231],a[52:55],v[84:87],v[228:231]\nds_read_b128 v[48:51],v58 offset:4096\n" \
"ds_read_b128 v[240:243],v58 offset:12288\nv_mfma_f32_16x16x32_bf16 v[232:235],a[24:27],v[80:83],v[232:235]\nds_read_b128 v[244:247],v58 offset:20480\nds_read_b128 v[248:251],v58 offset:28672\n" \
"v_mfma_f32_16x16x32_bf16 v[232:235],a[56:59],v[84:87],v[232:235]\nv_mfma_f32_16x16x32_bf16 v[236:239],a[28:31],v[80:83],v[236:239]\ns_waitcnt lgkmcnt(0)\ns_barrier\n" \
"v_mfma_f32_16x16x32_bf16 v[236:239],a[60:63],v[84:87],v[236:239]\ns_waitcnt vmcnt(13)\nv_mfma_f32_16x16x32_bf16 v[208:211],a[64:67],v[88:91],v[208:211]\nv_mfma_f32_16x16x32_bf16 v[208:211],a[96:99],v[92:95],v[208:211]\n" \
"v_mfma_f32_16x16x32_bf16 v[212:215],a[68:71],v[88:91],v[212:215]\nv_add_f32 v32,v32,v36\nv_mfma_f32_16x16x32_bf16 v[212:215],a[100:103],v[92:95],v[212:215]\nv_add_f32 v33,v33,v37\n" \
"v_add_f32 v34,v34,v38\nv_mfma_f32_16x16x32_bf16 v[216:219],a[72:75],v[88:91],v[216:219]\nv_add_f32 v35,v35,v39\nv_add_f32 v40,v40,v44\n" \
"v_mfma_f32_16x16x32_bf16 v[216:219],a[104:107],v[92:95],v[216:219]\nv_add_f32 v41,v41,v45\nv_add_f32 v42,v42,v46\nv_mfma_f32_16x16x32_bf16 v[220:223],a[76:79],v[88:91],v[220:223]\n" \
"v_add_f32 v43,v43,v47\nv_add_f32 v32,v32,v40\nv_mfma_f32_16x16x32_bf16 v[220:223],a[108:111],v[92:95],v[220:223]\nv_add_f32 v33,v33,v41\n" \
"v_add_f32 v34,v34,v42\nv_mfma_f32_16x16x32_bf16 v[224:227],a[80:83],v[88:91],v[224:227]\nv_add_f32 v35,v35,v43\nv_cvt_pk_bf16_f32 v36,v32,v33\n" \
"v_mfma_f32_16x16x32_bf16 v[224:227],a[112:115],v[92:95],v[224:227]\nv_cvt_pk_bf16_f32 v37,v34,v35\nv_mfma_f32_16x16x32_bf16 v[228:231],a[84:87],v[88:91],v[228:231]\nv_add_f32 v48,v48,v240\n" \
"v_add_f32 v49,v49,v241\nv_mfma_f32_16x16x32_bf16 v[228:231],a[116:119],v[92:95],v[228:231]\nv_add_f32 v50,v50,v242\nv_add_f32 v51,v51,v243\n" \
"v_mfma_f32_16x16x32_bf16 v[232:235],a[88:91],v[88:91],v[232:235]\nv_add_f32 v244,v244,v248\nv_add_f32 v245,v245,v249\nv_mfma_f32_16x16x32_bf16 v[232:235],a[120:123],v[92:95],v[232:235]\n" \
"v_add_f32 v246,v246,v250\nv_add_f32 v247,v247,v251\nv_mfma_f32_16x16x32_bf16 v[236:239],a[92:95],v[88:91],v[236:239]\nv_add_f32 v48,v48,v244\n" \
"v_add_f32 v49,v49,v245\nv_mfma_f32_16x16x32_bf16 v[236:239],a[124:127],v[92:95],v[236:239]\nv_add_f32 v50,v50,v246\ns_waitcnt vmcnt(11)\n" \
"v_mfma_f32_16x16x32_bf16 v[208:211],a[128:131],v[96:99],v[208:211]\nv_add_f32 v51,v51,v247\nv_cvt_pk_bf16_f32 v38,v48,v49\nv_mfma_f32_16x16x32_bf16 v[208:211],a[160:163],v[100:103],v[208:211]\n" \
"v_cvt_pk_bf16_f32 v39,v50,v51\nbuffer_store_dwordx4 v[36:39],v59,s[36:39],s74 offen sc1\nds_read_b128 v[32:35],v58 offset:32768\nv_mfma_f32_16x16x32_bf16 v[212:215],a[132:135],v[96:99],v[212:215]\n" \
"ds_read_b128 v[36:39],v58 offset:40960\nds_read_b128 v[40:43],v58 offset:49152\nv_mfma_f32_16x16x32_bf16 v[212:215],a[164:167],v[100:103],v[212:215]\nds_read_b128 v[44:47],v58 offset:57344\n" \
"ds_read_b128 v[48:51],v58 offset:36864\nv_mfma_f32_16x16x32_bf16 v[216:219],a[136:139],v[96:99],v[216:219]\nds_read_b128 v[240:243],v58 offset:45056\nds_read_b128 v[244:247],v58 offset:53248\n" \
"v_mfma_f32_16x16x32_bf16 v[216:219],a[168:171],v[100:103],v[216:219]\nds_read_b128 v[248:251],v58 offset:61440\nv_mfma_f32_16x16x32_bf16 v[220:223],a[140:143],v[96:99],v[220:223]\nv_mfma_f32_16x16x32_bf16 v[220:223],a[172:175],v[100:103],v[220:223]\n" \
"v_mfma_f32_16x16x32_bf16 v[224:227],a[144:147],v[96:99],v[224:227]\nv_mfma_f32_16x16x32_bf16 v[224:227],a[176:179],v[100:103],v[224:227]\nv_mfma_f32_16x16x32_bf16 v[228:231],a[148:151],v[96:99],v[228:231]\nv_mfma_f32_16x16x32_bf16 v[228:231],a[180:183],v[100:103],v[228:231]\n" \
"s_waitcnt lgkmcnt(4)\nv_add_f32 v32,v32,v36\nv_add_f32 v33,v33,v37\nv_mfma_f32_16x16x32_bf16 v[232:235],a[152:155],v[96:99],v[232:235]\n" \
"v_add_f32 v34,v34,v38\nv_add_f32 v35,v35,v39\nv_add_f32 v40,v40,v44\nv_mfma_f32_16x16x32_bf16 v[232:235],a[184:187],v[100:103],v[232:235]\n" \
"v_add_f32 v41,v41,v45\nv_add_f32 v42,v42,v46\nv_add_f32 v43,v43,v47\nv_mfma_f32_16x16x32_bf16 v[236:239],a[156:159],v[96:99],v[236:239]\n" \
"v_add_f32 v32,v32,v40\nv_add_f32 v33,v33,v41\nv_add_f32 v34,v34,v42\nv_mfma_f32_16x16x32_bf16 v[236:239],a[188:191],v[100:103],v[236:239]\n" \
"v_add_f32 v35,v35,v43\nv_cvt_pk_bf16_f32 v36,v32,v33\ns_waitcnt vmcnt(11)\nv_mfma_f32_16x16x32_bf16 v[208:211],a[192:195],v[104:107],v[208:211]\n" \
"v_cvt_pk_bf16_f32 v37,v34,v35\ns_waitcnt lgkmcnt(0)\nv_add_f32 v48,v48,v240\nv_mfma_f32_16x16x32_bf16 v[212:215],a[196:199],v[104:107],v[212:215]\n" \
"v_add_f32 v49,v49,v241\nv_add_f32 v50,v50,v242\nv_add_f32 v51,v51,v243\nv_mfma_f32_16x16x32_bf16 v[216:219],a[200:203],v[104:107],v[216:219]\n" \
"v_add_f32 v244,v244,v248\nv_add_f32 v245,v245,v249\nv_mfma_f32_16x16x32_bf16 v[220:223],a[204:207],v[104:107],v[220:223]\nv_add_f32 v246,v246,v250\n" \
"v_add_f32 v247,v247,v251\nv_mfma_f32_16x16x32_bf16 v[224:227],a[208:211],v[104:107],v[224:227]\nv_add_f32 v48,v48,v244\nv_add_f32 v49,v49,v245\n" \
"v_mfma_f32_16x16x32_bf16 v[228:231],a[212:215],v[104:107],v[228:231]\nv_add_f32 v50,v50,v246\nv_add_f32 v51,v51,v247\nv_mfma_f32_16x16x32_bf16 v[232:235],a[216:219],v[104:107],v[232:235]\n" \
"v_cvt_pk_bf16_f32 v38,v48,v49\nv_cvt_pk_bf16_f32 v39,v50,v51\nv_mfma_f32_16x16x32_bf16 v[236:239],a[220:223],v[104:107],v[236:239]\nv_add_u32 v59,409600,v59\n" \
"buffer_store_dwordx4 v[36:39],v59,s[36:39],s74 offen sc1\n.Le_%=:\ns_waitcnt vmcnt(9)\nv_mfma_f32_16x16x32_bf16 v[0:3],v[128:131],v[108:111],0\n" \
"v_add_u32 v59,409600,v59\nv_mfma_f32_16x16x32_bf16 v[0:3],v[160:163],v[112:115],v[0:3]\nds_write_addtid_b32 v208\nv_mfma_f32_16x16x32_bf16 v[4:7],v[132:135],v[108:111],0\n" \
"global_load_dwordx4 v[72:75],v59,s[12:13] sc1\nds_write_addtid_b32 v209 offset:256\nv_mfma_f32_16x16x32_bf16 v[4:7],v[164:167],v[112:115],v[4:7]\nds_write_addtid_b32 v210 offset:512\n" \
"v_mfma_f32_16x16x32_bf16 v[8:11],v[136:139],v[108:111],0\nds_write_addtid_b32 v211 offset:768\nv_mfma_f32_16x16x32_bf16 v[8:11],v[168:171],v[112:115],v[8:11]\nds_write_addtid_b32 v212 offset:1024\n" \
"v_mfma_f32_16x16x32_bf16 v[12:15],v[140:143],v[108:111],0\nds_write_addtid_b32 v213 offset:1280\nv_mfma_f32_16x16x32_bf16 v[12:15],v[172:175],v[112:115],v[12:15]\nds_write_addtid_b32 v214 offset:1536\n" \
"v_mfma_f32_16x16x32_bf16 v[16:19],v[144:147],v[108:111],0\nglobal_load_dwordx4 v[76:79],v59,s[12:13] offset:1024 sc1\nds_write_addtid_b32 v215 offset:1792\nv_mfma_f32_16x16x32_bf16 v[16:19],v[176:179],v[112:115],v[16:19]\n" \
"ds_write_addtid_b32 v216 offset:2048\nv_mfma_f32_16x16x32_bf16 v[20:23],v[148:151],v[108:111],0\nds_write_addtid_b32 v217 offset:2304\nv_mfma_f32_16x16x32_bf16 v[20:23],v[180:183],v[112:115],v[20:23]\n" \
"ds_write_addtid_b32 v218 offset:2560\nv_mfma_f32_16x16x32_bf16 v[24:27],v[152:155],v[108:111],0\nds_write_addtid_b32 v219 offset:2816\nv_mfma_f32_16x16x32_bf16 v[24:27],v[184:187],v[112:115],v[24:27]\n" \
"ds_write_addtid_b32 v220 offset:3072\nv_mfma_f32_16x16x32_bf16 v[28:31],v[156:159],v[108:111],0\nglobal_load_dwordx4 v[80:83],v59,s[12:13] offset:2048 sc1\nds_write_addtid_b32 v221 offset:3328\n" \
"v_mfma_f32_16x16x32_bf16 v[28:31],v[188:191],v[112:115],v[28:31]\nds_write_addtid_b32 v222 offset:3584\ns_waitcnt vmcnt(10)\nv_mfma_f32_16x16x32_bf16 v[0:3],a[0:3],v[116:119],v[0:3]\n" \
"ds_write_addtid_b32 v223 offset:3840\nv_bfe_u32 v69,v59,2,8\nv_mfma_f32_16x16x32_bf16 v[0:3],a[32:35],v[120:123],v[0:3]\nds_write_addtid_b32 v224 offset:4096\n" \
"buffer_load_dword v64,v69,s[36:39],s79 offen sc1\nv_mfma_f32_16x16x32_bf16 v[4:7],a[4:7],v[116:119],v[4:7]\nds_write_addtid_b32 v225 offset:4352\nbuffer_load_dword v65,v69,s[36:39],s80 offen sc1\n" \
"v_mfma_f32_16x16x32_bf16 v[4:7],a[36:39],v[120:123],v[4:7]\nds_write_addtid_b32 v226 offset:4608\nbuffer_load_dword v66,v69,s[36:39],s81 offen sc1\nv_mfma_f32_16x16x32_bf16 v[8:11],a[8:11],v[116:119],v[8:11]\n" \
"global_load_dwordx4 v[84:87],v59,s[14:15] sc1\nds_write_addtid_b32 v227 offset:4864\nv_mfma_f32_16x16x32_bf16 v[8:11],a[40:43],v[120:123],v[8:11]\nds_write_addtid_b32 v228 offset:5120\n" \
"buffer_load_dword v67,v69,s[36:39],s82 offen sc1\nv_mfma_f32_16x16x32_bf16 v[12:15],a[12:15],v[116:119],v[12:15]\nds_write_addtid_b32 v229 offset:5376\nbuffer_load_dword v68,v57,s[36:39],s90 offen sc1\n" \
"v_mfma_f32_16x16x32_bf16 v[12:15],a[44:47],v[120:123],v[12:15]\nds_write_addtid_b32 v230 offset:5632\nv_add_u32 v70,s98,v58\nv_mfma_f32_16x16x32_bf16 v[16:19],a[16:19],v[116:119],v[16:19]\n" \
"s_add_u32 s59,s59,0x10000\nbuffer_load_dword v63,v61,s[36:39],s59 offen sc1\nv_mfma_f32_16x16x32_bf16 v[16:19],a[48:51],v[120:123],v[16:19]\nds_write_addtid_b32 v231 offset:5888\n" \
"v_mfma_f32_16x16x32_bf16 v[20:23],a[20:23],v[116:119],v[20:23]\nglobal_load_dwordx4 v[88:91],v59,s[14:15] offset:1024 sc1\nds_write_addtid_b32 v232 offset:6144\nv_mfma_f32_16x16x32_bf16 v[20:23],a[52:55],v[120:123],v[20:23]\n" \
"ds_write_addtid_b32 v233 offset:6400\nv_mfma_f32_16x16x32_bf16 v[24:27],a[24:27],v[116:119],v[24:27]\nds_write_addtid_b32 v234 offset:6656\nv_mfma_f32_16x16x32_bf16 v[24:27],a[56:59],v[120:123],v[24:27]\n" \
"ds_write_addtid_b32 v235 offset:6912\nv_mfma_f32_16x16x32_bf16 v[28:31],a[28:31],v[116:119],v[28:31]\nds_write_addtid_b32 v236 offset:7168\nv_mfma_f32_16x16x32_bf16 v[28:31],a[60:63],v[120:123],v[28:31]\n" \
"ds_write_addtid_b32 v237 offset:7424\ns_waitcnt vmcnt(16)\nv_mfma_f32_16x16x32_bf16 v[0:3],a[64:67],v[124:127],v[0:3]\nglobal_load_dwordx4 v[92:95],v59,s[14:15] offset:2048 sc1\n" \
"ds_write_addtid_b32 v238 offset:7680\nv_mfma_f32_16x16x32_bf16 v[0:3],a[96:99],v[192:195],v[0:3]\nds_write_addtid_b32 v239 offset:7936\nv_mfma_f32_16x16x32_bf16 v[4:7],a[68:71],v[124:127],v[4:7]\n" \
"ds_read_b64 v[70:71],v70 offset:29704\nv_mfma_f32_16x16x32_bf16 v[4:7],a[100:103],v[192:195],v[4:7]\nv_mfma_f32_16x16x32_bf16 v[8:11],a[72:75],v[124:127],v[8:11]\nv_mfma_f32_16x16x32_bf16 v[8:11],a[104:107],v[192:195],v[8:11]\n" \
"v_mfma_f32_16x16x32_bf16 v[12:15],a[76:79],v[124:127],v[12:15]\nglobal_load_dwordx4 v[96:99],v59,s[16:17] sc1\nv_mfma_f32_16x16x32_bf16 v[12:15],a[108:111],v[192:195],v[12:15]\ns_waitcnt lgkmcnt(0)\n" \
"s_barrier\nv_mfma_f32_16x16x32_bf16 v[16:19],a[80:83],v[124:127],v[16:19]\nds_read_b128 v[32:35],v58\nv_mfma_f32_16x16x32_bf16 v[16:19],a[112:115],v[192:195],v[16:19]\n" \
"ds_read_b128 v[36:39],v58 offset:8192\nv_mfma_f32_16x16x32_bf16 v[20:23],a[84:87],v[124:127],v[20:23]\nds_read_b128 v[40:43],v58 offset:16384\nv_mfma_f32_16x16x32_bf16 v[20:23],a[116:119],v[192:195],v[20:23]\n" \
"ds_read_b128 v[44:47],v58 offset:24576\nv_mfma_f32_16x16x32_bf16 v[24:27],a[88:91],v[124:127],v[24:27]\nglobal_load_dwordx4 v[100:103],v59,s[16:17] offset:1024 sc1\nds_read_b128 v[48:51],v58 offset:4096\n" \
"v_mfma_f32_16x16x32_bf16 v[24:27],a[120:123],v[192:195],v[24:27]\nds_read_b128 v[240:243],v58 offset:12288\nv_mfma_f32_16x16x32_bf16 v[28:31],a[92:95],v[124:127],v[28:31]\nds_read_b128 v[244:247],v58 offset:20480\n" \
"v_mfma_f32_16x16x32_bf16 v[28:31],a[124:127],v[192:195],v[28:31]\nds_read_b128 v[248:251],v58 offset:28672\ns_waitcnt vmcnt(17)\nv_mfma_f32_16x16x32_bf16 v[0:3],a[128:131],v[196:199],v[0:3]\n" \
"v_mfma_f32_16x16x32_bf16 v[0:3],a[160:163],v[200:203],v[0:3]\nv_mfma_f32_16x16x32_bf16 v[4:7],a[132:135],v[196:199],v[4:7]\nglobal_load_dwordx4 v[104:107],v59,s[16:17] offset:2048 sc1\nv_mfma_f32_16x16x32_bf16 v[4:7],a[164:167],v[200:203],v[4:7]\n" \
"s_waitcnt lgkmcnt(4)\nv_add_f32 v32,v32,v36\nv_mfma_f32_16x16x32_bf16 v[8:11],a[136:139],v[196:199],v[8:11]\nv_add_f32 v33,v33,v37\n" \
"v_add_f32 v34,v34,v38\nv_mfma_f32_16x16x32_bf16 v[8:11],a[168:171],v[200:203],v[8:11]\nv_add_f32 v35,v35,v39\nv_add_f32 v40,v40,v44\n" \
"v_mfma_f32_16x16x32_bf16 v[12:15],a[140:143],v[196:199],v[12:15]\nv_add_f32 v41,v41,v45\nv_add_f32 v42,v42,v46\nv_mfma_f32_16x16x32_bf16 v[12:15],a[172:175],v[200:203],v[12:15]\n" \
"v_add_f32 v43,v43,v47\nv_add_f32 v32,v32,v40\nv_mfma_f32_16x16x32_bf16 v[16:19],a[144:147],v[196:199],v[16:19]\nv_add_f32 v33,v33,v41\n" \
"v_add_f32 v34,v34,v42\nv_mfma_f32_16x16x32_bf16 v[16:19],a[176:179],v[200:203],v[16:19]\nv_add_f32 v35,v35,v43\nv_cvt_pk_bf16_f32 v36,v32,v33\n" \
"v_mfma_f32_16x16x32_bf16 v[20:23],a[148:151],v[196:199],v[20:23]\nv_cvt_pk_bf16_f32 v37,v34,v35\ns_waitcnt lgkmcnt(0)\nv_mfma_f32_16x16x32_bf16 v[20:23],a[180:183],v[200:203],v[20:23]\n" \
"v_add_f32 v48,v48,v240\nv_add_f32 v49,v49,v241\nv_mfma_f32_16x16x32_bf16 v[24:27],a[152:155],v[196:199],v[24:27]\nv_add_f32 v50,v50,v242\n" \
"v_add_f32 v51,v51,v243\nv_mfma_f32_16x16x32_bf16 v[24:27],a[184:187],v[200:203],v[24:27]\nv_add_f32 v244,v244,v248\nv_add_f32 v245,v245,v249\n" \
"v_mfma_f32_16x16x32_bf16 v[28:31],a[156:159],v[196:199],v[28:31]\nv_add_f32 v246,v246,v250\nv_add_f32 v247,v247,v251\nv_mfma_f32_16x16x32_bf16 v[28:31],a[188:191],v[200:203],v[28:31]\n" \
"v_add_f32 v48,v48,v244\ns_waitcnt vmcnt(17)\nv_mfma_f32_16x16x32_bf16 v[0:3],a[192:195],v[204:207],v[0:3]\nv_add_f32 v49,v49,v245\n" \
"v_add_f32 v50,v50,v246\nv_mfma_f32_16x16x32_bf16 v[4:7],a[196:199],v[204:207],v[4:7]\nv_add_f32 v51,v51,v247\nv_cvt_pk_bf16_f32 v38,v48,v49\n" \
"v_mfma_f32_16x16x32_bf16 v[8:11],a[200:203],v[204:207],v[8:11]\nv_cvt_pk_bf16_f32 v39,v50,v51\nbuffer_store_dwordx4 v[36:39],v59,s[36:39],s74 offen sc1\nv_mfma_f32_16x16x32_bf16 v[12:15],a[204:207],v[204:207],v[12:15]\n" \
"v_mfma_f32_16x16x32_bf16 v[16:19],a[208:211],v[204:207],v[16:19]\nv_mfma_f32_16x16x32_bf16 v[20:23],a[212:215],v[204:207],v[20:23]\nv_mfma_f32_16x16x32_bf16 v[24:27],a[216:219],v[204:207],v[24:27]\nv_mfma_f32_16x16x32_bf16 v[28:31],a[220:223],v[204:207],v[28:31]\n" \
"s_waitcnt vmcnt(14)\nv_mfma_f32_16x16x32_bf16 v[208:211],v[128:131],v[72:75],0\nv_add_u32 v59,409600,v59\nv_mfma_f32_16x16x32_bf16 v[208:211],v[160:163],v[76:79],v[208:211]\n" \
"s_waitcnt vmcnt(6)\nv_cmp_eq_u32 vcc,0,v63\nv_mfma_f32_16x16x32_bf16 v[212:215],v[132:135],v[72:75],0\nds_write_addtid_b32 v0 offset:32768\n" \
"v_mfma_f32_16x16x32_bf16 v[212:215],v[164:167],v[76:79],v[212:215]\ns_and_b64 vcc,vcc,s[86:87]\ns_cbranch_scc1 .Lsg4_%=\n.Lbg4_%=:\n" \
"v_mfma_f32_16x16x32_bf16 v[216:219],v[136:139],v[72:75],0\nglobal_load_dwordx4 v[108:111],v59,s[12:13] sc1\nds_write_addtid_b32 v1 offset:33024\nv_mfma_f32_16x16x32_bf16 v[216:219],v[168:171],v[76:79],v[216:219]\n" \
"ds_write_addtid_b32 v2 offset:33280\nv_mfma_f32_16x16x32_bf16 v[220:223],v[140:143],v[72:75],0\nds_write_addtid_b32 v3 offset:33536\nv_max3_u32 v52,v64,v65,v66\n" \
"v_mfma_f32_16x16x32_bf16 v[220:223],v[172:175],v[76:79],v[220:223]\nds_write_addtid_b32 v4 offset:33792\nv_max_u32 v52,v52,v67\nv_mfma_f32_16x16x32_bf16 v[224:227],v[144:147],v[72:75],0\n" \
"global_load_dwordx4 v[112:115],v59,s[12:13] offset:1024 sc1\nds_write_addtid_b32 v5 offset:34048\nv_mfma_f32_16x16x32_bf16 v[224:227],v[176:179],v[76:79],v[224:227]\nds_write_addtid_b32 v6 offset:34304\n" \
"v_cmp_eq_u32 vcc,-1,v52\nv_mfma_f32_16x16x32_bf16 v[228:231],v[148:151],v[72:75],0\nds_write_addtid_b32 v7 offset:34560\nv_mfma_f32_16x16x32_bf16 v[228:231],v[180:183],v[76:79],v[228:231]\n" \
"ds_write_addtid_b32 v8 offset:34816\ns_cbranch_vccnz .Lse_%=\n.Lbe_%=:\nv_mfma_f32_16x16x32_bf16 v[232:235],v[152:155],v[72:75],0\n" \
"ds_write_addtid_b32 v9 offset:35072\nv_lshlrev_b32 v52,16,v64\nv_mfma_f32_16x16x32_bf16 v[232:235],v[184:187],v[76:79],v[232:235]\nds_write_addtid_b32 v10 offset:35328\n" \
"v_and_b32 v53,0xffff0000,v64\nv_mfma_f32_16x16x32_bf16 v[236:239],v[156:159],v[72:75],0\nglobal_load_dwordx4 v[116:119],v59,s[12:13] offset:2048 sc1\nds_write_addtid_b32 v11 offset:35584\n" \
"v_mfma_f32_16x16x32_bf16 v[236:239],v[188:191],v[76:79],v[236:239]\nds_write_addtid_b32 v12 offset:35840\nv_mfma_f32_16x16x32_bf16 v[208:211],a[0:3],v[80:83],v[208:211]\nds_write_addtid_b32 v13 offset:36096\n" \
"v_lshlrev_b32 v54,16,v65\nv_mfma_f32_16x16x32_bf16 v[208:211],a[32:35],v[84:87],v[208:211]\nds_write_addtid_b32 v14 offset:36352\nv_and_b32 v55,0xffff0000,v65\n" \
"v_mfma_f32_16x16x32_bf16 v[212:215],a[4:7],v[80:83],v[212:215]\nds_write_addtid_b32 v15 offset:36608\nv_lshlrev_b32 v252,16,v66\nv_mfma_f32_16x16x32_bf16 v[212:215],a[36:39],v[84:87],v[212:215]\n" \
"ds_write_addtid_b32 v16 offset:36864\nv_and_b32 v253,0xffff0000,v66\nv_mfma_f32_16x16x32_bf16 v[216:219],a[8:11],v[80:83],v[216:219]\nglobal_load_dwordx4 v[120:123],v59,s[14:15] sc1\n" \
"ds_write_addtid_b32 v17 offset:37120\nv_mfma_f32_16x16x32_bf16 v[216:219],a[40:43],v[84:87],v[216:219]\nds_write_addtid_b32 v18 offset:37376\nv_lshlrev_b32 v254,16,v67\n" \
"v_mfma_f32_16x16x32_bf16 v[220:223],a[12:15],v[80:83],v[220:223]\nds_write_addtid_b32 v19 offset:37632\nv_and_b32 v255,0xffff0000,v67\nv_mfma_f32_16x16x32_bf16 v[220:223],a[44:47],v[84:87],v[220:223]\n" \
"ds_write_addtid_b32 v20 offset:37888\nv_add_f32 v52,v52,v54\nv_mfma_f32_16x16x32_bf16 v[224:227],a[16:19],v[80:83],v[224:227]\nds_write_addtid_b32 v21 offset:38144\n" \
"v_add_f32 v53,v53,v55\nv_mfma_f32_16x16x32_bf16 v[224:227],a[48:51],v[84:87],v[224:227]\nds_write_addtid_b32 v22 offset:38400\nv_add_f32 v252,v252,v254\n" \
"v_mfma_f32_16x16x32_bf16 v[228:231],a[20:23],v[80:83],v[228:231]\nglobal_load_dwordx4 v[124:127],v59,s[14:15] offset:1024 sc1\nds_write_addtid_b32 v23 offset:38656\nv_mfma_f32_16x16x32_bf16 v[228:231],a[52:55],v[84:87],v[228:231]\n" \
"ds_write_addtid_b32 v24 offset:38912\nv_add_f32 v253,v253,v255\nv_mfma_f32_16x16x32_bf16 v[232:235],a[24:27],v[80:83],v[232:235]\nds_write_addtid_b32 v25 offset:39168\n" \
"s_waitcnt lgkmcnt(15)\nv_mfma_f32_16x16x32_bf16 v[232:235],a[56:59],v[84:87],v[232:235]\nds_write_addtid_b32 v26 offset:39424\nv_add_f32 v52,v52,v70\n" \
"v_mfma_f32_16x16x32_bf16 v[236:239],a[28:31],v[80:83],v[236:239]\nds_write_addtid_b32 v27 offset:39680\nv_add_f32 v53,v53,v70\nv_mfma_f32_16x16x32_bf16 v[236:239],a[60:63],v[84:87],v[236:239]\n" \
"ds_write_addtid_b32 v28 offset:39936\ns_waitcnt vmcnt(9)\nv_mfma_f32_16x16x32_bf16 v[208:211],a[64:67],v[88:91],v[208:211]\nglobal_load_dwordx4 v[192:195],v59,s[14:15] offset:2048 sc1\n" \
"ds_write_addtid_b32 v29 offset:40192\nv_mfma_f32_16x16x32_bf16 v[208:211],a[96:99],v[92:95],v[208:211]\nds_write_addtid_b32 v30 offset:40448\nv_lshlrev_b32 v54,16,v71\n" \
"v_mfma_f32_16x16x32_bf16 v[212:215],a[68:71],v[88:91],v[212:215]\nds_write_addtid_b32 v31 offset:40704\nv_and_b32 v55,0xffff0000,v71\nv_mfma_f32_16x16x32_bf16 v[212:215],a[100:103],v[92:95],v[212:215]\n" \
"v_lshlrev_b32 v254,16,v68\nv_and_b32 v255,0xffff0000,v68\nv_mfma_f32_16x16x32_bf16 v[216:219],a[72:75],v[88:91],v[216:219]\nv_add_f32 v52,v52,v252\n" \
"v_add_f32 v53,v53,v253\nv_mfma_f32_16x16x32_bf16 v[216:219],a[104:107],v[92:95],v[216:219]\nv_fma_f32 v52,v54,v52,v254\nv_fma_f32 v53,v55,v53,v255\n" \
"v_mfma_f32_16x16x32_bf16 v[220:223],a[76:79],v[88:91],v[220:223]\nglobal_load_dwordx4 v[196:199],v59,s[16:17] sc1\nv_cvt_pk_bf16_f32 v52,v52,v53\nv_mfma_f32_16x16x32_bf16 v[220:223],a[108:111],v[92:95],v[220:223]\n" \
"buffer_store_dword v52,v57,s[44:47],0 offen sc1\nv_mfma_f32_16x16x32_bf16 v[224:227],a[80:83],v[88:91],v[224:227]\ns_waitcnt lgkmcnt(0)\ns_barrier\n" \
"v_mfma_f32_16x16x32_bf16 v[224:227],a[112:115],v[92:95],v[224:227]\nds_read_b128 v[32:35],v58 offset:32768\nv_mfma_f32_16x16x32_bf16 v[228:231],a[84:87],v[88:91],v[228:231]\nds_read_b128 v[36:39],v58 offset:40960\n" \
"v_mfma_f32_16x16x32_bf16 v[228:231],a[116:119],v[92:95],v[228:231]\nds_read_b128 v[40:43],v58 offset:49152\nv_mfma_f32_16x16x32_bf16 v[232:235],a[88:91],v[88:91],v[232:235]\nglobal_load_dwordx4 v[200:203],v59,s[16:17] offset:1024 sc1\n" \
"ds_read_b128 v[44:47],v58 offset:57344\nv_mfma_f32_16x16x32_bf16 v[232:235],a[120:123],v[92:95],v[232:235]\nds_read_b128 v[48:51],v58 offset:36864\nv_mfma_f32_16x16x32_bf16 v[236:239],a[92:95],v[88:91],v[236:239]\n" \
"ds_read_b128 v[240:243],v58 offset:45056\nv_mfma_f32_16x16x32_bf16 v[236:239],a[124:127],v[92:95],v[236:239]\nds_read_b128 v[244:247],v58 offset:53248\ns_waitcnt vmcnt(11)\n" \
"v_mfma_f32_16x16x32_bf16 v[208:211],a[128:131],v[96:99],v[208:211]\nds_read_b128 v[248:251],v58 offset:61440\nv_mfma_f32_16x16x32_bf16 v[208:211],a[160:163],v[100:103],v[208:211]\nv_mfma_f32_16x16x32_bf16 v[212:215],a[132:135],v[96:99],v[212:215]\n" \
"global_load_dwordx4 v[204:207],v59,s[16:17] offset:2048 sc1\nv_mfma_f32_16x16x32_bf16 v[212:215],a[164:167],v[100:103],v[212:215]\nv_mfma_f32_16x16x32_bf16 v[216:219],a[136:139],v[96:99],v[216:219]\ns_waitcnt lgkmcnt(4)\n" \
"v_add_f32 v32,v32,v36\nv_mfma_f32_16x16x32_bf16 v[216:219],a[168:171],v[100:103],v[216:219]\nv_add_f32 v33,v33,v37\nv_add_f32 v34,v34,v38\n" \
"v_mfma_f32_16x16x32_bf16 v[220:223],a[140:143],v[96:99],v[220:223]\nv_add_f32 v35,v35,v39\nv_add_f32 v40,v40,v44\nv_mfma_f32_16x16x32_bf16 v[220:223],a[172:175],v[100:103],v[220:223]\n" \
"v_add_f32 v41,v41,v45\nv_add_f32 v42,v42,v46\nv_mfma_f32_16x16x32_bf16 v[224:227],a[144:147],v[96:99],v[224:227]\nv_add_f32 v43,v43,v47\n" \
"v_add_f32 v32,v32,v40\nv_mfma_f32_16x16x32_bf16 v[224:227],a[176:179],v[100:103],v[224:227]\nv_add_f32 v33,v33,v41\nv_add_f32 v34,v34,v42\n" \
"v_mfma_f32_16x16x32_bf16 v[228:231],a[148:151],v[96:99],v[228:231]\nv_add_f32 v35,v35,v43\nv_cvt_pk_bf16_f32 v36,v32,v33\nv_mfma_f32_16x16x32_bf16 v[228:231],a[180:183],v[100:103],v[228:231]\n" \
"v_cvt_pk_bf16_f32 v37,v34,v35\ns_waitcnt lgkmcnt(0)\nv_mfma_f32_16x16x32_bf16 v[232:235],a[152:155],v[96:99],v[232:235]\nv_add_f32 v48,v48,v240\n" \
"v_add_f32 v49,v49,v241\nv_mfma_f32_16x16x32_bf16 v[232:235],a[184:187],v[100:103],v[232:235]\nv_add_f32 v50,v50,v242\nv_add_f32 v51,v51,v243\n" \
"v_mfma_f32_16x16x32_bf16 v[236:239],a[156:159],v[96:99],v[236:239]\nv_add_f32 v244,v244,v248\nv_add_f32 v245,v245,v249\nv_mfma_f32_16x16x32_bf16 v[236:239],a[188:191],v[100:103],v[236:239]\n" \
"v_add_f32 v246,v246,v250\ns_waitcnt vmcnt(11)\nv_mfma_f32_16x16x32_bf16 v[208:211],a[192:195],v[104:107],v[208:211]\nv_add_f32 v247,v247,v251\n" \
"v_add_f32 v48,v48,v244\nv_mfma_f32_16x16x32_bf16 v[212:215],a[196:199],v[104:107],v[212:215]\nv_add_f32 v49,v49,v245\nv_add_f32 v50,v50,v246\n" \
"v_mfma_f32_16x16x32_bf16 v[216:219],a[200:203],v[104:107],v[216:219]\nv_add_f32 v51,v51,v247\nv_cvt_pk_bf16_f32 v38,v48,v49\nv_mfma_f32_16x16x32_bf16 v[220:223],a[204:207],v[104:107],v[220:223]\n" \
"v_cvt_pk_bf16_f32 v39,v50,v51\nbuffer_store_dwordx4 v[36:39],v59,s[36:39],s74 offen sc1\nv_mfma_f32_16x16x32_bf16 v[224:227],a[208:211],v[104:107],v[224:227]\nv_mfma_f32_16x16x32_bf16 v[228:231],a[212:215],v[104:107],v[228:231]\n" \
"v_mfma_f32_16x16x32_bf16 v[232:235],a[216:219],v[104:107],v[232:235]\nv_mfma_f32_16x16x32_bf16 v[236:239],a[220:223],v[104:107],v[236:239]\ns_waitcnt vmcnt(9)\nv_mfma_f32_16x16x32_bf16 v[0:3],v[128:131],v[108:111],0\n" \
"v_add_u32 v59,409600,v59\nv_add_u32 v60,s98,v58\nv_mfma_f32_16x16x32_bf16 v[0:3],v[160:163],v[112:115],v[0:3]\nds_write_addtid_b32 v208\n" \
"v_mfma_f32_16x16x32_bf16 v[4:7],v[132:135],v[108:111],0\nglobal_load_dwordx4 v[72:75],v59,s[12:13] sc1\nds_write_addtid_b32 v209 offset:256\nv_mfma_f32_16x16x32_bf16 v[4:7],v[164:167],v[112:115],v[4:7]\n" \
"ds_write_addtid_b32 v210 offset:512\nv_mfma_f32_16x16x32_bf16 v[8:11],v[136:139],v[108:111],0\nds_write_addtid_b32 v211 offset:768\nv_mfma_f32_16x16x32_bf16 v[8:11],v[168:171],v[112:115],v[8:11]\n" \
"buffer_load_dwordx4 v[64:67],v59,s[36:39],s71 offen sc1\nds_write_addtid_b32 v212 offset:1024\nv_mfma_f32_16x16x32_bf16 v[12:15],v[140:143],v[108:111],0\nds_write_addtid_b32 v213 offset:1280\n" \
"v_mfma_f32_16x16x32_bf16 v[12:15],v[172:175],v[112:115],v[12:15]\nds_write_addtid_b32 v214 offset:1536\nv_mfma_f32_16x16x32_bf16 v[16:19],v[144:147],v[108:111],0\nglobal_load_dwordx4 v[76:79],v59,s[12:13] offset:1024 sc1\n" \
"ds_write_addtid_b32 v215 offset:1792\nv_mfma_f32_16x16x32_bf16 v[16:19],v[176:179],v[112:115],v[16:19]\nds_write_addtid_b32 v216 offset:2048\nv_mfma_f32_16x16x32_bf16 v[20:23],v[148:151],v[108:111],0\n" \
"ds_write_addtid_b32 v217 offset:2304\nv_mfma_f32_16x16x32_bf16 v[20:23],v[180:183],v[112:115],v[20:23]\nds_write_addtid_b32 v218 offset:2560\nv_mfma_f32_16x16x32_bf16 v[24:27],v[152:155],v[108:111],0\n" \
"ds_write_addtid_b32 v219 offset:2816\nv_mfma_f32_16x16x32_bf16 v[24:27],v[184:187],v[112:115],v[24:27]\nds_write_addtid_b32 v220 offset:3072\nv_mfma_f32_16x16x32_bf16 v[28:31],v[156:159],v[108:111],0\n" \
"global_load_dwordx4 v[80:83],v59,s[12:13] offset:2048 sc1\nds_write_addtid_b32 v221 offset:3328\nv_mfma_f32_16x16x32_bf16 v[28:31],v[188:191],v[112:115],v[28:31]\nds_write_addtid_b32 v222 offset:3584\n" \
"s_waitcnt vmcnt(11)\nv_mfma_f32_16x16x32_bf16 v[0:3],a[0:3],v[116:119],v[0:3]\nds_write_addtid_b32 v223 offset:3840\nv_mfma_f32_16x16x32_bf16 v[0:3],a[32:35],v[120:123],v[0:3]\n" \
"buffer_load_dwordx4 v[52:55],v59,s[36:39],s76 offen sc1\nds_write_addtid_b32 v224 offset:4096\nv_mfma_f32_16x16x32_bf16 v[4:7],a[4:7],v[116:119],v[4:7]\nds_write_addtid_b32 v225 offset:4352\n" \
"v_mfma_f32_16x16x32_bf16 v[4:7],a[36:39],v[120:123],v[4:7]\nds_write_addtid_b32 v226 offset:4608\nv_mfma_f32_16x16x32_bf16 v[8:11],a[8:11],v[116:119],v[8:11]\nglobal_load_dwordx4 v[84:87],v59,s[14:15] sc1\n" \
"ds_write_addtid_b32 v227 offset:4864\nv_mfma_f32_16x16x32_bf16 v[8:11],a[40:43],v[120:123],v[8:11]\nds_write_addtid_b32 v228 offset:5120\nv_mfma_f32_16x16x32_bf16 v[12:15],a[12:15],v[116:119],v[12:15]\n" \
"ds_write_addtid_b32 v229 offset:5376\nv_mfma_f32_16x16x32_bf16 v[12:15],a[44:47],v[120:123],v[12:15]\nds_write_addtid_b32 v230 offset:5632\nv_mfma_f32_16x16x32_bf16 v[16:19],a[16:19],v[116:119],v[16:19]\n" \
"ds_write_addtid_b32 v231 offset:5888\nv_mfma_f32_16x16x32_bf16 v[16:19],a[48:51],v[120:123],v[16:19]\nds_write_addtid_b32 v232 offset:6144\nv_mfma_f32_16x16x32_bf16 v[20:23],a[20:23],v[116:119],v[20:23]\n" \
"global_load_dwordx4 v[88:91],v59,s[14:15] offset:1024 sc1\nds_write_addtid_b32 v233 offset:6400\nv_mfma_f32_16x16x32_bf16 v[20:23],a[52:55],v[120:123],v[20:23]\nds_write_addtid_b32 v234 offset:6656\n" \
"v_mfma_f32_16x16x32_bf16 v[24:27],a[24:27],v[116:119],v[24:27]\nds_write_addtid_b32 v235 offset:6912\nv_mfma_f32_16x16x32_bf16 v[24:27],a[56:59],v[120:123],v[24:27]\nds_write_addtid_b32 v236 offset:7168\n" \
"v_mfma_f32_16x16x32_bf16 v[28:31],a[28:31],v[116:119],v[28:31]\nds_write_addtid_b32 v237 offset:7424\nv_mfma_f32_16x16x32_bf16 v[28:31],a[60:63],v[120:123],v[28:31]\nds_write_addtid_b32 v238 offset:7680\n" \
"s_waitcnt vmcnt(12)\nv_mfma_f32_16x16x32_bf16 v[0:3],a[64:67],v[124:127],v[0:3]\nglobal_load_dwordx4 v[92:95],v59,s[14:15] offset:2048 sc1\nds_write_addtid_b32 v239 offset:7936\n" \
"v_mfma_f32_16x16x32_bf16 v[0:3],a[96:99],v[192:195],v[0:3]\nds_read_b32 v61,v60 offset:28676\nv_mfma_f32_16x16x32_bf16 v[4:7],a[68:71],v[124:127],v[4:7]\nv_mfma_f32_16x16x32_bf16 v[4:7],a[100:103],v[192:195],v[4:7]\n" \
"v_mfma_f32_16x16x32_bf16 v[8:11],a[72:75],v[124:127],v[8:11]\nv_mfma_f32_16x16x32_bf16 v[8:11],a[104:107],v[192:195],v[8:11]\nv_mfma_f32_16x16x32_bf16 v[12:15],a[76:79],v[124:127],v[12:15]\nglobal_load_dwordx4 v[96:99],v59,s[16:17] sc1\n" \
"v_mfma_f32_16x16x32_bf16 v[12:15],a[108:111],v[192:195],v[12:15]\ns_waitcnt lgkmcnt(0)\ns_barrier\nv_mfma_f32_16x16x32_bf16 v[16:19],a[80:83],v[124:127],v[16:19]\n" \
"ds_read_b128 v[32:35],v58\nv_mfma_f32_16x16x32_bf16 v[16:19],a[112:115],v[192:195],v[16:19]\nds_read_b128 v[36:39],v58 offset:8192\nv_mfma_f32_16x16x32_bf16 v[20:23],a[84:87],v[124:127],v[20:23]\n" \
"ds_read_b128 v[40:43],v58 offset:16384\nv_lshlrev_b32 v60,16,v61\nv_mfma_f32_16x16x32_bf16 v[20:23],a[116:119],v[192:195],v[20:23]\nds_read_b128 v[44:47],v58 offset:24576\n" \
"v_and_b32 v61,0xffff0000,v61\nv_mfma_f32_16x16x32_bf16 v[24:27],a[88:91],v[124:127],v[24:27]\nglobal_load_dwordx4 v[100:103],v59,s[16:17] offset:1024 sc1\nds_read_b128 v[48:51],v58 offset:4096\n" \
"v_mfma_f32_16x16x32_bf16 v[24:27],a[120:123],v[192:195],v[24:27]\nds_read_b128 v[240:243],v58 offset:12288\nv_mfma_f32_16x16x32_bf16 v[28:31],a[92:95],v[124:127],v[28:31]\nds_read_b128 v[244:247],v58 offset:20480\n" \
"v_mfma_f32_16x16x32_bf16 v[28:31],a[124:127],v[192:195],v[28:31]\nds_read_b128 v[248:251],v58 offset:28672\ns_waitcnt vmcnt(12)\nv_mfma_f32_16x16x32_bf16 v[0:3],a[128:131],v[196:199],v[0:3]\n" \
"v_mfma_f32_16x16x32_bf16 v[0:3],a[160:163],v[200:203],v[0:3]\nv_mfma_f32_16x16x32_bf16 v[4:7],a[132:135],v[196:199],v[4:7]\nglobal_load_dwordx4 v[104:107],v59,s[16:17] offset:2048 sc1\nv_mfma_f32_16x16x32_bf16 v[4:7],a[164:167],v[200:203],v[4:7]\n" \
"s_waitcnt lgkmcnt(4)\nv_add_f32 v32,v32,v36\nv_mfma_f32_16x16x32_bf16 v[8:11],a[136:139],v[196:199],v[8:11]\nv_add_f32 v33,v33,v37\n" \
"v_add_f32 v34,v34,v38\nv_mfma_f32_16x16x32_bf16 v[8:11],a[168:171],v[200:203],v[8:11]\nv_add_f32 v35,v35,v39\nv_add_f32 v40,v40,v44\n" \
"v_mfma_f32_16x16x32_bf16 v[12:15],a[140:143],v[196:199],v[12:15]\nv_add_f32 v41,v41,v45\nv_add_f32 v42,v42,v46\nv_mfma_f32_16x16x32_bf16 v[12:15],a[172:175],v[200:203],v[12:15]\n" \
"v_add_f32 v43,v43,v47\nv_add_f32 v32,v32,v40\nv_mfma_f32_16x16x32_bf16 v[16:19],a[144:147],v[196:199],v[16:19]\nv_add_f32 v33,v33,v41\n" \
"v_add_f32 v34,v34,v42\nv_mfma_f32_16x16x32_bf16 v[16:19],a[176:179],v[200:203],v[16:19]\nv_add_f32 v35,v35,v43\nv_cvt_pk_bf16_f32 v36,v32,v33\n" \
"v_mfma_f32_16x16x32_bf16 v[20:23],a[148:151],v[196:199],v[20:23]\nv_cvt_pk_bf16_f32 v37,v34,v35\ns_waitcnt lgkmcnt(0)\nv_mfma_f32_16x16x32_bf16 v[20:23],a[180:183],v[200:203],v[20:23]\n" \
"v_add_f32 v48,v48,v240\nv_add_f32 v49,v49,v241\nv_mfma_f32_16x16x32_bf16 v[24:27],a[152:155],v[196:199],v[24:27]\nv_add_f32 v50,v50,v242\n" \
"v_add_f32 v51,v51,v243\nv_mfma_f32_16x16x32_bf16 v[24:27],a[184:187],v[200:203],v[24:27]\nv_add_f32 v244,v244,v248\nv_add_f32 v245,v245,v249\n" \
"v_mfma_f32_16x16x32_bf16 v[28:31],a[156:159],v[196:199],v[28:31]\nv_add_f32 v246,v246,v250\nv_add_f32 v247,v247,v251\nv_mfma_f32_16x16x32_bf16 v[28:31],a[188:191],v[200:203],v[28:31]\n" \
"v_add_f32 v48,v48,v244\ns_waitcnt vmcnt(12)\nv_mfma_f32_16x16x32_bf16 v[0:3],a[192:195],v[204:207],v[0:3]\nv_add_f32 v49,v49,v245\n" \
"v_add_f32 v50,v50,v246\nv_mfma_f32_16x16x32_bf16 v[4:7],a[196:199],v[204:207],v[4:7]\nv_add_f32 v51,v51,v247\nv_cvt_pk_bf16_f32 v38,v48,v49\n" \
"v_mfma_f32_16x16x32_bf16 v[8:11],a[200:203],v[204:207],v[8:11]\nv_cvt_pk_bf16_f32 v39,v50,v51\nbuffer_store_dwordx4 v[36:39],v59,s[36:39],s74 offen sc1\nv_mfma_f32_16x16x32_bf16 v[12:15],a[204:207],v[204:207],v[12:15]\n" \
"v_mfma_f32_16x16x32_bf16 v[16:19],a[208:211],v[204:207],v[16:19]\nv_mfma_f32_16x16x32_bf16 v[20:23],a[212:215],v[204:207],v[20:23]\nv_mfma_f32_16x16x32_bf16 v[24:27],a[216:219],v[204:207],v[24:27]\nv_mfma_f32_16x16x32_bf16 v[28:31],a[220:223],v[204:207],v[28:31]\n" \
"s_waitcnt vmcnt(9)\nv_mfma_f32_16x16x32_bf16 v[208:211],v[128:131],v[72:75],0\nv_add_u32 v59,409600,v59\ns_waitcnt vmcnt(7)\n" \
"v_mfma_f32_16x16x32_bf16 v[208:211],v[160:163],v[76:79],v[208:211]\nds_write_addtid_b32 v0 offset:32768\nv_max_u32 v62,v64,v52\nv_mfma_f32_16x16x32_bf16 v[212:215],v[132:135],v[72:75],0\n" \
"global_load_dwordx4 v[108:111],v59,s[12:13] sc1\nds_write_addtid_b32 v1 offset:33024\nv_mfma_f32_16x16x32_bf16 v[212:215],v[164:167],v[76:79],v[212:215]\nds_write_addtid_b32 v2 offset:33280\n" \
"v_cmp_eq_u32 vcc,-1,v62\nv_mfma_f32_16x16x32_bf16 v[216:219],v[136:139],v[72:75],0\nds_write_addtid_b32 v3 offset:33536\nv_mfma_f32_16x16x32_bf16 v[216:219],v[168:171],v[76:79],v[216:219]\n" \
"ds_write_addtid_b32 v4 offset:33792\ns_cbranch_vccnz .Lsp1_%=\n.Lbp1_%=:\nv_mfma_f32_16x16x32_bf16 v[220:223],v[140:143],v[72:75],0\n" \
"ds_write_addtid_b32 v5 offset:34048\nv_and_b32 v71,0xffff0000,v67\nv_mfma_f32_16x16x32_bf16 v[220:223],v[172:175],v[76:79],v[220:223]\nds_write_addtid_b32 v6 offset:34304\n" \
"v_lshlrev_b32 v70,16,v67\nv_mfma_f32_16x16x32_bf16 v[224:227],v[144:147],v[72:75],0\nglobal_load_dwordx4 v[112:115],v59,s[12:13] offset:1024 sc1\nds_write_addtid_b32 v7 offset:34560\n" \
"v_mfma_f32_16x16x32_bf16 v[224:227],v[176:179],v[76:79],v[224:227]\nds_write_addtid_b32 v8 offset:34816\nv_and_b32 v69,0xffff0000,v66\nv_mfma_f32_16x16x32_bf16 v[228:231],v[148:151],v[72:75],0\n" \
"ds_write_addtid_b32 v9 offset:35072\nv_lshlrev_b32 v68,16,v66\nv_mfma_f32_16x16x32_bf16 v[228:231],v[180:183],v[76:79],v[228:231]\nds_write_addtid_b32 v10 offset:35328\n" \
"v_and_b32 v67,0xffff0000,v65\nv_mfma_f32_16x16x32_bf16 v[232:235],v[152:155],v[72:75],0\nds_write_addtid_b32 v11 offset:35584\nv_lshlrev_b32 v66,16,v65\n" \
"v_mfma_f32_16x16x32_bf16 v[232:235],v[184:187],v[76:79],v[232:235]\nds_write_addtid_b32 v12 offset:35840\nv_and_b32 v65,0xffff0000,v64\nv_mfma_f32_16x16x32_bf16 v[236:239],v[156:159],v[72:75],0\n" \
"global_load_dwordx4 v[116:119],v59,s[12:13] offset:2048 sc1\nds_write_addtid_b32 v13 offset:36096\nv_mfma_f32_16x16x32_bf16 v[236:239],v[188:191],v[76:79],v[236:239]\nds_write_addtid_b32 v14 offset:36352\n" \
"s_waitcnt vmcnt(9)\nv_mfma_f32_16x16x32_bf16 v[208:211],a[0:3],v[80:83],v[208:211]\nds_write_addtid_b32 v15 offset:36608\nv_lshlrev_b32 v64,16,v64\n" \
"v_mfma_f32_16x16x32_bf16 v[208:211],a[32:35],v[84:87],v[208:211]\nds_write_addtid_b32 v16 offset:36864\nv_add_f32 v64,v64,v60\nv_mfma_f32_16x16x32_bf16 v[212:215],a[4:7],v[80:83],v[212:215]\n" \
"ds_write_addtid_b32 v17 offset:37120\nv_add_f32 v65,v65,v60\nv_mfma_f32_16x16x32_bf16 v[212:215],a[36:39],v[84:87],v[212:215]\nds_write_addtid_b32 v18 offset:37376\n" \
"v_add_f32 v66,v66,v60\nv_mfma_f32_16x16x32_bf16 v[216:219],a[8:11],v[80:83],v[216:219]\nglobal_load_dwordx4 v[120:123],v59,s[14:15] sc1\nds_write_addtid_b32 v19 offset:37632\n" \
"v_mfma_f32_16x16x32_bf16 v[216:219],a[40:43],v[84:87],v[216:219]\nds_write_addtid_b32 v20 offset:37888\nv_add_f32 v67,v67,v60\nv_mfma_f32_16x16x32_bf16 v[220:223],a[12:15],v[80:83],v[220:223]\n" \
"ds_write_addtid_b32 v21 offset:38144\nv_add_f32 v68,v68,v61\nv_mfma_f32_16x16x32_bf16 v[220:223],a[44:47],v[84:87],v[220:223]\nds_write_addtid_b32 v22 offset:38400\n" \
"v_add_f32 v69,v69,v61\nv_mfma_f32_16x16x32_bf16 v[224:227],a[16:19],v[80:83],v[224:227]\nds_write_addtid_b32 v23 offset:38656\nv_add_f32 v70,v70,v61\n" \
"v_mfma_f32_16x16x32_bf16 v[224:227],a[48:51],v[84:87],v[224:227]\nds_write_addtid_b32 v24 offset:38912\nv_add_f32 v71,v71,v61\nv_mfma_f32_16x16x32_bf16 v[228:231],a[20:23],v[80:83],v[228:231]\n" \
"global_load_dwordx4 v[124:127],v59,s[14:15] offset:1024 sc1\nds_write_addtid_b32 v25 offset:39168\nv_mfma_f32_16x16x32_bf16 v[228:231],a[52:55],v[84:87],v[228:231]\nds_write_addtid_b32 v26 offset:39424\n" \
"v_and_b32 v255,0xffff0000,v55\nv_mfma_f32_16x16x32_bf16 v[232:235],a[24:27],v[80:83],v[232:235]\nds_write_addtid_b32 v27 offset:39680\nv_lshlrev_b32 v254,16,v55\n" \
"v_mfma_f32_16x16x32_bf16 v[232:235],a[56:59],v[84:87],v[232:235]\nds_write_addtid_b32 v28 offset:39936\nv_and_b32 v253,0xffff0000,v54\nv_mfma_f32_16x16x32_bf16 v[236:239],a[28:31],v[80:83],v[236:239]\n" \
"ds_write_addtid_b32 v29 offset:40192\nv_lshlrev_b32 v252,16,v54\nv_mfma_f32_16x16x32_bf16 v[236:239],a[60:63],v[84:87],v[236:239]\nds_write_addtid_b32 v30 offset:40448\n" \
"s_waitcnt vmcnt(9)\nv_mfma_f32_16x16x32_bf16 v[208:211],a[64:67],v[88:91],v[208:211]\nglobal_load_dwordx4 v[192:195],v59,s[14:15] offset:2048 sc1\nds_write_addtid_b32 v31 offset:40704\n" \
"v_mfma_f32_16x16x32_bf16 v[208:211],a[96:99],v[92:95],v[208:211]\nv_and_b32 v55,0xffff0000,v53\nv_lshlrev_b32 v54,16,v53\nv_mfma_f32_16x16x32_bf16 v[212:215],a[68:71],v[88:91],v[212:215]\n" \
"v_and_b32 v53,0xffff0000,v52\nv_lshlrev_b32 v52,16,v52\nv_mfma_f32_16x16x32_bf16 v[212:215],a[100:103],v[92:95],v[212:215]\nv_add_f32 v52,v52,v60\n" \
"v_add_f32 v53,v53,v60\nv_mfma_f32_16x16x32_bf16 v[216:219],a[72:75],v[88:91],v[216:219]\nv_add_f32 v54,v54,v60\nv_add_f32 v55,v55,v60\n" \
"v_mfma_f32_16x16x32_bf16 v[216:219],a[104:107],v[92:95],v[216:219]\nv_add_f32 v252,v252,v61\nv_add_f32 v253,v253,v61\nv_mfma_f32_16x16x32_bf16 v[220:223],a[76:79],v[88:91],v[220:223]\n" \
"global_load_dwordx4 v[196:199],v59,s[16:17] sc1\nv_add_f32 v254,v254,v61\nv_mfma_f32_16x16x32_bf16 v[220:223],a[108:111],v[92:95],v[220:223]\ns_waitcnt lgkmcnt(0)\n" \
"s_barrier\nv_mfma_f32_16x16x32_bf16 v[224:227],a[80:83],v[88:91],v[224:227]\nds_read_b128 v[32:35],v58 offset:32768\nv_add_f32 v255,v255,v61\n" \
"v_mfma_f32_16x16x32_bf16 v[224:227],a[112:115],v[92:95],v[224:227]\nds_read_b128 v[36:39],v58 offset:40960\nv_mfma_f32_16x16x32_bf16 v[228:231],a[84:87],v[88:91],v[228:231]\nds_read_b128 v[40:43],v58 offset:49152\n" \
"v_mfma_f32_16x16x32_bf16 v[228:231],a[116:119],v[92:95],v[228:231]\nds_read_b128 v[44:47],v58 offset:57344\nv_mfma_f32_16x16x32_bf16 v[232:235],a[88:91],v[88:91],v[232:235]\nglobal_load_dwordx4 v[200:203],v59,s[16:17] offset:1024 sc1\n" \
"ds_read_b128 v[48:51],v58 offset:36864\nv_mfma_f32_16x16x32_bf16 v[232:235],a[120:123],v[92:95],v[232:235]\nds_read_b128 v[240:243],v58 offset:45056\nv_mfma_f32_16x16x32_bf16 v[236:239],a[92:95],v[88:91],v[236:239]\n" \
"ds_read_b128 v[244:247],v58 offset:53248\nv_mfma_f32_16x16x32_bf16 v[236:239],a[124:127],v[92:95],v[236:239]\nbuffer_load_dwordx4 v[60:63],v59,s[36:39],s72 offen sc1\ns_waitcnt vmcnt(11)\n" \
"v_mfma_f32_16x16x32_bf16 v[208:211],a[128:131],v[96:99],v[208:211]\nds_read_b128 v[248:251],v58 offset:61440\nv_mfma_f32_16x16x32_bf16 v[208:211],a[160:163],v[100:103],v[208:211]\nv_mfma_f32_16x16x32_bf16 v[212:215],a[132:135],v[96:99],v[212:215]\n" \
"global_load_dwordx4 v[204:207],v59,s[16:17] offset:2048 sc1\nv_mfma_f32_16x16x32_bf16 v[212:215],a[164:167],v[100:103],v[212:215]\ns_waitcnt lgkmcnt(4)\nv_add_f32 v32,v32,v36\n" \
"v_mfma_f32_16x16x32_bf16 v[216:219],a[136:139],v[96:99],v[216:219]\nv_add_f32 v33,v33,v37\nv_add_f32 v34,v34,v38\nv_mfma_f32_16x16x32_bf16 v[216:219],a[168:171],v[100:103],v[216:219]\n" \
"v_add_f32 v35,v35,v39\nv_add_f32 v40,v40,v44\nv_mfma_f32_16x16x32_bf16 v[220:223],a[140:143],v[96:99],v[220:223]\nv_add_f32 v41,v41,v45\n" \
"v_add_f32 v42,v42,v46\nv_mfma_f32_16x16x32_bf16 v[220:223],a[172:175],v[100:103],v[220:223]\nv_add_f32 v43,v43,v47\nv_add_f32 v32,v32,v40\n" \
"v_mfma_f32_16x16x32_bf16 v[224:227],a[144:147],v[96:99],v[224:227]\nv_add_f32 v33,v33,v41\nv_add_f32 v34,v34,v42\nv_mfma_f32_16x16x32_bf16 v[224:227],a[176:179],v[100:103],v[224:227]\n" \
"v_add_f32 v35,v35,v43\nv_cvt_pk_bf16_f32 v36,v32,v33\nv_mfma_f32_16x16x32_bf16 v[228:231],a[148:151],v[96:99],v[228:231]\nv_cvt_pk_bf16_f32 v37,v34,v35\n" \
"s_waitcnt lgkmcnt(0)\nv_mfma_f32_16x16x32_bf16 v[228:231],a[180:183],v[100:103],v[228:231]\nv_add_f32 v48,v48,v240\nv_add_f32 v49,v49,v241\n" \
"v_mfma_f32_16x16x32_bf16 v[232:235],a[152:155],v[96:99],v[232:235]\nv_add_f32 v50,v50,v242\nv_add_f32 v51,v51,v243\nv_mfma_f32_16x16x32_bf16 v[232:235],a[184:187],v[100:103],v[232:235]\n" \
"v_add_f32 v244,v244,v248\nv_add_f32 v245,v245,v249\nv_mfma_f32_16x16x32_bf16 v[236:239],a[156:159],v[96:99],v[236:239]\nv_add_f32 v246,v246,v250\n" \
"v_add_f32 v247,v247,v251\nv_mfma_f32_16x16x32_bf16 v[236:239],a[188:191],v[100:103],v[236:239]\nv_add_f32 v48,v48,v244\ns_waitcnt vmcnt(11)\n" \
"v_mfma_f32_16x16x32_bf16 v[208:211],a[192:195],v[104:107],v[208:211]\nv_add_f32 v49,v49,v245\nv_add_f32 v50,v50,v246\nv_mfma_f32_16x16x32_bf16 v[212:215],a[196:199],v[104:107],v[212:215]\n" \
"v_add_f32 v51,v51,v247\nv_cvt_pk_bf16_f32 v38,v48,v49\nv_mfma_f32_16x16x32_bf16 v[216:219],a[200:203],v[104:107],v[216:219]\nv_cvt_pk_bf16_f32 v39,v50,v51\n" \
"buffer_store_dwordx4 v[36:39],v59,s[36:39],s74 offen sc1\nv_mfma_f32_16x16x32_bf16 v[220:223],a[204:207],v[104:107],v[220:223]\nv_mfma_f32_16x16x32_bf16 v[224:227],a[208:211],v[104:107],v[224:227]\nv_mfma_f32_16x16x32_bf16 v[228:231],a[212:215],v[104:107],v[228:231]\n" \
"v_mfma_f32_16x16x32_bf16 v[232:235],a[216:219],v[104:107],v[232:235]\nv_mfma_f32_16x16x32_bf16 v[236:239],a[220:223],v[104:107],v[236:239]\ns_waitcnt vmcnt(9)\nv_mfma_f32_16x16x32_bf16 v[0:3],v[128:131],v[108:111],0\n" \
"v_add_u32 v59,409600,v59\nv_mfma_f32_16x16x32_bf16 v[0:3],v[160:163],v[112:115],v[0:3]\nds_write_addtid_b32 v208\nds_write_addtid_b32 v209 offset:256\n" \
"v_mfma_f32_16x16x32_bf16 v[4:7],v[132:135],v[108:111],0\nglobal_load_dwordx4 v[72:75],v59,s[12:13] sc1\nds_write_addtid_b32 v210 offset:512\nv_mfma_f32_16x16x32_bf16 v[4:7],v[164:167],v[112:115],v[4:7]\n" \
"ds_write_addtid_b32 v211 offset:768\nds_write_addtid_b32 v212 offset:1024\nv_mfma_f32_16x16x32_bf16 v[8:11],v[136:139],v[108:111],0\nds_write_addtid_b32 v213 offset:1280\n" \
"ds_write_addtid_b32 v214 offset:1536\nv_mfma_f32_16x16x32_bf16 v[8:11],v[168:171],v[112:115],v[8:11]\nds_write_addtid_b32 v215 offset:1792\nds_write_addtid_b32 v216 offset:2048\n" \
"v_mfma_f32_16x16x32_bf16 v[12:15],v[140:143],v[108:111],0\nds_write_addtid_b32 v217 offset:2304\nds_write_addtid_b32 v218 offset:2560\nv_mfma_f32_16x16x32_bf16 v[12:15],v[172:175],v[112:115],v[12:15]\n" \
"ds_write_addtid_b32 v219 offset:2816\nds_write_addtid_b32 v220 offset:3072\nv_mfma_f32_16x16x32_bf16 v[16:19],v[144:147],v[108:111],0\nglobal_load_dwordx4 v[76:79],v59,s[12:13] offset:1024 sc1\n" \
"ds_write_addtid_b32 v221 offset:3328\nv_mfma_f32_16x16x32_bf16 v[16:19],v[176:179],v[112:115],v[16:19]\nds_write_addtid_b32 v222 offset:3584\nds_write_addtid_b32 v223 offset:3840\n" \
"v_mfma_f32_16x16x32_bf16 v[20:23],v[148:151],v[108:111],0\nds_write_addtid_b32 v224 offset:4096\nds_write_addtid_b32 v225 offset:4352\nv_mfma_f32_16x16x32_bf16 v[20:23],v[180:183],v[112:115],v[20:23]\n" \
"ds_write_addtid_b32 v226 offset:4608\nds_write_addtid_b32 v227 offset:4864\nv_mfma_f32_16x16x32_bf16 v[24:27],v[152:155],v[108:111],0\nds_write_addtid_b32 v228 offset:5120\n" \
"ds_write_addtid_b32 v229 offset:5376\nv_mfma_f32_16x16x32_bf16 v[24:27],v[184:187],v[112:115],v[24:27]\nds_write_addtid_b32 v230 offset:5632\nds_write_addtid_b32 v231 offset:5888\n" \
"v_mfma_f32_16x16x32_bf16 v[28:31],v[156:159],v[108:111],0\nglobal_load_dwordx4 v[80:83],v59,s[12:13] offset:2048 sc1\nds_write_addtid_b32 v232 offset:6144\nv_mfma_f32_16x16x32_bf16 v[28:31],v[188:191],v[112:115],v[28:31]\n" \
"ds_write_addtid_b32 v233 offset:6400\ns_waitcnt vmcnt(10)\nv_mfma_f32_16x16x32_bf16 v[0:3],a[0:3],v[116:119],v[0:3]\nds_write_addtid_b32 v234 offset:6656\n" \
"ds_write_addtid_b32 v235 offset:6912\nv_mfma_f32_16x16x32_bf16 v[0:3],a[32:35],v[120:123],v[0:3]\nbuffer_load_dwordx4 v[108:111],v59,s[36:39],s77 offen sc1\nds_write_addtid_b32 v236 offset:7168\n" \
"v_mfma_f32_16x16x32_bf16 v[4:7],a[4:7],v[116:119],v[4:7]\nds_write_addtid_b32 v237 offset:7424\nds_write_addtid_b32 v238 offset:7680\nv_mfma_f32_16x16x32_bf16 v[4:7],a[36:39],v[120:123],v[4:7]\n" \
"ds_write_addtid_b32 v239 offset:7936\nv_mfma_f32_16x16x32_bf16 v[8:11],a[8:11],v[116:119],v[8:11]\nglobal_load_dwordx4 v[84:87],v59,s[14:15] sc1\nv_mfma_f32_16x16x32_bf16 v[8:11],a[40:43],v[120:123],v[8:11]\n" \
"v_mfma_f32_16x16x32_bf16 v[12:15],a[12:15],v[116:119],v[12:15]\nv_mfma_f32_16x16x32_bf16 v[12:15],a[44:47],v[120:123],v[12:15]\nv_mfma_f32_16x16x32_bf16 v[16:19],a[16:19],v[116:119],v[16:19]\nv_mfma_f32_16x16x32_bf16 v[16:19],a[48:51],v[120:123],v[16:19]\n" \
"s_waitcnt lgkmcnt(0)\ns_barrier\nv_mfma_f32_16x16x32_bf16 v[20:23],a[20:23],v[116:119],v[20:23]\nglobal_load_dwordx4 v[88:91],v59,s[14:15] offset:1024 sc1\n" \
"ds_read_b128 v[32:35],v58\nv_mfma_f32_16x16x32_bf16 v[20:23],a[52:55],v[120:123],v[20:23]\nds_read_b128 v[36:39],v58 offset:8192\nds_read_b128 v[40:43],v58 offset:16384\n" \
"v_mfma_f32_16x16x32_bf16 v[24:27],a[24:27],v[116:119],v[24:27]\nds_read_b128 v[44:47],v58 offset:24576\nds_read_b128 v[48:51],v58 offset:4096\nv_mfma_f32_16x16x32_bf16 v[24:27],a[56:59],v[120:123],v[24:27]\n" \
"ds_read_b128 v[240:243],v58 offset:12288\nds_read_b128 v[244:247],v58 offset:20480\nv_mfma_f32_16x16x32_bf16 v[28:31],a[28:31],v[116:119],v[28:31]\nds_read_b128 v[248:251],v58 offset:28672\n" \
"s_waitcnt vmcnt(8)\nv_mfma_f32_16x16x32_bf16 v[28:31],a[60:63],v[120:123],v[28:31]\nv_cmp_eq_u32 vcc,-1,v60\nv_mfma_f32_16x16x32_bf16 v[0:3],a[64:67],v[124:127],v[0:3]\n" \
"global_load_dwordx4 v[92:95],v59,s[14:15] offset:2048 sc1\nv_mfma_f32_16x16x32_bf16 v[0:3],a[96:99],v[192:195],v[0:3]\nbuffer_load_dwordx2 v[116:117],v56,s[36:39],s90 offen sc1\ns_cbranch_vccnz .Lsp2A_%=\n" \
".Lbp2A_%=:\nv_mfma_f32_16x16x32_bf16 v[4:7],a[68:71],v[124:127],v[4:7]\nbuffer_load_dwordx2 v[118:119],v56,s[36:39],s90 offen offset:32 sc1\nv_lshlrev_b32 v112,16,v60\n" \
"v_mfma_f32_16x16x32_bf16 v[4:7],a[100:103],v[192:195],v[4:7]\nv_and_b32 v113,0xffff0000,v60\nv_add_f32 v64,v64,v112\nv_mfma_f32_16x16x32_bf16 v[8:11],a[72:75],v[124:127],v[8:11]\n" \
"s_waitcnt lgkmcnt(4)\nv_add_f32 v32,v32,v36\nv_mfma_f32_16x16x32_bf16 v[8:11],a[104:107],v[192:195],v[8:11]\nv_add_f32 v33,v33,v37\n" \
"v_add_f32 v34,v34,v38\nv_mfma_f32_16x16x32_bf16 v[12:15],a[76:79],v[124:127],v[12:15]\nglobal_load_dwordx4 v[96:99],v59,s[16:17] sc1\nv_add_f32 v35,v35,v39\n" \
"v_mfma_f32_16x16x32_bf16 v[12:15],a[108:111],v[192:195],v[12:15]\nbuffer_load_dwordx2 v[120:121],v56,s[36:39],s100 offen sc1\nv_add_f32 v40,v40,v44\nv_mfma_f32_16x16x32_bf16 v[16:19],a[80:83],v[124:127],v[16:19]\n" \
"buffer_load_dwordx2 v[122:123],v56,s[36:39],s100 offen offset:32 sc1\nv_add_f32 v41,v41,v45\nv_mfma_f32_16x16x32_bf16 v[16:19],a[112:115],v[192:195],v[16:19]\nv_add_f32 v42,v42,v46\n" \
"v_add_f32 v43,v43,v47\nv_mfma_f32_16x16x32_bf16 v[20:23],a[84:87],v[124:127],v[20:23]\nv_add_f32 v32,v32,v40\nv_add_f32 v33,v33,v41\n" \
"v_mfma_f32_16x16x32_bf16 v[20:23],a[116:119],v[192:195],v[20:23]\nv_add_f32 v34,v34,v42\nv_add_f32 v35,v35,v43\nv_mfma_f32_16x16x32_bf16 v[24:27],a[88:91],v[124:127],v[24:27]\n" \
"global_load_dwordx4 v[100:103],v59,s[16:17] offset:1024 sc1\nv_cvt_pk_bf16_f32 v36,v32,v33\nv_mfma_f32_16x16x32_bf16 v[24:27],a[120:123],v[192:195],v[24:27]\nv_cvt_pk_bf16_f32 v37,v34,v35\n" \
"s_waitcnt lgkmcnt(0)\nv_mfma_f32_16x16x32_bf16 v[28:31],a[92:95],v[124:127],v[28:31]\nv_add_f32 v48,v48,v240\nv_add_f32 v49,v49,v241\n" \
"v_mfma_f32_16x16x32_bf16 v[28:31],a[124:127],v[192:195],v[28:31]\nv_add_f32 v50,v50,v242\nv_mfma_f32_16x16x32_bf16 v[0:3],a[128:131],v[196:199],v[0:3]\nv_add_f32 v65,v65,v113\n" \
"v_add_f32 v51,v51,v243\nv_mfma_f32_16x16x32_bf16 v[0:3],a[160:163],v[200:203],v[0:3]\nv_lshlrev_b32 v112,16,v61\nv_add_f32 v244,v244,v248\n" \
"v_mfma_f32_16x16x32_bf16 v[4:7],a[132:135],v[196:199],v[4:7]\nglobal_load_dwordx4 v[104:107],v59,s[16:17] offset:2048 sc1\nv_and_b32 v113,0xffff0000,v61\nv_mfma_f32_16x16x32_bf16 v[4:7],a[164:167],v[200:203],v[4:7]\n" \
"v_add_f32 v245,v245,v249\nv_add_f32 v66,v66,v112\nv_mfma_f32_16x16x32_bf16 v[8:11],a[136:139],v[196:199],v[8:11]\nv_add_f32 v246,v246,v250\n" \
"v_add_f32 v67,v67,v113\nv_mfma_f32_16x16x32_bf16 v[8:11],a[168:171],v[200:203],v[8:11]\nv_add_f32 v247,v247,v251\nv_lshlrev_b32 v112,16,v62\n" \
"v_mfma_f32_16x16x32_bf16 v[12:15],a[140:143],v[196:199],v[12:15]\nv_add_f32 v48,v48,v244\nv_and_b32 v113,0xffff0000,v62\nv_mfma_f32_16x16x32_bf16 v[12:15],a[172:175],v[200:203],v[12:15]\n" \
"v_add_f32 v49,v49,v245\nv_add_f32 v68,v68,v112\nv_mfma_f32_16x16x32_bf16 v[16:19],a[144:147],v[196:199],v[16:19]\nv_add_f32 v50,v50,v246\n" \
"v_add_f32 v69,v69,v113\nv_mfma_f32_16x16x32_bf16 v[16:19],a[176:179],v[200:203],v[16:19]\nv_add_f32 v51,v51,v247\nv_lshlrev_b32 v112,16,v63\n" \
"v_mfma_f32_16x16x32_bf16 v[20:23],a[148:151],v[196:199],v[20:23]\nv_cvt_pk_bf16_f32 v38,v48,v49\nv_and_b32 v113,0xffff0000,v63\nv_mfma_f32_16x16x32_bf16 v[20:23],a[180:183],v[200:203],v[20:23]\n" \
"v_cvt_pk_bf16_f32 v39,v50,v51\nv_add_f32 v70,v70,v112\nv_mfma_f32_16x16x32_bf16 v[24:27],a[152:155],v[196:199],v[24:27]\nbuffer_store_dwordx4 v[36:39],v59,s[36:39],s74 offen sc1\n" \
"v_add_f32 v71,v71,v113\nv_mfma_f32_16x16x32_bf16 v[24:27],a[184:187],v[200:203],v[24:27]\nv_mfma_f32_16x16x32_bf16 v[28:31],a[156:159],v[196:199],v[28:31]\nv_mfma_f32_16x16x32_bf16 v[28:31],a[188:191],v[200:203],v[28:31]\n" \
"s_waitcnt vmcnt(16)\nv_mfma_f32_16x16x32_bf16 v[0:3],a[192:195],v[204:207],v[0:3]\nv_mfma_f32_16x16x32_bf16 v[4:7],a[196:199],v[204:207],v[4:7]\nv_mfma_f32_16x16x32_bf16 v[8:11],a[200:203],v[204:207],v[8:11]\n" \
"v_mfma_f32_16x16x32_bf16 v[12:15],a[204:207],v[204:207],v[12:15]\nv_mfma_f32_16x16x32_bf16 v[16:19],a[208:211],v[204:207],v[16:19]\nv_mfma_f32_16x16x32_bf16 v[20:23],a[212:215],v[204:207],v[20:23]\nv_mfma_f32_16x16x32_bf16 v[24:27],a[216:219],v[204:207],v[24:27]\n" \
"v_mfma_f32_16x16x32_bf16 v[28:31],a[220:223],v[204:207],v[28:31]\ns_waitcnt vmcnt(13)\nv_mfma_f32_16x16x32_bf16 v[208:211],v[128:131],v[72:75],0\nv_add_u32 v59,409600,v59\n" \
"s_waitcnt vmcnt(11)\nv_mfma_f32_16x16x32_bf16 v[208:211],v[160:163],v[76:79],v[208:211]\nds_write_addtid_b32 v0 offset:32768\nds_write_addtid_b32 v1 offset:33024\n" \
"v_mfma_f32_16x16x32_bf16 v[212:215],v[132:135],v[72:75],0\nds_write_addtid_b32 v2 offset:33280\nds_write_addtid_b32 v3 offset:33536\nv_mfma_f32_16x16x32_bf16 v[212:215],v[164:167],v[76:79],v[212:215]\n" \
"buffer_load_dwordx4 v[112:115],v59,s[36:39],s73 offen sc1\nds_write_addtid_b32 v4 offset:33792\nv_mfma_f32_16x16x32_bf16 v[216:219],v[136:139],v[72:75],0\nds_write_addtid_b32 v5 offset:34048\n" \
"ds_write_addtid_b32 v6 offset:34304\nv_mfma_f32_16x16x32_bf16 v[216:219],v[168:171],v[76:79],v[216:219]\nds_write_addtid_b32 v7 offset:34560\nds_write_addtid_b32 v8 offset:34816\n" \
"v_mfma_f32_16x16x32_bf16 v[220:223],v[140:143],v[72:75],0\nds_write_addtid_b32 v9 offset:35072\nds_write_addtid_b32 v10 offset:35328\nv_mfma_f32_16x16x32_bf16 v[220:223],v[172:175],v[76:79],v[220:223]\n" \
"ds_write_addtid_b32 v11 offset:35584\ns_waitcnt vmcnt(11)\nv_mfma_f32_16x16x32_bf16 v[208:211],a[0:3],v[80:83],v[208:211]\nds_write_addtid_b32 v12 offset:35840\n" \
"ds_write_addtid_b32 v13 offset:36096\nv_mfma_f32_16x16x32_bf16 v[208:211],a[32:35],v[84:87],v[208:211]\nds_write_addtid_b32 v14 offset:36352\nds_write_addtid_b32 v15 offset:36608\n" \
"v_mfma_f32_16x16x32_bf16 v[212:215],a[4:7],v[80:83],v[212:215]\nds_write_addtid_b32 v16 offset:36864\nds_write_addtid_b32 v17 offset:37120\nv_mfma_f32_16x16x32_bf16 v[212:215],a[36:39],v[84:87],v[212:215]\n" \
"ds_write_addtid_b32 v18 offset:37376\nds_write_addtid_b32 v19 offset:37632\nv_mfma_f32_16x16x32_bf16 v[216:219],a[8:11],v[80:83],v[216:219]\nds_write_addtid_b32 v20 offset:37888\n" \
"ds_write_addtid_b32 v21 offset:38144\nv_mfma_f32_16x16x32_bf16 v[216:219],a[40:43],v[84:87],v[216:219]\nds_write_addtid_b32 v22 offset:38400\nds_write_addtid_b32 v23 offset:38656\n" \
"v_mfma_f32_16x16x32_bf16 v[220:223],a[12:15],v[80:83],v[220:223]\nds_write_addtid_b32 v24 offset:38912\nds_write_addtid_b32 v25 offset:39168\nv_mfma_f32_16x16x32_bf16 v[220:223],a[44:47],v[84:87],v[220:223]\n" \
"ds_write_addtid_b32 v26 offset:39424\ns_waitcnt vmcnt(9)\nv_mfma_f32_16x16x32_bf16 v[208:211],a[64:67],v[88:91],v[208:211]\nds_write_addtid_b32 v27 offset:39680\n" \
"ds_write_addtid_b32 v28 offset:39936\nv_mfma_f32_16x16x32_bf16 v[208:211],a[96:99],v[92:95],v[208:211]\nds_write_addtid_b32 v29 offset:40192\nds_write_addtid_b32 v30 offset:40448\n" \
"v_mfma_f32_16x16x32_bf16 v[212:215],a[68:71],v[88:91],v[212:215]\nds_write_addtid_b32 v31 offset:40704\nv_cmp_eq_u32 vcc,-1,v108\nv_mfma_f32_16x16x32_bf16 v[212:215],a[100:103],v[92:95],v[212:215]\n" \
"v_and_b32 v62,0x30,v59\nv_add_u32 v62,s67,v62\nv_mfma_f32_16x16x32_bf16 v[216:219],a[72:75],v[88:91],v[216:219]\ns_cbranch_vccnz .Lsp2B_%=\n" \
".Lbp2B_%=:\nv_lshlrev_b32 v60,16,v108\nv_mfma_f32_16x16x32_bf16 v[216:219],a[104:107],v[92:95],v[216:219]\nv_and_b32 v61,0xffff0000,v108\n" \
"v_add_f32 v52,v52,v60\nv_mfma_f32_16x16x32_bf16 v[220:223],a[76:79],v[88:91],v[220:223]\nv_add_f32 v53,v53,v61\nv_lshlrev_b32 v60,16,v109\n" \
"v_mfma_f32_16x16x32_bf16 v[220:223],a[108:111],v[92:95],v[220:223]\nv_and_b32 v61,0xffff0000,v109\ns_waitcnt vmcnt(3)\nv_mfma_f32_16x16x32_bf16 v[208:211],a[128:131],v[96:99],v[208:211]\n" \
"s_waitcnt lgkmcnt(0)\ns_barrier\nv_mfma_f32_16x16x32_bf16 v[208:211],a[160:163],v[100:103],v[208:211]\nds_read_b128 v[32:35],v58 offset:32768\n" \
"ds_read_b128 v[36:39],v58 offset:40960\nv_mfma_f32_16x16x32_bf16 v[212:215],a[132:135],v[96:99],v[212:215]\nds_read_b128 v[40:43],v58 offset:49152\nds_read_b128 v[44:47],v58 offset:57344\n" \
"v_mfma_f32_16x16x32_bf16 v[212:215],a[164:167],v[100:103],v[212:215]\nds_read_b128 v[48:51],v58 offset:36864\nds_read_b128 v[240:243],v58 offset:45056\nv_mfma_f32_16x16x32_bf16 v[216:219],a[136:139],v[96:99],v[216:219]\n" \
"ds_read_b128 v[244:247],v58 offset:53248\nds_read_b128 v[248:251],v58 offset:61440\nv_mfma_f32_16x16x32_bf16 v[216:219],a[168:171],v[100:103],v[216:219]\nv_add_f32 v54,v54,v60\n" \
"v_add_f32 v55,v55,v61\nv_mfma_f32_16x16x32_bf16 v[220:223],a[140:143],v[96:99],v[220:223]\nv_lshlrev_b32 v60,16,v110\nv_and_b32 v61,0xffff0000,v110\n" \
"v_mfma_f32_16x16x32_bf16 v[220:223],a[172:175],v[100:103],v[220:223]\nv_add_f32 v252,v252,v60\ns_waitcnt vmcnt(2)\nv_mfma_f32_16x16x32_bf16 v[208:211],a[192:195],v[104:107],v[208:211]\n" \
"v_add_f32 v253,v253,v61\nv_lshlrev_b32 v60,16,v111\nv_mfma_f32_16x16x32_bf16 v[212:215],a[196:199],v[104:107],v[212:215]\nv_and_b32 v61,0xffff0000,v111\n" \
"v_add_f32 v254,v254,v60\nv_mfma_f32_16x16x32_bf16 v[216:219],a[200:203],v[104:107],v[216:219]\ns_waitcnt lgkmcnt(4)\nv_add_f32 v32,v32,v36\n" \
"v_mfma_f32_16x16x32_bf16 v[220:223],a[204:207],v[104:107],v[220:223]\nv_add_f32 v33,v33,v37\nv_add_f32 v34,v34,v38\nv_mfma_f32_16x16x32_bf16 v[224:227],v[144:147],v[72:75],0\n" \
"v_add_f32 v35,v35,v39\nv_add_f32 v40,v40,v44\nv_mfma_f32_16x16x32_bf16 v[224:227],v[176:179],v[76:79],v[224:227]\nv_add_f32 v41,v41,v45\n" \
"v_add_f32 v42,v42,v46\nv_mfma_f32_16x16x32_bf16 v[228:231],v[148:151],v[72:75],0\nds_write_addtid_b32 v208\nds_write_addtid_b32 v209 offset:256\n" \
"v_mfma_f32_16x16x32_bf16 v[228:231],v[180:183],v[76:79],v[228:231]\nds_write_addtid_b32 v210 offset:512\nds_write_addtid_b32 v211 offset:768\nv_mfma_f32_16x16x32_bf16 v[232:235],v[152:155],v[72:75],0\n" \
"ds_write_addtid_b32 v212 offset:1024\nds_write_addtid_b32 v213 offset:1280\nv_mfma_f32_16x16x32_bf16 v[232:235],v[184:187],v[76:79],v[232:235]\nds_write_addtid_b32 v214 offset:1536\n" \
"ds_write_addtid_b32 v215 offset:1792\nv_mfma_f32_16x16x32_bf16 v[236:239],v[156:159],v[72:75],0\nds_write_addtid_b32 v216 offset:2048\nds_write_addtid_b32 v217 offset:2304\n" \
"v_mfma_f32_16x16x32_bf16 v[236:239],v[188:191],v[76:79],v[236:239]\nds_write_addtid_b32 v218 offset:2560\nds_write_addtid_b32 v219 offset:2816\nv_mfma_f32_16x16x32_bf16 v[224:227],a[16:19],v[80:83],v[224:227]\n" \
"ds_write_addtid_b32 v220 offset:3072\nds_write_addtid_b32 v221 offset:3328\nv_mfma_f32_16x16x32_bf16 v[224:227],a[48:51],v[84:87],v[224:227]\nds_write_addtid_b32 v222 offset:3584\n" \
"ds_write_addtid_b32 v223 offset:3840\nv_mfma_f32_16x16x32_bf16 v[228:231],a[20:23],v[80:83],v[228:231]\nv_add_f32 v43,v43,v47\nv_add_f32 v32,v32,v40\n" \
"v_mfma_f32_16x16x32_bf16 v[228:231],a[52:55],v[84:87],v[228:231]\nv_add_f32 v33,v33,v41\nv_add_f32 v34,v34,v42\nv_mfma_f32_16x16x32_bf16 v[232:235],a[24:27],v[80:83],v[232:235]\n" \
"v_add_f32 v255,v255,v61\nv_add_f32 v35,v35,v43\nv_mfma_f32_16x16x32_bf16 v[232:235],a[56:59],v[84:87],v[232:235]\ns_waitcnt vmcnt(0)\n" \
"v_mfma_f32_16x16x32_bf16 v[236:239],a[28:31],v[80:83],v[236:239]\nbuffer_load_dwordx4 v[124:127],v59,s[36:39],s78 offen sc1\nv_cmp_eq_u32 vcc,-1,v112\nv_mfma_f32_16x16x32_bf16 v[236:239],a[60:63],v[84:87],v[236:239]\n" \
"s_waitcnt lgkmcnt(0)\ns_barrier\nv_mfma_f32_16x16x32_bf16 v[224:227],a[80:83],v[88:91],v[224:227]\nds_read_b128 v[16:19],v58\n" \
"ds_read_b128 v[20:23],v58 offset:8192\nv_mfma_f32_16x16x32_bf16 v[224:227],a[112:115],v[92:95],v[224:227]\nds_read_b128 v[24:27],v58 offset:16384\nds_read_b128 v[28:31],v58 offset:24576\n" \
"v_mfma_f32_16x16x32_bf16 v[228:231],a[84:87],v[88:91],v[228:231]\nv_add_f32 v48,v48,v240\ns_cbranch_vccnz .Lsp3A_%=\n.Lbp3A_%=:\n" \
"v_mfma_f32_16x16x32_bf16 v[228:231],a[116:119],v[92:95],v[228:231]\nv_add_f32 v49,v49,v241\nv_lshlrev_b32 v60,16,v112\nv_mfma_f32_16x16x32_bf16 v[232:235],a[88:91],v[88:91],v[232:235]\n" \
"v_add_f32 v50,v50,v242\nv_and_b32 v61,0xffff0000,v112\nv_mfma_f32_16x16x32_bf16 v[232:235],a[120:123],v[92:95],v[232:235]\nv_add_f32 v51,v51,v243\n" \
"v_add_f32 v64,v64,v60\nv_mfma_f32_16x16x32_bf16 v[236:239],a[92:95],v[88:91],v[236:239]\nv_add_f32 v244,v244,v248\nv_add_f32 v65,v65,v61\n" \
"v_mfma_f32_16x16x32_bf16 v[236:239],a[124:127],v[92:95],v[236:239]\nv_add_f32 v245,v245,v249\nv_lshlrev_b32 v60,16,v113\nv_mfma_f32_16x16x32_bf16 v[224:227],a[144:147],v[96:99],v[224:227]\n" \
"v_add_f32 v246,v246,v250\nv_and_b32 v61,0xffff0000,v113\nv_mfma_f32_16x16x32_bf16 v[224:227],a[176:179],v[100:103],v[224:227]\nv_add_f32 v247,v247,v251\n" \
"v_add_f32 v66,v66,v60\nv_mfma_f32_16x16x32_bf16 v[228:231],a[148:151],v[96:99],v[228:231]\nv_add_f32 v48,v48,v244\nv_add_f32 v67,v67,v61\n" \
"v_mfma_f32_16x16x32_bf16 v[228:231],a[180:183],v[100:103],v[228:231]\nv_add_f32 v49,v49,v245\nv_lshlrev_b32 v60,16,v114\nv_add_f32 v50,v50,v246\n" \
"v_mfma_f32_16x16x32_bf16 v[232:235],a[152:155],v[96:99],v[232:235]\nv_and_b32 v61,0xffff0000,v114\nv_add_f32 v51,v51,v247\nv_add_f32 v68,v68,v60\n" \
"v_mfma_f32_16x16x32_bf16 v[232:235],a[184:187],v[100:103],v[232:235]\nv_add_f32 v69,v69,v61\nv_lshlrev_b32 v60,16,v115\nv_and_b32 v61,0xffff0000,v115\n" \
"v_mfma_f32_16x16x32_bf16 v[236:239],a[156:159],v[96:99],v[236:239]\nv_add_f32 v70,v70,v60\nds_read_b128 v[8:11],v62\nv_mfma_f32_16x16x32_bf16 v[236:239],a[188:191],v[100:103],v[236:239]\n" \
"v_add_f32 v71,v71,v61\nds_read_b128 v[12:15],v62 offset:64\nv_mfma_f32_16x16x32_bf16 v[224:227],a[208:211],v[104:107],v[224:227]\nv_lshlrev_b32 v0,16,v116\n" \
"v_and_b32 v1,0xffff0000,v116\nv_mfma_f32_16x16x32_bf16 v[228:231],a[212:215],v[104:107],v[228:231]\nv_lshlrev_b32 v2,16,v117\nv_and_b32 v3,0xffff0000,v117\n" \
"v_mfma_f32_16x16x32_bf16 v[232:235],a[216:219],v[104:107],v[232:235]\nv_lshlrev_b32 v4,16,v120\nv_and_b32 v5,0xffff0000,v120\nv_mfma_f32_16x16x32_bf16 v[236:239],a[220:223],v[104:107],v[236:239]\n" \
"v_lshlrev_b32 v6,16,v121\nv_and_b32 v7,0xffff0000,v121\ns_waitcnt lgkmcnt(0)\nv_fma_f32 v0,v8,v64,v0\n" \
"v_fma_f32 v1,v9,v65,v1\nv_fma_f32 v2,v10,v66,v2\nv_fma_f32 v3,v11,v67,v3\nv_fma_f32 v4,v8,v68,v4\n" \
"v_fma_f32 v5,v9,v69,v5\nv_fma_f32 v6,v10,v70,v6\nv_fma_f32 v7,v11,v71,v7\nv_fma_f32 v0,v8,v32,v0\n" \
"v_fma_f32 v1,v9,v33,v1\nv_fma_f32 v2,v10,v34,v2\nv_fma_f32 v3,v11,v35,v3\nv_cvt_pk_bf16_f32 v0,v0,v1\n" \
"v_cvt_pk_bf16_f32 v1,v2,v3\nbuffer_store_dwordx2 v[0:1],v56,s[44:47],0 offen sc1\nv_fma_f32 v4,v8,v48,v4\nv_fma_f32 v5,v9,v49,v5\n" \
"v_fma_f32 v6,v10,v50,v6\nv_fma_f32 v7,v11,v51,v7\nv_cvt_pk_bf16_f32 v4,v4,v5\nv_cvt_pk_bf16_f32 v5,v6,v7\n" \
"buffer_store_dwordx2 v[4:5],v56,s[44:47],s35 offen sc1\nv_lshlrev_b32 v64,16,v118\nv_and_b32 v65,0xffff0000,v118\nv_lshlrev_b32 v66,16,v119\n" \
"v_and_b32 v67,0xffff0000,v119\nds_write_addtid_b32 v224 offset:4096\nds_write_addtid_b32 v225 offset:4352\nds_write_addtid_b32 v226 offset:4608\n" \
"ds_write_addtid_b32 v227 offset:4864\nds_write_addtid_b32 v228 offset:5120\nds_write_addtid_b32 v229 offset:5376\nds_write_addtid_b32 v230 offset:5632\n" \
"ds_write_addtid_b32 v231 offset:5888\nds_write_addtid_b32 v232 offset:6144\nds_write_addtid_b32 v233 offset:6400\nds_write_addtid_b32 v234 offset:6656\n" \
"ds_write_addtid_b32 v235 offset:6912\nds_write_addtid_b32 v236 offset:7168\nds_write_addtid_b32 v237 offset:7424\nds_write_addtid_b32 v238 offset:7680\n" \
"ds_write_addtid_b32 v239 offset:7936\nv_lshlrev_b32 v68,16,v122\nv_and_b32 v69,0xffff0000,v122\nv_lshlrev_b32 v70,16,v123\n" \
"v_and_b32 v71,0xffff0000,v123\ns_waitcnt lgkmcnt(15)\nv_pk_add_f32 v[16:17],v[16:17],v[20:21]\nv_pk_add_f32 v[18:19],v[18:19],v[22:23]\n" \
"v_pk_add_f32 v[24:25],v[24:25],v[28:29]\nv_pk_add_f32 v[26:27],v[26:27],v[30:31]\nv_pk_add_f32 v[16:17],v[16:17],v[24:25]\nv_pk_add_f32 v[18:19],v[18:19],v[26:27]\n" \
"s_waitcnt lgkmcnt(0)\ns_barrier\nds_read_b128 v[32:35],v58 offset:4096\nds_read_b128 v[36:39],v58 offset:12288\n" \
"ds_read_b128 v[40:43],v58 offset:20480\nds_read_b128 v[44:47],v58 offset:28672\ns_waitcnt vmcnt(2)\nv_cmp_eq_u32 vcc,-1,v124\n" \
"s_cbranch_vccnz .Lsp3B_%=\n.Lbp3B_%=:\nv_lshlrev_b32 v20,16,v124\nv_and_b32 v21,0xffff0000,v124\n" \
"v_lshlrev_b32 v22,16,v125\nv_and_b32 v23,0xffff0000,v125\nv_lshlrev_b32 v24,16,v126\nv_and_b32 v25,0xffff0000,v126\n" \
"v_lshlrev_b32 v26,16,v127\nv_and_b32 v27,0xffff0000,v127\nv_pk_add_f32 v[52:53],v[52:53],v[20:21]\nv_pk_add_f32 v[54:55],v[54:55],v[22:23]\n" \
"v_pk_add_f32 v[252:253],v[252:253],v[24:25]\nv_pk_add_f32 v[254:255],v[254:255],v[26:27]\nv_pk_fma_f32 v[64:65],v[12:13],v[52:53],v[64:65]\nv_pk_fma_f32 v[66:67],v[14:15],v[54:55],v[66:67]\n" \
"v_pk_fma_f32 v[68:69],v[12:13],v[252:253],v[68:69]\nv_pk_fma_f32 v[70:71],v[14:15],v[254:255],v[70:71]\nv_pk_fma_f32 v[64:65],v[12:13],v[16:17],v[64:65]\nv_pk_fma_f32 v[66:67],v[14:15],v[18:19],v[66:67]\n" \
"v_cvt_pk_bf16_f32 v64,v64,v65\nv_cvt_pk_bf16_f32 v65,v66,v67\nbuffer_store_dwordx2 v[64:65],v56,s[44:47],0 offen offset:32 sc1\ns_waitcnt lgkmcnt(0)\n" \
"v_pk_add_f32 v[32:33],v[32:33],v[36:37]\nv_pk_add_f32 v[34:35],v[34:35],v[38:39]\nv_pk_add_f32 v[40:41],v[40:41],v[44:45]\nv_pk_add_f32 v[42:43],v[42:43],v[46:47]\n" \
"v_pk_add_f32 v[32:33],v[32:33],v[40:41]\nv_pk_add_f32 v[34:35],v[34:35],v[42:43]\nv_pk_fma_f32 v[68:69],v[12:13],v[32:33],v[68:69]\nv_pk_fma_f32 v[70:71],v[14:15],v[34:35],v[70:71]\n" \
"v_cvt_pk_bf16_f32 v68,v68,v69\nv_cvt_pk_bf16_f32 v69,v70,v71\nbuffer_store_dwordx2 v[68:69],v56,s[44:47],s35 offen offset:32 sc1\ns_branch .Lend_%=\n" \
".LsaF_%=:\ns_memrealtime s[94:95]\ns_waitcnt lgkmcnt(0)\n.LpaF_%=:\n" \
"s_sleep 2\nbuffer_load_dword v63,v61,s[36:39],s59 offen sc1\ns_memrealtime s[96:97]\ns_waitcnt vmcnt(0) lgkmcnt(0)\n" \
"s_sub_u32 s96,s96,s94\ns_cmp_gt_u32 s96,0x4c4b40\ns_cbranch_scc1 .LbaF_%=\nv_cmp_eq_u32 vcc,0,v63\n" \
"s_and_b64 vcc,vcc,s[86:87]\ns_cbranch_scc1 .LpaF_%=\ns_branch .LbaF_%=\n.LsbF_%=:\n" \
"s_memrealtime s[94:95]\ns_waitcnt lgkmcnt(0)\n.LpbF_%=:\ns_sleep 2\n" \
"buffer_load_dword v63,v61,s[36:39],s59 offen sc1\ns_memrealtime s[96:97]\ns_waitcnt vmcnt(0) lgkmcnt(0)\ns_sub_u32 s96,s96,s94\n" \
"s_cmp_gt_u32 s96,0x4c4b40\ns_cbranch_scc1 .LbbF_%=\nv_cmp_eq_u32 vcc,0,v63\ns_and_b64 vcc,vcc,s[86:87]\n" \
"s_cbranch_scc1 .LpbF_%=\ns_branch .LbbF_%=\n.LsaS_%=:\ns_memrealtime s[94:95]\n" \
"s_waitcnt lgkmcnt(0)\n.LpaS_%=:\ns_sleep 2\nbuffer_load_dword v63,v61,s[36:39],s59 offen sc1\n" \
"s_memrealtime s[96:97]\ns_waitcnt vmcnt(0) lgkmcnt(0)\ns_sub_u32 s96,s96,s94\ns_cmp_gt_u32 s96,0x4c4b40\n" \
"s_cbranch_scc1 .LbaS_%=\nv_cmp_eq_u32 vcc,0,v63\ns_and_b64 vcc,vcc,s[86:87]\ns_cbranch_scc1 .LpaS_%=\n" \
"s_branch .LbaS_%=\n.LsbS_%=:\ns_memrealtime s[94:95]\ns_waitcnt lgkmcnt(0)\n" \
".LpbS_%=:\ns_sleep 2\nbuffer_load_dword v63,v61,s[36:39],s59 offen sc1\ns_memrealtime s[96:97]\n" \
"s_waitcnt vmcnt(0) lgkmcnt(0)\ns_sub_u32 s96,s96,s94\ns_cmp_gt_u32 s96,0x4c4b40\ns_cbranch_scc1 .LbbS_%=\n" \
"v_cmp_eq_u32 vcc,0,v63\ns_and_b64 vcc,vcc,s[86:87]\ns_cbranch_scc1 .LpbS_%=\ns_branch .LbbS_%=\n" \
".Lsp3B_%=:\ns_memrealtime s[94:95]\ns_waitcnt lgkmcnt(0)\n.Lpp3B_%=:\n" \
"s_sub_u32 s101,s78,0\nbuffer_load_dwordx4 v[124:127],v59,s[36:39],s101 offen sc1\ns_memrealtime s[96:97]\ns_waitcnt vmcnt(0) lgkmcnt(0)\n" \
"s_sub_u32 s96,s96,s94\ns_cmp_gt_u32 s96,0x4c4b40\ns_cbranch_scc1 .Lbp3B_%=\nv_cmp_eq_u32 vcc,-1,v124\n" \
"s_cbranch_vccnz .Lpp3B_%=\ns_branch .Lbp3B_%=\n.Lsg4_%=:\ns_memrealtime s[94:95]\n" \
"s_waitcnt lgkmcnt(0)\n.Lpg4_%=:\ns_sleep 2\nbuffer_load_dword v63,v61,s[36:39],s59 offen sc1\n" \
"s_memrealtime s[96:97]\ns_waitcnt vmcnt(0) lgkmcnt(0)\ns_sub_u32 s96,s96,s94\ns_cmp_gt_u32 s96,0x4c4b40\n" \
"s_cbranch_scc1 .Lbg4_%=\nv_cmp_eq_u32 vcc,0,v63\ns_and_b64 vcc,vcc,s[86:87]\ns_cbranch_scc1 .Lpg4_%=\n" \
"s_branch .Lbg4_%=\n.Lse_%=:\ns_memrealtime s[94:95]\ns_waitcnt lgkmcnt(0)\n" \
".Lpe_%=:\nbuffer_load_dword v64,v69,s[36:39],s79 offen sc1\nbuffer_load_dword v65,v69,s[36:39],s80 offen sc1\nbuffer_load_dword v66,v69,s[36:39],s81 offen sc1\n" \
"buffer_load_dword v67,v69,s[36:39],s82 offen sc1\ns_memrealtime s[96:97]\ns_waitcnt vmcnt(0) lgkmcnt(0)\ns_sub_u32 s96,s96,s94\n" \
"s_cmp_gt_u32 s96,0x4c4b40\ns_cbranch_scc1 .Lbe_%=\nv_max3_u32 v52,v64,v65,v66\nv_max_u32 v52,v52,v67\n" \
"v_cmp_eq_u32 vcc,-1,v52\ns_cbranch_vccnz .Lpe_%=\ns_branch .Lbe_%=\n.Lsp1_%=:\n" \
"s_memrealtime s[94:95]\ns_waitcnt lgkmcnt(0)\n.Lpp1_%=:\ns_sub_u32 s101,s71,409600\n" \
"buffer_load_dwordx4 v[64:67],v59,s[36:39],s101 offen sc1\ns_sub_u32 s101,s76,409600\nbuffer_load_dwordx4 v[52:55],v59,s[36:39],s101 offen sc1\ns_memrealtime s[96:97]\n" \
"s_waitcnt vmcnt(0) lgkmcnt(0)\ns_sub_u32 s96,s96,s94\ns_cmp_gt_u32 s96,0x4c4b40\ns_cbranch_scc1 .Lbp1_%=\n" \
"v_max_u32 v62,v64,v52\nv_cmp_eq_u32 vcc,-1,v62\ns_cbranch_vccnz .Lpp1_%=\ns_branch .Lbp1_%=\n" \
".Lsp2A_%=:\ns_memrealtime s[94:95]\ns_waitcnt lgkmcnt(0)\n.Lpp2A_%=:\n" \
"s_sub_u32 s101,s72,409600\nbuffer_load_dwordx4 v[60:63],v59,s[36:39],s101 offen sc1\ns_memrealtime s[96:97]\ns_waitcnt vmcnt(0) lgkmcnt(0)\n" \
"s_sub_u32 s96,s96,s94\ns_cmp_gt_u32 s96,0x4c4b40\ns_cbranch_scc1 .Lbp2A_%=\nv_cmp_eq_u32 vcc,-1,v60\n" \
"s_cbranch_vccnz .Lpp2A_%=\ns_branch .Lbp2A_%=\n.Lsp2B_%=:\ns_memrealtime s[94:95]\n" \
"s_waitcnt lgkmcnt(0)\n.Lpp2B_%=:\ns_sub_u32 s101,s77,409600\nbuffer_load_dwordx4 v[108:111],v59,s[36:39],s101 offen sc1\n" \
"s_memrealtime s[96:97]\ns_waitcnt vmcnt(0) lgkmcnt(0)\ns_sub_u32 s96,s96,s94\ns_cmp_gt_u32 s96,0x4c4b40\n" \
"s_cbranch_scc1 .Lbp2B_%=\nv_cmp_eq_u32 vcc,-1,v108\ns_cbranch_vccnz .Lpp2B_%=\ns_branch .Lbp2B_%=\n" \
".Lsp3A_%=:\ns_memrealtime s[94:95]\ns_waitcnt lgkmcnt(0)\n.Lpp3A_%=:\n" \
"s_sub_u32 s101,s73,0\nbuffer_load_dwordx4 v[112:115],v59,s[36:39],s101 offen sc1\ns_memrealtime s[96:97]\ns_waitcnt vmcnt(0) lgkmcnt(0)\n" \
"s_sub_u32 s96,s96,s94\ns_cmp_gt_u32 s96,0x4c4b40\ns_cbranch_scc1 .Lbp3A_%=\nv_cmp_eq_u32 vcc,-1,v112\n" \
"s_cbranch_vccnz .Lpp3A_%=\ns_branch .Lbp3A_%=\n.Lz0_%=:\ns_branch .Lz1_%=\n" \
".Lxq5_%=:\ns_mov_b64 exec,1\nbuffer_store_dword v61,off,s[36:39],s54 sc1\ns_mov_b64 exec,-1\n" \
"buffer_load_dword v63,v61,s[36:39],s75 offen sc1\ns_mov_b32 s75,s59\ns_add_u32 s54,s54,0x10000\ns_add_u32 s83,s83,819200\n" \
"s_branch .Lyq5_%=\n.Lz1_%=:\ns_branch .Lz2_%=\n.Lxt5_%=:\n" \
"v_and_b32 v20,0x300,v59\nv_and_b32 v21,0xf0,v59\nv_lshlrev_b32 v20,4,v20\ns_mov_b32 m0,162176\n" \
"v_lshl_or_b32 v20,v21,3,v20\nbuffer_load_dword v20,s[40:43],s101 offen lds\ns_mov_b32 m0,s56\ns_branch .Lyt5_%=\n" \
".Lz2_%=:\ns_branch .Lz3_%=\n.Lxq4_%=:\ns_mov_b64 exec,1\n" \
"buffer_store_dword v61,off,s[36:39],s54 sc1\ns_mov_b64 exec,-1\nbuffer_load_dword v63,v61,s[36:39],s75 offen sc1\ns_mov_b32 s75,s59\n" \
"s_add_u32 s54,s54,0x10000\ns_add_u32 s83,s83,819200\ns_branch .Lyq4_%=\n.Lz3_%=:\n" \
"s_branch .Lz4_%=\n.Lxt4_%=:\nv_and_b32 v0,0x300,v59\nv_and_b32 v1,0xf0,v59\n" \
"v_lshlrev_b32 v0,4,v0\ns_mov_b32 m0,162176\nv_lshl_or_b32 v0,v1,3,v0\nbuffer_load_dword v0,s[40:43],s101 offen lds\n" \
"s_mov_b32 m0,s56\ns_branch .Lyt4_%=\n.Lz4_%=:\ns_branch .Lzl_%=\n" \
".LsmP_%=:\ns_sleep 2\nbuffer_load_dwordx4 v[48:51],v2,s[36:39],s73 offen sc1\nbuffer_load_dwordx4 v[60:63],v2,s[36:39],s75 offen sc1\n" \
"buffer_load_dwordx4 v[52:55],v2,s[36:39],s74 offen sc1\nbuffer_load_dwordx4 v[64:67],v2,s[36:39],s76 offen sc1\nbuffer_load_dwordx4 v[56:59],v1,s[36:39],s77 offen sc1\nbuffer_load_dwordx4 v[68:71],v1,s[36:39],s78 offen sc1\n" \
"s_memrealtime s[96:97]\ns_waitcnt vmcnt(0) lgkmcnt(0)\ns_sub_u32 s96,s96,s30\ns_cmp_gt_u32 s96,0x4c4b40\n" \
"s_cbranch_scc1 .LbmP_%=\nv_cmp_ne_u64 vcc,s[28:29],v[48:49]\nv_cmp_ne_u64 s[84:85],s[28:29],v[52:53]\nv_cmp_ne_u64 s[82:83],s[28:29],v[56:57]\n" \
"s_or_b64 vcc,vcc,s[84:85]\nv_cmp_ne_u64 s[84:85],s[28:29],v[60:61]\ns_or_b64 vcc,vcc,s[82:83]\nv_cmp_ne_u64 s[82:83],s[28:29],v[64:65]\n" \
"s_or_b64 vcc,vcc,s[84:85]\nv_cmp_ne_u64 s[84:85],s[28:29],v[68:69]\ns_or_b64 vcc,vcc,s[82:83]\ns_or_b64 vcc,vcc,s[84:85]\n" \
"s_cbranch_vccnz .LsmP_%=\ns_branch .LbmP_%=\n.LsxP_%=:\ns_waitcnt lgkmcnt(0)\n" \
"v_mul_f32 v74,0.5,v74\nv_pk_add_f32 v[124:125],v[54:55],v[74:75] op_sel_hi:[1,0]\nv_pk_add_f32 v[126:127],v[58:59],1.0 op_sel_hi:[1,0]\nv_max3_f32 v112,|v50|,|v51|,|v124|\n" \
"v_max3_f32 v112,|v125|,|v126|,v112\nv_max_f32 v112,|v127|,v112\ns_nop 1\nv_max_u32_dpp v112,v112,v112 quad_perm:[1,0,3,2] row_mask:0xf bank_mask:0xf bound_ctrl:1\n" \
"s_nop 1\nv_max_u32_dpp v112,v112,v112 quad_perm:[2,3,0,1] row_mask:0xf bank_mask:0xf bound_ctrl:1\ns_nop 1\nv_max_u32_dpp v112,v112,v112 row_half_mirror row_mask:0xf bank_mask:0xf bound_ctrl:1\n" \
"s_nop 1\nv_max_u32_dpp v112,v112,v112 row_mirror row_mask:0xf bank_mask:0xf bound_ctrl:1\ns_nop 0\nv_readlane_b32 s82,v112,0\n" \
"v_readlane_b32 s100,v112,16\nv_readlane_b32 s101,v112,32\nv_readlane_b32 s31,v112,48\ns_max_u32 s100,s100,s101\n" \
"s_max_u32 s82,s82,s31\ns_max_u32 s82,s82,s100\nv_mov_b32 v113,s82\nds_write_b32 v251,v113 offset:16\n" \
"s_waitcnt lgkmcnt(0)\ns_barrier\nds_read_b128 v[124:127],v114 offset:16\ns_waitcnt lgkmcnt(0)\n" \
"v_max3_u32 v124,v124,v125,v126\nv_max_u32 v124,v124,v127\ns_nop 0\nv_readfirstlane_b32 s93,v124\n" \
"s_nop 0\ns_lshr_b32 s93,s93,23\ns_max_u32 s93,s93,64\ns_min_u32 s93,s93,254\n" \
"s_sub_u32 s82,256,s93\ns_lshl_b32 s82,s82,23\nv_mov_b32 v124,s82\nv_mul_f32 v74,v74,v124\n" \
"v_pk_mul_f32 v[62:63],v[50:51],v[124:125] op_sel_hi:[1,0]\nv_max3_f32 v112,|v62|,|v63|,0\nv_cvt_pk_f16_f32 v62,v62,v63\nds_write_b32 v252,v62 offset:62848\n" \
"v_pk_fma_f32 v[66:67],v[54:55],v[124:125],v[74:75] op_sel_hi:[1,0,0]\nv_max3_f32 v112,|v66|,|v67|,v112\nv_cvt_pk_f16_f32 v66,v66,v67\nds_write_b32 v253,v66 offset:62848\n" \
"v_pk_fma_f32 v[70:71],v[58:59],v[124:125],v[124:125] op_sel_hi:[1,0,0]\nv_max3_f32 v112,|v70|,|v71|,v112\nv_cvt_pk_f16_f32 v70,v70,v71\nds_write_b32 v254,v70 offset:62848\n" \
"s_waitcnt lgkmcnt(0)\ns_barrier\nds_read_b64 v[96:97],v234 offset:62848\nds_read_b64 v[98:99],v235 offset:62848\n" \
"ds_read_b64 v[100:101],v236 offset:62848\nds_read_b64 v[102:103],v237 offset:62848\nds_read_b64 v[104:105],v238 offset:62848\nds_read_b64 v[106:107],v239 offset:62848\n" \
"ds_read_b64 v[108:109],v240 offset:62848\nds_read_b64 v[110:111],v241 offset:62848\ns_waitcnt lgkmcnt(0)\ns_branch .LbxP_%=\n" \
".LsrP_%=:\ns_sleep 2\nbuffer_load_dwordx4 v[104:107],v1,s[36:39],s7 offen sc1\nbuffer_load_dwordx4 v[108:111],v1,s[36:39],s7 offen offset:1024 sc1\n" \
"s_memrealtime s[96:97]\ns_waitcnt vmcnt(0) lgkmcnt(0)\ns_sub_u32 s96,s96,s30\ns_cmp_gt_u32 s96,0x4c4b40\n" \
"s_cbranch_scc1 .LbrP_%=\nv_xor_b32 v112,s29,v105\nv_xor_b32 v113,s29,v107\nv_xor_b32 v114,s29,v109\n" \
"v_xor_b32 v115,s29,v111\nv_or3_b32 v112,v112,v113,v114\nv_xor_b32 v113,s28,v104\nv_xor_b32 v114,s28,v106\n" \
"v_or_b32 v112,v112,v115\nv_xor_b32 v115,s28,v108\nv_lshl_or_b32 v112,v112,8,v113\nv_xor_b32 v113,s28,v110\n" \
"v_or3_b32 v112,v112,v114,v115\nv_or_b32 v112,v112,v113\nv_cmp_ne_u32 vcc,0,v112\ns_nop 0\n" \
"s_cbranch_vccnz .LsrP_%=\ns_branch .LbrP_%=\n.LsnP_%=:\ns_sleep 2\n" \
"buffer_load_dwordx4 v[80:83],v120,s[36:39],s5 offen sc1\nbuffer_load_dwordx4 v[84:87],v120,s[36:39],s5 offen offset:2304 sc1\nbuffer_load_dwordx4 v[88:91],v120,s[36:39],s6 offen sc1\nbuffer_load_dwordx4 v[92:95],v120,s[36:39],s6 offen offset:2304 sc1\n" \
"s_memrealtime s[96:97]\ns_waitcnt vmcnt(0) lgkmcnt(0)\ns_sub_u32 s96,s96,s30\ns_cmp_gt_u32 s96,0x4c4b40\n" \
"s_cbranch_scc1 .LbnP_%=\nv_cmp_ne_u64 vcc,s[28:29],v[80:81]\nv_cmp_ne_u64 s[24:25],s[28:29],v[84:85]\nv_cmp_ne_u64 s[26:27],s[28:29],v[88:89]\n" \
"s_or_b64 vcc,vcc,s[24:25]\nv_cmp_ne_u64 s[24:25],s[28:29],v[92:93]\ns_or_b64 vcc,vcc,s[26:27]\ns_or_b64 vcc,vcc,s[24:25]\n" \
"s_cbranch_vccnz .LsnP_%=\ns_branch .LbnP_%=\n.LsuP_%=:\nv_and_b32 v125,1,v112\n" \
"v_cmpx_eq_u32 vcc,0,v125\nbuffer_load_dwordx4 v[112:115],v3,s[36:39],s16 offen sc1\ns_mov_b64 exec,-1\nv_and_b32 v125,1,v116\n" \
"v_cmpx_eq_u32 vcc,0,v125\nbuffer_load_dwordx4 v[116:119],v3,s[36:39],s17 offen sc1\ns_mov_b64 exec,-1\nv_and_b32 v125,1,v120\n" \
"v_cmpx_eq_u32 vcc,0,v125\nbuffer_load_dwordx4 v[120:123],v124,s[36:39],s18 offen sc1\ns_mov_b64 exec,-1\ns_memrealtime s[96:97]\n" \
"s_waitcnt vmcnt(0) lgkmcnt(0)\ns_sub_u32 s96,s96,s30\ns_cmp_gt_u32 s96,0x4c4b40\ns_cbranch_scc1 .LbuP_%=\n" \
"v_and_b32 v125,v112,v116\nv_bitop3_b32 v125,v125,v120,1 bitop3:0x80\nv_cmp_eq_u32 vcc,0,v125\ns_nop 0\n" \
"s_cbranch_vccnz .LsuP_%=\ns_branch .LbuP_%=\n.LsmN_%=:\ns_sleep 2\n" \
"buffer_load_dwordx4 v[48:51],v2,s[36:39],s73 offen sc1\nbuffer_load_dwordx4 v[60:63],v2,s[36:39],s75 offen sc1\nbuffer_load_dwordx4 v[52:55],v2,s[36:39],s74 offen sc1\nbuffer_load_dwordx4 v[64:67],v2,s[36:39],s76 offen sc1\n" \
"buffer_load_dwordx4 v[56:59],v1,s[36:39],s77 offen sc1\nbuffer_load_dwordx4 v[68:71],v1,s[36:39],s78 offen sc1\ns_memrealtime s[96:97]\ns_waitcnt vmcnt(0) lgkmcnt(0)\n" \
"s_sub_u32 s96,s96,s30\ns_cmp_gt_u32 s96,0x4c4b40\ns_cbranch_scc1 .LbmN_%=\nv_cmp_ne_u64 vcc,s[28:29],v[48:49]\n" \
"v_cmp_ne_u64 s[84:85],s[28:29],v[52:53]\nv_cmp_ne_u64 s[82:83],s[28:29],v[56:57]\ns_or_b64 vcc,vcc,s[84:85]\nv_cmp_ne_u64 s[84:85],s[28:29],v[60:61]\n" \
"s_or_b64 vcc,vcc,s[82:83]\nv_cmp_ne_u64 s[82:83],s[28:29],v[64:65]\ns_or_b64 vcc,vcc,s[84:85]\nv_cmp_ne_u64 s[84:85],s[28:29],v[68:69]\n" \
"s_or_b64 vcc,vcc,s[82:83]\ns_or_b64 vcc,vcc,s[84:85]\ns_cbranch_vccnz .LsmN_%=\ns_branch .LbmN_%=\n" \
".LsxN_%=:\ns_waitcnt lgkmcnt(0)\nv_mul_f32 v74,0.5,v74\nv_pk_add_f32 v[124:125],v[54:55],v[74:75] op_sel_hi:[1,0]\n" \
"v_pk_add_f32 v[126:127],v[58:59],1.0 op_sel_hi:[1,0]\nv_max3_f32 v112,|v50|,|v51|,|v124|\nv_max3_f32 v112,|v125|,|v126|,v112\nv_max_f32 v112,|v127|,v112\n" \
"s_nop 1\nv_max_u32_dpp v112,v112,v112 quad_perm:[1,0,3,2] row_mask:0xf bank_mask:0xf bound_ctrl:1\ns_nop 1\nv_max_u32_dpp v112,v112,v112 quad_perm:[2,3,0,1] row_mask:0xf bank_mask:0xf bound_ctrl:1\n" \
"s_nop 1\nv_max_u32_dpp v112,v112,v112 row_half_mirror row_mask:0xf bank_mask:0xf bound_ctrl:1\ns_nop 1\nv_max_u32_dpp v112,v112,v112 row_mirror row_mask:0xf bank_mask:0xf bound_ctrl:1\n" \
"s_nop 0\nv_readlane_b32 s82,v112,0\nv_readlane_b32 s100,v112,16\nv_readlane_b32 s101,v112,32\n" \
"v_readlane_b32 s31,v112,48\ns_max_u32 s100,s100,s101\ns_max_u32 s82,s82,s31\ns_max_u32 s82,s82,s100\n" \
"v_mov_b32 v113,s82\nds_write_b32 v251,v113 offset:16\ns_waitcnt lgkmcnt(0)\ns_barrier\n" \
"ds_read_b128 v[124:127],v114 offset:16\ns_waitcnt lgkmcnt(0)\nv_max3_u32 v124,v124,v125,v126\nv_max_u32 v124,v124,v127\n" \
"s_nop 0\nv_readfirstlane_b32 s93,v124\ns_nop 0\ns_lshr_b32 s93,s93,23\n" \
"s_max_u32 s93,s93,64\ns_min_u32 s93,s93,254\ns_sub_u32 s82,256,s93\ns_lshl_b32 s82,s82,23\n" \
"v_mov_b32 v124,s82\nv_mul_f32 v74,v74,v124\nv_pk_mul_f32 v[62:63],v[50:51],v[124:125] op_sel_hi:[1,0]\nv_max3_f32 v112,|v62|,|v63|,0\n" \
"v_cvt_pk_f16_f32 v62,v62,v63\nds_write_b32 v252,v62 offset:62848\nv_pk_fma_f32 v[66:67],v[54:55],v[124:125],v[74:75] op_sel_hi:[1,0,0]\nv_max3_f32 v112,|v66|,|v67|,v112\n" \
"v_cvt_pk_f16_f32 v66,v66,v67\nds_write_b32 v253,v66 offset:62848\nv_pk_fma_f32 v[70:71],v[58:59],v[124:125],v[124:125] op_sel_hi:[1,0,0]\nv_max3_f32 v112,|v70|,|v71|,v112\n" \
"v_cvt_pk_f16_f32 v70,v70,v71\nds_write_b32 v254,v70 offset:62848\ns_waitcnt lgkmcnt(0)\ns_barrier\n" \
"ds_read_b64 v[96:97],v234 offset:62848\nds_read_b64 v[98:99],v235 offset:62848\nds_read_b64 v[100:101],v236 offset:62848\nds_read_b64 v[102:103],v237 offset:62848\n" \
"ds_read_b64 v[104:105],v238 offset:62848\nds_read_b64 v[106:107],v239 offset:62848\nds_read_b64 v[108:109],v240 offset:62848\nds_read_b64 v[110:111],v241 offset:62848\n" \
"s_waitcnt lgkmcnt(0)\ns_branch .LbxN_%=\n.LsrN_%=:\ns_sleep 2\n" \
"buffer_load_dwordx4 v[104:107],v1,s[36:39],s7 offen sc1\nbuffer_load_dwordx4 v[108:111],v1,s[36:39],s7 offen offset:1024 sc1\ns_memrealtime s[96:97]\ns_waitcnt vmcnt(0) lgkmcnt(0)\n" \
"s_sub_u32 s96,s96,s30\ns_cmp_gt_u32 s96,0x4c4b40\ns_cbranch_scc1 .LbrN_%=\nv_xor_b32 v112,s29,v105\n" \
"v_xor_b32 v113,s29,v107\nv_xor_b32 v114,s29,v109\nv_xor_b32 v115,s29,v111\nv_or3_b32 v112,v112,v113,v114\n" \
"v_xor_b32 v113,s28,v104\nv_xor_b32 v114,s28,v106\nv_or_b32 v112,v112,v115\nv_xor_b32 v115,s28,v108\n" \
"v_lshl_or_b32 v112,v112,8,v113\nv_xor_b32 v113,s28,v110\nv_or3_b32 v112,v112,v114,v115\nv_or_b32 v112,v112,v113\n" \
"v_cmp_ne_u32 vcc,0,v112\ns_nop 0\ns_cbranch_vccnz .LsrN_%=\ns_branch .LbrN_%=\n" \
".LsnN_%=:\ns_sleep 2\nbuffer_load_dwordx4 v[80:83],v120,s[36:39],s5 offen sc1\nbuffer_load_dwordx4 v[84:87],v120,s[36:39],s5 offen offset:2304 sc1\n" \
"buffer_load_dwordx4 v[88:91],v120,s[36:39],s6 offen sc1\nbuffer_load_dwordx4 v[92:95],v120,s[36:39],s6 offen offset:2304 sc1\ns_memrealtime s[96:97]\ns_waitcnt vmcnt(0) lgkmcnt(0)\n" \
"s_sub_u32 s96,s96,s30\ns_cmp_gt_u32 s96,0x4c4b40\ns_cbranch_scc1 .LbnN_%=\nv_cmp_ne_u64 vcc,s[28:29],v[80:81]\n" \
"v_cmp_ne_u64 s[24:25],s[28:29],v[84:85]\nv_cmp_ne_u64 s[26:27],s[28:29],v[88:89]\ns_or_b64 vcc,vcc,s[24:25]\nv_cmp_ne_u64 s[24:25],s[28:29],v[92:93]\n" \
"s_or_b64 vcc,vcc,s[26:27]\ns_or_b64 vcc,vcc,s[24:25]\ns_cbranch_vccnz .LsnN_%=\ns_branch .LbnN_%=\n" \
".LsuN_%=:\nv_and_b32 v125,1,v112\nv_cmpx_eq_u32 vcc,0,v125\nbuffer_load_dwordx4 v[112:115],v3,s[36:39],s16 offen sc1\n" \
"s_mov_b64 exec,-1\nv_and_b32 v125,1,v116\nv_cmpx_eq_u32 vcc,0,v125\nbuffer_load_dwordx4 v[116:119],v3,s[36:39],s17 offen sc1\n" \
"s_mov_b64 exec,-1\nv_and_b32 v125,1,v120\nv_cmpx_eq_u32 vcc,0,v125\nbuffer_load_dwordx4 v[120:123],v124,s[36:39],s18 offen sc1\n" \
"s_mov_b64 exec,-1\ns_memrealtime s[96:97]\ns_waitcnt vmcnt(0) lgkmcnt(0)\ns_sub_u32 s96,s96,s30\n" \
"s_cmp_gt_u32 s96,0x4c4b40\ns_cbranch_scc1 .LbuN_%=\nv_and_b32 v125,v112,v116\nv_bitop3_b32 v125,v125,v120,1 bitop3:0x80\n" \
"v_cmp_eq_u32 vcc,0,v125\ns_nop 0\ns_cbranch_vccnz .LsuN_%=\ns_branch .LbuN_%=\n" \
".LsmC_%=:\ns_sleep 2\nbuffer_load_dwordx4 v[48:51],v2,s[36:39],s73 offen sc1\nbuffer_load_dwordx4 v[60:63],v2,s[36:39],s75 offen sc1\n" \
"buffer_load_dwordx4 v[52:55],v2,s[36:39],s74 offen sc1\nbuffer_load_dwordx4 v[64:67],v2,s[36:39],s76 offen sc1\nbuffer_load_dwordx4 v[56:59],v1,s[36:39],s77 offen sc1\nbuffer_load_dwordx4 v[68:71],v1,s[36:39],s78 offen sc1\n" \
"s_memrealtime s[96:97]\ns_waitcnt vmcnt(0) lgkmcnt(0)\ns_sub_u32 s96,s96,s30\ns_cmp_gt_u32 s96,0x4c4b40\n" \
"s_cbranch_scc1 .LbmC_%=\nv_cmp_ne_u64 vcc,s[28:29],v[48:49]\nv_cmp_ne_u64 s[84:85],s[28:29],v[52:53]\nv_cmp_ne_u64 s[82:83],s[28:29],v[56:57]\n" \
"s_or_b64 vcc,vcc,s[84:85]\nv_cmp_ne_u64 s[84:85],s[28:29],v[60:61]\ns_or_b64 vcc,vcc,s[82:83]\nv_cmp_ne_u64 s[82:83],s[28:29],v[64:65]\n" \
"s_or_b64 vcc,vcc,s[84:85]\nv_cmp_ne_u64 s[84:85],s[28:29],v[68:69]\ns_or_b64 vcc,vcc,s[82:83]\ns_or_b64 vcc,vcc,s[84:85]\n" \
"s_cbranch_vccnz .LsmC_%=\ns_branch .LbmC_%=\n.LsxC_%=:\ns_waitcnt lgkmcnt(0)\n" \
"v_mul_f32 v74,0.5,v74\nv_pk_add_f32 v[124:125],v[54:55],v[74:75] op_sel_hi:[1,0]\nv_pk_add_f32 v[126:127],v[58:59],1.0 op_sel_hi:[1,0]\nv_max3_f32 v112,|v50|,|v51|,|v124|\n" \
"v_max3_f32 v112,|v125|,|v126|,v112\nv_max_f32 v112,|v127|,v112\ns_nop 1\nv_max_u32_dpp v112,v112,v112 quad_perm:[1,0,3,2] row_mask:0xf bank_mask:0xf bound_ctrl:1\n" \
"s_nop 1\nv_max_u32_dpp v112,v112,v112 quad_perm:[2,3,0,1] row_mask:0xf bank_mask:0xf bound_ctrl:1\ns_nop 1\nv_max_u32_dpp v112,v112,v112 row_half_mirror row_mask:0xf bank_mask:0xf bound_ctrl:1\n" \
"s_nop 1\nv_max_u32_dpp v112,v112,v112 row_mirror row_mask:0xf bank_mask:0xf bound_ctrl:1\ns_nop 0\nv_readlane_b32 s82,v112,0\n" \
"v_readlane_b32 s100,v112,16\nv_readlane_b32 s101,v112,32\nv_readlane_b32 s31,v112,48\ns_max_u32 s100,s100,s101\n" \
"s_max_u32 s82,s82,s31\ns_max_u32 s82,s82,s100\nv_mov_b32 v113,s82\nds_write_b32 v251,v113 offset:16\n" \
"s_waitcnt lgkmcnt(0)\ns_barrier\nds_read_b128 v[124:127],v114 offset:16\ns_waitcnt lgkmcnt(0)\n" \
"v_max3_u32 v124,v124,v125,v126\nv_max_u32 v124,v124,v127\ns_nop 0\nv_readfirstlane_b32 s93,v124\n" \
"s_nop 0\ns_lshr_b32 s93,s93,23\ns_max_u32 s93,s93,64\ns_min_u32 s93,s93,254\n" \
"s_sub_u32 s82,256,s93\ns_lshl_b32 s82,s82,23\nv_mov_b32 v124,s82\nv_mul_f32 v74,v74,v124\n" \
"v_pk_mul_f32 v[62:63],v[50:51],v[124:125] op_sel_hi:[1,0]\nv_max3_f32 v112,|v62|,|v63|,0\nv_cvt_pk_f16_f32 v62,v62,v63\nds_write_b32 v252,v62 offset:62848\n" \
"v_pk_fma_f32 v[66:67],v[54:55],v[124:125],v[74:75] op_sel_hi:[1,0,0]\nv_max3_f32 v112,|v66|,|v67|,v112\nv_cvt_pk_f16_f32 v66,v66,v67\nds_write_b32 v253,v66 offset:62848\n" \
"v_pk_fma_f32 v[70:71],v[58:59],v[124:125],v[124:125] op_sel_hi:[1,0,0]\nv_max3_f32 v112,|v70|,|v71|,v112\nv_cvt_pk_f16_f32 v70,v70,v71\nds_write_b32 v254,v70 offset:62848\n" \
"s_waitcnt lgkmcnt(0)\ns_barrier\nds_read_b64 v[96:97],v234 offset:62848\nds_read_b64 v[98:99],v235 offset:62848\n" \
"ds_read_b64 v[100:101],v236 offset:62848\nds_read_b64 v[102:103],v237 offset:62848\nds_read_b64 v[104:105],v238 offset:62848\nds_read_b64 v[106:107],v239 offset:62848\n" \
"ds_read_b64 v[108:109],v240 offset:62848\nds_read_b64 v[110:111],v241 offset:62848\ns_waitcnt lgkmcnt(0)\ns_branch .LbxC_%=\n" \
".Lsw_%=:\nbuffer_store_dwordx4 v[80:83],v3,s[36:39],s70 offen sc1\nbuffer_store_dwordx4 v[84:87],v3,s[36:39],s71 offen sc1\nbuffer_store_dwordx4 v[88:91],v115,s[36:39],s72 offen sc1\n" \
".Lpw_%=:\nbuffer_load_dwordx4 v[104:107],v1,s[36:39],s7 offen sc1\nbuffer_load_dwordx4 v[108:111],v1,s[36:39],s7 offen offset:1024 sc1\ns_memrealtime s[96:97]\n" \
"s_waitcnt vmcnt(0) lgkmcnt(0)\ns_sub_u32 s96,s96,s30\ns_cmp_gt_u32 s96,0x4c4b40\ns_cbranch_scc1 .Lqw_%=\n" \
"v_xor_b32 v116,s29,v105\nv_xor_b32 v117,s29,v107\nv_xor_b32 v118,s29,v109\nv_xor_b32 v119,s29,v111\n" \
"v_or3_b32 v116,v116,v117,v118\nv_xor_b32 v117,s28,v104\nv_xor_b32 v118,s28,v106\nv_or_b32 v116,v116,v119\n" \
"v_xor_b32 v119,s28,v108\nv_lshl_or_b32 v116,v116,8,v117\nv_xor_b32 v117,s28,v110\nv_or3_b32 v116,v116,v118,v119\n" \
"v_or_b32 v116,v116,v117\nv_cmp_ne_u32 vcc,0,v116\ns_nop 0\ns_cbranch_vccnz .Lpw_%=\n" \
".Lqw_%=:\ns_add_u32 m0,s88,16384\ns_nop 0\nbuffer_load_dwordx4 v124,s[36:39],s66 offen sc1 lds\n" \
"s_add_u32 m0,s88,20480\ns_nop 0\nbuffer_load_dwordx4 v125,s[36:39],s67 offen sc1 lds\ns_add_u32 m0,s88,24576\n" \
"s_nop 0\nbuffer_load_dwordx4 v124,s[36:39],s68 offen sc1 lds\ns_add_u32 m0,s88,28672\ns_nop 0\n" \
"buffer_load_dwordx4 v125,s[36:39],s69 offen sc1 lds\ns_waitcnt vmcnt(0)\ns_branch .Lbw_%=\n.Lsz_%=:\n" \
"v_subrev_u32 v124,16384,v80\nv_lshrrev_b32 v124,6,v124\nv_mul_u32_u24 v125,9344,v124\nv_lshl_add_u32 v124,v84,2,v125\n" \
"v_sub_u32 v125,2382780,v124\ns_mov_b32 s79,2925632\nbuffer_load_dword v64,v124,s[36:39],s79 offen sc1\nv_subrev_u32 v126,0,v125\n" \
"s_mov_b32 s79,2925632\nbuffer_load_dword v72,v126,s[36:39],s79 offen sc1\ns_mov_b32 s79,3075136\nbuffer_load_dword v65,v124,s[36:39],s79 offen sc1\n" \
"v_subrev_u32 v126,149504,v125\ns_mov_b32 s79,2925632\nbuffer_load_dword v73,v126,s[36:39],s79 offen sc1\ns_mov_b32 s79,3224640\n" \
"buffer_load_dword v66,v124,s[36:39],s79 offen sc1\nv_subrev_u32 v126,299008,v125\ns_mov_b32 s79,2925632\nbuffer_load_dword v74,v126,s[36:39],s79 offen sc1\n" \
"s_mov_b32 s79,3374144\nbuffer_load_dword v67,v124,s[36:39],s79 offen sc1\nv_subrev_u32 v126,448512,v125\ns_mov_b32 s79,2925632\n" \
"buffer_load_dword v75,v126,s[36:39],s79 offen sc1\ns_mov_b32 s79,2944320\nbuffer_load_dword v68,v124,s[36:39],s79 offen sc1\nv_subrev_u32 v126,18688,v125\n" \
"s_mov_b32 s79,2925632\nbuffer_load_dword v76,v126,s[36:39],s79 offen sc1\ns_mov_b32 s79,3093824\nbuffer_load_dword v69,v124,s[36:39],s79 offen sc1\n" \
"v_subrev_u32 v126,168192,v125\ns_mov_b32 s79,2925632\nbuffer_load_dword v77,v126,s[36:39],s79 offen sc1\ns_mov_b32 s79,3243328\n" \
"buffer_load_dword v70,v124,s[36:39],s79 offen sc1\nv_subrev_u32 v126,317696,v125\ns_mov_b32 s79,2925632\nbuffer_load_dword v78,v126,s[36:39],s79 offen sc1\n" \
"s_mov_b32 s79,3392832\nbuffer_load_dword v71,v124,s[36:39],s79 offen sc1\nv_subrev_u32 v126,467200,v125\ns_mov_b32 s79,2925632\n" \
"buffer_load_dword v79,v126,s[36:39],s79 offen sc1\ns_waitcnt vmcnt(0)\nv_lshlrev_b32 v124,16,v72\nv_and_b32 v125,0xffff0000,v72\n" \
"v_add_f32 v16,v16,v124\nv_sub_f32 v20,v20,v125\nv_lshlrev_b32 v124,16,v64\nv_and_b32 v125,0xffff0000,v64\n" \
"v_add_f32 v32,v32,v124\nv_sub_f32 v36,v36,v125\nv_lshlrev_b32 v124,16,v73\nv_and_b32 v125,0xffff0000,v73\n" \
"v_add_f32 v17,v17,v124\nv_sub_f32 v21,v21,v125\nv_lshlrev_b32 v124,16,v65\nv_and_b32 v125,0xffff0000,v65\n" \
"v_add_f32 v33,v33,v124\nv_sub_f32 v37,v37,v125\nv_lshlrev_b32 v124,16,v74\nv_and_b32 v125,0xffff0000,v74\n" \
"v_add_f32 v18,v18,v124\nv_sub_f32 v22,v22,v125\nv_lshlrev_b32 v124,16,v66\nv_and_b32 v125,0xffff0000,v66\n" \
"v_add_f32 v34,v34,v124\nv_sub_f32 v38,v38,v125\nv_lshlrev_b32 v124,16,v75\nv_and_b32 v125,0xffff0000,v75\n" \
"v_add_f32 v19,v19,v124\nv_sub_f32 v23,v23,v125\nv_lshlrev_b32 v124,16,v67\nv_and_b32 v125,0xffff0000,v67\n" \
"v_add_f32 v35,v35,v124\nv_sub_f32 v39,v39,v125\nv_lshlrev_b32 v124,16,v76\nv_and_b32 v125,0xffff0000,v76\n" \
"v_add_f32 v40,v40,v124\nv_sub_f32 v44,v44,v125\nv_lshlrev_b32 v124,16,v68\nv_and_b32 v125,0xffff0000,v68\n" \
"v_add_f32 v56,v56,v124\nv_sub_f32 v60,v60,v125\nv_lshlrev_b32 v124,16,v77\nv_and_b32 v125,0xffff0000,v77\n" \
"v_add_f32 v41,v41,v124\nv_sub_f32 v45,v45,v125\nv_lshlrev_b32 v124,16,v69\nv_and_b32 v125,0xffff0000,v69\n" \
"v_add_f32 v57,v57,v124\nv_sub_f32 v61,v61,v125\nv_lshlrev_b32 v124,16,v78\nv_and_b32 v125,0xffff0000,v78\n" \
"v_add_f32 v42,v42,v124\nv_sub_f32 v46,v46,v125\nv_lshlrev_b32 v124,16,v70\nv_and_b32 v125,0xffff0000,v70\n" \
"v_add_f32 v58,v58,v124\nv_sub_f32 v62,v62,v125\nv_lshlrev_b32 v124,16,v79\nv_and_b32 v125,0xffff0000,v79\n" \
"v_add_f32 v43,v43,v124\nv_sub_f32 v47,v47,v125\nv_lshlrev_b32 v124,16,v71\nv_and_b32 v125,0xffff0000,v71\n" \
"v_add_f32 v59,v59,v124\nv_sub_f32 v63,v63,v125\ns_branch .Lbz_%=\n.Lst_%=:\n" \
"s_sleep 1\nbuffer_load_dword v96,v120,s[36:39],s80 offen sc1\nbuffer_load_dword v97,v121,s[36:39],s80 offen sc1\nbuffer_load_dword v98,v122,s[36:39],s80 offen sc1\n" \
"buffer_load_dword v99,v123,s[36:39],s80 offen sc1\nbuffer_load_dword v100,v120,s[36:39],s81 offen sc1\nbuffer_load_dword v101,v121,s[36:39],s81 offen sc1\nbuffer_load_dword v102,v122,s[36:39],s81 offen sc1\n" \
"buffer_load_dword v103,v123,s[36:39],s81 offen sc1\nbuffer_load_dword v104,v120,s[36:39],s82 offen sc1\nbuffer_load_dword v105,v121,s[36:39],s82 offen sc1\nbuffer_load_dword v106,v122,s[36:39],s82 offen sc1\n" \
"buffer_load_dword v107,v123,s[36:39],s82 offen sc1\nbuffer_load_dword v108,v120,s[36:39],s83 offen sc1\nbuffer_load_dword v109,v121,s[36:39],s83 offen sc1\nbuffer_load_dword v110,v122,s[36:39],s83 offen sc1\n" \
"buffer_load_dword v111,v123,s[36:39],s83 offen sc1\ns_memrealtime s[96:97]\ns_waitcnt vmcnt(0) lgkmcnt(0)\ns_sub_u32 s96,s96,s30\n" \
"s_cmp_gt_u32 s96,0x4c4b40\ns_cbranch_scc1 .Lbt_%=\nv_mfma_f32_16x16x32_f16 v[64:67],v[128:131],v[96:99],0\nv_or3_b32 v124,v96,v97,v98\n" \
"v_or3_b32 v124,v124,v99,v100\nv_mfma_f32_16x16x32_f16 v[68:71],v[132:135],v[96:99],0\nv_or3_b32 v124,v124,v101,v102\nv_mfma_f32_16x16x32_f16 v[72:75],v[136:139],v[100:103],0\n" \
"v_or3_b32 v124,v124,v103,v104\nv_mfma_f32_16x16x32_f16 v[76:79],v[140:143],v[100:103],0\nv_or3_b32 v124,v124,v105,v106\nv_mfma_f32_16x16x32_f16 v[80:83],v[144:147],v[104:107],0\n" \
"v_or3_b32 v124,v124,v107,v108\nv_mfma_f32_16x16x32_f16 v[84:87],v[148:151],v[104:107],0\nv_or3_b32 v124,v124,v109,v110\nv_mfma_f32_16x16x32_f16 v[88:91],v[152:155],v[108:111],0\n" \
"v_or_b32 v124,v124,v111\nv_mfma_f32_16x16x32_f16 v[92:95],v[156:159],v[108:111],0\nv_and_b32 v124,1,v124\nv_cmp_ne_u32 vcc,0,v124\n" \
"s_nop 0\ns_and_b64 vcc,vcc,s[78:79]\ns_cbranch_vccnz .Lst_%=\ns_branch .Lbt_%=\n" \
".LsnC_%=:\ns_sleep 2\nbuffer_load_dwordx4 v[16:19],v32,s[36:39],s5 offen sc1\nbuffer_load_dwordx4 v[20:23],v32,s[36:39],s5 offen offset:2304 sc1\n" \
"buffer_load_dwordx4 v[24:27],v32,s[36:39],s6 offen sc1\nbuffer_load_dwordx4 v[28:31],v32,s[36:39],s6 offen offset:2304 sc1\ns_memrealtime s[96:97]\ns_waitcnt vmcnt(0) lgkmcnt(0)\n" \
"s_sub_u32 s96,s96,s30\ns_cmp_gt_u32 s96,0x4c4b40\ns_cbranch_scc1 .LbnC_%=\nv_cmp_ne_u64 vcc,s[28:29],v[16:17]\n" \
"v_cmp_ne_u64 s[24:25],s[28:29],v[20:21]\nv_cmp_ne_u64 s[26:27],s[28:29],v[24:25]\ns_or_b64 vcc,vcc,s[24:25]\nv_cmp_ne_u64 s[24:25],s[28:29],v[28:29]\n" \
"s_or_b64 vcc,vcc,s[26:27]\ns_or_b64 vcc,vcc,s[24:25]\ns_cbranch_vccnz .LsnC_%=\ns_branch .LbnC_%=\n" \
".LsuC_%=:\nv_and_b32 v125,1,v112\nv_cmpx_eq_u32 vcc,0,v125\nbuffer_load_dwordx4 v[112:115],v3,s[36:39],s16 offen sc1\n" \
"s_mov_b64 exec,-1\nv_and_b32 v125,1,v116\nv_cmpx_eq_u32 vcc,0,v125\nbuffer_load_dwordx4 v[116:119],v3,s[36:39],s17 offen sc1\n" \
"s_mov_b64 exec,-1\nv_and_b32 v125,1,v120\nv_cmpx_eq_u32 vcc,0,v125\nbuffer_load_dwordx4 v[120:123],v124,s[36:39],s18 offen sc1\n" \
"s_mov_b64 exec,-1\ns_memrealtime s[96:97]\ns_waitcnt vmcnt(0) lgkmcnt(0)\ns_sub_u32 s96,s96,s30\n" \
"s_cmp_gt_u32 s96,0x4c4b40\ns_cbranch_scc1 .LbuC_%=\nv_and_b32 v125,v112,v116\nv_bitop3_b32 v125,v125,v120,1 bitop3:0x80\n" \
"v_cmp_eq_u32 vcc,0,v125\ns_nop 0\ns_cbranch_vccnz .LsuC_%=\ns_branch .LbuC_%=\n" \
".Lend_%=:\n"
#define ASM_CLOB "v1", "v2", "v3", "v4", "v5", "v6", "v7", "v8", "v9", "v10", "v11", "v12", "v13", "v14", "v15", "v16", "v17", "v18", "v19", "v20", "v21", "v22", "v23", "v24", "v25", "v26", "v27", "v28", "v29", "v30", "v31", "v32", "v33", "v34", "v35", "v36", "v37", "v38", "v39", "v40", "v41", "v42", "v43", "v44", "v45", "v46", "v47", "v48", "v49", "v50", "v51", "v52", "v53", "v54", "v55", "v56", "v57", "v58", "v59", "v60", "v61", "v62", "v63", "v64", "v65", "v66", "v67", "v68", "v69", "v70", "v71", "v72", "v73", "v74", "v75", "v76", "v77", "v78", "v79", "v80", "v81", "v82", "v83", "v84", "v85", "v86", "v87", "v88", "v89", "v90", "v91", "v92", "v93", "v94", "v95", "v96", "v97", "v98", "v99", "v100", "v101", "v102", "v103", "v104", "v105", "v106", "v107", "v108", "v109", "v110", "v111", "v112", "v113", "v114", "v115", "v116", "v117", "v118", "v119", "v120", "v121", "v122", "v123", "v124", "v125", "v126", "v127", "v128", "v129", "v130", "v131", "v132", "v133", "v134", "v135", "v136", "v137", "v138", "v139", "v140", "v141", "v142", "v143", "v144", "v145", "v146", "v147", "v148", "v149", "v150", "v151", "v152", "v153", "v154", "v155", "v156", "v157", "v158", "v159", "v160", "v161", "v162", "v163", "v164", "v165", "v166", "v167", "v168", "v169", "v170", "v171", "v172", "v173", "v174", "v175", "v176", "v177", "v178", "v179", "v180", "v181", "v182", "v183", "v184", "v185", "v186", "v187", "v188", "v189", "v190", "v191", "v192", "v193", "v194", "v195", "v196", "v197", "v198", "v199", "v200", "v201", "v202", "v203", "v204", "v205", "v206", "v207", "v208", "v209", "v210", "v211", "v212", "v213", "v214", "v215", "v216", "v217", "v218", "v219", "v220", "v221", "v222", "v223", "v224", "v225", "v226", "v227", "v228", "v229", "v230", "v231", "v232", "v233", "v234", "v235", "v236", "v237", "v238", "v239", "v240", "v241", "v242", "v243", "v244", "v245", "v246", "v247", "v248", "v249", "v250", "v251", "v252", "v253", "v254", "v255", "a0", "a1", "a2", "a3", "a4", "a5", "a6", "a7", "a8", "a9", "a10", "a11", "a12", "a13", "a14", "a15", "a16", "a17", "a18", "a19", "a20", "a21", "a22", "a23", "a24", "a25", "a26", "a27", "a28", "a29", "a30", "a31", "a32", "a33", "a34", "a35", "a36", "a37", "a38", "a39", "a40", "a41", "a42", "a43", "a44", "a45", "a46", "a47", "a48", "a49", "a50", "a51", "a52", "a53", "a54", "a55", "a56", "a57", "a58", "a59", "a60", "a61", "a62", "a63", "a64", "a65", "a66", "a67", "a68", "a69", "a70", "a71", "a72", "a73", "a74", "a75", "a76", "a77", "a78", "a79", "a80", "a81", "a82", "a83", "a84", "a85", "a86", "a87", "a88", "a89", "a90", "a91", "a92", "a93", "a94", "a95", "a96", "a97", "a98", "a99", "a100", "a101", "a102", "a103", "a104", "a105", "a106", "a107", "a108", "a109", "a110", "a111", "a112", "a113", "a114", "a115", "a116", "a117", "a118", "a119", "a120", "a121", "a122", "a123", "a124", "a125", "a126", "a127", "a128", "a129", "a130", "a131", "a132", "a133", "a134", "a135", "a136", "a137", "a138", "a139", "a140", "a141", "a142", "a143", "a144", "a145", "a146", "a147", "a148", "a149", "a150", "a151", "a152", "a153", "a154", "a155", "a156", "a157", "a158", "a159", "a160", "a161", "a162", "a163", "a164", "a165", "a166", "a167", "a168", "a169", "a170", "a171", "a172", "a173", "a174", "a175", "a176", "a177", "a178", "a179", "a180", "a181", "a182", "a183", "a184", "a185", "a186", "a187", "a188", "a189", "a190", "a191", "a192", "a193", "a194", "a195", "a196", "a197", "a198", "a199", "a200", "a201", "a202", "a203", "a204", "a205", "a206", "a207", "a208", "a209", "a210", "a211", "a212", "a213", "a214", "a215", "a216", "a217", "a218", "a219", "a220", "a221", "a222", "a223", "a224", "a225", "a226", "a227", "a228", "a229", "a230", "a231", "a232", "a233", "a234", "a235", "a236", "a237", "a238", "a239", "a240", "a241", "a242", "a243", "a244", "a245", "a246", "a247", "a248", "a249", "a250", "a251", "a252", "a253", "a254", "a255", "s3", "s6", "s7", "s8", "s9", "s10", "s11", "s12", "s13", "s14", "s15", "s16", "s17", "s18", "s19", "s20", "s21", "s22", "s23", "s24", "s25", "s26", "s27", "s28", "s29", "s30", "s31", "s32", "s33", "s34", "s35", "s36", "s37", "s38", "s39", "s40", "s41", "s42", "s43", "s44", "s45", "s46", "s47", "s48", "s49", "s50", "s51", "s52", "s53", "s54", "s55", "s56", "s57", "s58", "s59", "s60", "s61", "s62", "s63", "s64", "s65", "s66", "s67", "s68", "s69", "s70", "s71", "s72", "s73", "s74", "s75", "s76", "s77", "s78", "s79", "s80", "s81", "s82", "s83", "s84", "s85", "s86", "s87", "s88", "s89", "s90", "s91", "s92", "s93", "s94", "s95", "s96", "s97", "s98", "s99", "s100", "s101"
extern "C" __global__ __launch_bounds__(256, 1) void adm(
    const u16* x, const u16* wr, const u16* wi, const u16* w1, const u16* cvec, const u16* wmod, char* wsb, const u16* b1, const u16* b2, const u16* w2, u16* outp) {
  unsigned long long ka = (unsigned long long)__builtin_amdgcn_kernarg_segment_ptr(), tab = (unsigned long long)TAB;
  unsigned bid = blockIdx.x, tid = threadIdx.x;
  asm volatile(ASM_ALL : "+{s[0:1]}"(ka), "+{s2}"(bid), "+{s[4:5]}"(tab), "+{v0}"(tid) : : "vcc", "scc", "memory", ASM_CLOB);
}

extern "C" void kernel_launch(void* const* d_in, const int* in_sizes, int n_in, void* d_out, int out_size, void* d_ws,
                              size_t ws_size, hipStream_t stream) {
  (void)hipFuncSetAttribute((const void*)adm, hipFuncAttributeMaxDynamicSharedMemorySize, 163840);
  adm<<<256, 256, 163840, stream>>>((const u16*)d_in[0], (const u16*)d_in[1], (const u16*)d_in[2], (const u16*)d_in[4], (const u16*)d_in[8], (const u16*)d_in[9],
                                    (char*)d_ws, (const u16*)d_in[5], (const u16*)d_in[7], (const u16*)d_in[6], (u16*)d_out);
}
```
